```python
import torch

def _softmax(x, axis=-1):
    return torch.softmax(x, dim=axis)

def _gelu(x):
    return torch.nn.functional.gelu(x, approximate='tanh')

def _silu(x):
    return torch.nn.functional.silu(x)

def _sigmoid(x):
    return torch.sigmoid(x)

def _layernorm(x, axis=-1, eps=1e-05):
    m = x.mean(dim=axis, keepdim=True)
    v = x.var(dim=axis, keepdim=True, unbiased=False)
    return (x - m) / torch.sqrt(v + eps)

def _scatter_add(tgt, idx, src, axis=0):
    return tgt.clone().index_add_(axis, idx, src)

def _segment_sum(src, seg, n):
    out = torch.zeros((n,) + src.shape[1:], dtype=src.dtype)
    return out.index_add_(0, seg, src)

def _pad(x, pw):
    flat = tuple((v for pair in reversed(pw) for v in pair))
    return torch.nn.functional.pad(x, flat)
kernel_name = 'interleave_layers__memory_aug_recurrent_mem_tr__moe_arch_mod__prompt'

def setup_inputs():

    _w = lambda a: (lambda t: t.to(torch.bfloat16) if t.is_floating_point() else t)(torch.as_tensor(a))

    def _A_setup():
        B, S, Dm = (1, 2048, 512)
        H, Dh = (32, 128)
        M, seg = (8, 32)
        seg = min(seg, S)
        S = S // seg * seg
        return {'x': _w((lambda *s: torch.randn(*s, dtype=torch.bfloat16) * 0.05)(B, S, Dm)), 'mem0': _w((lambda *s: torch.randn(*s, dtype=torch.bfloat16) * 0.8)(B, M, Dm)), 'Wq': _w((lambda *s: torch.randn(*s, dtype=torch.bfloat16) * 0.8)(Dm, H, Dh) * Dm ** (-0.5)), 'Wk': _w((lambda *s: torch.randn(*s, dtype=torch.bfloat16) * 0.8)(Dm, H, Dh) * Dm ** (-0.5)), 'Wv': _w((lambda *s: torch.randn(*s, dtype=torch.bfloat16) * 0.8)(Dm, H, Dh) * Dm ** (-0.5)), 'Wo': _w((lambda *s: torch.randn(*s, dtype=torch.bfloat16) * 0.8)(H, Dh, Dm) * (H * Dh) ** (-0.5)), 'seg_len': seg}
    _A_in = _A_setup()

    def _B_setup():
        B, S, D, F, C = (1, 2048, 512, 1792, 1)
        return {'x': _w((lambda *s: torch.randn(*s, dtype=torch.bfloat16) * 0.05)(B, S, D)), 'w_r': _w((lambda *s: torch.randn(*s, dtype=torch.bfloat16) * 0.05)(D)), 'w_gate': _w((lambda *s: torch.randn(*s, dtype=torch.bfloat16) * 0.05)(D, F)), 'w_up': _w((lambda *s: torch.randn(*s, dtype=torch.bfloat16) * 0.05)(D, F)), 'w_down': _w((lambda *s: torch.randn(*s, dtype=torch.bfloat16) * 0.05)(F, D))}
    _B_in = _B_setup()
    return {'A_x': _A_in['x'], 'A_mem0': _A_in['mem0'], 'A_Wq': _A_in['Wq'], 'A_Wk': _A_in['Wk'], 'A_Wv': _A_in['Wv'], 'A_Wo': _A_in['Wo'], 'A_seg_len': _A_in['seg_len'], 'B_x': _B_in['x'], 'B_w_r': _B_in['w_r'], 'B_w_gate': _B_in['w_gate'], 'B_w_up': _B_in['w_up'], 'B_w_down': _B_in['w_down']}

def reference(A_x, A_mem0, A_Wq, A_Wk, A_Wv, A_Wo, A_seg_len, B_x, B_w_r, B_w_gate, B_w_up, B_w_down):

    def _A_ref(x, mem0, Wq, Wk, Wv, Wo, seg_len):
        B, S, Dm = x.shape
        H, Dh = (Wq.shape[1], Wq.shape[2])
        M = mem0.shape[1]
        scale = Dh ** (-0.5)
        n_seg = S // seg_len
        L = M + seg_len
        idx = torch.arange(L)
        mask = (idx[:, None] < M) | (idx[:, None] >= idx[None, :])
        mem = mem0
        outs = []
        for s in range(n_seg):
            xs = x[:, s * seg_len:(s + 1) * seg_len, :].clone()
            h = torch.concatenate([mem, xs], axis=1)
            q = torch.einsum('btd,dhe->bhte', h, Wq)
            k = torch.einsum('btd,dhe->bhte', h, Wk)
            v = torch.einsum('btd,dhe->bhte', h, Wv)
            scores = torch.einsum('bhqe,bhke->bhqk', q, k) * scale
            scores = torch.where(mask[None, None], scores, -1000000000.0)
            p = _softmax(scores, axis=-1)
            o = torch.einsum('bhqk,bhke->bhqe', p, v)
            o = torch.einsum('bhte,hed->btd', o, Wo)
            mem = o[:, :M, :].clone()
            outs.append(o[:, M:, :])
        return torch.concatenate(outs, axis=1)

    def _B_ref(x, w_r, w_gate, w_up, w_down):
        B, S, D = x.shape
        C = 1
        r = x @ w_r
        idx = torch.argsort(-r, axis=-1)[:, :C]
        idx_e = (lambda x, n, axis: torch.repeat_interleave(x, n, dim=axis))(idx.reshape(B, C, 1), D, axis=2)
        x_sel = (lambda x, i, axis: torch.take_along_dim(x, i, dim=axis))(x, idx_e, axis=1)
        g = _silu(x_sel @ w_gate)
        u = x_sel @ w_up
        y = g * u @ w_down
        r_sel = _sigmoid((lambda x, i, axis: torch.take_along_dim(x, i, dim=axis))(r, idx, axis=1))
        y = y * r_sel.reshape(B, C, 1)
        b_off = (torch.arange(B) * S).reshape(B, 1)
        flat_idx = (idx + b_off).reshape(B * C)
        zero = (x * 0.0).reshape(B * S, D)
        upd = _scatter_add(zero, flat_idx, y.reshape(B * C, D), axis=0)
        return x + upd.reshape(B, S, D)
    x = A_x
    x = x + _A_ref(_layernorm(x, axis=-1), A_mem0, A_Wq, A_Wk, A_Wv, A_Wo, A_seg_len)
    x = x + _B_ref(_layernorm(x, axis=-1), B_w_r, B_w_gate, B_w_up, B_w_down)
    x = x + _A_ref(_layernorm(x, axis=-1), A_mem0, A_Wq, A_Wk, A_Wv, A_Wo, A_seg_len)
    x = x + _B_ref(_layernorm(x, axis=-1), B_w_r, B_w_gate, B_w_up, B_w_down)
    return x
```

```cpp
#include <hip/hip_runtime.h>
#include <hip/hip_bf16.h>
#include <stdint.h>

typedef unsigned short u16;
using bf16x8 = __attribute__((ext_vector_type(8))) short;
using f32x4 = __attribute__((ext_vector_type(4))) float;

#define S_TOK 2048
#define DM 512
#define NH 32
#define DH 128
#define HD 4096
#define MEMT 8
#define SEG 32
#define NSEG 64
#define FF 1792

__device__ __forceinline__ float bf2f(u16 v) { return __uint_as_float(((unsigned)v) << 16); }
__device__ __forceinline__ float rbf(float f) { return (float)(__bf16)f; }

template <int N> __device__ __forceinline__ float ror_add(float v) {
  return v + __int_as_float(__builtin_amdgcn_update_dpp(0, __float_as_int(v), 0x120 + N, 0xf, 0xf, false));
}

typedef unsigned v4u __attribute__((ext_vector_type(4)));
typedef unsigned v2u __attribute__((ext_vector_type(2)));
typedef unsigned v8u __attribute__((ext_vector_type(8)));
typedef __bf16 bfx8 __attribute__((ext_vector_type(8)));
typedef __bf16 bfx16 __attribute__((ext_vector_type(16)));
typedef short s16x4 __attribute__((ext_vector_type(4)));
typedef __attribute__((address_space(3))) s16x4 lds_s16x4;
#define AUX_SC1 16
#define SPINCAP (1u << 19)

__device__ __forceinline__ unsigned pk_bf16(float a, float b) {
  typedef __bf16 bfx2 __attribute__((ext_vector_type(2)));
  bfx2 v; v[0] = (__bf16)a; v[1] = (__bf16)b;
  return __builtin_bit_cast(unsigned, v);
}
__device__ __forceinline__ f32x4 mf32(v4u a, v4u b, f32x4 c) {
  return __builtin_amdgcn_mfma_f32_16x16x32_bf16(__builtin_bit_cast(bfx8, a), __builtin_bit_cast(bfx8, b), c, 0, 0, 0);
}
#define MBY(x) ((unsigned)(x) << 20)
#define SEGST 2621440u
#define Z_TOKP MBY(160)
#define Z_PART MBY(172)
#define Z_CH MBY(220)
#define Z_XN MBY(226)
#define Z_X2 MBY(230)
#define Z_X1 MBY(234)
#define Z_X3A MBY(242)
#define Z_FLG MBY(250)
#define Z_FT (Z_FLG)
#define Z_FO (Z_FT + 32u * 16384u)
#define Z_RS (Z_FO + 3u * 16384u)
#define Z_FP (Z_RS + 3u * 16384u)
#define Z_R1 (Z_FP + 3u * 16384u)
#define Z_DEC (Z_R1 + 16384u)
#define Z_T0 (Z_DEC + 16384u)
#define Z_ERR (Z_T0 + 3u * 128u)
#define Z_ZEND (Z_ERR + 128u)
#define Z_NEAR (Z_ZEND)
#define Z_X2P (MBY(251))
#define Z_HB (Z_X2P + 131072u)
#define Z_SEND (Z_HB + 33u * 16384u)
#define Z_XNP (Z_SEND)
#define Z_DBG MBY(252)
#define ZWS_BYTES MBY(252)
#define OOR 0x80000000u
__host__ __device__ __forceinline__ unsigned nearof(unsigned o, unsigned c13) { return o | (((o >> 8) ^ c13) & 0x2000u); }
__host__ __device__ __forceinline__ unsigned blk(unsigned base, unsigned o, unsigned c13) { return nearof(base + (o >> 13 << 14), c13) + (o & 8191u); }
typedef __amdgpu_buffer_rsrc_t rsrc_t;
__device__ __forceinline__ v4u ld4(rsrc_t rs, unsigned off) { return __builtin_amdgcn_raw_buffer_load_b128(rs, off, 0, 0); }
__device__ __forceinline__ v4u ld4c(rsrc_t rs, unsigned off) { return __builtin_amdgcn_raw_buffer_load_b128(rs, off, 0, AUX_SC1); }
__device__ __forceinline__ v2u ld2c(rsrc_t rs, unsigned off) { return __builtin_amdgcn_raw_buffer_load_b64(rs, off, 0, AUX_SC1); }
__device__ __forceinline__ unsigned ld1c(rsrc_t rs, unsigned off) { return __builtin_amdgcn_raw_buffer_load_b32(rs, off, 0, AUX_SC1); }
__device__ __forceinline__ void st4c(rsrc_t rs, unsigned off, v4u v) { __builtin_amdgcn_raw_buffer_store_b128(v, rs, off, 0, AUX_SC1); }
__device__ __forceinline__ void st1c(rsrc_t rs, unsigned off, unsigned v) { __builtin_amdgcn_raw_buffer_store_b32(v, rs, off, 0, AUX_SC1); }
__device__ __forceinline__ v4u ld4s(rsrc_t rs, unsigned off, unsigned so) { return __builtin_amdgcn_raw_buffer_load_b128(rs, off, so, AUX_SC1); }
__device__ __forceinline__ void st4s(rsrc_t rs, unsigned off, unsigned so, v4u v) {
  __builtin_amdgcn_raw_buffer_store_b128(v, rs, off, so, AUX_SC1);
  asm volatile("s_nop 1" :: "v"(v) : "memory");
}
__device__ __forceinline__ void vm0() { asm volatile("s_waitcnt vmcnt(0)" ::: "memory"); }
#define XSTR 1040
#define PSLEEP 1
#define ZACC ((f32x4){0.f, 0.f, 0.f, 0.f})
__device__ __forceinline__ void mfw(bool first, f32x4& acc, const v4u& wa, const v4u& xb) {
  if (first) asm volatile("s_nop 1\n v_mfma_f32_16x16x32_bf16 %0, %1, %2, 0" : "=&v"(acc) : "a"(wa), "v"(xb));
  else asm volatile("s_nop 1\n v_mfma_f32_16x16x32_bf16 %0, %1, %2, %0" : "+v"(acc) : "a"(wa), "v"(xb));
}
__device__ __forceinline__ void mfence4(f32x4& a, f32x4& b, f32x4& c, f32x4& d) { asm volatile("s_nop 7\n s_nop 7" : "+v"(a), "+v"(b), "+v"(c), "+v"(d)); }
__device__ __forceinline__ void unpack8(v4u v, float (&x)[8]) {
#pragma unroll
  for (int i = 0; i < 4; ++i) { x[2 * i] = __uint_as_float(v[i] << 16); x[2 * i + 1] = __uint_as_float(v[i] & 0xffff0000u); }
}

__device__ __forceinline__ void mfxv(bool first, f32x4& acc, const v4u& xa, const v4u& wb) {
  if (first) asm volatile("s_nop 1\n v_mfma_f32_16x16x32_bf16 %0, %1, %2, 0" : "=&v"(acc) : "v"(xa), "v"(wb));
  else asm volatile("s_nop 1\n v_mfma_f32_16x16x32_bf16 %0, %1, %2, %0" : "+v"(acc) : "v"(xa), "v"(wb));
}
__device__ __forceinline__ int lane_id() { int l; asm volatile("v_mbcnt_lo_u32_b32 %0, -1, 0\n v_mbcnt_hi_u32_b32 %0, -1, %0" : "=v"(l)); return l; }
#define CL_K 122880
#define CL_Q (CL_K + 16384)
#define CL_MEM (CL_K + 24576)
#define CL_QX (CL_K + 32768)
#define CL_SEL (CL_QX + 2560 + 256)
#define CL_VX (CL_MEM + 2304)
#define CL_PX (CL_MEM + 4352)
#define CL_OX CL_QX
__device__ __forceinline__ v4u mkrs(const void* p, unsigned bytes) {
  return (v4u){(unsigned)(size_t)p, (unsigned)((size_t)p >> 32) & 0xffffu, bytes, 0x00020000u};
}
#define T0D 900u
struct Pace { int per, gat, late, rsd, fck; };
template <bool RST>
__device__ __forceinline__ void chain8(int w, int h, int V, unsigned c1, char* ws, char* lds, const u16* mem0, const u16* Wq, const u16* Wk, const u16* Wv, const u16* Wo, const Pace tm) {
  const int lane = lane_id(), c = lane & 15, g = lane >> 4, l16 = lane * 16;
  const rsrc_t rs = __builtin_amdgcn_make_buffer_rsrc((void*)ws, 0, ZWS_BYTES, 0x00020000);
  const unsigned cm = V ? c1 ^ 0x2000u : c1, ps = nearof(Z_CH + V * MBY(2), cm), mo = ps + MBY(1);
  unsigned t0 = 0;
  if (lane == 0) {
    const unsigned t = ((unsigned)__builtin_amdgcn_s_memrealtime() + T0D) | 3u, o = atomicCAS((unsigned*)(ws + Z_T0 + V * 128), 0u, t);
    t0 = (o ? o : t) + 1u;
  }
  t0 = __builtin_amdgcn_readfirstlane(t0);
  const int gp = (h >> 1) & 3, sp = (h >> 3) * 2 + (h & 1);
  const v4u EMPTY = (v4u){~0u, ~0u, ~0u, ~0u};
#pragma unroll
  for (int k = 0; k < 4; ++k) {
    st4c(rs, ps + (k >> 1) * 0x80000 + h * 16384 + (k & 1) * 4096 + w * 1024 + l16, EMPTY);
    v4u v = EMPTY;
    if (w == 0 && k == 0 && lane < 16) {
      const int n7 = (lane & 7) ^ (gp >= 2 ? 4 : 0);
      const u16* p = mem0 + (2 * gp + (lane >> 3)) * DM + 64 * sp + 32 * (n7 >> 2) + (n7 & 3);
#pragma unroll
      for (int d = 0; d < 4; ++d) v[d] = (unsigned)p[8 * d] | ((unsigned)p[8 * d + 4] << 16);
      v[3] = min(v[3], 0xfffffffeu);
    }
    st4c(rs, mo + (w * 16 + k * 4 + g) * 16384 + sp * 1024 + gp * 256 + c * 16, v);
  }
  if (w == 0) {
    const unsigned one = (c & 1) ? 0x3f800000u : 0x3f80u;
    *(v4u*)(lds + CL_SEL + l16) = (v4u){(c >> 1) == 0 ? one : 0u, (c >> 1) == 1 ? one : 0u, (c >> 1) == 2 ? one : 0u, (c >> 1) == 3 ? one : 0u};
  }
  const int tk = 2 * (c >> 2) + (c & 1), hf = (c >> 1) & 1, j = 8 * w + (lane >> 3), q = (lane >> 2) & 3, p = lane & 3;
  const int pv[36] = {w, (int)ps, h * 16384, (int)mo, h * 16384 + w * 2048, (int)cm, (V == 1 ? 4 : 3) * 524288 + h * 16384 + (w & 1) * 4096 + (w >> 1) * 256, (int)cm,
                      (int)((nearof(Z_FO + V * 16384, c1 ^ 0x2000u) + h * 256 - 4u) | OOR), (int)nearof(Z_FT + h * 16384, cm), (int)nearof(Z_RS + V * 16384, cm),
                      (int)(nearof(Z_CH + MBY(2), cm) + MBY(1)), -1, (int)Z_TOKP, 32768, h * 4, (int)nearof(Z_RS + (V == 2 ? 1 : V) * 16384, cm), (int)t0, (int)Z_ERR, (int)(Z_DBG + (V * 32 + h) * 4096),
                      (int)(size_t)Wq, (int)((size_t)Wq >> 32) & 0xffff, (int)(size_t)Wk, (int)((size_t)Wk >> 32) & 0xffff, (int)(size_t)Wv, (int)((size_t)Wv >> 32) & 0xffff, (int)(size_t)Wo, (int)((size_t)Wo >> 32) & 0xffff,
                      (int)(size_t)ws, (int)((size_t)ws >> 32) & 0xffff, (int)ZWS_BYTES, tm.per, tm.gat, tm.late, tm.rsd, tm.fck};
  int hv0 = l16, hv1 = hf ? 0xEEEE : 0x4444, hv2 = CL_MEM + tk * 128 + ((2 * g + hf) ^ (c >= 8 ? 4 : 0)) * 16;
  int hv3 = CL_QX + 2 * g * 288 + (32 * w + 2 * c) * 2, hv4 = hv3 - CL_QX + CL_MEM, hv5 = CL_QX + (c & 7) * 288 + g * 16, hv6 = CL_MEM + (2 * ((c >> 2) ^ (c & 2)) + (c & 1)) * 288 + g * 16;
  int hv7 = w * 30720 + l16, hv8 = CL_PX + w * 512 + g * 128 + (c & 7) * 16, hv9 = CL_K + w * 2048 + l16, hv10 = CL_VX + w * 512 + lane * 8, hv11 = CL_OX + (4 * w + g) * 128 + (c & 7) * 16, hv12 = CL_OX + (2 * g + hf) * 128 + tk * 16;
  int hv13 = j * 16384 + ((h + j) & 31) * 128 + (lane & 7) * 16;
  int hv14 = w ? CL_Q + (w & 1) * 4096 + l16 : ((lane & 0x28) ? OOR : sp * 1024 + (2 * gp + g) * 128 + ((lane & 7) ^ (gp >= 2 ? 4 : 0)) * 16), hv15 = CL_PX + (c >> 3) * 1024 + g * 128 + (c & 7) * 16, hv20 = 0;
  int hv16 = (w * 16 + g) * 8192 + h * 256 + c * 16, hv17 = (h * 128 + 16 * (w & 1) + 8 * (w >> 1)) * 1024 + l16;
  int hv18 = (16 * (g & 1) + 4 * (q & 1) + 2 * (g >> 1) + (q >> 1)) * 256 + 64 * w + 8 * p, hv19 = (lane >> 2) * 1024 + 256 * w + 64 * p;
#pragma unroll
  for (int i = 0; i < 36; ++i) hv20 = lane == i ? pv[i] : hv20;
  __builtin_amdgcn_s_waitcnt(0);
  if constexpr (RST) {
  asm volatile(
    "s_mov_b32 s33, 0\n"
    "s_nop 1\n"
    "v_mov_b32 v119, v20\n"
    "s_nop 0\n"
    "v_readlane_b32 s61, v119, 0\n"
    "v_readlane_b32 s46, v119, 1\n"
    "v_readlane_b32 s63, v119, 2\n"
    "v_readlane_b32 s48, v119, 3\n"
    "v_readlane_b32 s55, v119, 4\n"
    "v_readlane_b32 s56, v119, 5\n"
    "v_readlane_b32 s57, v119, 6\n"
    "v_readlane_b32 s74, v119, 7\n"
    "v_readlane_b32 s58, v119, 8\n"
    "v_readlane_b32 s59, v119, 9\n"
    "v_readlane_b32 s60, v119, 10\n"
    "v_readlane_b32 s67, v119, 11\n"
    "v_readlane_b32 s68, v119, 12\n"
    "v_readlane_b32 s69, v119, 13\n"
    "v_readlane_b32 s70, v119, 14\n"
    "v_readlane_b32 s71, v119, 15\n"
    "v_readlane_b32 s73, v119, 16\n"
    "v_readlane_b32 s44, v119, 17\n"
    "v_readlane_b32 s35, v119, 18\n"
    "v_readlane_b32 s75, v119, 19\n"
    "v_readlane_b32 s16, v119, 28\n"
    "v_readlane_b32 s17, v119, 29\n"
    "v_readlane_b32 s18, v119, 30\n"
    "v_readlane_b32 s8, v119, 31\n"
    "v_readlane_b32 s9, v119, 32\n"
    "v_readlane_b32 s10, v119, 33\n"
    "v_readlane_b32 s11, v119, 34\n"
    "v_readlane_b32 s12, v119, 35\n"
    "s_mov_b32 s19, 0x20000\n"
    "v_readlane_b32 s84, v119, 20\n"
    "v_readlane_b32 s85, v119, 21\n"
    "v_readlane_b32 s86, v119, 22\n"
    "v_readlane_b32 s87, v119, 23\n"
    "v_readlane_b32 s88, v119, 24\n"
    "v_readlane_b32 s89, v119, 25\n"
    "v_readlane_b32 s90, v119, 26\n"
    "v_readlane_b32 s91, v119, 27\n"
    "s_mov_b32 s76, 0xff00ff00\n"
    "s_mov_b32 s77, 0xff00ff00\n"
    "s_mov_b32 s78, 0x00ff00ff\n"
    "s_mov_b32 s79, 0x00ff00ff\n"
    "s_mov_b32 s65, 0x3e0293ee\n"
    "s_mov_b32 s66, 0xfffefffe\n"
    "s_lshl_b32 s62, s61, 11\n"
    "s_mov_b32 s36, 0\n"
    "s_mov_b32 s64, 0\n"
    "s_mov_b32 s50, 0\n"
    "s_lshl_b32 s72, s61, 2\n"
    "s_add_u32 s72, s72, 158208\n"
    "s_add_u32 s47, s46, 4096\n"
    "s_and_b32 s20, s61, 1\n"
    "s_lshl_b32 s20, s20, 7\n"
    "s_mov_b32 s14, 0x3f803f80\n"
    "s_mov_b32 s15, 0x3f803f80\n"
    "s_mov_b32 s82, 0x400000\n"
    "s_mov_b32 s83, 0x20000\n"
    "v_mov_b32 v112, v16\n"
    "v_mov_b32 v113, v17\n"
    "v_mov_b32 v114, v18\n"
    "v_mov_b32 v115, v19\n"
    "s_mov_b32 s80, s84\n"
    "s_mov_b32 s81, s85\n"
    "s_lshl_b32 s38, s62, 1\n"
    "s_add_u32 m0, s38, 0\n"
    "s_mov_b32 s39, 0\n"
    "buffer_load_dwordx4 v112, s[80:83], s39 offen lds\n"
    "s_add_u32 m0, s38, 1024\n"
    "s_mov_b32 s39, 32768\n"
    "buffer_load_dwordx4 v112, s[80:83], s39 offen lds\n"
    "s_add_u32 m0, s38, 2048\n"
    "s_mov_b32 s39, 65536\n"
    "buffer_load_dwordx4 v112, s[80:83], s39 offen lds\n"
    "s_add_u32 m0, s38, 3072\n"
    "s_mov_b32 s39, 98304\n"
    "buffer_load_dwordx4 v112, s[80:83], s39 offen lds\n"
    "s_lshl_b32 s38, s62, 1\n"
    "s_add_u32 m0, s38, 16384\n"
    "s_mov_b32 s39, 524288\n"
    "buffer_load_dwordx4 v112, s[80:83], s39 offen lds\n"
    "s_add_u32 m0, s38, 17408\n"
    "s_mov_b32 s39, 557056\n"
    "buffer_load_dwordx4 v112, s[80:83], s39 offen lds\n"
    "s_add_u32 m0, s38, 18432\n"
    "s_mov_b32 s39, 589824\n"
    "buffer_load_dwordx4 v112, s[80:83], s39 offen lds\n"
    "s_add_u32 m0, s38, 19456\n"
    "s_mov_b32 s39, 622592\n"
    "buffer_load_dwordx4 v112, s[80:83], s39 offen lds\n"
    "s_lshl_b32 s38, s62, 1\n"
    "s_add_u32 m0, s38, 32768\n"
    "s_mov_b32 s39, 1048576\n"
    "buffer_load_dwordx4 v112, s[80:83], s39 offen lds\n"
    "s_add_u32 m0, s38, 33792\n"
    "s_mov_b32 s39, 1081344\n"
    "buffer_load_dwordx4 v112, s[80:83], s39 offen lds\n"
    "s_add_u32 m0, s38, 34816\n"
    "s_mov_b32 s39, 1114112\n"
    "buffer_load_dwordx4 v112, s[80:83], s39 offen lds\n"
    "s_add_u32 m0, s38, 35840\n"
    "s_mov_b32 s39, 1146880\n"
    "buffer_load_dwordx4 v112, s[80:83], s39 offen lds\n"
    "s_lshl_b32 s38, s62, 1\n"
    "s_add_u32 m0, s38, 49152\n"
    "s_mov_b32 s39, 1572864\n"
    "buffer_load_dwordx4 v112, s[80:83], s39 offen lds\n"
    "s_add_u32 m0, s38, 50176\n"
    "s_mov_b32 s39, 1605632\n"
    "buffer_load_dwordx4 v112, s[80:83], s39 offen lds\n"
    "s_add_u32 m0, s38, 51200\n"
    "s_mov_b32 s39, 1638400\n"
    "buffer_load_dwordx4 v112, s[80:83], s39 offen lds\n"
    "s_add_u32 m0, s38, 52224\n"
    "s_mov_b32 s39, 1671168\n"
    "buffer_load_dwordx4 v112, s[80:83], s39 offen lds\n"
    "s_lshl_b32 s38, s62, 1\n"
    "s_add_u32 m0, s38, 65536\n"
    "s_mov_b32 s39, 2097152\n"
    "buffer_load_dwordx4 v112, s[80:83], s39 offen lds\n"
    "s_add_u32 m0, s38, 66560\n"
    "s_mov_b32 s39, 2129920\n"
    "buffer_load_dwordx4 v112, s[80:83], s39 offen lds\n"
    "s_add_u32 m0, s38, 67584\n"
    "s_mov_b32 s39, 2162688\n"
    "buffer_load_dwordx4 v112, s[80:83], s39 offen lds\n"
    "s_add_u32 m0, s38, 68608\n"
    "s_mov_b32 s39, 2195456\n"
    "buffer_load_dwordx4 v112, s[80:83], s39 offen lds\n"
    "s_lshl_b32 s38, s62, 1\n"
    "s_add_u32 m0, s38, 81920\n"
    "s_mov_b32 s39, 2621440\n"
    "buffer_load_dwordx4 v112, s[80:83], s39 offen lds\n"
    "s_add_u32 m0, s38, 82944\n"
    "s_mov_b32 s39, 2654208\n"
    "buffer_load_dwordx4 v112, s[80:83], s39 offen lds\n"
    "s_add_u32 m0, s38, 83968\n"
    "s_mov_b32 s39, 2686976\n"
    "buffer_load_dwordx4 v112, s[80:83], s39 offen lds\n"
    "s_add_u32 m0, s38, 84992\n"
    "s_mov_b32 s39, 2719744\n"
    "buffer_load_dwordx4 v112, s[80:83], s39 offen lds\n"
    "s_sub_u32 s51, s44, 100\n"
    "s_mov_b32 s45, 0\n"
    "s_cmp_eq_u32 s61, 1\n"
    "s_cselect_b32 s38, 0, 0x40000000\n"
    "s_add_u32 s51, s51, s38\n"
    "s_waitcnt vmcnt(20)\n"
    "s_barrier\n"
    "s_lshl_b32 s38, s62, 1\n"
    "s_add_u32 m0, s38, 98304\n"
    "s_mov_b32 s39, 3145728\n"
    "buffer_load_dwordx4 v112, s[80:83], s39 offen lds\n"
    "s_add_u32 m0, s38, 99328\n"
    "s_mov_b32 s39, 3178496\n"
    "buffer_load_dwordx4 v112, s[80:83], s39 offen lds\n"
    "s_add_u32 m0, s38, 100352\n"
    "s_mov_b32 s39, 3211264\n"
    "buffer_load_dwordx4 v112, s[80:83], s39 offen lds\n"
    "s_add_u32 m0, s38, 101376\n"
    "s_mov_b32 s39, 3244032\n"
    "buffer_load_dwordx4 v112, s[80:83], s39 offen lds\n"
    "v_add_u32 v116, 0, v114\n"
    "ds_read_b64_tr_b16 v[104:105], v116 offset:0\n"
    "ds_read_b64_tr_b16 v[106:107], v116 offset:2048\n"
    "ds_read_b64_tr_b16 v[108:109], v116 offset:8192\n"
    "ds_read_b64_tr_b16 v[110:111], v116 offset:10240\n"
    "ds_read_b64_tr_b16 v[96:97], v116 offset:32\n"
    "ds_read_b64_tr_b16 v[98:99], v116 offset:2080\n"
    "ds_read_b64_tr_b16 v[100:101], v116 offset:8224\n"
    "ds_read_b64_tr_b16 v[102:103], v116 offset:10272\n"
    "s_waitcnt lgkmcnt(0)\n"
    "s_memrealtime s[42:43]\n"
    "s_waitcnt lgkmcnt(0)\n"
    "s_sub_u32 s42, s42, s51\n"
    "s_cmp_lt_i32 s42, 0\n"
    "s_cbranch_scc1 LCJ0_%=\n"
    "s_getpc_b64 s[52:53]\n"
    "s_branch LCA%=\n"
    "LCJ0_%=:\n"
    "s_waitcnt vmcnt(20)\n"
    "s_barrier\n"
    "s_lshl_b32 s38, s62, 1\n"
    "s_add_u32 m0, s38, 0\n"
    "s_mov_b32 s39, 3670016\n"
    "buffer_load_dwordx4 v112, s[80:83], s39 offen lds\n"
    "s_add_u32 m0, s38, 1024\n"
    "s_mov_b32 s39, 3702784\n"
    "buffer_load_dwordx4 v112, s[80:83], s39 offen lds\n"
    "s_add_u32 m0, s38, 2048\n"
    "s_mov_b32 s39, 3735552\n"
    "buffer_load_dwordx4 v112, s[80:83], s39 offen lds\n"
    "s_add_u32 m0, s38, 3072\n"
    "s_mov_b32 s39, 3768320\n"
    "buffer_load_dwordx4 v112, s[80:83], s39 offen lds\n"
    "v_add_u32 v116, 16384, v114\n"
    "ds_read_b64_tr_b16 v[88:89], v116 offset:0\n"
    "ds_read_b64_tr_b16 v[90:91], v116 offset:2048\n"
    "ds_read_b64_tr_b16 v[92:93], v116 offset:8192\n"
    "ds_read_b64_tr_b16 v[94:95], v116 offset:10240\n"
    "ds_read_b64_tr_b16 v[16:17], v116 offset:32\n"
    "ds_read_b64_tr_b16 v[18:19], v116 offset:2080\n"
    "ds_read_b64_tr_b16 v[20:21], v116 offset:8224\n"
    "ds_read_b64_tr_b16 v[22:23], v116 offset:10272\n"
    "ds_write_b128 v9, v[104:107] offset:0\n"
    "ds_write_b128 v9, v[108:111] offset:1024\n"
    "ds_write_b128 v9, v[96:99] offset:8192\n"
    "ds_write_b128 v9, v[100:103] offset:9216\n"
    "s_waitcnt lgkmcnt(10)\n"
    "ds_write_b128 v9, v[88:91] offset:16384\n"
    "s_waitcnt lgkmcnt(9)\n"
    "ds_write_b128 v9, v[92:95] offset:17408\n"
    "s_waitcnt lgkmcnt(0)\n"
    "s_memrealtime s[42:43]\n"
    "s_waitcnt lgkmcnt(0)\n"
    "s_sub_u32 s42, s42, s51\n"
    "s_cmp_lt_i32 s42, 0\n"
    "s_cbranch_scc1 LCJ1_%=\n"
    "s_getpc_b64 s[52:53]\n"
    "s_branch LCA%=\n"
    "LCJ1_%=:\n"
    "s_waitcnt vmcnt(20)\n"
    "s_barrier\n"
    "s_mov_b32 s80, s86\n"
    "s_mov_b32 s81, s87\n"
    "s_lshl_b32 s38, s62, 1\n"
    "s_add_u32 m0, s38, 16384\n"
    "s_mov_b32 s39, 0\n"
    "buffer_load_dwordx4 v112, s[80:83], s39 offen lds\n"
    "s_add_u32 m0, s38, 17408\n"
    "s_mov_b32 s39, 32768\n"
    "buffer_load_dwordx4 v112, s[80:83], s39 offen lds\n"
    "s_add_u32 m0, s38, 18432\n"
    "s_mov_b32 s39, 65536\n"
    "buffer_load_dwordx4 v112, s[80:83], s39 offen lds\n"
    "s_add_u32 m0, s38, 19456\n"
    "s_mov_b32 s39, 98304\n"
    "buffer_load_dwordx4 v112, s[80:83], s39 offen lds\n"
    "v_add_u32 v116, 32768, v114\n"
    "ds_read_b64_tr_b16 v[24:25], v116 offset:0\n"
    "ds_read_b64_tr_b16 v[26:27], v116 offset:2048\n"
    "ds_read_b64_tr_b16 v[28:29], v116 offset:8192\n"
    "ds_read_b64_tr_b16 v[30:31], v116 offset:10240\n"
    "ds_read_b64_tr_b16 v[32:33], v116 offset:32\n"
    "ds_read_b64_tr_b16 v[34:35], v116 offset:2080\n"
    "ds_read_b64_tr_b16 v[36:37], v116 offset:8224\n"
    "ds_read_b64_tr_b16 v[38:39], v116 offset:10272\n"
    "s_waitcnt lgkmcnt(0)\n"
    "s_memrealtime s[42:43]\n"
    "s_waitcnt lgkmcnt(0)\n"
    "s_sub_u32 s42, s42, s51\n"
    "s_cmp_lt_i32 s42, 0\n"
    "s_cbranch_scc1 LCJ2_%=\n"
    "s_getpc_b64 s[52:53]\n"
    "s_branch LCA%=\n"
    "LCJ2_%=:\n"
    "s_waitcnt vmcnt(20)\n"
    "s_barrier\n"
    "s_lshl_b32 s38, s62, 1\n"
    "s_add_u32 m0, s38, 32768\n"
    "s_mov_b32 s39, 524288\n"
    "buffer_load_dwordx4 v112, s[80:83], s39 offen lds\n"
    "s_add_u32 m0, s38, 33792\n"
    "s_mov_b32 s39, 557056\n"
    "buffer_load_dwordx4 v112, s[80:83], s39 offen lds\n"
    "s_add_u32 m0, s38, 34816\n"
    "s_mov_b32 s39, 589824\n"
    "buffer_load_dwordx4 v112, s[80:83], s39 offen lds\n"
    "s_add_u32 m0, s38, 35840\n"
    "s_mov_b32 s39, 622592\n"
    "buffer_load_dwordx4 v112, s[80:83], s39 offen lds\n"
    "v_add_u32 v116, 49152, v114\n"
    "ds_read_b64_tr_b16 a[0:1], v116 offset:0\n"
    "ds_read_b64_tr_b16 a[2:3], v116 offset:2048\n"
    "ds_read_b64_tr_b16 a[4:5], v116 offset:8192\n"
    "ds_read_b64_tr_b16 a[6:7], v116 offset:10240\n"
    "ds_read_b64_tr_b16 a[8:9], v116 offset:32\n"
    "ds_read_b64_tr_b16 a[10:11], v116 offset:2080\n"
    "ds_read_b64_tr_b16 a[12:13], v116 offset:8224\n"
    "ds_read_b64_tr_b16 a[14:15], v116 offset:10272\n"
    "s_waitcnt lgkmcnt(0)\n"
    "s_memrealtime s[42:43]\n"
    "s_waitcnt lgkmcnt(0)\n"
    "s_sub_u32 s42, s42, s51\n"
    "s_cmp_lt_i32 s42, 0\n"
    "s_cbranch_scc1 LCJ3_%=\n"
    "s_getpc_b64 s[52:53]\n"
    "s_branch LCA%=\n"
    "LCJ3_%=:\n"
    "s_waitcnt vmcnt(20)\n"
    "s_barrier\n"
    "s_lshl_b32 s38, s62, 1\n"
    "s_add_u32 m0, s38, 49152\n"
    "s_mov_b32 s39, 1048576\n"
    "buffer_load_dwordx4 v112, s[80:83], s39 offen lds\n"
    "s_add_u32 m0, s38, 50176\n"
    "s_mov_b32 s39, 1081344\n"
    "buffer_load_dwordx4 v112, s[80:83], s39 offen lds\n"
    "s_add_u32 m0, s38, 51200\n"
    "s_mov_b32 s39, 1114112\n"
    "buffer_load_dwordx4 v112, s[80:83], s39 offen lds\n"
    "s_add_u32 m0, s38, 52224\n"
    "s_mov_b32 s39, 1146880\n"
    "buffer_load_dwordx4 v112, s[80:83], s39 offen lds\n"
    "v_add_u32 v116, 65536, v114\n"
    "ds_read_b64_tr_b16 a[16:17], v116 offset:0\n"
    "ds_read_b64_tr_b16 a[18:19], v116 offset:2048\n"
    "ds_read_b64_tr_b16 a[20:21], v116 offset:8192\n"
    "ds_read_b64_tr_b16 a[22:23], v116 offset:10240\n"
    "ds_read_b64_tr_b16 a[24:25], v116 offset:32\n"
    "ds_read_b64_tr_b16 a[26:27], v116 offset:2080\n"
    "ds_read_b64_tr_b16 a[28:29], v116 offset:8224\n"
    "ds_read_b64_tr_b16 a[30:31], v116 offset:10272\n"
    "s_waitcnt lgkmcnt(0)\n"
    "s_memrealtime s[42:43]\n"
    "s_waitcnt lgkmcnt(0)\n"
    "s_sub_u32 s42, s42, s51\n"
    "s_cmp_lt_i32 s42, 0\n"
    "s_cbranch_scc1 LCJ4_%=\n"
    "s_getpc_b64 s[52:53]\n"
    "s_branch LCA%=\n"
    "LCJ4_%=:\n"
    "s_waitcnt vmcnt(20)\n"
    "s_barrier\n"
    "s_lshl_b32 s38, s62, 1\n"
    "s_add_u32 m0, s38, 65536\n"
    "s_mov_b32 s39, 1572864\n"
    "buffer_load_dwordx4 v112, s[80:83], s39 offen lds\n"
    "s_add_u32 m0, s38, 66560\n"
    "s_mov_b32 s39, 1605632\n"
    "buffer_load_dwordx4 v112, s[80:83], s39 offen lds\n"
    "s_add_u32 m0, s38, 67584\n"
    "s_mov_b32 s39, 1638400\n"
    "buffer_load_dwordx4 v112, s[80:83], s39 offen lds\n"
    "s_add_u32 m0, s38, 68608\n"
    "s_mov_b32 s39, 1671168\n"
    "buffer_load_dwordx4 v112, s[80:83], s39 offen lds\n"
    "v_add_u32 v116, 81920, v114\n"
    "ds_read_b64_tr_b16 a[32:33], v116 offset:0\n"
    "ds_read_b64_tr_b16 a[34:35], v116 offset:2048\n"
    "ds_read_b64_tr_b16 a[36:37], v116 offset:8192\n"
    "ds_read_b64_tr_b16 a[38:39], v116 offset:10240\n"
    "ds_read_b64_tr_b16 a[40:41], v116 offset:32\n"
    "ds_read_b64_tr_b16 a[42:43], v116 offset:2080\n"
    "ds_read_b64_tr_b16 a[44:45], v116 offset:8224\n"
    "ds_read_b64_tr_b16 a[46:47], v116 offset:10272\n"
    "s_waitcnt lgkmcnt(0)\n"
    "s_memrealtime s[42:43]\n"
    "s_waitcnt lgkmcnt(0)\n"
    "s_sub_u32 s42, s42, s51\n"
    "s_cmp_lt_i32 s42, 0\n"
    "s_cbranch_scc1 LCJ5_%=\n"
    "s_getpc_b64 s[52:53]\n"
    "s_branch LCA%=\n"
    "LCJ5_%=:\n"
    "s_waitcnt vmcnt(20)\n"
    "s_barrier\n"
    "s_lshl_b32 s38, s62, 1\n"
    "s_add_u32 m0, s38, 81920\n"
    "s_mov_b32 s39, 2097152\n"
    "buffer_load_dwordx4 v112, s[80:83], s39 offen lds\n"
    "s_add_u32 m0, s38, 82944\n"
    "s_mov_b32 s39, 2129920\n"
    "buffer_load_dwordx4 v112, s[80:83], s39 offen lds\n"
    "s_add_u32 m0, s38, 83968\n"
    "s_mov_b32 s39, 2162688\n"
    "buffer_load_dwordx4 v112, s[80:83], s39 offen lds\n"
    "s_add_u32 m0, s38, 84992\n"
    "s_mov_b32 s39, 2195456\n"
    "buffer_load_dwordx4 v112, s[80:83], s39 offen lds\n"
    "v_add_u32 v116, 98304, v114\n"
    "ds_read_b64_tr_b16 a[48:49], v116 offset:0\n"
    "ds_read_b64_tr_b16 a[50:51], v116 offset:2048\n"
    "ds_read_b64_tr_b16 a[52:53], v116 offset:8192\n"
    "ds_read_b64_tr_b16 a[54:55], v116 offset:10240\n"
    "ds_read_b64_tr_b16 a[56:57], v116 offset:32\n"
    "ds_read_b64_tr_b16 a[58:59], v116 offset:2080\n"
    "ds_read_b64_tr_b16 a[60:61], v116 offset:8224\n"
    "ds_read_b64_tr_b16 a[62:63], v116 offset:10272\n"
    "s_waitcnt lgkmcnt(0)\n"
    "s_memrealtime s[42:43]\n"
    "s_waitcnt lgkmcnt(0)\n"
    "s_sub_u32 s42, s42, s51\n"
    "s_cmp_lt_i32 s42, 0\n"
    "s_cbranch_scc1 LCJ6_%=\n"
    "s_getpc_b64 s[52:53]\n"
    "s_branch LCA%=\n"
    "LCJ6_%=:\n"
    "s_waitcnt vmcnt(20)\n"
    "s_barrier\n"
    "s_lshl_b32 s38, s62, 1\n"
    "s_add_u32 m0, s38, 98304\n"
    "s_mov_b32 s39, 2621440\n"
    "buffer_load_dwordx4 v112, s[80:83], s39 offen lds\n"
    "s_add_u32 m0, s38, 99328\n"
    "s_mov_b32 s39, 2654208\n"
    "buffer_load_dwordx4 v112, s[80:83], s39 offen lds\n"
    "s_add_u32 m0, s38, 100352\n"
    "s_mov_b32 s39, 2686976\n"
    "buffer_load_dwordx4 v112, s[80:83], s39 offen lds\n"
    "s_add_u32 m0, s38, 101376\n"
    "s_mov_b32 s39, 2719744\n"
    "buffer_load_dwordx4 v112, s[80:83], s39 offen lds\n"
    "v_add_u32 v116, 0, v114\n"
    "ds_read_b64_tr_b16 a[64:65], v116 offset:0\n"
    "ds_read_b64_tr_b16 a[66:67], v116 offset:2048\n"
    "ds_read_b64_tr_b16 a[68:69], v116 offset:8192\n"
    "ds_read_b64_tr_b16 a[70:71], v116 offset:10240\n"
    "ds_read_b64_tr_b16 a[72:73], v116 offset:32\n"
    "ds_read_b64_tr_b16 a[74:75], v116 offset:2080\n"
    "ds_read_b64_tr_b16 a[76:77], v116 offset:8224\n"
    "ds_read_b64_tr_b16 a[78:79], v116 offset:10272\n"
    "s_waitcnt lgkmcnt(0)\n"
    "s_memrealtime s[42:43]\n"
    "s_waitcnt lgkmcnt(0)\n"
    "s_sub_u32 s42, s42, s51\n"
    "s_cmp_lt_i32 s42, 0\n"
    "s_cbranch_scc1 LCJ7_%=\n"
    "s_getpc_b64 s[52:53]\n"
    "s_branch LCA%=\n"
    "LCJ7_%=:\n"
    "s_waitcnt vmcnt(20)\n"
    "s_barrier\n"
    "s_lshl_b32 s38, s62, 1\n"
    "s_add_u32 m0, s38, 0\n"
    "s_mov_b32 s39, 3145728\n"
    "buffer_load_dwordx4 v112, s[80:83], s39 offen lds\n"
    "s_add_u32 m0, s38, 1024\n"
    "s_mov_b32 s39, 3178496\n"
    "buffer_load_dwordx4 v112, s[80:83], s39 offen lds\n"
    "s_add_u32 m0, s38, 2048\n"
    "s_mov_b32 s39, 3211264\n"
    "buffer_load_dwordx4 v112, s[80:83], s39 offen lds\n"
    "s_add_u32 m0, s38, 3072\n"
    "s_mov_b32 s39, 3244032\n"
    "buffer_load_dwordx4 v112, s[80:83], s39 offen lds\n"
    "v_add_u32 v116, 16384, v114\n"
    "ds_read_b64_tr_b16 v[40:41], v116 offset:0\n"
    "ds_read_b64_tr_b16 v[42:43], v116 offset:2048\n"
    "ds_read_b64_tr_b16 v[44:45], v116 offset:8192\n"
    "ds_read_b64_tr_b16 v[46:47], v116 offset:10240\n"
    "ds_read_b64_tr_b16 v[48:49], v116 offset:32\n"
    "ds_read_b64_tr_b16 v[50:51], v116 offset:2080\n"
    "ds_read_b64_tr_b16 v[52:53], v116 offset:8224\n"
    "ds_read_b64_tr_b16 v[54:55], v116 offset:10272\n"
    "s_waitcnt lgkmcnt(0)\n"
    "s_memrealtime s[42:43]\n"
    "s_waitcnt lgkmcnt(0)\n"
    "s_sub_u32 s42, s42, s51\n"
    "s_cmp_lt_i32 s42, 0\n"
    "s_cbranch_scc1 LCJ8_%=\n"
    "s_getpc_b64 s[52:53]\n"
    "s_branch LCA%=\n"
    "LCJ8_%=:\n"
    "s_waitcnt vmcnt(20)\n"
    "s_barrier\n"
    "s_lshl_b32 s38, s62, 1\n"
    "s_add_u32 m0, s38, 16384\n"
    "s_mov_b32 s39, 3670016\n"
    "buffer_load_dwordx4 v112, s[80:83], s39 offen lds\n"
    "s_add_u32 m0, s38, 17408\n"
    "s_mov_b32 s39, 3702784\n"
    "buffer_load_dwordx4 v112, s[80:83], s39 offen lds\n"
    "s_add_u32 m0, s38, 18432\n"
    "s_mov_b32 s39, 3735552\n"
    "buffer_load_dwordx4 v112, s[80:83], s39 offen lds\n"
    "s_add_u32 m0, s38, 19456\n"
    "s_mov_b32 s39, 3768320\n"
    "buffer_load_dwordx4 v112, s[80:83], s39 offen lds\n"
    "v_add_u32 v116, 32768, v114\n"
    "ds_read_b64_tr_b16 v[56:57], v116 offset:0\n"
    "ds_read_b64_tr_b16 v[58:59], v116 offset:2048\n"
    "ds_read_b64_tr_b16 v[60:61], v116 offset:8192\n"
    "ds_read_b64_tr_b16 v[62:63], v116 offset:10240\n"
    "ds_read_b64_tr_b16 v[64:65], v116 offset:32\n"
    "ds_read_b64_tr_b16 v[66:67], v116 offset:2080\n"
    "ds_read_b64_tr_b16 v[68:69], v116 offset:8224\n"
    "ds_read_b64_tr_b16 v[70:71], v116 offset:10272\n"
    "s_waitcnt lgkmcnt(0)\n"
    "s_memrealtime s[42:43]\n"
    "s_waitcnt lgkmcnt(0)\n"
    "s_sub_u32 s42, s42, s51\n"
    "s_cmp_lt_i32 s42, 0\n"
    "s_cbranch_scc1 LCJ9_%=\n"
    "s_getpc_b64 s[52:53]\n"
    "s_branch LCA%=\n"
    "LCJ9_%=:\n"
    "s_waitcnt vmcnt(20)\n"
    "s_barrier\n"
    "s_mov_b32 s80, s88\n"
    "s_mov_b32 s81, s89\n"
    "s_lshl_b32 s38, s62, 1\n"
    "s_add_u32 m0, s38, 32768\n"
    "s_mov_b32 s39, 0\n"
    "buffer_load_dwordx4 v112, s[80:83], s39 offen lds\n"
    "s_add_u32 m0, s38, 33792\n"
    "s_mov_b32 s39, 32768\n"
    "buffer_load_dwordx4 v112, s[80:83], s39 offen lds\n"
    "s_add_u32 m0, s38, 34816\n"
    "s_mov_b32 s39, 65536\n"
    "buffer_load_dwordx4 v112, s[80:83], s39 offen lds\n"
    "s_add_u32 m0, s38, 35840\n"
    "s_mov_b32 s39, 98304\n"
    "buffer_load_dwordx4 v112, s[80:83], s39 offen lds\n"
    "v_add_u32 v116, 49152, v114\n"
    "ds_read_b64_tr_b16 a[80:81], v116 offset:0\n"
    "ds_read_b64_tr_b16 a[82:83], v116 offset:2048\n"
    "ds_read_b64_tr_b16 a[84:85], v116 offset:8192\n"
    "ds_read_b64_tr_b16 a[86:87], v116 offset:10240\n"
    "ds_read_b64_tr_b16 a[88:89], v116 offset:32\n"
    "ds_read_b64_tr_b16 a[90:91], v116 offset:2080\n"
    "ds_read_b64_tr_b16 a[92:93], v116 offset:8224\n"
    "ds_read_b64_tr_b16 a[94:95], v116 offset:10272\n"
    "s_waitcnt lgkmcnt(0)\n"
    "s_memrealtime s[42:43]\n"
    "s_waitcnt lgkmcnt(0)\n"
    "s_sub_u32 s42, s42, s51\n"
    "s_cmp_lt_i32 s42, 0\n"
    "s_cbranch_scc1 LCJ10_%=\n"
    "s_getpc_b64 s[52:53]\n"
    "s_branch LCA%=\n"
    "LCJ10_%=:\n"
    "s_waitcnt vmcnt(20)\n"
    "s_barrier\n"
    "s_lshl_b32 s38, s62, 1\n"
    "s_add_u32 m0, s38, 49152\n"
    "s_mov_b32 s39, 524288\n"
    "buffer_load_dwordx4 v112, s[80:83], s39 offen lds\n"
    "s_add_u32 m0, s38, 50176\n"
    "s_mov_b32 s39, 557056\n"
    "buffer_load_dwordx4 v112, s[80:83], s39 offen lds\n"
    "s_add_u32 m0, s38, 51200\n"
    "s_mov_b32 s39, 589824\n"
    "buffer_load_dwordx4 v112, s[80:83], s39 offen lds\n"
    "s_add_u32 m0, s38, 52224\n"
    "s_mov_b32 s39, 622592\n"
    "buffer_load_dwordx4 v112, s[80:83], s39 offen lds\n"
    "v_add_u32 v116, 65536, v114\n"
    "ds_read_b64_tr_b16 a[96:97], v116 offset:0\n"
    "ds_read_b64_tr_b16 a[98:99], v116 offset:2048\n"
    "ds_read_b64_tr_b16 a[100:101], v116 offset:8192\n"
    "ds_read_b64_tr_b16 a[102:103], v116 offset:10240\n"
    "ds_read_b64_tr_b16 a[104:105], v116 offset:32\n"
    "ds_read_b64_tr_b16 a[106:107], v116 offset:2080\n"
    "ds_read_b64_tr_b16 a[108:109], v116 offset:8224\n"
    "ds_read_b64_tr_b16 a[110:111], v116 offset:10272\n"
    "s_waitcnt lgkmcnt(0)\n"
    "s_memrealtime s[42:43]\n"
    "s_waitcnt lgkmcnt(0)\n"
    "s_sub_u32 s42, s42, s51\n"
    "s_cmp_lt_i32 s42, 0\n"
    "s_cbranch_scc1 LCJ11_%=\n"
    "s_getpc_b64 s[52:53]\n"
    "s_branch LCA%=\n"
    "LCJ11_%=:\n"
    "s_waitcnt vmcnt(20)\n"
    "s_barrier\n"
    "s_lshl_b32 s38, s62, 1\n"
    "s_add_u32 m0, s38, 65536\n"
    "s_mov_b32 s39, 1048576\n"
    "buffer_load_dwordx4 v112, s[80:83], s39 offen lds\n"
    "s_add_u32 m0, s38, 66560\n"
    "s_mov_b32 s39, 1081344\n"
    "buffer_load_dwordx4 v112, s[80:83], s39 offen lds\n"
    "s_add_u32 m0, s38, 67584\n"
    "s_mov_b32 s39, 1114112\n"
    "buffer_load_dwordx4 v112, s[80:83], s39 offen lds\n"
    "s_add_u32 m0, s38, 68608\n"
    "s_mov_b32 s39, 1146880\n"
    "buffer_load_dwordx4 v112, s[80:83], s39 offen lds\n"
    "v_add_u32 v116, 81920, v114\n"
    "ds_read_b64_tr_b16 a[112:113], v116 offset:0\n"
    "ds_read_b64_tr_b16 a[114:115], v116 offset:2048\n"
    "ds_read_b64_tr_b16 a[116:117], v116 offset:8192\n"
    "ds_read_b64_tr_b16 a[118:119], v116 offset:10240\n"
    "ds_read_b64_tr_b16 a[120:121], v116 offset:32\n"
    "ds_read_b64_tr_b16 a[122:123], v116 offset:2080\n"
    "ds_read_b64_tr_b16 a[124:125], v116 offset:8224\n"
    "ds_read_b64_tr_b16 a[126:127], v116 offset:10272\n"
    "s_waitcnt lgkmcnt(0)\n"
    "s_memrealtime s[42:43]\n"
    "s_waitcnt lgkmcnt(0)\n"
    "s_sub_u32 s42, s42, s51\n"
    "s_cmp_lt_i32 s42, 0\n"
    "s_cbranch_scc1 LCJ12_%=\n"
    "s_getpc_b64 s[52:53]\n"
    "s_branch LCA%=\n"
    "LCJ12_%=:\n"
    "s_waitcnt vmcnt(20)\n"
    "s_barrier\n"
    "s_lshl_b32 s38, s62, 1\n"
    "s_add_u32 m0, s38, 81920\n"
    "s_mov_b32 s39, 1572864\n"
    "buffer_load_dwordx4 v112, s[80:83], s39 offen lds\n"
    "s_add_u32 m0, s38, 82944\n"
    "s_mov_b32 s39, 1605632\n"
    "buffer_load_dwordx4 v112, s[80:83], s39 offen lds\n"
    "s_add_u32 m0, s38, 83968\n"
    "s_mov_b32 s39, 1638400\n"
    "buffer_load_dwordx4 v112, s[80:83], s39 offen lds\n"
    "s_add_u32 m0, s38, 84992\n"
    "s_mov_b32 s39, 1671168\n"
    "buffer_load_dwordx4 v112, s[80:83], s39 offen lds\n"
    "v_add_u32 v116, 98304, v114\n"
    "ds_read_b64_tr_b16 a[128:129], v116 offset:0\n"
    "ds_read_b64_tr_b16 a[130:131], v116 offset:2048\n"
    "ds_read_b64_tr_b16 a[132:133], v116 offset:8192\n"
    "ds_read_b64_tr_b16 a[134:135], v116 offset:10240\n"
    "ds_read_b64_tr_b16 a[136:137], v116 offset:32\n"
    "ds_read_b64_tr_b16 a[138:139], v116 offset:2080\n"
    "ds_read_b64_tr_b16 a[140:141], v116 offset:8224\n"
    "ds_read_b64_tr_b16 a[142:143], v116 offset:10272\n"
    "s_waitcnt lgkmcnt(0)\n"
    "s_memrealtime s[42:43]\n"
    "s_waitcnt lgkmcnt(0)\n"
    "s_sub_u32 s42, s42, s51\n"
    "s_cmp_lt_i32 s42, 0\n"
    "s_cbranch_scc1 LCJ13_%=\n"
    "s_getpc_b64 s[52:53]\n"
    "s_branch LCA%=\n"
    "LCJ13_%=:\n"
    "s_waitcnt vmcnt(20)\n"
    "s_barrier\n"
    "s_lshl_b32 s38, s62, 1\n"
    "s_add_u32 m0, s38, 98304\n"
    "s_mov_b32 s39, 2097152\n"
    "buffer_load_dwordx4 v112, s[80:83], s39 offen lds\n"
    "s_add_u32 m0, s38, 99328\n"
    "s_mov_b32 s39, 2129920\n"
    "buffer_load_dwordx4 v112, s[80:83], s39 offen lds\n"
    "s_add_u32 m0, s38, 100352\n"
    "s_mov_b32 s39, 2162688\n"
    "buffer_load_dwordx4 v112, s[80:83], s39 offen lds\n"
    "s_add_u32 m0, s38, 101376\n"
    "s_mov_b32 s39, 2195456\n"
    "buffer_load_dwordx4 v112, s[80:83], s39 offen lds\n"
    "v_add_u32 v116, 0, v114\n"
    "ds_read_b64_tr_b16 a[144:145], v116 offset:0\n"
    "ds_read_b64_tr_b16 a[146:147], v116 offset:2048\n"
    "ds_read_b64_tr_b16 a[148:149], v116 offset:8192\n"
    "ds_read_b64_tr_b16 a[150:151], v116 offset:10240\n"
    "ds_read_b64_tr_b16 a[152:153], v116 offset:32\n"
    "ds_read_b64_tr_b16 a[154:155], v116 offset:2080\n"
    "ds_read_b64_tr_b16 a[156:157], v116 offset:8224\n"
    "ds_read_b64_tr_b16 a[158:159], v116 offset:10272\n"
    "s_waitcnt lgkmcnt(0)\n"
    "s_memrealtime s[42:43]\n"
    "s_waitcnt lgkmcnt(0)\n"
    "s_sub_u32 s42, s42, s51\n"
    "s_cmp_lt_i32 s42, 0\n"
    "s_cbranch_scc1 LCJ14_%=\n"
    "s_getpc_b64 s[52:53]\n"
    "s_branch LCA%=\n"
    "LCJ14_%=:\n"
    "s_waitcnt vmcnt(20)\n"
    "s_barrier\n"
    "s_lshl_b32 s38, s62, 1\n"
    "s_add_u32 m0, s38, 0\n"
    "s_mov_b32 s39, 2621440\n"
    "buffer_load_dwordx4 v112, s[80:83], s39 offen lds\n"
    "s_add_u32 m0, s38, 1024\n"
    "s_mov_b32 s39, 2654208\n"
    "buffer_load_dwordx4 v112, s[80:83], s39 offen lds\n"
    "s_add_u32 m0, s38, 2048\n"
    "s_mov_b32 s39, 2686976\n"
    "buffer_load_dwordx4 v112, s[80:83], s39 offen lds\n"
    "s_add_u32 m0, s38, 3072\n"
    "s_mov_b32 s39, 2719744\n"
    "buffer_load_dwordx4 v112, s[80:83], s39 offen lds\n"
    "v_add_u32 v116, 16384, v114\n"
    "ds_read_b64_tr_b16 a[160:161], v116 offset:0\n"
    "ds_read_b64_tr_b16 a[162:163], v116 offset:2048\n"
    "ds_read_b64_tr_b16 a[164:165], v116 offset:8192\n"
    "ds_read_b64_tr_b16 a[166:167], v116 offset:10240\n"
    "ds_read_b64_tr_b16 a[168:169], v116 offset:32\n"
    "ds_read_b64_tr_b16 a[170:171], v116 offset:2080\n"
    "ds_read_b64_tr_b16 a[172:173], v116 offset:8224\n"
    "ds_read_b64_tr_b16 a[174:175], v116 offset:10272\n"
    "s_waitcnt lgkmcnt(0)\n"
    "s_memrealtime s[42:43]\n"
    "s_waitcnt lgkmcnt(0)\n"
    "s_sub_u32 s42, s42, s51\n"
    "s_cmp_lt_i32 s42, 0\n"
    "s_cbranch_scc1 LCJ15_%=\n"
    "s_getpc_b64 s[52:53]\n"
    "s_branch LCA%=\n"
    "LCJ15_%=:\n"
    "s_waitcnt vmcnt(20)\n"
    "s_barrier\n"
    "s_lshl_b32 s38, s62, 1\n"
    "s_add_u32 m0, s38, 16384\n"
    "s_mov_b32 s39, 3145728\n"
    "buffer_load_dwordx4 v112, s[80:83], s39 offen lds\n"
    "s_add_u32 m0, s38, 17408\n"
    "s_mov_b32 s39, 3178496\n"
    "buffer_load_dwordx4 v112, s[80:83], s39 offen lds\n"
    "s_add_u32 m0, s38, 18432\n"
    "s_mov_b32 s39, 3211264\n"
    "buffer_load_dwordx4 v112, s[80:83], s39 offen lds\n"
    "s_add_u32 m0, s38, 19456\n"
    "s_mov_b32 s39, 3244032\n"
    "buffer_load_dwordx4 v112, s[80:83], s39 offen lds\n"
    "v_add_u32 v116, 32768, v114\n"
    "ds_read_b64_tr_b16 a[176:177], v116 offset:0\n"
    "ds_read_b64_tr_b16 a[178:179], v116 offset:2048\n"
    "ds_read_b64_tr_b16 a[180:181], v116 offset:8192\n"
    "ds_read_b64_tr_b16 a[182:183], v116 offset:10240\n"
    "ds_read_b64_tr_b16 a[184:185], v116 offset:32\n"
    "ds_read_b64_tr_b16 a[186:187], v116 offset:2080\n"
    "ds_read_b64_tr_b16 a[188:189], v116 offset:8224\n"
    "ds_read_b64_tr_b16 a[190:191], v116 offset:10272\n"
    "s_waitcnt lgkmcnt(0)\n"
    "s_memrealtime s[42:43]\n"
    "s_waitcnt lgkmcnt(0)\n"
    "s_sub_u32 s42, s42, s51\n"
    "s_cmp_lt_i32 s42, 0\n"
    "s_cbranch_scc1 LCJ16_%=\n"
    "s_getpc_b64 s[52:53]\n"
    "s_branch LCA%=\n"
    "LCJ16_%=:\n"
    "s_waitcnt vmcnt(20)\n"
    "s_barrier\n"
    "s_lshl_b32 s38, s62, 1\n"
    "s_add_u32 m0, s38, 32768\n"
    "s_mov_b32 s39, 3670016\n"
    "buffer_load_dwordx4 v112, s[80:83], s39 offen lds\n"
    "s_add_u32 m0, s38, 33792\n"
    "s_mov_b32 s39, 3702784\n"
    "buffer_load_dwordx4 v112, s[80:83], s39 offen lds\n"
    "s_add_u32 m0, s38, 34816\n"
    "s_mov_b32 s39, 3735552\n"
    "buffer_load_dwordx4 v112, s[80:83], s39 offen lds\n"
    "s_add_u32 m0, s38, 35840\n"
    "s_mov_b32 s39, 3768320\n"
    "buffer_load_dwordx4 v112, s[80:83], s39 offen lds\n"
    "v_add_u32 v116, 49152, v114\n"
    "ds_read_b64_tr_b16 a[192:193], v116 offset:0\n"
    "ds_read_b64_tr_b16 a[194:195], v116 offset:2048\n"
    "ds_read_b64_tr_b16 a[196:197], v116 offset:8192\n"
    "ds_read_b64_tr_b16 a[198:199], v116 offset:10240\n"
    "ds_read_b64_tr_b16 a[200:201], v116 offset:32\n"
    "ds_read_b64_tr_b16 a[202:203], v116 offset:2080\n"
    "ds_read_b64_tr_b16 a[204:205], v116 offset:8224\n"
    "ds_read_b64_tr_b16 a[206:207], v116 offset:10272\n"
    "s_waitcnt lgkmcnt(0)\n"
    "s_memrealtime s[42:43]\n"
    "s_waitcnt lgkmcnt(0)\n"
    "s_sub_u32 s42, s42, s51\n"
    "s_cmp_lt_i32 s42, 0\n"
    "s_cbranch_scc1 LCJ17_%=\n"
    "s_getpc_b64 s[52:53]\n"
    "s_branch LCA%=\n"
    "LCJ17_%=:\n"
    "s_waitcnt vmcnt(20)\n"
    "s_barrier\n"
    "s_mov_b32 s80, s90\n"
    "s_mov_b32 s81, s91\n"
    "s_lshl_b32 s38, s62, 1\n"
    "s_add_u32 m0, s38, 49152\n"
    "s_mov_b32 s39, 0\n"
    "buffer_load_dwordx4 v113, s[80:83], s39 offen lds\n"
    "s_add_u32 m0, s38, 50176\n"
    "s_mov_b32 s39, 1024\n"
    "buffer_load_dwordx4 v113, s[80:83], s39 offen lds\n"
    "s_add_u32 m0, s38, 51200\n"
    "s_mov_b32 s39, 4096\n"
    "buffer_load_dwordx4 v113, s[80:83], s39 offen lds\n"
    "s_add_u32 m0, s38, 52224\n"
    "s_mov_b32 s39, 5120\n"
    "buffer_load_dwordx4 v113, s[80:83], s39 offen lds\n"
    "v_add_u32 v116, 65536, v114\n"
    "ds_read_b64_tr_b16 a[208:209], v116 offset:0\n"
    "ds_read_b64_tr_b16 a[210:211], v116 offset:2048\n"
    "ds_read_b64_tr_b16 a[212:213], v116 offset:8192\n"
    "ds_read_b64_tr_b16 a[214:215], v116 offset:10240\n"
    "ds_read_b64_tr_b16 a[216:217], v116 offset:32\n"
    "ds_read_b64_tr_b16 a[218:219], v116 offset:2080\n"
    "ds_read_b64_tr_b16 a[220:221], v116 offset:8224\n"
    "ds_read_b64_tr_b16 a[222:223], v116 offset:10272\n"
    "s_waitcnt lgkmcnt(0)\n"
    "s_memrealtime s[42:43]\n"
    "s_waitcnt lgkmcnt(0)\n"
    "s_sub_u32 s42, s42, s51\n"
    "s_cmp_lt_i32 s42, 0\n"
    "s_cbranch_scc1 LCJ18_%=\n"
    "s_getpc_b64 s[52:53]\n"
    "s_branch LCA%=\n"
    "LCJ18_%=:\n"
    "s_waitcnt vmcnt(20)\n"
    "s_barrier\n"
    "s_lshl_b32 s38, s62, 1\n"
    "s_add_u32 m0, s38, 65536\n"
    "s_mov_b32 s39, 2048\n"
    "buffer_load_dwordx4 v113, s[80:83], s39 offen lds\n"
    "s_add_u32 m0, s38, 66560\n"
    "s_mov_b32 s39, 3072\n"
    "buffer_load_dwordx4 v113, s[80:83], s39 offen lds\n"
    "s_add_u32 m0, s38, 67584\n"
    "s_mov_b32 s39, 6144\n"
    "buffer_load_dwordx4 v113, s[80:83], s39 offen lds\n"
    "s_add_u32 m0, s38, 68608\n"
    "s_mov_b32 s39, 7168\n"
    "buffer_load_dwordx4 v113, s[80:83], s39 offen lds\n"
    "v_add_u32 v116, 81920, v114\n"
    "ds_read_b64_tr_b16 a[224:225], v116 offset:0\n"
    "ds_read_b64_tr_b16 a[226:227], v116 offset:2048\n"
    "ds_read_b64_tr_b16 a[228:229], v116 offset:8192\n"
    "ds_read_b64_tr_b16 a[230:231], v116 offset:10240\n"
    "ds_read_b64_tr_b16 a[232:233], v116 offset:32\n"
    "ds_read_b64_tr_b16 a[234:235], v116 offset:2080\n"
    "ds_read_b64_tr_b16 a[236:237], v116 offset:8224\n"
    "ds_read_b64_tr_b16 a[238:239], v116 offset:10272\n"
    "s_waitcnt lgkmcnt(0)\n"
    "s_memrealtime s[42:43]\n"
    "s_waitcnt lgkmcnt(0)\n"
    "s_sub_u32 s42, s42, s51\n"
    "s_cmp_lt_i32 s42, 0\n"
    "s_cbranch_scc1 LCJ19_%=\n"
    "s_getpc_b64 s[52:53]\n"
    "s_branch LCA%=\n"
    "LCJ19_%=:\n"
    "s_waitcnt vmcnt(20)\n"
    "s_barrier\n"
    "s_lshl_b32 s38, s62, 1\n"
    "s_add_u32 m0, s38, 81920\n"
    "s_mov_b32 s39, 32768\n"
    "buffer_load_dwordx4 v113, s[80:83], s39 offen lds\n"
    "s_add_u32 m0, s38, 82944\n"
    "s_mov_b32 s39, 33792\n"
    "buffer_load_dwordx4 v113, s[80:83], s39 offen lds\n"
    "s_add_u32 m0, s38, 83968\n"
    "s_mov_b32 s39, 36864\n"
    "buffer_load_dwordx4 v113, s[80:83], s39 offen lds\n"
    "s_add_u32 m0, s38, 84992\n"
    "s_mov_b32 s39, 37888\n"
    "buffer_load_dwordx4 v113, s[80:83], s39 offen lds\n"
    "v_add_u32 v116, 98304, v114\n"
    "ds_read_b64_tr_b16 a[240:241], v116 offset:0\n"
    "ds_read_b64_tr_b16 a[242:243], v116 offset:2048\n"
    "ds_read_b64_tr_b16 a[244:245], v116 offset:8192\n"
    "ds_read_b64_tr_b16 a[246:247], v116 offset:10240\n"
    "ds_read_b64_tr_b16 a[248:249], v116 offset:32\n"
    "ds_read_b64_tr_b16 a[250:251], v116 offset:2080\n"
    "ds_read_b64_tr_b16 a[252:253], v116 offset:8224\n"
    "ds_read_b64_tr_b16 a[254:255], v116 offset:10272\n"
    "s_waitcnt lgkmcnt(0)\n"
    "s_memrealtime s[42:43]\n"
    "s_waitcnt lgkmcnt(0)\n"
    "s_sub_u32 s42, s42, s51\n"
    "s_cmp_lt_i32 s42, 0\n"
    "s_cbranch_scc1 LCJ20_%=\n"
    "s_getpc_b64 s[52:53]\n"
    "s_branch LCA%=\n"
    "LCJ20_%=:\n"
    "s_waitcnt vmcnt(20)\n"
    "s_barrier\n"
    "s_lshl_b32 s38, s62, 1\n"
    "s_add_u32 m0, s38, 98304\n"
    "s_mov_b32 s39, 34816\n"
    "buffer_load_dwordx4 v113, s[80:83], s39 offen lds\n"
    "s_add_u32 m0, s38, 99328\n"
    "s_mov_b32 s39, 35840\n"
    "buffer_load_dwordx4 v113, s[80:83], s39 offen lds\n"
    "s_add_u32 m0, s38, 100352\n"
    "s_mov_b32 s39, 38912\n"
    "buffer_load_dwordx4 v113, s[80:83], s39 offen lds\n"
    "s_add_u32 m0, s38, 101376\n"
    "s_mov_b32 s39, 39936\n"
    "buffer_load_dwordx4 v113, s[80:83], s39 offen lds\n"
    "v_add_u32 v116, 0, v114\n"
    "ds_read_b64_tr_b16 v[120:121], v116 offset:0\n"
    "ds_read_b64_tr_b16 v[122:123], v116 offset:2048\n"
    "ds_read_b64_tr_b16 v[124:125], v116 offset:8192\n"
    "ds_read_b64_tr_b16 v[126:127], v116 offset:10240\n"
    "ds_read_b64_tr_b16 v[128:129], v116 offset:32\n"
    "ds_read_b64_tr_b16 v[130:131], v116 offset:2080\n"
    "ds_read_b64_tr_b16 v[132:133], v116 offset:8224\n"
    "ds_read_b64_tr_b16 v[134:135], v116 offset:10272\n"
    "s_waitcnt lgkmcnt(0)\n"
    "s_memrealtime s[42:43]\n"
    "s_waitcnt lgkmcnt(0)\n"
    "s_sub_u32 s42, s42, s51\n"
    "s_cmp_lt_i32 s42, 0\n"
    "s_cbranch_scc1 LCJ21_%=\n"
    "s_getpc_b64 s[52:53]\n"
    "s_branch LCA%=\n"
    "LCJ21_%=:\n"
    "s_waitcnt vmcnt(20)\n"
    "s_barrier\n"
    "s_lshl_b32 s38, s62, 1\n"
    "s_add_u32 m0, s38, 0\n"
    "s_mov_b32 s39, 65536\n"
    "buffer_load_dwordx4 v113, s[80:83], s39 offen lds\n"
    "s_add_u32 m0, s38, 1024\n"
    "s_mov_b32 s39, 66560\n"
    "buffer_load_dwordx4 v113, s[80:83], s39 offen lds\n"
    "s_add_u32 m0, s38, 2048\n"
    "s_mov_b32 s39, 69632\n"
    "buffer_load_dwordx4 v113, s[80:83], s39 offen lds\n"
    "s_add_u32 m0, s38, 3072\n"
    "s_mov_b32 s39, 70656\n"
    "buffer_load_dwordx4 v113, s[80:83], s39 offen lds\n"
    "v_add_u32 v116, 16384, v114\n"
    "ds_read_b64_tr_b16 v[136:137], v116 offset:0\n"
    "ds_read_b64_tr_b16 v[138:139], v116 offset:2048\n"
    "ds_read_b64_tr_b16 v[140:141], v116 offset:8192\n"
    "ds_read_b64_tr_b16 v[142:143], v116 offset:10240\n"
    "ds_read_b64_tr_b16 v[144:145], v116 offset:32\n"
    "ds_read_b64_tr_b16 v[146:147], v116 offset:2080\n"
    "ds_read_b64_tr_b16 v[148:149], v116 offset:8224\n"
    "ds_read_b64_tr_b16 v[150:151], v116 offset:10272\n"
    "s_waitcnt lgkmcnt(0)\n"
    "s_memrealtime s[42:43]\n"
    "s_waitcnt lgkmcnt(0)\n"
    "s_sub_u32 s42, s42, s51\n"
    "s_cmp_lt_i32 s42, 0\n"
    "s_cbranch_scc1 LCJ22_%=\n"
    "s_getpc_b64 s[52:53]\n"
    "s_branch LCA%=\n"
    "LCJ22_%=:\n"
    "s_waitcnt vmcnt(20)\n"
    "s_barrier\n"
    "s_lshl_b32 s38, s62, 1\n"
    "s_add_u32 m0, s38, 16384\n"
    "s_mov_b32 s39, 67584\n"
    "buffer_load_dwordx4 v113, s[80:83], s39 offen lds\n"
    "s_add_u32 m0, s38, 17408\n"
    "s_mov_b32 s39, 68608\n"
    "buffer_load_dwordx4 v113, s[80:83], s39 offen lds\n"
    "s_add_u32 m0, s38, 18432\n"
    "s_mov_b32 s39, 71680\n"
    "buffer_load_dwordx4 v113, s[80:83], s39 offen lds\n"
    "s_add_u32 m0, s38, 19456\n"
    "s_mov_b32 s39, 72704\n"
    "buffer_load_dwordx4 v113, s[80:83], s39 offen lds\n"
    "v_add_u32 v116, 32768, v114\n"
    "ds_read_b64_tr_b16 v[152:153], v116 offset:0\n"
    "ds_read_b64_tr_b16 v[154:155], v116 offset:2048\n"
    "ds_read_b64_tr_b16 v[156:157], v116 offset:8192\n"
    "ds_read_b64_tr_b16 v[158:159], v116 offset:10240\n"
    "ds_read_b64_tr_b16 v[160:161], v116 offset:32\n"
    "ds_read_b64_tr_b16 v[162:163], v116 offset:2080\n"
    "ds_read_b64_tr_b16 v[164:165], v116 offset:8224\n"
    "ds_read_b64_tr_b16 v[166:167], v116 offset:10272\n"
    "s_waitcnt lgkmcnt(0)\n"
    "s_memrealtime s[42:43]\n"
    "s_waitcnt lgkmcnt(0)\n"
    "s_sub_u32 s42, s42, s51\n"
    "s_cmp_lt_i32 s42, 0\n"
    "s_cbranch_scc1 LCJ23_%=\n"
    "s_getpc_b64 s[52:53]\n"
    "s_branch LCA%=\n"
    "LCJ23_%=:\n"
    "s_waitcnt vmcnt(20)\n"
    "s_barrier\n"
    "s_lshl_b32 s38, s62, 1\n"
    "s_add_u32 m0, s38, 32768\n"
    "s_mov_b32 s39, 98304\n"
    "buffer_load_dwordx4 v113, s[80:83], s39 offen lds\n"
    "s_add_u32 m0, s38, 33792\n"
    "s_mov_b32 s39, 99328\n"
    "buffer_load_dwordx4 v113, s[80:83], s39 offen lds\n"
    "s_add_u32 m0, s38, 34816\n"
    "s_mov_b32 s39, 102400\n"
    "buffer_load_dwordx4 v113, s[80:83], s39 offen lds\n"
    "s_add_u32 m0, s38, 35840\n"
    "s_mov_b32 s39, 103424\n"
    "buffer_load_dwordx4 v113, s[80:83], s39 offen lds\n"
    "v_add_u32 v116, 49152, v115\n"
    "ds_read_b64_tr_b16 v[168:169], v116 offset:0\n"
    "ds_read_b64_tr_b16 v[184:185], v116 offset:8\n"
    "ds_read_b64_tr_b16 v[200:201], v116 offset:16\n"
    "ds_read_b64_tr_b16 v[216:217], v116 offset:24\n"
    "ds_read_b64_tr_b16 v[232:233], v116 offset:32\n"
    "ds_read_b64_tr_b16 v[248:249], v116 offset:40\n"
    "ds_read_b64_tr_b16 v[80:81], v116 offset:48\n"
    "ds_read_b64_tr_b16 v[96:97], v116 offset:56\n"
    "s_waitcnt lgkmcnt(0)\n"
    "s_memrealtime s[42:43]\n"
    "s_waitcnt lgkmcnt(0)\n"
    "s_sub_u32 s42, s42, s51\n"
    "s_cmp_lt_i32 s42, 0\n"
    "s_cbranch_scc1 LCJ24_%=\n"
    "s_getpc_b64 s[52:53]\n"
    "s_branch LCA%=\n"
    "LCJ24_%=:\n"
    "s_waitcnt vmcnt(20)\n"
    "s_barrier\n"
    "s_lshl_b32 s38, s62, 1\n"
    "s_add_u32 m0, s38, 49152\n"
    "s_mov_b32 s39, 100352\n"
    "buffer_load_dwordx4 v113, s[80:83], s39 offen lds\n"
    "s_add_u32 m0, s38, 50176\n"
    "s_mov_b32 s39, 101376\n"
    "buffer_load_dwordx4 v113, s[80:83], s39 offen lds\n"
    "s_add_u32 m0, s38, 51200\n"
    "s_mov_b32 s39, 104448\n"
    "buffer_load_dwordx4 v113, s[80:83], s39 offen lds\n"
    "s_add_u32 m0, s38, 52224\n"
    "s_mov_b32 s39, 105472\n"
    "buffer_load_dwordx4 v113, s[80:83], s39 offen lds\n"
    "v_add_u32 v116, 65536, v115\n"
    "ds_read_b64_tr_b16 v[170:171], v116 offset:0\n"
    "ds_read_b64_tr_b16 v[186:187], v116 offset:8\n"
    "ds_read_b64_tr_b16 v[202:203], v116 offset:16\n"
    "ds_read_b64_tr_b16 v[218:219], v116 offset:24\n"
    "ds_read_b64_tr_b16 v[234:235], v116 offset:32\n"
    "ds_read_b64_tr_b16 v[250:251], v116 offset:40\n"
    "ds_read_b64_tr_b16 v[82:83], v116 offset:48\n"
    "ds_read_b64_tr_b16 v[98:99], v116 offset:56\n"
    "s_waitcnt lgkmcnt(0)\n"
    "s_memrealtime s[42:43]\n"
    "s_waitcnt lgkmcnt(0)\n"
    "s_sub_u32 s42, s42, s51\n"
    "s_cmp_lt_i32 s42, 0\n"
    "s_cbranch_scc1 LCJ25_%=\n"
    "s_getpc_b64 s[52:53]\n"
    "s_branch LCA%=\n"
    "LCJ25_%=:\n"
    "s_waitcnt vmcnt(20)\n"
    "s_barrier\n"
    "v_add_u32 v116, 81920, v115\n"
    "ds_read_b64_tr_b16 v[172:173], v116 offset:0\n"
    "ds_read_b64_tr_b16 v[188:189], v116 offset:8\n"
    "ds_read_b64_tr_b16 v[204:205], v116 offset:16\n"
    "ds_read_b64_tr_b16 v[220:221], v116 offset:24\n"
    "ds_read_b64_tr_b16 v[236:237], v116 offset:32\n"
    "ds_read_b64_tr_b16 v[252:253], v116 offset:40\n"
    "ds_read_b64_tr_b16 v[84:85], v116 offset:48\n"
    "ds_read_b64_tr_b16 v[100:101], v116 offset:56\n"
    "s_waitcnt lgkmcnt(0)\n"
    "s_memrealtime s[42:43]\n"
    "s_waitcnt lgkmcnt(0)\n"
    "s_sub_u32 s42, s42, s51\n"
    "s_cmp_lt_i32 s42, 0\n"
    "s_cbranch_scc1 LCJ26_%=\n"
    "s_getpc_b64 s[52:53]\n"
    "s_branch LCA%=\n"
    "LCJ26_%=:\n"
    "s_waitcnt vmcnt(16)\n"
    "s_barrier\n"
    "v_add_u32 v116, 98304, v115\n"
    "ds_read_b64_tr_b16 v[174:175], v116 offset:0\n"
    "ds_read_b64_tr_b16 v[190:191], v116 offset:8\n"
    "ds_read_b64_tr_b16 v[206:207], v116 offset:16\n"
    "ds_read_b64_tr_b16 v[222:223], v116 offset:24\n"
    "ds_read_b64_tr_b16 v[238:239], v116 offset:32\n"
    "ds_read_b64_tr_b16 v[254:255], v116 offset:40\n"
    "ds_read_b64_tr_b16 v[86:87], v116 offset:48\n"
    "ds_read_b64_tr_b16 v[102:103], v116 offset:56\n"
    "s_waitcnt lgkmcnt(0)\n"
    "s_memrealtime s[42:43]\n"
    "s_waitcnt lgkmcnt(0)\n"
    "s_sub_u32 s42, s42, s51\n"
    "s_cmp_lt_i32 s42, 0\n"
    "s_cbranch_scc1 LCJ27_%=\n"
    "s_getpc_b64 s[52:53]\n"
    "s_branch LCA%=\n"
    "LCJ27_%=:\n"
    "s_waitcnt vmcnt(12)\n"
    "s_barrier\n"
    "v_add_u32 v116, 0, v115\n"
    "ds_read_b64_tr_b16 v[176:177], v116 offset:0\n"
    "ds_read_b64_tr_b16 v[192:193], v116 offset:8\n"
    "ds_read_b64_tr_b16 v[208:209], v116 offset:16\n"
    "ds_read_b64_tr_b16 v[224:225], v116 offset:24\n"
    "ds_read_b64_tr_b16 v[240:241], v116 offset:32\n"
    "ds_read_b64_tr_b16 v[72:73], v116 offset:40\n"
    "ds_read_b64_tr_b16 v[88:89], v116 offset:48\n"
    "ds_read_b64_tr_b16 v[104:105], v116 offset:56\n"
    "s_waitcnt lgkmcnt(0)\n"
    "s_memrealtime s[42:43]\n"
    "s_waitcnt lgkmcnt(0)\n"
    "s_sub_u32 s42, s42, s51\n"
    "s_cmp_lt_i32 s42, 0\n"
    "s_cbranch_scc1 LCJ28_%=\n"
    "s_getpc_b64 s[52:53]\n"
    "s_branch LCA%=\n"
    "LCJ28_%=:\n"
    "s_waitcnt vmcnt(8)\n"
    "s_barrier\n"
    "v_add_u32 v116, 16384, v115\n"
    "ds_read_b64_tr_b16 v[178:179], v116 offset:0\n"
    "ds_read_b64_tr_b16 v[194:195], v116 offset:8\n"
    "ds_read_b64_tr_b16 v[210:211], v116 offset:16\n"
    "ds_read_b64_tr_b16 v[226:227], v116 offset:24\n"
    "ds_read_b64_tr_b16 v[242:243], v116 offset:32\n"
    "ds_read_b64_tr_b16 v[74:75], v116 offset:40\n"
    "ds_read_b64_tr_b16 v[90:91], v116 offset:48\n"
    "ds_read_b64_tr_b16 v[106:107], v116 offset:56\n"
    "s_waitcnt lgkmcnt(0)\n"
    "s_memrealtime s[42:43]\n"
    "s_waitcnt lgkmcnt(0)\n"
    "s_sub_u32 s42, s42, s51\n"
    "s_cmp_lt_i32 s42, 0\n"
    "s_cbranch_scc1 LCJ29_%=\n"
    "s_getpc_b64 s[52:53]\n"
    "s_branch LCA%=\n"
    "LCJ29_%=:\n"
    "s_waitcnt vmcnt(4)\n"
    "s_barrier\n"
    "v_add_u32 v116, 32768, v115\n"
    "ds_read_b64_tr_b16 v[180:181], v116 offset:0\n"
    "ds_read_b64_tr_b16 v[196:197], v116 offset:8\n"
    "ds_read_b64_tr_b16 v[212:213], v116 offset:16\n"
    "ds_read_b64_tr_b16 v[228:229], v116 offset:24\n"
    "ds_read_b64_tr_b16 v[244:245], v116 offset:32\n"
    "ds_read_b64_tr_b16 v[76:77], v116 offset:40\n"
    "ds_read_b64_tr_b16 v[92:93], v116 offset:48\n"
    "ds_read_b64_tr_b16 v[108:109], v116 offset:56\n"
    "s_waitcnt lgkmcnt(0)\n"
    "s_memrealtime s[42:43]\n"
    "s_waitcnt lgkmcnt(0)\n"
    "s_sub_u32 s42, s42, s51\n"
    "s_cmp_lt_i32 s42, 0\n"
    "s_cbranch_scc1 LCJ30_%=\n"
    "s_getpc_b64 s[52:53]\n"
    "s_branch LCA%=\n"
    "LCJ30_%=:\n"
    "s_waitcnt vmcnt(0)\n"
    "s_barrier\n"
    "v_add_u32 v116, 49152, v115\n"
    "ds_read_b64_tr_b16 v[182:183], v116 offset:0\n"
    "ds_read_b64_tr_b16 v[198:199], v116 offset:8\n"
    "ds_read_b64_tr_b16 v[214:215], v116 offset:16\n"
    "ds_read_b64_tr_b16 v[230:231], v116 offset:24\n"
    "ds_read_b64_tr_b16 v[246:247], v116 offset:32\n"
    "ds_read_b64_tr_b16 v[78:79], v116 offset:40\n"
    "ds_read_b64_tr_b16 v[94:95], v116 offset:48\n"
    "ds_read_b64_tr_b16 v[110:111], v116 offset:56\n"
    "s_waitcnt lgkmcnt(0)\n"
    "s_memrealtime s[42:43]\n"
    "s_waitcnt lgkmcnt(0)\n"
    "s_sub_u32 s42, s42, s51\n"
    "s_cmp_lt_i32 s42, -536870912\n"
    "s_cbranch_scc1 LCJ31_%=\n"
    "s_getpc_b64 s[52:53]\n"
    "s_branch LCA%=\n"
    "LCJ31_%=:\n"
    "s_barrier\n"
    "ds_write_b128 v7, v[16:19] offset:6144\n"
    "ds_write_b128 v7, v[20:23] offset:7168\n"
    "ds_write_b128 v7, v[24:27] offset:8192\n"
    "ds_write_b128 v7, v[28:31] offset:9216\n"
    "ds_write_b128 v7, v[32:35] offset:10240\n"
    "ds_write_b128 v7, v[36:39] offset:11264\n"
    "ds_write_b128 v7, v[40:43] offset:12288\n"
    "ds_write_b128 v7, v[44:47] offset:13312\n"
    "ds_write_b128 v7, v[48:51] offset:14336\n"
    "ds_write_b128 v7, v[52:55] offset:15360\n"
    "ds_write_b128 v7, v[56:59] offset:16384\n"
    "ds_write_b128 v7, v[60:63] offset:17408\n"
    "ds_write_b128 v7, v[64:67] offset:18432\n"
    "ds_write_b128 v7, v[68:71] offset:19456\n"
    "ds_write_b128 v7, v[72:75] offset:20480\n"
    "s_waitcnt lgkmcnt(11)\n"
    "ds_write_b128 v7, v[76:79] offset:21504\n"
    "ds_write_b128 v7, v[80:83] offset:22528\n"
    "ds_write_b128 v7, v[84:87] offset:23552\n"
    "ds_write_b128 v7, v[88:91] offset:24576\n"
    "s_waitcnt lgkmcnt(11)\n"
    "ds_write_b128 v7, v[92:95] offset:25600\n"
    "ds_write_b128 v7, v[96:99] offset:26624\n"
    "ds_write_b128 v7, v[100:103] offset:27648\n"
    "ds_write_b128 v7, v[104:107] offset:28672\n"
    "s_waitcnt lgkmcnt(11)\n"
    "ds_write_b128 v7, v[108:111] offset:29696\n"
    "s_waitcnt lgkmcnt(0)\n"
    "ds_read_b128 v[16:19], v9 offset:0\n"
    "ds_read_b128 v[20:23], v9 offset:1024\n"
    "ds_read_b128 v[24:27], v9 offset:8192\n"
    "ds_read_b128 v[28:31], v9 offset:9216\n"
    "ds_read_b128 v[32:35], v9 offset:16384\n"
    "ds_read_b128 v[36:39], v9 offset:17408\n"
    "s_waitcnt lgkmcnt(5)\n"
    "ds_write_b128 v7, v[16:19] offset:0\n"
    "s_waitcnt lgkmcnt(5)\n"
    "ds_write_b128 v7, v[20:23] offset:1024\n"
    "s_waitcnt lgkmcnt(5)\n"
    "ds_write_b128 v7, v[24:27] offset:2048\n"
    "s_waitcnt lgkmcnt(5)\n"
    "ds_write_b128 v7, v[28:31] offset:3072\n"
    "s_waitcnt lgkmcnt(5)\n"
    "ds_write_b128 v7, v[32:35] offset:4096\n"
    "s_waitcnt lgkmcnt(5)\n"
    "ds_write_b128 v7, v[36:39] offset:5120\n"
    "v_lshrrev_b32 v64, 4, v0\n"
    "v_lshrrev_b32 v65, 4, v64\n"
    "v_and_b32 v64, 15, v64\n"
    "v_lshlrev_b32 v65, 2, v65\n"
    "v_sub_u32 v64, v64, v65\n"
    "s_and_b32 s38, s61, 1\n"
    "s_lshl_b32 s38, s38, 4\n"
    "v_add_u32 v64, s38, v64\n"
    "v_cmp_le_i32 s[80:81], 0, v64\n"
    "v_cmp_le_i32 s[82:83], 1, v64\n"
    "v_cmp_le_i32 s[84:85], 2, v64\n"
    "v_cmp_le_i32 s[86:87], 3, v64\n"
    "v_cmp_le_i32 s[88:89], 16, v64\n"
    "v_cmp_le_i32 s[90:91], 17, v64\n"
    "v_cmp_le_i32 s[92:93], 18, v64\n"
    "v_cmp_le_i32 s[94:95], 19, v64\n"
    "v_mov_b32 v64, 158224\n"
    "v_mov_b32 v65, 1\n"
    "ds_write_b32 v64, v65\n"
    "v_mov_b64 v[66:67], 0\n"
    "ds_write_b64 v64, v[66:67] offset:16\n"
    "s_mov_b32 s54, 0x80000000\n"
    "s_mov_b64 exec, 0xff\n"
    "buffer_load_dword v116, v0, s[16:19], s59 offen sc1\n"
    "s_mov_b64 exec, -1\n"
    "s_waitcnt vmcnt(0)\n"
    "v_cmp_ne_u32 vcc, 0, v116\n"
    "v_readfirstlane_b32 s70, v116\n"
    "s_lshr_b32 s70, s70, 16\n"
    "s_cmp_gt_i32 s70, s68\n"
    "s_cselect_b32 s39, 1, 0\n"
    "s_cselect_b32 s38, 0x77, 7\n"
    "s_andn2_b32 s38, s38, vcc_lo\n"
    "s_cbranch_scc0 LS0K%=\n"
    "s_mov_b32 s37, 0x80000\n"
    "LS0W%=:\n"
    "s_mov_b64 exec, 0xff\n"
    "buffer_load_dword v116, v0, s[16:19], s59 offen sc1\n"
    "s_mov_b64 exec, -1\n"
    "s_waitcnt vmcnt(0)\n"
    "v_cmp_ne_u32 vcc, 0, v116\n"
    "v_readfirstlane_b32 s70, v116\n"
    "s_lshr_b32 s70, s70, 16\n"
    "s_cmp_gt_i32 s70, s68\n"
    "s_cselect_b32 s39, 1, 0\n"
    "s_cselect_b32 s38, 0x77, 7\n"
    "s_andn2_b32 s38, s38, vcc_lo\n"
    "s_cbranch_scc0 LS0K%=\n"
    "s_sleep 2\n"
    "s_sub_u32 s37, s37, 1\n"
    "s_cbranch_scc0 LS0W%=\n"
    "s_branch LFAIL%=\n"
    "LS0K%=:\n"
    "s_max_i32 s68, s68, s70\n"
    "s_and_b32 s53, s36, 7\n"
    "s_mul_i32 s53, s53, 1572864\n"
    "s_add_u32 s53, s53, s69\n"
    "s_cmp_lg_u32 s39, 0\n"
    "s_cselect_b32 s53, s53, s50\n"
    "s_cselect_b32 s49, s67, s48\n"
    "s_add_u32 s53, s53, s55\n"
    "s_add_u32 s67, s67, 16384\n"
    "s_add_u32 s49, s49, s62\n"
    "s_add_u32 s59, s59, 128\n"
    "s_add_u32 s51, s53, 0\n"
    "s_lshr_b32 s38, s51, 8\n"
    "s_xor_b32 s38, s38, s56\n"
    "s_and_b32 s38, s38, 0x2000\n"
    "s_or_b32 s51, s51, s38\n"
    "s_add_u32 s52, s53, 524288\n"
    "s_lshr_b32 s38, s52, 8\n"
    "s_xor_b32 s38, s38, s56\n"
    "s_and_b32 s38, s38, 0x2000\n"
    "s_or_b32 s52, s52, s38\n"
    "s_add_u32 s53, s53, 1048576\n"
    "s_lshr_b32 s38, s53, 8\n"
    "s_xor_b32 s38, s38, s56\n"
    "s_and_b32 s38, s38, 0x2000\n"
    "s_or_b32 s53, s53, s38\n"
    "s_waitcnt lgkmcnt(0)\n"
    "s_barrier\n"
    "s_cmp_lg_u32 s61, 1\n"
    "s_cbranch_scc1 LBM%=\n"
    "s_sub_u32 s51, s51, 2048\n"
    "s_mov_b32 m0, 122880\n"
    "s_nop 0\n"
    "buffer_load_dwordx4 v0, s[16:19], s51 offen offset:0 sc1 lds\n"
    "buffer_load_dwordx4 v0, s[16:19], s51 offen offset:1024 sc1 lds\n"
    "buffer_load_dwordx4 v0, s[16:19], s51 offen offset:2048 sc1 lds\n"
    "buffer_load_dwordx4 v0, s[16:19], s51 offen offset:3072 sc1 lds\n"
    "s_mov_b32 m0, 126976\n"
    "s_add_u32 s51, s51, 4096\n"
    "buffer_load_dwordx4 v0, s[16:19], s51 offen offset:0 sc1 lds\n"
    "buffer_load_dwordx4 v0, s[16:19], s51 offen offset:1024 sc1 lds\n"
    "buffer_load_dwordx4 v0, s[16:19], s51 offen offset:2048 sc1 lds\n"
    "buffer_load_dwordx4 v0, s[16:19], s51 offen offset:3072 sc1 lds\n"
    "LBM%=:\n"
    "s_waitcnt lgkmcnt(0)\n"
    "s_barrier\n"
    "s_add_u32 m0, s62, 147456\n"
    "s_nop 0\n"
    "buffer_load_dwordx4 v0, s[16:19], s49 offen offset:0 sc1 lds\n"
    "buffer_load_dwordx4 v0, s[16:19], s49 offen offset:1024 sc1 lds\n"
    "v_mov_b32 v112, 158224\n"
    "ds_read_b128 v[16:19], v9 offset:16384\n"
    "ds_read_b128 v[20:23], v9 offset:17408\n"
    "ds_read_b32 v112, v112 offset:0\n"
    "s_nop 3\n"
    "s_add_u32 m0, s62, 131072\n"
    "s_nop 0\n"
    "buffer_load_dwordx4 v0, s[16:19], s52 offen offset:0 sc1 lds\n"
    "buffer_load_dwordx4 v0, s[16:19], s52 offen offset:1024 sc1 lds\n"
    "v_lshrrev_b32 v113, 4, v0\n"
    "v_and_b32 v113, 0x30, v113\n"
    "v_lshl_add_u32 v113, v113, 4, v0\n"
    "s_waitcnt lgkmcnt(0)\n"
    "v_readfirstlane_b32 s39, v112\n"
    "ds_read_b128 v[64:67], v7 offset:0\n"
    "ds_read_b128 v[68:71], v7 offset:1024\n"
    "ds_read_b128 v[72:75], v7 offset:2048\n"
    "ds_read_b128 v[76:79], v7 offset:3072\n"
    "ds_read_b128 v[80:83], v7 offset:4096\n"
    "ds_read_b128 v[84:87], v7 offset:5120\n"
    "ds_read_b128 v[88:91], v7 offset:6144\n"
    "ds_read_b128 v[92:95], v7 offset:7168\n"
    "ds_read_b128 v[96:99], v7 offset:8192\n"
    "ds_read_b128 v[100:103], v7 offset:9216\n"
    "ds_read_b128 v[104:107], v7 offset:10240\n"
    "ds_read_b128 v[108:111], v7 offset:11264\n"
    "v_mov_b64 v[114:115], 0\n"
    "s_nop 1\n"
    "v_mfma_f32_32x32x8_bf16 v[48:63], v[114:115], v[114:115], 0\n"
    "buffer_store_dwordx4 v[16:19], v113, s[16:19], s54 offen offset:0 sc1\n"
    "buffer_store_dwordx4 v[20:23], v113, s[16:19], s54 offen offset:2048 sc1\n"
    "s_cmp_lg_u32 s39, 0\n"
    "s_cbranch_scc1 LRS%=\n"
    "s_waitcnt vmcnt(4)\n"
    "s_waitcnt lgkmcnt(0)\n"
    "LC%=:\n"
    "s_barrier\n"
    "ds_read_b128 v[16:19], v2 offset:0\n"
    "ds_read_b128 v[20:23], v2 offset:1024\n"
    "ds_read_b128 v[24:27], v2 offset:2048\n"
    "s_add_u32 m0, s62, 139264\n"
    "s_nop 0\n"
    "buffer_load_dwordx4 v0, s[16:19], s53 offen offset:0 sc1 lds\n"
    "buffer_load_dwordx4 v0, s[16:19], s53 offen offset:1024 sc1 lds\n"
    "s_waitcnt lgkmcnt(2)\n"
    "v_smfmac_f32_16x16x64_bf16 v[48:51], v[16:19], v[64:71], v1\n"
    "ds_read_b128 v[28:31], v2 offset:3072\n"
    "v_smfmac_f32_16x16x64_bf16 v[52:55], v[16:19], v[72:79], v1\n"
    "ds_read_b128 v[32:35], v2 offset:4096\n"
    "s_waitcnt lgkmcnt(3)\n"
    "v_smfmac_f32_16x16x64_bf16 v[48:51], v[20:23], v[80:87], v1\n"
    "ds_read_b128 v[36:39], v2 offset:5120\n"
    "v_smfmac_f32_16x16x64_bf16 v[52:55], v[20:23], v[88:95], v1\n"
    "ds_read_b128 v[40:43], v2 offset:6144\n"
    "s_waitcnt lgkmcnt(4)\n"
    "v_smfmac_f32_16x16x64_bf16 v[48:51], v[24:27], v[96:103], v1\n"
    "ds_read_b128 v[44:47], v2 offset:7168\n"
    "v_smfmac_f32_16x16x64_bf16 v[52:55], v[24:27], v[104:111], v1\n"
    "ds_read_b128 v[64:67], v7 offset:12288\n"
    "s_waitcnt lgkmcnt(5)\n"
    "v_smfmac_f32_16x16x64_bf16 v[48:51], v[28:31], a[0:7], v1\n"
    "ds_read_b128 v[68:71], v7 offset:13312\n"
    "v_max3_u32 v112, v19, v23, v27\n"
    "v_smfmac_f32_16x16x64_bf16 v[52:55], v[28:31], a[8:15], v1\n"
    "ds_read_b128 v[72:75], v7 offset:14336\n"
    "s_waitcnt lgkmcnt(6)\n"
    "v_smfmac_f32_16x16x64_bf16 v[48:51], v[32:35], a[16:23], v1\n"
    "ds_read_b128 v[76:79], v7 offset:15360\n"
    "v_smfmac_f32_16x16x64_bf16 v[52:55], v[32:35], a[24:31], v1\n"
    "ds_read_b128 v[80:83], v7 offset:16384\n"
    "s_waitcnt lgkmcnt(7)\n"
    "v_smfmac_f32_16x16x64_bf16 v[48:51], v[36:39], a[32:39], v1\n"
    "ds_read_b128 v[84:87], v7 offset:17408\n"
    "v_max3_u32 v112, v112, v31, v35\n"
    "v_smfmac_f32_16x16x64_bf16 v[52:55], v[36:39], a[40:47], v1\n"
    "ds_read_b128 v[88:91], v7 offset:18432\n"
    "s_waitcnt lgkmcnt(8)\n"
    "v_smfmac_f32_16x16x64_bf16 v[48:51], v[40:43], a[48:55], v1\n"
    "ds_read_b128 v[92:95], v7 offset:19456\n"
    "v_max3_u32 v112, v112, v39, v43\n"
    "v_smfmac_f32_16x16x64_bf16 v[52:55], v[40:43], a[56:63], v1\n"
    "s_waitcnt lgkmcnt(8)\n"
    "v_smfmac_f32_16x16x64_bf16 v[48:51], v[44:47], a[64:71], v1\n"
    "v_max_u32 v112, v112, v47\n"
    "v_smfmac_f32_16x16x64_bf16 v[52:55], v[44:47], a[72:79], v1\n"
    "s_waitcnt lgkmcnt(6)\n"
    "v_smfmac_f32_16x16x64_bf16 v[56:59], v[16:19], v[64:71], v1\n"
    "s_waitcnt lgkmcnt(4)\n"
    "v_cmp_eq_u32 vcc, -1, v112\n"
    "v_smfmac_f32_16x16x64_bf16 v[60:63], v[16:19], v[72:79], v1\n"
    "s_waitcnt lgkmcnt(2)\n"
    "v_add_f32 v48, v48, v50\n"
    "v_add_f32 v52, v52, v54\n"
    "v_smfmac_f32_16x16x64_bf16 v[56:59], v[20:23], v[80:87], v1\n"
    "s_waitcnt lgkmcnt(0)\n"
    "v_add_f32 v49, v49, v51\n"
    "v_add_f32 v53, v53, v55\n"
    "v_smfmac_f32_16x16x64_bf16 v[60:63], v[20:23], v[88:95], v1\n"
    "s_cbranch_vccnz LMR%=\n"
    "v_cvt_pk_bf16_f32 v48, v48, v52\n"
    "v_smfmac_f32_16x16x64_bf16 v[56:59], v[24:27], a[80:87], v1\n"
    "v_cvt_pk_bf16_f32 v49, v49, v53\n"
    "v_smfmac_f32_16x16x64_bf16 v[60:63], v[24:27], a[88:95], v1\n"
    "ds_write2_b32 v3, v48, v49 offset0:0 offset1:72\n"
    "v_add_u32 v117, s20, v5\n"
    "v_smfmac_f32_16x16x64_bf16 v[56:59], v[28:31], a[96:103], v1\n"
    "v_mov_b64 v[50:51], 0\n"
    "v_smfmac_f32_16x16x64_bf16 v[60:63], v[28:31], a[104:111], v1\n"
    "v_mov_b64 v[52:53], 0\n"
    "v_smfmac_f32_16x16x64_bf16 v[56:59], v[32:35], a[112:119], v1\n"
    "v_mov_b64 v[54:55], 0\n"
    "v_smfmac_f32_16x16x64_bf16 v[60:63], v[32:35], a[120:127], v1\n"
    "s_waitcnt lgkmcnt(0)\n"
    "s_barrier\n"
    "v_smfmac_f32_16x16x64_bf16 v[56:59], v[36:39], a[128:135], v1\n"
    "ds_read_b128 v[80:83], v117 offset:0\n"
    "ds_read_b128 v[84:87], v117 offset:64\n"
    "v_smfmac_f32_16x16x64_bf16 v[60:63], v[36:39], a[136:143], v1\n"
    "ds_read_b128 v[88:91], v9 offset:0\n"
    "ds_read_b128 v[92:95], v9 offset:1024\n"
    "v_smfmac_f32_16x16x64_bf16 v[56:59], v[40:43], a[144:151], v1\n"
    "ds_read_b128 v[96:99], v5 offset:0\n"
    "v_mov_b64 v[48:49], 0\n"
    "v_smfmac_f32_16x16x64_bf16 v[56:59], v[44:47], a[160:167], v1\n"
    "ds_read_b128 v[100:103], v5 offset:64\n"
    "v_smfmac_f32_16x16x64_bf16 v[60:63], v[40:43], a[152:159], v1\n"
    "ds_read_b128 v[104:107], v5 offset:128\n"
    "v_smfmac_f32_16x16x64_bf16 v[60:63], v[44:47], a[168:175], v1\n"
    "ds_read_b128 v[108:111], v5 offset:192\n"
    "s_waitcnt lgkmcnt(5)\n"
    "v_add_f32 v56, v56, v58\n"
    "v_mfma_f32_16x16x32_bf16 v[64:67], v[88:91], v[80:83], 0\n"
    "s_waitcnt lgkmcnt(4)\n"
    "v_add_f32 v57, v57, v59\n"
    "v_mfma_f32_16x16x32_bf16 v[64:67], v[92:95], v[84:87], v[64:67]\n"
    "v_add_f32 v60, v60, v62\n"
    "v_add_f32 v61, v61, v63\n"
    "v_cvt_pk_bf16_f32 v56, v56, v60\n"
    "v_cvt_pk_bf16_f32 v57, v57, v61\n"
    "v_smfmac_f32_16x16x64_bf16 v[48:51], v[16:19], a[176:183], v1\n"
    "ds_write2_b32 v4, v56, v57 offset0:0 offset1:72\n"
    "s_memtime s[40:41]\n"
    "v_smfmac_f32_16x16x64_bf16 v[52:55], v[16:19], a[184:191], v1\n"
    "ds_write_b128 v8, v[64:67]\n"
    "v_smfmac_f32_16x16x64_bf16 v[48:51], v[20:23], a[192:199], v1\n"
    "v_mov_b64 v[60:61], s[14:15]\n"
    "v_smfmac_f32_16x16x64_bf16 v[52:55], v[20:23], a[200:207], v1\n"
    "v_mov_b64 v[62:63], s[14:15]\n"
    "v_smfmac_f32_16x16x64_bf16 v[48:51], v[24:27], a[208:215], v1\n"
    "v_mov_b32 v118, s72\n"
    "v_smfmac_f32_16x16x64_bf16 v[52:55], v[24:27], a[216:223], v1\n"
    "s_waitcnt lgkmcnt(0)\n"
    "s_barrier\n"
    "ds_read_b128 v[64:67], v6 offset:0\n"
    "ds_read_b128 v[88:91], v15 offset:0\n"
    "v_smfmac_f32_16x16x64_bf16 v[48:51], v[28:31], a[224:231], v1\n"
    "ds_read_b128 v[92:95], v15 offset:512\n"
    "ds_read_b128 v[68:71], v6 offset:64\n"
    "v_smfmac_f32_16x16x64_bf16 v[52:55], v[28:31], a[232:239], v1\n"
    "ds_read_b128 v[72:75], v6 offset:128\n"
    "s_sub_u32 s40, s40, s44\n"
    "s_cmp_gt_u32 s40, s10\n"
    "v_smfmac_f32_16x16x64_bf16 v[48:51], v[32:35], a[240:247], v1\n"
    "ds_read_b128 v[76:79], v6 offset:192\n"
    "s_cselect_b32 s40, 0x10000, 0\n"
    "s_or_b32 s40, s40, s64\n"
    "s_waitcnt lgkmcnt(5)\n"
    "v_mov_b32 v119, s40\n"
    "v_mfma_f32_16x16x32_bf16 v[112:115], v[64:67], v[96:99], 0\n"
    "ds_write_b32 v118, v119\n"
    "s_waitcnt lgkmcnt(4)\n"
    "s_waitcnt lgkmcnt(3)\n"
    "v_add_f32 v88, v88, v92\n"
    "v_add_f32 v89, v89, v93\n"
    "v_mfma_f32_16x16x32_bf16 v[112:115], v[68:71], v[100:103], v[112:115]\n"
    "s_waitcnt lgkmcnt(2)\n"
    "v_add_f32 v90, v90, v94\n"
    "v_add_f32 v91, v91, v95\n"
    "v_mfma_f32_16x16x32_bf16 v[112:115], v[72:75], v[104:107], v[112:115]\n"
    "s_waitcnt lgkmcnt(1)\n"
    "v_max3_f32 v117, v88, v89, v90\n"
    "v_max_f32 v117, v117, v91\n"
    "v_mfma_f32_16x16x32_bf16 v[112:115], v[76:79], v[108:111], v[112:115]\n"
    "s_nop 0\n"
    "v_max_f32_dpp v116, v117, v117 row_ror:8 row_mask:0xf bank_mask:0xf\n"
    "v_mov_b32 v117, v116\n"
    "v_smfmac_f32_16x16x64_bf16 v[52:55], v[32:35], a[248:255], v1\n"
    "s_nop 0\n"
    "v_permlane32_swap_b32 v116, v117\n"
    "v_smfmac_f32_16x16x64_bf16 v[48:51], v[36:39], v[120:127], v1\n"
    "v_max_f32 v116, v116, v117\n"
    "v_smfmac_f32_16x16x64_bf16 v[48:51], v[40:43], v[136:143], v1\n"
    "v_max3_f32 v117, v112, v113, v116\n"
    "v_smfmac_f32_16x16x64_bf16 v[48:51], v[44:47], v[152:159], v1\n"
    "v_max3_f32 v117, v114, v115, v117\n"
    "v_smfmac_f32_16x16x64_bf16 v[52:55], v[36:39], v[128:135], v1\n"
    "v_mov_b32 v118, v117\n"
    "v_smfmac_f32_16x16x64_bf16 v[52:55], v[40:43], v[144:151], v1\n"
    "s_waitcnt vmcnt(4)\n"
    "ds_read_b128 v[80:83], v9 offset:8192\n"
    "v_permlane16_swap_b32 v117, v118\n"
    "v_smfmac_f32_16x16x64_bf16 v[52:55], v[44:47], v[160:167], v1\n"
    "ds_read_b128 v[84:87], v9 offset:9216\n"
    "v_max_f32 v117, v117, v118\n"
    "v_mul_f32 v117, 0xbe0293ee, v117\n"
    "v_fma_f32 v88, v88, s65, v117\n"
    "v_fma_f32 v89, v89, s65, v117\n"
    "v_fma_f32 v90, v90, s65, v117\n"
    "v_fma_f32 v91, v91, s65, v117\n"
    "v_fma_f32 v112, v112, s65, v117\n"
    "v_fma_f32 v113, v113, s65, v117\n"
    "v_exp_f32 v88, v88\n"
    "v_exp_f32 v89, v89\n"
    "v_exp_f32 v90, v90\n"
    "v_exp_f32 v91, v91\n"
    "v_cvt_pk_bf16_f32 v56, v88, v89\n"
    "v_cvt_pk_bf16_f32 v57, v90, v91\n"
    "v_exp_f32 v112, v112\n"
    "v_mov_b32_dpp v58, v56 row_shl:8 row_mask:0xf bank_mask:0xf bound_ctrl:1\n"
    "v_mov_b32_dpp v59, v57 row_shl:8 row_mask:0xf bank_mask:0xf bound_ctrl:1\n"
    "v_exp_f32 v113, v113\n"
    "v_mov_b32 v93, 0\n"
    "v_mfma_f32_16x16x32_bf16 v[104:107], v[60:63], v[56:59], 0\n"
    "s_waitcnt lgkmcnt(1)\n"
    "v_cvt_pk_bf16_f32 v92, v112, v113\n"
    "v_add_f32 v48, v48, v50\n"
    "v_mfma_f32_16x16x32_bf16 v[96:99], v[80:83], v[56:59], 0\n"
    "v_add_f32 v49, v49, v51\n"
    "v_cvt_pk_bf16_f32 v48, v48, v49\n"
    "v_mfma_f32_16x16x16_bf16 v[104:107], v[60:61], v[92:93], v[104:107]\n"
    "s_waitcnt lgkmcnt(0)\n"
    "v_add_f32 v52, v52, v54\n"
    "v_add_f32 v53, v53, v55\n"
    "v_mfma_f32_16x16x32_bf16 v[100:103], v[84:87], v[56:59], 0\n"
    "v_cvt_pk_bf16_f32 v49, v52, v53\n"
    "v_cvt_pk_bf16_f32 v95, v112, v113\n"
    "v_mov_b32 v94, 0\n"
    "v_mfma_f32_16x16x16_bf16 v[96:99], v[48:49], v[92:93], v[96:99]\n"
    "v_rcp_f32 v104, v104\n"
    "v_mfma_f32_16x16x16_bf16 v[100:103], v[48:49], v[94:95], v[100:103]\n"
    "ds_write_b64 v10, v[48:49]\n"
    "v_mov_b64 v[114:115], 0\n"
    "v_mov_b32 v118, 158212\n"
    "s_mov_b64 exec, s[78:79]\n"
    "v_pk_mul_f32 v[96:97], v[96:97], v[104:105] op_sel_hi:[1,0]\n"
    "v_pk_mul_f32 v[98:99], v[98:99], v[104:105] op_sel_hi:[1,0]\n"
    "v_cvt_pk_bf16_f32 v96, v96, v97\n"
    "v_cvt_pk_bf16_f32 v97, v98, v99\n"
    "ds_write_b64 v11, v[96:97] offset:0\n"
    "v_pk_mul_f32 v[100:101], v[100:101], v[104:105] op_sel_hi:[1,0]\n"
    "v_pk_mul_f32 v[102:103], v[102:103], v[104:105] op_sel_hi:[1,0]\n"
    "v_cvt_pk_bf16_f32 v98, v100, v101\n"
    "v_cvt_pk_bf16_f32 v99, v102, v103\n"
    "ds_write_b64 v11, v[98:99] offset:8\n"
    "s_mov_b64 exec, 0xff\n"
    "buffer_load_dword v116, v0, s[16:19], s59 offen sc1\n"
    "s_mov_b64 exec, -1\n"
    "v_mfma_f32_32x32x8_bf16 v[72:87], v[114:115], v[114:115], 0\n"
    "s_add_u32 s36, s36, 1\n"
    "s_cmp_eq_u32 s36, 64\n"
    "s_waitcnt lgkmcnt(0)\n"
    "s_waitcnt vmcnt(1)\n"
    "s_barrier\n"
    "ds_read_b128 v[104:107], v12 offset:0\n"
    "ds_read_b128 v[108:111], v12 offset:1024\n"
    "v_mfma_f32_32x32x8_bf16 v[48:63], v[114:115], v[114:115], 0\n"
    "ds_read_b32 v119, v118 offset:0\n"
    "s_cbranch_scc1 LEND%=\n"
    "s_waitcnt lgkmcnt(2)\n"
    "v_smfmac_f32_16x16x64_bf16 v[72:75], v[104:107], v[168:175], v1\n"
    "ds_read_b128 v[64:67], v7 offset:20480\n"
    "s_waitcnt lgkmcnt(2)\n"
    "v_smfmac_f32_16x16x64_bf16 v[72:75], v[108:111], v[176:183], v1\n"
    "ds_read_b128 v[68:71], v7 offset:21504\n"
    "v_smfmac_f32_16x16x64_bf16 v[76:79], v[104:107], v[184:191], v1\n"
    "ds_read_b128 v[16:19], v7 offset:22528\n"
    "v_smfmac_f32_16x16x64_bf16 v[76:79], v[108:111], v[192:199], v1\n"
    "ds_read_b128 v[20:23], v7 offset:23552\n"
    "v_add_f32 v72, v72, v74\n"
    "v_smfmac_f32_16x16x64_bf16 v[80:83], v[104:107], v[200:207], v1\n"
    "ds_read_b128 v[24:27], v7 offset:24576\n"
    "v_add_f32 v73, v73, v75\n"
    "v_smfmac_f32_16x16x64_bf16 v[80:83], v[108:111], v[208:215], v1\n"
    "ds_read_b128 v[28:31], v7 offset:25600\n"
    "v_add_f32 v76, v76, v78\n"
    "v_smfmac_f32_16x16x64_bf16 v[84:87], v[104:107], v[216:223], v1\n"
    "ds_read_b128 v[32:35], v7 offset:26624\n"
    "v_add_f32 v77, v77, v79\n"
    "v_smfmac_f32_16x16x64_bf16 v[84:87], v[108:111], v[224:231], v1\n"
    "ds_read_b128 v[36:39], v7 offset:27648\n"
    "v_cvt_pk_bf16_f32 v88, v72, v76\n"
    "v_cvt_pk_bf16_f32 v90, v73, v77\n"
    "v_smfmac_f32_16x16x64_bf16 v[48:51], v[104:107], v[232:239], v1\n"
    "ds_read_b128 v[40:43], v7 offset:28672\n"
    "v_add_f32 v80, v80, v82\n"
    "v_add_f32 v81, v81, v83\n"
    "v_smfmac_f32_16x16x64_bf16 v[48:51], v[108:111], v[240:247], v1\n"
    "ds_read_b128 v[44:47], v7 offset:29696\n"
    "v_add_f32 v85, v85, v87\n"
    "v_add_f32 v84, v84, v86\n"
    "v_cvt_pk_bf16_f32 v91, v81, v85\n"
    "v_cvt_pk_bf16_f32 v89, v80, v84\n"
    "s_waitcnt lgkmcnt(10)\n"
    "v_and_or_b32 v91, v91, s66, v119\n"
    "buffer_store_dwordx4 v[88:91], v13, s[16:19], s46 offen sc1\n"
    "v_smfmac_f32_16x16x64_bf16 v[52:55], v[104:107], v[248:255], v1\n"
    "s_waitcnt lgkmcnt(8)\n"
    "v_smfmac_f32_16x16x64_bf16 v[52:55], v[108:111], v[64:71], v1\n"
    "s_waitcnt lgkmcnt(6)\n"
    "v_add_f32 v48, v48, v50\n"
    "v_smfmac_f32_16x16x64_bf16 v[56:59], v[104:107], v[16:23], v1\n"
    "s_waitcnt lgkmcnt(4)\n"
    "v_add_f32 v49, v49, v51\n"
    "v_smfmac_f32_16x16x64_bf16 v[56:59], v[108:111], v[24:31], v1\n"
    "s_waitcnt lgkmcnt(2)\n"
    "v_add_f32 v52, v52, v54\n"
    "v_add_f32 v53, v53, v55\n"
    "v_smfmac_f32_16x16x64_bf16 v[60:63], v[104:107], v[32:39], v1\n"
    "s_waitcnt lgkmcnt(0)\n"
    "v_cvt_pk_bf16_f32 v92, v48, v52\n"
    "v_cvt_pk_bf16_f32 v94, v49, v53\n"
    "v_smfmac_f32_16x16x64_bf16 v[60:63], v[108:111], v[40:47], v1\n"
    "v_add_f32 v56, v56, v58\n"
    "v_add_f32 v57, v57, v59\n"
    "s_nop 5\n"
    "v_add_f32 v61, v61, v63\n"
    "v_add_f32 v60, v60, v62\n"
    "v_cvt_pk_bf16_f32 v95, v57, v61\n"
    "v_cvt_pk_bf16_f32 v93, v56, v60\n"
    "v_and_or_b32 v95, v95, s66, v119\n"
    "buffer_store_dwordx4 v[92:95], v13, s[16:19], s47 offen sc1\n"
    "s_add_u32 s54, s50, s57\n"
    "s_lshr_b32 s38, s54, 8\n"
    "s_xor_b32 s38, s38, s74\n"
    "s_and_b32 s38, s38, 0x2000\n"
    "s_or_b32 s54, s54, s38\n"
    "s_add_u32 s50, s50, 2621440\n"
    "s_add_u32 s48, s48, 16384\n"
    "s_add_u32 s60, s60, 128\n"
    "s_cmp_eq_u32 s61, 1\n"
    "s_cbranch_scc1 LW1%=\n"
    "s_cmp_gt_u32 s61, 1\n"
    "s_cbranch_scc1 LW2%=\n"
    "ds_read_b128 v[64:67], v9 offset:35584\n"
    "s_add_u32 s51, s46, s63\n"
    "s_add_u32 s52, s51, 4096\n"
    "s_add_u32 s39, s44, s9\n"
    "s_waitcnt lgkmcnt(0)\n"
    "LUG%=:\n"
    "s_memtime s[40:41]\n"
    "s_waitcnt lgkmcnt(0)\n"
    "s_sub_u32 s40, s39, s40\n"
    "s_sub_u32 s41, s40, 253\n"
    "s_cmp_lt_u32 s41, 16384\n"
    "s_cbranch_scc1 LUG%=\n"
    "s_cmp_lt_u32 s40, 253\n"
    "s_cselect_b32 s40, s40, 0\n"
    "s_and_b32 s40, s40, 0xfc\n"
    "s_getpc_b64 s[42:43]\n"
    "s_sub_u32 s40, 272, s40\n"
    "s_add_u32 s42, s42, s40\n"
    "s_addc_u32 s43, s43, 0\n"
    "s_setpc_b64 s[42:43]\n"
    ".rept 63\n"
    "s_nop 0\n"
    ".endr\n"
    "buffer_load_dwordx4 v[16:19], v0, s[16:19], s51 offen offset:0 sc1\n"
    "buffer_load_dwordx4 v[20:23], v0, s[16:19], s51 offen offset:1024 sc1\n"
    "buffer_load_dwordx4 v[24:27], v0, s[16:19], s51 offen offset:2048 sc1\n"
    "buffer_load_dwordx4 v[28:31], v0, s[16:19], s51 offen offset:3072 sc1\n"
    "buffer_load_dwordx4 v[32:35], v0, s[16:19], s52 offen offset:0 sc1\n"
    "buffer_load_dwordx4 v[36:39], v0, s[16:19], s52 offen offset:1024 sc1\n"
    "buffer_load_dwordx4 v[40:43], v0, s[16:19], s52 offen offset:2048 sc1\n"
    "buffer_load_dwordx4 v[44:47], v0, s[16:19], s52 offen offset:3072 sc1\n"
    "LGA%=:\n"
    "s_waitcnt vmcnt(7)\n"
    "v_mfma_f32_16x16x32_bf16 v[68:71], v[64:67], v[16:19], 0\n"
    "s_waitcnt vmcnt(6)\n"
    "v_mfma_f32_16x16x32_bf16 v[68:71], v[64:67], v[20:23], v[68:71]\n"
    "s_waitcnt vmcnt(5)\n"
    "v_bitop3_b32 v76, v19, v23, s64 bitop3:0x7e\n"
    "v_mfma_f32_16x16x32_bf16 v[68:71], v[64:67], v[24:27], v[68:71]\n"
    "s_waitcnt vmcnt(4)\n"
    "v_mfma_f32_16x16x32_bf16 v[68:71], v[64:67], v[28:31], v[68:71]\n"
    "s_waitcnt vmcnt(3)\n"
    "v_bitop3_b32 v77, v27, v31, s64 bitop3:0x7e\n"
    "v_mfma_f32_16x16x32_bf16 v[72:75], v[64:67], v[32:35], 0\n"
    "s_waitcnt vmcnt(2)\n"
    "v_mfma_f32_16x16x32_bf16 v[72:75], v[64:67], v[36:39], v[72:75]\n"
    "v_bitop3_b32 v78, v35, v39, s64 bitop3:0x7e\n"
    "v_or3_b32 v76, v76, v77, v78\n"
    "v_add_f32_dpp v68, v68, v68 row_ror:8 row_mask:0xf bank_mask:0xf\n"
    "v_add_f32_dpp v69, v69, v69 row_ror:8 row_mask:0xf bank_mask:0xf\n"
    "v_add_f32_dpp v70, v70, v70 row_ror:8 row_mask:0xf bank_mask:0xf\n"
    "v_add_f32_dpp v71, v71, v71 row_ror:8 row_mask:0xf bank_mask:0xf\n"
    "v_cvt_pk_bf16_f32 v81, v70, v71\n"
    "v_cvt_pk_bf16_f32 v80, v68, v69\n"
    "s_waitcnt vmcnt(1)\n"
    "v_mfma_f32_16x16x32_bf16 v[72:75], v[64:67], v[40:43], v[72:75]\n"
    "s_waitcnt vmcnt(0)\n"
    "v_mfma_f32_16x16x32_bf16 v[72:75], v[64:67], v[44:47], v[72:75]\n"
    "v_bitop3_b32 v79, v43, v47, s64 bitop3:0x7e\n"
    "v_or_b32 v76, v76, v79\n"
    "v_and_b32 v77, 1, v76\n"
    "v_cmp_ne_u32 vcc, 0, v77\n"
    "s_cbranch_vccnz LGR%=\n"
    "s_nop 2\n"
    "v_add_f32_dpp v72, v72, v72 row_ror:8 row_mask:0xf bank_mask:0xf\n"
    "v_add_f32_dpp v73, v73, v73 row_ror:8 row_mask:0xf bank_mask:0xf\n"
    "v_add_f32_dpp v74, v74, v74 row_ror:8 row_mask:0xf bank_mask:0xf\n"
    "v_add_f32_dpp v75, v75, v75 row_ror:8 row_mask:0xf bank_mask:0xf\n"
    "v_cvt_pk_bf16_f32 v83, v74, v75\n"
    "v_cvt_pk_bf16_f32 v82, v72, v73\n"
    "v_min_u32 v83, -2, v83\n"
    "buffer_store_dwordx4 v[80:83], v14, s[16:19], s48 offen sc1\n"
    "v_and_b32 v76, 0x10000, v76\n"
    "v_cmp_ne_u32 vcc, 0, v76\n"
    "v_mov_b32 v77, 158224\n"
    "s_nop 0\n"
    "s_or_b32 s38, vcc_lo, vcc_hi\n"
    "v_mov_b32 v76, s38\n"
    "ds_write_b32 v77, v76\n"
    "v_cmp_ne_u32 vcc, 0, v116\n"
    "v_readfirstlane_b32 s70, v116\n"
    "s_lshr_b32 s70, s70, 16\n"
    "s_cmp_gt_i32 s70, s68\n"
    "s_cselect_b32 s39, 1, 0\n"
    "s_cselect_b32 s38, 0x77, 7\n"
    "s_andn2_b32 s38, s38, vcc_lo\n"
    "s_cbranch_scc0 LS1K%=\n"
    "s_mov_b32 s37, 0x80000\n"
    "LS1W%=:\n"
    "s_mov_b64 exec, 0xff\n"
    "buffer_load_dword v116, v0, s[16:19], s59 offen sc1\n"
    "s_mov_b64 exec, -1\n"
    "s_waitcnt vmcnt(0)\n"
    "v_cmp_ne_u32 vcc, 0, v116\n"
    "v_readfirstlane_b32 s70, v116\n"
    "s_lshr_b32 s70, s70, 16\n"
    "s_cmp_gt_i32 s70, s68\n"
    "s_cselect_b32 s39, 1, 0\n"
    "s_cselect_b32 s38, 0x77, 7\n"
    "s_andn2_b32 s38, s38, vcc_lo\n"
    "s_cbranch_scc0 LS1K%=\n"
    "s_sleep 2\n"
    "s_sub_u32 s37, s37, 1\n"
    "s_cbranch_scc0 LS1W%=\n"
    "s_branch LFAIL%=\n"
    "LS1K%=:\n"
    "s_max_i32 s68, s68, s70\n"
    "s_and_b32 s53, s36, 7\n"
    "s_mul_i32 s53, s53, 1572864\n"
    "s_add_u32 s53, s53, s69\n"
    "s_cmp_lg_u32 s39, 0\n"
    "s_cselect_b32 s53, s53, s50\n"
    "s_cselect_b32 s49, s67, s48\n"
    "s_add_u32 s53, s53, s55\n"
    "s_add_u32 s67, s67, 16384\n"
    "s_add_u32 s49, s49, s62\n"
    "s_add_u32 s59, s59, 128\n"
    "s_add_u32 s51, s53, 0\n"
    "s_lshr_b32 s38, s51, 8\n"
    "s_xor_b32 s38, s38, s56\n"
    "s_and_b32 s38, s38, 0x2000\n"
    "s_or_b32 s51, s51, s38\n"
    "s_add_u32 s52, s53, 524288\n"
    "s_lshr_b32 s38, s52, 8\n"
    "s_xor_b32 s38, s38, s56\n"
    "s_and_b32 s38, s38, 0x2000\n"
    "s_or_b32 s52, s52, s38\n"
    "s_add_u32 s53, s53, 1048576\n"
    "s_lshr_b32 s38, s53, 8\n"
    "s_xor_b32 s38, s38, s56\n"
    "s_and_b32 s38, s38, 0x2000\n"
    "s_or_b32 s53, s53, s38\n"
    "s_xor_b32 s46, s46, 0x80000\n"
    "s_add_u32 s47, s46, 4096\n"
    "s_bfe_u32 s64, s36, 0x10001\n"
    "s_add_u32 s44, s44, s8\n"
    "buffer_load_dword v117, v14, s[16:19], s48 offen sc1\n"
    "s_branch LBM%=\n"
    "LW1%=:\n"
    "buffer_store_dword v1, off, s[16:19], s58 sc1\n"
    "s_add_u32 s58, s58, 4\n"
    "s_bitset0_b32 s58, 31\n"
    "s_waitcnt vmcnt(3)\n"
    "v_cmp_ne_u32 vcc, 0, v116\n"
    "v_readfirstlane_b32 s70, v116\n"
    "s_lshr_b32 s70, s70, 16\n"
    "s_cmp_gt_i32 s70, s68\n"
    "s_cselect_b32 s39, 1, 0\n"
    "s_cselect_b32 s38, 0x77, 7\n"
    "s_andn2_b32 s38, s38, vcc_lo\n"
    "s_cbranch_scc0 LS2K%=\n"
    "s_mov_b32 s37, 0x80000\n"
    "LS2W%=:\n"
    "s_mov_b64 exec, 0xff\n"
    "buffer_load_dword v116, v0, s[16:19], s59 offen sc1\n"
    "s_mov_b64 exec, -1\n"
    "s_waitcnt vmcnt(0)\n"
    "v_cmp_ne_u32 vcc, 0, v116\n"
    "v_readfirstlane_b32 s70, v116\n"
    "s_lshr_b32 s70, s70, 16\n"
    "s_cmp_gt_i32 s70, s68\n"
    "s_cselect_b32 s39, 1, 0\n"
    "s_cselect_b32 s38, 0x77, 7\n"
    "s_andn2_b32 s38, s38, vcc_lo\n"
    "s_cbranch_scc0 LS2K%=\n"
    "s_sleep 2\n"
    "s_sub_u32 s37, s37, 1\n"
    "s_cbranch_scc0 LS2W%=\n"
    "s_branch LFAIL%=\n"
    "LS2K%=:\n"
    "s_max_i32 s68, s68, s70\n"
    "s_and_b32 s53, s36, 7\n"
    "s_mul_i32 s53, s53, 1572864\n"
    "s_add_u32 s53, s53, s69\n"
    "s_cmp_lg_u32 s39, 0\n"
    "s_cselect_b32 s53, s53, s50\n"
    "s_cselect_b32 s49, s67, s48\n"
    "s_add_u32 s53, s53, s55\n"
    "s_add_u32 s67, s67, 16384\n"
    "s_add_u32 s49, s49, s62\n"
    "s_add_u32 s59, s59, 128\n"
    "s_add_u32 s51, s53, 0\n"
    "s_lshr_b32 s38, s51, 8\n"
    "s_xor_b32 s38, s38, s56\n"
    "s_and_b32 s38, s38, 0x2000\n"
    "s_or_b32 s51, s51, s38\n"
    "s_add_u32 s52, s53, 524288\n"
    "s_lshr_b32 s38, s52, 8\n"
    "s_xor_b32 s38, s38, s56\n"
    "s_and_b32 s38, s38, 0x2000\n"
    "s_or_b32 s52, s52, s38\n"
    "s_add_u32 s53, s53, 1048576\n"
    "s_lshr_b32 s38, s53, 8\n"
    "s_xor_b32 s38, s38, s56\n"
    "s_and_b32 s38, s38, 0x2000\n"
    "s_or_b32 s53, s53, s38\n"
    "v_mov_b32 v64, 158240\n"
    "s_mov_b32 s37, 0x80000\n"
    "LKW%=:\n"
    "ds_read_b64 v[66:67], v64\n"
    "s_waitcnt lgkmcnt(0)\n"
    "v_min_u32 v66, v66, v67\n"
    "v_cmp_eq_u32 vcc, s36, v66\n"
    "s_cbranch_vccnz LKK%=\n"
    "s_sleep 1\n"
    "s_sub_u32 s37, s37, 1\n"
    "s_cbranch_scc0 LKW%=\n"
    "s_branch LFAIL%=\n"
    "LKK%=:\n"
    "s_add_u32 s39, s44, s12\n"
    "LUF%=:\n"
    "s_memtime s[40:41]\n"
    "s_waitcnt lgkmcnt(0)\n"
    "s_sub_u32 s40, s39, s40\n"
    "s_sub_u32 s41, s40, 253\n"
    "s_cmp_lt_u32 s41, 16384\n"
    "s_cbranch_scc1 LUF%=\n"
    "s_cmp_lt_u32 s40, 253\n"
    "s_cselect_b32 s40, s40, 0\n"
    "s_and_b32 s40, s40, 0xfc\n"
    "s_getpc_b64 s[42:43]\n"
    "s_sub_u32 s40, 272, s40\n"
    "s_add_u32 s42, s42, s40\n"
    "s_addc_u32 s43, s43, 0\n"
    "s_setpc_b64 s[42:43]\n"
    ".rept 63\n"
    "s_nop 0\n"
    ".endr\n"
    "s_sub_u32 s51, s51, 2048\n"
    "s_mov_b32 m0, 122880\n"
    "s_nop 0\n"
    "buffer_load_dwordx4 v0, s[16:19], s51 offen offset:0 sc1 lds\n"
    "buffer_load_dwordx4 v0, s[16:19], s51 offen offset:1024 sc1 lds\n"
    "buffer_load_dwordx4 v0, s[16:19], s51 offen offset:2048 sc1 lds\n"
    "buffer_load_dwordx4 v0, s[16:19], s51 offen offset:3072 sc1 lds\n"
    "s_mov_b32 m0, 126976\n"
    "s_add_u32 s51, s51, 4096\n"
    "buffer_load_dwordx4 v0, s[16:19], s51 offen offset:0 sc1 lds\n"
    "buffer_load_dwordx4 v0, s[16:19], s51 offen offset:1024 sc1 lds\n"
    "buffer_load_dwordx4 v0, s[16:19], s51 offen offset:2048 sc1 lds\n"
    "buffer_load_dwordx4 v0, s[16:19], s51 offen offset:3072 sc1 lds\n"
    "s_xor_b32 s46, s46, 0x80000\n"
    "s_add_u32 s47, s46, 4096\n"
    "s_bfe_u32 s64, s36, 0x10001\n"
    "s_add_u32 s44, s44, s8\n"
    "LUM%=:\n"
    "s_memtime s[40:41]\n"
    "s_waitcnt lgkmcnt(0)\n"
    "s_sub_u32 s40, s44, s40\n"
    "s_sub_u32 s41, s40, 253\n"
    "s_cmp_lt_u32 s41, 16384\n"
    "s_cbranch_scc1 LUM%=\n"
    "s_cmp_lt_u32 s40, 253\n"
    "s_cselect_b32 s40, s40, 0\n"
    "s_and_b32 s40, s40, 0xfc\n"
    "s_getpc_b64 s[42:43]\n"
    "s_sub_u32 s40, 272, s40\n"
    "s_add_u32 s42, s42, s40\n"
    "s_addc_u32 s43, s43, 0\n"
    "s_setpc_b64 s[42:43]\n"
    ".rept 63\n"
    "s_nop 0\n"
    ".endr\n"
    "s_branch LBM%=\n"
    "LW2%=:\n"
    "v_add_u32 v118, 122880, v0\n"
    "ds_read_b128 v[16:19], v14 offset:0\n"
    "ds_read_b128 v[32:35], v118 offset:0\n"
    "ds_read_b128 v[48:51], v118 offset:4096\n"
    "ds_read_b128 v[20:23], v14 offset:1024\n"
    "ds_read_b128 v[36:39], v118 offset:1024\n"
    "ds_read_b128 v[52:55], v118 offset:5120\n"
    "ds_read_b128 v[24:27], v14 offset:2048\n"
    "ds_read_b128 v[40:43], v118 offset:2048\n"
    "ds_read_b128 v[56:59], v118 offset:6144\n"
    "ds_read_b128 v[28:31], v14 offset:3072\n"
    "ds_read_b128 v[44:47], v118 offset:3072\n"
    "ds_read_b128 v[60:63], v118 offset:7168\n"
    "s_waitcnt lgkmcnt(10)\n"
    "v_lshrrev_b32 v112, 1, v0\n"
    "v_add_u32 v112, 149760, v112\n"
    "v_mov_b32 v111, 0xd0289000\n"
    "v_mfma_f32_16x16x32_bf16 v[88:91], v[32:35], v[16:19], 0\n"
    "ds_read_b128 v[64:67], v6 offset:0\n"
    "s_waitcnt lgkmcnt(10)\n"
    "v_mfma_f32_16x16x32_bf16 v[92:95], v[48:51], v[16:19], 0\n"
    "ds_read_b128 v[68:71], v6 offset:64\n"
    "s_waitcnt lgkmcnt(9)\n"
    "v_mfma_f32_16x16x32_bf16 v[88:91], v[36:39], v[20:23], v[88:91]\n"
    "ds_read_b128 v[72:75], v6 offset:128\n"
    "s_waitcnt lgkmcnt(9)\n"
    "v_mfma_f32_16x16x32_bf16 v[92:95], v[52:55], v[20:23], v[92:95]\n"
    "ds_read_b128 v[76:79], v6 offset:192\n"
    "s_waitcnt lgkmcnt(8)\n"
    "v_mfma_f32_16x16x32_bf16 v[88:91], v[40:43], v[24:27], v[88:91]\n"
    "ds_read_b64 v[80:81], v112 offset:0\n"
    "s_waitcnt lgkmcnt(8)\n"
    "v_mfma_f32_16x16x32_bf16 v[92:95], v[56:59], v[24:27], v[92:95]\n"
    "ds_read_b64 v[82:83], v112 offset:512\n"
    "s_waitcnt lgkmcnt(7)\n"
    "v_mfma_f32_16x16x32_bf16 v[88:91], v[44:47], v[28:31], v[88:91]\n"
    "ds_read_b64 v[84:85], v112 offset:1024\n"
    "s_waitcnt lgkmcnt(7)\n"
    "v_mfma_f32_16x16x32_bf16 v[92:95], v[60:63], v[28:31], v[92:95]\n"
    "ds_read_b64 v[86:87], v112 offset:1536\n"
    "v_mov_b32 v108, s72\n"
    "v_mov_b32 v109, s36\n"
    "ds_write_b32 v108, v109 offset:24\n"
    "s_waitcnt lgkmcnt(8)\n"
    "v_mfma_f32_16x16x32_bf16 v[96:99], v[64:67], v[16:19], 0\n"
    "ds_read_b128 v[32:35], v118 offset:8192\n"
    "ds_read_b128 v[36:39], v118 offset:9216\n"
    "s_waitcnt lgkmcnt(9)\n"
    "v_mfma_f32_16x16x32_bf16 v[96:99], v[68:71], v[20:23], v[96:99]\n"
    "ds_read_b128 v[40:43], v118 offset:10240\n"
    "ds_read_b128 v[44:47], v118 offset:11264\n"
    "s_waitcnt lgkmcnt(10)\n"
    "v_cndmask_b32 v88, v111, v88, s[80:81]\n"
    "v_cndmask_b32 v89, v111, v89, s[82:83]\n"
    "v_mfma_f32_16x16x32_bf16 v[96:99], v[72:75], v[24:27], v[96:99]\n"
    "ds_read_b128 v[48:51], v118 offset:12288\n"
    "ds_read_b128 v[52:55], v118 offset:13312\n"
    "s_waitcnt lgkmcnt(11)\n"
    "v_cndmask_b32 v90, v111, v90, s[84:85]\n"
    "v_cndmask_b32 v91, v111, v91, s[86:87]\n"
    "v_mfma_f32_16x16x32_bf16 v[96:99], v[76:79], v[28:31], v[96:99]\n"
    "ds_read_b128 v[56:59], v118 offset:14336\n"
    "ds_read_b128 v[60:63], v118 offset:15360\n"
    "v_cndmask_b32 v92, v111, v92, s[88:89]\n"
    "v_cndmask_b32 v93, v111, v93, s[90:91]\n"
    "v_cndmask_b32 v94, v111, v94, s[92:93]\n"
    "v_cndmask_b32 v95, v111, v95, s[94:95]\n"
    "v_max3_f32 v108, v88, v89, v90\n"
    "v_max3_f32 v109, v91, v92, v93\n"
    "v_max3_f32 v108, v108, v94, v95\n"
    "v_max3_f32 v109, v109, v96, v97\n"
    "v_max_f32 v108, v108, v109\n"
    "v_mov_b32 v109, v108\n"
    "s_nop 1\n"
    "v_permlane16_swap_b32 v108, v109\n"
    "v_max_f32 v108, v108, v109\n"
    "v_mov_b32 v109, v108\n"
    "s_nop 1\n"
    "v_permlane32_swap_b32 v108, v109\n"
    "v_max_f32 v108, v108, v109\n"
    "v_mul_f32 v108, 0xbe0293ee, v108\n"
    "v_fma_f32 v88, v88, s65, v108\n"
    "v_fma_f32 v89, v89, s65, v108\n"
    "v_fma_f32 v90, v90, s65, v108\n"
    "v_fma_f32 v91, v91, s65, v108\n"
    "v_fma_f32 v92, v92, s65, v108\n"
    "v_fma_f32 v93, v93, s65, v108\n"
    "v_fma_f32 v94, v94, s65, v108\n"
    "v_fma_f32 v95, v95, s65, v108\n"
    "v_fma_f32 v96, v96, s65, v108\n"
    "v_fma_f32 v97, v97, s65, v108\n"
    "v_exp_f32 v88, v88\n"
    "v_exp_f32 v89, v89\n"
    "v_exp_f32 v90, v90\n"
    "v_exp_f32 v91, v91\n"
    "v_exp_f32 v92, v92\n"
    "v_exp_f32 v93, v93\n"
    "v_exp_f32 v94, v94\n"
    "v_exp_f32 v95, v95\n"
    "v_exp_f32 v96, v96\n"
    "v_exp_f32 v97, v97\n"
    "v_add_f32 v108, v88, v89\n"
    "v_add_f32 v110, v90, v91\n"
    "v_add_f32 v108, v108, v92\n"
    "v_add_f32 v110, v110, v93\n"
    "v_add_f32 v108, v108, v94\n"
    "v_add_f32 v110, v110, v95\n"
    "v_add_f32 v108, v108, v96\n"
    "v_add_f32 v110, v110, v97\n"
    "v_add_f32 v108, v108, v110\n"
    "v_mov_b32 v109, v108\n"
    "s_nop 1\n"
    "v_permlane16_swap_b32 v108, v109\n"
    "v_add_f32 v108, v108, v109\n"
    "v_mov_b32 v109, v108\n"
    "s_nop 1\n"
    "v_permlane32_swap_b32 v108, v109\n"
    "v_add_f32 v108, v108, v109\n"
    "v_rcp_f32 v108, v108\n"
    "v_mov_b32 v105, 0\n"
    "v_mov_b32 v106, 0\n"
    "v_mul_f32 v88, v88, v108\n"
    "v_mul_f32 v89, v89, v108\n"
    "v_mul_f32 v90, v90, v108\n"
    "v_mul_f32 v91, v91, v108\n"
    "v_mul_f32 v92, v92, v108\n"
    "v_mul_f32 v93, v93, v108\n"
    "v_mul_f32 v94, v94, v108\n"
    "v_mul_f32 v95, v95, v108\n"
    "v_mul_f32 v96, v96, v108\n"
    "v_mul_f32 v97, v97, v108\n"
    "v_cvt_pk_bf16_f32 v100, v88, v89\n"
    "v_cvt_pk_bf16_f32 v101, v90, v91\n"
    "v_cvt_pk_bf16_f32 v102, v92, v93\n"
    "v_cvt_pk_bf16_f32 v103, v94, v95\n"
    "v_cvt_pk_bf16_f32 v104, v96, v97\n"
    "v_cvt_pk_bf16_f32 v107, v96, v97\n"
    "s_waitcnt lgkmcnt(7)\n"
    "v_mfma_f32_16x16x32_bf16 v[64:67], v[32:35], v[100:103], 0\n"
    "s_waitcnt lgkmcnt(6)\n"
    "v_mfma_f32_16x16x32_bf16 v[68:71], v[36:39], v[100:103], 0\n"
    "v_mfma_f32_16x16x16_bf16 v[64:67], v[80:81], v[104:105], v[64:67]\n"
    "v_mfma_f32_16x16x16_bf16 v[68:71], v[80:81], v[106:107], v[68:71]\n"
    "s_waitcnt lgkmcnt(5)\n"
    "v_mfma_f32_16x16x32_bf16 v[72:75], v[40:43], v[100:103], 0\n"
    "s_waitcnt lgkmcnt(4)\n"
    "v_mfma_f32_16x16x32_bf16 v[76:79], v[44:47], v[100:103], 0\n"
    "v_cvt_pk_bf16_f32 v88, v64, v65\n"
    "v_cvt_pk_bf16_f32 v89, v66, v67\n"
    "v_cvt_pk_bf16_f32 v90, v68, v69\n"
    "v_mfma_f32_16x16x16_bf16 v[72:75], v[82:83], v[104:105], v[72:75]\n"
    "v_cvt_pk_bf16_f32 v91, v70, v71\n"
    "ds_write_b128 v14, v[88:91] offset:0\n"
    "v_mfma_f32_16x16x16_bf16 v[76:79], v[82:83], v[106:107], v[76:79]\n"
    "s_waitcnt lgkmcnt(4)\n"
    "v_mfma_f32_16x16x32_bf16 v[16:19], v[48:51], v[100:103], 0\n"
    "s_waitcnt lgkmcnt(3)\n"
    "v_mfma_f32_16x16x32_bf16 v[20:23], v[52:55], v[100:103], 0\n"
    "v_cvt_pk_bf16_f32 v92, v72, v73\n"
    "v_cvt_pk_bf16_f32 v93, v74, v75\n"
    "v_cvt_pk_bf16_f32 v94, v76, v77\n"
    "v_mfma_f32_16x16x16_bf16 v[16:19], v[84:85], v[104:105], v[16:19]\n"
    "v_cvt_pk_bf16_f32 v95, v78, v79\n"
    "ds_write_b128 v14, v[92:95] offset:1024\n"
    "v_mfma_f32_16x16x16_bf16 v[20:23], v[84:85], v[106:107], v[20:23]\n"
    "s_waitcnt lgkmcnt(3)\n"
    "v_mfma_f32_16x16x32_bf16 v[24:27], v[56:59], v[100:103], 0\n"
    "s_waitcnt lgkmcnt(2)\n"
    "v_mfma_f32_16x16x32_bf16 v[28:31], v[60:63], v[100:103], 0\n"
    "v_cvt_pk_bf16_f32 v88, v16, v17\n"
    "v_cvt_pk_bf16_f32 v89, v18, v19\n"
    "v_cvt_pk_bf16_f32 v90, v20, v21\n"
    "v_mfma_f32_16x16x16_bf16 v[24:27], v[86:87], v[104:105], v[24:27]\n"
    "v_cvt_pk_bf16_f32 v91, v22, v23\n"
    "ds_write_b128 v14, v[88:91] offset:2048\n"
    "v_mfma_f32_16x16x16_bf16 v[28:31], v[86:87], v[106:107], v[28:31]\n"
    "s_nop 3\n"
    "v_cvt_pk_bf16_f32 v92, v24, v25\n"
    "v_cvt_pk_bf16_f32 v93, v26, v27\n"
    "s_nop 1\n"
    "v_cvt_pk_bf16_f32 v94, v28, v29\n"
    "v_cvt_pk_bf16_f32 v95, v30, v31\n"
    "ds_write_b128 v14, v[92:95] offset:3072\n"
    "s_waitcnt vmcnt(2)\n"
    "v_cmp_ne_u32 vcc, 0, v116\n"
    "v_readfirstlane_b32 s70, v116\n"
    "s_lshr_b32 s70, s70, 16\n"
    "s_cmp_gt_i32 s70, s68\n"
    "s_cselect_b32 s39, 1, 0\n"
    "s_cselect_b32 s38, 0x77, 7\n"
    "s_andn2_b32 s38, s38, vcc_lo\n"
    "s_cbranch_scc0 LS3K%=\n"
    "s_mov_b32 s37, 0x80000\n"
    "LS3W%=:\n"
    "s_mov_b64 exec, 0xff\n"
    "buffer_load_dword v116, v0, s[16:19], s59 offen sc1\n"
    "s_mov_b64 exec, -1\n"
    "s_waitcnt vmcnt(0)\n"
    "v_cmp_ne_u32 vcc, 0, v116\n"
    "v_readfirstlane_b32 s70, v116\n"
    "s_lshr_b32 s70, s70, 16\n"
    "s_cmp_gt_i32 s70, s68\n"
    "s_cselect_b32 s39, 1, 0\n"
    "s_cselect_b32 s38, 0x77, 7\n"
    "s_andn2_b32 s38, s38, vcc_lo\n"
    "s_cbranch_scc0 LS3K%=\n"
    "s_sleep 2\n"
    "s_sub_u32 s37, s37, 1\n"
    "s_cbranch_scc0 LS3W%=\n"
    "s_branch LFAIL%=\n"
    "LS3K%=:\n"
    "s_max_i32 s68, s68, s70\n"
    "s_and_b32 s53, s36, 7\n"
    "s_mul_i32 s53, s53, 1572864\n"
    "s_add_u32 s53, s53, s69\n"
    "s_cmp_lg_u32 s39, 0\n"
    "s_cselect_b32 s53, s53, s50\n"
    "s_cselect_b32 s49, s67, s48\n"
    "s_add_u32 s53, s53, s55\n"
    "s_add_u32 s67, s67, 16384\n"
    "s_add_u32 s49, s49, s62\n"
    "s_add_u32 s59, s59, 128\n"
    "s_add_u32 s51, s53, 0\n"
    "s_lshr_b32 s38, s51, 8\n"
    "s_xor_b32 s38, s38, s56\n"
    "s_and_b32 s38, s38, 0x2000\n"
    "s_or_b32 s51, s51, s38\n"
    "s_add_u32 s52, s53, 524288\n"
    "s_lshr_b32 s38, s52, 8\n"
    "s_xor_b32 s38, s38, s56\n"
    "s_and_b32 s38, s38, 0x2000\n"
    "s_or_b32 s52, s52, s38\n"
    "s_add_u32 s53, s53, 1048576\n"
    "s_lshr_b32 s38, s53, 8\n"
    "s_xor_b32 s38, s38, s56\n"
    "s_and_b32 s38, s38, 0x2000\n"
    "s_or_b32 s53, s53, s38\n"
    "s_xor_b32 s46, s46, 0x80000\n"
    "s_add_u32 s47, s46, 4096\n"
    "s_bfe_u32 s64, s36, 0x10001\n"
    "s_add_u32 s44, s44, s8\n"
    "s_branch LBM%=\n"
    "LEND%=:\n"
    "s_waitcnt lgkmcnt(0)\n"
    "s_cmp_lg_u32 s61, 1\n"
    "s_cbranch_scc1 LE0%=\n"
    "buffer_store_dword v1, off, s[16:19], s58 sc1\n"
    "s_add_u32 s58, s58, 4\n"
    "s_bitset0_b32 s58, 31\n"
    "LE0%=:\n"
    "s_add_u32 s54, s50, s57\n"
    "s_lshr_b32 s38, s54, 8\n"
    "s_xor_b32 s38, s38, s74\n"
    "s_and_b32 s38, s38, 0x2000\n"
    "s_or_b32 s54, s54, s38\n"
    "s_cmp_lt_u32 s61, 2\n"
    "s_cbranch_scc1 LE1%=\n"
    "v_add_u32 v118, 122880, v0\n"
    "ds_read_b128 v[16:19], v14 offset:0\n"
    "ds_read_b128 v[32:35], v118 offset:0\n"
    "ds_read_b128 v[48:51], v118 offset:4096\n"
    "ds_read_b128 v[20:23], v14 offset:1024\n"
    "ds_read_b128 v[36:39], v118 offset:1024\n"
    "ds_read_b128 v[52:55], v118 offset:5120\n"
    "ds_read_b128 v[24:27], v14 offset:2048\n"
    "ds_read_b128 v[40:43], v118 offset:2048\n"
    "ds_read_b128 v[56:59], v118 offset:6144\n"
    "ds_read_b128 v[28:31], v14 offset:3072\n"
    "ds_read_b128 v[44:47], v118 offset:3072\n"
    "ds_read_b128 v[60:63], v118 offset:7168\n"
    "s_waitcnt lgkmcnt(10)\n"
    "v_lshrrev_b32 v112, 1, v0\n"
    "v_add_u32 v112, 149760, v112\n"
    "v_mov_b32 v111, 0xd0289000\n"
    "v_mfma_f32_16x16x32_bf16 v[88:91], v[32:35], v[16:19], 0\n"
    "ds_read_b128 v[64:67], v6 offset:0\n"
    "s_waitcnt lgkmcnt(10)\n"
    "v_mfma_f32_16x16x32_bf16 v[92:95], v[48:51], v[16:19], 0\n"
    "ds_read_b128 v[68:71], v6 offset:64\n"
    "s_waitcnt lgkmcnt(9)\n"
    "v_mfma_f32_16x16x32_bf16 v[88:91], v[36:39], v[20:23], v[88:91]\n"
    "ds_read_b128 v[72:75], v6 offset:128\n"
    "s_waitcnt lgkmcnt(9)\n"
    "v_mfma_f32_16x16x32_bf16 v[92:95], v[52:55], v[20:23], v[92:95]\n"
    "ds_read_b128 v[76:79], v6 offset:192\n"
    "s_waitcnt lgkmcnt(8)\n"
    "v_mfma_f32_16x16x32_bf16 v[88:91], v[40:43], v[24:27], v[88:91]\n"
    "ds_read_b64 v[80:81], v112 offset:0\n"
    "s_waitcnt lgkmcnt(8)\n"
    "v_mfma_f32_16x16x32_bf16 v[92:95], v[56:59], v[24:27], v[92:95]\n"
    "ds_read_b64 v[82:83], v112 offset:512\n"
    "s_waitcnt lgkmcnt(7)\n"
    "v_mfma_f32_16x16x32_bf16 v[88:91], v[44:47], v[28:31], v[88:91]\n"
    "ds_read_b64 v[84:85], v112 offset:1024\n"
    "s_waitcnt lgkmcnt(7)\n"
    "v_mfma_f32_16x16x32_bf16 v[92:95], v[60:63], v[28:31], v[92:95]\n"
    "ds_read_b64 v[86:87], v112 offset:1536\n"
    "v_mov_b32 v108, s72\n"
    "v_mov_b32 v109, s36\n"
    "ds_write_b32 v108, v109 offset:24\n"
    "s_waitcnt lgkmcnt(8)\n"
    "v_mfma_f32_16x16x32_bf16 v[96:99], v[64:67], v[16:19], 0\n"
    "ds_read_b128 v[32:35], v118 offset:8192\n"
    "ds_read_b128 v[36:39], v118 offset:9216\n"
    "s_waitcnt lgkmcnt(9)\n"
    "v_mfma_f32_16x16x32_bf16 v[96:99], v[68:71], v[20:23], v[96:99]\n"
    "ds_read_b128 v[40:43], v118 offset:10240\n"
    "ds_read_b128 v[44:47], v118 offset:11264\n"
    "s_waitcnt lgkmcnt(10)\n"
    "v_cndmask_b32 v88, v111, v88, s[80:81]\n"
    "v_cndmask_b32 v89, v111, v89, s[82:83]\n"
    "v_mfma_f32_16x16x32_bf16 v[96:99], v[72:75], v[24:27], v[96:99]\n"
    "ds_read_b128 v[48:51], v118 offset:12288\n"
    "ds_read_b128 v[52:55], v118 offset:13312\n"
    "s_waitcnt lgkmcnt(11)\n"
    "v_cndmask_b32 v90, v111, v90, s[84:85]\n"
    "v_cndmask_b32 v91, v111, v91, s[86:87]\n"
    "v_mfma_f32_16x16x32_bf16 v[96:99], v[76:79], v[28:31], v[96:99]\n"
    "ds_read_b128 v[56:59], v118 offset:14336\n"
    "ds_read_b128 v[60:63], v118 offset:15360\n"
    "v_cndmask_b32 v92, v111, v92, s[88:89]\n"
    "v_cndmask_b32 v93, v111, v93, s[90:91]\n"
    "v_cndmask_b32 v94, v111, v94, s[92:93]\n"
    "v_cndmask_b32 v95, v111, v95, s[94:95]\n"
    "v_max3_f32 v108, v88, v89, v90\n"
    "v_max3_f32 v109, v91, v92, v93\n"
    "v_max3_f32 v108, v108, v94, v95\n"
    "v_max3_f32 v109, v109, v96, v97\n"
    "v_max_f32 v108, v108, v109\n"
    "v_mov_b32 v109, v108\n"
    "s_nop 1\n"
    "v_permlane16_swap_b32 v108, v109\n"
    "v_max_f32 v108, v108, v109\n"
    "v_mov_b32 v109, v108\n"
    "s_nop 1\n"
    "v_permlane32_swap_b32 v108, v109\n"
    "v_max_f32 v108, v108, v109\n"
    "v_mul_f32 v108, 0xbe0293ee, v108\n"
    "v_fma_f32 v88, v88, s65, v108\n"
    "v_fma_f32 v89, v89, s65, v108\n"
    "v_fma_f32 v90, v90, s65, v108\n"
    "v_fma_f32 v91, v91, s65, v108\n"
    "v_fma_f32 v92, v92, s65, v108\n"
    "v_fma_f32 v93, v93, s65, v108\n"
    "v_fma_f32 v94, v94, s65, v108\n"
    "v_fma_f32 v95, v95, s65, v108\n"
    "v_fma_f32 v96, v96, s65, v108\n"
    "v_fma_f32 v97, v97, s65, v108\n"
    "v_exp_f32 v88, v88\n"
    "v_exp_f32 v89, v89\n"
    "v_exp_f32 v90, v90\n"
    "v_exp_f32 v91, v91\n"
    "v_exp_f32 v92, v92\n"
    "v_exp_f32 v93, v93\n"
    "v_exp_f32 v94, v94\n"
    "v_exp_f32 v95, v95\n"
    "v_exp_f32 v96, v96\n"
    "v_exp_f32 v97, v97\n"
    "v_add_f32 v108, v88, v89\n"
    "v_add_f32 v110, v90, v91\n"
    "v_add_f32 v108, v108, v92\n"
    "v_add_f32 v110, v110, v93\n"
    "v_add_f32 v108, v108, v94\n"
    "v_add_f32 v110, v110, v95\n"
    "v_add_f32 v108, v108, v96\n"
    "v_add_f32 v110, v110, v97\n"
    "v_add_f32 v108, v108, v110\n"
    "v_mov_b32 v109, v108\n"
    "s_nop 1\n"
    "v_permlane16_swap_b32 v108, v109\n"
    "v_add_f32 v108, v108, v109\n"
    "v_mov_b32 v109, v108\n"
    "s_nop 1\n"
    "v_permlane32_swap_b32 v108, v109\n"
    "v_add_f32 v108, v108, v109\n"
    "v_rcp_f32 v108, v108\n"
    "v_mov_b32 v105, 0\n"
    "v_mov_b32 v106, 0\n"
    "v_mul_f32 v88, v88, v108\n"
    "v_mul_f32 v89, v89, v108\n"
    "v_mul_f32 v90, v90, v108\n"
    "v_mul_f32 v91, v91, v108\n"
    "v_mul_f32 v92, v92, v108\n"
    "v_mul_f32 v93, v93, v108\n"
    "v_mul_f32 v94, v94, v108\n"
    "v_mul_f32 v95, v95, v108\n"
    "v_mul_f32 v96, v96, v108\n"
    "v_mul_f32 v97, v97, v108\n"
    "v_cvt_pk_bf16_f32 v100, v88, v89\n"
    "v_cvt_pk_bf16_f32 v101, v90, v91\n"
    "v_cvt_pk_bf16_f32 v102, v92, v93\n"
    "v_cvt_pk_bf16_f32 v103, v94, v95\n"
    "v_cvt_pk_bf16_f32 v104, v96, v97\n"
    "v_cvt_pk_bf16_f32 v107, v96, v97\n"
    "s_waitcnt lgkmcnt(7)\n"
    "v_mfma_f32_16x16x32_bf16 v[64:67], v[32:35], v[100:103], 0\n"
    "s_waitcnt lgkmcnt(6)\n"
    "v_mfma_f32_16x16x32_bf16 v[68:71], v[36:39], v[100:103], 0\n"
    "v_mfma_f32_16x16x16_bf16 v[64:67], v[80:81], v[104:105], v[64:67]\n"
    "v_mfma_f32_16x16x16_bf16 v[68:71], v[80:81], v[106:107], v[68:71]\n"
    "s_waitcnt lgkmcnt(5)\n"
    "v_mfma_f32_16x16x32_bf16 v[72:75], v[40:43], v[100:103], 0\n"
    "s_waitcnt lgkmcnt(4)\n"
    "v_mfma_f32_16x16x32_bf16 v[76:79], v[44:47], v[100:103], 0\n"
    "v_cvt_pk_bf16_f32 v88, v64, v65\n"
    "v_cvt_pk_bf16_f32 v89, v66, v67\n"
    "v_cvt_pk_bf16_f32 v90, v68, v69\n"
    "v_mfma_f32_16x16x16_bf16 v[72:75], v[82:83], v[104:105], v[72:75]\n"
    "v_cvt_pk_bf16_f32 v91, v70, v71\n"
    "ds_write_b128 v14, v[88:91] offset:0\n"
    "v_mfma_f32_16x16x16_bf16 v[76:79], v[82:83], v[106:107], v[76:79]\n"
    "s_waitcnt lgkmcnt(4)\n"
    "v_mfma_f32_16x16x32_bf16 v[16:19], v[48:51], v[100:103], 0\n"
    "s_waitcnt lgkmcnt(3)\n"
    "v_mfma_f32_16x16x32_bf16 v[20:23], v[52:55], v[100:103], 0\n"
    "v_cvt_pk_bf16_f32 v92, v72, v73\n"
    "v_cvt_pk_bf16_f32 v93, v74, v75\n"
    "v_cvt_pk_bf16_f32 v94, v76, v77\n"
    "v_mfma_f32_16x16x16_bf16 v[16:19], v[84:85], v[104:105], v[16:19]\n"
    "v_cvt_pk_bf16_f32 v95, v78, v79\n"
    "ds_write_b128 v14, v[92:95] offset:1024\n"
    "v_mfma_f32_16x16x16_bf16 v[20:23], v[84:85], v[106:107], v[20:23]\n"
    "s_waitcnt lgkmcnt(3)\n"
    "v_mfma_f32_16x16x32_bf16 v[24:27], v[56:59], v[100:103], 0\n"
    "s_waitcnt lgkmcnt(2)\n"
    "v_mfma_f32_16x16x32_bf16 v[28:31], v[60:63], v[100:103], 0\n"
    "v_cvt_pk_bf16_f32 v88, v16, v17\n"
    "v_cvt_pk_bf16_f32 v89, v18, v19\n"
    "v_cvt_pk_bf16_f32 v90, v20, v21\n"
    "v_mfma_f32_16x16x16_bf16 v[24:27], v[86:87], v[104:105], v[24:27]\n"
    "v_cvt_pk_bf16_f32 v91, v22, v23\n"
    "ds_write_b128 v14, v[88:91] offset:2048\n"
    "v_mfma_f32_16x16x16_bf16 v[28:31], v[86:87], v[106:107], v[28:31]\n"
    "s_nop 3\n"
    "v_cvt_pk_bf16_f32 v92, v24, v25\n"
    "v_cvt_pk_bf16_f32 v93, v26, v27\n"
    "s_nop 1\n"
    "v_cvt_pk_bf16_f32 v94, v28, v29\n"
    "v_cvt_pk_bf16_f32 v95, v30, v31\n"
    "ds_write_b128 v14, v[92:95] offset:3072\n"
    "s_waitcnt lgkmcnt(0)\n"
    "LE1%=:\n"
    "s_barrier\n"
    "ds_read_b128 v[16:19], v9 offset:16384\n"
    "ds_read_b128 v[20:23], v9 offset:17408\n"
    "v_lshrrev_b32 v113, 4, v0\n"
    "v_and_b32 v113, 0x30, v113\n"
    "v_lshl_add_u32 v113, v113, 4, v0\n"
    "s_waitcnt lgkmcnt(1)\n"
    "buffer_store_dwordx4 v[16:19], v113, s[16:19], s54 offen offset:0 sc1\n"
    "s_waitcnt lgkmcnt(0)\n"
    "buffer_store_dwordx4 v[20:23], v113, s[16:19], s54 offen offset:2048 sc1\n"
    "s_waitcnt vmcnt(0)\n"
    "s_barrier\n"
    "buffer_store_dword v1, off, s[16:19], s58 sc1\n"
    "s_endpgm\n"
    "LRS%=:\n"
    "s_waitcnt vmcnt(0)\n"
    "s_cmp_lg_u32 s61, 1\n"
    "s_barrier\n"
    "s_cbranch_scc1 LRB%=\n"
    "s_memtime s[40:41]\n"
    "s_waitcnt lgkmcnt(0)\n"
    "s_sub_u32 s40, s40, s45\n"
    "s_or_b32 s40, s40, 1\n"
    "v_mov_b32 v112, s40\n"
    "s_add_u32 s38, s60, s71\n"
    "buffer_store_dword v112, off, s[16:19], s38 sc1\n"
    "v_lshrrev_b32 v113, 2, v0\n"
    "v_and_b32 v113, 0x7c, v113\n"
    "s_waitcnt vmcnt(0)\n"
    "s_mov_b32 s37, 0x80000\n"
    "LRW%=:\n"
    "buffer_load_dword v112, v113, s[16:19], s60 offen sc1\n"
    "buffer_load_dword v114, v113, s[16:19], s73 offen sc1\n"
    "s_waitcnt vmcnt(0)\n"
    "v_min_u32 v114, v114, v112\n"
    "v_cmp_eq_u32 vcc, 0, v114\n"
    "s_cbranch_vccz LRK%=\n"
    "s_sleep 2\n"
    "s_sub_u32 s37, s37, 1\n"
    "s_cbranch_scc0 LRW%=\n"
    "s_branch LFAIL%=\n"
    "LRK%=:\n"
    "v_max_u32_dpp v112, v112, v112 row_ror:8 row_mask:0xf bank_mask:0xf\n"
    "s_nop 1\n"
    "v_max_u32_dpp v112, v112, v112 row_ror:4 row_mask:0xf bank_mask:0xf\n"
    "s_nop 1\n"
    "v_max_u32_dpp v112, v112, v112 row_ror:2 row_mask:0xf bank_mask:0xf\n"
    "s_nop 1\n"
    "v_max_u32_dpp v112, v112, v112 row_ror:1 row_mask:0xf bank_mask:0xf\n"
    "s_nop 0\n"
    "v_readlane_b32 s44, v112, 0\n"
    "v_readlane_b32 s38, v112, 16\n"
    "s_max_u32 s44, s44, s38\n"
    "s_add_u32 s44, s44, s45\n"
    "s_add_u32 s44, s44, s11\n"
    "v_mov_b32 v112, 158228\n"
    "v_mov_b32 v113, s44\n"
    "ds_write_b32 v112, v113\n"
    "s_waitcnt lgkmcnt(0)\n"
    "LUR%=:\n"
    "s_memtime s[40:41]\n"
    "s_waitcnt lgkmcnt(0)\n"
    "s_sub_u32 s40, s44, s40\n"
    "s_sub_u32 s41, s40, 253\n"
    "s_cmp_lt_u32 s41, 16384\n"
    "s_cbranch_scc1 LUR%=\n"
    "s_cmp_lt_u32 s40, 253\n"
    "s_cselect_b32 s40, s40, 0\n"
    "s_and_b32 s40, s40, 0xfc\n"
    "s_getpc_b64 s[42:43]\n"
    "s_sub_u32 s40, 272, s40\n"
    "s_add_u32 s42, s42, s40\n"
    "s_addc_u32 s43, s43, 0\n"
    "s_setpc_b64 s[42:43]\n"
    ".rept 63\n"
    "s_nop 0\n"
    ".endr\n"
    "LRB%=:\n"
    "s_barrier\n"
    "s_add_u32 m0, s62, 147456\n"
    "s_nop 0\n"
    "buffer_load_dwordx4 v0, s[16:19], s49 offen offset:0 sc1 lds\n"
    "buffer_load_dwordx4 v0, s[16:19], s49 offen offset:1024 sc1 lds\n"
    "v_mov_b32 v112, 158228\n"
    "ds_read_b32 v112, v112 offset:0\n"
    "s_waitcnt lgkmcnt(0)\n"
    "v_readfirstlane_b32 s44, v112\n"
    "s_waitcnt vmcnt(0)\n"
    "s_branch LC%=\n"
    "LMR%=:\n"
    "s_nop 7\n"
    "s_waitcnt lgkmcnt(0)\n"
    "s_barrier\n"
    "s_add_u32 s33, s33, 1\n"
    "s_cmp_gt_u32 s33, 0x80000\n"
    "s_cbranch_scc1 LFAIL%=\n"
    "s_add_u32 m0, s62, 147456\n"
    "s_nop 0\n"
    "buffer_load_dwordx4 v0, s[16:19], s49 offen offset:0 sc1 lds\n"
    "buffer_load_dwordx4 v0, s[16:19], s49 offen offset:1024 sc1 lds\n"
    "ds_read_b128 v[64:67], v7 offset:0\n"
    "ds_read_b128 v[68:71], v7 offset:1024\n"
    "ds_read_b128 v[72:75], v7 offset:2048\n"
    "ds_read_b128 v[76:79], v7 offset:3072\n"
    "ds_read_b128 v[80:83], v7 offset:4096\n"
    "ds_read_b128 v[84:87], v7 offset:5120\n"
    "ds_read_b128 v[88:91], v7 offset:6144\n"
    "ds_read_b128 v[92:95], v7 offset:7168\n"
    "ds_read_b128 v[96:99], v7 offset:8192\n"
    "ds_read_b128 v[100:103], v7 offset:9216\n"
    "ds_read_b128 v[104:107], v7 offset:10240\n"
    "ds_read_b128 v[108:111], v7 offset:11264\n"
    "v_mov_b64 v[114:115], 0\n"
    "s_nop 1\n"
    "v_mfma_f32_32x32x8_bf16 v[48:63], v[114:115], v[114:115], 0\n"
    "s_waitcnt vmcnt(0) lgkmcnt(0)\n"
    "s_branch LC%=\n"
    "LCA%=:\n"
    "s_mov_b32 s37, 8\n"
    "LCB%=:\n"
    "s_memrealtime s[42:43]\n"
    "s_memtime s[40:41]\n"
    "s_waitcnt lgkmcnt(0)\n"
    "s_sub_u32 s42, s42, s44\n"
    "s_cmp_gt_u32 s42, -2000\n"
    "s_cbranch_scc1 LCB%=\n"
    "s_mul_i32 s42, s42, 24\n"
    "s_sub_u32 s40, s40, s42\n"
    "s_cmp_eq_u32 s37, 8\n"
    "s_cselect_b32 s54, s40, s54\n"
    "s_sub_u32 s40, s40, s54\n"
    "s_add_u32 s45, s45, s40\n"
    "s_add_u32 s44, s44, 12\n"
    "s_sub_u32 s37, s37, 1\n"
    "s_cmp_lg_u32 s37, 0\n"
    "s_cbranch_scc1 LCB%=\n"
    "s_ashr_i32 s45, s45, 3\n"
    "s_add_u32 s45, s45, s54\n"
    "s_sub_u32 s45, s45, 0x100000\n"
    "s_add_u32 s51, s51, 0x40000000\n"
    "s_add_u32 s52, s52, 4\n"
    "s_addc_u32 s53, s53, 0\n"
    "s_setpc_b64 s[52:53]\n"
    "LGR%=:\n"
    "s_mov_b32 s37, 0x80000\n"
    "LGL%=:\n"
    "s_mov_b32 s38, 0\n"
    "v_and_b32 v76, 1, v19\n"
    "v_cmp_ne_u32 vcc, s64, v76\n"
    "s_cbranch_vccz LGJ0_%=\n"
    "s_mov_b64 exec, vcc\n"
    "buffer_load_dwordx4 v[16:19], v0, s[16:19], s51 offen offset:0 sc1\n"
    "s_mov_b64 exec, -1\n"
    "s_mov_b32 s38, 1\n"
    "LGJ0_%=:\n"
    "v_and_b32 v76, 1, v23\n"
    "v_cmp_ne_u32 vcc, s64, v76\n"
    "s_cbranch_vccz LGJ1_%=\n"
    "s_mov_b64 exec, vcc\n"
    "buffer_load_dwordx4 v[20:23], v0, s[16:19], s51 offen offset:1024 sc1\n"
    "s_mov_b64 exec, -1\n"
    "s_mov_b32 s38, 1\n"
    "LGJ1_%=:\n"
    "v_and_b32 v76, 1, v27\n"
    "v_cmp_ne_u32 vcc, s64, v76\n"
    "s_cbranch_vccz LGJ2_%=\n"
    "s_mov_b64 exec, vcc\n"
    "buffer_load_dwordx4 v[24:27], v0, s[16:19], s51 offen offset:2048 sc1\n"
    "s_mov_b64 exec, -1\n"
    "s_mov_b32 s38, 1\n"
    "LGJ2_%=:\n"
    "v_and_b32 v76, 1, v31\n"
    "v_cmp_ne_u32 vcc, s64, v76\n"
    "s_cbranch_vccz LGJ3_%=\n"
    "s_mov_b64 exec, vcc\n"
    "buffer_load_dwordx4 v[28:31], v0, s[16:19], s51 offen offset:3072 sc1\n"
    "s_mov_b64 exec, -1\n"
    "s_mov_b32 s38, 1\n"
    "LGJ3_%=:\n"
    "v_and_b32 v76, 1, v35\n"
    "v_cmp_ne_u32 vcc, s64, v76\n"
    "s_cbranch_vccz LGJ4_%=\n"
    "s_mov_b64 exec, vcc\n"
    "buffer_load_dwordx4 v[32:35], v0, s[16:19], s52 offen offset:0 sc1\n"
    "s_mov_b64 exec, -1\n"
    "s_mov_b32 s38, 1\n"
    "LGJ4_%=:\n"
    "v_and_b32 v76, 1, v39\n"
    "v_cmp_ne_u32 vcc, s64, v76\n"
    "s_cbranch_vccz LGJ5_%=\n"
    "s_mov_b64 exec, vcc\n"
    "buffer_load_dwordx4 v[36:39], v0, s[16:19], s52 offen offset:1024 sc1\n"
    "s_mov_b64 exec, -1\n"
    "s_mov_b32 s38, 1\n"
    "LGJ5_%=:\n"
    "v_and_b32 v76, 1, v43\n"
    "v_cmp_ne_u32 vcc, s64, v76\n"
    "s_cbranch_vccz LGJ6_%=\n"
    "s_mov_b64 exec, vcc\n"
    "buffer_load_dwordx4 v[40:43], v0, s[16:19], s52 offen offset:2048 sc1\n"
    "s_mov_b64 exec, -1\n"
    "s_mov_b32 s38, 1\n"
    "LGJ6_%=:\n"
    "v_and_b32 v76, 1, v47\n"
    "v_cmp_ne_u32 vcc, s64, v76\n"
    "s_cbranch_vccz LGJ7_%=\n"
    "s_mov_b64 exec, vcc\n"
    "buffer_load_dwordx4 v[44:47], v0, s[16:19], s52 offen offset:3072 sc1\n"
    "s_mov_b64 exec, -1\n"
    "s_mov_b32 s38, 1\n"
    "LGJ7_%=:\n"
    "s_cmp_eq_u32 s38, 0\n"
    "s_cbranch_scc1 LGA%=\n"
    "s_waitcnt vmcnt(0)\n"
    "s_sleep 1\n"
    "s_sub_u32 s37, s37, 1\n"
    "s_cbranch_scc0 LGL%=\n"
    "s_branch LFAIL%=\n"
    "LFAIL%=:\n"
    "buffer_store_dword v1, off, s[16:19], s35 sc1\n"
    "s_endpgm\n"
    :
    : "{v0}"(hv0), "{v1}"(hv1), "{v2}"(hv2), "{v3}"(hv3), "{v4}"(hv4), "{v5}"(hv5), "{v6}"(hv6), "{v7}"(hv7), "{v8}"(hv8), "{v9}"(hv9), "{v10}"(hv10), "{v11}"(hv11), "{v12}"(hv12), "{v13}"(hv13), "{v14}"(hv14), "{v15}"(hv15), "{v16}"(hv16), "{v17}"(hv17), "{v18}"(hv18), "{v19}"(hv19), "{v20}"(hv20)
    : "v255", "a255", "s101", "vcc", "scc", "memory");
  } else {
  asm volatile(
    "s_mov_b32 s33, 0\n"
    "s_nop 1\n"
    "v_mov_b32 v119, v20\n"
    "s_nop 0\n"
    "v_readlane_b32 s61, v119, 0\n"
    "v_readlane_b32 s46, v119, 1\n"
    "v_readlane_b32 s63, v119, 2\n"
    "v_readlane_b32 s48, v119, 3\n"
    "v_readlane_b32 s55, v119, 4\n"
    "v_readlane_b32 s56, v119, 5\n"
    "v_readlane_b32 s57, v119, 6\n"
    "v_readlane_b32 s74, v119, 7\n"
    "v_readlane_b32 s58, v119, 8\n"
    "v_readlane_b32 s59, v119, 9\n"
    "v_readlane_b32 s60, v119, 10\n"
    "v_readlane_b32 s67, v119, 11\n"
    "v_readlane_b32 s68, v119, 12\n"
    "v_readlane_b32 s69, v119, 13\n"
    "v_readlane_b32 s70, v119, 14\n"
    "v_readlane_b32 s71, v119, 15\n"
    "v_readlane_b32 s73, v119, 16\n"
    "v_readlane_b32 s44, v119, 17\n"
    "v_readlane_b32 s35, v119, 18\n"
    "v_readlane_b32 s75, v119, 19\n"
    "v_readlane_b32 s16, v119, 28\n"
    "v_readlane_b32 s17, v119, 29\n"
    "v_readlane_b32 s18, v119, 30\n"
    "v_readlane_b32 s8, v119, 31\n"
    "v_readlane_b32 s9, v119, 32\n"
    "v_readlane_b32 s10, v119, 33\n"
    "v_readlane_b32 s11, v119, 34\n"
    "v_readlane_b32 s12, v119, 35\n"
    "s_mov_b32 s19, 0x20000\n"
    "v_readlane_b32 s84, v119, 20\n"
    "v_readlane_b32 s85, v119, 21\n"
    "v_readlane_b32 s86, v119, 22\n"
    "v_readlane_b32 s87, v119, 23\n"
    "v_readlane_b32 s88, v119, 24\n"
    "v_readlane_b32 s89, v119, 25\n"
    "v_readlane_b32 s90, v119, 26\n"
    "v_readlane_b32 s91, v119, 27\n"
    "s_mov_b32 s76, 0xff00ff00\n"
    "s_mov_b32 s77, 0xff00ff00\n"
    "s_mov_b32 s78, 0x00ff00ff\n"
    "s_mov_b32 s79, 0x00ff00ff\n"
    "s_mov_b32 s65, 0x3e0293ee\n"
    "s_mov_b32 s66, 0xfffefffe\n"
    "s_lshl_b32 s62, s61, 11\n"
    "s_mov_b32 s36, 0\n"
    "s_mov_b32 s64, 0\n"
    "s_mov_b32 s50, 0\n"
    "s_lshl_b32 s72, s61, 2\n"
    "s_add_u32 s72, s72, 158208\n"
    "s_add_u32 s47, s46, 4096\n"
    "s_and_b32 s20, s61, 1\n"
    "s_lshl_b32 s20, s20, 7\n"
    "s_mov_b32 s14, 0x3f803f80\n"
    "s_mov_b32 s15, 0x3f803f80\n"
    "s_mov_b32 s82, 0x400000\n"
    "s_mov_b32 s83, 0x20000\n"
    "v_mov_b32 v112, v16\n"
    "v_mov_b32 v113, v17\n"
    "v_mov_b32 v114, v18\n"
    "v_mov_b32 v115, v19\n"
    "s_mov_b32 s80, s84\n"
    "s_mov_b32 s81, s85\n"
    "s_lshl_b32 s38, s62, 1\n"
    "s_add_u32 m0, s38, 0\n"
    "s_mov_b32 s39, 0\n"
    "buffer_load_dwordx4 v112, s[80:83], s39 offen lds\n"
    "s_add_u32 m0, s38, 1024\n"
    "s_mov_b32 s39, 32768\n"
    "buffer_load_dwordx4 v112, s[80:83], s39 offen lds\n"
    "s_add_u32 m0, s38, 2048\n"
    "s_mov_b32 s39, 65536\n"
    "buffer_load_dwordx4 v112, s[80:83], s39 offen lds\n"
    "s_add_u32 m0, s38, 3072\n"
    "s_mov_b32 s39, 98304\n"
    "buffer_load_dwordx4 v112, s[80:83], s39 offen lds\n"
    "s_lshl_b32 s38, s62, 1\n"
    "s_add_u32 m0, s38, 16384\n"
    "s_mov_b32 s39, 524288\n"
    "buffer_load_dwordx4 v112, s[80:83], s39 offen lds\n"
    "s_add_u32 m0, s38, 17408\n"
    "s_mov_b32 s39, 557056\n"
    "buffer_load_dwordx4 v112, s[80:83], s39 offen lds\n"
    "s_add_u32 m0, s38, 18432\n"
    "s_mov_b32 s39, 589824\n"
    "buffer_load_dwordx4 v112, s[80:83], s39 offen lds\n"
    "s_add_u32 m0, s38, 19456\n"
    "s_mov_b32 s39, 622592\n"
    "buffer_load_dwordx4 v112, s[80:83], s39 offen lds\n"
    "s_lshl_b32 s38, s62, 1\n"
    "s_add_u32 m0, s38, 32768\n"
    "s_mov_b32 s39, 1048576\n"
    "buffer_load_dwordx4 v112, s[80:83], s39 offen lds\n"
    "s_add_u32 m0, s38, 33792\n"
    "s_mov_b32 s39, 1081344\n"
    "buffer_load_dwordx4 v112, s[80:83], s39 offen lds\n"
    "s_add_u32 m0, s38, 34816\n"
    "s_mov_b32 s39, 1114112\n"
    "buffer_load_dwordx4 v112, s[80:83], s39 offen lds\n"
    "s_add_u32 m0, s38, 35840\n"
    "s_mov_b32 s39, 1146880\n"
    "buffer_load_dwordx4 v112, s[80:83], s39 offen lds\n"
    "s_lshl_b32 s38, s62, 1\n"
    "s_add_u32 m0, s38, 49152\n"
    "s_mov_b32 s39, 1572864\n"
    "buffer_load_dwordx4 v112, s[80:83], s39 offen lds\n"
    "s_add_u32 m0, s38, 50176\n"
    "s_mov_b32 s39, 1605632\n"
    "buffer_load_dwordx4 v112, s[80:83], s39 offen lds\n"
    "s_add_u32 m0, s38, 51200\n"
    "s_mov_b32 s39, 1638400\n"
    "buffer_load_dwordx4 v112, s[80:83], s39 offen lds\n"
    "s_add_u32 m0, s38, 52224\n"
    "s_mov_b32 s39, 1671168\n"
    "buffer_load_dwordx4 v112, s[80:83], s39 offen lds\n"
    "s_lshl_b32 s38, s62, 1\n"
    "s_add_u32 m0, s38, 65536\n"
    "s_mov_b32 s39, 2097152\n"
    "buffer_load_dwordx4 v112, s[80:83], s39 offen lds\n"
    "s_add_u32 m0, s38, 66560\n"
    "s_mov_b32 s39, 2129920\n"
    "buffer_load_dwordx4 v112, s[80:83], s39 offen lds\n"
    "s_add_u32 m0, s38, 67584\n"
    "s_mov_b32 s39, 2162688\n"
    "buffer_load_dwordx4 v112, s[80:83], s39 offen lds\n"
    "s_add_u32 m0, s38, 68608\n"
    "s_mov_b32 s39, 2195456\n"
    "buffer_load_dwordx4 v112, s[80:83], s39 offen lds\n"
    "s_lshl_b32 s38, s62, 1\n"
    "s_add_u32 m0, s38, 81920\n"
    "s_mov_b32 s39, 2621440\n"
    "buffer_load_dwordx4 v112, s[80:83], s39 offen lds\n"
    "s_add_u32 m0, s38, 82944\n"
    "s_mov_b32 s39, 2654208\n"
    "buffer_load_dwordx4 v112, s[80:83], s39 offen lds\n"
    "s_add_u32 m0, s38, 83968\n"
    "s_mov_b32 s39, 2686976\n"
    "buffer_load_dwordx4 v112, s[80:83], s39 offen lds\n"
    "s_add_u32 m0, s38, 84992\n"
    "s_mov_b32 s39, 2719744\n"
    "buffer_load_dwordx4 v112, s[80:83], s39 offen lds\n"
    "s_sub_u32 s51, s44, 100\n"
    "s_mov_b32 s45, 0\n"
    "s_cmp_eq_u32 s61, 1\n"
    "s_cselect_b32 s38, 0, 0x40000000\n"
    "s_add_u32 s51, s51, s38\n"
    "s_waitcnt vmcnt(20)\n"
    "s_barrier\n"
    "s_lshl_b32 s38, s62, 1\n"
    "s_add_u32 m0, s38, 98304\n"
    "s_mov_b32 s39, 3145728\n"
    "buffer_load_dwordx4 v112, s[80:83], s39 offen lds\n"
    "s_add_u32 m0, s38, 99328\n"
    "s_mov_b32 s39, 3178496\n"
    "buffer_load_dwordx4 v112, s[80:83], s39 offen lds\n"
    "s_add_u32 m0, s38, 100352\n"
    "s_mov_b32 s39, 3211264\n"
    "buffer_load_dwordx4 v112, s[80:83], s39 offen lds\n"
    "s_add_u32 m0, s38, 101376\n"
    "s_mov_b32 s39, 3244032\n"
    "buffer_load_dwordx4 v112, s[80:83], s39 offen lds\n"
    "v_add_u32 v116, 0, v114\n"
    "ds_read_b64_tr_b16 v[104:105], v116 offset:0\n"
    "ds_read_b64_tr_b16 v[106:107], v116 offset:2048\n"
    "ds_read_b64_tr_b16 v[108:109], v116 offset:8192\n"
    "ds_read_b64_tr_b16 v[110:111], v116 offset:10240\n"
    "ds_read_b64_tr_b16 v[96:97], v116 offset:32\n"
    "ds_read_b64_tr_b16 v[98:99], v116 offset:2080\n"
    "ds_read_b64_tr_b16 v[100:101], v116 offset:8224\n"
    "ds_read_b64_tr_b16 v[102:103], v116 offset:10272\n"
    "s_waitcnt lgkmcnt(0)\n"
    "s_memrealtime s[42:43]\n"
    "s_waitcnt lgkmcnt(0)\n"
    "s_sub_u32 s42, s42, s51\n"
    "s_cmp_lt_i32 s42, 0\n"
    "s_cbranch_scc1 LCJ0_%=\n"
    "s_getpc_b64 s[52:53]\n"
    "s_branch LCA%=\n"
    "LCJ0_%=:\n"
    "s_waitcnt vmcnt(20)\n"
    "s_barrier\n"
    "s_lshl_b32 s38, s62, 1\n"
    "s_add_u32 m0, s38, 0\n"
    "s_mov_b32 s39, 3670016\n"
    "buffer_load_dwordx4 v112, s[80:83], s39 offen lds\n"
    "s_add_u32 m0, s38, 1024\n"
    "s_mov_b32 s39, 3702784\n"
    "buffer_load_dwordx4 v112, s[80:83], s39 offen lds\n"
    "s_add_u32 m0, s38, 2048\n"
    "s_mov_b32 s39, 3735552\n"
    "buffer_load_dwordx4 v112, s[80:83], s39 offen lds\n"
    "s_add_u32 m0, s38, 3072\n"
    "s_mov_b32 s39, 3768320\n"
    "buffer_load_dwordx4 v112, s[80:83], s39 offen lds\n"
    "v_add_u32 v116, 16384, v114\n"
    "ds_read_b64_tr_b16 v[88:89], v116 offset:0\n"
    "ds_read_b64_tr_b16 v[90:91], v116 offset:2048\n"
    "ds_read_b64_tr_b16 v[92:93], v116 offset:8192\n"
    "ds_read_b64_tr_b16 v[94:95], v116 offset:10240\n"
    "ds_read_b64_tr_b16 v[16:17], v116 offset:32\n"
    "ds_read_b64_tr_b16 v[18:19], v116 offset:2080\n"
    "ds_read_b64_tr_b16 v[20:21], v116 offset:8224\n"
    "ds_read_b64_tr_b16 v[22:23], v116 offset:10272\n"
    "ds_write_b128 v9, v[104:107] offset:0\n"
    "ds_write_b128 v9, v[108:111] offset:1024\n"
    "ds_write_b128 v9, v[96:99] offset:8192\n"
    "ds_write_b128 v9, v[100:103] offset:9216\n"
    "s_waitcnt lgkmcnt(10)\n"
    "ds_write_b128 v9, v[88:91] offset:16384\n"
    "s_waitcnt lgkmcnt(9)\n"
    "ds_write_b128 v9, v[92:95] offset:17408\n"
    "s_waitcnt lgkmcnt(0)\n"
    "s_memrealtime s[42:43]\n"
    "s_waitcnt lgkmcnt(0)\n"
    "s_sub_u32 s42, s42, s51\n"
    "s_cmp_lt_i32 s42, 0\n"
    "s_cbranch_scc1 LCJ1_%=\n"
    "s_getpc_b64 s[52:53]\n"
    "s_branch LCA%=\n"
    "LCJ1_%=:\n"
    "s_waitcnt vmcnt(20)\n"
    "s_barrier\n"
    "s_mov_b32 s80, s86\n"
    "s_mov_b32 s81, s87\n"
    "s_lshl_b32 s38, s62, 1\n"
    "s_add_u32 m0, s38, 16384\n"
    "s_mov_b32 s39, 0\n"
    "buffer_load_dwordx4 v112, s[80:83], s39 offen lds\n"
    "s_add_u32 m0, s38, 17408\n"
    "s_mov_b32 s39, 32768\n"
    "buffer_load_dwordx4 v112, s[80:83], s39 offen lds\n"
    "s_add_u32 m0, s38, 18432\n"
    "s_mov_b32 s39, 65536\n"
    "buffer_load_dwordx4 v112, s[80:83], s39 offen lds\n"
    "s_add_u32 m0, s38, 19456\n"
    "s_mov_b32 s39, 98304\n"
    "buffer_load_dwordx4 v112, s[80:83], s39 offen lds\n"
    "v_add_u32 v116, 32768, v114\n"
    "ds_read_b64_tr_b16 v[24:25], v116 offset:0\n"
    "ds_read_b64_tr_b16 v[26:27], v116 offset:2048\n"
    "ds_read_b64_tr_b16 v[28:29], v116 offset:8192\n"
    "ds_read_b64_tr_b16 v[30:31], v116 offset:10240\n"
    "ds_read_b64_tr_b16 v[32:33], v116 offset:32\n"
    "ds_read_b64_tr_b16 v[34:35], v116 offset:2080\n"
    "ds_read_b64_tr_b16 v[36:37], v116 offset:8224\n"
    "ds_read_b64_tr_b16 v[38:39], v116 offset:10272\n"
    "s_waitcnt lgkmcnt(0)\n"
    "s_memrealtime s[42:43]\n"
    "s_waitcnt lgkmcnt(0)\n"
    "s_sub_u32 s42, s42, s51\n"
    "s_cmp_lt_i32 s42, 0\n"
    "s_cbranch_scc1 LCJ2_%=\n"
    "s_getpc_b64 s[52:53]\n"
    "s_branch LCA%=\n"
    "LCJ2_%=:\n"
    "s_waitcnt vmcnt(20)\n"
    "s_barrier\n"
    "s_lshl_b32 s38, s62, 1\n"
    "s_add_u32 m0, s38, 32768\n"
    "s_mov_b32 s39, 524288\n"
    "buffer_load_dwordx4 v112, s[80:83], s39 offen lds\n"
    "s_add_u32 m0, s38, 33792\n"
    "s_mov_b32 s39, 557056\n"
    "buffer_load_dwordx4 v112, s[80:83], s39 offen lds\n"
    "s_add_u32 m0, s38, 34816\n"
    "s_mov_b32 s39, 589824\n"
    "buffer_load_dwordx4 v112, s[80:83], s39 offen lds\n"
    "s_add_u32 m0, s38, 35840\n"
    "s_mov_b32 s39, 622592\n"
    "buffer_load_dwordx4 v112, s[80:83], s39 offen lds\n"
    "v_add_u32 v116, 49152, v114\n"
    "ds_read_b64_tr_b16 a[0:1], v116 offset:0\n"
    "ds_read_b64_tr_b16 a[2:3], v116 offset:2048\n"
    "ds_read_b64_tr_b16 a[4:5], v116 offset:8192\n"
    "ds_read_b64_tr_b16 a[6:7], v116 offset:10240\n"
    "ds_read_b64_tr_b16 a[8:9], v116 offset:32\n"
    "ds_read_b64_tr_b16 a[10:11], v116 offset:2080\n"
    "ds_read_b64_tr_b16 a[12:13], v116 offset:8224\n"
    "ds_read_b64_tr_b16 a[14:15], v116 offset:10272\n"
    "s_waitcnt lgkmcnt(0)\n"
    "s_memrealtime s[42:43]\n"
    "s_waitcnt lgkmcnt(0)\n"
    "s_sub_u32 s42, s42, s51\n"
    "s_cmp_lt_i32 s42, 0\n"
    "s_cbranch_scc1 LCJ3_%=\n"
    "s_getpc_b64 s[52:53]\n"
    "s_branch LCA%=\n"
    "LCJ3_%=:\n"
    "s_waitcnt vmcnt(20)\n"
    "s_barrier\n"
    "s_lshl_b32 s38, s62, 1\n"
    "s_add_u32 m0, s38, 49152\n"
    "s_mov_b32 s39, 1048576\n"
    "buffer_load_dwordx4 v112, s[80:83], s39 offen lds\n"
    "s_add_u32 m0, s38, 50176\n"
    "s_mov_b32 s39, 1081344\n"
    "buffer_load_dwordx4 v112, s[80:83], s39 offen lds\n"
    "s_add_u32 m0, s38, 51200\n"
    "s_mov_b32 s39, 1114112\n"
    "buffer_load_dwordx4 v112, s[80:83], s39 offen lds\n"
    "s_add_u32 m0, s38, 52224\n"
    "s_mov_b32 s39, 1146880\n"
    "buffer_load_dwordx4 v112, s[80:83], s39 offen lds\n"
    "v_add_u32 v116, 65536, v114\n"
    "ds_read_b64_tr_b16 a[16:17], v116 offset:0\n"
    "ds_read_b64_tr_b16 a[18:19], v116 offset:2048\n"
    "ds_read_b64_tr_b16 a[20:21], v116 offset:8192\n"
    "ds_read_b64_tr_b16 a[22:23], v116 offset:10240\n"
    "ds_read_b64_tr_b16 a[24:25], v116 offset:32\n"
    "ds_read_b64_tr_b16 a[26:27], v116 offset:2080\n"
    "ds_read_b64_tr_b16 a[28:29], v116 offset:8224\n"
    "ds_read_b64_tr_b16 a[30:31], v116 offset:10272\n"
    "s_waitcnt lgkmcnt(0)\n"
    "s_memrealtime s[42:43]\n"
    "s_waitcnt lgkmcnt(0)\n"
    "s_sub_u32 s42, s42, s51\n"
    "s_cmp_lt_i32 s42, 0\n"
    "s_cbranch_scc1 LCJ4_%=\n"
    "s_getpc_b64 s[52:53]\n"
    "s_branch LCA%=\n"
    "LCJ4_%=:\n"
    "s_waitcnt vmcnt(20)\n"
    "s_barrier\n"
    "s_lshl_b32 s38, s62, 1\n"
    "s_add_u32 m0, s38, 65536\n"
    "s_mov_b32 s39, 1572864\n"
    "buffer_load_dwordx4 v112, s[80:83], s39 offen lds\n"
    "s_add_u32 m0, s38, 66560\n"
    "s_mov_b32 s39, 1605632\n"
    "buffer_load_dwordx4 v112, s[80:83], s39 offen lds\n"
    "s_add_u32 m0, s38, 67584\n"
    "s_mov_b32 s39, 1638400\n"
    "buffer_load_dwordx4 v112, s[80:83], s39 offen lds\n"
    "s_add_u32 m0, s38, 68608\n"
    "s_mov_b32 s39, 1671168\n"
    "buffer_load_dwordx4 v112, s[80:83], s39 offen lds\n"
    "v_add_u32 v116, 81920, v114\n"
    "ds_read_b64_tr_b16 a[32:33], v116 offset:0\n"
    "ds_read_b64_tr_b16 a[34:35], v116 offset:2048\n"
    "ds_read_b64_tr_b16 a[36:37], v116 offset:8192\n"
    "ds_read_b64_tr_b16 a[38:39], v116 offset:10240\n"
    "ds_read_b64_tr_b16 a[40:41], v116 offset:32\n"
    "ds_read_b64_tr_b16 a[42:43], v116 offset:2080\n"
    "ds_read_b64_tr_b16 a[44:45], v116 offset:8224\n"
    "ds_read_b64_tr_b16 a[46:47], v116 offset:10272\n"
    "s_waitcnt lgkmcnt(0)\n"
    "s_memrealtime s[42:43]\n"
    "s_waitcnt lgkmcnt(0)\n"
    "s_sub_u32 s42, s42, s51\n"
    "s_cmp_lt_i32 s42, 0\n"
    "s_cbranch_scc1 LCJ5_%=\n"
    "s_getpc_b64 s[52:53]\n"
    "s_branch LCA%=\n"
    "LCJ5_%=:\n"
    "s_waitcnt vmcnt(20)\n"
    "s_barrier\n"
    "s_lshl_b32 s38, s62, 1\n"
    "s_add_u32 m0, s38, 81920\n"
    "s_mov_b32 s39, 2097152\n"
    "buffer_load_dwordx4 v112, s[80:83], s39 offen lds\n"
    "s_add_u32 m0, s38, 82944\n"
    "s_mov_b32 s39, 2129920\n"
    "buffer_load_dwordx4 v112, s[80:83], s39 offen lds\n"
    "s_add_u32 m0, s38, 83968\n"
    "s_mov_b32 s39, 2162688\n"
    "buffer_load_dwordx4 v112, s[80:83], s39 offen lds\n"
    "s_add_u32 m0, s38, 84992\n"
    "s_mov_b32 s39, 2195456\n"
    "buffer_load_dwordx4 v112, s[80:83], s39 offen lds\n"
    "v_add_u32 v116, 98304, v114\n"
    "ds_read_b64_tr_b16 a[48:49], v116 offset:0\n"
    "ds_read_b64_tr_b16 a[50:51], v116 offset:2048\n"
    "ds_read_b64_tr_b16 a[52:53], v116 offset:8192\n"
    "ds_read_b64_tr_b16 a[54:55], v116 offset:10240\n"
    "ds_read_b64_tr_b16 a[56:57], v116 offset:32\n"
    "ds_read_b64_tr_b16 a[58:59], v116 offset:2080\n"
    "ds_read_b64_tr_b16 a[60:61], v116 offset:8224\n"
    "ds_read_b64_tr_b16 a[62:63], v116 offset:10272\n"
    "s_waitcnt lgkmcnt(0)\n"
    "s_memrealtime s[42:43]\n"
    "s_waitcnt lgkmcnt(0)\n"
    "s_sub_u32 s42, s42, s51\n"
    "s_cmp_lt_i32 s42, 0\n"
    "s_cbranch_scc1 LCJ6_%=\n"
    "s_getpc_b64 s[52:53]\n"
    "s_branch LCA%=\n"
    "LCJ6_%=:\n"
    "s_waitcnt vmcnt(20)\n"
    "s_barrier\n"
    "s_lshl_b32 s38, s62, 1\n"
    "s_add_u32 m0, s38, 98304\n"
    "s_mov_b32 s39, 2621440\n"
    "buffer_load_dwordx4 v112, s[80:83], s39 offen lds\n"
    "s_add_u32 m0, s38, 99328\n"
    "s_mov_b32 s39, 2654208\n"
    "buffer_load_dwordx4 v112, s[80:83], s39 offen lds\n"
    "s_add_u32 m0, s38, 100352\n"
    "s_mov_b32 s39, 2686976\n"
    "buffer_load_dwordx4 v112, s[80:83], s39 offen lds\n"
    "s_add_u32 m0, s38, 101376\n"
    "s_mov_b32 s39, 2719744\n"
    "buffer_load_dwordx4 v112, s[80:83], s39 offen lds\n"
    "v_add_u32 v116, 0, v114\n"
    "ds_read_b64_tr_b16 a[64:65], v116 offset:0\n"
    "ds_read_b64_tr_b16 a[66:67], v116 offset:2048\n"
    "ds_read_b64_tr_b16 a[68:69], v116 offset:8192\n"
    "ds_read_b64_tr_b16 a[70:71], v116 offset:10240\n"
    "ds_read_b64_tr_b16 a[72:73], v116 offset:32\n"
    "ds_read_b64_tr_b16 a[74:75], v116 offset:2080\n"
    "ds_read_b64_tr_b16 a[76:77], v116 offset:8224\n"
    "ds_read_b64_tr_b16 a[78:79], v116 offset:10272\n"
    "s_waitcnt lgkmcnt(0)\n"
    "s_memrealtime s[42:43]\n"
    "s_waitcnt lgkmcnt(0)\n"
    "s_sub_u32 s42, s42, s51\n"
    "s_cmp_lt_i32 s42, 0\n"
    "s_cbranch_scc1 LCJ7_%=\n"
    "s_getpc_b64 s[52:53]\n"
    "s_branch LCA%=\n"
    "LCJ7_%=:\n"
    "s_waitcnt vmcnt(20)\n"
    "s_barrier\n"
    "s_lshl_b32 s38, s62, 1\n"
    "s_add_u32 m0, s38, 0\n"
    "s_mov_b32 s39, 3145728\n"
    "buffer_load_dwordx4 v112, s[80:83], s39 offen lds\n"
    "s_add_u32 m0, s38, 1024\n"
    "s_mov_b32 s39, 3178496\n"
    "buffer_load_dwordx4 v112, s[80:83], s39 offen lds\n"
    "s_add_u32 m0, s38, 2048\n"
    "s_mov_b32 s39, 3211264\n"
    "buffer_load_dwordx4 v112, s[80:83], s39 offen lds\n"
    "s_add_u32 m0, s38, 3072\n"
    "s_mov_b32 s39, 3244032\n"
    "buffer_load_dwordx4 v112, s[80:83], s39 offen lds\n"
    "v_add_u32 v116, 16384, v114\n"
    "ds_read_b64_tr_b16 v[40:41], v116 offset:0\n"
    "ds_read_b64_tr_b16 v[42:43], v116 offset:2048\n"
    "ds_read_b64_tr_b16 v[44:45], v116 offset:8192\n"
    "ds_read_b64_tr_b16 v[46:47], v116 offset:10240\n"
    "ds_read_b64_tr_b16 v[48:49], v116 offset:32\n"
    "ds_read_b64_tr_b16 v[50:51], v116 offset:2080\n"
    "ds_read_b64_tr_b16 v[52:53], v116 offset:8224\n"
    "ds_read_b64_tr_b16 v[54:55], v116 offset:10272\n"
    "s_waitcnt lgkmcnt(0)\n"
    "s_memrealtime s[42:43]\n"
    "s_waitcnt lgkmcnt(0)\n"
    "s_sub_u32 s42, s42, s51\n"
    "s_cmp_lt_i32 s42, 0\n"
    "s_cbranch_scc1 LCJ8_%=\n"
    "s_getpc_b64 s[52:53]\n"
    "s_branch LCA%=\n"
    "LCJ8_%=:\n"
    "s_waitcnt vmcnt(20)\n"
    "s_barrier\n"
    "s_lshl_b32 s38, s62, 1\n"
    "s_add_u32 m0, s38, 16384\n"
    "s_mov_b32 s39, 3670016\n"
    "buffer_load_dwordx4 v112, s[80:83], s39 offen lds\n"
    "s_add_u32 m0, s38, 17408\n"
    "s_mov_b32 s39, 3702784\n"
    "buffer_load_dwordx4 v112, s[80:83], s39 offen lds\n"
    "s_add_u32 m0, s38, 18432\n"
    "s_mov_b32 s39, 3735552\n"
    "buffer_load_dwordx4 v112, s[80:83], s39 offen lds\n"
    "s_add_u32 m0, s38, 19456\n"
    "s_mov_b32 s39, 3768320\n"
    "buffer_load_dwordx4 v112, s[80:83], s39 offen lds\n"
    "v_add_u32 v116, 32768, v114\n"
    "ds_read_b64_tr_b16 v[56:57], v116 offset:0\n"
    "ds_read_b64_tr_b16 v[58:59], v116 offset:2048\n"
    "ds_read_b64_tr_b16 v[60:61], v116 offset:8192\n"
    "ds_read_b64_tr_b16 v[62:63], v116 offset:10240\n"
    "ds_read_b64_tr_b16 v[64:65], v116 offset:32\n"
    "ds_read_b64_tr_b16 v[66:67], v116 offset:2080\n"
    "ds_read_b64_tr_b16 v[68:69], v116 offset:8224\n"
    "ds_read_b64_tr_b16 v[70:71], v116 offset:10272\n"
    "s_waitcnt lgkmcnt(0)\n"
    "s_memrealtime s[42:43]\n"
    "s_waitcnt lgkmcnt(0)\n"
    "s_sub_u32 s42, s42, s51\n"
    "s_cmp_lt_i32 s42, 0\n"
    "s_cbranch_scc1 LCJ9_%=\n"
    "s_getpc_b64 s[52:53]\n"
    "s_branch LCA%=\n"
    "LCJ9_%=:\n"
    "s_waitcnt vmcnt(20)\n"
    "s_barrier\n"
    "s_mov_b32 s80, s88\n"
    "s_mov_b32 s81, s89\n"
    "s_lshl_b32 s38, s62, 1\n"
    "s_add_u32 m0, s38, 32768\n"
    "s_mov_b32 s39, 0\n"
    "buffer_load_dwordx4 v112, s[80:83], s39 offen lds\n"
    "s_add_u32 m0, s38, 33792\n"
    "s_mov_b32 s39, 32768\n"
    "buffer_load_dwordx4 v112, s[80:83], s39 offen lds\n"
    "s_add_u32 m0, s38, 34816\n"
    "s_mov_b32 s39, 65536\n"
    "buffer_load_dwordx4 v112, s[80:83], s39 offen lds\n"
    "s_add_u32 m0, s38, 35840\n"
    "s_mov_b32 s39, 98304\n"
    "buffer_load_dwordx4 v112, s[80:83], s39 offen lds\n"
    "v_add_u32 v116, 49152, v114\n"
    "ds_read_b64_tr_b16 a[80:81], v116 offset:0\n"
    "ds_read_b64_tr_b16 a[82:83], v116 offset:2048\n"
    "ds_read_b64_tr_b16 a[84:85], v116 offset:8192\n"
    "ds_read_b64_tr_b16 a[86:87], v116 offset:10240\n"
    "ds_read_b64_tr_b16 a[88:89], v116 offset:32\n"
    "ds_read_b64_tr_b16 a[90:91], v116 offset:2080\n"
    "ds_read_b64_tr_b16 a[92:93], v116 offset:8224\n"
    "ds_read_b64_tr_b16 a[94:95], v116 offset:10272\n"
    "s_waitcnt lgkmcnt(0)\n"
    "s_memrealtime s[42:43]\n"
    "s_waitcnt lgkmcnt(0)\n"
    "s_sub_u32 s42, s42, s51\n"
    "s_cmp_lt_i32 s42, 0\n"
    "s_cbranch_scc1 LCJ10_%=\n"
    "s_getpc_b64 s[52:53]\n"
    "s_branch LCA%=\n"
    "LCJ10_%=:\n"
    "s_waitcnt vmcnt(20)\n"
    "s_barrier\n"
    "s_lshl_b32 s38, s62, 1\n"
    "s_add_u32 m0, s38, 49152\n"
    "s_mov_b32 s39, 524288\n"
    "buffer_load_dwordx4 v112, s[80:83], s39 offen lds\n"
    "s_add_u32 m0, s38, 50176\n"
    "s_mov_b32 s39, 557056\n"
    "buffer_load_dwordx4 v112, s[80:83], s39 offen lds\n"
    "s_add_u32 m0, s38, 51200\n"
    "s_mov_b32 s39, 589824\n"
    "buffer_load_dwordx4 v112, s[80:83], s39 offen lds\n"
    "s_add_u32 m0, s38, 52224\n"
    "s_mov_b32 s39, 622592\n"
    "buffer_load_dwordx4 v112, s[80:83], s39 offen lds\n"
    "v_add_u32 v116, 65536, v114\n"
    "ds_read_b64_tr_b16 a[96:97], v116 offset:0\n"
    "ds_read_b64_tr_b16 a[98:99], v116 offset:2048\n"
    "ds_read_b64_tr_b16 a[100:101], v116 offset:8192\n"
    "ds_read_b64_tr_b16 a[102:103], v116 offset:10240\n"
    "ds_read_b64_tr_b16 a[104:105], v116 offset:32\n"
    "ds_read_b64_tr_b16 a[106:107], v116 offset:2080\n"
    "ds_read_b64_tr_b16 a[108:109], v116 offset:8224\n"
    "ds_read_b64_tr_b16 a[110:111], v116 offset:10272\n"
    "s_waitcnt lgkmcnt(0)\n"
    "s_memrealtime s[42:43]\n"
    "s_waitcnt lgkmcnt(0)\n"
    "s_sub_u32 s42, s42, s51\n"
    "s_cmp_lt_i32 s42, 0\n"
    "s_cbranch_scc1 LCJ11_%=\n"
    "s_getpc_b64 s[52:53]\n"
    "s_branch LCA%=\n"
    "LCJ11_%=:\n"
    "s_waitcnt vmcnt(20)\n"
    "s_barrier\n"
    "s_lshl_b32 s38, s62, 1\n"
    "s_add_u32 m0, s38, 65536\n"
    "s_mov_b32 s39, 1048576\n"
    "buffer_load_dwordx4 v112, s[80:83], s39 offen lds\n"
    "s_add_u32 m0, s38, 66560\n"
    "s_mov_b32 s39, 1081344\n"
    "buffer_load_dwordx4 v112, s[80:83], s39 offen lds\n"
    "s_add_u32 m0, s38, 67584\n"
    "s_mov_b32 s39, 1114112\n"
    "buffer_load_dwordx4 v112, s[80:83], s39 offen lds\n"
    "s_add_u32 m0, s38, 68608\n"
    "s_mov_b32 s39, 1146880\n"
    "buffer_load_dwordx4 v112, s[80:83], s39 offen lds\n"
    "v_add_u32 v116, 81920, v114\n"
    "ds_read_b64_tr_b16 a[112:113], v116 offset:0\n"
    "ds_read_b64_tr_b16 a[114:115], v116 offset:2048\n"
    "ds_read_b64_tr_b16 a[116:117], v116 offset:8192\n"
    "ds_read_b64_tr_b16 a[118:119], v116 offset:10240\n"
    "ds_read_b64_tr_b16 a[120:121], v116 offset:32\n"
    "ds_read_b64_tr_b16 a[122:123], v116 offset:2080\n"
    "ds_read_b64_tr_b16 a[124:125], v116 offset:8224\n"
    "ds_read_b64_tr_b16 a[126:127], v116 offset:10272\n"
    "s_waitcnt lgkmcnt(0)\n"
    "s_memrealtime s[42:43]\n"
    "s_waitcnt lgkmcnt(0)\n"
    "s_sub_u32 s42, s42, s51\n"
    "s_cmp_lt_i32 s42, 0\n"
    "s_cbranch_scc1 LCJ12_%=\n"
    "s_getpc_b64 s[52:53]\n"
    "s_branch LCA%=\n"
    "LCJ12_%=:\n"
    "s_waitcnt vmcnt(20)\n"
    "s_barrier\n"
    "s_lshl_b32 s38, s62, 1\n"
    "s_add_u32 m0, s38, 81920\n"
    "s_mov_b32 s39, 1572864\n"
    "buffer_load_dwordx4 v112, s[80:83], s39 offen lds\n"
    "s_add_u32 m0, s38, 82944\n"
    "s_mov_b32 s39, 1605632\n"
    "buffer_load_dwordx4 v112, s[80:83], s39 offen lds\n"
    "s_add_u32 m0, s38, 83968\n"
    "s_mov_b32 s39, 1638400\n"
    "buffer_load_dwordx4 v112, s[80:83], s39 offen lds\n"
    "s_add_u32 m0, s38, 84992\n"
    "s_mov_b32 s39, 1671168\n"
    "buffer_load_dwordx4 v112, s[80:83], s39 offen lds\n"
    "v_add_u32 v116, 98304, v114\n"
    "ds_read_b64_tr_b16 a[128:129], v116 offset:0\n"
    "ds_read_b64_tr_b16 a[130:131], v116 offset:2048\n"
    "ds_read_b64_tr_b16 a[132:133], v116 offset:8192\n"
    "ds_read_b64_tr_b16 a[134:135], v116 offset:10240\n"
    "ds_read_b64_tr_b16 a[136:137], v116 offset:32\n"
    "ds_read_b64_tr_b16 a[138:139], v116 offset:2080\n"
    "ds_read_b64_tr_b16 a[140:141], v116 offset:8224\n"
    "ds_read_b64_tr_b16 a[142:143], v116 offset:10272\n"
    "s_waitcnt lgkmcnt(0)\n"
    "s_memrealtime s[42:43]\n"
    "s_waitcnt lgkmcnt(0)\n"
    "s_sub_u32 s42, s42, s51\n"
    "s_cmp_lt_i32 s42, 0\n"
    "s_cbranch_scc1 LCJ13_%=\n"
    "s_getpc_b64 s[52:53]\n"
    "s_branch LCA%=\n"
    "LCJ13_%=:\n"
    "s_waitcnt vmcnt(20)\n"
    "s_barrier\n"
    "s_lshl_b32 s38, s62, 1\n"
    "s_add_u32 m0, s38, 98304\n"
    "s_mov_b32 s39, 2097152\n"
    "buffer_load_dwordx4 v112, s[80:83], s39 offen lds\n"
    "s_add_u32 m0, s38, 99328\n"
    "s_mov_b32 s39, 2129920\n"
    "buffer_load_dwordx4 v112, s[80:83], s39 offen lds\n"
    "s_add_u32 m0, s38, 100352\n"
    "s_mov_b32 s39, 2162688\n"
    "buffer_load_dwordx4 v112, s[80:83], s39 offen lds\n"
    "s_add_u32 m0, s38, 101376\n"
    "s_mov_b32 s39, 2195456\n"
    "buffer_load_dwordx4 v112, s[80:83], s39 offen lds\n"
    "v_add_u32 v116, 0, v114\n"
    "ds_read_b64_tr_b16 a[144:145], v116 offset:0\n"
    "ds_read_b64_tr_b16 a[146:147], v116 offset:2048\n"
    "ds_read_b64_tr_b16 a[148:149], v116 offset:8192\n"
    "ds_read_b64_tr_b16 a[150:151], v116 offset:10240\n"
    "ds_read_b64_tr_b16 a[152:153], v116 offset:32\n"
    "ds_read_b64_tr_b16 a[154:155], v116 offset:2080\n"
    "ds_read_b64_tr_b16 a[156:157], v116 offset:8224\n"
    "ds_read_b64_tr_b16 a[158:159], v116 offset:10272\n"
    "s_waitcnt lgkmcnt(0)\n"
    "s_memrealtime s[42:43]\n"
    "s_waitcnt lgkmcnt(0)\n"
    "s_sub_u32 s42, s42, s51\n"
    "s_cmp_lt_i32 s42, 0\n"
    "s_cbranch_scc1 LCJ14_%=\n"
    "s_getpc_b64 s[52:53]\n"
    "s_branch LCA%=\n"
    "LCJ14_%=:\n"
    "s_waitcnt vmcnt(20)\n"
    "s_barrier\n"
    "s_lshl_b32 s38, s62, 1\n"
    "s_add_u32 m0, s38, 0\n"
    "s_mov_b32 s39, 2621440\n"
    "buffer_load_dwordx4 v112, s[80:83], s39 offen lds\n"
    "s_add_u32 m0, s38, 1024\n"
    "s_mov_b32 s39, 2654208\n"
    "buffer_load_dwordx4 v112, s[80:83], s39 offen lds\n"
    "s_add_u32 m0, s38, 2048\n"
    "s_mov_b32 s39, 2686976\n"
    "buffer_load_dwordx4 v112, s[80:83], s39 offen lds\n"
    "s_add_u32 m0, s38, 3072\n"
    "s_mov_b32 s39, 2719744\n"
    "buffer_load_dwordx4 v112, s[80:83], s39 offen lds\n"
    "v_add_u32 v116, 16384, v114\n"
    "ds_read_b64_tr_b16 a[160:161], v116 offset:0\n"
    "ds_read_b64_tr_b16 a[162:163], v116 offset:2048\n"
    "ds_read_b64_tr_b16 a[164:165], v116 offset:8192\n"
    "ds_read_b64_tr_b16 a[166:167], v116 offset:10240\n"
    "ds_read_b64_tr_b16 a[168:169], v116 offset:32\n"
    "ds_read_b64_tr_b16 a[170:171], v116 offset:2080\n"
    "ds_read_b64_tr_b16 a[172:173], v116 offset:8224\n"
    "ds_read_b64_tr_b16 a[174:175], v116 offset:10272\n"
    "s_waitcnt lgkmcnt(0)\n"
    "s_memrealtime s[42:43]\n"
    "s_waitcnt lgkmcnt(0)\n"
    "s_sub_u32 s42, s42, s51\n"
    "s_cmp_lt_i32 s42, 0\n"
    "s_cbranch_scc1 LCJ15_%=\n"
    "s_getpc_b64 s[52:53]\n"
    "s_branch LCA%=\n"
    "LCJ15_%=:\n"
    "s_waitcnt vmcnt(20)\n"
    "s_barrier\n"
    "s_lshl_b32 s38, s62, 1\n"
    "s_add_u32 m0, s38, 16384\n"
    "s_mov_b32 s39, 3145728\n"
    "buffer_load_dwordx4 v112, s[80:83], s39 offen lds\n"
    "s_add_u32 m0, s38, 17408\n"
    "s_mov_b32 s39, 3178496\n"
    "buffer_load_dwordx4 v112, s[80:83], s39 offen lds\n"
    "s_add_u32 m0, s38, 18432\n"
    "s_mov_b32 s39, 3211264\n"
    "buffer_load_dwordx4 v112, s[80:83], s39 offen lds\n"
    "s_add_u32 m0, s38, 19456\n"
    "s_mov_b32 s39, 3244032\n"
    "buffer_load_dwordx4 v112, s[80:83], s39 offen lds\n"
    "v_add_u32 v116, 32768, v114\n"
    "ds_read_b64_tr_b16 a[176:177], v116 offset:0\n"
    "ds_read_b64_tr_b16 a[178:179], v116 offset:2048\n"
    "ds_read_b64_tr_b16 a[180:181], v116 offset:8192\n"
    "ds_read_b64_tr_b16 a[182:183], v116 offset:10240\n"
    "ds_read_b64_tr_b16 a[184:185], v116 offset:32\n"
    "ds_read_b64_tr_b16 a[186:187], v116 offset:2080\n"
    "ds_read_b64_tr_b16 a[188:189], v116 offset:8224\n"
    "ds_read_b64_tr_b16 a[190:191], v116 offset:10272\n"
    "s_waitcnt lgkmcnt(0)\n"
    "s_memrealtime s[42:43]\n"
    "s_waitcnt lgkmcnt(0)\n"
    "s_sub_u32 s42, s42, s51\n"
    "s_cmp_lt_i32 s42, 0\n"
    "s_cbranch_scc1 LCJ16_%=\n"
    "s_getpc_b64 s[52:53]\n"
    "s_branch LCA%=\n"
    "LCJ16_%=:\n"
    "s_waitcnt vmcnt(20)\n"
    "s_barrier\n"
    "s_lshl_b32 s38, s62, 1\n"
    "s_add_u32 m0, s38, 32768\n"
    "s_mov_b32 s39, 3670016\n"
    "buffer_load_dwordx4 v112, s[80:83], s39 offen lds\n"
    "s_add_u32 m0, s38, 33792\n"
    "s_mov_b32 s39, 3702784\n"
    "buffer_load_dwordx4 v112, s[80:83], s39 offen lds\n"
    "s_add_u32 m0, s38, 34816\n"
    "s_mov_b32 s39, 3735552\n"
    "buffer_load_dwordx4 v112, s[80:83], s39 offen lds\n"
    "s_add_u32 m0, s38, 35840\n"
    "s_mov_b32 s39, 3768320\n"
    "buffer_load_dwordx4 v112, s[80:83], s39 offen lds\n"
    "v_add_u32 v116, 49152, v114\n"
    "ds_read_b64_tr_b16 a[192:193], v116 offset:0\n"
    "ds_read_b64_tr_b16 a[194:195], v116 offset:2048\n"
    "ds_read_b64_tr_b16 a[196:197], v116 offset:8192\n"
    "ds_read_b64_tr_b16 a[198:199], v116 offset:10240\n"
    "ds_read_b64_tr_b16 a[200:201], v116 offset:32\n"
    "ds_read_b64_tr_b16 a[202:203], v116 offset:2080\n"
    "ds_read_b64_tr_b16 a[204:205], v116 offset:8224\n"
    "ds_read_b64_tr_b16 a[206:207], v116 offset:10272\n"
    "s_waitcnt lgkmcnt(0)\n"
    "s_memrealtime s[42:43]\n"
    "s_waitcnt lgkmcnt(0)\n"
    "s_sub_u32 s42, s42, s51\n"
    "s_cmp_lt_i32 s42, 0\n"
    "s_cbranch_scc1 LCJ17_%=\n"
    "s_getpc_b64 s[52:53]\n"
    "s_branch LCA%=\n"
    "LCJ17_%=:\n"
    "s_waitcnt vmcnt(20)\n"
    "s_barrier\n"
    "s_mov_b32 s80, s90\n"
    "s_mov_b32 s81, s91\n"
    "s_lshl_b32 s38, s62, 1\n"
    "s_add_u32 m0, s38, 49152\n"
    "s_mov_b32 s39, 0\n"
    "buffer_load_dwordx4 v113, s[80:83], s39 offen lds\n"
    "s_add_u32 m0, s38, 50176\n"
    "s_mov_b32 s39, 1024\n"
    "buffer_load_dwordx4 v113, s[80:83], s39 offen lds\n"
    "s_add_u32 m0, s38, 51200\n"
    "s_mov_b32 s39, 4096\n"
    "buffer_load_dwordx4 v113, s[80:83], s39 offen lds\n"
    "s_add_u32 m0, s38, 52224\n"
    "s_mov_b32 s39, 5120\n"
    "buffer_load_dwordx4 v113, s[80:83], s39 offen lds\n"
    "v_add_u32 v116, 65536, v114\n"
    "ds_read_b64_tr_b16 a[208:209], v116 offset:0\n"
    "ds_read_b64_tr_b16 a[210:211], v116 offset:2048\n"
    "ds_read_b64_tr_b16 a[212:213], v116 offset:8192\n"
    "ds_read_b64_tr_b16 a[214:215], v116 offset:10240\n"
    "ds_read_b64_tr_b16 a[216:217], v116 offset:32\n"
    "ds_read_b64_tr_b16 a[218:219], v116 offset:2080\n"
    "ds_read_b64_tr_b16 a[220:221], v116 offset:8224\n"
    "ds_read_b64_tr_b16 a[222:223], v116 offset:10272\n"
    "s_waitcnt lgkmcnt(0)\n"
    "s_memrealtime s[42:43]\n"
    "s_waitcnt lgkmcnt(0)\n"
    "s_sub_u32 s42, s42, s51\n"
    "s_cmp_lt_i32 s42, 0\n"
    "s_cbranch_scc1 LCJ18_%=\n"
    "s_getpc_b64 s[52:53]\n"
    "s_branch LCA%=\n"
    "LCJ18_%=:\n"
    "s_waitcnt vmcnt(20)\n"
    "s_barrier\n"
    "s_lshl_b32 s38, s62, 1\n"
    "s_add_u32 m0, s38, 65536\n"
    "s_mov_b32 s39, 2048\n"
    "buffer_load_dwordx4 v113, s[80:83], s39 offen lds\n"
    "s_add_u32 m0, s38, 66560\n"
    "s_mov_b32 s39, 3072\n"
    "buffer_load_dwordx4 v113, s[80:83], s39 offen lds\n"
    "s_add_u32 m0, s38, 67584\n"
    "s_mov_b32 s39, 6144\n"
    "buffer_load_dwordx4 v113, s[80:83], s39 offen lds\n"
    "s_add_u32 m0, s38, 68608\n"
    "s_mov_b32 s39, 7168\n"
    "buffer_load_dwordx4 v113, s[80:83], s39 offen lds\n"
    "v_add_u32 v116, 81920, v114\n"
    "ds_read_b64_tr_b16 a[224:225], v116 offset:0\n"
    "ds_read_b64_tr_b16 a[226:227], v116 offset:2048\n"
    "ds_read_b64_tr_b16 a[228:229], v116 offset:8192\n"
    "ds_read_b64_tr_b16 a[230:231], v116 offset:10240\n"
    "ds_read_b64_tr_b16 a[232:233], v116 offset:32\n"
    "ds_read_b64_tr_b16 a[234:235], v116 offset:2080\n"
    "ds_read_b64_tr_b16 a[236:237], v116 offset:8224\n"
    "ds_read_b64_tr_b16 a[238:239], v116 offset:10272\n"
    "s_waitcnt lgkmcnt(0)\n"
    "s_memrealtime s[42:43]\n"
    "s_waitcnt lgkmcnt(0)\n"
    "s_sub_u32 s42, s42, s51\n"
    "s_cmp_lt_i32 s42, 0\n"
    "s_cbranch_scc1 LCJ19_%=\n"
    "s_getpc_b64 s[52:53]\n"
    "s_branch LCA%=\n"
    "LCJ19_%=:\n"
    "s_waitcnt vmcnt(20)\n"
    "s_barrier\n"
    "s_lshl_b32 s38, s62, 1\n"
    "s_add_u32 m0, s38, 81920\n"
    "s_mov_b32 s39, 32768\n"
    "buffer_load_dwordx4 v113, s[80:83], s39 offen lds\n"
    "s_add_u32 m0, s38, 82944\n"
    "s_mov_b32 s39, 33792\n"
    "buffer_load_dwordx4 v113, s[80:83], s39 offen lds\n"
    "s_add_u32 m0, s38, 83968\n"
    "s_mov_b32 s39, 36864\n"
    "buffer_load_dwordx4 v113, s[80:83], s39 offen lds\n"
    "s_add_u32 m0, s38, 84992\n"
    "s_mov_b32 s39, 37888\n"
    "buffer_load_dwordx4 v113, s[80:83], s39 offen lds\n"
    "v_add_u32 v116, 98304, v114\n"
    "ds_read_b64_tr_b16 a[240:241], v116 offset:0\n"
    "ds_read_b64_tr_b16 a[242:243], v116 offset:2048\n"
    "ds_read_b64_tr_b16 a[244:245], v116 offset:8192\n"
    "ds_read_b64_tr_b16 a[246:247], v116 offset:10240\n"
    "ds_read_b64_tr_b16 a[248:249], v116 offset:32\n"
    "ds_read_b64_tr_b16 a[250:251], v116 offset:2080\n"
    "ds_read_b64_tr_b16 a[252:253], v116 offset:8224\n"
    "ds_read_b64_tr_b16 a[254:255], v116 offset:10272\n"
    "s_waitcnt lgkmcnt(0)\n"
    "s_memrealtime s[42:43]\n"
    "s_waitcnt lgkmcnt(0)\n"
    "s_sub_u32 s42, s42, s51\n"
    "s_cmp_lt_i32 s42, 0\n"
    "s_cbranch_scc1 LCJ20_%=\n"
    "s_getpc_b64 s[52:53]\n"
    "s_branch LCA%=\n"
    "LCJ20_%=:\n"
    "s_waitcnt vmcnt(20)\n"
    "s_barrier\n"
    "s_lshl_b32 s38, s62, 1\n"
    "s_add_u32 m0, s38, 98304\n"
    "s_mov_b32 s39, 34816\n"
    "buffer_load_dwordx4 v113, s[80:83], s39 offen lds\n"
    "s_add_u32 m0, s38, 99328\n"
    "s_mov_b32 s39, 35840\n"
    "buffer_load_dwordx4 v113, s[80:83], s39 offen lds\n"
    "s_add_u32 m0, s38, 100352\n"
    "s_mov_b32 s39, 38912\n"
    "buffer_load_dwordx4 v113, s[80:83], s39 offen lds\n"
    "s_add_u32 m0, s38, 101376\n"
    "s_mov_b32 s39, 39936\n"
    "buffer_load_dwordx4 v113, s[80:83], s39 offen lds\n"
    "v_add_u32 v116, 0, v114\n"
    "ds_read_b64_tr_b16 v[120:121], v116 offset:0\n"
    "ds_read_b64_tr_b16 v[122:123], v116 offset:2048\n"
    "ds_read_b64_tr_b16 v[124:125], v116 offset:8192\n"
    "ds_read_b64_tr_b16 v[126:127], v116 offset:10240\n"
    "ds_read_b64_tr_b16 v[128:129], v116 offset:32\n"
    "ds_read_b64_tr_b16 v[130:131], v116 offset:2080\n"
    "ds_read_b64_tr_b16 v[132:133], v116 offset:8224\n"
    "ds_read_b64_tr_b16 v[134:135], v116 offset:10272\n"
    "s_waitcnt lgkmcnt(0)\n"
    "s_memrealtime s[42:43]\n"
    "s_waitcnt lgkmcnt(0)\n"
    "s_sub_u32 s42, s42, s51\n"
    "s_cmp_lt_i32 s42, 0\n"
    "s_cbranch_scc1 LCJ21_%=\n"
    "s_getpc_b64 s[52:53]\n"
    "s_branch LCA%=\n"
    "LCJ21_%=:\n"
    "s_waitcnt vmcnt(20)\n"
    "s_barrier\n"
    "s_lshl_b32 s38, s62, 1\n"
    "s_add_u32 m0, s38, 0\n"
    "s_mov_b32 s39, 65536\n"
    "buffer_load_dwordx4 v113, s[80:83], s39 offen lds\n"
    "s_add_u32 m0, s38, 1024\n"
    "s_mov_b32 s39, 66560\n"
    "buffer_load_dwordx4 v113, s[80:83], s39 offen lds\n"
    "s_add_u32 m0, s38, 2048\n"
    "s_mov_b32 s39, 69632\n"
    "buffer_load_dwordx4 v113, s[80:83], s39 offen lds\n"
    "s_add_u32 m0, s38, 3072\n"
    "s_mov_b32 s39, 70656\n"
    "buffer_load_dwordx4 v113, s[80:83], s39 offen lds\n"
    "v_add_u32 v116, 16384, v114\n"
    "ds_read_b64_tr_b16 v[136:137], v116 offset:0\n"
    "ds_read_b64_tr_b16 v[138:139], v116 offset:2048\n"
    "ds_read_b64_tr_b16 v[140:141], v116 offset:8192\n"
    "ds_read_b64_tr_b16 v[142:143], v116 offset:10240\n"
    "ds_read_b64_tr_b16 v[144:145], v116 offset:32\n"
    "ds_read_b64_tr_b16 v[146:147], v116 offset:2080\n"
    "ds_read_b64_tr_b16 v[148:149], v116 offset:8224\n"
    "ds_read_b64_tr_b16 v[150:151], v116 offset:10272\n"
    "s_waitcnt lgkmcnt(0)\n"
    "s_memrealtime s[42:43]\n"
    "s_waitcnt lgkmcnt(0)\n"
    "s_sub_u32 s42, s42, s51\n"
    "s_cmp_lt_i32 s42, 0\n"
    "s_cbranch_scc1 LCJ22_%=\n"
    "s_getpc_b64 s[52:53]\n"
    "s_branch LCA%=\n"
    "LCJ22_%=:\n"
    "s_waitcnt vmcnt(20)\n"
    "s_barrier\n"
    "s_lshl_b32 s38, s62, 1\n"
    "s_add_u32 m0, s38, 16384\n"
    "s_mov_b32 s39, 67584\n"
    "buffer_load_dwordx4 v113, s[80:83], s39 offen lds\n"
    "s_add_u32 m0, s38, 17408\n"
    "s_mov_b32 s39, 68608\n"
    "buffer_load_dwordx4 v113, s[80:83], s39 offen lds\n"
    "s_add_u32 m0, s38, 18432\n"
    "s_mov_b32 s39, 71680\n"
    "buffer_load_dwordx4 v113, s[80:83], s39 offen lds\n"
    "s_add_u32 m0, s38, 19456\n"
    "s_mov_b32 s39, 72704\n"
    "buffer_load_dwordx4 v113, s[80:83], s39 offen lds\n"
    "v_add_u32 v116, 32768, v114\n"
    "ds_read_b64_tr_b16 v[152:153], v116 offset:0\n"
    "ds_read_b64_tr_b16 v[154:155], v116 offset:2048\n"
    "ds_read_b64_tr_b16 v[156:157], v116 offset:8192\n"
    "ds_read_b64_tr_b16 v[158:159], v116 offset:10240\n"
    "ds_read_b64_tr_b16 v[160:161], v116 offset:32\n"
    "ds_read_b64_tr_b16 v[162:163], v116 offset:2080\n"
    "ds_read_b64_tr_b16 v[164:165], v116 offset:8224\n"
    "ds_read_b64_tr_b16 v[166:167], v116 offset:10272\n"
    "s_waitcnt lgkmcnt(0)\n"
    "s_memrealtime s[42:43]\n"
    "s_waitcnt lgkmcnt(0)\n"
    "s_sub_u32 s42, s42, s51\n"
    "s_cmp_lt_i32 s42, 0\n"
    "s_cbranch_scc1 LCJ23_%=\n"
    "s_getpc_b64 s[52:53]\n"
    "s_branch LCA%=\n"
    "LCJ23_%=:\n"
    "s_waitcnt vmcnt(20)\n"
    "s_barrier\n"
    "s_lshl_b32 s38, s62, 1\n"
    "s_add_u32 m0, s38, 32768\n"
    "s_mov_b32 s39, 98304\n"
    "buffer_load_dwordx4 v113, s[80:83], s39 offen lds\n"
    "s_add_u32 m0, s38, 33792\n"
    "s_mov_b32 s39, 99328\n"
    "buffer_load_dwordx4 v113, s[80:83], s39 offen lds\n"
    "s_add_u32 m0, s38, 34816\n"
    "s_mov_b32 s39, 102400\n"
    "buffer_load_dwordx4 v113, s[80:83], s39 offen lds\n"
    "s_add_u32 m0, s38, 35840\n"
    "s_mov_b32 s39, 103424\n"
    "buffer_load_dwordx4 v113, s[80:83], s39 offen lds\n"
    "v_add_u32 v116, 49152, v115\n"
    "ds_read_b64_tr_b16 v[168:169], v116 offset:0\n"
    "ds_read_b64_tr_b16 v[184:185], v116 offset:8\n"
    "ds_read_b64_tr_b16 v[200:201], v116 offset:16\n"
    "ds_read_b64_tr_b16 v[216:217], v116 offset:24\n"
    "ds_read_b64_tr_b16 v[232:233], v116 offset:32\n"
    "ds_read_b64_tr_b16 v[248:249], v116 offset:40\n"
    "ds_read_b64_tr_b16 v[80:81], v116 offset:48\n"
    "ds_read_b64_tr_b16 v[96:97], v116 offset:56\n"
    "s_waitcnt lgkmcnt(0)\n"
    "s_memrealtime s[42:43]\n"
    "s_waitcnt lgkmcnt(0)\n"
    "s_sub_u32 s42, s42, s51\n"
    "s_cmp_lt_i32 s42, 0\n"
    "s_cbranch_scc1 LCJ24_%=\n"
    "s_getpc_b64 s[52:53]\n"
    "s_branch LCA%=\n"
    "LCJ24_%=:\n"
    "s_waitcnt vmcnt(20)\n"
    "s_barrier\n"
    "s_lshl_b32 s38, s62, 1\n"
    "s_add_u32 m0, s38, 49152\n"
    "s_mov_b32 s39, 100352\n"
    "buffer_load_dwordx4 v113, s[80:83], s39 offen lds\n"
    "s_add_u32 m0, s38, 50176\n"
    "s_mov_b32 s39, 101376\n"
    "buffer_load_dwordx4 v113, s[80:83], s39 offen lds\n"
    "s_add_u32 m0, s38, 51200\n"
    "s_mov_b32 s39, 104448\n"
    "buffer_load_dwordx4 v113, s[80:83], s39 offen lds\n"
    "s_add_u32 m0, s38, 52224\n"
    "s_mov_b32 s39, 105472\n"
    "buffer_load_dwordx4 v113, s[80:83], s39 offen lds\n"
    "v_add_u32 v116, 65536, v115\n"
    "ds_read_b64_tr_b16 v[170:171], v116 offset:0\n"
    "ds_read_b64_tr_b16 v[186:187], v116 offset:8\n"
    "ds_read_b64_tr_b16 v[202:203], v116 offset:16\n"
    "ds_read_b64_tr_b16 v[218:219], v116 offset:24\n"
    "ds_read_b64_tr_b16 v[234:235], v116 offset:32\n"
    "ds_read_b64_tr_b16 v[250:251], v116 offset:40\n"
    "ds_read_b64_tr_b16 v[82:83], v116 offset:48\n"
    "ds_read_b64_tr_b16 v[98:99], v116 offset:56\n"
    "s_waitcnt lgkmcnt(0)\n"
    "s_memrealtime s[42:43]\n"
    "s_waitcnt lgkmcnt(0)\n"
    "s_sub_u32 s42, s42, s51\n"
    "s_cmp_lt_i32 s42, 0\n"
    "s_cbranch_scc1 LCJ25_%=\n"
    "s_getpc_b64 s[52:53]\n"
    "s_branch LCA%=\n"
    "LCJ25_%=:\n"
    "s_waitcnt vmcnt(20)\n"
    "s_barrier\n"
    "v_add_u32 v116, 81920, v115\n"
    "ds_read_b64_tr_b16 v[172:173], v116 offset:0\n"
    "ds_read_b64_tr_b16 v[188:189], v116 offset:8\n"
    "ds_read_b64_tr_b16 v[204:205], v116 offset:16\n"
    "ds_read_b64_tr_b16 v[220:221], v116 offset:24\n"
    "ds_read_b64_tr_b16 v[236:237], v116 offset:32\n"
    "ds_read_b64_tr_b16 v[252:253], v116 offset:40\n"
    "ds_read_b64_tr_b16 v[84:85], v116 offset:48\n"
    "ds_read_b64_tr_b16 v[100:101], v116 offset:56\n"
    "s_waitcnt lgkmcnt(0)\n"
    "s_memrealtime s[42:43]\n"
    "s_waitcnt lgkmcnt(0)\n"
    "s_sub_u32 s42, s42, s51\n"
    "s_cmp_lt_i32 s42, 0\n"
    "s_cbranch_scc1 LCJ26_%=\n"
    "s_getpc_b64 s[52:53]\n"
    "s_branch LCA%=\n"
    "LCJ26_%=:\n"
    "s_waitcnt vmcnt(16)\n"
    "s_barrier\n"
    "v_add_u32 v116, 98304, v115\n"
    "ds_read_b64_tr_b16 v[174:175], v116 offset:0\n"
    "ds_read_b64_tr_b16 v[190:191], v116 offset:8\n"
    "ds_read_b64_tr_b16 v[206:207], v116 offset:16\n"
    "ds_read_b64_tr_b16 v[222:223], v116 offset:24\n"
    "ds_read_b64_tr_b16 v[238:239], v116 offset:32\n"
    "ds_read_b64_tr_b16 v[254:255], v116 offset:40\n"
    "ds_read_b64_tr_b16 v[86:87], v116 offset:48\n"
    "ds_read_b64_tr_b16 v[102:103], v116 offset:56\n"
    "s_waitcnt lgkmcnt(0)\n"
    "s_memrealtime s[42:43]\n"
    "s_waitcnt lgkmcnt(0)\n"
    "s_sub_u32 s42, s42, s51\n"
    "s_cmp_lt_i32 s42, 0\n"
    "s_cbranch_scc1 LCJ27_%=\n"
    "s_getpc_b64 s[52:53]\n"
    "s_branch LCA%=\n"
    "LCJ27_%=:\n"
    "s_waitcnt vmcnt(12)\n"
    "s_barrier\n"
    "v_add_u32 v116, 0, v115\n"
    "ds_read_b64_tr_b16 v[176:177], v116 offset:0\n"
    "ds_read_b64_tr_b16 v[192:193], v116 offset:8\n"
    "ds_read_b64_tr_b16 v[208:209], v116 offset:16\n"
    "ds_read_b64_tr_b16 v[224:225], v116 offset:24\n"
    "ds_read_b64_tr_b16 v[240:241], v116 offset:32\n"
    "ds_read_b64_tr_b16 v[72:73], v116 offset:40\n"
    "ds_read_b64_tr_b16 v[88:89], v116 offset:48\n"
    "ds_read_b64_tr_b16 v[104:105], v116 offset:56\n"
    "s_waitcnt lgkmcnt(0)\n"
    "s_memrealtime s[42:43]\n"
    "s_waitcnt lgkmcnt(0)\n"
    "s_sub_u32 s42, s42, s51\n"
    "s_cmp_lt_i32 s42, 0\n"
    "s_cbranch_scc1 LCJ28_%=\n"
    "s_getpc_b64 s[52:53]\n"
    "s_branch LCA%=\n"
    "LCJ28_%=:\n"
    "s_waitcnt vmcnt(8)\n"
    "s_barrier\n"
    "v_add_u32 v116, 16384, v115\n"
    "ds_read_b64_tr_b16 v[178:179], v116 offset:0\n"
    "ds_read_b64_tr_b16 v[194:195], v116 offset:8\n"
    "ds_read_b64_tr_b16 v[210:211], v116 offset:16\n"
    "ds_read_b64_tr_b16 v[226:227], v116 offset:24\n"
    "ds_read_b64_tr_b16 v[242:243], v116 offset:32\n"
    "ds_read_b64_tr_b16 v[74:75], v116 offset:40\n"
    "ds_read_b64_tr_b16 v[90:91], v116 offset:48\n"
    "ds_read_b64_tr_b16 v[106:107], v116 offset:56\n"
    "s_waitcnt lgkmcnt(0)\n"
    "s_memrealtime s[42:43]\n"
    "s_waitcnt lgkmcnt(0)\n"
    "s_sub_u32 s42, s42, s51\n"
    "s_cmp_lt_i32 s42, 0\n"
    "s_cbranch_scc1 LCJ29_%=\n"
    "s_getpc_b64 s[52:53]\n"
    "s_branch LCA%=\n"
    "LCJ29_%=:\n"
    "s_waitcnt vmcnt(4)\n"
    "s_barrier\n"
    "v_add_u32 v116, 32768, v115\n"
    "ds_read_b64_tr_b16 v[180:181], v116 offset:0\n"
    "ds_read_b64_tr_b16 v[196:197], v116 offset:8\n"
    "ds_read_b64_tr_b16 v[212:213], v116 offset:16\n"
    "ds_read_b64_tr_b16 v[228:229], v116 offset:24\n"
    "ds_read_b64_tr_b16 v[244:245], v116 offset:32\n"
    "ds_read_b64_tr_b16 v[76:77], v116 offset:40\n"
    "ds_read_b64_tr_b16 v[92:93], v116 offset:48\n"
    "ds_read_b64_tr_b16 v[108:109], v116 offset:56\n"
    "s_waitcnt lgkmcnt(0)\n"
    "s_memrealtime s[42:43]\n"
    "s_waitcnt lgkmcnt(0)\n"
    "s_sub_u32 s42, s42, s51\n"
    "s_cmp_lt_i32 s42, 0\n"
    "s_cbranch_scc1 LCJ30_%=\n"
    "s_getpc_b64 s[52:53]\n"
    "s_branch LCA%=\n"
    "LCJ30_%=:\n"
    "s_waitcnt vmcnt(0)\n"
    "s_barrier\n"
    "v_add_u32 v116, 49152, v115\n"
    "ds_read_b64_tr_b16 v[182:183], v116 offset:0\n"
    "ds_read_b64_tr_b16 v[198:199], v116 offset:8\n"
    "ds_read_b64_tr_b16 v[214:215], v116 offset:16\n"
    "ds_read_b64_tr_b16 v[230:231], v116 offset:24\n"
    "ds_read_b64_tr_b16 v[246:247], v116 offset:32\n"
    "ds_read_b64_tr_b16 v[78:79], v116 offset:40\n"
    "ds_read_b64_tr_b16 v[94:95], v116 offset:48\n"
    "ds_read_b64_tr_b16 v[110:111], v116 offset:56\n"
    "s_waitcnt lgkmcnt(0)\n"
    "s_memrealtime s[42:43]\n"
    "s_waitcnt lgkmcnt(0)\n"
    "s_sub_u32 s42, s42, s51\n"
    "s_cmp_lt_i32 s42, -536870912\n"
    "s_cbranch_scc1 LCJ31_%=\n"
    "s_getpc_b64 s[52:53]\n"
    "s_branch LCA%=\n"
    "LCJ31_%=:\n"
    "s_barrier\n"
    "ds_write_b128 v7, v[16:19] offset:6144\n"
    "ds_write_b128 v7, v[20:23] offset:7168\n"
    "ds_write_b128 v7, v[24:27] offset:8192\n"
    "ds_write_b128 v7, v[28:31] offset:9216\n"
    "ds_write_b128 v7, v[32:35] offset:10240\n"
    "ds_write_b128 v7, v[36:39] offset:11264\n"
    "ds_write_b128 v7, v[40:43] offset:12288\n"
    "ds_write_b128 v7, v[44:47] offset:13312\n"
    "ds_write_b128 v7, v[48:51] offset:14336\n"
    "ds_write_b128 v7, v[52:55] offset:15360\n"
    "ds_write_b128 v7, v[56:59] offset:16384\n"
    "ds_write_b128 v7, v[60:63] offset:17408\n"
    "ds_write_b128 v7, v[64:67] offset:18432\n"
    "ds_write_b128 v7, v[68:71] offset:19456\n"
    "ds_write_b128 v7, v[72:75] offset:20480\n"
    "s_waitcnt lgkmcnt(11)\n"
    "ds_write_b128 v7, v[76:79] offset:21504\n"
    "ds_write_b128 v7, v[80:83] offset:22528\n"
    "ds_write_b128 v7, v[84:87] offset:23552\n"
    "ds_write_b128 v7, v[88:91] offset:24576\n"
    "s_waitcnt lgkmcnt(11)\n"
    "ds_write_b128 v7, v[92:95] offset:25600\n"
    "ds_write_b128 v7, v[96:99] offset:26624\n"
    "ds_write_b128 v7, v[100:103] offset:27648\n"
    "ds_write_b128 v7, v[104:107] offset:28672\n"
    "s_waitcnt lgkmcnt(11)\n"
    "ds_write_b128 v7, v[108:111] offset:29696\n"
    "s_waitcnt lgkmcnt(0)\n"
    "ds_read_b128 v[16:19], v9 offset:0\n"
    "ds_read_b128 v[20:23], v9 offset:1024\n"
    "ds_read_b128 v[24:27], v9 offset:8192\n"
    "ds_read_b128 v[28:31], v9 offset:9216\n"
    "ds_read_b128 v[32:35], v9 offset:16384\n"
    "ds_read_b128 v[36:39], v9 offset:17408\n"
    "s_waitcnt lgkmcnt(5)\n"
    "ds_write_b128 v7, v[16:19] offset:0\n"
    "s_waitcnt lgkmcnt(5)\n"
    "ds_write_b128 v7, v[20:23] offset:1024\n"
    "s_waitcnt lgkmcnt(5)\n"
    "ds_write_b128 v7, v[24:27] offset:2048\n"
    "s_waitcnt lgkmcnt(5)\n"
    "ds_write_b128 v7, v[28:31] offset:3072\n"
    "s_waitcnt lgkmcnt(5)\n"
    "ds_write_b128 v7, v[32:35] offset:4096\n"
    "s_waitcnt lgkmcnt(5)\n"
    "ds_write_b128 v7, v[36:39] offset:5120\n"
    "v_lshrrev_b32 v64, 4, v0\n"
    "v_lshrrev_b32 v65, 4, v64\n"
    "v_and_b32 v64, 15, v64\n"
    "v_lshlrev_b32 v65, 2, v65\n"
    "v_sub_u32 v64, v64, v65\n"
    "s_and_b32 s38, s61, 1\n"
    "s_lshl_b32 s38, s38, 4\n"
    "v_add_u32 v64, s38, v64\n"
    "v_cmp_le_i32 s[80:81], 0, v64\n"
    "v_cmp_le_i32 s[82:83], 1, v64\n"
    "v_cmp_le_i32 s[84:85], 2, v64\n"
    "v_cmp_le_i32 s[86:87], 3, v64\n"
    "v_cmp_le_i32 s[88:89], 16, v64\n"
    "v_cmp_le_i32 s[90:91], 17, v64\n"
    "v_cmp_le_i32 s[92:93], 18, v64\n"
    "v_cmp_le_i32 s[94:95], 19, v64\n"
    "v_mov_b32 v64, 158224\n"
    "v_mov_b32 v65, 1\n"
    "ds_write_b32 v64, v65\n"
    "v_mov_b64 v[66:67], 0\n"
    "ds_write_b64 v64, v[66:67] offset:16\n"
    "s_mov_b32 s54, 0x80000000\n"
    "s_mov_b64 exec, 0xff\n"
    "buffer_load_dword v116, v0, s[16:19], s59 offen sc1\n"
    "s_mov_b64 exec, -1\n"
    "s_waitcnt vmcnt(0)\n"
    "v_cmp_ne_u32 vcc, 0, v116\n"
    "s_nop 1\n"
    "s_andn2_b32 s38, 7, vcc_lo\n"
    "s_cbranch_scc0 LS0K%=\n"
    "s_mov_b32 s37, 0x80000\n"
    "LS0W%=:\n"
    "s_mov_b64 exec, 0xff\n"
    "buffer_load_dword v116, v0, s[16:19], s59 offen sc1\n"
    "s_mov_b64 exec, -1\n"
    "s_waitcnt vmcnt(0)\n"
    "v_cmp_ne_u32 vcc, 0, v116\n"
    "s_nop 1\n"
    "s_andn2_b32 s38, 7, vcc_lo\n"
    "s_cbranch_scc0 LS0K%=\n"
    "s_sleep 2\n"
    "s_sub_u32 s37, s37, 1\n"
    "s_cbranch_scc0 LS0W%=\n"
    "s_branch LFAIL%=\n"
    "LS0K%=:\n"
    "s_add_u32 s53, s50, s55\n"
    "s_add_u32 s49, s48, s62\n"
    "s_add_u32 s59, s59, 128\n"
    "s_add_u32 s51, s53, 0\n"
    "s_lshr_b32 s38, s51, 8\n"
    "s_xor_b32 s38, s38, s56\n"
    "s_and_b32 s38, s38, 0x2000\n"
    "s_or_b32 s51, s51, s38\n"
    "s_add_u32 s52, s53, 524288\n"
    "s_lshr_b32 s38, s52, 8\n"
    "s_xor_b32 s38, s38, s56\n"
    "s_and_b32 s38, s38, 0x2000\n"
    "s_or_b32 s52, s52, s38\n"
    "s_add_u32 s53, s53, 1048576\n"
    "s_lshr_b32 s38, s53, 8\n"
    "s_xor_b32 s38, s38, s56\n"
    "s_and_b32 s38, s38, 0x2000\n"
    "s_or_b32 s53, s53, s38\n"
    "s_waitcnt lgkmcnt(0)\n"
    "s_barrier\n"
    "s_cmp_lg_u32 s61, 1\n"
    "s_cbranch_scc1 LBM%=\n"
    "s_sub_u32 s51, s51, 2048\n"
    "s_mov_b32 m0, 122880\n"
    "s_nop 0\n"
    "buffer_load_dwordx4 v0, s[16:19], s51 offen offset:0 sc1 lds\n"
    "buffer_load_dwordx4 v0, s[16:19], s51 offen offset:1024 sc1 lds\n"
    "buffer_load_dwordx4 v0, s[16:19], s51 offen offset:2048 sc1 lds\n"
    "buffer_load_dwordx4 v0, s[16:19], s51 offen offset:3072 sc1 lds\n"
    "s_mov_b32 m0, 126976\n"
    "s_add_u32 s51, s51, 4096\n"
    "buffer_load_dwordx4 v0, s[16:19], s51 offen offset:0 sc1 lds\n"
    "buffer_load_dwordx4 v0, s[16:19], s51 offen offset:1024 sc1 lds\n"
    "buffer_load_dwordx4 v0, s[16:19], s51 offen offset:2048 sc1 lds\n"
    "buffer_load_dwordx4 v0, s[16:19], s51 offen offset:3072 sc1 lds\n"
    "LBM%=:\n"
    "s_waitcnt lgkmcnt(0)\n"
    "s_barrier\n"
    "s_add_u32 m0, s62, 147456\n"
    "s_nop 0\n"
    "buffer_load_dwordx4 v0, s[16:19], s49 offen offset:0 sc1 lds\n"
    "buffer_load_dwordx4 v0, s[16:19], s49 offen offset:1024 sc1 lds\n"
    "v_mov_b32 v112, 158224\n"
    "ds_read_b128 v[16:19], v9 offset:16384\n"
    "ds_read_b128 v[20:23], v9 offset:17408\n"
    "ds_read_b32 v112, v112 offset:0\n"
    "s_nop 3\n"
    "s_add_u32 m0, s62, 131072\n"
    "s_nop 0\n"
    "buffer_load_dwordx4 v0, s[16:19], s52 offen offset:0 sc1 lds\n"
    "buffer_load_dwordx4 v0, s[16:19], s52 offen offset:1024 sc1 lds\n"
    "v_lshrrev_b32 v113, 4, v0\n"
    "v_and_b32 v113, 0x30, v113\n"
    "v_lshl_add_u32 v113, v113, 4, v0\n"
    "s_waitcnt lgkmcnt(0)\n"
    "v_readfirstlane_b32 s39, v112\n"
    "ds_read_b128 v[64:67], v7 offset:0\n"
    "ds_read_b128 v[68:71], v7 offset:1024\n"
    "ds_read_b128 v[72:75], v7 offset:2048\n"
    "ds_read_b128 v[76:79], v7 offset:3072\n"
    "ds_read_b128 v[80:83], v7 offset:4096\n"
    "ds_read_b128 v[84:87], v7 offset:5120\n"
    "ds_read_b128 v[88:91], v7 offset:6144\n"
    "ds_read_b128 v[92:95], v7 offset:7168\n"
    "ds_read_b128 v[96:99], v7 offset:8192\n"
    "ds_read_b128 v[100:103], v7 offset:9216\n"
    "ds_read_b128 v[104:107], v7 offset:10240\n"
    "ds_read_b128 v[108:111], v7 offset:11264\n"
    "v_mov_b64 v[114:115], 0\n"
    "s_nop 1\n"
    "v_mfma_f32_32x32x8_bf16 v[48:63], v[114:115], v[114:115], 0\n"
    "buffer_store_dwordx4 v[16:19], v113, s[16:19], s54 offen offset:0 sc1\n"
    "buffer_store_dwordx4 v[20:23], v113, s[16:19], s54 offen offset:2048 sc1\n"
    "s_cmp_lg_u32 s39, 0\n"
    "s_cbranch_scc1 LRS%=\n"
    "s_waitcnt vmcnt(4)\n"
    "s_waitcnt lgkmcnt(0)\n"
    "LC%=:\n"
    "s_barrier\n"
    "ds_read_b128 v[16:19], v2 offset:0\n"
    "ds_read_b128 v[20:23], v2 offset:1024\n"
    "ds_read_b128 v[24:27], v2 offset:2048\n"
    "s_add_u32 m0, s62, 139264\n"
    "s_nop 0\n"
    "buffer_load_dwordx4 v0, s[16:19], s53 offen offset:0 sc1 lds\n"
    "buffer_load_dwordx4 v0, s[16:19], s53 offen offset:1024 sc1 lds\n"
    "s_waitcnt lgkmcnt(2)\n"
    "v_smfmac_f32_16x16x64_bf16 v[48:51], v[16:19], v[64:71], v1\n"
    "ds_read_b128 v[28:31], v2 offset:3072\n"
    "v_smfmac_f32_16x16x64_bf16 v[52:55], v[16:19], v[72:79], v1\n"
    "ds_read_b128 v[32:35], v2 offset:4096\n"
    "s_waitcnt lgkmcnt(3)\n"
    "v_smfmac_f32_16x16x64_bf16 v[48:51], v[20:23], v[80:87], v1\n"
    "ds_read_b128 v[36:39], v2 offset:5120\n"
    "v_smfmac_f32_16x16x64_bf16 v[52:55], v[20:23], v[88:95], v1\n"
    "ds_read_b128 v[40:43], v2 offset:6144\n"
    "s_waitcnt lgkmcnt(4)\n"
    "v_smfmac_f32_16x16x64_bf16 v[48:51], v[24:27], v[96:103], v1\n"
    "ds_read_b128 v[44:47], v2 offset:7168\n"
    "v_smfmac_f32_16x16x64_bf16 v[52:55], v[24:27], v[104:111], v1\n"
    "ds_read_b128 v[64:67], v7 offset:12288\n"
    "s_waitcnt lgkmcnt(5)\n"
    "v_smfmac_f32_16x16x64_bf16 v[48:51], v[28:31], a[0:7], v1\n"
    "ds_read_b128 v[68:71], v7 offset:13312\n"
    "v_max3_u32 v112, v19, v23, v27\n"
    "v_smfmac_f32_16x16x64_bf16 v[52:55], v[28:31], a[8:15], v1\n"
    "ds_read_b128 v[72:75], v7 offset:14336\n"
    "s_waitcnt lgkmcnt(6)\n"
    "v_smfmac_f32_16x16x64_bf16 v[48:51], v[32:35], a[16:23], v1\n"
    "ds_read_b128 v[76:79], v7 offset:15360\n"
    "v_smfmac_f32_16x16x64_bf16 v[52:55], v[32:35], a[24:31], v1\n"
    "ds_read_b128 v[80:83], v7 offset:16384\n"
    "s_waitcnt lgkmcnt(7)\n"
    "v_smfmac_f32_16x16x64_bf16 v[48:51], v[36:39], a[32:39], v1\n"
    "ds_read_b128 v[84:87], v7 offset:17408\n"
    "v_max3_u32 v112, v112, v31, v35\n"
    "v_smfmac_f32_16x16x64_bf16 v[52:55], v[36:39], a[40:47], v1\n"
    "ds_read_b128 v[88:91], v7 offset:18432\n"
    "s_waitcnt lgkmcnt(8)\n"
    "v_smfmac_f32_16x16x64_bf16 v[48:51], v[40:43], a[48:55], v1\n"
    "ds_read_b128 v[92:95], v7 offset:19456\n"
    "v_max3_u32 v112, v112, v39, v43\n"
    "v_smfmac_f32_16x16x64_bf16 v[52:55], v[40:43], a[56:63], v1\n"
    "s_waitcnt lgkmcnt(8)\n"
    "v_smfmac_f32_16x16x64_bf16 v[48:51], v[44:47], a[64:71], v1\n"
    "v_max_u32 v112, v112, v47\n"
    "v_smfmac_f32_16x16x64_bf16 v[52:55], v[44:47], a[72:79], v1\n"
    "s_waitcnt lgkmcnt(6)\n"
    "v_smfmac_f32_16x16x64_bf16 v[56:59], v[16:19], v[64:71], v1\n"
    "s_waitcnt lgkmcnt(4)\n"
    "v_cmp_eq_u32 vcc, -1, v112\n"
    "v_smfmac_f32_16x16x64_bf16 v[60:63], v[16:19], v[72:79], v1\n"
    "s_waitcnt lgkmcnt(2)\n"
    "v_add_f32 v48, v48, v50\n"
    "v_add_f32 v52, v52, v54\n"
    "v_smfmac_f32_16x16x64_bf16 v[56:59], v[20:23], v[80:87], v1\n"
    "s_waitcnt lgkmcnt(0)\n"
    "v_add_f32 v49, v49, v51\n"
    "v_add_f32 v53, v53, v55\n"
    "v_smfmac_f32_16x16x64_bf16 v[60:63], v[20:23], v[88:95], v1\n"
    "s_cbranch_vccnz LMR%=\n"
    "v_cvt_pk_bf16_f32 v48, v48, v52\n"
    "v_smfmac_f32_16x16x64_bf16 v[56:59], v[24:27], a[80:87], v1\n"
    "v_cvt_pk_bf16_f32 v49, v49, v53\n"
    "v_smfmac_f32_16x16x64_bf16 v[60:63], v[24:27], a[88:95], v1\n"
    "ds_write2_b32 v3, v48, v49 offset0:0 offset1:72\n"
    "v_add_u32 v117, s20, v5\n"
    "v_smfmac_f32_16x16x64_bf16 v[56:59], v[28:31], a[96:103], v1\n"
    "v_mov_b64 v[50:51], 0\n"
    "v_smfmac_f32_16x16x64_bf16 v[60:63], v[28:31], a[104:111], v1\n"
    "v_mov_b64 v[52:53], 0\n"
    "v_smfmac_f32_16x16x64_bf16 v[56:59], v[32:35], a[112:119], v1\n"
    "v_mov_b64 v[54:55], 0\n"
    "v_smfmac_f32_16x16x64_bf16 v[60:63], v[32:35], a[120:127], v1\n"
    "s_waitcnt lgkmcnt(0)\n"
    "s_barrier\n"
    "v_smfmac_f32_16x16x64_bf16 v[56:59], v[36:39], a[128:135], v1\n"
    "ds_read_b128 v[80:83], v117 offset:0\n"
    "ds_read_b128 v[84:87], v117 offset:64\n"
    "v_smfmac_f32_16x16x64_bf16 v[60:63], v[36:39], a[136:143], v1\n"
    "ds_read_b128 v[88:91], v9 offset:0\n"
    "ds_read_b128 v[92:95], v9 offset:1024\n"
    "v_smfmac_f32_16x16x64_bf16 v[56:59], v[40:43], a[144:151], v1\n"
    "ds_read_b128 v[96:99], v5 offset:0\n"
    "v_mov_b64 v[48:49], 0\n"
    "v_smfmac_f32_16x16x64_bf16 v[56:59], v[44:47], a[160:167], v1\n"
    "ds_read_b128 v[100:103], v5 offset:64\n"
    "v_smfmac_f32_16x16x64_bf16 v[60:63], v[40:43], a[152:159], v1\n"
    "ds_read_b128 v[104:107], v5 offset:128\n"
    "v_smfmac_f32_16x16x64_bf16 v[60:63], v[44:47], a[168:175], v1\n"
    "ds_read_b128 v[108:111], v5 offset:192\n"
    "s_waitcnt lgkmcnt(5)\n"
    "v_add_f32 v56, v56, v58\n"
    "v_mfma_f32_16x16x32_bf16 v[64:67], v[88:91], v[80:83], 0\n"
    "s_waitcnt lgkmcnt(4)\n"
    "v_add_f32 v57, v57, v59\n"
    "v_mfma_f32_16x16x32_bf16 v[64:67], v[92:95], v[84:87], v[64:67]\n"
    "v_add_f32 v60, v60, v62\n"
    "v_add_f32 v61, v61, v63\n"
    "v_cvt_pk_bf16_f32 v56, v56, v60\n"
    "v_cvt_pk_bf16_f32 v57, v57, v61\n"
    "v_smfmac_f32_16x16x64_bf16 v[48:51], v[16:19], a[176:183], v1\n"
    "ds_write2_b32 v4, v56, v57 offset0:0 offset1:72\n"
    "s_memtime s[40:41]\n"
    "v_smfmac_f32_16x16x64_bf16 v[52:55], v[16:19], a[184:191], v1\n"
    "ds_write_b128 v8, v[64:67]\n"
    "v_smfmac_f32_16x16x64_bf16 v[48:51], v[20:23], a[192:199], v1\n"
    "v_mov_b64 v[60:61], s[14:15]\n"
    "v_smfmac_f32_16x16x64_bf16 v[52:55], v[20:23], a[200:207], v1\n"
    "v_mov_b64 v[62:63], s[14:15]\n"
    "v_smfmac_f32_16x16x64_bf16 v[48:51], v[24:27], a[208:215], v1\n"
    "v_mov_b32 v118, s72\n"
    "v_smfmac_f32_16x16x64_bf16 v[52:55], v[24:27], a[216:223], v1\n"
    "s_waitcnt lgkmcnt(0)\n"
    "s_barrier\n"
    "ds_read_b128 v[64:67], v6 offset:0\n"
    "ds_read_b128 v[88:91], v15 offset:0\n"
    "v_smfmac_f32_16x16x64_bf16 v[48:51], v[28:31], a[224:231], v1\n"
    "ds_read_b128 v[92:95], v15 offset:512\n"
    "ds_read_b128 v[68:71], v6 offset:64\n"
    "v_smfmac_f32_16x16x64_bf16 v[52:55], v[28:31], a[232:239], v1\n"
    "ds_read_b128 v[72:75], v6 offset:128\n"
    "s_sub_u32 s40, s40, s44\n"
    "s_cmp_gt_u32 s40, s10\n"
    "v_smfmac_f32_16x16x64_bf16 v[48:51], v[32:35], a[240:247], v1\n"
    "ds_read_b128 v[76:79], v6 offset:192\n"
    "s_cselect_b32 s40, 0x10000, 0\n"
    "s_or_b32 s40, s40, s64\n"
    "s_waitcnt lgkmcnt(5)\n"
    "v_mov_b32 v119, s40\n"
    "v_mfma_f32_16x16x32_bf16 v[112:115], v[64:67], v[96:99], 0\n"
    "ds_write_b32 v118, v119\n"
    "s_waitcnt lgkmcnt(4)\n"
    "s_waitcnt lgkmcnt(3)\n"
    "v_add_f32 v88, v88, v92\n"
    "v_add_f32 v89, v89, v93\n"
    "v_mfma_f32_16x16x32_bf16 v[112:115], v[68:71], v[100:103], v[112:115]\n"
    "s_waitcnt lgkmcnt(2)\n"
    "v_add_f32 v90, v90, v94\n"
    "v_add_f32 v91, v91, v95\n"
    "v_mfma_f32_16x16x32_bf16 v[112:115], v[72:75], v[104:107], v[112:115]\n"
    "s_waitcnt lgkmcnt(1)\n"
    "v_max3_f32 v117, v88, v89, v90\n"
    "v_max_f32 v117, v117, v91\n"
    "v_mfma_f32_16x16x32_bf16 v[112:115], v[76:79], v[108:111], v[112:115]\n"
    "s_nop 0\n"
    "v_max_f32_dpp v116, v117, v117 row_ror:8 row_mask:0xf bank_mask:0xf\n"
    "v_mov_b32 v117, v116\n"
    "v_smfmac_f32_16x16x64_bf16 v[52:55], v[32:35], a[248:255], v1\n"
    "s_nop 0\n"
    "v_permlane32_swap_b32 v116, v117\n"
    "v_smfmac_f32_16x16x64_bf16 v[48:51], v[36:39], v[120:127], v1\n"
    "v_max_f32 v116, v116, v117\n"
    "v_smfmac_f32_16x16x64_bf16 v[48:51], v[40:43], v[136:143], v1\n"
    "v_max3_f32 v117, v112, v113, v116\n"
    "v_smfmac_f32_16x16x64_bf16 v[48:51], v[44:47], v[152:159], v1\n"
    "v_max3_f32 v117, v114, v115, v117\n"
    "v_smfmac_f32_16x16x64_bf16 v[52:55], v[36:39], v[128:135], v1\n"
    "v_mov_b32 v118, v117\n"
    "v_smfmac_f32_16x16x64_bf16 v[52:55], v[40:43], v[144:151], v1\n"
    "s_waitcnt vmcnt(4)\n"
    "ds_read_b128 v[80:83], v9 offset:8192\n"
    "v_permlane16_swap_b32 v117, v118\n"
    "v_smfmac_f32_16x16x64_bf16 v[52:55], v[44:47], v[160:167], v1\n"
    "ds_read_b128 v[84:87], v9 offset:9216\n"
    "v_max_f32 v117, v117, v118\n"
    "v_mul_f32 v117, 0xbe0293ee, v117\n"
    "v_fma_f32 v88, v88, s65, v117\n"
    "v_fma_f32 v89, v89, s65, v117\n"
    "v_fma_f32 v90, v90, s65, v117\n"
    "v_fma_f32 v91, v91, s65, v117\n"
    "v_fma_f32 v112, v112, s65, v117\n"
    "v_fma_f32 v113, v113, s65, v117\n"
    "v_exp_f32 v88, v88\n"
    "v_exp_f32 v89, v89\n"
    "v_exp_f32 v90, v90\n"
    "v_exp_f32 v91, v91\n"
    "v_cvt_pk_bf16_f32 v56, v88, v89\n"
    "v_cvt_pk_bf16_f32 v57, v90, v91\n"
    "v_exp_f32 v112, v112\n"
    "v_mov_b32_dpp v58, v56 row_shl:8 row_mask:0xf bank_mask:0xf bound_ctrl:1\n"
    "v_mov_b32_dpp v59, v57 row_shl:8 row_mask:0xf bank_mask:0xf bound_ctrl:1\n"
    "v_exp_f32 v113, v113\n"
    "v_mov_b32 v93, 0\n"
    "v_mfma_f32_16x16x32_bf16 v[104:107], v[60:63], v[56:59], 0\n"
    "s_waitcnt lgkmcnt(1)\n"
    "v_cvt_pk_bf16_f32 v92, v112, v113\n"
    "v_add_f32 v48, v48, v50\n"
    "v_mfma_f32_16x16x32_bf16 v[96:99], v[80:83], v[56:59], 0\n"
    "v_add_f32 v49, v49, v51\n"
    "v_cvt_pk_bf16_f32 v48, v48, v49\n"
    "v_mfma_f32_16x16x16_bf16 v[104:107], v[60:61], v[92:93], v[104:107]\n"
    "s_waitcnt lgkmcnt(0)\n"
    "v_add_f32 v52, v52, v54\n"
    "v_add_f32 v53, v53, v55\n"
    "v_mfma_f32_16x16x32_bf16 v[100:103], v[84:87], v[56:59], 0\n"
    "v_cvt_pk_bf16_f32 v49, v52, v53\n"
    "v_cvt_pk_bf16_f32 v95, v112, v113\n"
    "v_mov_b32 v94, 0\n"
    "v_mfma_f32_16x16x16_bf16 v[96:99], v[48:49], v[92:93], v[96:99]\n"
    "v_rcp_f32 v104, v104\n"
    "v_mfma_f32_16x16x16_bf16 v[100:103], v[48:49], v[94:95], v[100:103]\n"
    "ds_write_b64 v10, v[48:49]\n"
    "v_mov_b64 v[114:115], 0\n"
    "v_mov_b32 v118, 158212\n"
    "s_mov_b64 exec, s[78:79]\n"
    "v_pk_mul_f32 v[96:97], v[96:97], v[104:105] op_sel_hi:[1,0]\n"
    "v_pk_mul_f32 v[98:99], v[98:99], v[104:105] op_sel_hi:[1,0]\n"
    "v_cvt_pk_bf16_f32 v96, v96, v97\n"
    "v_cvt_pk_bf16_f32 v97, v98, v99\n"
    "ds_write_b64 v11, v[96:97] offset:0\n"
    "v_pk_mul_f32 v[100:101], v[100:101], v[104:105] op_sel_hi:[1,0]\n"
    "v_pk_mul_f32 v[102:103], v[102:103], v[104:105] op_sel_hi:[1,0]\n"
    "v_cvt_pk_bf16_f32 v98, v100, v101\n"
    "v_cvt_pk_bf16_f32 v99, v102, v103\n"
    "ds_write_b64 v11, v[98:99] offset:8\n"
    "s_mov_b64 exec, 0xff\n"
    "buffer_load_dword v116, v0, s[16:19], s59 offen sc1\n"
    "s_mov_b64 exec, -1\n"
    "v_mfma_f32_32x32x8_bf16 v[72:87], v[114:115], v[114:115], 0\n"
    "s_add_u32 s36, s36, 1\n"
    "s_cmp_eq_u32 s36, 64\n"
    "s_waitcnt lgkmcnt(0)\n"
    "s_waitcnt vmcnt(1)\n"
    "s_barrier\n"
    "ds_read_b128 v[104:107], v12 offset:0\n"
    "ds_read_b128 v[108:111], v12 offset:1024\n"
    "v_mfma_f32_32x32x8_bf16 v[48:63], v[114:115], v[114:115], 0\n"
    "ds_read_b32 v119, v118 offset:0\n"
    "s_cbranch_scc1 LEND%=\n"
    "s_waitcnt lgkmcnt(2)\n"
    "v_smfmac_f32_16x16x64_bf16 v[72:75], v[104:107], v[168:175], v1\n"
    "ds_read_b128 v[64:67], v7 offset:20480\n"
    "s_waitcnt lgkmcnt(2)\n"
    "v_smfmac_f32_16x16x64_bf16 v[72:75], v[108:111], v[176:183], v1\n"
    "ds_read_b128 v[68:71], v7 offset:21504\n"
    "v_smfmac_f32_16x16x64_bf16 v[76:79], v[104:107], v[184:191], v1\n"
    "ds_read_b128 v[16:19], v7 offset:22528\n"
    "v_smfmac_f32_16x16x64_bf16 v[76:79], v[108:111], v[192:199], v1\n"
    "ds_read_b128 v[20:23], v7 offset:23552\n"
    "v_add_f32 v72, v72, v74\n"
    "v_smfmac_f32_16x16x64_bf16 v[80:83], v[104:107], v[200:207], v1\n"
    "ds_read_b128 v[24:27], v7 offset:24576\n"
    "v_add_f32 v73, v73, v75\n"
    "v_smfmac_f32_16x16x64_bf16 v[80:83], v[108:111], v[208:215], v1\n"
    "ds_read_b128 v[28:31], v7 offset:25600\n"
    "v_add_f32 v76, v76, v78\n"
    "v_smfmac_f32_16x16x64_bf16 v[84:87], v[104:107], v[216:223], v1\n"
    "ds_read_b128 v[32:35], v7 offset:26624\n"
    "v_add_f32 v77, v77, v79\n"
    "v_smfmac_f32_16x16x64_bf16 v[84:87], v[108:111], v[224:231], v1\n"
    "ds_read_b128 v[36:39], v7 offset:27648\n"
    "v_cvt_pk_bf16_f32 v88, v72, v76\n"
    "v_cvt_pk_bf16_f32 v90, v73, v77\n"
    "v_smfmac_f32_16x16x64_bf16 v[48:51], v[104:107], v[232:239], v1\n"
    "ds_read_b128 v[40:43], v7 offset:28672\n"
    "v_add_f32 v80, v80, v82\n"
    "v_add_f32 v81, v81, v83\n"
    "v_smfmac_f32_16x16x64_bf16 v[48:51], v[108:111], v[240:247], v1\n"
    "ds_read_b128 v[44:47], v7 offset:29696\n"
    "v_add_f32 v85, v85, v87\n"
    "v_add_f32 v84, v84, v86\n"
    "v_cvt_pk_bf16_f32 v91, v81, v85\n"
    "v_cvt_pk_bf16_f32 v89, v80, v84\n"
    "s_waitcnt lgkmcnt(10)\n"
    "v_and_or_b32 v91, v91, s66, v119\n"
    "buffer_store_dwordx4 v[88:91], v13, s[16:19], s46 offen sc1\n"
    "v_smfmac_f32_16x16x64_bf16 v[52:55], v[104:107], v[248:255], v1\n"
    "s_waitcnt lgkmcnt(8)\n"
    "v_smfmac_f32_16x16x64_bf16 v[52:55], v[108:111], v[64:71], v1\n"
    "s_waitcnt lgkmcnt(6)\n"
    "v_add_f32 v48, v48, v50\n"
    "v_smfmac_f32_16x16x64_bf16 v[56:59], v[104:107], v[16:23], v1\n"
    "s_waitcnt lgkmcnt(4)\n"
    "v_add_f32 v49, v49, v51\n"
    "v_smfmac_f32_16x16x64_bf16 v[56:59], v[108:111], v[24:31], v1\n"
    "s_waitcnt lgkmcnt(2)\n"
    "v_add_f32 v52, v52, v54\n"
    "v_add_f32 v53, v53, v55\n"
    "v_smfmac_f32_16x16x64_bf16 v[60:63], v[104:107], v[32:39], v1\n"
    "s_waitcnt lgkmcnt(0)\n"
    "v_cvt_pk_bf16_f32 v92, v48, v52\n"
    "v_cvt_pk_bf16_f32 v94, v49, v53\n"
    "v_smfmac_f32_16x16x64_bf16 v[60:63], v[108:111], v[40:47], v1\n"
    "v_add_f32 v56, v56, v58\n"
    "v_add_f32 v57, v57, v59\n"
    "s_nop 5\n"
    "v_add_f32 v61, v61, v63\n"
    "v_add_f32 v60, v60, v62\n"
    "v_cvt_pk_bf16_f32 v95, v57, v61\n"
    "v_cvt_pk_bf16_f32 v93, v56, v60\n"
    "v_and_or_b32 v95, v95, s66, v119\n"
    "buffer_store_dwordx4 v[92:95], v13, s[16:19], s47 offen sc1\n"
    "s_add_u32 s54, s50, s57\n"
    "s_lshr_b32 s38, s54, 8\n"
    "s_xor_b32 s38, s38, s74\n"
    "s_and_b32 s38, s38, 0x2000\n"
    "s_or_b32 s54, s54, s38\n"
    "s_add_u32 s50, s50, 2621440\n"
    "s_add_u32 s48, s48, 16384\n"
    "s_add_u32 s60, s60, 128\n"
    "s_cmp_eq_u32 s61, 1\n"
    "s_cbranch_scc1 LW1%=\n"
    "s_cmp_gt_u32 s61, 1\n"
    "s_cbranch_scc1 LW2%=\n"
    "ds_read_b128 v[64:67], v9 offset:35584\n"
    "s_add_u32 s51, s46, s63\n"
    "s_add_u32 s52, s51, 4096\n"
    "s_add_u32 s39, s44, s9\n"
    "s_waitcnt lgkmcnt(0)\n"
    "LUG%=:\n"
    "s_memtime s[40:41]\n"
    "s_waitcnt lgkmcnt(0)\n"
    "s_sub_u32 s40, s39, s40\n"
    "s_sub_u32 s41, s40, 253\n"
    "s_cmp_lt_u32 s41, 16384\n"
    "s_cbranch_scc1 LUG%=\n"
    "s_cmp_lt_u32 s40, 253\n"
    "s_cselect_b32 s40, s40, 0\n"
    "s_and_b32 s40, s40, 0xfc\n"
    "s_getpc_b64 s[42:43]\n"
    "s_sub_u32 s40, 272, s40\n"
    "s_add_u32 s42, s42, s40\n"
    "s_addc_u32 s43, s43, 0\n"
    "s_setpc_b64 s[42:43]\n"
    ".rept 63\n"
    "s_nop 0\n"
    ".endr\n"
    "buffer_load_dwordx4 v[16:19], v0, s[16:19], s51 offen offset:0 sc1\n"
    "buffer_load_dwordx4 v[20:23], v0, s[16:19], s51 offen offset:1024 sc1\n"
    "buffer_load_dwordx4 v[24:27], v0, s[16:19], s51 offen offset:2048 sc1\n"
    "buffer_load_dwordx4 v[28:31], v0, s[16:19], s51 offen offset:3072 sc1\n"
    "buffer_load_dwordx4 v[32:35], v0, s[16:19], s52 offen offset:0 sc1\n"
    "buffer_load_dwordx4 v[36:39], v0, s[16:19], s52 offen offset:1024 sc1\n"
    "buffer_load_dwordx4 v[40:43], v0, s[16:19], s52 offen offset:2048 sc1\n"
    "buffer_load_dwordx4 v[44:47], v0, s[16:19], s52 offen offset:3072 sc1\n"
    "LGA%=:\n"
    "s_waitcnt vmcnt(7)\n"
    "v_mfma_f32_16x16x32_bf16 v[68:71], v[64:67], v[16:19], 0\n"
    "s_waitcnt vmcnt(6)\n"
    "v_mfma_f32_16x16x32_bf16 v[68:71], v[64:67], v[20:23], v[68:71]\n"
    "s_waitcnt vmcnt(5)\n"
    "v_bitop3_b32 v76, v19, v23, s64 bitop3:0x7e\n"
    "v_mfma_f32_16x16x32_bf16 v[68:71], v[64:67], v[24:27], v[68:71]\n"
    "s_waitcnt vmcnt(4)\n"
    "v_mfma_f32_16x16x32_bf16 v[68:71], v[64:67], v[28:31], v[68:71]\n"
    "s_waitcnt vmcnt(3)\n"
    "v_bitop3_b32 v77, v27, v31, s64 bitop3:0x7e\n"
    "v_mfma_f32_16x16x32_bf16 v[72:75], v[64:67], v[32:35], 0\n"
    "s_waitcnt vmcnt(2)\n"
    "v_mfma_f32_16x16x32_bf16 v[72:75], v[64:67], v[36:39], v[72:75]\n"
    "v_bitop3_b32 v78, v35, v39, s64 bitop3:0x7e\n"
    "v_or3_b32 v76, v76, v77, v78\n"
    "v_add_f32_dpp v68, v68, v68 row_ror:8 row_mask:0xf bank_mask:0xf\n"
    "v_add_f32_dpp v69, v69, v69 row_ror:8 row_mask:0xf bank_mask:0xf\n"
    "v_add_f32_dpp v70, v70, v70 row_ror:8 row_mask:0xf bank_mask:0xf\n"
    "v_add_f32_dpp v71, v71, v71 row_ror:8 row_mask:0xf bank_mask:0xf\n"
    "v_cvt_pk_bf16_f32 v81, v70, v71\n"
    "v_cvt_pk_bf16_f32 v80, v68, v69\n"
    "s_waitcnt vmcnt(1)\n"
    "v_mfma_f32_16x16x32_bf16 v[72:75], v[64:67], v[40:43], v[72:75]\n"
    "s_waitcnt vmcnt(0)\n"
    "v_mfma_f32_16x16x32_bf16 v[72:75], v[64:67], v[44:47], v[72:75]\n"
    "v_bitop3_b32 v79, v43, v47, s64 bitop3:0x7e\n"
    "v_or_b32 v76, v76, v79\n"
    "v_and_b32 v77, 1, v76\n"
    "v_cmp_ne_u32 vcc, 0, v77\n"
    "s_cbranch_vccnz LGR%=\n"
    "s_nop 2\n"
    "v_add_f32_dpp v72, v72, v72 row_ror:8 row_mask:0xf bank_mask:0xf\n"
    "v_add_f32_dpp v73, v73, v73 row_ror:8 row_mask:0xf bank_mask:0xf\n"
    "v_add_f32_dpp v74, v74, v74 row_ror:8 row_mask:0xf bank_mask:0xf\n"
    "v_add_f32_dpp v75, v75, v75 row_ror:8 row_mask:0xf bank_mask:0xf\n"
    "v_cvt_pk_bf16_f32 v83, v74, v75\n"
    "v_cvt_pk_bf16_f32 v82, v72, v73\n"
    "v_min_u32 v83, -2, v83\n"
    "buffer_store_dwordx4 v[80:83], v14, s[16:19], s48 offen sc1\n"
    "v_and_b32 v76, 0x10000, v76\n"
    "v_cmp_ne_u32 vcc, 0, v76\n"
    "v_mov_b32 v77, 158224\n"
    "s_nop 0\n"
    "s_or_b32 s38, vcc_lo, vcc_hi\n"
    "v_mov_b32 v76, s38\n"
    "ds_write_b32 v77, v76\n"
    "s_add_u32 s53, s50, s55\n"
    "s_add_u32 s49, s48, s62\n"
    "s_add_u32 s59, s59, 128\n"
    "s_add_u32 s51, s53, 0\n"
    "s_lshr_b32 s38, s51, 8\n"
    "s_xor_b32 s38, s38, s56\n"
    "s_and_b32 s38, s38, 0x2000\n"
    "s_or_b32 s51, s51, s38\n"
    "s_add_u32 s52, s53, 524288\n"
    "s_lshr_b32 s38, s52, 8\n"
    "s_xor_b32 s38, s38, s56\n"
    "s_and_b32 s38, s38, 0x2000\n"
    "s_or_b32 s52, s52, s38\n"
    "s_add_u32 s53, s53, 1048576\n"
    "s_lshr_b32 s38, s53, 8\n"
    "s_xor_b32 s38, s38, s56\n"
    "s_and_b32 s38, s38, 0x2000\n"
    "s_or_b32 s53, s53, s38\n"
    "s_xor_b32 s46, s46, 0x80000\n"
    "s_add_u32 s47, s46, 4096\n"
    "s_bfe_u32 s64, s36, 0x10001\n"
    "s_add_u32 s44, s44, s8\n"
    "buffer_load_dword v117, v14, s[16:19], s48 offen sc1\n"
    "s_branch LBM%=\n"
    "LW1%=:\n"
    "buffer_store_dword v1, off, s[16:19], s58 sc1\n"
    "s_add_u32 s58, s58, 4\n"
    "s_bitset0_b32 s58, 31\n"
    "s_waitcnt vmcnt(3)\n"
    "v_cmp_ne_u32 vcc, 0, v116\n"
    "s_nop 1\n"
    "s_andn2_b32 s38, 7, vcc_lo\n"
    "s_cbranch_scc0 LS2K%=\n"
    "s_mov_b32 s37, 0x80000\n"
    "LS2W%=:\n"
    "s_mov_b64 exec, 0xff\n"
    "buffer_load_dword v116, v0, s[16:19], s59 offen sc1\n"
    "s_mov_b64 exec, -1\n"
    "s_waitcnt vmcnt(0)\n"
    "v_cmp_ne_u32 vcc, 0, v116\n"
    "s_nop 1\n"
    "s_andn2_b32 s38, 7, vcc_lo\n"
    "s_cbranch_scc0 LS2K%=\n"
    "s_sleep 2\n"
    "s_sub_u32 s37, s37, 1\n"
    "s_cbranch_scc0 LS2W%=\n"
    "s_branch LFAIL%=\n"
    "LS2K%=:\n"
    "s_add_u32 s53, s50, s55\n"
    "s_add_u32 s49, s48, s62\n"
    "s_add_u32 s59, s59, 128\n"
    "s_add_u32 s51, s53, 0\n"
    "s_lshr_b32 s38, s51, 8\n"
    "s_xor_b32 s38, s38, s56\n"
    "s_and_b32 s38, s38, 0x2000\n"
    "s_or_b32 s51, s51, s38\n"
    "s_add_u32 s52, s53, 524288\n"
    "s_lshr_b32 s38, s52, 8\n"
    "s_xor_b32 s38, s38, s56\n"
    "s_and_b32 s38, s38, 0x2000\n"
    "s_or_b32 s52, s52, s38\n"
    "s_add_u32 s53, s53, 1048576\n"
    "s_lshr_b32 s38, s53, 8\n"
    "s_xor_b32 s38, s38, s56\n"
    "s_and_b32 s38, s38, 0x2000\n"
    "s_or_b32 s53, s53, s38\n"
    "v_mov_b32 v64, 158240\n"
    "s_mov_b32 s37, 0x80000\n"
    "LKW%=:\n"
    "ds_read_b64 v[66:67], v64\n"
    "s_waitcnt lgkmcnt(0)\n"
    "v_min_u32 v66, v66, v67\n"
    "v_cmp_eq_u32 vcc, s36, v66\n"
    "s_cbranch_vccnz LKK%=\n"
    "s_sleep 1\n"
    "s_sub_u32 s37, s37, 1\n"
    "s_cbranch_scc0 LKW%=\n"
    "s_branch LFAIL%=\n"
    "LKK%=:\n"
    "s_add_u32 s39, s44, s12\n"
    "LUF%=:\n"
    "s_memtime s[40:41]\n"
    "s_waitcnt lgkmcnt(0)\n"
    "s_sub_u32 s40, s39, s40\n"
    "s_sub_u32 s41, s40, 253\n"
    "s_cmp_lt_u32 s41, 16384\n"
    "s_cbranch_scc1 LUF%=\n"
    "s_cmp_lt_u32 s40, 253\n"
    "s_cselect_b32 s40, s40, 0\n"
    "s_and_b32 s40, s40, 0xfc\n"
    "s_getpc_b64 s[42:43]\n"
    "s_sub_u32 s40, 272, s40\n"
    "s_add_u32 s42, s42, s40\n"
    "s_addc_u32 s43, s43, 0\n"
    "s_setpc_b64 s[42:43]\n"
    ".rept 63\n"
    "s_nop 0\n"
    ".endr\n"
    "s_sub_u32 s51, s51, 2048\n"
    "s_mov_b32 m0, 122880\n"
    "s_nop 0\n"
    "buffer_load_dwordx4 v0, s[16:19], s51 offen offset:0 sc1 lds\n"
    "buffer_load_dwordx4 v0, s[16:19], s51 offen offset:1024 sc1 lds\n"
    "buffer_load_dwordx4 v0, s[16:19], s51 offen offset:2048 sc1 lds\n"
    "buffer_load_dwordx4 v0, s[16:19], s51 offen offset:3072 sc1 lds\n"
    "s_mov_b32 m0, 126976\n"
    "s_add_u32 s51, s51, 4096\n"
    "buffer_load_dwordx4 v0, s[16:19], s51 offen offset:0 sc1 lds\n"
    "buffer_load_dwordx4 v0, s[16:19], s51 offen offset:1024 sc1 lds\n"
    "buffer_load_dwordx4 v0, s[16:19], s51 offen offset:2048 sc1 lds\n"
    "buffer_load_dwordx4 v0, s[16:19], s51 offen offset:3072 sc1 lds\n"
    "s_xor_b32 s46, s46, 0x80000\n"
    "s_add_u32 s47, s46, 4096\n"
    "s_bfe_u32 s64, s36, 0x10001\n"
    "s_add_u32 s44, s44, s8\n"
    "LUM%=:\n"
    "s_memtime s[40:41]\n"
    "s_waitcnt lgkmcnt(0)\n"
    "s_sub_u32 s40, s44, s40\n"
    "s_sub_u32 s41, s40, 253\n"
    "s_cmp_lt_u32 s41, 16384\n"
    "s_cbranch_scc1 LUM%=\n"
    "s_cmp_lt_u32 s40, 253\n"
    "s_cselect_b32 s40, s40, 0\n"
    "s_and_b32 s40, s40, 0xfc\n"
    "s_getpc_b64 s[42:43]\n"
    "s_sub_u32 s40, 272, s40\n"
    "s_add_u32 s42, s42, s40\n"
    "s_addc_u32 s43, s43, 0\n"
    "s_setpc_b64 s[42:43]\n"
    ".rept 63\n"
    "s_nop 0\n"
    ".endr\n"
    "s_branch LBM%=\n"
    "LW2%=:\n"
    "v_add_u32 v118, 122880, v0\n"
    "ds_read_b128 v[16:19], v14 offset:0\n"
    "ds_read_b128 v[32:35], v118 offset:0\n"
    "ds_read_b128 v[48:51], v118 offset:4096\n"
    "ds_read_b128 v[20:23], v14 offset:1024\n"
    "ds_read_b128 v[36:39], v118 offset:1024\n"
    "ds_read_b128 v[52:55], v118 offset:5120\n"
    "ds_read_b128 v[24:27], v14 offset:2048\n"
    "ds_read_b128 v[40:43], v118 offset:2048\n"
    "ds_read_b128 v[56:59], v118 offset:6144\n"
    "ds_read_b128 v[28:31], v14 offset:3072\n"
    "ds_read_b128 v[44:47], v118 offset:3072\n"
    "ds_read_b128 v[60:63], v118 offset:7168\n"
    "s_waitcnt lgkmcnt(10)\n"
    "v_lshrrev_b32 v112, 1, v0\n"
    "v_add_u32 v112, 149760, v112\n"
    "v_mov_b32 v111, 0xd0289000\n"
    "v_mfma_f32_16x16x32_bf16 v[88:91], v[32:35], v[16:19], 0\n"
    "ds_read_b128 v[64:67], v6 offset:0\n"
    "s_waitcnt lgkmcnt(10)\n"
    "v_mfma_f32_16x16x32_bf16 v[92:95], v[48:51], v[16:19], 0\n"
    "ds_read_b128 v[68:71], v6 offset:64\n"
    "s_waitcnt lgkmcnt(9)\n"
    "v_mfma_f32_16x16x32_bf16 v[88:91], v[36:39], v[20:23], v[88:91]\n"
    "ds_read_b128 v[72:75], v6 offset:128\n"
    "s_waitcnt lgkmcnt(9)\n"
    "v_mfma_f32_16x16x32_bf16 v[92:95], v[52:55], v[20:23], v[92:95]\n"
    "ds_read_b128 v[76:79], v6 offset:192\n"
    "s_waitcnt lgkmcnt(8)\n"
    "v_mfma_f32_16x16x32_bf16 v[88:91], v[40:43], v[24:27], v[88:91]\n"
    "ds_read_b64 v[80:81], v112 offset:0\n"
    "s_waitcnt lgkmcnt(8)\n"
    "v_mfma_f32_16x16x32_bf16 v[92:95], v[56:59], v[24:27], v[92:95]\n"
    "ds_read_b64 v[82:83], v112 offset:512\n"
    "s_waitcnt lgkmcnt(7)\n"
    "v_mfma_f32_16x16x32_bf16 v[88:91], v[44:47], v[28:31], v[88:91]\n"
    "ds_read_b64 v[84:85], v112 offset:1024\n"
    "s_waitcnt lgkmcnt(7)\n"
    "v_mfma_f32_16x16x32_bf16 v[92:95], v[60:63], v[28:31], v[92:95]\n"
    "ds_read_b64 v[86:87], v112 offset:1536\n"
    "v_mov_b32 v108, s72\n"
    "v_mov_b32 v109, s36\n"
    "ds_write_b32 v108, v109 offset:24\n"
    "s_waitcnt lgkmcnt(8)\n"
    "v_mfma_f32_16x16x32_bf16 v[96:99], v[64:67], v[16:19], 0\n"
    "ds_read_b128 v[32:35], v118 offset:8192\n"
    "ds_read_b128 v[36:39], v118 offset:9216\n"
    "s_waitcnt lgkmcnt(9)\n"
    "v_mfma_f32_16x16x32_bf16 v[96:99], v[68:71], v[20:23], v[96:99]\n"
    "ds_read_b128 v[40:43], v118 offset:10240\n"
    "ds_read_b128 v[44:47], v118 offset:11264\n"
    "s_waitcnt lgkmcnt(10)\n"
    "v_cndmask_b32 v88, v111, v88, s[80:81]\n"
    "v_cndmask_b32 v89, v111, v89, s[82:83]\n"
    "v_mfma_f32_16x16x32_bf16 v[96:99], v[72:75], v[24:27], v[96:99]\n"
    "ds_read_b128 v[48:51], v118 offset:12288\n"
    "ds_read_b128 v[52:55], v118 offset:13312\n"
    "s_waitcnt lgkmcnt(11)\n"
    "v_cndmask_b32 v90, v111, v90, s[84:85]\n"
    "v_cndmask_b32 v91, v111, v91, s[86:87]\n"
    "v_mfma_f32_16x16x32_bf16 v[96:99], v[76:79], v[28:31], v[96:99]\n"
    "ds_read_b128 v[56:59], v118 offset:14336\n"
    "ds_read_b128 v[60:63], v118 offset:15360\n"
    "v_cndmask_b32 v92, v111, v92, s[88:89]\n"
    "v_cndmask_b32 v93, v111, v93, s[90:91]\n"
    "v_cndmask_b32 v94, v111, v94, s[92:93]\n"
    "v_cndmask_b32 v95, v111, v95, s[94:95]\n"
    "v_max3_f32 v108, v88, v89, v90\n"
    "v_max3_f32 v109, v91, v92, v93\n"
    "v_max3_f32 v108, v108, v94, v95\n"
    "v_max3_f32 v109, v109, v96, v97\n"
    "v_max_f32 v108, v108, v109\n"
    "v_mov_b32 v109, v108\n"
    "s_nop 1\n"
    "v_permlane16_swap_b32 v108, v109\n"
    "v_max_f32 v108, v108, v109\n"
    "v_mov_b32 v109, v108\n"
    "s_nop 1\n"
    "v_permlane32_swap_b32 v108, v109\n"
    "v_max_f32 v108, v108, v109\n"
    "v_mul_f32 v108, 0xbe0293ee, v108\n"
    "v_fma_f32 v88, v88, s65, v108\n"
    "v_fma_f32 v89, v89, s65, v108\n"
    "v_fma_f32 v90, v90, s65, v108\n"
    "v_fma_f32 v91, v91, s65, v108\n"
    "v_fma_f32 v92, v92, s65, v108\n"
    "v_fma_f32 v93, v93, s65, v108\n"
    "v_fma_f32 v94, v94, s65, v108\n"
    "v_fma_f32 v95, v95, s65, v108\n"
    "v_fma_f32 v96, v96, s65, v108\n"
    "v_fma_f32 v97, v97, s65, v108\n"
    "v_exp_f32 v88, v88\n"
    "v_exp_f32 v89, v89\n"
    "v_exp_f32 v90, v90\n"
    "v_exp_f32 v91, v91\n"
    "v_exp_f32 v92, v92\n"
    "v_exp_f32 v93, v93\n"
    "v_exp_f32 v94, v94\n"
    "v_exp_f32 v95, v95\n"
    "v_exp_f32 v96, v96\n"
    "v_exp_f32 v97, v97\n"
    "v_add_f32 v108, v88, v89\n"
    "v_add_f32 v110, v90, v91\n"
    "v_add_f32 v108, v108, v92\n"
    "v_add_f32 v110, v110, v93\n"
    "v_add_f32 v108, v108, v94\n"
    "v_add_f32 v110, v110, v95\n"
    "v_add_f32 v108, v108, v96\n"
    "v_add_f32 v110, v110, v97\n"
    "v_add_f32 v108, v108, v110\n"
    "v_mov_b32 v109, v108\n"
    "s_nop 1\n"
    "v_permlane16_swap_b32 v108, v109\n"
    "v_add_f32 v108, v108, v109\n"
    "v_mov_b32 v109, v108\n"
    "s_nop 1\n"
    "v_permlane32_swap_b32 v108, v109\n"
    "v_add_f32 v108, v108, v109\n"
    "v_rcp_f32 v108, v108\n"
    "v_mov_b32 v105, 0\n"
    "v_mov_b32 v106, 0\n"
    "v_mul_f32 v88, v88, v108\n"
    "v_mul_f32 v89, v89, v108\n"
    "v_mul_f32 v90, v90, v108\n"
    "v_mul_f32 v91, v91, v108\n"
    "v_mul_f32 v92, v92, v108\n"
    "v_mul_f32 v93, v93, v108\n"
    "v_mul_f32 v94, v94, v108\n"
    "v_mul_f32 v95, v95, v108\n"
    "v_mul_f32 v96, v96, v108\n"
    "v_mul_f32 v97, v97, v108\n"
    "v_cvt_pk_bf16_f32 v100, v88, v89\n"
    "v_cvt_pk_bf16_f32 v101, v90, v91\n"
    "v_cvt_pk_bf16_f32 v102, v92, v93\n"
    "v_cvt_pk_bf16_f32 v103, v94, v95\n"
    "v_cvt_pk_bf16_f32 v104, v96, v97\n"
    "v_cvt_pk_bf16_f32 v107, v96, v97\n"
    "s_waitcnt lgkmcnt(7)\n"
    "v_mfma_f32_16x16x32_bf16 v[64:67], v[32:35], v[100:103], 0\n"
    "s_waitcnt lgkmcnt(6)\n"
    "v_mfma_f32_16x16x32_bf16 v[68:71], v[36:39], v[100:103], 0\n"
    "v_mfma_f32_16x16x16_bf16 v[64:67], v[80:81], v[104:105], v[64:67]\n"
    "v_mfma_f32_16x16x16_bf16 v[68:71], v[80:81], v[106:107], v[68:71]\n"
    "s_waitcnt lgkmcnt(5)\n"
    "v_mfma_f32_16x16x32_bf16 v[72:75], v[40:43], v[100:103], 0\n"
    "s_waitcnt lgkmcnt(4)\n"
    "v_mfma_f32_16x16x32_bf16 v[76:79], v[44:47], v[100:103], 0\n"
    "v_cvt_pk_bf16_f32 v88, v64, v65\n"
    "v_cvt_pk_bf16_f32 v89, v66, v67\n"
    "v_cvt_pk_bf16_f32 v90, v68, v69\n"
    "v_mfma_f32_16x16x16_bf16 v[72:75], v[82:83], v[104:105], v[72:75]\n"
    "v_cvt_pk_bf16_f32 v91, v70, v71\n"
    "ds_write_b128 v14, v[88:91] offset:0\n"
    "v_mfma_f32_16x16x16_bf16 v[76:79], v[82:83], v[106:107], v[76:79]\n"
    "s_waitcnt lgkmcnt(4)\n"
    "v_mfma_f32_16x16x32_bf16 v[16:19], v[48:51], v[100:103], 0\n"
    "s_waitcnt lgkmcnt(3)\n"
    "v_mfma_f32_16x16x32_bf16 v[20:23], v[52:55], v[100:103], 0\n"
    "v_cvt_pk_bf16_f32 v92, v72, v73\n"
    "v_cvt_pk_bf16_f32 v93, v74, v75\n"
    "v_cvt_pk_bf16_f32 v94, v76, v77\n"
    "v_mfma_f32_16x16x16_bf16 v[16:19], v[84:85], v[104:105], v[16:19]\n"
    "v_cvt_pk_bf16_f32 v95, v78, v79\n"
    "ds_write_b128 v14, v[92:95] offset:1024\n"
    "v_mfma_f32_16x16x16_bf16 v[20:23], v[84:85], v[106:107], v[20:23]\n"
    "s_waitcnt lgkmcnt(3)\n"
    "v_mfma_f32_16x16x32_bf16 v[24:27], v[56:59], v[100:103], 0\n"
    "s_waitcnt lgkmcnt(2)\n"
    "v_mfma_f32_16x16x32_bf16 v[28:31], v[60:63], v[100:103], 0\n"
    "v_cvt_pk_bf16_f32 v88, v16, v17\n"
    "v_cvt_pk_bf16_f32 v89, v18, v19\n"
    "v_cvt_pk_bf16_f32 v90, v20, v21\n"
    "v_mfma_f32_16x16x16_bf16 v[24:27], v[86:87], v[104:105], v[24:27]\n"
    "v_cvt_pk_bf16_f32 v91, v22, v23\n"
    "ds_write_b128 v14, v[88:91] offset:2048\n"
    "v_mfma_f32_16x16x16_bf16 v[28:31], v[86:87], v[106:107], v[28:31]\n"
    "s_nop 3\n"
    "v_cvt_pk_bf16_f32 v92, v24, v25\n"
    "v_cvt_pk_bf16_f32 v93, v26, v27\n"
    "s_nop 1\n"
    "v_cvt_pk_bf16_f32 v94, v28, v29\n"
    "v_cvt_pk_bf16_f32 v95, v30, v31\n"
    "ds_write_b128 v14, v[92:95] offset:3072\n"
    "s_add_u32 s53, s50, s55\n"
    "s_add_u32 s49, s48, s62\n"
    "s_add_u32 s59, s59, 128\n"
    "s_add_u32 s51, s53, 0\n"
    "s_lshr_b32 s38, s51, 8\n"
    "s_xor_b32 s38, s38, s56\n"
    "s_and_b32 s38, s38, 0x2000\n"
    "s_or_b32 s51, s51, s38\n"
    "s_add_u32 s52, s53, 524288\n"
    "s_lshr_b32 s38, s52, 8\n"
    "s_xor_b32 s38, s38, s56\n"
    "s_and_b32 s38, s38, 0x2000\n"
    "s_or_b32 s52, s52, s38\n"
    "s_add_u32 s53, s53, 1048576\n"
    "s_lshr_b32 s38, s53, 8\n"
    "s_xor_b32 s38, s38, s56\n"
    "s_and_b32 s38, s38, 0x2000\n"
    "s_or_b32 s53, s53, s38\n"
    "s_xor_b32 s46, s46, 0x80000\n"
    "s_add_u32 s47, s46, 4096\n"
    "s_bfe_u32 s64, s36, 0x10001\n"
    "s_add_u32 s44, s44, s8\n"
    "s_branch LBM%=\n"
    "LEND%=:\n"
    "s_waitcnt lgkmcnt(0)\n"
    "s_cmp_lg_u32 s61, 1\n"
    "s_cbranch_scc1 LE0%=\n"
    "buffer_store_dword v1, off, s[16:19], s58 sc1\n"
    "s_add_u32 s58, s58, 4\n"
    "s_bitset0_b32 s58, 31\n"
    "LE0%=:\n"
    "s_add_u32 s54, s50, s57\n"
    "s_lshr_b32 s38, s54, 8\n"
    "s_xor_b32 s38, s38, s74\n"
    "s_and_b32 s38, s38, 0x2000\n"
    "s_or_b32 s54, s54, s38\n"
    "s_cmp_lt_u32 s61, 2\n"
    "s_cbranch_scc1 LE1%=\n"
    "v_add_u32 v118, 122880, v0\n"
    "ds_read_b128 v[16:19], v14 offset:0\n"
    "ds_read_b128 v[32:35], v118 offset:0\n"
    "ds_read_b128 v[48:51], v118 offset:4096\n"
    "ds_read_b128 v[20:23], v14 offset:1024\n"
    "ds_read_b128 v[36:39], v118 offset:1024\n"
    "ds_read_b128 v[52:55], v118 offset:5120\n"
    "ds_read_b128 v[24:27], v14 offset:2048\n"
    "ds_read_b128 v[40:43], v118 offset:2048\n"
    "ds_read_b128 v[56:59], v118 offset:6144\n"
    "ds_read_b128 v[28:31], v14 offset:3072\n"
    "ds_read_b128 v[44:47], v118 offset:3072\n"
    "ds_read_b128 v[60:63], v118 offset:7168\n"
    "s_waitcnt lgkmcnt(10)\n"
    "v_lshrrev_b32 v112, 1, v0\n"
    "v_add_u32 v112, 149760, v112\n"
    "v_mov_b32 v111, 0xd0289000\n"
    "v_mfma_f32_16x16x32_bf16 v[88:91], v[32:35], v[16:19], 0\n"
    "ds_read_b128 v[64:67], v6 offset:0\n"
    "s_waitcnt lgkmcnt(10)\n"
    "v_mfma_f32_16x16x32_bf16 v[92:95], v[48:51], v[16:19], 0\n"
    "ds_read_b128 v[68:71], v6 offset:64\n"
    "s_waitcnt lgkmcnt(9)\n"
    "v_mfma_f32_16x16x32_bf16 v[88:91], v[36:39], v[20:23], v[88:91]\n"
    "ds_read_b128 v[72:75], v6 offset:128\n"
    "s_waitcnt lgkmcnt(9)\n"
    "v_mfma_f32_16x16x32_bf16 v[92:95], v[52:55], v[20:23], v[92:95]\n"
    "ds_read_b128 v[76:79], v6 offset:192\n"
    "s_waitcnt lgkmcnt(8)\n"
    "v_mfma_f32_16x16x32_bf16 v[88:91], v[40:43], v[24:27], v[88:91]\n"
    "ds_read_b64 v[80:81], v112 offset:0\n"
    "s_waitcnt lgkmcnt(8)\n"
    "v_mfma_f32_16x16x32_bf16 v[92:95], v[56:59], v[24:27], v[92:95]\n"
    "ds_read_b64 v[82:83], v112 offset:512\n"
    "s_waitcnt lgkmcnt(7)\n"
    "v_mfma_f32_16x16x32_bf16 v[88:91], v[44:47], v[28:31], v[88:91]\n"
    "ds_read_b64 v[84:85], v112 offset:1024\n"
    "s_waitcnt lgkmcnt(7)\n"
    "v_mfma_f32_16x16x32_bf16 v[92:95], v[60:63], v[28:31], v[92:95]\n"
    "ds_read_b64 v[86:87], v112 offset:1536\n"
    "v_mov_b32 v108, s72\n"
    "v_mov_b32 v109, s36\n"
    "ds_write_b32 v108, v109 offset:24\n"
    "s_waitcnt lgkmcnt(8)\n"
    "v_mfma_f32_16x16x32_bf16 v[96:99], v[64:67], v[16:19], 0\n"
    "ds_read_b128 v[32:35], v118 offset:8192\n"
    "ds_read_b128 v[36:39], v118 offset:9216\n"
    "s_waitcnt lgkmcnt(9)\n"
    "v_mfma_f32_16x16x32_bf16 v[96:99], v[68:71], v[20:23], v[96:99]\n"
    "ds_read_b128 v[40:43], v118 offset:10240\n"
    "ds_read_b128 v[44:47], v118 offset:11264\n"
    "s_waitcnt lgkmcnt(10)\n"
    "v_cndmask_b32 v88, v111, v88, s[80:81]\n"
    "v_cndmask_b32 v89, v111, v89, s[82:83]\n"
    "v_mfma_f32_16x16x32_bf16 v[96:99], v[72:75], v[24:27], v[96:99]\n"
    "ds_read_b128 v[48:51], v118 offset:12288\n"
    "ds_read_b128 v[52:55], v118 offset:13312\n"
    "s_waitcnt lgkmcnt(11)\n"
    "v_cndmask_b32 v90, v111, v90, s[84:85]\n"
    "v_cndmask_b32 v91, v111, v91, s[86:87]\n"
    "v_mfma_f32_16x16x32_bf16 v[96:99], v[76:79], v[28:31], v[96:99]\n"
    "ds_read_b128 v[56:59], v118 offset:14336\n"
    "ds_read_b128 v[60:63], v118 offset:15360\n"
    "v_cndmask_b32 v92, v111, v92, s[88:89]\n"
    "v_cndmask_b32 v93, v111, v93, s[90:91]\n"
    "v_cndmask_b32 v94, v111, v94, s[92:93]\n"
    "v_cndmask_b32 v95, v111, v95, s[94:95]\n"
    "v_max3_f32 v108, v88, v89, v90\n"
    "v_max3_f32 v109, v91, v92, v93\n"
    "v_max3_f32 v108, v108, v94, v95\n"
    "v_max3_f32 v109, v109, v96, v97\n"
    "v_max_f32 v108, v108, v109\n"
    "v_mov_b32 v109, v108\n"
    "s_nop 1\n"
    "v_permlane16_swap_b32 v108, v109\n"
    "v_max_f32 v108, v108, v109\n"
    "v_mov_b32 v109, v108\n"
    "s_nop 1\n"
    "v_permlane32_swap_b32 v108, v109\n"
    "v_max_f32 v108, v108, v109\n"
    "v_mul_f32 v108, 0xbe0293ee, v108\n"
    "v_fma_f32 v88, v88, s65, v108\n"
    "v_fma_f32 v89, v89, s65, v108\n"
    "v_fma_f32 v90, v90, s65, v108\n"
    "v_fma_f32 v91, v91, s65, v108\n"
    "v_fma_f32 v92, v92, s65, v108\n"
    "v_fma_f32 v93, v93, s65, v108\n"
    "v_fma_f32 v94, v94, s65, v108\n"
    "v_fma_f32 v95, v95, s65, v108\n"
    "v_fma_f32 v96, v96, s65, v108\n"
    "v_fma_f32 v97, v97, s65, v108\n"
    "v_exp_f32 v88, v88\n"
    "v_exp_f32 v89, v89\n"
    "v_exp_f32 v90, v90\n"
    "v_exp_f32 v91, v91\n"
    "v_exp_f32 v92, v92\n"
    "v_exp_f32 v93, v93\n"
    "v_exp_f32 v94, v94\n"
    "v_exp_f32 v95, v95\n"
    "v_exp_f32 v96, v96\n"
    "v_exp_f32 v97, v97\n"
    "v_add_f32 v108, v88, v89\n"
    "v_add_f32 v110, v90, v91\n"
    "v_add_f32 v108, v108, v92\n"
    "v_add_f32 v110, v110, v93\n"
    "v_add_f32 v108, v108, v94\n"
    "v_add_f32 v110, v110, v95\n"
    "v_add_f32 v108, v108, v96\n"
    "v_add_f32 v110, v110, v97\n"
    "v_add_f32 v108, v108, v110\n"
    "v_mov_b32 v109, v108\n"
    "s_nop 1\n"
    "v_permlane16_swap_b32 v108, v109\n"
    "v_add_f32 v108, v108, v109\n"
    "v_mov_b32 v109, v108\n"
    "s_nop 1\n"
    "v_permlane32_swap_b32 v108, v109\n"
    "v_add_f32 v108, v108, v109\n"
    "v_rcp_f32 v108, v108\n"
    "v_mov_b32 v105, 0\n"
    "v_mov_b32 v106, 0\n"
    "v_mul_f32 v88, v88, v108\n"
    "v_mul_f32 v89, v89, v108\n"
    "v_mul_f32 v90, v90, v108\n"
    "v_mul_f32 v91, v91, v108\n"
    "v_mul_f32 v92, v92, v108\n"
    "v_mul_f32 v93, v93, v108\n"
    "v_mul_f32 v94, v94, v108\n"
    "v_mul_f32 v95, v95, v108\n"
    "v_mul_f32 v96, v96, v108\n"
    "v_mul_f32 v97, v97, v108\n"
    "v_cvt_pk_bf16_f32 v100, v88, v89\n"
    "v_cvt_pk_bf16_f32 v101, v90, v91\n"
    "v_cvt_pk_bf16_f32 v102, v92, v93\n"
    "v_cvt_pk_bf16_f32 v103, v94, v95\n"
    "v_cvt_pk_bf16_f32 v104, v96, v97\n"
    "v_cvt_pk_bf16_f32 v107, v96, v97\n"
    "s_waitcnt lgkmcnt(7)\n"
    "v_mfma_f32_16x16x32_bf16 v[64:67], v[32:35], v[100:103], 0\n"
    "s_waitcnt lgkmcnt(6)\n"
    "v_mfma_f32_16x16x32_bf16 v[68:71], v[36:39], v[100:103], 0\n"
    "v_mfma_f32_16x16x16_bf16 v[64:67], v[80:81], v[104:105], v[64:67]\n"
    "v_mfma_f32_16x16x16_bf16 v[68:71], v[80:81], v[106:107], v[68:71]\n"
    "s_waitcnt lgkmcnt(5)\n"
    "v_mfma_f32_16x16x32_bf16 v[72:75], v[40:43], v[100:103], 0\n"
    "s_waitcnt lgkmcnt(4)\n"
    "v_mfma_f32_16x16x32_bf16 v[76:79], v[44:47], v[100:103], 0\n"
    "v_cvt_pk_bf16_f32 v88, v64, v65\n"
    "v_cvt_pk_bf16_f32 v89, v66, v67\n"
    "v_cvt_pk_bf16_f32 v90, v68, v69\n"
    "v_mfma_f32_16x16x16_bf16 v[72:75], v[82:83], v[104:105], v[72:75]\n"
    "v_cvt_pk_bf16_f32 v91, v70, v71\n"
    "ds_write_b128 v14, v[88:91] offset:0\n"
    "v_mfma_f32_16x16x16_bf16 v[76:79], v[82:83], v[106:107], v[76:79]\n"
    "s_waitcnt lgkmcnt(4)\n"
    "v_mfma_f32_16x16x32_bf16 v[16:19], v[48:51], v[100:103], 0\n"
    "s_waitcnt lgkmcnt(3)\n"
    "v_mfma_f32_16x16x32_bf16 v[20:23], v[52:55], v[100:103], 0\n"
    "v_cvt_pk_bf16_f32 v92, v72, v73\n"
    "v_cvt_pk_bf16_f32 v93, v74, v75\n"
    "v_cvt_pk_bf16_f32 v94, v76, v77\n"
    "v_mfma_f32_16x16x16_bf16 v[16:19], v[84:85], v[104:105], v[16:19]\n"
    "v_cvt_pk_bf16_f32 v95, v78, v79\n"
    "ds_write_b128 v14, v[92:95] offset:1024\n"
    "v_mfma_f32_16x16x16_bf16 v[20:23], v[84:85], v[106:107], v[20:23]\n"
    "s_waitcnt lgkmcnt(3)\n"
    "v_mfma_f32_16x16x32_bf16 v[24:27], v[56:59], v[100:103], 0\n"
    "s_waitcnt lgkmcnt(2)\n"
    "v_mfma_f32_16x16x32_bf16 v[28:31], v[60:63], v[100:103], 0\n"
    "v_cvt_pk_bf16_f32 v88, v16, v17\n"
    "v_cvt_pk_bf16_f32 v89, v18, v19\n"
    "v_cvt_pk_bf16_f32 v90, v20, v21\n"
    "v_mfma_f32_16x16x16_bf16 v[24:27], v[86:87], v[104:105], v[24:27]\n"
    "v_cvt_pk_bf16_f32 v91, v22, v23\n"
    "ds_write_b128 v14, v[88:91] offset:2048\n"
    "v_mfma_f32_16x16x16_bf16 v[28:31], v[86:87], v[106:107], v[28:31]\n"
    "s_nop 3\n"
    "v_cvt_pk_bf16_f32 v92, v24, v25\n"
    "v_cvt_pk_bf16_f32 v93, v26, v27\n"
    "s_nop 1\n"
    "v_cvt_pk_bf16_f32 v94, v28, v29\n"
    "v_cvt_pk_bf16_f32 v95, v30, v31\n"
    "ds_write_b128 v14, v[92:95] offset:3072\n"
    "s_waitcnt lgkmcnt(0)\n"
    "LE1%=:\n"
    "s_barrier\n"
    "ds_read_b128 v[16:19], v9 offset:16384\n"
    "ds_read_b128 v[20:23], v9 offset:17408\n"
    "v_lshrrev_b32 v113, 4, v0\n"
    "v_and_b32 v113, 0x30, v113\n"
    "v_lshl_add_u32 v113, v113, 4, v0\n"
    "s_waitcnt lgkmcnt(1)\n"
    "buffer_store_dwordx4 v[16:19], v113, s[16:19], s54 offen offset:0 sc1\n"
    "s_waitcnt lgkmcnt(0)\n"
    "buffer_store_dwordx4 v[20:23], v113, s[16:19], s54 offen offset:2048 sc1\n"
    "s_waitcnt vmcnt(0)\n"
    "s_barrier\n"
    "buffer_store_dword v1, off, s[16:19], s58 sc1\n"
    "s_endpgm\n"
    "LRS%=:\n"
    "s_waitcnt vmcnt(0)\n"
    "s_cmp_lg_u32 s61, 1\n"
    "s_barrier\n"
    "s_cbranch_scc1 LRB%=\n"
    "s_memtime s[40:41]\n"
    "s_waitcnt lgkmcnt(0)\n"
    "s_sub_u32 s40, s40, s45\n"
    "s_or_b32 s40, s40, 1\n"
    "v_mov_b32 v112, s40\n"
    "s_add_u32 s38, s60, s71\n"
    "buffer_store_dword v112, off, s[16:19], s38 sc1\n"
    "v_lshrrev_b32 v113, 2, v0\n"
    "v_and_b32 v113, 0x7c, v113\n"
    "s_waitcnt vmcnt(0)\n"
    "s_mov_b32 s37, 0x80000\n"
    "LRW%=:\n"
    "buffer_load_dword v112, v113, s[16:19], s60 offen sc1\n"
    "buffer_load_dword v114, v113, s[16:19], s73 offen sc1\n"
    "s_waitcnt vmcnt(0)\n"
    "v_min_u32 v114, v114, v112\n"
    "v_cmp_eq_u32 vcc, 0, v114\n"
    "s_cbranch_vccz LRK%=\n"
    "s_sleep 2\n"
    "s_sub_u32 s37, s37, 1\n"
    "s_cbranch_scc0 LRW%=\n"
    "s_branch LFAIL%=\n"
    "LRK%=:\n"
    "v_max_u32_dpp v112, v112, v112 row_ror:8 row_mask:0xf bank_mask:0xf\n"
    "s_nop 1\n"
    "v_max_u32_dpp v112, v112, v112 row_ror:4 row_mask:0xf bank_mask:0xf\n"
    "s_nop 1\n"
    "v_max_u32_dpp v112, v112, v112 row_ror:2 row_mask:0xf bank_mask:0xf\n"
    "s_nop 1\n"
    "v_max_u32_dpp v112, v112, v112 row_ror:1 row_mask:0xf bank_mask:0xf\n"
    "s_nop 0\n"
    "v_readlane_b32 s44, v112, 0\n"
    "v_readlane_b32 s38, v112, 16\n"
    "s_max_u32 s44, s44, s38\n"
    "s_add_u32 s44, s44, s45\n"
    "s_add_u32 s44, s44, s11\n"
    "v_mov_b32 v112, 158228\n"
    "v_mov_b32 v113, s44\n"
    "ds_write_b32 v112, v113\n"
    "s_waitcnt lgkmcnt(0)\n"
    "LUR%=:\n"
    "s_memtime s[40:41]\n"
    "s_waitcnt lgkmcnt(0)\n"
    "s_sub_u32 s40, s44, s40\n"
    "s_sub_u32 s41, s40, 253\n"
    "s_cmp_lt_u32 s41, 16384\n"
    "s_cbranch_scc1 LUR%=\n"
    "s_cmp_lt_u32 s40, 253\n"
    "s_cselect_b32 s40, s40, 0\n"
    "s_and_b32 s40, s40, 0xfc\n"
    "s_getpc_b64 s[42:43]\n"
    "s_sub_u32 s40, 272, s40\n"
    "s_add_u32 s42, s42, s40\n"
    "s_addc_u32 s43, s43, 0\n"
    "s_setpc_b64 s[42:43]\n"
    ".rept 63\n"
    "s_nop 0\n"
    ".endr\n"
    "LRB%=:\n"
    "s_barrier\n"
    "s_add_u32 m0, s62, 147456\n"
    "s_nop 0\n"
    "buffer_load_dwordx4 v0, s[16:19], s49 offen offset:0 sc1 lds\n"
    "buffer_load_dwordx4 v0, s[16:19], s49 offen offset:1024 sc1 lds\n"
    "v_mov_b32 v112, 158228\n"
    "ds_read_b32 v112, v112 offset:0\n"
    "s_waitcnt lgkmcnt(0)\n"
    "v_readfirstlane_b32 s44, v112\n"
    "s_waitcnt vmcnt(0)\n"
    "s_branch LC%=\n"
    "LMR%=:\n"
    "s_nop 7\n"
    "s_waitcnt lgkmcnt(0)\n"
    "s_barrier\n"
    "s_add_u32 s33, s33, 1\n"
    "s_cmp_gt_u32 s33, 0x80000\n"
    "s_cbranch_scc1 LFAIL%=\n"
    "s_add_u32 m0, s62, 147456\n"
    "s_nop 0\n"
    "buffer_load_dwordx4 v0, s[16:19], s49 offen offset:0 sc1 lds\n"
    "buffer_load_dwordx4 v0, s[16:19], s49 offen offset:1024 sc1 lds\n"
    "ds_read_b128 v[64:67], v7 offset:0\n"
    "ds_read_b128 v[68:71], v7 offset:1024\n"
    "ds_read_b128 v[72:75], v7 offset:2048\n"
    "ds_read_b128 v[76:79], v7 offset:3072\n"
    "ds_read_b128 v[80:83], v7 offset:4096\n"
    "ds_read_b128 v[84:87], v7 offset:5120\n"
    "ds_read_b128 v[88:91], v7 offset:6144\n"
    "ds_read_b128 v[92:95], v7 offset:7168\n"
    "ds_read_b128 v[96:99], v7 offset:8192\n"
    "ds_read_b128 v[100:103], v7 offset:9216\n"
    "ds_read_b128 v[104:107], v7 offset:10240\n"
    "ds_read_b128 v[108:111], v7 offset:11264\n"
    "v_mov_b64 v[114:115], 0\n"
    "s_nop 1\n"
    "v_mfma_f32_32x32x8_bf16 v[48:63], v[114:115], v[114:115], 0\n"
    "s_waitcnt vmcnt(0) lgkmcnt(0)\n"
    "s_branch LC%=\n"
    "LCA%=:\n"
    "s_mov_b32 s37, 8\n"
    "LCB%=:\n"
    "s_memrealtime s[42:43]\n"
    "s_memtime s[40:41]\n"
    "s_waitcnt lgkmcnt(0)\n"
    "s_sub_u32 s42, s42, s44\n"
    "s_cmp_gt_u32 s42, -2000\n"
    "s_cbranch_scc1 LCB%=\n"
    "s_mul_i32 s42, s42, 24\n"
    "s_sub_u32 s40, s40, s42\n"
    "s_cmp_eq_u32 s37, 8\n"
    "s_cselect_b32 s54, s40, s54\n"
    "s_sub_u32 s40, s40, s54\n"
    "s_add_u32 s45, s45, s40\n"
    "s_add_u32 s44, s44, 12\n"
    "s_sub_u32 s37, s37, 1\n"
    "s_cmp_lg_u32 s37, 0\n"
    "s_cbranch_scc1 LCB%=\n"
    "s_ashr_i32 s45, s45, 3\n"
    "s_add_u32 s45, s45, s54\n"
    "s_sub_u32 s45, s45, 0x100000\n"
    "s_add_u32 s51, s51, 0x40000000\n"
    "s_add_u32 s52, s52, 4\n"
    "s_addc_u32 s53, s53, 0\n"
    "s_setpc_b64 s[52:53]\n"
    "LGR%=:\n"
    "s_mov_b32 s37, 0x80000\n"
    "LGL%=:\n"
    "s_mov_b32 s38, 0\n"
    "v_and_b32 v76, 1, v19\n"
    "v_cmp_ne_u32 vcc, s64, v76\n"
    "s_cbranch_vccz LGJ0_%=\n"
    "s_mov_b64 exec, vcc\n"
    "buffer_load_dwordx4 v[16:19], v0, s[16:19], s51 offen offset:0 sc1\n"
    "s_mov_b64 exec, -1\n"
    "s_mov_b32 s38, 1\n"
    "LGJ0_%=:\n"
    "v_and_b32 v76, 1, v23\n"
    "v_cmp_ne_u32 vcc, s64, v76\n"
    "s_cbranch_vccz LGJ1_%=\n"
    "s_mov_b64 exec, vcc\n"
    "buffer_load_dwordx4 v[20:23], v0, s[16:19], s51 offen offset:1024 sc1\n"
    "s_mov_b64 exec, -1\n"
    "s_mov_b32 s38, 1\n"
    "LGJ1_%=:\n"
    "v_and_b32 v76, 1, v27\n"
    "v_cmp_ne_u32 vcc, s64, v76\n"
    "s_cbranch_vccz LGJ2_%=\n"
    "s_mov_b64 exec, vcc\n"
    "buffer_load_dwordx4 v[24:27], v0, s[16:19], s51 offen offset:2048 sc1\n"
    "s_mov_b64 exec, -1\n"
    "s_mov_b32 s38, 1\n"
    "LGJ2_%=:\n"
    "v_and_b32 v76, 1, v31\n"
    "v_cmp_ne_u32 vcc, s64, v76\n"
    "s_cbranch_vccz LGJ3_%=\n"
    "s_mov_b64 exec, vcc\n"
    "buffer_load_dwordx4 v[28:31], v0, s[16:19], s51 offen offset:3072 sc1\n"
    "s_mov_b64 exec, -1\n"
    "s_mov_b32 s38, 1\n"
    "LGJ3_%=:\n"
    "v_and_b32 v76, 1, v35\n"
    "v_cmp_ne_u32 vcc, s64, v76\n"
    "s_cbranch_vccz LGJ4_%=\n"
    "s_mov_b64 exec, vcc\n"
    "buffer_load_dwordx4 v[32:35], v0, s[16:19], s52 offen offset:0 sc1\n"
    "s_mov_b64 exec, -1\n"
    "s_mov_b32 s38, 1\n"
    "LGJ4_%=:\n"
    "v_and_b32 v76, 1, v39\n"
    "v_cmp_ne_u32 vcc, s64, v76\n"
    "s_cbranch_vccz LGJ5_%=\n"
    "s_mov_b64 exec, vcc\n"
    "buffer_load_dwordx4 v[36:39], v0, s[16:19], s52 offen offset:1024 sc1\n"
    "s_mov_b64 exec, -1\n"
    "s_mov_b32 s38, 1\n"
    "LGJ5_%=:\n"
    "v_and_b32 v76, 1, v43\n"
    "v_cmp_ne_u32 vcc, s64, v76\n"
    "s_cbranch_vccz LGJ6_%=\n"
    "s_mov_b64 exec, vcc\n"
    "buffer_load_dwordx4 v[40:43], v0, s[16:19], s52 offen offset:2048 sc1\n"
    "s_mov_b64 exec, -1\n"
    "s_mov_b32 s38, 1\n"
    "LGJ6_%=:\n"
    "v_and_b32 v76, 1, v47\n"
    "v_cmp_ne_u32 vcc, s64, v76\n"
    "s_cbranch_vccz LGJ7_%=\n"
    "s_mov_b64 exec, vcc\n"
    "buffer_load_dwordx4 v[44:47], v0, s[16:19], s52 offen offset:3072 sc1\n"
    "s_mov_b64 exec, -1\n"
    "s_mov_b32 s38, 1\n"
    "LGJ7_%=:\n"
    "s_cmp_eq_u32 s38, 0\n"
    "s_cbranch_scc1 LGA%=\n"
    "s_waitcnt vmcnt(0)\n"
    "s_sleep 1\n"
    "s_sub_u32 s37, s37, 1\n"
    "s_cbranch_scc0 LGL%=\n"
    "s_branch LFAIL%=\n"
    "LFAIL%=:\n"
    "buffer_store_dword v1, off, s[16:19], s35 sc1\n"
    "s_endpgm\n"
    :
    : "{v0}"(hv0), "{v1}"(hv1), "{v2}"(hv2), "{v3}"(hv3), "{v4}"(hv4), "{v5}"(hv5), "{v6}"(hv6), "{v7}"(hv7), "{v8}"(hv8), "{v9}"(hv9), "{v10}"(hv10), "{v11}"(hv11), "{v12}"(hv12), "{v13}"(hv13), "{v14}"(hv14), "{v15}"(hv15), "{v16}"(hv16), "{v17}"(hv17), "{v18}"(hv18), "{v19}"(hv19), "{v20}"(hv20)
    : "v255", "a255", "s101", "vcc", "scc", "memory");
  }
  __builtin_unreachable();
}
#define MSLEEP2 8
#define MSLEEP 8
#define MEMFENCE() asm volatile("" ::: "memory")
__device__ __forceinline__ void t_role8(int w, int m, int i, unsigned c1, char* ws, const u16* W) {
  const int lane = lane_id(), g = lane >> 4, q = (lane >> 2) & 3, p = lane & 3;
  const unsigned me = w ? OOR : 0u, c2 = c1 ^ 0x2000u;
  const int h = i & 30, par = i & 1, ob = m * 524288 + h * 16384 + (m == 1 ? w * 2048 : w * 1024);
  const int pv[21] = {w, (int)Z_XN + par * 65536 + w * 16384, (int)Z_XN + w * 16384, ob, (int)c1, (int)nearof(Z_FT + h * 16384, c1) + m * 16, (int)nearof(Z_FT + h * 16384, c2) + m * 16,
                      (int)((nearof(Z_DEC, c1) + m * 512 + par * 4) | me), (int)((nearof(Z_DEC, c1) + 2048 + m * 512) | me), (int)nearof(Z_XNP, c2), (int)Z_TOKP + ob, (int)(nearof(Z_FO + 32768, c2) + h * 256 - 32u), (int)Z_ERR, m, par,
                      (int)(size_t)W, (int)((size_t)W >> 32) & 0xffff, (int)(size_t)ws, (int)((size_t)ws >> 32) & 0xffff, (int)ZWS_BYTES, (int)((Z_DBG + MBY(1) + (m * 32 + i) * 4096) | (w == 2 ? 0u : OOR))};
  int hv0 = lane * 16, hv1 = (lane & 15) * 1056 + g * 16, hv2 = 0;
  int hv3 = (w * 128 + g) * 8192 + h * 256 + (lane & 15) * 16, hv4 = (8 * g + q) * 256 + 64 * w + 8 * p, hv5 = 0;
#pragma unroll
  for (int j = 0; j < 21; ++j) hv5 = lane == j ? pv[j] : hv5;
  asm volatile(
    "s_nop 1\n"
    "v_readlane_b32 s36, v5, 0\n"
    "v_readlane_b32 s45, v5, 1\n"
    "v_readlane_b32 s46, v5, 2\n"
    "v_readlane_b32 s47, v5, 3\n"
    "v_readlane_b32 s48, v5, 4\n"
    "v_readlane_b32 s50, v5, 5\n"
    "v_readlane_b32 s51, v5, 6\n"
    "v_readlane_b32 s53, v5, 7\n"
    "v_readlane_b32 s54, v5, 8\n"
    "v_readlane_b32 s55, v5, 9\n"
    "v_readlane_b32 s56, v5, 10\n"
    "v_readlane_b32 s57, v5, 11\n"
    "v_readlane_b32 s58, v5, 12\n"
    "v_readlane_b32 s61, v5, 13\n"
    "v_readlane_b32 s40, v5, 14\n"
    "v_readlane_b32 s20, v5, 15\n"
    "v_readlane_b32 s21, v5, 16\n"
    "v_readlane_b32 s16, v5, 17\n"
    "v_readlane_b32 s17, v5, 18\n"
    "v_readlane_b32 s18, v5, 19\n"
    "v_readlane_b32 s65, v5, 20\n"
    "v_mov_b32 v5, 1\n"
    "s_mov_b32 s19, 0x20000\n"
    "s_mov_b32 s22, 0x400000\n"
    "s_mov_b32 s23, 0x20000\n"
    "s_xor_b32 s49, s48, 0x2000\n"
    "s_mul_i32 s44, s36, 8448\n"
    "s_lshl_b32 s39, s36, 15\n"
    "s_mov_b32 s43, 0\n"
    "s_mov_b32 s41, s40\n"
    "s_mov_b32 s42, s40\n"
    "s_mov_b32 s52, 0x80000000\n"
    "s_cmp_eq_u32 s36, 1\n"
    "s_cselect_b32 s68, 0, 0x80000000\n"
    "s_add_u32 m0, s39, 0\n"
    "s_mov_b32 s38, 0\n"
    "buffer_load_dwordx4 v3, s[20:23], s38 offen lds\n"
    "s_add_u32 m0, s39, 1024\n"
    "s_mov_b32 s38, 32768\n"
    "buffer_load_dwordx4 v3, s[20:23], s38 offen lds\n"
    "s_add_u32 m0, s39, 2048\n"
    "s_mov_b32 s38, 65536\n"
    "buffer_load_dwordx4 v3, s[20:23], s38 offen lds\n"
    "s_add_u32 m0, s39, 3072\n"
    "s_mov_b32 s38, 98304\n"
    "buffer_load_dwordx4 v3, s[20:23], s38 offen lds\n"
    "s_add_u32 m0, s39, 4096\n"
    "s_mov_b32 s38, 131072\n"
    "buffer_load_dwordx4 v3, s[20:23], s38 offen lds\n"
    "s_add_u32 m0, s39, 5120\n"
    "s_mov_b32 s38, 163840\n"
    "buffer_load_dwordx4 v3, s[20:23], s38 offen lds\n"
    "s_add_u32 m0, s39, 6144\n"
    "s_mov_b32 s38, 196608\n"
    "buffer_load_dwordx4 v3, s[20:23], s38 offen lds\n"
    "s_add_u32 m0, s39, 7168\n"
    "s_mov_b32 s38, 229376\n"
    "buffer_load_dwordx4 v3, s[20:23], s38 offen lds\n"
    "s_add_u32 m0, s39, 8192\n"
    "s_mov_b32 s38, 262144\n"
    "buffer_load_dwordx4 v3, s[20:23], s38 offen lds\n"
    "s_add_u32 m0, s39, 9216\n"
    "s_mov_b32 s38, 294912\n"
    "buffer_load_dwordx4 v3, s[20:23], s38 offen lds\n"
    "s_add_u32 m0, s39, 10240\n"
    "s_mov_b32 s38, 327680\n"
    "buffer_load_dwordx4 v3, s[20:23], s38 offen lds\n"
    "s_add_u32 m0, s39, 11264\n"
    "s_mov_b32 s38, 360448\n"
    "buffer_load_dwordx4 v3, s[20:23], s38 offen lds\n"
    "s_add_u32 m0, s39, 12288\n"
    "s_mov_b32 s38, 393216\n"
    "buffer_load_dwordx4 v3, s[20:23], s38 offen lds\n"
    "s_add_u32 m0, s39, 13312\n"
    "s_mov_b32 s38, 425984\n"
    "buffer_load_dwordx4 v3, s[20:23], s38 offen lds\n"
    "s_add_u32 m0, s39, 14336\n"
    "s_mov_b32 s38, 458752\n"
    "buffer_load_dwordx4 v3, s[20:23], s38 offen lds\n"
    "s_add_u32 m0, s39, 15360\n"
    "s_mov_b32 s38, 491520\n"
    "buffer_load_dwordx4 v3, s[20:23], s38 offen lds\n"
    "s_add_u32 m0, s39, 16384\n"
    "s_mov_b32 s38, 524288\n"
    "buffer_load_dwordx4 v3, s[20:23], s38 offen lds\n"
    "s_add_u32 m0, s39, 17408\n"
    "s_mov_b32 s38, 557056\n"
    "buffer_load_dwordx4 v3, s[20:23], s38 offen lds\n"
    "s_add_u32 m0, s39, 18432\n"
    "s_mov_b32 s38, 589824\n"
    "buffer_load_dwordx4 v3, s[20:23], s38 offen lds\n"
    "s_add_u32 m0, s39, 19456\n"
    "s_mov_b32 s38, 622592\n"
    "buffer_load_dwordx4 v3, s[20:23], s38 offen lds\n"
    "s_add_u32 m0, s39, 20480\n"
    "s_mov_b32 s38, 655360\n"
    "buffer_load_dwordx4 v3, s[20:23], s38 offen lds\n"
    "s_add_u32 m0, s39, 21504\n"
    "s_mov_b32 s38, 688128\n"
    "buffer_load_dwordx4 v3, s[20:23], s38 offen lds\n"
    "s_add_u32 m0, s39, 22528\n"
    "s_mov_b32 s38, 720896\n"
    "buffer_load_dwordx4 v3, s[20:23], s38 offen lds\n"
    "s_add_u32 m0, s39, 23552\n"
    "s_mov_b32 s38, 753664\n"
    "buffer_load_dwordx4 v3, s[20:23], s38 offen lds\n"
    "s_add_u32 m0, s39, 24576\n"
    "s_mov_b32 s38, 786432\n"
    "buffer_load_dwordx4 v3, s[20:23], s38 offen lds\n"
    "s_add_u32 m0, s39, 25600\n"
    "s_mov_b32 s38, 819200\n"
    "buffer_load_dwordx4 v3, s[20:23], s38 offen lds\n"
    "s_add_u32 m0, s39, 26624\n"
    "s_mov_b32 s38, 851968\n"
    "buffer_load_dwordx4 v3, s[20:23], s38 offen lds\n"
    "s_add_u32 m0, s39, 27648\n"
    "s_mov_b32 s38, 884736\n"
    "buffer_load_dwordx4 v3, s[20:23], s38 offen lds\n"
    "s_add_u32 m0, s39, 28672\n"
    "s_mov_b32 s38, 917504\n"
    "buffer_load_dwordx4 v3, s[20:23], s38 offen lds\n"
    "s_add_u32 m0, s39, 29696\n"
    "s_mov_b32 s38, 950272\n"
    "buffer_load_dwordx4 v3, s[20:23], s38 offen lds\n"
    "s_add_u32 m0, s39, 30720\n"
    "s_mov_b32 s38, 983040\n"
    "buffer_load_dwordx4 v3, s[20:23], s38 offen lds\n"
    "s_add_u32 m0, s39, 31744\n"
    "s_mov_b32 s38, 1015808\n"
    "buffer_load_dwordx4 v3, s[20:23], s38 offen lds\n"
    "s_waitcnt vmcnt(0)\n"
    "s_barrier\n"
    "v_add_u32 v8, 0, v4\n"
    "ds_read_b64_tr_b16 a[0:1], v8 offset:0\n"
    "ds_read_b64_tr_b16 a[2:3], v8 offset:1024\n"
    "ds_read_b64_tr_b16 a[4:5], v8 offset:32\n"
    "ds_read_b64_tr_b16 a[6:7], v8 offset:1056\n"
    "ds_read_b64_tr_b16 a[8:9], v8 offset:8192\n"
    "ds_read_b64_tr_b16 a[10:11], v8 offset:9216\n"
    "ds_read_b64_tr_b16 a[12:13], v8 offset:8224\n"
    "ds_read_b64_tr_b16 a[14:15], v8 offset:9248\n"
    "ds_read_b64_tr_b16 a[16:17], v8 offset:16384\n"
    "ds_read_b64_tr_b16 a[18:19], v8 offset:17408\n"
    "ds_read_b64_tr_b16 a[20:21], v8 offset:16416\n"
    "ds_read_b64_tr_b16 a[22:23], v8 offset:17440\n"
    "ds_read_b64_tr_b16 a[24:25], v8 offset:24576\n"
    "ds_read_b64_tr_b16 a[26:27], v8 offset:25600\n"
    "ds_read_b64_tr_b16 a[28:29], v8 offset:24608\n"
    "s_waitcnt lgkmcnt(11)\n"
    "ds_read_b64_tr_b16 a[30:31], v8 offset:25632\n"
    "ds_read_b64_tr_b16 a[32:33], v8 offset:32768\n"
    "ds_read_b64_tr_b16 a[34:35], v8 offset:33792\n"
    "ds_read_b64_tr_b16 a[36:37], v8 offset:32800\n"
    "s_waitcnt lgkmcnt(11)\n"
    "ds_read_b64_tr_b16 a[38:39], v8 offset:33824\n"
    "ds_read_b64_tr_b16 a[40:41], v8 offset:40960\n"
    "ds_read_b64_tr_b16 a[42:43], v8 offset:41984\n"
    "ds_read_b64_tr_b16 a[44:45], v8 offset:40992\n"
    "s_waitcnt lgkmcnt(11)\n"
    "ds_read_b64_tr_b16 a[46:47], v8 offset:42016\n"
    "ds_read_b64_tr_b16 a[48:49], v8 offset:49152\n"
    "ds_read_b64_tr_b16 a[50:51], v8 offset:50176\n"
    "ds_read_b64_tr_b16 a[52:53], v8 offset:49184\n"
    "s_waitcnt lgkmcnt(11)\n"
    "ds_read_b64_tr_b16 a[54:55], v8 offset:50208\n"
    "ds_read_b64_tr_b16 a[56:57], v8 offset:57344\n"
    "ds_read_b64_tr_b16 a[58:59], v8 offset:58368\n"
    "ds_read_b64_tr_b16 a[60:61], v8 offset:57376\n"
    "s_waitcnt lgkmcnt(11)\n"
    "ds_read_b64_tr_b16 a[62:63], v8 offset:58400\n"
    "s_waitcnt lgkmcnt(0)\n"
    "v_add_u32 v8, 65536, v4\n"
    "ds_read_b64_tr_b16 a[64:65], v8 offset:0\n"
    "ds_read_b64_tr_b16 a[66:67], v8 offset:1024\n"
    "ds_read_b64_tr_b16 a[68:69], v8 offset:32\n"
    "ds_read_b64_tr_b16 a[70:71], v8 offset:1056\n"
    "ds_read_b64_tr_b16 a[72:73], v8 offset:8192\n"
    "ds_read_b64_tr_b16 a[74:75], v8 offset:9216\n"
    "ds_read_b64_tr_b16 a[76:77], v8 offset:8224\n"
    "ds_read_b64_tr_b16 a[78:79], v8 offset:9248\n"
    "ds_read_b64_tr_b16 a[80:81], v8 offset:16384\n"
    "ds_read_b64_tr_b16 a[82:83], v8 offset:17408\n"
    "ds_read_b64_tr_b16 a[84:85], v8 offset:16416\n"
    "ds_read_b64_tr_b16 a[86:87], v8 offset:17440\n"
    "ds_read_b64_tr_b16 a[88:89], v8 offset:24576\n"
    "ds_read_b64_tr_b16 a[90:91], v8 offset:25600\n"
    "ds_read_b64_tr_b16 a[92:93], v8 offset:24608\n"
    "s_waitcnt lgkmcnt(11)\n"
    "ds_read_b64_tr_b16 a[94:95], v8 offset:25632\n"
    "ds_read_b64_tr_b16 a[96:97], v8 offset:32768\n"
    "ds_read_b64_tr_b16 a[98:99], v8 offset:33792\n"
    "ds_read_b64_tr_b16 a[100:101], v8 offset:32800\n"
    "s_waitcnt lgkmcnt(11)\n"
    "ds_read_b64_tr_b16 a[102:103], v8 offset:33824\n"
    "ds_read_b64_tr_b16 a[104:105], v8 offset:40960\n"
    "ds_read_b64_tr_b16 a[106:107], v8 offset:41984\n"
    "ds_read_b64_tr_b16 a[108:109], v8 offset:40992\n"
    "s_waitcnt lgkmcnt(11)\n"
    "ds_read_b64_tr_b16 a[110:111], v8 offset:42016\n"
    "ds_read_b64_tr_b16 a[112:113], v8 offset:49152\n"
    "ds_read_b64_tr_b16 a[114:115], v8 offset:50176\n"
    "ds_read_b64_tr_b16 a[116:117], v8 offset:49184\n"
    "s_waitcnt lgkmcnt(11)\n"
    "ds_read_b64_tr_b16 a[118:119], v8 offset:50208\n"
    "ds_read_b64_tr_b16 a[120:121], v8 offset:57344\n"
    "ds_read_b64_tr_b16 a[122:123], v8 offset:58368\n"
    "ds_read_b64_tr_b16 a[124:125], v8 offset:57376\n"
    "s_waitcnt lgkmcnt(11)\n"
    "ds_read_b64_tr_b16 a[126:127], v8 offset:58400\n"
    "s_waitcnt lgkmcnt(0)\n"
    "s_barrier\n"
    "s_add_u32 m0, s39, 0\n"
    "s_mov_b32 s38, 256\n"
    "buffer_load_dwordx4 v3, s[20:23], s38 offen lds\n"
    "s_add_u32 m0, s39, 1024\n"
    "s_mov_b32 s38, 33024\n"
    "buffer_load_dwordx4 v3, s[20:23], s38 offen lds\n"
    "s_add_u32 m0, s39, 2048\n"
    "s_mov_b32 s38, 65792\n"
    "buffer_load_dwordx4 v3, s[20:23], s38 offen lds\n"
    "s_add_u32 m0, s39, 3072\n"
    "s_mov_b32 s38, 98560\n"
    "buffer_load_dwordx4 v3, s[20:23], s38 offen lds\n"
    "s_add_u32 m0, s39, 4096\n"
    "s_mov_b32 s38, 131328\n"
    "buffer_load_dwordx4 v3, s[20:23], s38 offen lds\n"
    "s_add_u32 m0, s39, 5120\n"
    "s_mov_b32 s38, 164096\n"
    "buffer_load_dwordx4 v3, s[20:23], s38 offen lds\n"
    "s_add_u32 m0, s39, 6144\n"
    "s_mov_b32 s38, 196864\n"
    "buffer_load_dwordx4 v3, s[20:23], s38 offen lds\n"
    "s_add_u32 m0, s39, 7168\n"
    "s_mov_b32 s38, 229632\n"
    "buffer_load_dwordx4 v3, s[20:23], s38 offen lds\n"
    "s_add_u32 m0, s39, 8192\n"
    "s_mov_b32 s38, 262400\n"
    "buffer_load_dwordx4 v3, s[20:23], s38 offen lds\n"
    "s_add_u32 m0, s39, 9216\n"
    "s_mov_b32 s38, 295168\n"
    "buffer_load_dwordx4 v3, s[20:23], s38 offen lds\n"
    "s_add_u32 m0, s39, 10240\n"
    "s_mov_b32 s38, 327936\n"
    "buffer_load_dwordx4 v3, s[20:23], s38 offen lds\n"
    "s_add_u32 m0, s39, 11264\n"
    "s_mov_b32 s38, 360704\n"
    "buffer_load_dwordx4 v3, s[20:23], s38 offen lds\n"
    "s_add_u32 m0, s39, 12288\n"
    "s_mov_b32 s38, 393472\n"
    "buffer_load_dwordx4 v3, s[20:23], s38 offen lds\n"
    "s_add_u32 m0, s39, 13312\n"
    "s_mov_b32 s38, 426240\n"
    "buffer_load_dwordx4 v3, s[20:23], s38 offen lds\n"
    "s_add_u32 m0, s39, 14336\n"
    "s_mov_b32 s38, 459008\n"
    "buffer_load_dwordx4 v3, s[20:23], s38 offen lds\n"
    "s_add_u32 m0, s39, 15360\n"
    "s_mov_b32 s38, 491776\n"
    "buffer_load_dwordx4 v3, s[20:23], s38 offen lds\n"
    "s_add_u32 m0, s39, 16384\n"
    "s_mov_b32 s38, 524544\n"
    "buffer_load_dwordx4 v3, s[20:23], s38 offen lds\n"
    "s_add_u32 m0, s39, 17408\n"
    "s_mov_b32 s38, 557312\n"
    "buffer_load_dwordx4 v3, s[20:23], s38 offen lds\n"
    "s_add_u32 m0, s39, 18432\n"
    "s_mov_b32 s38, 590080\n"
    "buffer_load_dwordx4 v3, s[20:23], s38 offen lds\n"
    "s_add_u32 m0, s39, 19456\n"
    "s_mov_b32 s38, 622848\n"
    "buffer_load_dwordx4 v3, s[20:23], s38 offen lds\n"
    "s_add_u32 m0, s39, 20480\n"
    "s_mov_b32 s38, 655616\n"
    "buffer_load_dwordx4 v3, s[20:23], s38 offen lds\n"
    "s_add_u32 m0, s39, 21504\n"
    "s_mov_b32 s38, 688384\n"
    "buffer_load_dwordx4 v3, s[20:23], s38 offen lds\n"
    "s_add_u32 m0, s39, 22528\n"
    "s_mov_b32 s38, 721152\n"
    "buffer_load_dwordx4 v3, s[20:23], s38 offen lds\n"
    "s_add_u32 m0, s39, 23552\n"
    "s_mov_b32 s38, 753920\n"
    "buffer_load_dwordx4 v3, s[20:23], s38 offen lds\n"
    "s_add_u32 m0, s39, 24576\n"
    "s_mov_b32 s38, 786688\n"
    "buffer_load_dwordx4 v3, s[20:23], s38 offen lds\n"
    "s_add_u32 m0, s39, 25600\n"
    "s_mov_b32 s38, 819456\n"
    "buffer_load_dwordx4 v3, s[20:23], s38 offen lds\n"
    "s_add_u32 m0, s39, 26624\n"
    "s_mov_b32 s38, 852224\n"
    "buffer_load_dwordx4 v3, s[20:23], s38 offen lds\n"
    "s_add_u32 m0, s39, 27648\n"
    "s_mov_b32 s38, 884992\n"
    "buffer_load_dwordx4 v3, s[20:23], s38 offen lds\n"
    "s_add_u32 m0, s39, 28672\n"
    "s_mov_b32 s38, 917760\n"
    "buffer_load_dwordx4 v3, s[20:23], s38 offen lds\n"
    "s_add_u32 m0, s39, 29696\n"
    "s_mov_b32 s38, 950528\n"
    "buffer_load_dwordx4 v3, s[20:23], s38 offen lds\n"
    "s_add_u32 m0, s39, 30720\n"
    "s_mov_b32 s38, 983296\n"
    "buffer_load_dwordx4 v3, s[20:23], s38 offen lds\n"
    "s_add_u32 m0, s39, 31744\n"
    "s_mov_b32 s38, 1016064\n"
    "buffer_load_dwordx4 v3, s[20:23], s38 offen lds\n"
    "s_waitcnt vmcnt(0)\n"
    "s_barrier\n"
    "v_add_u32 v8, 0, v4\n"
    "ds_read_b64_tr_b16 a[128:129], v8 offset:0\n"
    "ds_read_b64_tr_b16 a[130:131], v8 offset:1024\n"
    "ds_read_b64_tr_b16 a[132:133], v8 offset:32\n"
    "ds_read_b64_tr_b16 a[134:135], v8 offset:1056\n"
    "ds_read_b64_tr_b16 a[136:137], v8 offset:8192\n"
    "ds_read_b64_tr_b16 a[138:139], v8 offset:9216\n"
    "ds_read_b64_tr_b16 a[140:141], v8 offset:8224\n"
    "ds_read_b64_tr_b16 a[142:143], v8 offset:9248\n"
    "ds_read_b64_tr_b16 a[144:145], v8 offset:16384\n"
    "ds_read_b64_tr_b16 a[146:147], v8 offset:17408\n"
    "ds_read_b64_tr_b16 a[148:149], v8 offset:16416\n"
    "ds_read_b64_tr_b16 a[150:151], v8 offset:17440\n"
    "ds_read_b64_tr_b16 a[152:153], v8 offset:24576\n"
    "ds_read_b64_tr_b16 a[154:155], v8 offset:25600\n"
    "ds_read_b64_tr_b16 a[156:157], v8 offset:24608\n"
    "s_waitcnt lgkmcnt(11)\n"
    "ds_read_b64_tr_b16 a[158:159], v8 offset:25632\n"
    "ds_read_b64_tr_b16 a[160:161], v8 offset:32768\n"
    "ds_read_b64_tr_b16 a[162:163], v8 offset:33792\n"
    "ds_read_b64_tr_b16 a[164:165], v8 offset:32800\n"
    "s_waitcnt lgkmcnt(11)\n"
    "ds_read_b64_tr_b16 a[166:167], v8 offset:33824\n"
    "ds_read_b64_tr_b16 a[168:169], v8 offset:40960\n"
    "ds_read_b64_tr_b16 a[170:171], v8 offset:41984\n"
    "ds_read_b64_tr_b16 a[172:173], v8 offset:40992\n"
    "s_waitcnt lgkmcnt(11)\n"
    "ds_read_b64_tr_b16 a[174:175], v8 offset:42016\n"
    "ds_read_b64_tr_b16 a[176:177], v8 offset:49152\n"
    "ds_read_b64_tr_b16 a[178:179], v8 offset:50176\n"
    "ds_read_b64_tr_b16 a[180:181], v8 offset:49184\n"
    "s_waitcnt lgkmcnt(11)\n"
    "ds_read_b64_tr_b16 a[182:183], v8 offset:50208\n"
    "ds_read_b64_tr_b16 a[184:185], v8 offset:57344\n"
    "ds_read_b64_tr_b16 a[186:187], v8 offset:58368\n"
    "ds_read_b64_tr_b16 a[188:189], v8 offset:57376\n"
    "s_waitcnt lgkmcnt(11)\n"
    "ds_read_b64_tr_b16 a[190:191], v8 offset:58400\n"
    "s_waitcnt lgkmcnt(0)\n"
    "v_add_u32 v8, 65536, v4\n"
    "ds_read_b64_tr_b16 a[192:193], v8 offset:0\n"
    "ds_read_b64_tr_b16 a[194:195], v8 offset:1024\n"
    "ds_read_b64_tr_b16 a[196:197], v8 offset:32\n"
    "ds_read_b64_tr_b16 a[198:199], v8 offset:1056\n"
    "ds_read_b64_tr_b16 a[200:201], v8 offset:8192\n"
    "ds_read_b64_tr_b16 a[202:203], v8 offset:9216\n"
    "ds_read_b64_tr_b16 a[204:205], v8 offset:8224\n"
    "ds_read_b64_tr_b16 a[206:207], v8 offset:9248\n"
    "ds_read_b64_tr_b16 a[208:209], v8 offset:16384\n"
    "ds_read_b64_tr_b16 a[210:211], v8 offset:17408\n"
    "ds_read_b64_tr_b16 a[212:213], v8 offset:16416\n"
    "ds_read_b64_tr_b16 a[214:215], v8 offset:17440\n"
    "ds_read_b64_tr_b16 a[216:217], v8 offset:24576\n"
    "ds_read_b64_tr_b16 a[218:219], v8 offset:25600\n"
    "ds_read_b64_tr_b16 a[220:221], v8 offset:24608\n"
    "s_waitcnt lgkmcnt(11)\n"
    "ds_read_b64_tr_b16 a[222:223], v8 offset:25632\n"
    "ds_read_b64_tr_b16 a[224:225], v8 offset:32768\n"
    "ds_read_b64_tr_b16 a[226:227], v8 offset:33792\n"
    "ds_read_b64_tr_b16 a[228:229], v8 offset:32800\n"
    "s_waitcnt lgkmcnt(11)\n"
    "ds_read_b64_tr_b16 a[230:231], v8 offset:33824\n"
    "ds_read_b64_tr_b16 a[232:233], v8 offset:40960\n"
    "ds_read_b64_tr_b16 a[234:235], v8 offset:41984\n"
    "ds_read_b64_tr_b16 a[236:237], v8 offset:40992\n"
    "s_waitcnt lgkmcnt(11)\n"
    "ds_read_b64_tr_b16 a[238:239], v8 offset:42016\n"
    "ds_read_b64_tr_b16 a[240:241], v8 offset:49152\n"
    "ds_read_b64_tr_b16 a[242:243], v8 offset:50176\n"
    "ds_read_b64_tr_b16 a[244:245], v8 offset:49184\n"
    "s_waitcnt lgkmcnt(11)\n"
    "ds_read_b64_tr_b16 a[246:247], v8 offset:50208\n"
    "ds_read_b64_tr_b16 a[248:249], v8 offset:57344\n"
    "ds_read_b64_tr_b16 a[250:251], v8 offset:58368\n"
    "ds_read_b64_tr_b16 a[252:253], v8 offset:57376\n"
    "s_waitcnt lgkmcnt(11)\n"
    "ds_read_b64_tr_b16 a[254:255], v8 offset:58400\n"
    "s_waitcnt lgkmcnt(0)\n"
    "s_barrier\n"
    "v_bfe_u32 v9, v0, 4, 1\n"
    "v_lshlrev_b32 v4, 8, v9\n"
    "v_lshlrev_b32 v9, 14, v9\n"
    "s_mov_b32 s63, s45\n"
    "s_lshr_b32 s38, s63, 8\n"
    "s_xor_b32 s38, s38, s48\n"
    "s_and_b32 s38, s38, 0x2000\n"
    "s_or_b32 s63, s63, s38\n"
    "s_add_u32 s39, s44, s43\n"
    "s_add_u32 s69, s63, 4096\n"
    "s_add_u32 m0, s39, 0\n"
    "s_nop 0\n"
    "buffer_load_dwordx4 v0, s[16:19], s63 offen offset:0 sc1 lds\n"
    "s_add_u32 m0, s39, 32\n"
    "s_nop 0\n"
    "buffer_load_dwordx4 v0, s[16:19], s63 offen offset:1024 sc1 lds\n"
    "s_add_u32 m0, s39, 64\n"
    "s_nop 0\n"
    "buffer_load_dwordx4 v0, s[16:19], s63 offen offset:2048 sc1 lds\n"
    "s_add_u32 m0, s39, 96\n"
    "s_nop 0\n"
    "buffer_load_dwordx4 v0, s[16:19], s63 offen offset:3072 sc1 lds\n"
    "s_add_u32 m0, s39, 4224\n"
    "s_nop 0\n"
    "buffer_load_dwordx4 v0, s[16:19], s69 offen offset:0 sc1 lds\n"
    "s_add_u32 m0, s39, 4256\n"
    "s_nop 0\n"
    "buffer_load_dwordx4 v0, s[16:19], s69 offen offset:1024 sc1 lds\n"
    "s_add_u32 m0, s39, 4288\n"
    "s_nop 0\n"
    "buffer_load_dwordx4 v0, s[16:19], s69 offen offset:2048 sc1 lds\n"
    "s_add_u32 m0, s39, 4320\n"
    "s_nop 0\n"
    "buffer_load_dwordx4 v0, s[16:19], s69 offen offset:3072 sc1 lds\n"
    "s_add_u32 s45, s45, 131072\n"
    "v_mov_b32 v10, 101376\n"
    "v_lshl_add_u32 v11, s36, 3, v10\n"
    "LT%=:\n"
    "s_cmp_lg_u32 s36, 0\n"
    "s_cbranch_scc1 LB%=\n"
    "s_mov_b32 s37, 0x80000\n"
    "LP%=:\n"
    "buffer_load_dword v6, off, s[16:19], s54 sc1\n"
    "buffer_load_dword v7, off, s[16:19], s53 sc1\n"
    "s_waitcnt vmcnt(0)\n"
    "v_readfirstlane_b32 s60, v6\n"
    "v_cmp_eq_u32 vcc, 0, v7\n"
    "s_cmp_gt_u32 s60, 3\n"
    "s_cbranch_scc1 LD%=\n"
    "s_cmp_ge_u32 s41, 64\n"
    "s_cselect_b32 s60, s60, 0\n"
    "s_cselect_b32 vcc_hi, 1, 0\n"
    "s_cmp_lg_u32 s60, 0\n"
    "s_cbranch_scc1 LD%=\n"
    "s_mov_b32 s60, 2\n"
    "s_or_b32 s38, vcc_lo, vcc_hi\n"
    "s_cbranch_scc0 LD%=\n"
    "s_add_u32 s38, s41, 8\n"
    "s_cmp_lt_u32 s41, 2\n"
    "s_cselect_b32 s38, 12, s38\n"
    "s_min_u32 s38, s38, 64\n"
    "s_mov_b32 s60, 1\n"
    "s_cmp_lt_u32 s40, s38\n"
    "s_cbranch_scc1 LD%=\n"
    "s_mov_b32 s60, 0\n"
    "s_cmp_lt_u32 s52, 0x80000000\n"
    "s_cbranch_scc1 LD%=\n"
    "s_sleep 1\n"
    "s_sub_u32 s37, s37, 1\n"
    "s_cbranch_scc0 LP%=\n"
    "buffer_store_dword v5, off, s[16:19], s58 sc1\n"
    "s_mov_b32 s60, 3\n"
    "LD%=:\n"
    "v_mov_b32 v6, s60\n"
    "ds_write_b64 v10, v[6:7]\n"
    "LB%=:\n"
    "s_waitcnt vmcnt(0) lgkmcnt(0)\n"
    "s_barrier\n"
    "ds_read_b64 v[6:7], v10\n"
    "s_or_b32 s52, s52, s68\n"
    "buffer_store_dword v5, v9, s[16:19], s52 offen sc1\n"
    "s_mov_b32 s52, 0x80000000\n"
    "s_lshl_b32 s63, s41, 16\n"
    "s_add_u32 s63, s63, s46\n"
    "s_lshr_b32 s38, s63, 8\n"
    "s_xor_b32 s38, s38, s49\n"
    "s_and_b32 s38, s38, 0x2000\n"
    "s_or_b32 s63, s63, s38\n"
    "s_waitcnt lgkmcnt(0)\n"
    "v_readfirstlane_b32 s60, v6\n"
    "v_readfirstlane_b32 s66, v7\n"
    "s_cmp_eq_u32 s60, 1\n"
    "s_cbranch_scc1 L1%=\n"
    "s_cmp_eq_u32 s60, 2\n"
    "s_cbranch_scc0 LX%=\n"
    "s_add_u32 s39, s44, 67584\n"
    "s_add_u32 s69, s63, 4096\n"
    "s_add_u32 m0, s39, 0\n"
    "s_nop 0\n"
    "buffer_load_dwordx4 v0, s[16:19], s63 offen offset:0 sc1 lds\n"
    "s_add_u32 m0, s39, 32\n"
    "s_nop 0\n"
    "buffer_load_dwordx4 v0, s[16:19], s63 offen offset:1024 sc1 lds\n"
    "s_add_u32 m0, s39, 64\n"
    "s_nop 0\n"
    "buffer_load_dwordx4 v0, s[16:19], s63 offen offset:2048 sc1 lds\n"
    "s_add_u32 m0, s39, 96\n"
    "s_nop 0\n"
    "buffer_load_dwordx4 v0, s[16:19], s63 offen offset:3072 sc1 lds\n"
    "s_add_u32 m0, s39, 4224\n"
    "s_nop 0\n"
    "buffer_load_dwordx4 v0, s[16:19], s69 offen offset:0 sc1 lds\n"
    "s_add_u32 m0, s39, 4256\n"
    "s_nop 0\n"
    "buffer_load_dwordx4 v0, s[16:19], s69 offen offset:1024 sc1 lds\n"
    "s_add_u32 m0, s39, 4288\n"
    "s_nop 0\n"
    "buffer_load_dwordx4 v0, s[16:19], s69 offen offset:2048 sc1 lds\n"
    "s_add_u32 m0, s39, 4320\n"
    "s_nop 0\n"
    "buffer_load_dwordx4 v0, s[16:19], s69 offen offset:3072 sc1 lds\n"
    "s_waitcnt vmcnt(0)\n"
    "L2%=:\n"
    "s_mul_i32 s59, s41, 2621440\n"
    "s_add_u32 s59, s59, s47\n"
    "s_lshr_b32 s38, s59, 8\n"
    "s_xor_b32 s38, s38, s49\n"
    "s_and_b32 s38, s38, 0x2000\n"
    "s_or_b32 s59, s59, s38\n"
    "s_lshl_b32 s64, s41, 7\n"
    "s_add_u32 s64, s64, s51\n"
    "s_mov_b32 s67, s66\n"
    "s_add_u32 s41, s41, 2\n"
    "s_add_u32 s53, s53, 8\n"
    "s_mov_b32 s60, 2\n"
    "LG%=:\n"
    "v_add_u32 v8, 67584, v1\n"
    "s_waitcnt vmcnt(2)\n"
    "s_barrier\n"
    "s_cmp_eq_u32 s61, 1\n"
    "s_cbranch_scc1 LHV%=\n"
    "s_add_u32 s62, s59, 4096\n"
    "ds_read_b128 v[20:23], v8 offset:0\n"
    "ds_read_b128 v[24:27], v8 offset:16896\n"
    "ds_read_b128 v[28:31], v8 offset:64\n"
    "ds_read_b128 v[32:35], v8 offset:16960\n"
    "ds_read_b128 v[36:39], v8 offset:128\n"
    "ds_read_b128 v[40:43], v8 offset:17024\n"
    "ds_read_b128 v[44:47], v8 offset:192\n"
    "ds_read_b128 v[48:51], v8 offset:17088\n"
    "s_waitcnt lgkmcnt(7)\n"
    "v_mfma_f32_16x16x32_bf16 v[64:67], a[0:3], v[20:23], 0\n"
    "ds_read_b128 v[52:55], v8 offset:256\n"
    "s_waitcnt lgkmcnt(7)\n"
    "v_mfma_f32_16x16x32_bf16 v[68:71], a[0:3], v[24:27], 0\n"
    "ds_read_b128 v[56:59], v8 offset:17152\n"
    "v_mfma_f32_16x16x32_bf16 v[76:79], a[4:7], v[24:27], 0\n"
    "v_mfma_f32_16x16x32_bf16 v[72:75], a[4:7], v[20:23], 0\n"
    "s_waitcnt lgkmcnt(7)\n"
    "v_mfma_f32_16x16x32_bf16 v[64:67], a[8:11], v[28:31], v[64:67]\n"
    "ds_read_b128 v[20:23], v8 offset:320\n"
    "s_waitcnt lgkmcnt(7)\n"
    "v_mfma_f32_16x16x32_bf16 v[68:71], a[8:11], v[32:35], v[68:71]\n"
    "ds_read_b128 v[24:27], v8 offset:17216\n"
    "v_mfma_f32_16x16x32_bf16 v[76:79], a[12:15], v[32:35], v[76:79]\n"
    "v_mfma_f32_16x16x32_bf16 v[72:75], a[12:15], v[28:31], v[72:75]\n"
    "s_waitcnt lgkmcnt(7)\n"
    "v_mfma_f32_16x16x32_bf16 v[64:67], a[16:19], v[36:39], v[64:67]\n"
    "ds_read_b128 v[28:31], v8 offset:384\n"
    "s_waitcnt lgkmcnt(7)\n"
    "v_mfma_f32_16x16x32_bf16 v[68:71], a[16:19], v[40:43], v[68:71]\n"
    "ds_read_b128 v[32:35], v8 offset:17280\n"
    "v_mfma_f32_16x16x32_bf16 v[76:79], a[20:23], v[40:43], v[76:79]\n"
    "v_mfma_f32_16x16x32_bf16 v[72:75], a[20:23], v[36:39], v[72:75]\n"
    "s_waitcnt lgkmcnt(7)\n"
    "v_mfma_f32_16x16x32_bf16 v[64:67], a[24:27], v[44:47], v[64:67]\n"
    "ds_read_b128 v[36:39], v8 offset:448\n"
    "s_waitcnt lgkmcnt(7)\n"
    "v_mfma_f32_16x16x32_bf16 v[68:71], a[24:27], v[48:51], v[68:71]\n"
    "ds_read_b128 v[40:43], v8 offset:17344\n"
    "v_mfma_f32_16x16x32_bf16 v[76:79], a[28:31], v[48:51], v[76:79]\n"
    "v_mfma_f32_16x16x32_bf16 v[72:75], a[28:31], v[44:47], v[72:75]\n"
    "s_waitcnt lgkmcnt(7)\n"
    "v_mfma_f32_16x16x32_bf16 v[64:67], a[32:35], v[52:55], v[64:67]\n"
    "ds_read_b128 v[44:47], v8 offset:512\n"
    "s_waitcnt lgkmcnt(7)\n"
    "v_mfma_f32_16x16x32_bf16 v[68:71], a[32:35], v[56:59], v[68:71]\n"
    "ds_read_b128 v[48:51], v8 offset:17408\n"
    "v_mfma_f32_16x16x32_bf16 v[76:79], a[36:39], v[56:59], v[76:79]\n"
    "v_mfma_f32_16x16x32_bf16 v[72:75], a[36:39], v[52:55], v[72:75]\n"
    "s_waitcnt lgkmcnt(7)\n"
    "v_mfma_f32_16x16x32_bf16 v[64:67], a[40:43], v[20:23], v[64:67]\n"
    "ds_read_b128 v[52:55], v8 offset:576\n"
    "s_waitcnt lgkmcnt(7)\n"
    "v_mfma_f32_16x16x32_bf16 v[68:71], a[40:43], v[24:27], v[68:71]\n"
    "ds_read_b128 v[56:59], v8 offset:17472\n"
    "v_mfma_f32_16x16x32_bf16 v[76:79], a[44:47], v[24:27], v[76:79]\n"
    "v_mfma_f32_16x16x32_bf16 v[72:75], a[44:47], v[20:23], v[72:75]\n"
    "s_waitcnt lgkmcnt(7)\n"
    "v_mfma_f32_16x16x32_bf16 v[64:67], a[48:51], v[28:31], v[64:67]\n"
    "ds_read_b128 v[20:23], v8 offset:640\n"
    "s_waitcnt lgkmcnt(7)\n"
    "v_mfma_f32_16x16x32_bf16 v[68:71], a[48:51], v[32:35], v[68:71]\n"
    "ds_read_b128 v[24:27], v8 offset:17536\n"
    "v_mfma_f32_16x16x32_bf16 v[76:79], a[52:55], v[32:35], v[76:79]\n"
    "v_mfma_f32_16x16x32_bf16 v[72:75], a[52:55], v[28:31], v[72:75]\n"
    "s_waitcnt lgkmcnt(7)\n"
    "v_mfma_f32_16x16x32_bf16 v[64:67], a[56:59], v[36:39], v[64:67]\n"
    "ds_read_b128 v[28:31], v8 offset:704\n"
    "s_waitcnt lgkmcnt(7)\n"
    "v_mfma_f32_16x16x32_bf16 v[68:71], a[56:59], v[40:43], v[68:71]\n"
    "ds_read_b128 v[32:35], v8 offset:17600\n"
    "v_mfma_f32_16x16x32_bf16 v[76:79], a[60:63], v[40:43], v[76:79]\n"
    "v_mfma_f32_16x16x32_bf16 v[72:75], a[60:63], v[36:39], v[72:75]\n"
    "s_waitcnt lgkmcnt(7)\n"
    "v_mfma_f32_16x16x32_bf16 v[64:67], a[64:67], v[44:47], v[64:67]\n"
    "ds_read_b128 v[36:39], v8 offset:768\n"
    "s_waitcnt lgkmcnt(7)\n"
    "v_mfma_f32_16x16x32_bf16 v[68:71], a[64:67], v[48:51], v[68:71]\n"
    "ds_read_b128 v[40:43], v8 offset:17664\n"
    "v_mfma_f32_16x16x32_bf16 v[76:79], a[68:71], v[48:51], v[76:79]\n"
    "v_mfma_f32_16x16x32_bf16 v[72:75], a[68:71], v[44:47], v[72:75]\n"
    "s_waitcnt lgkmcnt(7)\n"
    "v_mfma_f32_16x16x32_bf16 v[64:67], a[72:75], v[52:55], v[64:67]\n"
    "ds_read_b128 v[44:47], v8 offset:832\n"
    "s_waitcnt lgkmcnt(7)\n"
    "v_mfma_f32_16x16x32_bf16 v[68:71], a[72:75], v[56:59], v[68:71]\n"
    "ds_read_b128 v[48:51], v8 offset:17728\n"
    "v_mfma_f32_16x16x32_bf16 v[76:79], a[76:79], v[56:59], v[76:79]\n"
    "v_mfma_f32_16x16x32_bf16 v[72:75], a[76:79], v[52:55], v[72:75]\n"
    "s_waitcnt lgkmcnt(7)\n"
    "v_mfma_f32_16x16x32_bf16 v[64:67], a[80:83], v[20:23], v[64:67]\n"
    "ds_read_b128 v[52:55], v8 offset:896\n"
    "s_waitcnt lgkmcnt(7)\n"
    "v_mfma_f32_16x16x32_bf16 v[68:71], a[80:83], v[24:27], v[68:71]\n"
    "ds_read_b128 v[56:59], v8 offset:17792\n"
    "v_mfma_f32_16x16x32_bf16 v[76:79], a[84:87], v[24:27], v[76:79]\n"
    "v_mfma_f32_16x16x32_bf16 v[72:75], a[84:87], v[20:23], v[72:75]\n"
    "s_waitcnt lgkmcnt(7)\n"
    "v_mfma_f32_16x16x32_bf16 v[64:67], a[88:91], v[28:31], v[64:67]\n"
    "ds_read_b128 v[20:23], v8 offset:960\n"
    "s_waitcnt lgkmcnt(7)\n"
    "v_mfma_f32_16x16x32_bf16 v[68:71], a[88:91], v[32:35], v[68:71]\n"
    "ds_read_b128 v[24:27], v8 offset:17856\n"
    "v_mfma_f32_16x16x32_bf16 v[76:79], a[92:95], v[32:35], v[76:79]\n"
    "v_mfma_f32_16x16x32_bf16 v[72:75], a[92:95], v[28:31], v[72:75]\n"
    "s_waitcnt lgkmcnt(7)\n"
    "v_mfma_f32_16x16x32_bf16 v[64:67], a[96:99], v[36:39], v[64:67]\n"
    "ds_read_b128 v[28:31], v8 offset:0\n"
    "s_waitcnt lgkmcnt(7)\n"
    "v_mfma_f32_16x16x32_bf16 v[68:71], a[96:99], v[40:43], v[68:71]\n"
    "ds_read_b128 v[32:35], v8 offset:16896\n"
    "s_waitcnt vmcnt(0)\n"
    "s_barrier\n"
    "s_or_b32 s52, s52, s68\n"
    "buffer_store_dword v5, v9, s[16:19], s52 offen sc1\n"
    "s_mov_b32 s52, 0x80000000\n"
    "v_mfma_f32_16x16x32_bf16 v[76:79], a[100:103], v[40:43], v[76:79]\n"
    "v_mfma_f32_16x16x32_bf16 v[72:75], a[100:103], v[36:39], v[72:75]\n"
    "s_waitcnt lgkmcnt(7)\n"
    "v_mfma_f32_16x16x32_bf16 v[64:67], a[104:107], v[44:47], v[64:67]\n"
    "ds_read_b128 v[36:39], v8 offset:64\n"
    "s_waitcnt lgkmcnt(7)\n"
    "v_mfma_f32_16x16x32_bf16 v[68:71], a[104:107], v[48:51], v[68:71]\n"
    "ds_read_b128 v[40:43], v8 offset:16960\n"
    "v_mfma_f32_16x16x32_bf16 v[76:79], a[108:111], v[48:51], v[76:79]\n"
    "v_mfma_f32_16x16x32_bf16 v[72:75], a[108:111], v[44:47], v[72:75]\n"
    "s_waitcnt lgkmcnt(7)\n"
    "v_mfma_f32_16x16x32_bf16 v[64:67], a[112:115], v[52:55], v[64:67]\n"
    "ds_read_b128 v[44:47], v8 offset:128\n"
    "s_waitcnt lgkmcnt(7)\n"
    "v_mfma_f32_16x16x32_bf16 v[68:71], a[112:115], v[56:59], v[68:71]\n"
    "ds_read_b128 v[48:51], v8 offset:17024\n"
    "v_mfma_f32_16x16x32_bf16 v[76:79], a[116:119], v[56:59], v[76:79]\n"
    "v_mfma_f32_16x16x32_bf16 v[72:75], a[116:119], v[52:55], v[72:75]\n"
    "s_waitcnt lgkmcnt(7)\n"
    "v_mfma_f32_16x16x32_bf16 v[64:67], a[120:123], v[20:23], v[64:67]\n"
    "ds_read_b128 v[52:55], v8 offset:192\n"
    "s_waitcnt lgkmcnt(7)\n"
    "v_mfma_f32_16x16x32_bf16 v[68:71], a[120:123], v[24:27], v[68:71]\n"
    "ds_read_b128 v[56:59], v8 offset:17088\n"
    "v_mfma_f32_16x16x32_bf16 v[76:79], a[124:127], v[24:27], v[76:79]\n"
    "v_mfma_f32_16x16x32_bf16 v[72:75], a[124:127], v[20:23], v[72:75]\n"
    "s_waitcnt lgkmcnt(7)\n"
    "v_mfma_f32_16x16x32_bf16 v[80:83], a[128:131], v[28:31], 0\n"
    "ds_read_b128 v[20:23], v8 offset:256\n"
    "s_waitcnt lgkmcnt(7)\n"
    "v_mfma_f32_16x16x32_bf16 v[84:87], a[128:131], v[32:35], 0\n"
    "ds_read_b128 v[24:27], v8 offset:17152\n"
    "v_mfma_f32_16x16x32_bf16 v[92:95], a[132:135], v[32:35], 0\n"
    "v_mfma_f32_16x16x32_bf16 v[88:91], a[132:135], v[28:31], 0\n"
    "s_waitcnt lgkmcnt(7)\n"
    "v_mfma_f32_16x16x32_bf16 v[80:83], a[136:139], v[36:39], v[80:83]\n"
    "ds_read_b128 v[28:31], v8 offset:320\n"
    "s_waitcnt lgkmcnt(7)\n"
    "v_mfma_f32_16x16x32_bf16 v[84:87], a[136:139], v[40:43], v[84:87]\n"
    "ds_read_b128 v[32:35], v8 offset:17216\n"
    "v_cvt_pk_bf16_f32 v12, v64, v72\n"
    "v_mfma_f32_16x16x32_bf16 v[92:95], a[140:143], v[40:43], v[92:95]\n"
    "v_cvt_pk_bf16_f32 v13, v65, v73\n"
    "v_mfma_f32_16x16x32_bf16 v[88:91], a[140:143], v[36:39], v[88:91]\n"
    "s_waitcnt lgkmcnt(7)\n"
    "v_cvt_pk_bf16_f32 v14, v66, v74\n"
    "v_mfma_f32_16x16x32_bf16 v[80:83], a[144:147], v[44:47], v[80:83]\n"
    "ds_read_b128 v[36:39], v8 offset:384\n"
    "s_waitcnt lgkmcnt(7)\n"
    "v_mfma_f32_16x16x32_bf16 v[84:87], a[144:147], v[48:51], v[84:87]\n"
    "ds_read_b128 v[40:43], v8 offset:17280\n"
    "v_cvt_pk_bf16_f32 v15, v67, v75\n"
    "v_mfma_f32_16x16x32_bf16 v[92:95], a[148:151], v[48:51], v[92:95]\n"
    "buffer_store_dwordx4 v[12:15], v0, s[16:19], s59 offen sc1\n"
    "v_mfma_f32_16x16x32_bf16 v[88:91], a[148:151], v[44:47], v[88:91]\n"
    "s_waitcnt lgkmcnt(7)\n"
    "v_cvt_pk_bf16_f32 v16, v68, v76\n"
    "v_mfma_f32_16x16x32_bf16 v[80:83], a[152:155], v[52:55], v[80:83]\n"
    "ds_read_b128 v[44:47], v8 offset:448\n"
    "s_waitcnt lgkmcnt(7)\n"
    "v_mfma_f32_16x16x32_bf16 v[84:87], a[152:155], v[56:59], v[84:87]\n"
    "ds_read_b128 v[48:51], v8 offset:17344\n"
    "v_cvt_pk_bf16_f32 v17, v69, v77\n"
    "v_mfma_f32_16x16x32_bf16 v[92:95], a[156:159], v[56:59], v[92:95]\n"
    "v_cvt_pk_bf16_f32 v18, v70, v78\n"
    "v_mfma_f32_16x16x32_bf16 v[88:91], a[156:159], v[52:55], v[88:91]\n"
    "s_waitcnt lgkmcnt(7)\n"
    "v_cvt_pk_bf16_f32 v19, v71, v79\n"
    "v_mfma_f32_16x16x32_bf16 v[80:83], a[160:163], v[20:23], v[80:83]\n"
    "ds_read_b128 v[52:55], v8 offset:512\n"
    "s_waitcnt lgkmcnt(7)\n"
    "v_mfma_f32_16x16x32_bf16 v[84:87], a[160:163], v[24:27], v[84:87]\n"
    "ds_read_b128 v[56:59], v8 offset:17408\n"
    "buffer_store_dwordx4 v[16:19], v0, s[16:19], s62 offen sc1\n"
    "v_mfma_f32_16x16x32_bf16 v[92:95], a[164:167], v[24:27], v[92:95]\n"
    "s_add_u32 s59, s59, 16384\n"
    "s_add_u32 s62, s62, 16384\n"
    "v_mfma_f32_16x16x32_bf16 v[88:91], a[164:167], v[20:23], v[88:91]\n"
    "s_waitcnt lgkmcnt(7)\n"
    "v_mfma_f32_16x16x32_bf16 v[80:83], a[168:171], v[28:31], v[80:83]\n"
    "ds_read_b128 v[20:23], v8 offset:576\n"
    "s_waitcnt lgkmcnt(7)\n"
    "v_mfma_f32_16x16x32_bf16 v[84:87], a[168:171], v[32:35], v[84:87]\n"
    "ds_read_b128 v[24:27], v8 offset:17472\n"
    "v_mfma_f32_16x16x32_bf16 v[92:95], a[172:175], v[32:35], v[92:95]\n"
    "v_mfma_f32_16x16x32_bf16 v[88:91], a[172:175], v[28:31], v[88:91]\n"
    "s_waitcnt lgkmcnt(7)\n"
    "v_mfma_f32_16x16x32_bf16 v[80:83], a[176:179], v[36:39], v[80:83]\n"
    "ds_read_b128 v[28:31], v8 offset:640\n"
    "s_waitcnt lgkmcnt(7)\n"
    "v_mfma_f32_16x16x32_bf16 v[84:87], a[176:179], v[40:43], v[84:87]\n"
    "ds_read_b128 v[32:35], v8 offset:17536\n"
    "v_mfma_f32_16x16x32_bf16 v[92:95], a[180:183], v[40:43], v[92:95]\n"
    "v_mfma_f32_16x16x32_bf16 v[88:91], a[180:183], v[36:39], v[88:91]\n"
    "s_waitcnt lgkmcnt(7)\n"
    "v_mfma_f32_16x16x32_bf16 v[80:83], a[184:187], v[44:47], v[80:83]\n"
    "ds_read_b128 v[36:39], v8 offset:704\n"
    "s_waitcnt lgkmcnt(7)\n"
    "v_mfma_f32_16x16x32_bf16 v[84:87], a[184:187], v[48:51], v[84:87]\n"
    "ds_read_b128 v[40:43], v8 offset:17600\n"
    "v_mfma_f32_16x16x32_bf16 v[92:95], a[188:191], v[48:51], v[92:95]\n"
    "v_mfma_f32_16x16x32_bf16 v[88:91], a[188:191], v[44:47], v[88:91]\n"
    "s_waitcnt lgkmcnt(7)\n"
    "v_mfma_f32_16x16x32_bf16 v[80:83], a[192:195], v[52:55], v[80:83]\n"
    "ds_read_b128 v[44:47], v8 offset:768\n"
    "s_waitcnt lgkmcnt(7)\n"
    "v_mfma_f32_16x16x32_bf16 v[84:87], a[192:195], v[56:59], v[84:87]\n"
    "ds_read_b128 v[48:51], v8 offset:17664\n"
    "v_mfma_f32_16x16x32_bf16 v[92:95], a[196:199], v[56:59], v[92:95]\n"
    "v_mfma_f32_16x16x32_bf16 v[88:91], a[196:199], v[52:55], v[88:91]\n"
    "s_waitcnt lgkmcnt(7)\n"
    "v_mfma_f32_16x16x32_bf16 v[80:83], a[200:203], v[20:23], v[80:83]\n"
    "ds_read_b128 v[52:55], v8 offset:832\n"
    "s_waitcnt lgkmcnt(7)\n"
    "v_mfma_f32_16x16x32_bf16 v[84:87], a[200:203], v[24:27], v[84:87]\n"
    "ds_read_b128 v[56:59], v8 offset:17728\n"
    "v_mfma_f32_16x16x32_bf16 v[92:95], a[204:207], v[24:27], v[92:95]\n"
    "v_mfma_f32_16x16x32_bf16 v[88:91], a[204:207], v[20:23], v[88:91]\n"
    "s_waitcnt lgkmcnt(7)\n"
    "v_mfma_f32_16x16x32_bf16 v[80:83], a[208:211], v[28:31], v[80:83]\n"
    "ds_read_b128 v[20:23], v8 offset:896\n"
    "s_waitcnt lgkmcnt(7)\n"
    "v_mfma_f32_16x16x32_bf16 v[84:87], a[208:211], v[32:35], v[84:87]\n"
    "ds_read_b128 v[24:27], v8 offset:17792\n"
    "v_mfma_f32_16x16x32_bf16 v[92:95], a[212:215], v[32:35], v[92:95]\n"
    "v_mfma_f32_16x16x32_bf16 v[88:91], a[212:215], v[28:31], v[88:91]\n"
    "s_waitcnt lgkmcnt(7)\n"
    "v_mfma_f32_16x16x32_bf16 v[80:83], a[216:219], v[36:39], v[80:83]\n"
    "ds_read_b128 v[28:31], v8 offset:960\n"
    "s_waitcnt lgkmcnt(7)\n"
    "v_mfma_f32_16x16x32_bf16 v[84:87], a[216:219], v[40:43], v[84:87]\n"
    "ds_read_b128 v[32:35], v8 offset:17856\n"
    "v_mfma_f32_16x16x32_bf16 v[92:95], a[220:223], v[40:43], v[92:95]\n"
    "v_mfma_f32_16x16x32_bf16 v[88:91], a[220:223], v[36:39], v[88:91]\n"
    "s_waitcnt lgkmcnt(7)\n"
    "v_mfma_f32_16x16x32_bf16 v[80:83], a[224:227], v[44:47], v[80:83]\n"
    "s_waitcnt lgkmcnt(6)\n"
    "v_mfma_f32_16x16x32_bf16 v[84:87], a[224:227], v[48:51], v[84:87]\n"
    "v_mfma_f32_16x16x32_bf16 v[92:95], a[228:231], v[48:51], v[92:95]\n"
    "v_mfma_f32_16x16x32_bf16 v[88:91], a[228:231], v[44:47], v[88:91]\n"
    "s_waitcnt lgkmcnt(5)\n"
    "v_mfma_f32_16x16x32_bf16 v[80:83], a[232:235], v[52:55], v[80:83]\n"
    "s_waitcnt lgkmcnt(4)\n"
    "v_mfma_f32_16x16x32_bf16 v[84:87], a[232:235], v[56:59], v[84:87]\n"
    "v_mfma_f32_16x16x32_bf16 v[92:95], a[236:239], v[56:59], v[92:95]\n"
    "v_mfma_f32_16x16x32_bf16 v[88:91], a[236:239], v[52:55], v[88:91]\n"
    "s_waitcnt lgkmcnt(3)\n"
    "v_mfma_f32_16x16x32_bf16 v[80:83], a[240:243], v[20:23], v[80:83]\n"
    "s_waitcnt lgkmcnt(2)\n"
    "v_mfma_f32_16x16x32_bf16 v[84:87], a[240:243], v[24:27], v[84:87]\n"
    "v_mfma_f32_16x16x32_bf16 v[92:95], a[244:247], v[24:27], v[92:95]\n"
    "v_mfma_f32_16x16x32_bf16 v[88:91], a[244:247], v[20:23], v[88:91]\n"
    "s_waitcnt lgkmcnt(1)\n"
    "v_mfma_f32_16x16x32_bf16 v[80:83], a[248:251], v[28:31], v[80:83]\n"
    "s_waitcnt lgkmcnt(0)\n"
    "v_mfma_f32_16x16x32_bf16 v[84:87], a[248:251], v[32:35], v[84:87]\n"
    "v_mfma_f32_16x16x32_bf16 v[92:95], a[252:255], v[32:35], v[92:95]\n"
    "v_mfma_f32_16x16x32_bf16 v[88:91], a[252:255], v[28:31], v[88:91]\n"
    "s_nop 7\n"
    "v_cvt_pk_bf16_f32 v12, v80, v88\n"
    "v_cvt_pk_bf16_f32 v13, v81, v89\n"
    "v_cvt_pk_bf16_f32 v14, v82, v90\n"
    "v_cvt_pk_bf16_f32 v15, v83, v91\n"
    "buffer_store_dwordx4 v[12:15], v0, s[16:19], s59 offen sc1\n"
    "v_cvt_pk_bf16_f32 v16, v84, v92\n"
    "v_cvt_pk_bf16_f32 v17, v85, v93\n"
    "v_cvt_pk_bf16_f32 v18, v86, v94\n"
    "v_cvt_pk_bf16_f32 v19, v87, v95\n"
    "buffer_store_dwordx4 v[16:19], v0, s[16:19], s62 offen sc1\n"
    "s_mov_b32 s52, s64\n"
    "v_mov_b32 v5, s67\n"
    "s_branch LHE%=\n"
    "LHV%=:\n"
    "s_add_u32 s62, s59, 1024\n"
    "ds_read_b128 v[20:23], v8 offset:0\n"
    "ds_read_b128 v[24:27], v8 offset:16896\n"
    "ds_read_b128 v[28:31], v8 offset:64\n"
    "ds_read_b128 v[32:35], v8 offset:16960\n"
    "ds_read_b128 v[36:39], v8 offset:128\n"
    "ds_read_b128 v[40:43], v8 offset:17024\n"
    "ds_read_b128 v[44:47], v8 offset:192\n"
    "ds_read_b128 v[48:51], v8 offset:17088\n"
    "s_waitcnt lgkmcnt(7)\n"
    "v_mfma_f32_16x16x32_bf16 v[64:67], v[20:23], a[0:3], 0\n"
    "ds_read_b128 v[52:55], v8 offset:256\n"
    "s_waitcnt lgkmcnt(7)\n"
    "v_mfma_f32_16x16x32_bf16 v[68:71], v[24:27], a[0:3], 0\n"
    "ds_read_b128 v[56:59], v8 offset:17152\n"
    "v_mfma_f32_16x16x32_bf16 v[76:79], v[24:27], a[4:7], 0\n"
    "v_mfma_f32_16x16x32_bf16 v[72:75], v[20:23], a[4:7], 0\n"
    "s_waitcnt lgkmcnt(7)\n"
    "v_mfma_f32_16x16x32_bf16 v[64:67], v[28:31], a[8:11], v[64:67]\n"
    "ds_read_b128 v[20:23], v8 offset:320\n"
    "s_waitcnt lgkmcnt(7)\n"
    "v_mfma_f32_16x16x32_bf16 v[68:71], v[32:35], a[8:11], v[68:71]\n"
    "ds_read_b128 v[24:27], v8 offset:17216\n"
    "v_mfma_f32_16x16x32_bf16 v[76:79], v[32:35], a[12:15], v[76:79]\n"
    "v_mfma_f32_16x16x32_bf16 v[72:75], v[28:31], a[12:15], v[72:75]\n"
    "s_waitcnt lgkmcnt(7)\n"
    "v_mfma_f32_16x16x32_bf16 v[64:67], v[36:39], a[16:19], v[64:67]\n"
    "ds_read_b128 v[28:31], v8 offset:384\n"
    "s_waitcnt lgkmcnt(7)\n"
    "v_mfma_f32_16x16x32_bf16 v[68:71], v[40:43], a[16:19], v[68:71]\n"
    "ds_read_b128 v[32:35], v8 offset:17280\n"
    "v_mfma_f32_16x16x32_bf16 v[76:79], v[40:43], a[20:23], v[76:79]\n"
    "v_mfma_f32_16x16x32_bf16 v[72:75], v[36:39], a[20:23], v[72:75]\n"
    "s_waitcnt lgkmcnt(7)\n"
    "v_mfma_f32_16x16x32_bf16 v[64:67], v[44:47], a[24:27], v[64:67]\n"
    "ds_read_b128 v[36:39], v8 offset:448\n"
    "s_waitcnt lgkmcnt(7)\n"
    "v_mfma_f32_16x16x32_bf16 v[68:71], v[48:51], a[24:27], v[68:71]\n"
    "ds_read_b128 v[40:43], v8 offset:17344\n"
    "v_mfma_f32_16x16x32_bf16 v[76:79], v[48:51], a[28:31], v[76:79]\n"
    "v_mfma_f32_16x16x32_bf16 v[72:75], v[44:47], a[28:31], v[72:75]\n"
    "s_waitcnt lgkmcnt(7)\n"
    "v_mfma_f32_16x16x32_bf16 v[64:67], v[52:55], a[32:35], v[64:67]\n"
    "ds_read_b128 v[44:47], v8 offset:512\n"
    "s_waitcnt lgkmcnt(7)\n"
    "v_mfma_f32_16x16x32_bf16 v[68:71], v[56:59], a[32:35], v[68:71]\n"
    "ds_read_b128 v[48:51], v8 offset:17408\n"
    "v_mfma_f32_16x16x32_bf16 v[76:79], v[56:59], a[36:39], v[76:79]\n"
    "v_mfma_f32_16x16x32_bf16 v[72:75], v[52:55], a[36:39], v[72:75]\n"
    "s_waitcnt lgkmcnt(7)\n"
    "v_mfma_f32_16x16x32_bf16 v[64:67], v[20:23], a[40:43], v[64:67]\n"
    "ds_read_b128 v[52:55], v8 offset:576\n"
    "s_waitcnt lgkmcnt(7)\n"
    "v_mfma_f32_16x16x32_bf16 v[68:71], v[24:27], a[40:43], v[68:71]\n"
    "ds_read_b128 v[56:59], v8 offset:17472\n"
    "v_mfma_f32_16x16x32_bf16 v[76:79], v[24:27], a[44:47], v[76:79]\n"
    "v_mfma_f32_16x16x32_bf16 v[72:75], v[20:23], a[44:47], v[72:75]\n"
    "s_waitcnt lgkmcnt(7)\n"
    "v_mfma_f32_16x16x32_bf16 v[64:67], v[28:31], a[48:51], v[64:67]\n"
    "ds_read_b128 v[20:23], v8 offset:640\n"
    "s_waitcnt lgkmcnt(7)\n"
    "v_mfma_f32_16x16x32_bf16 v[68:71], v[32:35], a[48:51], v[68:71]\n"
    "ds_read_b128 v[24:27], v8 offset:17536\n"
    "v_mfma_f32_16x16x32_bf16 v[76:79], v[32:35], a[52:55], v[76:79]\n"
    "v_mfma_f32_16x16x32_bf16 v[72:75], v[28:31], a[52:55], v[72:75]\n"
    "s_waitcnt lgkmcnt(7)\n"
    "v_mfma_f32_16x16x32_bf16 v[64:67], v[36:39], a[56:59], v[64:67]\n"
    "ds_read_b128 v[28:31], v8 offset:704\n"
    "s_waitcnt lgkmcnt(7)\n"
    "v_mfma_f32_16x16x32_bf16 v[68:71], v[40:43], a[56:59], v[68:71]\n"
    "ds_read_b128 v[32:35], v8 offset:17600\n"
    "v_mfma_f32_16x16x32_bf16 v[76:79], v[40:43], a[60:63], v[76:79]\n"
    "v_mfma_f32_16x16x32_bf16 v[72:75], v[36:39], a[60:63], v[72:75]\n"
    "s_waitcnt lgkmcnt(7)\n"
    "v_mfma_f32_16x16x32_bf16 v[64:67], v[44:47], a[64:67], v[64:67]\n"
    "ds_read_b128 v[36:39], v8 offset:768\n"
    "s_waitcnt lgkmcnt(7)\n"
    "v_mfma_f32_16x16x32_bf16 v[68:71], v[48:51], a[64:67], v[68:71]\n"
    "ds_read_b128 v[40:43], v8 offset:17664\n"
    "v_mfma_f32_16x16x32_bf16 v[76:79], v[48:51], a[68:71], v[76:79]\n"
    "v_mfma_f32_16x16x32_bf16 v[72:75], v[44:47], a[68:71], v[72:75]\n"
    "s_waitcnt lgkmcnt(7)\n"
    "v_mfma_f32_16x16x32_bf16 v[64:67], v[52:55], a[72:75], v[64:67]\n"
    "ds_read_b128 v[44:47], v8 offset:832\n"
    "s_waitcnt lgkmcnt(7)\n"
    "v_mfma_f32_16x16x32_bf16 v[68:71], v[56:59], a[72:75], v[68:71]\n"
    "ds_read_b128 v[48:51], v8 offset:17728\n"
    "v_mfma_f32_16x16x32_bf16 v[76:79], v[56:59], a[76:79], v[76:79]\n"
    "v_mfma_f32_16x16x32_bf16 v[72:75], v[52:55], a[76:79], v[72:75]\n"
    "s_waitcnt lgkmcnt(7)\n"
    "v_mfma_f32_16x16x32_bf16 v[64:67], v[20:23], a[80:83], v[64:67]\n"
    "ds_read_b128 v[52:55], v8 offset:896\n"
    "s_waitcnt lgkmcnt(7)\n"
    "v_mfma_f32_16x16x32_bf16 v[68:71], v[24:27], a[80:83], v[68:71]\n"
    "ds_read_b128 v[56:59], v8 offset:17792\n"
    "v_mfma_f32_16x16x32_bf16 v[76:79], v[24:27], a[84:87], v[76:79]\n"
    "v_mfma_f32_16x16x32_bf16 v[72:75], v[20:23], a[84:87], v[72:75]\n"
    "s_waitcnt lgkmcnt(7)\n"
    "v_mfma_f32_16x16x32_bf16 v[64:67], v[28:31], a[88:91], v[64:67]\n"
    "ds_read_b128 v[20:23], v8 offset:960\n"
    "s_waitcnt lgkmcnt(7)\n"
    "v_mfma_f32_16x16x32_bf16 v[68:71], v[32:35], a[88:91], v[68:71]\n"
    "ds_read_b128 v[24:27], v8 offset:17856\n"
    "v_mfma_f32_16x16x32_bf16 v[76:79], v[32:35], a[92:95], v[76:79]\n"
    "v_mfma_f32_16x16x32_bf16 v[72:75], v[28:31], a[92:95], v[72:75]\n"
    "s_waitcnt lgkmcnt(7)\n"
    "v_mfma_f32_16x16x32_bf16 v[64:67], v[36:39], a[96:99], v[64:67]\n"
    "ds_read_b128 v[28:31], v8 offset:0\n"
    "s_waitcnt lgkmcnt(7)\n"
    "v_mfma_f32_16x16x32_bf16 v[68:71], v[40:43], a[96:99], v[68:71]\n"
    "ds_read_b128 v[32:35], v8 offset:16896\n"
    "s_waitcnt vmcnt(0)\n"
    "s_barrier\n"
    "s_or_b32 s52, s52, s68\n"
    "buffer_store_dword v5, v9, s[16:19], s52 offen sc1\n"
    "s_mov_b32 s52, 0x80000000\n"
    "v_mfma_f32_16x16x32_bf16 v[76:79], v[40:43], a[100:103], v[76:79]\n"
    "v_mfma_f32_16x16x32_bf16 v[72:75], v[36:39], a[100:103], v[72:75]\n"
    "s_waitcnt lgkmcnt(7)\n"
    "v_mfma_f32_16x16x32_bf16 v[64:67], v[44:47], a[104:107], v[64:67]\n"
    "ds_read_b128 v[36:39], v8 offset:64\n"
    "s_waitcnt lgkmcnt(7)\n"
    "v_mfma_f32_16x16x32_bf16 v[68:71], v[48:51], a[104:107], v[68:71]\n"
    "ds_read_b128 v[40:43], v8 offset:16960\n"
    "v_mfma_f32_16x16x32_bf16 v[76:79], v[48:51], a[108:111], v[76:79]\n"
    "v_mfma_f32_16x16x32_bf16 v[72:75], v[44:47], a[108:111], v[72:75]\n"
    "s_waitcnt lgkmcnt(7)\n"
    "v_mfma_f32_16x16x32_bf16 v[64:67], v[52:55], a[112:115], v[64:67]\n"
    "ds_read_b128 v[44:47], v8 offset:128\n"
    "s_waitcnt lgkmcnt(7)\n"
    "v_mfma_f32_16x16x32_bf16 v[68:71], v[56:59], a[112:115], v[68:71]\n"
    "ds_read_b128 v[48:51], v8 offset:17024\n"
    "v_mfma_f32_16x16x32_bf16 v[76:79], v[56:59], a[116:119], v[76:79]\n"
    "v_mfma_f32_16x16x32_bf16 v[72:75], v[52:55], a[116:119], v[72:75]\n"
    "s_waitcnt lgkmcnt(7)\n"
    "v_mfma_f32_16x16x32_bf16 v[64:67], v[20:23], a[120:123], v[64:67]\n"
    "ds_read_b128 v[52:55], v8 offset:192\n"
    "s_waitcnt lgkmcnt(7)\n"
    "v_mfma_f32_16x16x32_bf16 v[68:71], v[24:27], a[120:123], v[68:71]\n"
    "ds_read_b128 v[56:59], v8 offset:17088\n"
    "v_mfma_f32_16x16x32_bf16 v[76:79], v[24:27], a[124:127], v[76:79]\n"
    "v_mfma_f32_16x16x32_bf16 v[72:75], v[20:23], a[124:127], v[72:75]\n"
    "s_waitcnt lgkmcnt(7)\n"
    "v_mfma_f32_16x16x32_bf16 v[80:83], v[28:31], a[128:131], 0\n"
    "ds_read_b128 v[20:23], v8 offset:256\n"
    "s_waitcnt lgkmcnt(7)\n"
    "v_mfma_f32_16x16x32_bf16 v[84:87], v[32:35], a[128:131], 0\n"
    "ds_read_b128 v[24:27], v8 offset:17152\n"
    "v_mfma_f32_16x16x32_bf16 v[92:95], v[32:35], a[132:135], 0\n"
    "v_mfma_f32_16x16x32_bf16 v[88:91], v[28:31], a[132:135], 0\n"
    "s_waitcnt lgkmcnt(7)\n"
    "v_mfma_f32_16x16x32_bf16 v[80:83], v[36:39], a[136:139], v[80:83]\n"
    "ds_read_b128 v[28:31], v8 offset:320\n"
    "s_waitcnt lgkmcnt(7)\n"
    "v_mfma_f32_16x16x32_bf16 v[84:87], v[40:43], a[136:139], v[84:87]\n"
    "ds_read_b128 v[32:35], v8 offset:17216\n"
    "v_cvt_pk_bf16_f32 v12, v64, v65\n"
    "v_mfma_f32_16x16x32_bf16 v[92:95], v[40:43], a[140:143], v[92:95]\n"
    "v_cvt_pk_bf16_f32 v13, v66, v67\n"
    "v_mfma_f32_16x16x32_bf16 v[88:91], v[36:39], a[140:143], v[88:91]\n"
    "s_waitcnt lgkmcnt(7)\n"
    "v_cvt_pk_bf16_f32 v14, v68, v69\n"
    "v_mfma_f32_16x16x32_bf16 v[80:83], v[44:47], a[144:147], v[80:83]\n"
    "ds_read_b128 v[36:39], v8 offset:384\n"
    "s_waitcnt lgkmcnt(7)\n"
    "v_mfma_f32_16x16x32_bf16 v[84:87], v[48:51], a[144:147], v[84:87]\n"
    "ds_read_b128 v[40:43], v8 offset:17280\n"
    "v_cvt_pk_bf16_f32 v15, v70, v71\n"
    "v_mfma_f32_16x16x32_bf16 v[92:95], v[48:51], a[148:151], v[92:95]\n"
    "buffer_store_dwordx4 v[12:15], v0, s[16:19], s59 offen sc1\n"
    "v_mfma_f32_16x16x32_bf16 v[88:91], v[44:47], a[148:151], v[88:91]\n"
    "s_waitcnt lgkmcnt(7)\n"
    "v_cvt_pk_bf16_f32 v16, v72, v73\n"
    "v_mfma_f32_16x16x32_bf16 v[80:83], v[52:55], a[152:155], v[80:83]\n"
    "ds_read_b128 v[44:47], v8 offset:448\n"
    "s_waitcnt lgkmcnt(7)\n"
    "v_mfma_f32_16x16x32_bf16 v[84:87], v[56:59], a[152:155], v[84:87]\n"
    "ds_read_b128 v[48:51], v8 offset:17344\n"
    "v_cvt_pk_bf16_f32 v17, v74, v75\n"
    "v_mfma_f32_16x16x32_bf16 v[92:95], v[56:59], a[156:159], v[92:95]\n"
    "v_cvt_pk_bf16_f32 v18, v76, v77\n"
    "v_mfma_f32_16x16x32_bf16 v[88:91], v[52:55], a[156:159], v[88:91]\n"
    "s_waitcnt lgkmcnt(7)\n"
    "v_cvt_pk_bf16_f32 v19, v78, v79\n"
    "v_mfma_f32_16x16x32_bf16 v[80:83], v[20:23], a[160:163], v[80:83]\n"
    "ds_read_b128 v[52:55], v8 offset:512\n"
    "s_waitcnt lgkmcnt(7)\n"
    "v_mfma_f32_16x16x32_bf16 v[84:87], v[24:27], a[160:163], v[84:87]\n"
    "ds_read_b128 v[56:59], v8 offset:17408\n"
    "buffer_store_dwordx4 v[16:19], v0, s[16:19], s62 offen sc1\n"
    "v_mfma_f32_16x16x32_bf16 v[92:95], v[24:27], a[164:167], v[92:95]\n"
    "s_add_u32 s59, s59, 16384\n"
    "s_add_u32 s62, s62, 16384\n"
    "v_mfma_f32_16x16x32_bf16 v[88:91], v[20:23], a[164:167], v[88:91]\n"
    "s_waitcnt lgkmcnt(7)\n"
    "v_mfma_f32_16x16x32_bf16 v[80:83], v[28:31], a[168:171], v[80:83]\n"
    "ds_read_b128 v[20:23], v8 offset:576\n"
    "s_waitcnt lgkmcnt(7)\n"
    "v_mfma_f32_16x16x32_bf16 v[84:87], v[32:35], a[168:171], v[84:87]\n"
    "ds_read_b128 v[24:27], v8 offset:17472\n"
    "v_mfma_f32_16x16x32_bf16 v[92:95], v[32:35], a[172:175], v[92:95]\n"
    "v_mfma_f32_16x16x32_bf16 v[88:91], v[28:31], a[172:175], v[88:91]\n"
    "s_waitcnt lgkmcnt(7)\n"
    "v_mfma_f32_16x16x32_bf16 v[80:83], v[36:39], a[176:179], v[80:83]\n"
    "ds_read_b128 v[28:31], v8 offset:640\n"
    "s_waitcnt lgkmcnt(7)\n"
    "v_mfma_f32_16x16x32_bf16 v[84:87], v[40:43], a[176:179], v[84:87]\n"
    "ds_read_b128 v[32:35], v8 offset:17536\n"
    "v_mfma_f32_16x16x32_bf16 v[92:95], v[40:43], a[180:183], v[92:95]\n"
    "v_mfma_f32_16x16x32_bf16 v[88:91], v[36:39], a[180:183], v[88:91]\n"
    "s_waitcnt lgkmcnt(7)\n"
    "v_mfma_f32_16x16x32_bf16 v[80:83], v[44:47], a[184:187], v[80:83]\n"
    "ds_read_b128 v[36:39], v8 offset:704\n"
    "s_waitcnt lgkmcnt(7)\n"
    "v_mfma_f32_16x16x32_bf16 v[84:87], v[48:51], a[184:187], v[84:87]\n"
    "ds_read_b128 v[40:43], v8 offset:17600\n"
    "v_mfma_f32_16x16x32_bf16 v[92:95], v[48:51], a[188:191], v[92:95]\n"
    "v_mfma_f32_16x16x32_bf16 v[88:91], v[44:47], a[188:191], v[88:91]\n"
    "s_waitcnt lgkmcnt(7)\n"
    "v_mfma_f32_16x16x32_bf16 v[80:83], v[52:55], a[192:195], v[80:83]\n"
    "ds_read_b128 v[44:47], v8 offset:768\n"
    "s_waitcnt lgkmcnt(7)\n"
    "v_mfma_f32_16x16x32_bf16 v[84:87], v[56:59], a[192:195], v[84:87]\n"
    "ds_read_b128 v[48:51], v8 offset:17664\n"
    "v_mfma_f32_16x16x32_bf16 v[92:95], v[56:59], a[196:199], v[92:95]\n"
    "v_mfma_f32_16x16x32_bf16 v[88:91], v[52:55], a[196:199], v[88:91]\n"
    "s_waitcnt lgkmcnt(7)\n"
    "v_mfma_f32_16x16x32_bf16 v[80:83], v[20:23], a[200:203], v[80:83]\n"
    "ds_read_b128 v[52:55], v8 offset:832\n"
    "s_waitcnt lgkmcnt(7)\n"
    "v_mfma_f32_16x16x32_bf16 v[84:87], v[24:27], a[200:203], v[84:87]\n"
    "ds_read_b128 v[56:59], v8 offset:17728\n"
    "v_mfma_f32_16x16x32_bf16 v[92:95], v[24:27], a[204:207], v[92:95]\n"
    "v_mfma_f32_16x16x32_bf16 v[88:91], v[20:23], a[204:207], v[88:91]\n"
    "s_waitcnt lgkmcnt(7)\n"
    "v_mfma_f32_16x16x32_bf16 v[80:83], v[28:31], a[208:211], v[80:83]\n"
    "ds_read_b128 v[20:23], v8 offset:896\n"
    "s_waitcnt lgkmcnt(7)\n"
    "v_mfma_f32_16x16x32_bf16 v[84:87], v[32:35], a[208:211], v[84:87]\n"
    "ds_read_b128 v[24:27], v8 offset:17792\n"
    "v_mfma_f32_16x16x32_bf16 v[92:95], v[32:35], a[212:215], v[92:95]\n"
    "v_mfma_f32_16x16x32_bf16 v[88:91], v[28:31], a[212:215], v[88:91]\n"
    "s_waitcnt lgkmcnt(7)\n"
    "v_mfma_f32_16x16x32_bf16 v[80:83], v[36:39], a[216:219], v[80:83]\n"
    "ds_read_b128 v[28:31], v8 offset:960\n"
    "s_waitcnt lgkmcnt(7)\n"
    "v_mfma_f32_16x16x32_bf16 v[84:87], v[40:43], a[216:219], v[84:87]\n"
    "ds_read_b128 v[32:35], v8 offset:17856\n"
    "v_mfma_f32_16x16x32_bf16 v[92:95], v[40:43], a[220:223], v[92:95]\n"
    "v_mfma_f32_16x16x32_bf16 v[88:91], v[36:39], a[220:223], v[88:91]\n"
    "s_waitcnt lgkmcnt(7)\n"
    "v_mfma_f32_16x16x32_bf16 v[80:83], v[44:47], a[224:227], v[80:83]\n"
    "s_waitcnt lgkmcnt(6)\n"
    "v_mfma_f32_16x16x32_bf16 v[84:87], v[48:51], a[224:227], v[84:87]\n"
    "v_mfma_f32_16x16x32_bf16 v[92:95], v[48:51], a[228:231], v[92:95]\n"
    "v_mfma_f32_16x16x32_bf16 v[88:91], v[44:47], a[228:231], v[88:91]\n"
    "s_waitcnt lgkmcnt(5)\n"
    "v_mfma_f32_16x16x32_bf16 v[80:83], v[52:55], a[232:235], v[80:83]\n"
    "s_waitcnt lgkmcnt(4)\n"
    "v_mfma_f32_16x16x32_bf16 v[84:87], v[56:59], a[232:235], v[84:87]\n"
    "v_mfma_f32_16x16x32_bf16 v[92:95], v[56:59], a[236:239], v[92:95]\n"
    "v_mfma_f32_16x16x32_bf16 v[88:91], v[52:55], a[236:239], v[88:91]\n"
    "s_waitcnt lgkmcnt(3)\n"
    "v_mfma_f32_16x16x32_bf16 v[80:83], v[20:23], a[240:243], v[80:83]\n"
    "s_waitcnt lgkmcnt(2)\n"
    "v_mfma_f32_16x16x32_bf16 v[84:87], v[24:27], a[240:243], v[84:87]\n"
    "v_mfma_f32_16x16x32_bf16 v[92:95], v[24:27], a[244:247], v[92:95]\n"
    "v_mfma_f32_16x16x32_bf16 v[88:91], v[20:23], a[244:247], v[88:91]\n"
    "s_waitcnt lgkmcnt(1)\n"
    "v_mfma_f32_16x16x32_bf16 v[80:83], v[28:31], a[248:251], v[80:83]\n"
    "s_waitcnt lgkmcnt(0)\n"
    "v_mfma_f32_16x16x32_bf16 v[84:87], v[32:35], a[248:251], v[84:87]\n"
    "v_mfma_f32_16x16x32_bf16 v[92:95], v[32:35], a[252:255], v[92:95]\n"
    "v_mfma_f32_16x16x32_bf16 v[88:91], v[28:31], a[252:255], v[88:91]\n"
    "v_cvt_pk_bf16_f32 v12, v80, v81\n"
    "v_cvt_pk_bf16_f32 v13, v82, v83\n"
    "v_cvt_pk_bf16_f32 v14, v84, v85\n"
    "v_cvt_pk_bf16_f32 v15, v86, v87\n"
    "buffer_store_dwordx4 v[12:15], v0, s[16:19], s59 offen sc1\n"
    "s_nop 2\n"
    "v_cvt_pk_bf16_f32 v16, v88, v89\n"
    "v_cvt_pk_bf16_f32 v17, v90, v91\n"
    "v_cvt_pk_bf16_f32 v18, v92, v93\n"
    "v_cvt_pk_bf16_f32 v19, v94, v95\n"
    "buffer_store_dwordx4 v[16:19], v0, s[16:19], s62 offen sc1\n"
    "s_mov_b32 s52, s64\n"
    "v_mov_b32 v5, s67\n"
    "LHE%=:\n"
    "s_cmp_ge_u32 s40, 64\n"
    "s_cbranch_scc1 LT%=\n"
    "s_cmp_lg_u32 s60, 2\n"
    "s_cbranch_scc1 LT%=\n"
    "L1%=:\n"
    "v_add_u32 v8, s43, v1\n"
    "s_xor_b32 s43, s43, 33792\n"
    "s_mov_b32 s63, s45\n"
    "s_lshr_b32 s38, s63, 8\n"
    "s_xor_b32 s38, s38, s48\n"
    "s_and_b32 s38, s38, 0x2000\n"
    "s_or_b32 s63, s63, s38\n"
    "s_add_u32 s39, s44, s43\n"
    "s_add_u32 s69, s63, 4096\n"
    "s_add_u32 m0, s39, 0\n"
    "s_nop 0\n"
    "buffer_load_dwordx4 v0, s[16:19], s63 offen offset:0 sc1 lds\n"
    "s_add_u32 m0, s39, 32\n"
    "s_nop 0\n"
    "buffer_load_dwordx4 v0, s[16:19], s63 offen offset:1024 sc1 lds\n"
    "s_add_u32 m0, s39, 64\n"
    "s_nop 0\n"
    "buffer_load_dwordx4 v0, s[16:19], s63 offen offset:2048 sc1 lds\n"
    "s_add_u32 m0, s39, 96\n"
    "s_nop 0\n"
    "buffer_load_dwordx4 v0, s[16:19], s63 offen offset:3072 sc1 lds\n"
    "s_add_u32 m0, s39, 4224\n"
    "s_nop 0\n"
    "buffer_load_dwordx4 v0, s[16:19], s69 offen offset:0 sc1 lds\n"
    "s_add_u32 m0, s39, 4256\n"
    "s_nop 0\n"
    "buffer_load_dwordx4 v0, s[16:19], s69 offen offset:1024 sc1 lds\n"
    "s_add_u32 m0, s39, 4288\n"
    "s_nop 0\n"
    "buffer_load_dwordx4 v0, s[16:19], s69 offen offset:2048 sc1 lds\n"
    "s_add_u32 m0, s39, 4320\n"
    "s_nop 0\n"
    "buffer_load_dwordx4 v0, s[16:19], s69 offen offset:3072 sc1 lds\n"
    "s_add_u32 s45, s45, 131072\n"
    "s_mul_i32 s59, s40, 2621440\n"
    "s_add_u32 s59, s59, s47\n"
    "s_lshr_b32 s38, s59, 8\n"
    "s_xor_b32 s38, s38, s48\n"
    "s_and_b32 s38, s38, 0x2000\n"
    "s_or_b32 s59, s59, s38\n"
    "s_lshl_b32 s64, s40, 7\n"
    "s_add_u32 s64, s64, s50\n"
    "s_mov_b32 s67, 1\n"
    "s_add_u32 s40, s40, 2\n"
    "s_cmp_eq_u32 s61, 1\n"
    "s_cbranch_scc1 LIV%=\n"
    "s_add_u32 s62, s59, 4096\n"
    "ds_read_b128 v[20:23], v8 offset:0\n"
    "ds_read_b128 v[24:27], v8 offset:16896\n"
    "ds_read_b128 v[28:31], v8 offset:64\n"
    "ds_read_b128 v[32:35], v8 offset:16960\n"
    "ds_read_b128 v[36:39], v8 offset:128\n"
    "ds_read_b128 v[40:43], v8 offset:17024\n"
    "ds_read_b128 v[44:47], v8 offset:192\n"
    "ds_read_b128 v[48:51], v8 offset:17088\n"
    "s_waitcnt lgkmcnt(7)\n"
    "v_mfma_f32_16x16x32_bf16 v[64:67], a[0:3], v[20:23], 0\n"
    "ds_read_b128 v[52:55], v8 offset:256\n"
    "s_waitcnt lgkmcnt(7)\n"
    "v_mfma_f32_16x16x32_bf16 v[68:71], a[0:3], v[24:27], 0\n"
    "ds_read_b128 v[56:59], v8 offset:17152\n"
    "v_mfma_f32_16x16x32_bf16 v[76:79], a[4:7], v[24:27], 0\n"
    "v_mfma_f32_16x16x32_bf16 v[72:75], a[4:7], v[20:23], 0\n"
    "s_waitcnt lgkmcnt(7)\n"
    "v_mfma_f32_16x16x32_bf16 v[64:67], a[8:11], v[28:31], v[64:67]\n"
    "ds_read_b128 v[20:23], v8 offset:320\n"
    "s_waitcnt lgkmcnt(7)\n"
    "v_mfma_f32_16x16x32_bf16 v[68:71], a[8:11], v[32:35], v[68:71]\n"
    "ds_read_b128 v[24:27], v8 offset:17216\n"
    "v_mfma_f32_16x16x32_bf16 v[76:79], a[12:15], v[32:35], v[76:79]\n"
    "v_mfma_f32_16x16x32_bf16 v[72:75], a[12:15], v[28:31], v[72:75]\n"
    "s_waitcnt lgkmcnt(7)\n"
    "v_mfma_f32_16x16x32_bf16 v[64:67], a[16:19], v[36:39], v[64:67]\n"
    "ds_read_b128 v[28:31], v8 offset:384\n"
    "s_waitcnt lgkmcnt(7)\n"
    "v_mfma_f32_16x16x32_bf16 v[68:71], a[16:19], v[40:43], v[68:71]\n"
    "ds_read_b128 v[32:35], v8 offset:17280\n"
    "v_mfma_f32_16x16x32_bf16 v[76:79], a[20:23], v[40:43], v[76:79]\n"
    "v_mfma_f32_16x16x32_bf16 v[72:75], a[20:23], v[36:39], v[72:75]\n"
    "s_waitcnt lgkmcnt(7)\n"
    "v_mfma_f32_16x16x32_bf16 v[64:67], a[24:27], v[44:47], v[64:67]\n"
    "ds_read_b128 v[36:39], v8 offset:448\n"
    "s_waitcnt lgkmcnt(7)\n"
    "v_mfma_f32_16x16x32_bf16 v[68:71], a[24:27], v[48:51], v[68:71]\n"
    "ds_read_b128 v[40:43], v8 offset:17344\n"
    "v_mfma_f32_16x16x32_bf16 v[76:79], a[28:31], v[48:51], v[76:79]\n"
    "v_mfma_f32_16x16x32_bf16 v[72:75], a[28:31], v[44:47], v[72:75]\n"
    "s_waitcnt lgkmcnt(7)\n"
    "v_mfma_f32_16x16x32_bf16 v[64:67], a[32:35], v[52:55], v[64:67]\n"
    "ds_read_b128 v[44:47], v8 offset:512\n"
    "s_waitcnt lgkmcnt(7)\n"
    "v_mfma_f32_16x16x32_bf16 v[68:71], a[32:35], v[56:59], v[68:71]\n"
    "ds_read_b128 v[48:51], v8 offset:17408\n"
    "v_mfma_f32_16x16x32_bf16 v[76:79], a[36:39], v[56:59], v[76:79]\n"
    "v_mfma_f32_16x16x32_bf16 v[72:75], a[36:39], v[52:55], v[72:75]\n"
    "s_waitcnt lgkmcnt(7)\n"
    "v_mfma_f32_16x16x32_bf16 v[64:67], a[40:43], v[20:23], v[64:67]\n"
    "ds_read_b128 v[52:55], v8 offset:576\n"
    "s_waitcnt lgkmcnt(7)\n"
    "v_mfma_f32_16x16x32_bf16 v[68:71], a[40:43], v[24:27], v[68:71]\n"
    "ds_read_b128 v[56:59], v8 offset:17472\n"
    "v_mfma_f32_16x16x32_bf16 v[76:79], a[44:47], v[24:27], v[76:79]\n"
    "v_mfma_f32_16x16x32_bf16 v[72:75], a[44:47], v[20:23], v[72:75]\n"
    "s_waitcnt lgkmcnt(7)\n"
    "v_mfma_f32_16x16x32_bf16 v[64:67], a[48:51], v[28:31], v[64:67]\n"
    "ds_read_b128 v[20:23], v8 offset:640\n"
    "s_waitcnt lgkmcnt(7)\n"
    "v_mfma_f32_16x16x32_bf16 v[68:71], a[48:51], v[32:35], v[68:71]\n"
    "ds_read_b128 v[24:27], v8 offset:17536\n"
    "v_mfma_f32_16x16x32_bf16 v[76:79], a[52:55], v[32:35], v[76:79]\n"
    "v_mfma_f32_16x16x32_bf16 v[72:75], a[52:55], v[28:31], v[72:75]\n"
    "s_waitcnt lgkmcnt(7)\n"
    "v_mfma_f32_16x16x32_bf16 v[64:67], a[56:59], v[36:39], v[64:67]\n"
    "ds_read_b128 v[28:31], v8 offset:704\n"
    "s_waitcnt lgkmcnt(7)\n"
    "v_mfma_f32_16x16x32_bf16 v[68:71], a[56:59], v[40:43], v[68:71]\n"
    "ds_read_b128 v[32:35], v8 offset:17600\n"
    "v_mfma_f32_16x16x32_bf16 v[76:79], a[60:63], v[40:43], v[76:79]\n"
    "v_mfma_f32_16x16x32_bf16 v[72:75], a[60:63], v[36:39], v[72:75]\n"
    "s_waitcnt lgkmcnt(7)\n"
    "v_mfma_f32_16x16x32_bf16 v[64:67], a[64:67], v[44:47], v[64:67]\n"
    "ds_read_b128 v[36:39], v8 offset:768\n"
    "s_waitcnt lgkmcnt(7)\n"
    "v_mfma_f32_16x16x32_bf16 v[68:71], a[64:67], v[48:51], v[68:71]\n"
    "ds_read_b128 v[40:43], v8 offset:17664\n"
    "v_mfma_f32_16x16x32_bf16 v[76:79], a[68:71], v[48:51], v[76:79]\n"
    "v_mfma_f32_16x16x32_bf16 v[72:75], a[68:71], v[44:47], v[72:75]\n"
    "s_waitcnt lgkmcnt(7)\n"
    "v_mfma_f32_16x16x32_bf16 v[64:67], a[72:75], v[52:55], v[64:67]\n"
    "ds_read_b128 v[44:47], v8 offset:832\n"
    "s_waitcnt lgkmcnt(7)\n"
    "v_mfma_f32_16x16x32_bf16 v[68:71], a[72:75], v[56:59], v[68:71]\n"
    "ds_read_b128 v[48:51], v8 offset:17728\n"
    "v_mfma_f32_16x16x32_bf16 v[76:79], a[76:79], v[56:59], v[76:79]\n"
    "v_mfma_f32_16x16x32_bf16 v[72:75], a[76:79], v[52:55], v[72:75]\n"
    "s_waitcnt lgkmcnt(7)\n"
    "v_mfma_f32_16x16x32_bf16 v[64:67], a[80:83], v[20:23], v[64:67]\n"
    "ds_read_b128 v[52:55], v8 offset:896\n"
    "s_waitcnt lgkmcnt(7)\n"
    "v_mfma_f32_16x16x32_bf16 v[68:71], a[80:83], v[24:27], v[68:71]\n"
    "ds_read_b128 v[56:59], v8 offset:17792\n"
    "v_mfma_f32_16x16x32_bf16 v[76:79], a[84:87], v[24:27], v[76:79]\n"
    "v_mfma_f32_16x16x32_bf16 v[72:75], a[84:87], v[20:23], v[72:75]\n"
    "s_waitcnt lgkmcnt(7)\n"
    "v_mfma_f32_16x16x32_bf16 v[64:67], a[88:91], v[28:31], v[64:67]\n"
    "ds_read_b128 v[20:23], v8 offset:960\n"
    "s_waitcnt lgkmcnt(7)\n"
    "v_mfma_f32_16x16x32_bf16 v[68:71], a[88:91], v[32:35], v[68:71]\n"
    "ds_read_b128 v[24:27], v8 offset:17856\n"
    "v_mfma_f32_16x16x32_bf16 v[76:79], a[92:95], v[32:35], v[76:79]\n"
    "v_mfma_f32_16x16x32_bf16 v[72:75], a[92:95], v[28:31], v[72:75]\n"
    "s_waitcnt lgkmcnt(7)\n"
    "v_mfma_f32_16x16x32_bf16 v[64:67], a[96:99], v[36:39], v[64:67]\n"
    "ds_read_b128 v[28:31], v8 offset:0\n"
    "s_waitcnt lgkmcnt(7)\n"
    "v_mfma_f32_16x16x32_bf16 v[68:71], a[96:99], v[40:43], v[68:71]\n"
    "ds_read_b128 v[32:35], v8 offset:16896\n"
    "s_waitcnt vmcnt(8)\n"
    "s_barrier\n"
    "s_or_b32 s52, s52, s68\n"
    "buffer_store_dword v5, v9, s[16:19], s52 offen sc1\n"
    "s_mov_b32 s52, 0x80000000\n"
    "v_mfma_f32_16x16x32_bf16 v[76:79], a[100:103], v[40:43], v[76:79]\n"
    "v_mfma_f32_16x16x32_bf16 v[72:75], a[100:103], v[36:39], v[72:75]\n"
    "s_waitcnt lgkmcnt(7)\n"
    "v_mfma_f32_16x16x32_bf16 v[64:67], a[104:107], v[44:47], v[64:67]\n"
    "ds_read_b128 v[36:39], v8 offset:64\n"
    "s_waitcnt lgkmcnt(7)\n"
    "v_mfma_f32_16x16x32_bf16 v[68:71], a[104:107], v[48:51], v[68:71]\n"
    "ds_read_b128 v[40:43], v8 offset:16960\n"
    "v_mfma_f32_16x16x32_bf16 v[76:79], a[108:111], v[48:51], v[76:79]\n"
    "v_mfma_f32_16x16x32_bf16 v[72:75], a[108:111], v[44:47], v[72:75]\n"
    "s_waitcnt lgkmcnt(7)\n"
    "v_mfma_f32_16x16x32_bf16 v[64:67], a[112:115], v[52:55], v[64:67]\n"
    "ds_read_b128 v[44:47], v8 offset:128\n"
    "s_waitcnt lgkmcnt(7)\n"
    "v_mfma_f32_16x16x32_bf16 v[68:71], a[112:115], v[56:59], v[68:71]\n"
    "ds_read_b128 v[48:51], v8 offset:17024\n"
    "v_mfma_f32_16x16x32_bf16 v[76:79], a[116:119], v[56:59], v[76:79]\n"
    "v_mfma_f32_16x16x32_bf16 v[72:75], a[116:119], v[52:55], v[72:75]\n"
    "s_waitcnt lgkmcnt(7)\n"
    "v_mfma_f32_16x16x32_bf16 v[64:67], a[120:123], v[20:23], v[64:67]\n"
    "ds_read_b128 v[52:55], v8 offset:192\n"
    "s_waitcnt lgkmcnt(7)\n"
    "v_mfma_f32_16x16x32_bf16 v[68:71], a[120:123], v[24:27], v[68:71]\n"
    "ds_read_b128 v[56:59], v8 offset:17088\n"
    "v_mfma_f32_16x16x32_bf16 v[76:79], a[124:127], v[24:27], v[76:79]\n"
    "v_mfma_f32_16x16x32_bf16 v[72:75], a[124:127], v[20:23], v[72:75]\n"
    "s_waitcnt lgkmcnt(7)\n"
    "v_mfma_f32_16x16x32_bf16 v[80:83], a[128:131], v[28:31], 0\n"
    "ds_read_b128 v[20:23], v8 offset:256\n"
    "s_waitcnt lgkmcnt(7)\n"
    "v_mfma_f32_16x16x32_bf16 v[84:87], a[128:131], v[32:35], 0\n"
    "ds_read_b128 v[24:27], v8 offset:17152\n"
    "buffer_load_dword v6, off, s[16:19], s54 sc1\n"
    "buffer_load_dword v7, off, s[16:19], s53 sc1\n"
    "v_mfma_f32_16x16x32_bf16 v[92:95], a[132:135], v[32:35], 0\n"
    "v_mfma_f32_16x16x32_bf16 v[88:91], a[132:135], v[28:31], 0\n"
    "s_waitcnt lgkmcnt(7)\n"
    "v_mfma_f32_16x16x32_bf16 v[80:83], a[136:139], v[36:39], v[80:83]\n"
    "ds_read_b128 v[28:31], v8 offset:320\n"
    "s_waitcnt lgkmcnt(7)\n"
    "v_mfma_f32_16x16x32_bf16 v[84:87], a[136:139], v[40:43], v[84:87]\n"
    "ds_read_b128 v[32:35], v8 offset:17216\n"
    "v_cvt_pk_bf16_f32 v12, v64, v72\n"
    "v_mfma_f32_16x16x32_bf16 v[92:95], a[140:143], v[40:43], v[92:95]\n"
    "v_cvt_pk_bf16_f32 v13, v65, v73\n"
    "v_mfma_f32_16x16x32_bf16 v[88:91], a[140:143], v[36:39], v[88:91]\n"
    "s_waitcnt lgkmcnt(7)\n"
    "v_cvt_pk_bf16_f32 v14, v66, v74\n"
    "v_mfma_f32_16x16x32_bf16 v[80:83], a[144:147], v[44:47], v[80:83]\n"
    "ds_read_b128 v[36:39], v8 offset:384\n"
    "s_waitcnt lgkmcnt(7)\n"
    "v_mfma_f32_16x16x32_bf16 v[84:87], a[144:147], v[48:51], v[84:87]\n"
    "ds_read_b128 v[40:43], v8 offset:17280\n"
    "v_cvt_pk_bf16_f32 v15, v67, v75\n"
    "v_mfma_f32_16x16x32_bf16 v[92:95], a[148:151], v[48:51], v[92:95]\n"
    "buffer_store_dwordx4 v[12:15], v0, s[16:19], s59 offen sc1\n"
    "v_mfma_f32_16x16x32_bf16 v[88:91], a[148:151], v[44:47], v[88:91]\n"
    "s_waitcnt lgkmcnt(7)\n"
    "v_cvt_pk_bf16_f32 v16, v68, v76\n"
    "v_mfma_f32_16x16x32_bf16 v[80:83], a[152:155], v[52:55], v[80:83]\n"
    "ds_read_b128 v[44:47], v8 offset:448\n"
    "s_waitcnt lgkmcnt(7)\n"
    "v_mfma_f32_16x16x32_bf16 v[84:87], a[152:155], v[56:59], v[84:87]\n"
    "ds_read_b128 v[48:51], v8 offset:17344\n"
    "v_cvt_pk_bf16_f32 v17, v69, v77\n"
    "v_mfma_f32_16x16x32_bf16 v[92:95], a[156:159], v[56:59], v[92:95]\n"
    "v_cvt_pk_bf16_f32 v18, v70, v78\n"
    "v_mfma_f32_16x16x32_bf16 v[88:91], a[156:159], v[52:55], v[88:91]\n"
    "s_waitcnt lgkmcnt(7)\n"
    "v_cvt_pk_bf16_f32 v19, v71, v79\n"
    "v_mfma_f32_16x16x32_bf16 v[80:83], a[160:163], v[20:23], v[80:83]\n"
    "ds_read_b128 v[52:55], v8 offset:512\n"
    "s_waitcnt lgkmcnt(7)\n"
    "v_mfma_f32_16x16x32_bf16 v[84:87], a[160:163], v[24:27], v[84:87]\n"
    "ds_read_b128 v[56:59], v8 offset:17408\n"
    "buffer_store_dwordx4 v[16:19], v0, s[16:19], s62 offen sc1\n"
    "v_mfma_f32_16x16x32_bf16 v[92:95], a[164:167], v[24:27], v[92:95]\n"
    "s_add_u32 s59, s59, 16384\n"
    "s_add_u32 s62, s62, 16384\n"
    "v_mfma_f32_16x16x32_bf16 v[88:91], a[164:167], v[20:23], v[88:91]\n"
    "s_waitcnt lgkmcnt(7)\n"
    "v_mfma_f32_16x16x32_bf16 v[80:83], a[168:171], v[28:31], v[80:83]\n"
    "ds_read_b128 v[20:23], v8 offset:576\n"
    "s_waitcnt lgkmcnt(7)\n"
    "v_mfma_f32_16x16x32_bf16 v[84:87], a[168:171], v[32:35], v[84:87]\n"
    "ds_read_b128 v[24:27], v8 offset:17472\n"
    "v_mfma_f32_16x16x32_bf16 v[92:95], a[172:175], v[32:35], v[92:95]\n"
    "v_mfma_f32_16x16x32_bf16 v[88:91], a[172:175], v[28:31], v[88:91]\n"
    "s_waitcnt lgkmcnt(7)\n"
    "v_mfma_f32_16x16x32_bf16 v[80:83], a[176:179], v[36:39], v[80:83]\n"
    "ds_read_b128 v[28:31], v8 offset:640\n"
    "s_waitcnt lgkmcnt(7)\n"
    "v_mfma_f32_16x16x32_bf16 v[84:87], a[176:179], v[40:43], v[84:87]\n"
    "ds_read_b128 v[32:35], v8 offset:17536\n"
    "v_mfma_f32_16x16x32_bf16 v[92:95], a[180:183], v[40:43], v[92:95]\n"
    "v_mfma_f32_16x16x32_bf16 v[88:91], a[180:183], v[36:39], v[88:91]\n"
    "s_waitcnt lgkmcnt(7)\n"
    "v_mfma_f32_16x16x32_bf16 v[80:83], a[184:187], v[44:47], v[80:83]\n"
    "ds_read_b128 v[36:39], v8 offset:704\n"
    "s_waitcnt lgkmcnt(7)\n"
    "v_mfma_f32_16x16x32_bf16 v[84:87], a[184:187], v[48:51], v[84:87]\n"
    "ds_read_b128 v[40:43], v8 offset:17600\n"
    "s_waitcnt vmcnt(2)\n"
    "v_readfirstlane_b32 s60, v6\n"
    "v_cmp_eq_u32 vcc, 0, v7\n"
    "s_cmp_gt_u32 s60, 3\n"
    "s_cselect_b32 vcc_hi, 1, 0\n"
    "s_cmp_ge_u32 s41, 64\n"
    "s_cselect_b32 vcc_hi, 1, vcc_hi\n"
    "s_or_b32 s60, vcc_lo, vcc_hi\n"
    "v_mfma_f32_16x16x32_bf16 v[92:95], a[188:191], v[48:51], v[92:95]\n"
    "v_mov_b32 v6, s60\n"
    "ds_write_b64 v11, v[6:7] offset:64\n"
    "v_mfma_f32_16x16x32_bf16 v[88:91], a[188:191], v[44:47], v[88:91]\n"
    "s_waitcnt lgkmcnt(8)\n"
    "v_mfma_f32_16x16x32_bf16 v[80:83], a[192:195], v[52:55], v[80:83]\n"
    "ds_read_b128 v[44:47], v8 offset:768\n"
    "s_waitcnt lgkmcnt(8)\n"
    "v_mfma_f32_16x16x32_bf16 v[84:87], a[192:195], v[56:59], v[84:87]\n"
    "ds_read_b128 v[48:51], v8 offset:17664\n"
    "v_mfma_f32_16x16x32_bf16 v[92:95], a[196:199], v[56:59], v[92:95]\n"
    "v_mfma_f32_16x16x32_bf16 v[88:91], a[196:199], v[52:55], v[88:91]\n"
    "s_waitcnt lgkmcnt(8)\n"
    "v_mfma_f32_16x16x32_bf16 v[80:83], a[200:203], v[20:23], v[80:83]\n"
    "ds_read_b128 v[52:55], v8 offset:832\n"
    "s_waitcnt lgkmcnt(8)\n"
    "v_mfma_f32_16x16x32_bf16 v[84:87], a[200:203], v[24:27], v[84:87]\n"
    "ds_read_b128 v[56:59], v8 offset:17728\n"
    "s_waitcnt lgkmcnt(2)\n"
    "s_barrier\n"
    "ds_read_b64 v[6:7], v10 offset:64\n"
    "v_mfma_f32_16x16x32_bf16 v[92:95], a[204:207], v[24:27], v[92:95]\n"
    "v_mfma_f32_16x16x32_bf16 v[88:91], a[204:207], v[20:23], v[88:91]\n"
    "v_mfma_f32_16x16x32_bf16 v[80:83], a[208:211], v[28:31], v[80:83]\n"
    "ds_read_b128 v[20:23], v8 offset:896\n"
    "v_mfma_f32_16x16x32_bf16 v[84:87], a[208:211], v[32:35], v[84:87]\n"
    "ds_read_b128 v[24:27], v8 offset:17792\n"
    "s_lshl_b32 s63, s41, 16\n"
    "s_add_u32 s63, s63, s46\n"
    "s_lshr_b32 s38, s63, 8\n"
    "s_xor_b32 s38, s38, s49\n"
    "s_and_b32 s38, s38, 0x2000\n"
    "s_or_b32 s63, s63, s38\n"
    "v_mfma_f32_16x16x32_bf16 v[92:95], a[212:215], v[32:35], v[92:95]\n"
    "v_mfma_f32_16x16x32_bf16 v[88:91], a[212:215], v[28:31], v[88:91]\n"
    "s_waitcnt lgkmcnt(2)\n"
    "v_readfirstlane_b32 s60, v6\n"
    "v_readfirstlane_b32 s66, v7\n"
    "s_cmp_eq_u32 s60, 0\n"
    "s_cbranch_scc1 LIKA%=\n"
    "v_mfma_f32_16x16x32_bf16 v[80:83], a[216:219], v[36:39], v[80:83]\n"
    "ds_read_b128 v[28:31], v8 offset:960\n"
    "v_mfma_f32_16x16x32_bf16 v[84:87], a[216:219], v[40:43], v[84:87]\n"
    "ds_read_b128 v[32:35], v8 offset:17856\n"
    "v_mfma_f32_16x16x32_bf16 v[92:95], a[220:223], v[40:43], v[92:95]\n"
    "v_mfma_f32_16x16x32_bf16 v[88:91], a[220:223], v[36:39], v[88:91]\n"
    "v_mfma_f32_16x16x32_bf16 v[80:83], a[224:227], v[44:47], v[80:83]\n"
    "v_mfma_f32_16x16x32_bf16 v[84:87], a[224:227], v[48:51], v[84:87]\n"
    "v_mfma_f32_16x16x32_bf16 v[92:95], a[228:231], v[48:51], v[92:95]\n"
    "v_mfma_f32_16x16x32_bf16 v[88:91], a[228:231], v[44:47], v[88:91]\n"
    "v_mfma_f32_16x16x32_bf16 v[80:83], a[232:235], v[52:55], v[80:83]\n"
    "v_mfma_f32_16x16x32_bf16 v[84:87], a[232:235], v[56:59], v[84:87]\n"
    "v_mfma_f32_16x16x32_bf16 v[92:95], a[236:239], v[56:59], v[92:95]\n"
    "v_mfma_f32_16x16x32_bf16 v[88:91], a[236:239], v[52:55], v[88:91]\n"
    "s_waitcnt lgkmcnt(3)\n"
    "v_mfma_f32_16x16x32_bf16 v[80:83], a[240:243], v[20:23], v[80:83]\n"
    "s_waitcnt lgkmcnt(2)\n"
    "v_mfma_f32_16x16x32_bf16 v[84:87], a[240:243], v[24:27], v[84:87]\n"
    "v_mfma_f32_16x16x32_bf16 v[92:95], a[244:247], v[24:27], v[92:95]\n"
    "v_mfma_f32_16x16x32_bf16 v[88:91], a[244:247], v[20:23], v[88:91]\n"
    "s_waitcnt lgkmcnt(1)\n"
    "v_mfma_f32_16x16x32_bf16 v[80:83], a[248:251], v[28:31], v[80:83]\n"
    "s_waitcnt lgkmcnt(0)\n"
    "v_mfma_f32_16x16x32_bf16 v[84:87], a[248:251], v[32:35], v[84:87]\n"
    "v_mfma_f32_16x16x32_bf16 v[92:95], a[252:255], v[32:35], v[92:95]\n"
    "v_mfma_f32_16x16x32_bf16 v[88:91], a[252:255], v[28:31], v[88:91]\n"
    "s_branch LIKB%=\n"
    "LIKA%=:\n"
    "v_mfma_f32_16x16x32_bf16 v[80:83], a[216:219], v[36:39], v[80:83]\n"
    "ds_read_b128 v[28:31], v8 offset:960\n"
    "v_mfma_f32_16x16x32_bf16 v[84:87], a[216:219], v[40:43], v[84:87]\n"
    "ds_read_b128 v[32:35], v8 offset:17856\n"
    "s_add_u32 s39, s44, 67584\n"
    "s_add_u32 s69, s63, 4096\n"
    "v_mfma_f32_16x16x32_bf16 v[92:95], a[220:223], v[40:43], v[92:95]\n"
    "s_add_u32 m0, s39, 0\n"
    "s_nop 0\n"
    "buffer_load_dwordx4 v0, s[16:19], s63 offen offset:0 sc1 lds\n"
    "v_mfma_f32_16x16x32_bf16 v[88:91], a[220:223], v[36:39], v[88:91]\n"
    "v_mfma_f32_16x16x32_bf16 v[80:83], a[224:227], v[44:47], v[80:83]\n"
    "v_mfma_f32_16x16x32_bf16 v[84:87], a[224:227], v[48:51], v[84:87]\n"
    "s_add_u32 m0, s39, 32\n"
    "s_nop 0\n"
    "buffer_load_dwordx4 v0, s[16:19], s63 offen offset:1024 sc1 lds\n"
    "v_mfma_f32_16x16x32_bf16 v[92:95], a[228:231], v[48:51], v[92:95]\n"
    "s_add_u32 m0, s39, 64\n"
    "s_nop 0\n"
    "buffer_load_dwordx4 v0, s[16:19], s63 offen offset:2048 sc1 lds\n"
    "v_mfma_f32_16x16x32_bf16 v[88:91], a[228:231], v[44:47], v[88:91]\n"
    "v_mfma_f32_16x16x32_bf16 v[80:83], a[232:235], v[52:55], v[80:83]\n"
    "v_mfma_f32_16x16x32_bf16 v[84:87], a[232:235], v[56:59], v[84:87]\n"
    "s_add_u32 m0, s39, 96\n"
    "s_nop 0\n"
    "buffer_load_dwordx4 v0, s[16:19], s63 offen offset:3072 sc1 lds\n"
    "v_mfma_f32_16x16x32_bf16 v[92:95], a[236:239], v[56:59], v[92:95]\n"
    "s_add_u32 m0, s39, 4224\n"
    "s_nop 0\n"
    "buffer_load_dwordx4 v0, s[16:19], s69 offen offset:0 sc1 lds\n"
    "v_mfma_f32_16x16x32_bf16 v[88:91], a[236:239], v[52:55], v[88:91]\n"
    "s_waitcnt lgkmcnt(3)\n"
    "v_mfma_f32_16x16x32_bf16 v[80:83], a[240:243], v[20:23], v[80:83]\n"
    "s_waitcnt lgkmcnt(2)\n"
    "v_mfma_f32_16x16x32_bf16 v[84:87], a[240:243], v[24:27], v[84:87]\n"
    "s_add_u32 m0, s39, 4256\n"
    "s_nop 0\n"
    "buffer_load_dwordx4 v0, s[16:19], s69 offen offset:1024 sc1 lds\n"
    "v_mfma_f32_16x16x32_bf16 v[92:95], a[244:247], v[24:27], v[92:95]\n"
    "s_add_u32 m0, s39, 4288\n"
    "s_nop 0\n"
    "buffer_load_dwordx4 v0, s[16:19], s69 offen offset:2048 sc1 lds\n"
    "v_mfma_f32_16x16x32_bf16 v[88:91], a[244:247], v[20:23], v[88:91]\n"
    "s_waitcnt lgkmcnt(1)\n"
    "v_mfma_f32_16x16x32_bf16 v[80:83], a[248:251], v[28:31], v[80:83]\n"
    "s_waitcnt lgkmcnt(0)\n"
    "v_mfma_f32_16x16x32_bf16 v[84:87], a[248:251], v[32:35], v[84:87]\n"
    "s_add_u32 m0, s39, 4320\n"
    "s_nop 0\n"
    "buffer_load_dwordx4 v0, s[16:19], s69 offen offset:3072 sc1 lds\n"
    "v_mfma_f32_16x16x32_bf16 v[92:95], a[252:255], v[32:35], v[92:95]\n"
    "v_mfma_f32_16x16x32_bf16 v[88:91], a[252:255], v[28:31], v[88:91]\n"
    "LIKB%=:\n"
    "s_nop 7\n"
    "v_cvt_pk_bf16_f32 v12, v80, v88\n"
    "v_cvt_pk_bf16_f32 v13, v81, v89\n"
    "v_cvt_pk_bf16_f32 v14, v82, v90\n"
    "v_cvt_pk_bf16_f32 v15, v83, v91\n"
    "buffer_store_dwordx4 v[12:15], v0, s[16:19], s59 offen sc1\n"
    "v_cvt_pk_bf16_f32 v16, v84, v92\n"
    "v_cvt_pk_bf16_f32 v17, v85, v93\n"
    "v_cvt_pk_bf16_f32 v18, v86, v94\n"
    "v_cvt_pk_bf16_f32 v19, v87, v95\n"
    "buffer_store_dwordx4 v[16:19], v0, s[16:19], s62 offen sc1\n"
    "s_mov_b32 s52, s64\n"
    "v_mov_b32 v5, s67\n"
    "s_branch LIE%=\n"
    "LIV%=:\n"
    "s_add_u32 s62, s59, 1024\n"
    "ds_read_b128 v[20:23], v8 offset:0\n"
    "ds_read_b128 v[24:27], v8 offset:16896\n"
    "ds_read_b128 v[28:31], v8 offset:64\n"
    "ds_read_b128 v[32:35], v8 offset:16960\n"
    "ds_read_b128 v[36:39], v8 offset:128\n"
    "ds_read_b128 v[40:43], v8 offset:17024\n"
    "ds_read_b128 v[44:47], v8 offset:192\n"
    "ds_read_b128 v[48:51], v8 offset:17088\n"
    "s_waitcnt lgkmcnt(7)\n"
    "v_mfma_f32_16x16x32_bf16 v[64:67], v[20:23], a[0:3], 0\n"
    "ds_read_b128 v[52:55], v8 offset:256\n"
    "s_waitcnt lgkmcnt(7)\n"
    "v_mfma_f32_16x16x32_bf16 v[68:71], v[24:27], a[0:3], 0\n"
    "ds_read_b128 v[56:59], v8 offset:17152\n"
    "v_mfma_f32_16x16x32_bf16 v[76:79], v[24:27], a[4:7], 0\n"
    "v_mfma_f32_16x16x32_bf16 v[72:75], v[20:23], a[4:7], 0\n"
    "s_waitcnt lgkmcnt(7)\n"
    "v_mfma_f32_16x16x32_bf16 v[64:67], v[28:31], a[8:11], v[64:67]\n"
    "ds_read_b128 v[20:23], v8 offset:320\n"
    "s_waitcnt lgkmcnt(7)\n"
    "v_mfma_f32_16x16x32_bf16 v[68:71], v[32:35], a[8:11], v[68:71]\n"
    "ds_read_b128 v[24:27], v8 offset:17216\n"
    "v_mfma_f32_16x16x32_bf16 v[76:79], v[32:35], a[12:15], v[76:79]\n"
    "v_mfma_f32_16x16x32_bf16 v[72:75], v[28:31], a[12:15], v[72:75]\n"
    "s_waitcnt lgkmcnt(7)\n"
    "v_mfma_f32_16x16x32_bf16 v[64:67], v[36:39], a[16:19], v[64:67]\n"
    "ds_read_b128 v[28:31], v8 offset:384\n"
    "s_waitcnt lgkmcnt(7)\n"
    "v_mfma_f32_16x16x32_bf16 v[68:71], v[40:43], a[16:19], v[68:71]\n"
    "ds_read_b128 v[32:35], v8 offset:17280\n"
    "v_mfma_f32_16x16x32_bf16 v[76:79], v[40:43], a[20:23], v[76:79]\n"
    "v_mfma_f32_16x16x32_bf16 v[72:75], v[36:39], a[20:23], v[72:75]\n"
    "s_waitcnt lgkmcnt(7)\n"
    "v_mfma_f32_16x16x32_bf16 v[64:67], v[44:47], a[24:27], v[64:67]\n"
    "ds_read_b128 v[36:39], v8 offset:448\n"
    "s_waitcnt lgkmcnt(7)\n"
    "v_mfma_f32_16x16x32_bf16 v[68:71], v[48:51], a[24:27], v[68:71]\n"
    "ds_read_b128 v[40:43], v8 offset:17344\n"
    "v_mfma_f32_16x16x32_bf16 v[76:79], v[48:51], a[28:31], v[76:79]\n"
    "v_mfma_f32_16x16x32_bf16 v[72:75], v[44:47], a[28:31], v[72:75]\n"
    "s_waitcnt lgkmcnt(7)\n"
    "v_mfma_f32_16x16x32_bf16 v[64:67], v[52:55], a[32:35], v[64:67]\n"
    "ds_read_b128 v[44:47], v8 offset:512\n"
    "s_waitcnt lgkmcnt(7)\n"
    "v_mfma_f32_16x16x32_bf16 v[68:71], v[56:59], a[32:35], v[68:71]\n"
    "ds_read_b128 v[48:51], v8 offset:17408\n"
    "v_mfma_f32_16x16x32_bf16 v[76:79], v[56:59], a[36:39], v[76:79]\n"
    "v_mfma_f32_16x16x32_bf16 v[72:75], v[52:55], a[36:39], v[72:75]\n"
    "s_waitcnt lgkmcnt(7)\n"
    "v_mfma_f32_16x16x32_bf16 v[64:67], v[20:23], a[40:43], v[64:67]\n"
    "ds_read_b128 v[52:55], v8 offset:576\n"
    "s_waitcnt lgkmcnt(7)\n"
    "v_mfma_f32_16x16x32_bf16 v[68:71], v[24:27], a[40:43], v[68:71]\n"
    "ds_read_b128 v[56:59], v8 offset:17472\n"
    "v_mfma_f32_16x16x32_bf16 v[76:79], v[24:27], a[44:47], v[76:79]\n"
    "v_mfma_f32_16x16x32_bf16 v[72:75], v[20:23], a[44:47], v[72:75]\n"
    "s_waitcnt lgkmcnt(7)\n"
    "v_mfma_f32_16x16x32_bf16 v[64:67], v[28:31], a[48:51], v[64:67]\n"
    "ds_read_b128 v[20:23], v8 offset:640\n"
    "s_waitcnt lgkmcnt(7)\n"
    "v_mfma_f32_16x16x32_bf16 v[68:71], v[32:35], a[48:51], v[68:71]\n"
    "ds_read_b128 v[24:27], v8 offset:17536\n"
    "v_mfma_f32_16x16x32_bf16 v[76:79], v[32:35], a[52:55], v[76:79]\n"
    "v_mfma_f32_16x16x32_bf16 v[72:75], v[28:31], a[52:55], v[72:75]\n"
    "s_waitcnt lgkmcnt(7)\n"
    "v_mfma_f32_16x16x32_bf16 v[64:67], v[36:39], a[56:59], v[64:67]\n"
    "ds_read_b128 v[28:31], v8 offset:704\n"
    "s_waitcnt lgkmcnt(7)\n"
    "v_mfma_f32_16x16x32_bf16 v[68:71], v[40:43], a[56:59], v[68:71]\n"
    "ds_read_b128 v[32:35], v8 offset:17600\n"
    "v_mfma_f32_16x16x32_bf16 v[76:79], v[40:43], a[60:63], v[76:79]\n"
    "v_mfma_f32_16x16x32_bf16 v[72:75], v[36:39], a[60:63], v[72:75]\n"
    "s_waitcnt lgkmcnt(7)\n"
    "v_mfma_f32_16x16x32_bf16 v[64:67], v[44:47], a[64:67], v[64:67]\n"
    "ds_read_b128 v[36:39], v8 offset:768\n"
    "s_waitcnt lgkmcnt(7)\n"
    "v_mfma_f32_16x16x32_bf16 v[68:71], v[48:51], a[64:67], v[68:71]\n"
    "ds_read_b128 v[40:43], v8 offset:17664\n"
    "v_mfma_f32_16x16x32_bf16 v[76:79], v[48:51], a[68:71], v[76:79]\n"
    "v_mfma_f32_16x16x32_bf16 v[72:75], v[44:47], a[68:71], v[72:75]\n"
    "s_waitcnt lgkmcnt(7)\n"
    "v_mfma_f32_16x16x32_bf16 v[64:67], v[52:55], a[72:75], v[64:67]\n"
    "ds_read_b128 v[44:47], v8 offset:832\n"
    "s_waitcnt lgkmcnt(7)\n"
    "v_mfma_f32_16x16x32_bf16 v[68:71], v[56:59], a[72:75], v[68:71]\n"
    "ds_read_b128 v[48:51], v8 offset:17728\n"
    "v_mfma_f32_16x16x32_bf16 v[76:79], v[56:59], a[76:79], v[76:79]\n"
    "v_mfma_f32_16x16x32_bf16 v[72:75], v[52:55], a[76:79], v[72:75]\n"
    "s_waitcnt lgkmcnt(7)\n"
    "v_mfma_f32_16x16x32_bf16 v[64:67], v[20:23], a[80:83], v[64:67]\n"
    "ds_read_b128 v[52:55], v8 offset:896\n"
    "s_waitcnt lgkmcnt(7)\n"
    "v_mfma_f32_16x16x32_bf16 v[68:71], v[24:27], a[80:83], v[68:71]\n"
    "ds_read_b128 v[56:59], v8 offset:17792\n"
    "v_mfma_f32_16x16x32_bf16 v[76:79], v[24:27], a[84:87], v[76:79]\n"
    "v_mfma_f32_16x16x32_bf16 v[72:75], v[20:23], a[84:87], v[72:75]\n"
    "s_waitcnt lgkmcnt(7)\n"
    "v_mfma_f32_16x16x32_bf16 v[64:67], v[28:31], a[88:91], v[64:67]\n"
    "ds_read_b128 v[20:23], v8 offset:960\n"
    "s_waitcnt lgkmcnt(7)\n"
    "v_mfma_f32_16x16x32_bf16 v[68:71], v[32:35], a[88:91], v[68:71]\n"
    "ds_read_b128 v[24:27], v8 offset:17856\n"
    "v_mfma_f32_16x16x32_bf16 v[76:79], v[32:35], a[92:95], v[76:79]\n"
    "v_mfma_f32_16x16x32_bf16 v[72:75], v[28:31], a[92:95], v[72:75]\n"
    "s_waitcnt lgkmcnt(7)\n"
    "v_mfma_f32_16x16x32_bf16 v[64:67], v[36:39], a[96:99], v[64:67]\n"
    "ds_read_b128 v[28:31], v8 offset:0\n"
    "s_waitcnt lgkmcnt(7)\n"
    "v_mfma_f32_16x16x32_bf16 v[68:71], v[40:43], a[96:99], v[68:71]\n"
    "ds_read_b128 v[32:35], v8 offset:16896\n"
    "s_waitcnt vmcnt(8)\n"
    "s_barrier\n"
    "s_or_b32 s52, s52, s68\n"
    "buffer_store_dword v5, v9, s[16:19], s52 offen sc1\n"
    "s_mov_b32 s52, 0x80000000\n"
    "v_mfma_f32_16x16x32_bf16 v[76:79], v[40:43], a[100:103], v[76:79]\n"
    "v_mfma_f32_16x16x32_bf16 v[72:75], v[36:39], a[100:103], v[72:75]\n"
    "s_waitcnt lgkmcnt(7)\n"
    "v_mfma_f32_16x16x32_bf16 v[64:67], v[44:47], a[104:107], v[64:67]\n"
    "ds_read_b128 v[36:39], v8 offset:64\n"
    "s_waitcnt lgkmcnt(7)\n"
    "v_mfma_f32_16x16x32_bf16 v[68:71], v[48:51], a[104:107], v[68:71]\n"
    "ds_read_b128 v[40:43], v8 offset:16960\n"
    "v_mfma_f32_16x16x32_bf16 v[76:79], v[48:51], a[108:111], v[76:79]\n"
    "v_mfma_f32_16x16x32_bf16 v[72:75], v[44:47], a[108:111], v[72:75]\n"
    "s_waitcnt lgkmcnt(7)\n"
    "v_mfma_f32_16x16x32_bf16 v[64:67], v[52:55], a[112:115], v[64:67]\n"
    "ds_read_b128 v[44:47], v8 offset:128\n"
    "s_waitcnt lgkmcnt(7)\n"
    "v_mfma_f32_16x16x32_bf16 v[68:71], v[56:59], a[112:115], v[68:71]\n"
    "ds_read_b128 v[48:51], v8 offset:17024\n"
    "v_mfma_f32_16x16x32_bf16 v[76:79], v[56:59], a[116:119], v[76:79]\n"
    "v_mfma_f32_16x16x32_bf16 v[72:75], v[52:55], a[116:119], v[72:75]\n"
    "s_waitcnt lgkmcnt(7)\n"
    "v_mfma_f32_16x16x32_bf16 v[64:67], v[20:23], a[120:123], v[64:67]\n"
    "ds_read_b128 v[52:55], v8 offset:192\n"
    "s_waitcnt lgkmcnt(7)\n"
    "v_mfma_f32_16x16x32_bf16 v[68:71], v[24:27], a[120:123], v[68:71]\n"
    "ds_read_b128 v[56:59], v8 offset:17088\n"
    "v_mfma_f32_16x16x32_bf16 v[76:79], v[24:27], a[124:127], v[76:79]\n"
    "v_mfma_f32_16x16x32_bf16 v[72:75], v[20:23], a[124:127], v[72:75]\n"
    "s_waitcnt lgkmcnt(7)\n"
    "v_mfma_f32_16x16x32_bf16 v[80:83], v[28:31], a[128:131], 0\n"
    "ds_read_b128 v[20:23], v8 offset:256\n"
    "s_waitcnt lgkmcnt(7)\n"
    "v_mfma_f32_16x16x32_bf16 v[84:87], v[32:35], a[128:131], 0\n"
    "ds_read_b128 v[24:27], v8 offset:17152\n"
    "buffer_load_dword v6, off, s[16:19], s54 sc1\n"
    "buffer_load_dword v7, off, s[16:19], s53 sc1\n"
    "v_mfma_f32_16x16x32_bf16 v[92:95], v[32:35], a[132:135], 0\n"
    "v_mfma_f32_16x16x32_bf16 v[88:91], v[28:31], a[132:135], 0\n"
    "s_waitcnt lgkmcnt(7)\n"
    "v_mfma_f32_16x16x32_bf16 v[80:83], v[36:39], a[136:139], v[80:83]\n"
    "ds_read_b128 v[28:31], v8 offset:320\n"
    "s_waitcnt lgkmcnt(7)\n"
    "v_mfma_f32_16x16x32_bf16 v[84:87], v[40:43], a[136:139], v[84:87]\n"
    "ds_read_b128 v[32:35], v8 offset:17216\n"
    "v_cvt_pk_bf16_f32 v12, v64, v65\n"
    "v_mfma_f32_16x16x32_bf16 v[92:95], v[40:43], a[140:143], v[92:95]\n"
    "v_cvt_pk_bf16_f32 v13, v66, v67\n"
    "v_mfma_f32_16x16x32_bf16 v[88:91], v[36:39], a[140:143], v[88:91]\n"
    "s_waitcnt lgkmcnt(7)\n"
    "v_cvt_pk_bf16_f32 v14, v68, v69\n"
    "v_mfma_f32_16x16x32_bf16 v[80:83], v[44:47], a[144:147], v[80:83]\n"
    "ds_read_b128 v[36:39], v8 offset:384\n"
    "s_waitcnt lgkmcnt(7)\n"
    "v_mfma_f32_16x16x32_bf16 v[84:87], v[48:51], a[144:147], v[84:87]\n"
    "ds_read_b128 v[40:43], v8 offset:17280\n"
    "v_cvt_pk_bf16_f32 v15, v70, v71\n"
    "v_mfma_f32_16x16x32_bf16 v[92:95], v[48:51], a[148:151], v[92:95]\n"
    "buffer_store_dwordx4 v[12:15], v0, s[16:19], s59 offen sc1\n"
    "v_mfma_f32_16x16x32_bf16 v[88:91], v[44:47], a[148:151], v[88:91]\n"
    "s_waitcnt lgkmcnt(7)\n"
    "v_cvt_pk_bf16_f32 v16, v72, v73\n"
    "v_mfma_f32_16x16x32_bf16 v[80:83], v[52:55], a[152:155], v[80:83]\n"
    "ds_read_b128 v[44:47], v8 offset:448\n"
    "s_waitcnt lgkmcnt(7)\n"
    "v_mfma_f32_16x16x32_bf16 v[84:87], v[56:59], a[152:155], v[84:87]\n"
    "ds_read_b128 v[48:51], v8 offset:17344\n"
    "v_cvt_pk_bf16_f32 v17, v74, v75\n"
    "v_mfma_f32_16x16x32_bf16 v[92:95], v[56:59], a[156:159], v[92:95]\n"
    "v_cvt_pk_bf16_f32 v18, v76, v77\n"
    "v_mfma_f32_16x16x32_bf16 v[88:91], v[52:55], a[156:159], v[88:91]\n"
    "s_waitcnt lgkmcnt(7)\n"
    "v_cvt_pk_bf16_f32 v19, v78, v79\n"
    "v_mfma_f32_16x16x32_bf16 v[80:83], v[20:23], a[160:163], v[80:83]\n"
    "ds_read_b128 v[52:55], v8 offset:512\n"
    "s_waitcnt lgkmcnt(7)\n"
    "v_mfma_f32_16x16x32_bf16 v[84:87], v[24:27], a[160:163], v[84:87]\n"
    "ds_read_b128 v[56:59], v8 offset:17408\n"
    "buffer_store_dwordx4 v[16:19], v0, s[16:19], s62 offen sc1\n"
    "v_mfma_f32_16x16x32_bf16 v[92:95], v[24:27], a[164:167], v[92:95]\n"
    "s_add_u32 s59, s59, 16384\n"
    "s_add_u32 s62, s62, 16384\n"
    "v_mfma_f32_16x16x32_bf16 v[88:91], v[20:23], a[164:167], v[88:91]\n"
    "s_waitcnt lgkmcnt(7)\n"
    "v_mfma_f32_16x16x32_bf16 v[80:83], v[28:31], a[168:171], v[80:83]\n"
    "ds_read_b128 v[20:23], v8 offset:576\n"
    "s_waitcnt lgkmcnt(7)\n"
    "v_mfma_f32_16x16x32_bf16 v[84:87], v[32:35], a[168:171], v[84:87]\n"
    "ds_read_b128 v[24:27], v8 offset:17472\n"
    "v_mfma_f32_16x16x32_bf16 v[92:95], v[32:35], a[172:175], v[92:95]\n"
    "v_mfma_f32_16x16x32_bf16 v[88:91], v[28:31], a[172:175], v[88:91]\n"
    "s_waitcnt lgkmcnt(7)\n"
    "v_mfma_f32_16x16x32_bf16 v[80:83], v[36:39], a[176:179], v[80:83]\n"
    "ds_read_b128 v[28:31], v8 offset:640\n"
    "s_waitcnt lgkmcnt(7)\n"
    "v_mfma_f32_16x16x32_bf16 v[84:87], v[40:43], a[176:179], v[84:87]\n"
    "ds_read_b128 v[32:35], v8 offset:17536\n"
    "v_mfma_f32_16x16x32_bf16 v[92:95], v[40:43], a[180:183], v[92:95]\n"
    "v_mfma_f32_16x16x32_bf16 v[88:91], v[36:39], a[180:183], v[88:91]\n"
    "s_waitcnt lgkmcnt(7)\n"
    "v_mfma_f32_16x16x32_bf16 v[80:83], v[44:47], a[184:187], v[80:83]\n"
    "ds_read_b128 v[36:39], v8 offset:704\n"
    "s_waitcnt lgkmcnt(7)\n"
    "v_mfma_f32_16x16x32_bf16 v[84:87], v[48:51], a[184:187], v[84:87]\n"
    "ds_read_b128 v[40:43], v8 offset:17600\n"
    "s_waitcnt vmcnt(2)\n"
    "v_readfirstlane_b32 s60, v6\n"
    "v_cmp_eq_u32 vcc, 0, v7\n"
    "s_cmp_gt_u32 s60, 3\n"
    "s_cselect_b32 vcc_hi, 1, 0\n"
    "s_cmp_ge_u32 s41, 64\n"
    "s_cselect_b32 vcc_hi, 1, vcc_hi\n"
    "s_or_b32 s60, vcc_lo, vcc_hi\n"
    "v_mfma_f32_16x16x32_bf16 v[92:95], v[48:51], a[188:191], v[92:95]\n"
    "v_mov_b32 v6, s60\n"
    "ds_write_b64 v11, v[6:7] offset:64\n"
    "v_mfma_f32_16x16x32_bf16 v[88:91], v[44:47], a[188:191], v[88:91]\n"
    "s_waitcnt lgkmcnt(8)\n"
    "v_mfma_f32_16x16x32_bf16 v[80:83], v[52:55], a[192:195], v[80:83]\n"
    "ds_read_b128 v[44:47], v8 offset:768\n"
    "s_waitcnt lgkmcnt(8)\n"
    "v_mfma_f32_16x16x32_bf16 v[84:87], v[56:59], a[192:195], v[84:87]\n"
    "ds_read_b128 v[48:51], v8 offset:17664\n"
    "v_mfma_f32_16x16x32_bf16 v[92:95], v[56:59], a[196:199], v[92:95]\n"
    "v_mfma_f32_16x16x32_bf16 v[88:91], v[52:55], a[196:199], v[88:91]\n"
    "s_waitcnt lgkmcnt(8)\n"
    "v_mfma_f32_16x16x32_bf16 v[80:83], v[20:23], a[200:203], v[80:83]\n"
    "ds_read_b128 v[52:55], v8 offset:832\n"
    "s_waitcnt lgkmcnt(8)\n"
    "v_mfma_f32_16x16x32_bf16 v[84:87], v[24:27], a[200:203], v[84:87]\n"
    "ds_read_b128 v[56:59], v8 offset:17728\n"
    "s_waitcnt lgkmcnt(2)\n"
    "s_barrier\n"
    "ds_read_b64 v[6:7], v10 offset:64\n"
    "v_mfma_f32_16x16x32_bf16 v[92:95], v[24:27], a[204:207], v[92:95]\n"
    "v_mfma_f32_16x16x32_bf16 v[88:91], v[20:23], a[204:207], v[88:91]\n"
    "v_mfma_f32_16x16x32_bf16 v[80:83], v[28:31], a[208:211], v[80:83]\n"
    "ds_read_b128 v[20:23], v8 offset:896\n"
    "v_mfma_f32_16x16x32_bf16 v[84:87], v[32:35], a[208:211], v[84:87]\n"
    "ds_read_b128 v[24:27], v8 offset:17792\n"
    "s_lshl_b32 s63, s41, 16\n"
    "s_add_u32 s63, s63, s46\n"
    "s_lshr_b32 s38, s63, 8\n"
    "s_xor_b32 s38, s38, s49\n"
    "s_and_b32 s38, s38, 0x2000\n"
    "s_or_b32 s63, s63, s38\n"
    "v_mfma_f32_16x16x32_bf16 v[92:95], v[32:35], a[212:215], v[92:95]\n"
    "v_mfma_f32_16x16x32_bf16 v[88:91], v[28:31], a[212:215], v[88:91]\n"
    "s_waitcnt lgkmcnt(2)\n"
    "v_readfirstlane_b32 s60, v6\n"
    "v_readfirstlane_b32 s66, v7\n"
    "s_cmp_eq_u32 s60, 0\n"
    "s_cbranch_scc1 LITA%=\n"
    "v_mfma_f32_16x16x32_bf16 v[80:83], v[36:39], a[216:219], v[80:83]\n"
    "ds_read_b128 v[28:31], v8 offset:960\n"
    "v_mfma_f32_16x16x32_bf16 v[84:87], v[40:43], a[216:219], v[84:87]\n"
    "ds_read_b128 v[32:35], v8 offset:17856\n"
    "v_mfma_f32_16x16x32_bf16 v[92:95], v[40:43], a[220:223], v[92:95]\n"
    "v_mfma_f32_16x16x32_bf16 v[88:91], v[36:39], a[220:223], v[88:91]\n"
    "v_mfma_f32_16x16x32_bf16 v[80:83], v[44:47], a[224:227], v[80:83]\n"
    "v_mfma_f32_16x16x32_bf16 v[84:87], v[48:51], a[224:227], v[84:87]\n"
    "v_mfma_f32_16x16x32_bf16 v[92:95], v[48:51], a[228:231], v[92:95]\n"
    "v_mfma_f32_16x16x32_bf16 v[88:91], v[44:47], a[228:231], v[88:91]\n"
    "v_mfma_f32_16x16x32_bf16 v[80:83], v[52:55], a[232:235], v[80:83]\n"
    "v_mfma_f32_16x16x32_bf16 v[84:87], v[56:59], a[232:235], v[84:87]\n"
    "v_mfma_f32_16x16x32_bf16 v[92:95], v[56:59], a[236:239], v[92:95]\n"
    "v_mfma_f32_16x16x32_bf16 v[88:91], v[52:55], a[236:239], v[88:91]\n"
    "s_waitcnt lgkmcnt(3)\n"
    "v_mfma_f32_16x16x32_bf16 v[80:83], v[20:23], a[240:243], v[80:83]\n"
    "s_waitcnt lgkmcnt(2)\n"
    "v_mfma_f32_16x16x32_bf16 v[84:87], v[24:27], a[240:243], v[84:87]\n"
    "v_mfma_f32_16x16x32_bf16 v[92:95], v[24:27], a[244:247], v[92:95]\n"
    "v_mfma_f32_16x16x32_bf16 v[88:91], v[20:23], a[244:247], v[88:91]\n"
    "s_waitcnt lgkmcnt(1)\n"
    "v_mfma_f32_16x16x32_bf16 v[80:83], v[28:31], a[248:251], v[80:83]\n"
    "s_waitcnt lgkmcnt(0)\n"
    "v_mfma_f32_16x16x32_bf16 v[84:87], v[32:35], a[248:251], v[84:87]\n"
    "v_mfma_f32_16x16x32_bf16 v[92:95], v[32:35], a[252:255], v[92:95]\n"
    "v_mfma_f32_16x16x32_bf16 v[88:91], v[28:31], a[252:255], v[88:91]\n"
    "s_branch LITB%=\n"
    "LITA%=:\n"
    "v_mfma_f32_16x16x32_bf16 v[80:83], v[36:39], a[216:219], v[80:83]\n"
    "ds_read_b128 v[28:31], v8 offset:960\n"
    "v_mfma_f32_16x16x32_bf16 v[84:87], v[40:43], a[216:219], v[84:87]\n"
    "ds_read_b128 v[32:35], v8 offset:17856\n"
    "s_add_u32 s39, s44, 67584\n"
    "s_add_u32 s69, s63, 4096\n"
    "v_mfma_f32_16x16x32_bf16 v[92:95], v[40:43], a[220:223], v[92:95]\n"
    "s_add_u32 m0, s39, 0\n"
    "s_nop 0\n"
    "buffer_load_dwordx4 v0, s[16:19], s63 offen offset:0 sc1 lds\n"
    "v_mfma_f32_16x16x32_bf16 v[88:91], v[36:39], a[220:223], v[88:91]\n"
    "v_mfma_f32_16x16x32_bf16 v[80:83], v[44:47], a[224:227], v[80:83]\n"
    "v_mfma_f32_16x16x32_bf16 v[84:87], v[48:51], a[224:227], v[84:87]\n"
    "s_add_u32 m0, s39, 32\n"
    "s_nop 0\n"
    "buffer_load_dwordx4 v0, s[16:19], s63 offen offset:1024 sc1 lds\n"
    "v_mfma_f32_16x16x32_bf16 v[92:95], v[48:51], a[228:231], v[92:95]\n"
    "s_add_u32 m0, s39, 64\n"
    "s_nop 0\n"
    "buffer_load_dwordx4 v0, s[16:19], s63 offen offset:2048 sc1 lds\n"
    "v_mfma_f32_16x16x32_bf16 v[88:91], v[44:47], a[228:231], v[88:91]\n"
    "v_mfma_f32_16x16x32_bf16 v[80:83], v[52:55], a[232:235], v[80:83]\n"
    "v_mfma_f32_16x16x32_bf16 v[84:87], v[56:59], a[232:235], v[84:87]\n"
    "s_add_u32 m0, s39, 96\n"
    "s_nop 0\n"
    "buffer_load_dwordx4 v0, s[16:19], s63 offen offset:3072 sc1 lds\n"
    "v_mfma_f32_16x16x32_bf16 v[92:95], v[56:59], a[236:239], v[92:95]\n"
    "s_add_u32 m0, s39, 4224\n"
    "s_nop 0\n"
    "buffer_load_dwordx4 v0, s[16:19], s69 offen offset:0 sc1 lds\n"
    "v_mfma_f32_16x16x32_bf16 v[88:91], v[52:55], a[236:239], v[88:91]\n"
    "s_waitcnt lgkmcnt(3)\n"
    "v_mfma_f32_16x16x32_bf16 v[80:83], v[20:23], a[240:243], v[80:83]\n"
    "s_waitcnt lgkmcnt(2)\n"
    "v_mfma_f32_16x16x32_bf16 v[84:87], v[24:27], a[240:243], v[84:87]\n"
    "s_add_u32 m0, s39, 4256\n"
    "s_nop 0\n"
    "buffer_load_dwordx4 v0, s[16:19], s69 offen offset:1024 sc1 lds\n"
    "v_mfma_f32_16x16x32_bf16 v[92:95], v[24:27], a[244:247], v[92:95]\n"
    "s_add_u32 m0, s39, 4288\n"
    "s_nop 0\n"
    "buffer_load_dwordx4 v0, s[16:19], s69 offen offset:2048 sc1 lds\n"
    "v_mfma_f32_16x16x32_bf16 v[88:91], v[20:23], a[244:247], v[88:91]\n"
    "s_waitcnt lgkmcnt(1)\n"
    "v_mfma_f32_16x16x32_bf16 v[80:83], v[28:31], a[248:251], v[80:83]\n"
    "s_waitcnt lgkmcnt(0)\n"
    "v_mfma_f32_16x16x32_bf16 v[84:87], v[32:35], a[248:251], v[84:87]\n"
    "s_add_u32 m0, s39, 4320\n"
    "s_nop 0\n"
    "buffer_load_dwordx4 v0, s[16:19], s69 offen offset:3072 sc1 lds\n"
    "v_mfma_f32_16x16x32_bf16 v[92:95], v[32:35], a[252:255], v[92:95]\n"
    "v_mfma_f32_16x16x32_bf16 v[88:91], v[28:31], a[252:255], v[88:91]\n"
    "LITB%=:\n"
    "s_nop 7\n"
    "v_cvt_pk_bf16_f32 v12, v80, v81\n"
    "v_cvt_pk_bf16_f32 v13, v82, v83\n"
    "v_cvt_pk_bf16_f32 v14, v84, v85\n"
    "v_cvt_pk_bf16_f32 v15, v86, v87\n"
    "buffer_store_dwordx4 v[12:15], v0, s[16:19], s59 offen sc1\n"
    "v_cvt_pk_bf16_f32 v16, v88, v89\n"
    "v_cvt_pk_bf16_f32 v17, v90, v91\n"
    "v_cvt_pk_bf16_f32 v18, v92, v93\n"
    "v_cvt_pk_bf16_f32 v19, v94, v95\n"
    "buffer_store_dwordx4 v[16:19], v0, s[16:19], s62 offen sc1\n"
    "s_mov_b32 s52, s64\n"
    "v_mov_b32 v5, s67\n"
    "LIE%=:\n"
    "s_cmp_eq_u32 s60, 0\n"
    "s_cbranch_scc1 L2%=\n"
    "s_branch LT%=\n"
    "LX%=:\n"
    "s_barrier\n"
    "s_cmp_eq_u32 s60, 0\n"
    "s_cbranch_scc1 LT%=\n"
    "s_cmp_eq_u32 s60, 3\n"
    "s_cbranch_scc1 LE%=\n"
    "s_sub_u32 s60, s60, 4\n"
    "s_lshr_b32 s64, s60, 5\n"
    "s_add_u32 s54, s54, 4\n"
    "s_xor_b32 s38, s64, s42\n"
    "s_bitcmp1_b32 s38, 0\n"
    "s_cbranch_scc1 LT%=\n"
    "s_lshl_b32 s63, s64, 16\n"
    "s_add_u32 s63, s63, s46\n"
    "s_lshr_b32 s38, s63, 8\n"
    "s_xor_b32 s38, s38, s49\n"
    "s_and_b32 s38, s38, 0x2000\n"
    "s_or_b32 s63, s63, s38\n"
    "s_lshr_b32 s66, s64, 3\n"
    "s_lshl_b32 s66, s66, 3\n"
    "s_add_u32 s66, s66, s64\n"
    "s_lshl_b32 s66, s66, 10\n"
    "s_add_u32 s66, s66, s55\n"
    "s_and_b32 s73, s60, 31\n"
    "s_lshl_b32 s38, s36, 3\n"
    "s_sub_u32 s73, s73, s38\n"
    "s_add_u32 s39, s44, 67584\n"
    "s_add_u32 s69, s63, 4096\n"
    "s_sub_u32 s38, s66, 0\n"
    "s_cmp_eq_u32 s73, 0\n"
    "s_cselect_b32 s38, s38, s63\n"
    "s_add_u32 m0, s39, 0\n"
    "s_nop 0\n"
    "buffer_load_dwordx4 v0, s[16:19], s38 offen offset:0 sc1 lds\n"
    "s_sub_u32 s38, s66, 1024\n"
    "s_cmp_eq_u32 s73, 1\n"
    "s_cselect_b32 s38, s38, s63\n"
    "s_add_u32 m0, s39, 32\n"
    "s_nop 0\n"
    "buffer_load_dwordx4 v0, s[16:19], s38 offen offset:1024 sc1 lds\n"
    "s_sub_u32 s38, s66, 2048\n"
    "s_cmp_eq_u32 s73, 2\n"
    "s_cselect_b32 s38, s38, s63\n"
    "s_add_u32 m0, s39, 64\n"
    "s_nop 0\n"
    "buffer_load_dwordx4 v0, s[16:19], s38 offen offset:2048 sc1 lds\n"
    "s_sub_u32 s38, s66, 3072\n"
    "s_cmp_eq_u32 s73, 3\n"
    "s_cselect_b32 s38, s38, s63\n"
    "s_add_u32 m0, s39, 96\n"
    "s_nop 0\n"
    "buffer_load_dwordx4 v0, s[16:19], s38 offen offset:3072 sc1 lds\n"
    "s_sub_u32 s38, s66, 0\n"
    "s_cmp_eq_u32 s73, 4\n"
    "s_cselect_b32 s38, s38, s69\n"
    "s_add_u32 m0, s39, 4224\n"
    "s_nop 0\n"
    "buffer_load_dwordx4 v0, s[16:19], s38 offen offset:0 sc1 lds\n"
    "s_sub_u32 s38, s66, 1024\n"
    "s_cmp_eq_u32 s73, 5\n"
    "s_cselect_b32 s38, s38, s69\n"
    "s_add_u32 m0, s39, 4256\n"
    "s_nop 0\n"
    "buffer_load_dwordx4 v0, s[16:19], s38 offen offset:1024 sc1 lds\n"
    "s_sub_u32 s38, s66, 2048\n"
    "s_cmp_eq_u32 s73, 6\n"
    "s_cselect_b32 s38, s38, s69\n"
    "s_add_u32 m0, s39, 4288\n"
    "s_nop 0\n"
    "buffer_load_dwordx4 v0, s[16:19], s38 offen offset:2048 sc1 lds\n"
    "s_sub_u32 s38, s66, 3072\n"
    "s_cmp_eq_u32 s73, 7\n"
    "s_cselect_b32 s38, s38, s69\n"
    "s_add_u32 m0, s39, 4320\n"
    "s_nop 0\n"
    "buffer_load_dwordx4 v0, s[16:19], s38 offen offset:3072 sc1 lds\n"
    "s_cmp_lt_u32 s64, 8\n"
    "s_cbranch_scc1 LQ%=\n"
    "s_lshl_b32 s63, s64, 2\n"
    "s_add_u32 s63, s63, s57\n"
    "s_mov_b32 s37, 0x80000\n"
    "LR%=:\n"
    "buffer_load_dword v6, v4, s[16:19], s63 offen sc1\n"
    "s_waitcnt vmcnt(0)\n"
    "v_cmp_eq_u32 vcc, 0, v6\n"
    "s_cbranch_vccz LQ%=\n"
    "s_sleep 4\n"
    "s_sub_u32 s37, s37, 1\n"
    "s_cbranch_scc0 LR%=\n"
    "LQ%=:\n"
    "s_and_b32 s59, s64, 7\n"
    "s_mul_i32 s59, s59, 1572864\n"
    "s_add_u32 s59, s59, s56\n"
    "s_lshr_b32 s38, s59, 8\n"
    "s_xor_b32 s38, s38, s49\n"
    "s_and_b32 s38, s38, 0x2000\n"
    "s_or_b32 s59, s59, s38\n"
    "s_lshl_b32 s64, s64, 7\n"
    "s_add_u32 s64, s64, s51\n"
    "s_add_u32 s64, s64, 64\n"
    "s_mov_b32 s67, 1\n"
    "s_mov_b32 s60, 4\n"
    "s_waitcnt vmcnt(0)\n"
    "s_branch LG%=\n"
    "LE%=:\n"
    "s_endpgm\n"
    :
    : "{v0}"(hv0), "{v1}"(hv1), "{v2}"(hv2), "{v3}"(hv3), "{v4}"(hv4), "{v5}"(hv5)
    : "v255", "a255", "s101", "vcc", "scc", "memory");
  __builtin_unreachable();
}
__device__ __forceinline__ void o_role8(int w, int V, int u, unsigned c1, char* ws, const u16* Wo) {
  const int lane = lane_id(), g = lane >> 4, q = (lane >> 2) & 3, p = lane & 3, hg = u >> 2, cs = u & 3;
  const unsigned me = w == 1 ? 0u : OOR;
  const int pv[14] = {w, (V == 1 ? 4 : 3) * 524288 + hg * 131072 + w * 2048, (int)(V ? c1 ^ 0x2000u : c1), (int)(c1 ^ 0x2000u), (int)nearof(Z_FO + V * 16384, c1 ^ 0x2000u) + hg * 2048, (int)((nearof(Z_FP + V * 16384, c1 ^ 0x2000u) + u * 256) | me),
                      (int)(Z_PART + V * MBY(16)) + u * 16384 + w * 2048, (int)Z_ERR, (int)(size_t)Wo, (int)((size_t)Wo >> 32) & 0xffff, (int)(size_t)ws, (int)((size_t)ws >> 32) & 0xffff, (int)ZWS_BYTES,
                      (int)((Z_DBG + MBY(2) + (V * 16 + u) * 1024) | me)};
  int hv0 = lane * 16, hv1 = 131072 + (lane & 31) * 272 + w * 64 + (lane >> 5) * 16, hv2 = 131072 + (8 * w + g) * 272 + (lane & 15) * 16;
  int hv3 = (hg * 1024 + w * 128 + g) * 1024 + cs * 256 + (lane & 15) * 16, hv4 = (4 * (g >> 1) + q) * 256 + 64 * w + 32 * (g & 1) + 16 * (p & 1) + 8 * (p >> 1), hv5 = 0;
#pragma unroll
  for (int i = 0; i < 14; ++i) hv5 = lane == i ? pv[i] : hv5;
  asm volatile(
    "s_nop 1\n"
    "v_readlane_b32 s36, v5, 0\n"
    "v_readlane_b32 s41, v5, 1\n"
    "v_readlane_b32 s42, v5, 2\n"
    "v_readlane_b32 s43, v5, 3\n"
    "v_readlane_b32 s44, v5, 4\n"
    "v_readlane_b32 s45, v5, 5\n"
    "v_readlane_b32 s46, v5, 6\n"
    "v_readlane_b32 s47, v5, 7\n"
    "v_readlane_b32 s20, v5, 8\n"
    "v_readlane_b32 s21, v5, 9\n"
    "v_readlane_b32 s16, v5, 10\n"
    "v_readlane_b32 s17, v5, 11\n"
    "v_readlane_b32 s18, v5, 12\n"
    "v_readlane_b32 s51, v5, 13\n"
    "v_mov_b32 v5, 1\n"
    "s_mov_b32 s19, 0x20000\n"
    "s_mov_b32 s22, 0x400000\n"
    "s_mov_b32 s23, 0x20000\n"
    "s_lshl_b32 s39, s36, 15\n"
    "s_lshl_b32 s49, s36, 11\n"
    "s_mov_b32 s40, 0\n"
    "s_add_u32 m0, s39, 0\n"
    "s_mov_b32 s38, 0\n"
    "buffer_load_dwordx4 v3, s[20:23], s38 offen lds\n"
    "s_add_u32 m0, s39, 1024\n"
    "s_mov_b32 s38, 4096\n"
    "buffer_load_dwordx4 v3, s[20:23], s38 offen lds\n"
    "s_add_u32 m0, s39, 2048\n"
    "s_mov_b32 s38, 8192\n"
    "buffer_load_dwordx4 v3, s[20:23], s38 offen lds\n"
    "s_add_u32 m0, s39, 3072\n"
    "s_mov_b32 s38, 12288\n"
    "buffer_load_dwordx4 v3, s[20:23], s38 offen lds\n"
    "s_add_u32 m0, s39, 4096\n"
    "s_mov_b32 s38, 16384\n"
    "buffer_load_dwordx4 v3, s[20:23], s38 offen lds\n"
    "s_add_u32 m0, s39, 5120\n"
    "s_mov_b32 s38, 20480\n"
    "buffer_load_dwordx4 v3, s[20:23], s38 offen lds\n"
    "s_add_u32 m0, s39, 6144\n"
    "s_mov_b32 s38, 24576\n"
    "buffer_load_dwordx4 v3, s[20:23], s38 offen lds\n"
    "s_add_u32 m0, s39, 7168\n"
    "s_mov_b32 s38, 28672\n"
    "buffer_load_dwordx4 v3, s[20:23], s38 offen lds\n"
    "s_add_u32 m0, s39, 8192\n"
    "s_mov_b32 s38, 32768\n"
    "buffer_load_dwordx4 v3, s[20:23], s38 offen lds\n"
    "s_add_u32 m0, s39, 9216\n"
    "s_mov_b32 s38, 36864\n"
    "buffer_load_dwordx4 v3, s[20:23], s38 offen lds\n"
    "s_add_u32 m0, s39, 10240\n"
    "s_mov_b32 s38, 40960\n"
    "buffer_load_dwordx4 v3, s[20:23], s38 offen lds\n"
    "s_add_u32 m0, s39, 11264\n"
    "s_mov_b32 s38, 45056\n"
    "buffer_load_dwordx4 v3, s[20:23], s38 offen lds\n"
    "s_add_u32 m0, s39, 12288\n"
    "s_mov_b32 s38, 49152\n"
    "buffer_load_dwordx4 v3, s[20:23], s38 offen lds\n"
    "s_add_u32 m0, s39, 13312\n"
    "s_mov_b32 s38, 53248\n"
    "buffer_load_dwordx4 v3, s[20:23], s38 offen lds\n"
    "s_add_u32 m0, s39, 14336\n"
    "s_mov_b32 s38, 57344\n"
    "buffer_load_dwordx4 v3, s[20:23], s38 offen lds\n"
    "s_add_u32 m0, s39, 15360\n"
    "s_mov_b32 s38, 61440\n"
    "buffer_load_dwordx4 v3, s[20:23], s38 offen lds\n"
    "s_add_u32 m0, s39, 16384\n"
    "s_mov_b32 s38, 65536\n"
    "buffer_load_dwordx4 v3, s[20:23], s38 offen lds\n"
    "s_add_u32 m0, s39, 17408\n"
    "s_mov_b32 s38, 69632\n"
    "buffer_load_dwordx4 v3, s[20:23], s38 offen lds\n"
    "s_add_u32 m0, s39, 18432\n"
    "s_mov_b32 s38, 73728\n"
    "buffer_load_dwordx4 v3, s[20:23], s38 offen lds\n"
    "s_add_u32 m0, s39, 19456\n"
    "s_mov_b32 s38, 77824\n"
    "buffer_load_dwordx4 v3, s[20:23], s38 offen lds\n"
    "s_add_u32 m0, s39, 20480\n"
    "s_mov_b32 s38, 81920\n"
    "buffer_load_dwordx4 v3, s[20:23], s38 offen lds\n"
    "s_add_u32 m0, s39, 21504\n"
    "s_mov_b32 s38, 86016\n"
    "buffer_load_dwordx4 v3, s[20:23], s38 offen lds\n"
    "s_add_u32 m0, s39, 22528\n"
    "s_mov_b32 s38, 90112\n"
    "buffer_load_dwordx4 v3, s[20:23], s38 offen lds\n"
    "s_add_u32 m0, s39, 23552\n"
    "s_mov_b32 s38, 94208\n"
    "buffer_load_dwordx4 v3, s[20:23], s38 offen lds\n"
    "s_add_u32 m0, s39, 24576\n"
    "s_mov_b32 s38, 98304\n"
    "buffer_load_dwordx4 v3, s[20:23], s38 offen lds\n"
    "s_add_u32 m0, s39, 25600\n"
    "s_mov_b32 s38, 102400\n"
    "buffer_load_dwordx4 v3, s[20:23], s38 offen lds\n"
    "s_add_u32 m0, s39, 26624\n"
    "s_mov_b32 s38, 106496\n"
    "buffer_load_dwordx4 v3, s[20:23], s38 offen lds\n"
    "s_add_u32 m0, s39, 27648\n"
    "s_mov_b32 s38, 110592\n"
    "buffer_load_dwordx4 v3, s[20:23], s38 offen lds\n"
    "s_add_u32 m0, s39, 28672\n"
    "s_mov_b32 s38, 114688\n"
    "buffer_load_dwordx4 v3, s[20:23], s38 offen lds\n"
    "s_add_u32 m0, s39, 29696\n"
    "s_mov_b32 s38, 118784\n"
    "buffer_load_dwordx4 v3, s[20:23], s38 offen lds\n"
    "s_add_u32 m0, s39, 30720\n"
    "s_mov_b32 s38, 122880\n"
    "buffer_load_dwordx4 v3, s[20:23], s38 offen lds\n"
    "s_add_u32 m0, s39, 31744\n"
    "s_mov_b32 s38, 126976\n"
    "buffer_load_dwordx4 v3, s[20:23], s38 offen lds\n"
    "s_waitcnt vmcnt(0)\n"
    "s_barrier\n"
    "v_add_u32 v7, 0, v4\n"
    "ds_read_b64_tr_b16 a[0:1], v7 offset:0\n"
    "ds_read_b64_tr_b16 a[2:3], v7 offset:4096\n"
    "ds_read_b64_tr_b16 a[4:5], v7 offset:2048\n"
    "ds_read_b64_tr_b16 a[6:7], v7 offset:6144\n"
    "ds_read_b64_tr_b16 a[8:9], v7 offset:8192\n"
    "ds_read_b64_tr_b16 a[10:11], v7 offset:12288\n"
    "ds_read_b64_tr_b16 a[12:13], v7 offset:10240\n"
    "ds_read_b64_tr_b16 a[14:15], v7 offset:14336\n"
    "ds_read_b64_tr_b16 a[16:17], v7 offset:16384\n"
    "ds_read_b64_tr_b16 a[18:19], v7 offset:20480\n"
    "ds_read_b64_tr_b16 a[20:21], v7 offset:18432\n"
    "ds_read_b64_tr_b16 a[22:23], v7 offset:22528\n"
    "ds_read_b64_tr_b16 a[24:25], v7 offset:24576\n"
    "ds_read_b64_tr_b16 a[26:27], v7 offset:28672\n"
    "ds_read_b64_tr_b16 a[28:29], v7 offset:26624\n"
    "s_waitcnt lgkmcnt(11)\n"
    "ds_read_b64_tr_b16 a[30:31], v7 offset:30720\n"
    "ds_read_b64_tr_b16 a[32:33], v7 offset:32768\n"
    "ds_read_b64_tr_b16 a[34:35], v7 offset:36864\n"
    "ds_read_b64_tr_b16 a[36:37], v7 offset:34816\n"
    "s_waitcnt lgkmcnt(11)\n"
    "ds_read_b64_tr_b16 a[38:39], v7 offset:38912\n"
    "ds_read_b64_tr_b16 a[40:41], v7 offset:40960\n"
    "ds_read_b64_tr_b16 a[42:43], v7 offset:45056\n"
    "ds_read_b64_tr_b16 a[44:45], v7 offset:43008\n"
    "s_waitcnt lgkmcnt(11)\n"
    "ds_read_b64_tr_b16 a[46:47], v7 offset:47104\n"
    "ds_read_b64_tr_b16 a[48:49], v7 offset:49152\n"
    "ds_read_b64_tr_b16 a[50:51], v7 offset:53248\n"
    "ds_read_b64_tr_b16 a[52:53], v7 offset:51200\n"
    "s_waitcnt lgkmcnt(11)\n"
    "ds_read_b64_tr_b16 a[54:55], v7 offset:55296\n"
    "ds_read_b64_tr_b16 a[56:57], v7 offset:57344\n"
    "ds_read_b64_tr_b16 a[58:59], v7 offset:61440\n"
    "ds_read_b64_tr_b16 a[60:61], v7 offset:59392\n"
    "s_waitcnt lgkmcnt(11)\n"
    "ds_read_b64_tr_b16 a[62:63], v7 offset:63488\n"
    "s_waitcnt lgkmcnt(0)\n"
    "v_add_u32 v7, 65536, v4\n"
    "ds_read_b64_tr_b16 a[64:65], v7 offset:0\n"
    "ds_read_b64_tr_b16 a[66:67], v7 offset:4096\n"
    "ds_read_b64_tr_b16 a[68:69], v7 offset:2048\n"
    "ds_read_b64_tr_b16 a[70:71], v7 offset:6144\n"
    "ds_read_b64_tr_b16 a[72:73], v7 offset:8192\n"
    "ds_read_b64_tr_b16 a[74:75], v7 offset:12288\n"
    "ds_read_b64_tr_b16 a[76:77], v7 offset:10240\n"
    "ds_read_b64_tr_b16 a[78:79], v7 offset:14336\n"
    "ds_read_b64_tr_b16 a[80:81], v7 offset:16384\n"
    "ds_read_b64_tr_b16 a[82:83], v7 offset:20480\n"
    "ds_read_b64_tr_b16 a[84:85], v7 offset:18432\n"
    "ds_read_b64_tr_b16 a[86:87], v7 offset:22528\n"
    "ds_read_b64_tr_b16 a[88:89], v7 offset:24576\n"
    "ds_read_b64_tr_b16 a[90:91], v7 offset:28672\n"
    "ds_read_b64_tr_b16 a[92:93], v7 offset:26624\n"
    "s_waitcnt lgkmcnt(11)\n"
    "ds_read_b64_tr_b16 a[94:95], v7 offset:30720\n"
    "ds_read_b64_tr_b16 a[96:97], v7 offset:32768\n"
    "ds_read_b64_tr_b16 a[98:99], v7 offset:36864\n"
    "ds_read_b64_tr_b16 a[100:101], v7 offset:34816\n"
    "s_waitcnt lgkmcnt(11)\n"
    "ds_read_b64_tr_b16 a[102:103], v7 offset:38912\n"
    "ds_read_b64_tr_b16 a[104:105], v7 offset:40960\n"
    "ds_read_b64_tr_b16 a[106:107], v7 offset:45056\n"
    "ds_read_b64_tr_b16 a[108:109], v7 offset:43008\n"
    "s_waitcnt lgkmcnt(11)\n"
    "ds_read_b64_tr_b16 a[110:111], v7 offset:47104\n"
    "ds_read_b64_tr_b16 a[112:113], v7 offset:49152\n"
    "ds_read_b64_tr_b16 a[114:115], v7 offset:53248\n"
    "ds_read_b64_tr_b16 a[116:117], v7 offset:51200\n"
    "s_waitcnt lgkmcnt(11)\n"
    "ds_read_b64_tr_b16 a[118:119], v7 offset:55296\n"
    "ds_read_b64_tr_b16 a[120:121], v7 offset:57344\n"
    "ds_read_b64_tr_b16 a[122:123], v7 offset:61440\n"
    "ds_read_b64_tr_b16 a[124:125], v7 offset:59392\n"
    "s_waitcnt lgkmcnt(11)\n"
    "ds_read_b64_tr_b16 a[126:127], v7 offset:63488\n"
    "s_waitcnt lgkmcnt(0)\n"
    "s_barrier\n"
    "s_add_u32 m0, s39, 0\n"
    "s_mov_b32 s38, 524288\n"
    "buffer_load_dwordx4 v3, s[20:23], s38 offen lds\n"
    "s_add_u32 m0, s39, 1024\n"
    "s_mov_b32 s38, 528384\n"
    "buffer_load_dwordx4 v3, s[20:23], s38 offen lds\n"
    "s_add_u32 m0, s39, 2048\n"
    "s_mov_b32 s38, 532480\n"
    "buffer_load_dwordx4 v3, s[20:23], s38 offen lds\n"
    "s_add_u32 m0, s39, 3072\n"
    "s_mov_b32 s38, 536576\n"
    "buffer_load_dwordx4 v3, s[20:23], s38 offen lds\n"
    "s_add_u32 m0, s39, 4096\n"
    "s_mov_b32 s38, 540672\n"
    "buffer_load_dwordx4 v3, s[20:23], s38 offen lds\n"
    "s_add_u32 m0, s39, 5120\n"
    "s_mov_b32 s38, 544768\n"
    "buffer_load_dwordx4 v3, s[20:23], s38 offen lds\n"
    "s_add_u32 m0, s39, 6144\n"
    "s_mov_b32 s38, 548864\n"
    "buffer_load_dwordx4 v3, s[20:23], s38 offen lds\n"
    "s_add_u32 m0, s39, 7168\n"
    "s_mov_b32 s38, 552960\n"
    "buffer_load_dwordx4 v3, s[20:23], s38 offen lds\n"
    "s_add_u32 m0, s39, 8192\n"
    "s_mov_b32 s38, 557056\n"
    "buffer_load_dwordx4 v3, s[20:23], s38 offen lds\n"
    "s_add_u32 m0, s39, 9216\n"
    "s_mov_b32 s38, 561152\n"
    "buffer_load_dwordx4 v3, s[20:23], s38 offen lds\n"
    "s_add_u32 m0, s39, 10240\n"
    "s_mov_b32 s38, 565248\n"
    "buffer_load_dwordx4 v3, s[20:23], s38 offen lds\n"
    "s_add_u32 m0, s39, 11264\n"
    "s_mov_b32 s38, 569344\n"
    "buffer_load_dwordx4 v3, s[20:23], s38 offen lds\n"
    "s_add_u32 m0, s39, 12288\n"
    "s_mov_b32 s38, 573440\n"
    "buffer_load_dwordx4 v3, s[20:23], s38 offen lds\n"
    "s_add_u32 m0, s39, 13312\n"
    "s_mov_b32 s38, 577536\n"
    "buffer_load_dwordx4 v3, s[20:23], s38 offen lds\n"
    "s_add_u32 m0, s39, 14336\n"
    "s_mov_b32 s38, 581632\n"
    "buffer_load_dwordx4 v3, s[20:23], s38 offen lds\n"
    "s_add_u32 m0, s39, 15360\n"
    "s_mov_b32 s38, 585728\n"
    "buffer_load_dwordx4 v3, s[20:23], s38 offen lds\n"
    "s_add_u32 m0, s39, 16384\n"
    "s_mov_b32 s38, 589824\n"
    "buffer_load_dwordx4 v3, s[20:23], s38 offen lds\n"
    "s_add_u32 m0, s39, 17408\n"
    "s_mov_b32 s38, 593920\n"
    "buffer_load_dwordx4 v3, s[20:23], s38 offen lds\n"
    "s_add_u32 m0, s39, 18432\n"
    "s_mov_b32 s38, 598016\n"
    "buffer_load_dwordx4 v3, s[20:23], s38 offen lds\n"
    "s_add_u32 m0, s39, 19456\n"
    "s_mov_b32 s38, 602112\n"
    "buffer_load_dwordx4 v3, s[20:23], s38 offen lds\n"
    "s_add_u32 m0, s39, 20480\n"
    "s_mov_b32 s38, 606208\n"
    "buffer_load_dwordx4 v3, s[20:23], s38 offen lds\n"
    "s_add_u32 m0, s39, 21504\n"
    "s_mov_b32 s38, 610304\n"
    "buffer_load_dwordx4 v3, s[20:23], s38 offen lds\n"
    "s_add_u32 m0, s39, 22528\n"
    "s_mov_b32 s38, 614400\n"
    "buffer_load_dwordx4 v3, s[20:23], s38 offen lds\n"
    "s_add_u32 m0, s39, 23552\n"
    "s_mov_b32 s38, 618496\n"
    "buffer_load_dwordx4 v3, s[20:23], s38 offen lds\n"
    "s_add_u32 m0, s39, 24576\n"
    "s_mov_b32 s38, 622592\n"
    "buffer_load_dwordx4 v3, s[20:23], s38 offen lds\n"
    "s_add_u32 m0, s39, 25600\n"
    "s_mov_b32 s38, 626688\n"
    "buffer_load_dwordx4 v3, s[20:23], s38 offen lds\n"
    "s_add_u32 m0, s39, 26624\n"
    "s_mov_b32 s38, 630784\n"
    "buffer_load_dwordx4 v3, s[20:23], s38 offen lds\n"
    "s_add_u32 m0, s39, 27648\n"
    "s_mov_b32 s38, 634880\n"
    "buffer_load_dwordx4 v3, s[20:23], s38 offen lds\n"
    "s_add_u32 m0, s39, 28672\n"
    "s_mov_b32 s38, 638976\n"
    "buffer_load_dwordx4 v3, s[20:23], s38 offen lds\n"
    "s_add_u32 m0, s39, 29696\n"
    "s_mov_b32 s38, 643072\n"
    "buffer_load_dwordx4 v3, s[20:23], s38 offen lds\n"
    "s_add_u32 m0, s39, 30720\n"
    "s_mov_b32 s38, 647168\n"
    "buffer_load_dwordx4 v3, s[20:23], s38 offen lds\n"
    "s_add_u32 m0, s39, 31744\n"
    "s_mov_b32 s38, 651264\n"
    "buffer_load_dwordx4 v3, s[20:23], s38 offen lds\n"
    "s_waitcnt vmcnt(0)\n"
    "s_barrier\n"
    "v_add_u32 v7, 0, v4\n"
    "ds_read_b64_tr_b16 a[128:129], v7 offset:0\n"
    "ds_read_b64_tr_b16 a[130:131], v7 offset:4096\n"
    "ds_read_b64_tr_b16 a[132:133], v7 offset:2048\n"
    "ds_read_b64_tr_b16 a[134:135], v7 offset:6144\n"
    "ds_read_b64_tr_b16 a[136:137], v7 offset:8192\n"
    "ds_read_b64_tr_b16 a[138:139], v7 offset:12288\n"
    "ds_read_b64_tr_b16 a[140:141], v7 offset:10240\n"
    "ds_read_b64_tr_b16 a[142:143], v7 offset:14336\n"
    "ds_read_b64_tr_b16 a[144:145], v7 offset:16384\n"
    "ds_read_b64_tr_b16 a[146:147], v7 offset:20480\n"
    "ds_read_b64_tr_b16 a[148:149], v7 offset:18432\n"
    "ds_read_b64_tr_b16 a[150:151], v7 offset:22528\n"
    "ds_read_b64_tr_b16 a[152:153], v7 offset:24576\n"
    "ds_read_b64_tr_b16 a[154:155], v7 offset:28672\n"
    "ds_read_b64_tr_b16 a[156:157], v7 offset:26624\n"
    "s_waitcnt lgkmcnt(11)\n"
    "ds_read_b64_tr_b16 a[158:159], v7 offset:30720\n"
    "ds_read_b64_tr_b16 a[160:161], v7 offset:32768\n"
    "ds_read_b64_tr_b16 a[162:163], v7 offset:36864\n"
    "ds_read_b64_tr_b16 a[164:165], v7 offset:34816\n"
    "s_waitcnt lgkmcnt(11)\n"
    "ds_read_b64_tr_b16 a[166:167], v7 offset:38912\n"
    "ds_read_b64_tr_b16 a[168:169], v7 offset:40960\n"
    "ds_read_b64_tr_b16 a[170:171], v7 offset:45056\n"
    "ds_read_b64_tr_b16 a[172:173], v7 offset:43008\n"
    "s_waitcnt lgkmcnt(11)\n"
    "ds_read_b64_tr_b16 a[174:175], v7 offset:47104\n"
    "ds_read_b64_tr_b16 a[176:177], v7 offset:49152\n"
    "ds_read_b64_tr_b16 a[178:179], v7 offset:53248\n"
    "ds_read_b64_tr_b16 a[180:181], v7 offset:51200\n"
    "s_waitcnt lgkmcnt(11)\n"
    "ds_read_b64_tr_b16 a[182:183], v7 offset:55296\n"
    "ds_read_b64_tr_b16 a[184:185], v7 offset:57344\n"
    "ds_read_b64_tr_b16 a[186:187], v7 offset:61440\n"
    "ds_read_b64_tr_b16 a[188:189], v7 offset:59392\n"
    "s_waitcnt lgkmcnt(11)\n"
    "ds_read_b64_tr_b16 a[190:191], v7 offset:63488\n"
    "s_waitcnt lgkmcnt(0)\n"
    "v_add_u32 v7, 65536, v4\n"
    "ds_read_b64_tr_b16 a[192:193], v7 offset:0\n"
    "ds_read_b64_tr_b16 a[194:195], v7 offset:4096\n"
    "ds_read_b64_tr_b16 a[196:197], v7 offset:2048\n"
    "ds_read_b64_tr_b16 a[198:199], v7 offset:6144\n"
    "ds_read_b64_tr_b16 a[200:201], v7 offset:8192\n"
    "ds_read_b64_tr_b16 a[202:203], v7 offset:12288\n"
    "ds_read_b64_tr_b16 a[204:205], v7 offset:10240\n"
    "ds_read_b64_tr_b16 a[206:207], v7 offset:14336\n"
    "ds_read_b64_tr_b16 a[208:209], v7 offset:16384\n"
    "ds_read_b64_tr_b16 a[210:211], v7 offset:20480\n"
    "ds_read_b64_tr_b16 a[212:213], v7 offset:18432\n"
    "ds_read_b64_tr_b16 a[214:215], v7 offset:22528\n"
    "ds_read_b64_tr_b16 a[216:217], v7 offset:24576\n"
    "ds_read_b64_tr_b16 a[218:219], v7 offset:28672\n"
    "ds_read_b64_tr_b16 a[220:221], v7 offset:26624\n"
    "s_waitcnt lgkmcnt(11)\n"
    "ds_read_b64_tr_b16 a[222:223], v7 offset:30720\n"
    "ds_read_b64_tr_b16 a[224:225], v7 offset:32768\n"
    "ds_read_b64_tr_b16 a[226:227], v7 offset:36864\n"
    "ds_read_b64_tr_b16 a[228:229], v7 offset:34816\n"
    "s_waitcnt lgkmcnt(11)\n"
    "ds_read_b64_tr_b16 a[230:231], v7 offset:38912\n"
    "ds_read_b64_tr_b16 a[232:233], v7 offset:40960\n"
    "ds_read_b64_tr_b16 a[234:235], v7 offset:45056\n"
    "ds_read_b64_tr_b16 a[236:237], v7 offset:43008\n"
    "s_waitcnt lgkmcnt(11)\n"
    "ds_read_b64_tr_b16 a[238:239], v7 offset:47104\n"
    "ds_read_b64_tr_b16 a[240:241], v7 offset:49152\n"
    "ds_read_b64_tr_b16 a[242:243], v7 offset:53248\n"
    "ds_read_b64_tr_b16 a[244:245], v7 offset:51200\n"
    "s_waitcnt lgkmcnt(11)\n"
    "ds_read_b64_tr_b16 a[246:247], v7 offset:55296\n"
    "ds_read_b64_tr_b16 a[248:249], v7 offset:57344\n"
    "ds_read_b64_tr_b16 a[250:251], v7 offset:61440\n"
    "ds_read_b64_tr_b16 a[252:253], v7 offset:59392\n"
    "s_waitcnt lgkmcnt(11)\n"
    "ds_read_b64_tr_b16 a[254:255], v7 offset:63488\n"
    "s_waitcnt lgkmcnt(0)\n"
    "s_barrier\n"
    "v_lshlrev_b32 v7, 4, v0\n"
    "v_and_b32 v7, 0x700, v7\n"
    "v_mov_b32 v9, 139776\n"
    "v_lshl_add_u32 v10, s36, 2, v9\n"
    "s_lshr_b32 s38, s41, 8\n"
    "s_xor_b32 s38, s38, s42\n"
    "s_and_b32 s38, s38, 0x2000\n"
    "s_or_b32 s48, s41, s38\n"
    "LT%=:\n"
    "s_cmp_lg_u32 s36, 0\n"
    "s_cbranch_scc1 LB%=\n"
    "s_mov_b32 s37, 0x80000\n"
    "LP%=:\n"
    "buffer_load_dword v6, v7, s[16:19], s44 offen sc1\n"
    "s_waitcnt vmcnt(0)\n"
    "v_cmp_eq_u32 vcc, 0, v6\n"
    "s_cbranch_vccz LB%=\n"
    "s_sleep 1\n"
    "s_sub_u32 s37, s37, 1\n"
    "s_cbranch_scc0 LP%=\n"
    "buffer_store_dword v5, off, s[16:19], s47 sc1\n"
    "s_endpgm\n"
    "LB%=:\n"
    "s_barrier\n"
    "LC%=:\n"
    "s_add_u32 m0, s49, 0\n"
    "s_nop 0\n"
    "buffer_load_dwordx4 v0, s[16:19], s48 offen offset:0 sc1 lds\n"
    "buffer_load_dwordx4 v0, s[16:19], s48 offen offset:1024 sc1 lds\n"
    "s_add_u32 s48, s48, 16384\n"
    "s_add_u32 m0, s49, 8192\n"
    "s_nop 0\n"
    "buffer_load_dwordx4 v0, s[16:19], s48 offen offset:0 sc1 lds\n"
    "buffer_load_dwordx4 v0, s[16:19], s48 offen offset:1024 sc1 lds\n"
    "s_add_u32 s48, s48, 16384\n"
    "s_add_u32 m0, s49, 16384\n"
    "s_nop 0\n"
    "buffer_load_dwordx4 v0, s[16:19], s48 offen offset:0 sc1 lds\n"
    "buffer_load_dwordx4 v0, s[16:19], s48 offen offset:1024 sc1 lds\n"
    "s_add_u32 s48, s48, 16384\n"
    "LG%=:\n"
    "s_lshr_b32 s38, s46, 8\n"
    "s_xor_b32 s38, s38, s43\n"
    "s_and_b32 s38, s38, 0x2000\n"
    "s_or_b32 s50, s46, s38\n"
    "s_add_u32 s41, s41, 2621440\n"
    "s_add_u32 s46, s46, 262144\n"
    "s_add_u32 s44, s44, 4\n"
    "s_add_u32 s40, s40, 1\n"
    "s_waitcnt vmcnt(4)\n"
    "s_barrier\n"
    "ds_read_b128 v[20:23], v0 offset:0\n"
    "ds_read_b128 v[24:27], v0 offset:1024\n"
    "ds_read_b128 v[28:31], v0 offset:2048\n"
    "ds_read_b128 v[32:35], v0 offset:3072\n"
    "s_waitcnt lgkmcnt(3)\n"
    "v_mfma_f32_32x32x16_bf16 v[64:79], a[0:3], v[20:23], 0\n"
    "s_add_u32 m0, s49, 24576\n"
    "s_nop 0\n"
    "buffer_load_dwordx4 v0, s[16:19], s48 offen offset:0 sc1 lds\n"
    "buffer_load_dwordx4 v0, s[16:19], s48 offen offset:1024 sc1 lds\n"
    "s_add_u32 s48, s48, 16384\n"
    "ds_read_b128 v[36:39], v0 offset:4096\n"
    "s_waitcnt lgkmcnt(3)\n"
    "v_mfma_f32_32x32x16_bf16 v[64:79], a[4:7], v[24:27], v[64:79]\n"
    "ds_read_b128 v[40:43], v0 offset:5120\n"
    "s_waitcnt lgkmcnt(3)\n"
    "v_mfma_f32_32x32x16_bf16 v[64:79], a[8:11], v[28:31], v[64:79]\n"
    "ds_read_b128 v[44:47], v0 offset:6144\n"
    "s_waitcnt lgkmcnt(3)\n"
    "v_mfma_f32_32x32x16_bf16 v[64:79], a[12:15], v[32:35], v[64:79]\n"
    "ds_read_b128 v[48:51], v0 offset:7168\n"
    "s_waitcnt lgkmcnt(3)\n"
    "v_mfma_f32_32x32x16_bf16 v[64:79], a[16:19], v[36:39], v[64:79]\n"
    "s_waitcnt vmcnt(4)\n"
    "s_barrier\n"
    "ds_read_b128 v[20:23], v0 offset:8192\n"
    "s_waitcnt lgkmcnt(3)\n"
    "v_mfma_f32_32x32x16_bf16 v[64:79], a[20:23], v[40:43], v[64:79]\n"
    "ds_read_b128 v[24:27], v0 offset:9216\n"
    "s_waitcnt lgkmcnt(3)\n"
    "v_mfma_f32_32x32x16_bf16 v[64:79], a[24:27], v[44:47], v[64:79]\n"
    "s_add_u32 m0, s49, 32768\n"
    "s_nop 0\n"
    "buffer_load_dwordx4 v0, s[16:19], s48 offen offset:0 sc1 lds\n"
    "buffer_load_dwordx4 v0, s[16:19], s48 offen offset:1024 sc1 lds\n"
    "s_add_u32 s48, s48, 16384\n"
    "ds_read_b128 v[28:31], v0 offset:10240\n"
    "s_waitcnt lgkmcnt(3)\n"
    "v_mfma_f32_32x32x16_bf16 v[64:79], a[28:31], v[48:51], v[64:79]\n"
    "ds_read_b128 v[32:35], v0 offset:11264\n"
    "s_waitcnt lgkmcnt(3)\n"
    "v_mfma_f32_32x32x16_bf16 v[64:79], a[32:35], v[20:23], v[64:79]\n"
    "ds_read_b128 v[36:39], v0 offset:12288\n"
    "s_waitcnt lgkmcnt(3)\n"
    "v_mfma_f32_32x32x16_bf16 v[64:79], a[36:39], v[24:27], v[64:79]\n"
    "ds_read_b128 v[40:43], v0 offset:13312\n"
    "s_waitcnt lgkmcnt(3)\n"
    "v_mfma_f32_32x32x16_bf16 v[64:79], a[40:43], v[28:31], v[64:79]\n"
    "ds_read_b128 v[44:47], v0 offset:14336\n"
    "s_waitcnt lgkmcnt(3)\n"
    "v_mfma_f32_32x32x16_bf16 v[64:79], a[44:47], v[32:35], v[64:79]\n"
    "ds_read_b128 v[48:51], v0 offset:15360\n"
    "s_waitcnt lgkmcnt(3)\n"
    "v_mfma_f32_32x32x16_bf16 v[64:79], a[48:51], v[36:39], v[64:79]\n"
    "s_add_u32 m0, s49, 40960\n"
    "s_nop 0\n"
    "buffer_load_dwordx4 v0, s[16:19], s48 offen offset:0 sc1 lds\n"
    "buffer_load_dwordx4 v0, s[16:19], s48 offen offset:1024 sc1 lds\n"
    "s_add_u32 s48, s48, 16384\n"
    "s_waitcnt vmcnt(6)\n"
    "s_barrier\n"
    "ds_read_b128 v[20:23], v0 offset:16384\n"
    "s_waitcnt lgkmcnt(3)\n"
    "v_mfma_f32_32x32x16_bf16 v[64:79], a[52:55], v[40:43], v[64:79]\n"
    "ds_read_b128 v[24:27], v0 offset:17408\n"
    "s_waitcnt lgkmcnt(3)\n"
    "v_mfma_f32_32x32x16_bf16 v[64:79], a[56:59], v[44:47], v[64:79]\n"
    "ds_read_b128 v[28:31], v0 offset:18432\n"
    "s_waitcnt lgkmcnt(3)\n"
    "v_mfma_f32_32x32x16_bf16 v[64:79], a[60:63], v[48:51], v[64:79]\n"
    "ds_read_b128 v[32:35], v0 offset:19456\n"
    "s_waitcnt lgkmcnt(3)\n"
    "v_mfma_f32_32x32x16_bf16 v[64:79], a[64:67], v[20:23], v[64:79]\n"
    "ds_read_b128 v[36:39], v0 offset:20480\n"
    "s_waitcnt lgkmcnt(3)\n"
    "v_mfma_f32_32x32x16_bf16 v[64:79], a[68:71], v[24:27], v[64:79]\n"
    "ds_read_b128 v[40:43], v0 offset:21504\n"
    "s_waitcnt lgkmcnt(3)\n"
    "v_mfma_f32_32x32x16_bf16 v[64:79], a[72:75], v[28:31], v[64:79]\n"
    "s_add_u32 m0, s49, 49152\n"
    "s_nop 0\n"
    "buffer_load_dwordx4 v0, s[16:19], s48 offen offset:0 sc1 lds\n"
    "buffer_load_dwordx4 v0, s[16:19], s48 offen offset:1024 sc1 lds\n"
    "s_add_u32 s48, s48, 16384\n"
    "ds_read_b128 v[44:47], v0 offset:22528\n"
    "s_waitcnt lgkmcnt(3)\n"
    "v_mfma_f32_32x32x16_bf16 v[64:79], a[76:79], v[32:35], v[64:79]\n"
    "ds_read_b128 v[48:51], v0 offset:23552\n"
    "s_waitcnt lgkmcnt(3)\n"
    "v_mfma_f32_32x32x16_bf16 v[64:79], a[80:83], v[36:39], v[64:79]\n"
    "s_waitcnt vmcnt(6)\n"
    "s_barrier\n"
    "ds_read_b128 v[20:23], v0 offset:24576\n"
    "s_waitcnt lgkmcnt(3)\n"
    "v_mfma_f32_32x32x16_bf16 v[64:79], a[84:87], v[40:43], v[64:79]\n"
    "ds_read_b128 v[24:27], v0 offset:25600\n"
    "s_waitcnt lgkmcnt(3)\n"
    "v_mfma_f32_32x32x16_bf16 v[64:79], a[88:91], v[44:47], v[64:79]\n"
    "ds_read_b128 v[28:31], v0 offset:26624\n"
    "s_waitcnt lgkmcnt(3)\n"
    "v_mfma_f32_32x32x16_bf16 v[64:79], a[92:95], v[48:51], v[64:79]\n"
    "ds_read_b128 v[32:35], v0 offset:27648\n"
    "s_waitcnt lgkmcnt(3)\n"
    "v_mfma_f32_32x32x16_bf16 v[64:79], a[96:99], v[20:23], v[64:79]\n"
    "s_add_u32 m0, s49, 57344\n"
    "s_nop 0\n"
    "buffer_load_dwordx4 v0, s[16:19], s48 offen offset:0 sc1 lds\n"
    "buffer_load_dwordx4 v0, s[16:19], s48 offen offset:1024 sc1 lds\n"
    "s_add_u32 s48, s48, 16384\n"
    "s_lshr_b32 s38, s41, 8\n"
    "s_xor_b32 s38, s38, s42\n"
    "s_and_b32 s38, s38, 0x2000\n"
    "s_or_b32 s48, s41, s38\n"
    "ds_read_b128 v[36:39], v0 offset:28672\n"
    "s_waitcnt lgkmcnt(3)\n"
    "v_mfma_f32_32x32x16_bf16 v[64:79], a[100:103], v[24:27], v[64:79]\n"
    "ds_read_b128 v[40:43], v0 offset:29696\n"
    "s_waitcnt lgkmcnt(3)\n"
    "v_mfma_f32_32x32x16_bf16 v[64:79], a[104:107], v[28:31], v[64:79]\n"
    "ds_read_b128 v[44:47], v0 offset:30720\n"
    "s_waitcnt lgkmcnt(3)\n"
    "v_mfma_f32_32x32x16_bf16 v[64:79], a[108:111], v[32:35], v[64:79]\n"
    "ds_read_b128 v[48:51], v0 offset:31744\n"
    "s_waitcnt lgkmcnt(3)\n"
    "v_mfma_f32_32x32x16_bf16 v[64:79], a[112:115], v[36:39], v[64:79]\n"
    "s_waitcnt vmcnt(6)\n"
    "s_barrier\n"
    "ds_read_b128 v[20:23], v0 offset:32768\n"
    "s_waitcnt lgkmcnt(3)\n"
    "v_mfma_f32_32x32x16_bf16 v[64:79], a[116:119], v[40:43], v[64:79]\n"
    "ds_read_b128 v[24:27], v0 offset:33792\n"
    "s_waitcnt lgkmcnt(3)\n"
    "v_mfma_f32_32x32x16_bf16 v[64:79], a[120:123], v[44:47], v[64:79]\n"
    "ds_read_b128 v[28:31], v0 offset:34816\n"
    "s_waitcnt lgkmcnt(3)\n"
    "v_mfma_f32_32x32x16_bf16 v[64:79], a[124:127], v[48:51], v[64:79]\n"
    "ds_read_b128 v[32:35], v0 offset:35840\n"
    "s_waitcnt lgkmcnt(3)\n"
    "v_mfma_f32_32x32x16_bf16 v[64:79], a[128:131], v[20:23], v[64:79]\n"
    "ds_read_b128 v[36:39], v0 offset:36864\n"
    "s_waitcnt lgkmcnt(3)\n"
    "v_mfma_f32_32x32x16_bf16 v[64:79], a[132:135], v[24:27], v[64:79]\n"
    "ds_read_b128 v[40:43], v0 offset:37888\n"
    "s_waitcnt lgkmcnt(3)\n"
    "v_mfma_f32_32x32x16_bf16 v[64:79], a[136:139], v[28:31], v[64:79]\n"
    "buffer_load_dword v6, v7, s[16:19], s44 offen sc1\n"
    "ds_read_b128 v[44:47], v0 offset:38912\n"
    "s_waitcnt lgkmcnt(3)\n"
    "v_mfma_f32_32x32x16_bf16 v[64:79], a[140:143], v[32:35], v[64:79]\n"
    "ds_read_b128 v[48:51], v0 offset:39936\n"
    "s_waitcnt lgkmcnt(3)\n"
    "v_mfma_f32_32x32x16_bf16 v[64:79], a[144:147], v[36:39], v[64:79]\n"
    "s_waitcnt vmcnt(5)\n"
    "s_barrier\n"
    "ds_read_b128 v[20:23], v0 offset:40960\n"
    "s_waitcnt lgkmcnt(3)\n"
    "v_mfma_f32_32x32x16_bf16 v[64:79], a[148:151], v[40:43], v[64:79]\n"
    "ds_read_b128 v[24:27], v0 offset:41984\n"
    "s_waitcnt lgkmcnt(3)\n"
    "v_mfma_f32_32x32x16_bf16 v[64:79], a[152:155], v[44:47], v[64:79]\n"
    "ds_read_b128 v[28:31], v0 offset:43008\n"
    "s_waitcnt lgkmcnt(3)\n"
    "v_mfma_f32_32x32x16_bf16 v[64:79], a[156:159], v[48:51], v[64:79]\n"
    "ds_read_b128 v[32:35], v0 offset:44032\n"
    "s_waitcnt lgkmcnt(3)\n"
    "v_mfma_f32_32x32x16_bf16 v[64:79], a[160:163], v[20:23], v[64:79]\n"
    "ds_read_b128 v[36:39], v0 offset:45056\n"
    "s_waitcnt lgkmcnt(3)\n"
    "v_mfma_f32_32x32x16_bf16 v[64:79], a[164:167], v[24:27], v[64:79]\n"
    "ds_read_b128 v[40:43], v0 offset:46080\n"
    "s_waitcnt lgkmcnt(3)\n"
    "v_mfma_f32_32x32x16_bf16 v[64:79], a[168:171], v[28:31], v[64:79]\n"
    "ds_read_b128 v[44:47], v0 offset:47104\n"
    "s_waitcnt lgkmcnt(3)\n"
    "v_mfma_f32_32x32x16_bf16 v[64:79], a[172:175], v[32:35], v[64:79]\n"
    "ds_read_b128 v[48:51], v0 offset:48128\n"
    "s_waitcnt lgkmcnt(3)\n"
    "v_mfma_f32_32x32x16_bf16 v[64:79], a[176:179], v[36:39], v[64:79]\n"
    "s_waitcnt vmcnt(3)\n"
    "s_barrier\n"
    "ds_read_b128 v[20:23], v0 offset:49152\n"
    "s_waitcnt lgkmcnt(3)\n"
    "v_mfma_f32_32x32x16_bf16 v[64:79], a[180:183], v[40:43], v[64:79]\n"
    "ds_read_b128 v[24:27], v0 offset:50176\n"
    "s_waitcnt lgkmcnt(3)\n"
    "v_mfma_f32_32x32x16_bf16 v[64:79], a[184:187], v[44:47], v[64:79]\n"
    "ds_read_b128 v[28:31], v0 offset:51200\n"
    "s_waitcnt lgkmcnt(3)\n"
    "v_mfma_f32_32x32x16_bf16 v[64:79], a[188:191], v[48:51], v[64:79]\n"
    "s_waitcnt vmcnt(0)\n"
    "v_cmp_eq_u32 vcc, 0, v6\n"
    "s_cmp_eq_u32 s40, 64\n"
    "s_cselect_b32 vcc_hi, 1, 0\n"
    "s_or_b32 s38, vcc_lo, vcc_hi\n"
    "v_mov_b32 v6, s38\n"
    "ds_write_b32 v10, v6\n"
    "ds_read_b128 v[32:35], v0 offset:52224\n"
    "s_waitcnt lgkmcnt(4)\n"
    "v_mfma_f32_32x32x16_bf16 v[64:79], a[192:195], v[20:23], v[64:79]\n"
    "ds_read_b128 v[36:39], v0 offset:53248\n"
    "s_waitcnt lgkmcnt(4)\n"
    "v_mfma_f32_32x32x16_bf16 v[64:79], a[196:199], v[24:27], v[64:79]\n"
    "ds_read_b128 v[40:43], v0 offset:54272\n"
    "s_waitcnt lgkmcnt(4)\n"
    "v_mfma_f32_32x32x16_bf16 v[64:79], a[200:203], v[28:31], v[64:79]\n"
    "ds_read_b128 v[44:47], v0 offset:55296\n"
    "s_waitcnt lgkmcnt(3)\n"
    "v_mfma_f32_32x32x16_bf16 v[64:79], a[204:207], v[32:35], v[64:79]\n"
    "ds_read_b128 v[48:51], v0 offset:56320\n"
    "s_waitcnt lgkmcnt(3)\n"
    "v_mfma_f32_32x32x16_bf16 v[64:79], a[208:211], v[36:39], v[64:79]\n"
    "s_barrier\n"
    "ds_read_b128 v[20:23], v0 offset:57344\n"
    "ds_read_b32 v6, v9\n"
    "s_waitcnt lgkmcnt(4)\n"
    "v_mfma_f32_32x32x16_bf16 v[64:79], a[212:215], v[40:43], v[64:79]\n"
    "ds_read_b128 v[24:27], v0 offset:58368\n"
    "s_waitcnt lgkmcnt(4)\n"
    "v_mfma_f32_32x32x16_bf16 v[64:79], a[216:219], v[44:47], v[64:79]\n"
    "ds_read_b128 v[28:31], v0 offset:59392\n"
    "s_waitcnt lgkmcnt(4)\n"
    "v_mfma_f32_32x32x16_bf16 v[64:79], a[220:223], v[48:51], v[64:79]\n"
    "ds_read_b128 v[32:35], v0 offset:60416\n"
    "s_waitcnt lgkmcnt(3)\n"
    "v_readfirstlane_b32 s52, v6\n"
    "s_cmp_eq_u32 s52, 0\n"
    "s_cbranch_scc1 LH%=\n"
    "v_mfma_f32_32x32x16_bf16 v[64:79], a[224:227], v[20:23], v[64:79]\n"
    "ds_read_b128 v[36:39], v0 offset:61440\n"
    "s_waitcnt lgkmcnt(3)\n"
    "v_mfma_f32_32x32x16_bf16 v[64:79], a[228:231], v[24:27], v[64:79]\n"
    "ds_read_b128 v[40:43], v0 offset:62464\n"
    "s_waitcnt lgkmcnt(3)\n"
    "v_mfma_f32_32x32x16_bf16 v[64:79], a[232:235], v[28:31], v[64:79]\n"
    "ds_read_b128 v[44:47], v0 offset:63488\n"
    "s_waitcnt lgkmcnt(3)\n"
    "v_mfma_f32_32x32x16_bf16 v[64:79], a[236:239], v[32:35], v[64:79]\n"
    "ds_read_b128 v[48:51], v0 offset:64512\n"
    "s_waitcnt lgkmcnt(3)\n"
    "v_mfma_f32_32x32x16_bf16 v[64:79], a[240:243], v[36:39], v[64:79]\n"
    "s_waitcnt lgkmcnt(2)\n"
    "v_mfma_f32_32x32x16_bf16 v[64:79], a[244:247], v[40:43], v[64:79]\n"
    "s_waitcnt lgkmcnt(1)\n"
    "v_mfma_f32_32x32x16_bf16 v[64:79], a[248:251], v[44:47], v[64:79]\n"
    "s_waitcnt lgkmcnt(0)\n"
    "v_mfma_f32_32x32x16_bf16 v[64:79], a[252:255], v[48:51], v[64:79]\n"
    "s_branch LI%=\n"
    "LH%=:\n"
    "v_mfma_f32_32x32x16_bf16 v[64:79], a[224:227], v[20:23], v[64:79]\n"
    "ds_read_b128 v[36:39], v0 offset:61440\n"
    "s_add_u32 m0, s49, 0\n"
    "s_nop 0\n"
    "buffer_load_dwordx4 v0, s[16:19], s48 offen offset:0 sc1 lds\n"
    "buffer_load_dwordx4 v0, s[16:19], s48 offen offset:1024 sc1 lds\n"
    "s_add_u32 s48, s48, 16384\n"
    "s_waitcnt lgkmcnt(3)\n"
    "v_mfma_f32_32x32x16_bf16 v[64:79], a[228:231], v[24:27], v[64:79]\n"
    "ds_read_b128 v[40:43], v0 offset:62464\n"
    "s_add_u32 m0, s49, 8192\n"
    "s_nop 0\n"
    "buffer_load_dwordx4 v0, s[16:19], s48 offen offset:0 sc1 lds\n"
    "buffer_load_dwordx4 v0, s[16:19], s48 offen offset:1024 sc1 lds\n"
    "s_add_u32 s48, s48, 16384\n"
    "s_waitcnt lgkmcnt(3)\n"
    "v_mfma_f32_32x32x16_bf16 v[64:79], a[232:235], v[28:31], v[64:79]\n"
    "ds_read_b128 v[44:47], v0 offset:63488\n"
    "s_add_u32 m0, s49, 16384\n"
    "s_nop 0\n"
    "buffer_load_dwordx4 v0, s[16:19], s48 offen offset:0 sc1 lds\n"
    "buffer_load_dwordx4 v0, s[16:19], s48 offen offset:1024 sc1 lds\n"
    "s_add_u32 s48, s48, 16384\n"
    "s_waitcnt lgkmcnt(3)\n"
    "v_mfma_f32_32x32x16_bf16 v[64:79], a[236:239], v[32:35], v[64:79]\n"
    "ds_read_b128 v[48:51], v0 offset:64512\n"
    "s_waitcnt lgkmcnt(3)\n"
    "v_mfma_f32_32x32x16_bf16 v[64:79], a[240:243], v[36:39], v[64:79]\n"
    "s_waitcnt lgkmcnt(2)\n"
    "v_mfma_f32_32x32x16_bf16 v[64:79], a[244:247], v[40:43], v[64:79]\n"
    "s_waitcnt lgkmcnt(1)\n"
    "v_mfma_f32_32x32x16_bf16 v[64:79], a[248:251], v[44:47], v[64:79]\n"
    "s_waitcnt lgkmcnt(0)\n"
    "v_mfma_f32_32x32x16_bf16 v[64:79], a[252:255], v[48:51], v[64:79]\n"
    "LI%=:\n"
    "s_nop 7\n"
    "s_nop 3\n"
    "v_cvt_pk_bf16_f32 v12, v64, v65\n"
    "v_cvt_pk_bf16_f32 v13, v66, v67\n"
    "v_cvt_pk_bf16_f32 v14, v68, v69\n"
    "v_cvt_pk_bf16_f32 v15, v70, v71\n"
    "ds_write_b128 v1, v[12:15] offset:0\n"
    "v_cvt_pk_bf16_f32 v16, v72, v73\n"
    "v_cvt_pk_bf16_f32 v17, v74, v75\n"
    "v_cvt_pk_bf16_f32 v18, v76, v77\n"
    "v_cvt_pk_bf16_f32 v19, v78, v79\n"
    "ds_write_b128 v1, v[16:19] offset:32\n"
    "s_waitcnt lgkmcnt(0)\n"
    "s_barrier\n"
    "ds_read_b128 v[12:15], v2 offset:0\n"
    "ds_read_b128 v[16:19], v2 offset:1088\n"
    "s_waitcnt lgkmcnt(1)\n"
    "buffer_store_dwordx4 v[12:15], v0, s[16:19], s50 offen offset:0 sc1\n"
    "s_waitcnt lgkmcnt(0)\n"
    "buffer_store_dwordx4 v[16:19], v0, s[16:19], s50 offen offset:1024 sc1\n"
    "s_nop 7\n"
    "s_nop 7\n"
    "s_nop 7\n"
    "s_nop 7\n"
    "s_nop 7\n"
    "buffer_load_dword v6, v7, s[16:19], s44 offen sc1\n"
    "s_waitcnt vmcnt(1)\n"
    "s_barrier\n"
    "buffer_store_dword v5, off, s[16:19], s45 sc1\n"
    "s_add_u32 s45, s45, 4\n"
    "s_cmp_eq_u32 s52, 0\n"
    "s_cbranch_scc1 LG%=\n"
    "s_cmp_eq_u32 s40, 64\n"
    "s_cbranch_scc1 LE%=\n"
    "s_cmp_lg_u32 s36, 0\n"
    "s_cbranch_scc1 LB%=\n"
    "s_waitcnt vmcnt(1)\n"
    "v_cmp_eq_u32 vcc, 0, v6\n"
    "s_cbranch_vccz LB%=\n"
    "s_branch LT%=\n"
    "LE%=:\n"
    "s_endpgm\n"
    :
    : "{v0}"(hv0), "{v1}"(hv1), "{v2}"(hv2), "{v3}"(hv3), "{v4}"(hv4), "{v5}"(hv5)
    : "v255", "a255", "s101", "vcc", "scc", "memory");
  __builtin_unreachable();
}

typedef float f2 __attribute__((ext_vector_type(2)));
__device__ __forceinline__ unsigned cvt2(f2 v) { unsigned p; asm("v_cvt_pk_bf16_f32 %0, %1, %2" : "=v"(p) : "v"(v[0]), "v"(v[1])); return p; }
__device__ __forceinline__ f2 up2(unsigned p) { return (f2){__uint_as_float(p << 16), __uint_as_float(p & 0xffff0000u)}; }
__device__ __forceinline__ v4u rb8(f2 (&x)[4]) {
  v4u p;
#pragma unroll
  for (int i = 0; i < 4; ++i) { p[i] = cvt2(x[i]); x[i] = up2(p[i]); }
  return p;
}
__device__ __forceinline__ void row_sum2(float& x, float& y) {
  x = ror_add<8>(x); y = ror_add<8>(y); x = ror_add<4>(x); y = ror_add<4>(y); x = ror_add<2>(x); y = ror_add<2>(y); x = ror_add<1>(x); y = ror_add<1>(y);
  auto q = __builtin_amdgcn_permlane16_swap(__float_as_uint(x), __float_as_uint(y), false, false);
  float t = __uint_as_float(q[0]) + __uint_as_float(q[1]);
  auto s = __builtin_amdgcn_permlane32_swap(__float_as_uint(t), __float_as_uint(t), false, false);
  t = __uint_as_float(s[0]) + __uint_as_float(s[1]);
  x = __int_as_float(__builtin_amdgcn_readlane(__float_as_int(t), 0)); y = __int_as_float(__builtin_amdgcn_readlane(__float_as_int(t), 16));
}
__device__ __forceinline__ void ln8(const f2 (&x)[4], f2 (&y)[4]) {
  const float c0 = __int_as_float(__builtin_amdgcn_readfirstlane(__float_as_int(x[0][0])));
  const f2 d0 = x[0] - c0, d1 = x[1] - c0, d2 = x[2] - c0, d3 = x[3] - c0;
  const f2 s2 = (d0 + d1) + (d2 + d3), v2 = d3 * d3 + (d2 * d2 + (d1 * d1 + d0 * d0));
  float s = s2[0] + s2[1], vs = v2[0] + v2[1];
  row_sum2(s, vs);
  s *= 1.f / DM;
  const f2 mv = up2(cvt2((f2){c0 + s, fmaxf(__builtin_fmaf(-s, s, vs * (1.f / DM)), 0.f)}));
  const float rc = __builtin_amdgcn_rcpf(rbf(__builtin_amdgcn_sqrtf(rbf(mv[1] + 1e-5f))));
#pragma unroll
  for (int i = 0; i < 4; ++i) y[i] = (x[i] - mv[0]) * rc;
}
__device__ __forceinline__ v4u ln_row(v4u q) {
  f2 x[4], y[4];
#pragma unroll
  for (int i = 0; i < 4; ++i) x[i] = up2(q[i]);
  ln8(x, y);
  return (v4u){cvt2(y[0]), cvt2(y[1]), cvt2(y[2]), cvt2(y[3])};
}
__device__ __forceinline__ unsigned mk_key(float t, int row) {
  const unsigned b = __float_as_uint(t) >> 16;
  return (t != t ? 0u : ((b & 0x8000u) ? (~b & 0xffffu) : (b | 0x8000u)) << 16) | (0xffffu - row);
}
__device__ __forceinline__ float key_val(unsigned k) { const unsigned o = k >> 16; return __uint_as_float((o == 0u ? 0x7fc0u : ((o & 0x8000u) ? o & 0x7fffu : ~o & 0xffffu)) << 16); }
__device__ __forceinline__ unsigned wave_umax(unsigned x) {
  x = max(x, (unsigned)__builtin_amdgcn_update_dpp(0, x, 0x128, 0xf, 0xf, false)); x = max(x, (unsigned)__builtin_amdgcn_update_dpp(0, x, 0x124, 0xf, 0xf, false));
  x = max(x, (unsigned)__builtin_amdgcn_update_dpp(0, x, 0x122, 0xf, 0xf, false)); x = max(x, (unsigned)__builtin_amdgcn_update_dpp(0, x, 0x121, 0xf, 0xf, false));
  return max(max((unsigned)__builtin_amdgcn_readlane(x, 0), (unsigned)__builtin_amdgcn_readlane(x, 16)), max((unsigned)__builtin_amdgcn_readlane(x, 32), (unsigned)__builtin_amdgcn_readlane(x, 48)));
}
__device__ __forceinline__ bool any_empty(v4u q) { return __ballot(max(max(q[0], q[1]), max(q[2], q[3])) == 0xffffffffu) != 0ull; }
#define SMF "v_smfmac_f32_16x16x64_bf16 "
__device__ __forceinline__ v8u tr_op(const char* p, int stride) {
  v8u o;
#pragma unroll
  for (int i = 0; i < 4; ++i) {
    const v2u t = __builtin_bit_cast(v2u, __builtin_amdgcn_ds_read_tr16_b64_v4i16((lds_s16x4*)(p + (32 * (i >> 1) + 2 * (i & 1)) * stride)));
    o[2 * i] = t[0]; o[2 * i + 1] = t[1];
  }
  return o;
}
__device__ __forceinline__ void dma16(rsrc_t r, char* lp, unsigned vo, unsigned so) { __builtin_amdgcn_raw_ptr_buffer_load_lds(r, (__attribute__((address_space(3))) void*)lp, 16, vo, so, 0, 0); }
#define STAGED() do { __builtin_amdgcn_s_waitcnt(0); __syncthreads(); } while (0)
template <bool L1>
__device__ __forceinline__ void l_role8(int w, rsrc_t rs, rsrc_t ro, char* lds, int u, unsigned c2, const u16* xin, const u16* w_r, const u16* wg, const u16* wu, const u16* wd, unsigned* err) {
  const int lane = lane_id(), c = lane & 15, g = lane >> 4, l16 = lane * 16;
  const int r = (u & 7) * 4 + w;
  v8u wa[32], wv[16];
  const int t0 = L1 ? (w < 2 ? 3 * w : 2 * w + 2) : w;
  {
    const rsrc_t rg = __builtin_amdgcn_make_buffer_rsrc((void*)wg, 0, DM * FF * 2, 0x00020000), ru = __builtin_amdgcn_make_buffer_rsrc((void*)wu, 0, DM * FF * 2, 0x00020000),
                 rd = __builtin_amdgcn_make_buffer_rsrc((void*)wd, 0, DM * FF * 2, 0x00020000);
    const int q = (lane >> 2) & 3, trow = 16 * (g >> 1) + 8 * (q >> 1) + 4 * (g & 1) + (q & 1);
    if (L1) {
      const char* tr = lds + trow * 320 + (lane & 3) * 8;
      unsigned vo[5];
#pragma unroll
      for (int i = 0; i < 5; ++i) { const int P = i * 64 + lane; vo[i] = (w * 16 + P / 20) * (FF * 2) + u * 320 + (P % 20) * 16; }
#pragma unroll
      for (int b0 = 0; b0 < 8; b0 += 3) {
#pragma unroll
        for (int k = b0; k < min(b0 + 3, 8); ++k)
#pragma unroll
          for (int i = 0; i < 10; ++i) dma16(i < 5 ? rg : ru, lds + (k - b0) * 40960 + (i / 5) * 20480 + w * 5120 + (i % 5) * 1024, vo[i % 5], k * 64 * (FF * 2));
        STAGED();
#pragma unroll
        for (int k = b0; k < min(b0 + 3, 8); ++k)
#pragma unroll
          for (int j = 0; j < 6; ++j) {
            const v8u o = tr_op(tr + (k - b0) * 40960 + (j & 1) * 20480 + min(t0 + (j >> 1), 9) * 32, 320);
            if (j < 4) wa[k * 4 + j] = o; else wv[k * 2 + j - 4] = o;
          }
        STAGED();
      }
    } else {
      const char* tr = lds + trow * 128 + w * 32 + (lane & 3) * 8;
      const unsigned vg = (w * 16 + (lane >> 3)) * (FF * 2) + 2560 + (u - 8) * 128 + (lane & 7) * 16, vd = (w * 16 + (lane >> 3)) * (DM * 2) + (u - 8) * 128 + (lane & 7) * 16;
#pragma unroll
      for (int k = 0; k < 32; ++k) dma16(k & 2 ? ru : rg, lds + (k >> 1) * 8192 + w * 2048 + (k & 1) * 1024, vg, ((k >> 2) * 64 + (k & 1) * 8) * (FF * 2));
      STAGED();
#pragma unroll
      for (int k = 0; k < 16; ++k) wv[k] = tr_op(tr + k * 8192, 128);
      STAGED();
#pragma unroll
      for (int b0 = 0; b0 < 28; b0 += 14) {
#pragma unroll
        for (int k = 0; k < 28; ++k) dma16(rd, lds + (k >> 1) * 8192 + w * 2048 + (k & 1) * 1024, vd, ((b0 + (k >> 1)) * 64 + (k & 1) * 8) * (DM * 2));
        STAGED();
#pragma unroll
        for (int k = 0; k < 14; ++k) wa[b0 + k] = tr_op(tr + k * 8192, 128);
        STAGED();
      }
    }
  }
  const unsigned one = (c >> 2) != g ? 0u : ((c & 1) ? 0x3f800000u : 0x3f80u), o0 = (c & 2) ? 0u : one, o1 = (c & 2) ? one : 0u;
  const v4u sl = (v4u){o0, o1, 0u, 0u}, sh_ = (v4u){0u, 0u, o0, o1};
  f2 wr[4];
  { const v4u q = *(const v4u*)((const char*)w_r + l16);
#pragma unroll
    for (int i = 0; i < 4; ++i) wr[i] = up2(q[i]); }
  char* sx = lds + w * 4096;
  const char* sa_ = sx + g * 32 + (c & 2) * 8;
  const int idx = (c & 2) ? 0xEEEE : 0x4444;
  int dead = 0, sd = 0, sb = 0, sa = L1 ? NSEG : 0, ssel = 0, rsel = 0, nrec = 0, told = L1, have = 0;
  unsigned ka = 0u, kb = 0u, best = 0u, f = 0u;
  unsigned long long pend = 0ull;
  const unsigned c1 = c2 ^ 0x2000u, pl = g * 16384 + r * 256 + c * 16, zr1 = nearof(Z_R1, c2), zd1 = nearof(Z_DEC, c1), zd2 = nearof(Z_DEC, c2);
  const unsigned fb = lane < 32 ? nearof(Z_FP + (L1 ? 0 : 16384 + g * 16384), c2) + c * 256 : zd2 + (L1 ? 512 : 0);
  const v4u rsv = mkrs((const void*)((const char*)err - Z_ERR), ZWS_BYTES);
  v4u xr = (v4u){0u, 0u, 0u, 0u};
  if (L1) xr = *(const v4u*)(xin + (size_t)r * DM + lane * 8);
  __builtin_amdgcn_s_waitcnt(0);
#define FADR (fb + (lane < 32 ? min(g ? (L1 ? sb + 1 : sa) : sb, NSEG - 1) : sd) * 4)
#define JOIN(n, k) asm volatile("s_mov_b64 exec, 1\n s_nop 3\n buffer_atomic_umax %0, off, %1, %2 offset:4 sc1\n s_mov_b64 exec, -1" :: "v"(k), "s"(rsv), "s"(zr1 + (n) * 64) : "memory")
#define ARRIVE(n) do { \
    v2u cm = (v2u){1u, 0u}; \
    asm volatile("s_mov_b64 exec, 1\n s_nop 3\n s_waitcnt vmcnt(0)\n buffer_atomic_add_x2 %0, off, %3, %4 sc0 sc1\n s_mov_b64 exec, -1\n" \
                 "buffer_load_dword %1, %2, %3, 0 offen sc1\n s_waitcnt vmcnt(0)" : "+&v"(cm), "=&v"(f) : "v"(FADR), "s"(rsv), "s"(zr1 + (n) * 64) : "memory"); \
    if (__builtin_amdgcn_readfirstlane(cm[0]) == 31) \
      asm volatile("s_nop 4\n buffer_store_dword %0, %1, %2, 0 offen sc1" :: "v"(__builtin_amdgcn_readfirstlane(cm[1])), "v"(lane < 5 ? (lane < 3 ? zd1 + lane * 512 : zd2 + (lane - 3) * 512) + (n) * 4 : OOR), "s"(rsv) : "memory"); \
    have = 1; } while (0)
#define GETP(V, s) do { const unsigned pb_ = nearof(Z_PART + (V) * MBY(16) + (s) * 262144, c2); _Pragma("unroll") for (int q = 0; q < 4; ++q) p[q] = ld4s(rs, pl, pb_ + q * 65536); } while (0)
  v4u p[4] = {xr, xr, xr, xr};
  int pre = 0;
  for (unsigned it = 0;; ++it) {
    int slot = -1, again = have;
    if (!have) { f = ld1c(rs, FADR); asm volatile("" : "+v"(f)); }
    have = 0;
    const unsigned nz = (unsigned)__ballot(f != 0u) | (dead ? ~0u : 0u), key = __builtin_amdgcn_readlane(f, 32);
    if (key != 0u || (dead && (sd < NSEG || told))) {
      if (sd == NSEG) slot = NSEG;
      else {
        if (sd == 0 || (key >> 16) > (best >> 16)) { best = key; rsel = (0xffff - (key & 0xffff)) & (S_TOK - 1); ssel = slot = sd; ++nrec; }
        ++sd; again = 1;
        if (!L1 && r == 1 && sd == NSEG) st1c(rs, lane < 3 ? zd1 + 2048 + lane * 512 + nrec * 4 : OOR, 3u);
      }
    }
    if (slot < 0) {
      if (!L1 && sd == NSEG && sb >= ssel) sb = NSEG;
      const bool ga = !L1 && sa < sb && (nz >> 16) == 0xffffu, gb = sb < NSEG && (nz & 0xffffu) == 0xffffu;
      if (ga || gb) {
        const int s = ga ? sa : sb, row = s * 32 + r;
        it = 0;
        if (!L1) xr = ld4s(rs, l16, ga && row == rsel ? blk(Z_X2P, s * 1024, c2) : blk(Z_X2, row * 1024, c2));
        if (ga) ++sa; else ++sb;
        f2 x[4], y[4], z[4];
        {
          f32x4 a0, a1;
#define SUMP() do { const f2 u0 = up2(xr[0]), u1 = up2(xr[1]), u2 = up2(xr[2]), u3 = up2(xr[3]); a0 = (f32x4){u0[0], u0[1], u1[0], u1[1]}; a1 = (f32x4){u2[0], u2[1], u3[0], u3[1]}; \
                    _Pragma("unroll") for (int q = 0; q < 4; ++q) { mfxv(false, a0, sl, p[q]); mfxv(false, a1, sh_, p[q]); } \
                    asm volatile("s_nop 7" : "+v"(a0), "+v"(a1)); } while (0)
          if (pre) SUMP();
          else {
            GETP(L1 ? 0 : (ga ? 2 : 1), s);
            if (!L1) { f = ld1c(rs, FADR); have = 1; }
            SUMP();
          }
          if (L1) {
            pre = sb < NSEG && (nz >> 16) == 0xffffu;
            if (pre) GETP(0, sb);
            xr = *(const v4u*)(xin + (size_t)min(row + 32, S_TOK - 1) * DM + lane * 8);
          }
          x[0] = (f2){a0[0], a0[1]}; x[1] = (f2){a0[2], a0[3]}; x[2] = (f2){a1[0], a1[1]}; x[3] = (f2){a1[2], a1[3]};
        }
        const unsigned xo = blk(ga ? Z_X3A : Z_X1, row * 2048, L1 || ga ? c2 : c1);
        st4s(rs, l16, xo, rb8(x));
        ln8(x, y);
        if (!L1) asm volatile("" : "+v"(f));
        st4s(rs, l16, xo + 1024, rb8(y));
        const f2 tt = y[3] * wr[3] + (y[2] * wr[2] + (y[1] * wr[1] + y[0] * wr[0]));
        float t = tt[0], t2 = tt[1];
#pragma unroll
        for (int i = 0; i < 4; ++i) x[i] += y[i];
        const v4u xp = rb8(x);
        if (L1) st4s(rs, l16, blk(Z_X2, row * 1024, c2), xp);
        else if (ga || s < ssel) st4s(ro, l16, row * 1024, xp);
        else { st4s(rs, l16, blk(Z_X2, row * 1024, c1), xp); pend |= 1ull << s; }
        row_sum2(t, t2);
        const unsigned k = mk_key(rbf(t + t2), row);
        if (L1) {
          JOIN(s, k);
          ln8(x, z);
          st4s(rs, l16, blk(Z_XN, row * 1024, c2), (v4u){cvt2(z[0]), cvt2(z[1]), cvt2(z[2]), cvt2(z[3])});
          __builtin_amdgcn_s_waitcnt(0x0f70);
          ARRIVE(s);
        } else if (ga) ka = lane == s ? k : ka;
        else kb = lane == s ? k : kb;
      } else if (sd == NSEG && !told && sa >= NSEG && sb >= NSEG) {
        JOIN(NSEG, wave_umax(lane < ssel ? kb : ka));
        ARRIVE(NSEG);
        told = 1; it = 0;
      } else if (!again) {
        if (it >= SPINCAP) { dead = 1; if (it >= 2 * SPINCAP) break; }
        __builtin_amdgcn_s_sleep(PSLEEP);
      }
      MEMFENCE();
      continue;
    }
    const int bi = (0xffff - (key & 0xffff)) & (S_TOK - 1);
    const bool va = slot == NSEG && (bi >> 5) >= ssel;
    const unsigned xo = blk(va ? Z_X3A : Z_X1, bi * 2048, slot < NSEG || va ? c2 : c1), ho = blk(Z_HB, slot * 4096, c2), xp_ = blk(Z_X2P, slot * 1024, c2);
    unsigned xe = 0u, ye = 0u;
    {
      *(v4u*)(sx + l16) = ld4c(rs, xo + 1024 + l16);
      if (!L1) { xe = ld1c(rs, xo + r * 32 + (c >> 1) * 4); ye = *(const unsigned*)(sx + r * 32 + (c >> 1) * 4); }
      v4u xb[8];
#pragma unroll
      for (int k = 0; k < 8; ++k) xb[k] = *(const v4u*)(sa_ + k * 128);
      f32x4 ac[6] = {ZACC, ZACC, ZACC, ZACC, ZACC, ZACC};
#pragma unroll
      for (int k = 0; k < 8; ++k) {
        if (L1) asm volatile("s_nop 1\n" SMF "%0, %4, %6, %5\n" SMF "%1, %4, %7, %5\n" SMF "%2, %4, %8, %5\n" SMF "%3, %4, %9, %5"
                             : "+v"(ac[0]), "+v"(ac[1]), "+v"(ac[2]), "+v"(ac[3]) : "v"(xb[k]), "v"(idx), "a"(wa[k * 4]), "a"(wa[k * 4 + 1]), "a"(wa[k * 4 + 2]), "a"(wa[k * 4 + 3]));
        asm volatile("s_nop 1\n" SMF "%0, %2, %4, %3\n" SMF "%1, %2, %5, %3" : "+v"(ac[4]), "+v"(ac[5]) : "v"(xb[k]), "v"(idx), "v"(wv[k * 2]), "v"(wv[k * 2 + 1]));
      }
      asm volatile("s_nop 7" : "+v"(ac[2]), "+v"(ac[3]), "+v"(ac[4]), "+v"(ac[5]));
      float gt = ac[4][0] + ac[4][2], up = ac[5][0] + ac[5][2];
      if (L1) { gt = g == 0 ? ac[0][0] + ac[0][2] : (g == 1 ? ac[2][0] + ac[2][2] : gt); up = g == 0 ? ac[1][0] + ac[1][2] : (g == 1 ? ac[3][0] + ac[3][2] : up); }
      const f2 gu = up2(cvt2((f2){gt, up}));
      const float hv = rbf(gu[0] * __builtin_amdgcn_rcpf(1.f + __expf(-gu[0]))) * gu[1];
      const unsigned pk = cvt2((f2){hv, __int_as_float(__builtin_amdgcn_update_dpp(0, __float_as_int(hv), 0xF5, 0xf, 0xf, false))});
      st1c(rs, !(c & 1) && g < (L1 ? (w < 2 ? 3 : 2) : 1) ? ho + (L1 ? u * 320 : 2560 + (u - 8) * 128) + (t0 + g) * 32 + c * 2 : OOR, min(pk, 0xfffffffeu));
    }
    if (!L1) {
      __builtin_amdgcn_s_sleep(MSLEEP);
      v4u q[4];
      for (unsigned i2 = 0;; ++i2) {
#pragma unroll
        for (int i = 0; i < 4; ++i) q[i] = ld4c(rs, i < 3 || lane < 32 ? ho + i * 1024 + l16 : OOR);
        if (!any_empty(__builtin_elementwise_max(__builtin_elementwise_max(q[0], q[1]), __builtin_elementwise_max(q[2], q[3]))) || dead) break;
        if (i2 >= SPINCAP) dead = 1;
        __builtin_amdgcn_s_sleep(2);
        MEMFENCE();
      }
#pragma unroll
      for (int i = 0; i < 4; ++i) *(v4u*)(sx + i * 1024 + l16) = q[i];
      f32x4 a0 = ZACC, a1 = ZACC;
#pragma unroll
      for (int k0 = 0; k0 < 28; k0 += 14) {
        v4u hb[14];
#pragma unroll
        for (int k = 0; k < 14; ++k) hb[k] = *(const v4u*)(sa_ + (k0 + k) * 128);
#pragma unroll
        for (int k = 0; k < 14; k += 2)
          asm volatile("s_nop 1\n" SMF "%0, %2, %5, %4\n" SMF "%1, %3, %6, %4" : "+v"(a0), "+v"(a1) : "v"(hb[k]), "v"(hb[k + 1]), "v"(idx), "a"(wa[k0 + k]), "a"(wa[k0 + k + 1]));
      }
      asm volatile("s_nop 7" : "+v"(a0), "+v"(a1));
      const float sg = rbf(__builtin_amdgcn_rcpf(1.f + __expf(-key_val(key))));
      const float o = up2(xe)[c & 1] + rbf(up2(ye)[c & 1] + rbf(rbf((a0[0] + a0[2]) + (a1[0] + a1[2])) * sg));
      const unsigned pk = cvt2((f2){o, __int_as_float(__builtin_amdgcn_update_dpp(0, __float_as_int(o), 0xF5, 0xf, 0xf, false))});
      const unsigned oo = (lane & 0x31) == 0 ? r * 32 + c * 2 : OOR;
      if (slot < NSEG) st1c(rs, oo + xp_, min(pk, 0xfffffffeu)); else st1c(ro, oo + bi * 1024, pk);
    }
    if (slot == NSEG) break;
    if (!L1 && r == 1) {
      v4u q;
      __builtin_amdgcn_s_sleep(MSLEEP2);
      for (unsigned i2 = 0;; ++i2) {
        q = ld4s(rs, l16, xp_);
        if (!any_empty(q) || dead) break;
        if (i2 >= SPINCAP) dead = 1;
        __builtin_amdgcn_s_sleep(2);
        MEMFENCE();
      }
      st4s(rs, l16, blk(Z_XNP, slot * 1024, c2), ln_row(q));
      vm0();
      st1c(rs, lane < 3 ? zd1 + 2048 + lane * 512 + (nrec - 1) * 4 : OOR, 4u + bi);
    }
    if (!L1) {
      unsigned long long m = pend & ((1ull << ssel) - 1ull);
      pend &= ~m; sa = max(sa, ssel);
      vm0();
      while (m) {
        v4u t[8]; int q[8];
#pragma unroll
        for (int i = 0; i < 8; ++i) { q[i] = m ? __builtin_ctzll(m) : q[i ? i - 1 : 0]; m &= m - 1ull; t[i] = ld4s(rs, l16, blk(Z_X2, (q[i] * 32 + r) * 1024, c1)); }
#pragma unroll
        for (int i = 0; i < 8; ++i) st4s(ro, l16, (q[i] * 32 + r) * 1024, t[i]);
      }
    }
    MEMFENCE();
    it = 0;
  }
  if (dead) st1c(rs, Z_ERR, 1u);
}
#define NPROBE 6
#define PACE_ARG
#define PACE (Pace{4400,3060,1800,2200,3250})
#define BIG_LDS 163840
#define IB1 8
#define IB2 (IB1 + 512)
#define IB3 (IB2 + (Z_ZEND - Z_FLG + 4095) / 4096)
#define IB4 (IB3 + (Z_SEND - Z_X2P + 4095) / 4096)
__global__ __launch_bounds__(256) void init_kernel(const u16* __restrict__ x, char* __restrict__ ws) {
  const rsrc_t rs = __builtin_amdgcn_make_buffer_rsrc((void*)ws, 0, ZWS_BYTES, 0x00020000);
  const int b = blockIdx.x, t = threadIdx.x;
  if (b < IB1) {
    const int p = t >> 6;
    if (t & 63) return;
    unsigned tm[2] = {~0u, ~0u};
    for (int q = 0; q < NPROBE; ++q) {
      const unsigned long long t0 = __builtin_amdgcn_s_memtime();
      asm volatile("s_waitcnt lgkmcnt(0)" ::: "memory");
      st1c(rs, p * MBY(2) + ((q ^ p) & 1) * 8192 + b * 128, 0u);
      vm0();
      tm[q & 1] = min(tm[q & 1], (unsigned)(__builtin_amdgcn_s_memtime() - t0));
    }
    st1c(rs, Z_NEAR + b * 16 + p * 4, (unsigned)min(max((int)(tm[0] - tm[1]), -200), 200));
    return;
  }
  if (b < IB2) {
    const int row = (b - IB1) * 4 + (t >> 6), lane = t & 63;
    const v4u y = ln_row(*(const v4u*)(x + (size_t)row * DM + lane * 8));
    st4c(rs, blk(Z_XN, row * 1024, 0u) + lane * 16, y);
    st4c(rs, blk(Z_XN, row * 1024, 0x2000u) + lane * 16, y);
    return;
  }
  if (b < IB3) {
    const unsigned o = Z_FLG + ((b - IB2) * 256 + t) * 16;
    if (o < Z_ZEND) st4c(rs, o, (v4u){0u, 0u, 0u, 0u});
    return;
  }
  const unsigned o = Z_X2P + ((b - IB3) * 256 + t) * 16;
  if (o < Z_SEND) st4c(rs, o, (v4u){~0u, ~0u, ~0u, ~0u});
}

__global__ __launch_bounds__(256, 1) void main_kernel(char* ws, const u16* x, const u16* mem0, const u16* Wq, const u16* Wk, const u16* Wv, const u16* Wo, const u16* w_r,
                                                      const u16* wg, const u16* wu, const u16* wd, u16* out PACE_ARG) {
  extern __shared__ char lds[];
  const int b = blockIdx.x, xc = b & 7, i = b >> 3, w = __builtin_amdgcn_readfirstlane(threadIdx.x >> 6);
  const v4u n0 = *(const v4u*)(ws + Z_NEAR), n4 = *(const v4u*)(ws + Z_NEAR + 64), n5 = *(const v4u*)(ws + Z_NEAR + 80), nd = n0 + n0 - n4 - n5;
  const unsigned c1 = __builtin_amdgcn_readfirstlane((int)(nd[0] + nd[1] + nd[2] + nd[3]) > 0 ? 0x2000u : 0u);
  if (xc >= 6 && (i >> 3) == 2) {
    const rsrc_t rs = __builtin_amdgcn_make_buffer_rsrc((void*)ws, 0, ZWS_BYTES, 0x00020000), ro = __builtin_amdgcn_make_buffer_rsrc((void*)out, 0, S_TOK * DM * 2, 0x00020000);
    unsigned* err = (unsigned*)(ws + Z_ERR);
    if (xc == 7) l_role8<true>(w, rs, ro, lds, i - 16, c1 ^ 0x2000u, x, w_r, wg, wu, wd, err); else l_role8<false>(w, rs, ro, lds, i - 8, c1 ^ 0x2000u, x, w_r, wg, wu, wd, err);
    return;
  }
  if (xc >= 6) o_role8(w, i >= 16 ? 1 : (xc == 7 ? 0 : 2), i >= 16 ? (i & 7) + (xc & 1) * 8 : i, c1, ws, Wo);
  if (xc >= 1 && xc <= 3) t_role8(w, xc - 1, i, c1, ws, xc == 1 ? Wk : (xc == 2 ? Wv : Wq));
  if (xc == 5) chain8<true>(w, i, 2, c1, ws, lds, mem0, Wq, Wk, Wv, Wo, PACE);
  chain8<false>(w, i, xc >> 2, c1, ws, lds, mem0, Wq, Wk, Wv, Wo, PACE);
}
extern "C" void kernel_launch(void* const* d_in, const int* in_sizes, int n_in, void* d_out, int out_size,
                              void* d_ws, size_t ws_size, hipStream_t stream) {
  char* ws = (char*)(((size_t)d_ws + MBY(2) - 1) & ~(size_t)(MBY(2) - 1));
  (void)hipFuncSetAttribute((const void*)main_kernel, hipFuncAttributeMaxDynamicSharedMemorySize, BIG_LDS);
  init_kernel<<<IB4, 256, 0, stream>>>((const u16*)d_in[0], ws);
  main_kernel<<<256, 256, BIG_LDS, stream>>>(ws, (const u16*)d_in[0], (const u16*)d_in[1], (const u16*)d_in[2], (const u16*)d_in[3], (const u16*)d_in[4], (const u16*)d_in[5],
                                             (const u16*)d_in[8], (const u16*)d_in[9], (const u16*)d_in[10], (const u16*)d_in[11], (u16*)d_out);
}
```

```cpp
#include <hip/hip_runtime.h>
#include <hip/hip_bf16.h>
#include <stdint.h>

typedef unsigned short u16;
using bf16x8 = __attribute__((ext_vector_type(8))) short;
using f32x4 = __attribute__((ext_vector_type(4))) float;

#define S_TOK 2048
#define DM 512
#define NH 32
#define DH 128
#define HD 4096
#define MEMT 8
#define SEG 32
#define NSEG 64
#define FF 1792

__device__ __forceinline__ float bf2f(u16 v) { return __uint_as_float(((unsigned)v) << 16); }
__device__ __forceinline__ float rbf(float f) { return (float)(__bf16)f; }

template <int N> __device__ __forceinline__ float ror_add(float v) {
  return v + __int_as_float(__builtin_amdgcn_update_dpp(0, __float_as_int(v), 0x120 + N, 0xf, 0xf, false));
}

typedef unsigned v4u __attribute__((ext_vector_type(4)));
typedef unsigned v2u __attribute__((ext_vector_type(2)));
typedef unsigned v8u __attribute__((ext_vector_type(8)));
typedef __bf16 bfx8 __attribute__((ext_vector_type(8)));
typedef __bf16 bfx16 __attribute__((ext_vector_type(16)));
typedef short s16x4 __attribute__((ext_vector_type(4)));
typedef __attribute__((address_space(3))) s16x4 lds_s16x4;
#define AUX_SC1 16
#define SPINCAP (1u << 19)

__device__ __forceinline__ unsigned pk_bf16(float a, float b) {
  typedef __bf16 bfx2 __attribute__((ext_vector_type(2)));
  bfx2 v; v[0] = (__bf16)a; v[1] = (__bf16)b;
  return __builtin_bit_cast(unsigned, v);
}
__device__ __forceinline__ f32x4 mf32(v4u a, v4u b, f32x4 c) {
  return __builtin_amdgcn_mfma_f32_16x16x32_bf16(__builtin_bit_cast(bfx8, a), __builtin_bit_cast(bfx8, b), c, 0, 0, 0);
}
#define MBY(x) ((unsigned)(x) << 20)
#define SEGST 2621440u
#define Z_PART MBY(172)
#define Z_CH MBY(220)
#define Z_XN MBY(226)
#define Z_X2 MBY(230)
#define Z_X1 MBY(234)
#define Z_X3A MBY(242)
#define Z_FLG MBY(250)
#define Z_FT (Z_FLG)
#define Z_FO (Z_FT + 32u * 16384u)
#define Z_RS (Z_FO + 3u * 16384u)
#define Z_FP (Z_RS + 3u * 16384u)
#define Z_R1 (Z_FP + 3u * 16384u)
#define Z_DEC (Z_R1 + 16384u)
#define Z_ERR (Z_DEC + 16384u)
#define Z_ZEND (Z_ERR + 128u)
#define Z_X2P (MBY(251))
#define Z_HB (Z_X2P + 131072u)
#define Z_SEND (Z_HB + 33u * 16384u)
#define Z_DBG MBY(252)
#define ZWS_BYTES MBY(252)
#define OOR 0x80000000u
__host__ __device__ __forceinline__ unsigned nearof(unsigned o, unsigned c13) { return o | (((o >> 8) ^ c13) & 0x2000u); }
__host__ __device__ __forceinline__ unsigned blk(unsigned base, unsigned o, unsigned c13) { return nearof(base + (o >> 13 << 14), c13) + (o & 8191u); }
typedef __amdgpu_buffer_rsrc_t rsrc_t;
__device__ __forceinline__ v4u ld4(rsrc_t rs, unsigned off) { return __builtin_amdgcn_raw_buffer_load_b128(rs, off, 0, 0); }
__device__ __forceinline__ v4u ld4c(rsrc_t rs, unsigned off) { return __builtin_amdgcn_raw_buffer_load_b128(rs, off, 0, AUX_SC1); }
__device__ __forceinline__ v2u ld2c(rsrc_t rs, unsigned off) { return __builtin_amdgcn_raw_buffer_load_b64(rs, off, 0, AUX_SC1); }
__device__ __forceinline__ unsigned ld1c(rsrc_t rs, unsigned off) { return __builtin_amdgcn_raw_buffer_load_b32(rs, off, 0, AUX_SC1); }
__device__ __forceinline__ void st4c(rsrc_t rs, unsigned off, v4u v) { __builtin_amdgcn_raw_buffer_store_b128(v, rs, off, 0, AUX_SC1); }
__device__ __forceinline__ void st1c(rsrc_t rs, unsigned off, unsigned v) { __builtin_amdgcn_raw_buffer_store_b32(v, rs, off, 0, AUX_SC1); }
__device__ __forceinline__ v4u ld4s(rsrc_t rs, unsigned off, unsigned so) { return __builtin_amdgcn_raw_buffer_load_b128(rs, off, so, AUX_SC1); }
__device__ __forceinline__ void st4s(rsrc_t rs, unsigned off, unsigned so, v4u v) {
  __builtin_amdgcn_raw_buffer_store_b128(v, rs, off, so, AUX_SC1);
  asm volatile("s_nop 1" :: "v"(v) : "memory");
}
__device__ __forceinline__ void vm0() { asm volatile("s_waitcnt vmcnt(0)" ::: "memory"); }
#define XSTR 1040
#define PSLEEP 1
#define ZACC ((f32x4){0.f, 0.f, 0.f, 0.f})
__device__ __forceinline__ void mfw(bool first, f32x4& acc, const v4u& wa, const v4u& xb) {
  if (first) asm volatile("s_nop 1\n v_mfma_f32_16x16x32_bf16 %0, %1, %2, 0" : "=&v"(acc) : "a"(wa), "v"(xb));
  else asm volatile("s_nop 1\n v_mfma_f32_16x16x32_bf16 %0, %1, %2, %0" : "+v"(acc) : "a"(wa), "v"(xb));
}
__device__ __forceinline__ void mfence4(f32x4& a, f32x4& b, f32x4& c, f32x4& d) { asm volatile("s_nop 7\n s_nop 7" : "+v"(a), "+v"(b), "+v"(c), "+v"(d)); }
__device__ __forceinline__ void unpack8(v4u v, float (&x)[8]) {
#pragma unroll
  for (int i = 0; i < 4; ++i) { x[2 * i] = __uint_as_float(v[i] << 16); x[2 * i + 1] = __uint_as_float(v[i] & 0xffff0000u); }
}

__device__ __forceinline__ void mfxv(bool first, f32x4& acc, const v4u& xa, const v4u& wb) {
  if (first) asm volatile("s_nop 1\n v_mfma_f32_16x16x32_bf16 %0, %1, %2, 0" : "=&v"(acc) : "v"(xa), "v"(wb));
  else asm volatile("s_nop 1\n v_mfma_f32_16x16x32_bf16 %0, %1, %2, %0" : "+v"(acc) : "v"(xa), "v"(wb));
}
__device__ __forceinline__ int lane_id() { int l; asm volatile("v_mbcnt_lo_u32_b32 %0, -1, 0\n v_mbcnt_hi_u32_b32 %0, -1, %0" : "=v"(l)); return l; }
typedef __attribute__((address_space(1))) unsigned gu32;
typedef __attribute__((address_space(1))) unsigned long long gu64;
#define RLX_AGENT __ATOMIC_RELAXED, __HIP_MEMORY_SCOPE_AGENT
__device__ __forceinline__ unsigned wait_word(unsigned* p) {
  unsigned v = 0u;
  for (unsigned it = 0; it < SPINCAP && v == 0u; ++it) { v = __builtin_amdgcn_readfirstlane(__hip_atomic_load((gu32*)p, RLX_AGENT)); if (v == 0u) __builtin_amdgcn_s_sleep(2); }
  return v;
}
#define CL_K 122880
#define CL_Q (CL_K + 16384)
#define CL_MEM (CL_K + 24576)
#define CL_QX (CL_K + 32768)
#define CL_SEL (CL_QX + 2560 + 256)
#define CL_VX (CL_MEM + 2304)
#define CL_PX (CL_MEM + 4352)
#define CL_OX CL_QX
__device__ __forceinline__ v4u mkrs(const void* p, unsigned bytes) {
  return (v4u){(unsigned)(size_t)p, (unsigned)((size_t)p >> 32) & 0xffffu, bytes, 0x00020000u};
}
#define T0D 150
struct Pace { int per, gat, late, rsd, fck, slip, xl; };
template <bool RST>
__device__ __forceinline__ void chain8(int w, int h, int V, const unsigned* rdy, char* ws, char* lds, const u16* Wq, const u16* Wk, const u16* Wv, const u16* Wo, const Pace tm) {
  const int lane = lane_id(), c = lane & 15, g = lane >> 4, l16 = lane * 16;
  const unsigned c1 = 0u, cm = V ? c1 ^ 0x2000u : c1, ps = nearof(Z_CH + V * MBY(2), cm), mo = ps + MBY(1);
  const int gp = (h >> 1) & 3, sp = (h >> 3) * 2 + (h & 1);
  if (w == 0) {
    const unsigned one = (c & 1) ? 0x3f800000u : 0x3f80u;
    *(v4u*)(lds + CL_SEL + l16) = (v4u){(c >> 1) == 0 ? one : 0u, (c >> 1) == 1 ? one : 0u, (c >> 1) == 2 ? one : 0u, (c >> 1) == 3 ? one : 0u};
  }
  const int tk = 2 * (c >> 2) + (c & 1), hf = (c >> 1) & 1, j = 8 * w + (lane >> 3), q = (lane >> 2) & 3, p = lane & 3;
  const int pv[40] = {w, (int)ps, h * 16384, (int)mo, h * 16384 + w * 2048, (int)cm, (V == 1 ? 4 : 3) * 524288 + h * 16384 + (w & 1) * 4096 + (w >> 1) * 256, (int)cm,
                      (int)((nearof(Z_FO + V * 16384, c1 ^ 0x2000u) + h * 256) | OOR), (int)nearof(Z_FT + h * 16384, cm), (int)nearof(Z_RS + V * 16384, cm),
                      (int)(nearof(Z_CH + MBY(2), cm) + MBY(1)), -1, (int)nearof(Z_X2P, cm), 0, h * 4, (int)nearof(Z_RS + (V == 2 ? 1 : V) * 16384, cm), T0D, (int)Z_ERR, (int)(Z_DBG + (V * 32 + h) * 4096),
                      (int)(size_t)Wq, (int)((size_t)Wq >> 32) & 0xffff, (int)(size_t)Wk, (int)((size_t)Wk >> 32) & 0xffff, (int)(size_t)Wv, (int)((size_t)Wv >> 32) & 0xffff, (int)(size_t)Wo, (int)((size_t)Wo >> 32) & 0xffff,
                      (int)(size_t)ws, (int)((size_t)ws >> 32) & 0xffff, (int)ZWS_BYTES, tm.per, tm.gat, tm.late, tm.rsd, tm.fck, (int)(size_t)rdy, (int)((size_t)rdy >> 32), tm.slip, tm.xl};
  int hv0 = l16, hv1 = hf ? 0xEEEE : 0x4444, hv2 = CL_MEM + tk * 128 + ((2 * g + hf) ^ (c >= 8 ? 4 : 0)) * 16;
  int hv3 = CL_QX + 2 * g * 288 + (32 * w + 2 * c) * 2, hv4 = hv3 - CL_QX + CL_MEM, hv5 = CL_QX + (c & 7) * 288 + g * 16, hv6 = CL_MEM + (2 * ((c >> 2) ^ (c & 2)) + (c & 1)) * 288 + g * 16;
  int hv7 = w * 30720 + l16, hv8 = CL_PX + w * 512 + g * 128 + (c & 7) * 16, hv9 = CL_K + w * 2048 + l16, hv10 = CL_VX + w * 512 + lane * 8, hv11 = CL_OX + (4 * w + g) * 128 + (c & 7) * 16, hv12 = CL_OX + (2 * g + hf) * 128 + tk * 16;
  int hv13 = j * 16384 + ((h + j) & 31) * 128 + (lane & 7) * 16;
  int hv14 = w ? CL_Q + (w & 1) * 4096 + l16 : ((lane & 0x28) ? OOR : sp * 1024 + (2 * gp + g) * 128 + ((lane & 7) ^ (gp >= 2 ? 4 : 0)) * 16), hv15 = CL_PX + (c >> 3) * 1024 + g * 128 + (c & 7) * 16, hv20 = 0;
  int hv16 = (w * 16 + g) * 8192 + h * 256 + (c ^ ((g & 1) * 2)) * 16, hv17 = (h * 128 + 16 * (w & 1) + 8 * (w >> 1)) * 1024 + l16;
  int hv18 = (g & 1) * 4480 + (q & 1) * 1088 + (2 * (g >> 1) + (q >> 1)) * 256 + 64 * w + 32 * (q >> 1) + 8 * p, hv19 = (lane >> 2) * 1040 + 256 * w + 64 * p;
#pragma unroll
  for (int i = 0; i < 40; ++i) hv20 = lane == i ? pv[i] : hv20;
  if constexpr (RST) {
  asm volatile(
    "s_mov_b32 s33, 0\n"
    "s_nop 1\n"
    "v_mov_b32 v119, v20\n"
    "s_nop 0\n"
    "v_readlane_b32 s61, v119, 0\n"
    "v_readlane_b32 s46, v119, 1\n"
    "v_readlane_b32 s63, v119, 2\n"
    "v_readlane_b32 s48, v119, 3\n"
    "v_readlane_b32 s55, v119, 4\n"
    "v_readlane_b32 s56, v119, 5\n"
    "v_readlane_b32 s57, v119, 6\n"
    "v_readlane_b32 s74, v119, 7\n"
    "v_readlane_b32 s58, v119, 8\n"
    "v_readlane_b32 s59, v119, 9\n"
    "v_readlane_b32 s60, v119, 10\n"
    "v_readlane_b32 s67, v119, 11\n"
    "v_readlane_b32 s68, v119, 12\n"
    "v_readlane_b32 s69, v119, 13\n"
    "v_readlane_b32 s70, v119, 14\n"
    "v_readlane_b32 s71, v119, 15\n"
    "v_readlane_b32 s73, v119, 16\n"
    "v_readlane_b32 s37, v119, 17\n"
    "v_readlane_b32 s92, v119, 36\n"
    "v_readlane_b32 s93, v119, 37\n"
    "v_readlane_b32 s35, v119, 18\n"
    "v_readlane_b32 s75, v119, 19\n"
    "v_readlane_b32 s16, v119, 28\n"
    "v_readlane_b32 s17, v119, 29\n"
    "v_readlane_b32 s18, v119, 30\n"
    "v_readlane_b32 s8, v119, 31\n"
    "v_readlane_b32 s9, v119, 32\n"
    "v_readlane_b32 s10, v119, 33\n"
    "v_readlane_b32 s11, v119, 34\n"
    "v_readlane_b32 s12, v119, 35\n"
    "v_readlane_b32 s13, v119, 38\n"
    "v_readlane_b32 s21, v119, 39\n"
    "s_mov_b32 s19, 0x20000\n"
    "v_readlane_b32 s84, v119, 20\n"
    "v_readlane_b32 s85, v119, 21\n"
    "v_readlane_b32 s86, v119, 22\n"
    "v_readlane_b32 s87, v119, 23\n"
    "v_readlane_b32 s88, v119, 24\n"
    "v_readlane_b32 s89, v119, 25\n"
    "v_readlane_b32 s90, v119, 26\n"
    "v_readlane_b32 s91, v119, 27\n"
    "s_mov_b32 s76, 0xff00ff00\n"
    "s_mov_b32 s77, 0xff00ff00\n"
    "s_mov_b32 s78, 0x00ff00ff\n"
    "s_mov_b32 s79, 0x00ff00ff\n"
    "s_mov_b32 s65, 0x3e0293ee\n"
    "s_mov_b32 s66, 0xfffefffe\n"
    "s_lshl_b32 s62, s61, 11\n"
    "s_mov_b32 s36, 0\n"
    "s_mov_b32 s64, 0\n"
    "s_mov_b32 s50, 0\n"
    "s_lshl_b32 s72, s61, 2\n"
    "s_add_u32 s72, s72, 158208\n"
    "s_add_u32 s47, s46, 4096\n"
    "s_and_b32 s20, s61, 1\n"
    "s_lshl_b32 s20, s20, 7\n"
    "s_mov_b32 s14, 0x3f803f80\n"
    "s_mov_b32 s15, 0x3f803f80\n"
    "s_mov_b32 s82, 0x400000\n"
    "s_mov_b32 s83, 0x20000\n"
    "v_mov_b32 v112, v16\n"
    "v_mov_b32 v113, v17\n"
    "v_mov_b32 v114, v18\n"
    "v_mov_b32 v115, v19\n"
    "v_mov_b32 v117, 0\n"
    "v_mov_b32 v118, 0\n"
    "s_memrealtime s[42:43]\n"
    "s_cmp_eq_u32 s61, 1\n"
    "s_cselect_b64 exec, 1, 0\n"
    "s_waitcnt lgkmcnt(0)\n"
    "s_add_u32 s44, s42, s37\n"
    "s_or_b32 s44, s44, 3\n"
    "v_mov_b32 v16, s44\n"
    "v_mov_b32 v17, 0\n"
    "s_cbranch_execz LCS%=\n"
    "global_atomic_cmpswap v16, v118, v[16:17], s[92:93] offset:1024 sc0 sc1\n"
    "LCS%=:\n"
    "s_mov_b64 exec, -1\n"
    "s_mov_b32 s80, s90\n"
    "s_mov_b32 s81, s91\n"
    "s_mul_i32 s38, s61, 4160\n"
    "s_add_u32 m0, s38, 0\n"
    "s_mov_b32 s39, 0\n"
    "buffer_load_dwordx4 v113, s[80:83], s39 offen lds\n"
    "s_add_u32 m0, s38, 1040\n"
    "s_mov_b32 s39, 1024\n"
    "buffer_load_dwordx4 v113, s[80:83], s39 offen lds\n"
    "s_add_u32 m0, s38, 2080\n"
    "s_mov_b32 s39, 4096\n"
    "buffer_load_dwordx4 v113, s[80:83], s39 offen lds\n"
    "s_add_u32 m0, s38, 3120\n"
    "s_mov_b32 s39, 5120\n"
    "buffer_load_dwordx4 v113, s[80:83], s39 offen lds\n"
    "s_mul_i32 s38, s61, 4160\n"
    "s_add_u32 m0, s38, 17920\n"
    "s_mov_b32 s39, 2048\n"
    "buffer_load_dwordx4 v113, s[80:83], s39 offen lds\n"
    "s_add_u32 m0, s38, 18960\n"
    "s_mov_b32 s39, 3072\n"
    "buffer_load_dwordx4 v113, s[80:83], s39 offen lds\n"
    "s_add_u32 m0, s38, 20000\n"
    "s_mov_b32 s39, 6144\n"
    "buffer_load_dwordx4 v113, s[80:83], s39 offen lds\n"
    "s_add_u32 m0, s38, 21040\n"
    "s_mov_b32 s39, 7168\n"
    "buffer_load_dwordx4 v113, s[80:83], s39 offen lds\n"
    "s_cmp_lg_u32 s61, 1\n"
    "s_cbranch_scc1 LCE%=\n"
    "s_waitcnt vmcnt(8)\n"
    "v_readfirstlane_b32 s38, v16\n"
    "s_cmp_eq_u32 s38, 0\n"
    "s_cselect_b32 s44, s44, s38\n"
    "s_add_u32 s44, s44, 1\n"
    "s_mov_b32 s45, 0\n"
    "s_mov_b32 s37, 8\n"
    "LCB%=:\n"
    "s_memrealtime s[42:43]\n"
    "s_memtime s[40:41]\n"
    "s_waitcnt lgkmcnt(0)\n"
    "s_sub_u32 s42, s42, s44\n"
    "s_cmp_gt_u32 s42, -2000\n"
    "s_cbranch_scc1 LCB%=\n"
    "s_mul_i32 s42, s42, 24\n"
    "s_sub_u32 s40, s40, s42\n"
    "s_cmp_eq_u32 s37, 8\n"
    "s_cselect_b32 s54, s40, s54\n"
    "s_sub_u32 s40, s40, s54\n"
    "s_add_u32 s45, s45, s40\n"
    "s_add_u32 s44, s44, 8\n"
    "s_sub_u32 s37, s37, 1\n"
    "s_cmp_lg_u32 s37, 0\n"
    "s_cbranch_scc1 LCB%=\n"
    "s_ashr_i32 s45, s45, 3\n"
    "s_add_u32 s45, s45, s54\n"
    "s_waitcnt vmcnt(0)\n"
    "s_branch LCG%=\n"
    "LCE%=:\n"
    "s_cmp_lg_u32 s61, 0\n"
    "s_cbranch_scc1 LCF2_%=\n"
    "s_mul_i32 s38, s61, 4160\n"
    "s_add_u32 m0, s38, 40000\n"
    "s_mov_b32 s39, 49152\n"
    "buffer_load_dwordx4 v113, s[80:83], s39 offen lds\n"
    "s_add_u32 m0, s38, 41040\n"
    "s_mov_b32 s39, 50176\n"
    "buffer_load_dwordx4 v113, s[80:83], s39 offen lds\n"
    "s_add_u32 m0, s38, 42080\n"
    "s_mov_b32 s39, 53248\n"
    "buffer_load_dwordx4 v113, s[80:83], s39 offen lds\n"
    "s_add_u32 m0, s38, 43120\n"
    "s_mov_b32 s39, 54272\n"
    "buffer_load_dwordx4 v113, s[80:83], s39 offen lds\n"
    "LCF2_%=:\n"
    "s_mul_i32 s38, s61, 4160\n"
    "s_add_u32 m0, s38, 35840\n"
    "s_mov_b32 s39, 32768\n"
    "buffer_load_dwordx4 v113, s[80:83], s39 offen lds\n"
    "s_add_u32 m0, s38, 36880\n"
    "s_mov_b32 s39, 33792\n"
    "buffer_load_dwordx4 v113, s[80:83], s39 offen lds\n"
    "s_add_u32 m0, s38, 37920\n"
    "s_mov_b32 s39, 36864\n"
    "buffer_load_dwordx4 v113, s[80:83], s39 offen lds\n"
    "s_add_u32 m0, s38, 38960\n"
    "s_mov_b32 s39, 37888\n"
    "buffer_load_dwordx4 v113, s[80:83], s39 offen lds\n"
    "s_cmp_lg_u32 s61, 0\n"
    "s_cbranch_scc1 LCF3_%=\n"
    "s_mul_i32 s38, s61, 4160\n"
    "s_add_u32 m0, s38, 57920\n"
    "s_mov_b32 s39, 51200\n"
    "buffer_load_dwordx4 v113, s[80:83], s39 offen lds\n"
    "s_add_u32 m0, s38, 58960\n"
    "s_mov_b32 s39, 52224\n"
    "buffer_load_dwordx4 v113, s[80:83], s39 offen lds\n"
    "s_add_u32 m0, s38, 60000\n"
    "s_mov_b32 s39, 55296\n"
    "buffer_load_dwordx4 v113, s[80:83], s39 offen lds\n"
    "s_add_u32 m0, s38, 61040\n"
    "s_mov_b32 s39, 56320\n"
    "buffer_load_dwordx4 v113, s[80:83], s39 offen lds\n"
    "LCF3_%=:\n"
    "s_mul_i32 s38, s61, 4160\n"
    "s_add_u32 m0, s38, 53760\n"
    "s_mov_b32 s39, 34816\n"
    "buffer_load_dwordx4 v113, s[80:83], s39 offen lds\n"
    "s_add_u32 m0, s38, 54800\n"
    "s_mov_b32 s39, 35840\n"
    "buffer_load_dwordx4 v113, s[80:83], s39 offen lds\n"
    "s_add_u32 m0, s38, 55840\n"
    "s_mov_b32 s39, 38912\n"
    "buffer_load_dwordx4 v113, s[80:83], s39 offen lds\n"
    "s_add_u32 m0, s38, 56880\n"
    "s_mov_b32 s39, 39936\n"
    "buffer_load_dwordx4 v113, s[80:83], s39 offen lds\n"
    "LCG%=:\n"
    "s_waitcnt vmcnt(12)\n"
    "s_barrier\n"
    "s_mul_i32 s38, s61, 4160\n"
    "s_add_u32 m0, s38, 71680\n"
    "s_mov_b32 s39, 65536\n"
    "buffer_load_dwordx4 v113, s[80:83], s39 offen lds\n"
    "s_add_u32 m0, s38, 72720\n"
    "s_mov_b32 s39, 66560\n"
    "buffer_load_dwordx4 v113, s[80:83], s39 offen lds\n"
    "s_add_u32 m0, s38, 73760\n"
    "s_mov_b32 s39, 69632\n"
    "buffer_load_dwordx4 v113, s[80:83], s39 offen lds\n"
    "s_add_u32 m0, s38, 74800\n"
    "s_mov_b32 s39, 70656\n"
    "buffer_load_dwordx4 v113, s[80:83], s39 offen lds\n"
    "v_add_u32 v116, 0, v115\n"
    "ds_read_b64_tr_b16 v[168:169], v116 offset:0\n"
    "ds_read_b64_tr_b16 v[184:185], v116 offset:8\n"
    "ds_read_b64_tr_b16 v[200:201], v116 offset:16\n"
    "ds_read_b64_tr_b16 v[216:217], v116 offset:24\n"
    "ds_read_b64_tr_b16 v[232:233], v116 offset:32\n"
    "ds_read_b64_tr_b16 v[248:249], v116 offset:40\n"
    "ds_read_b64_tr_b16 v[104:105], v116 offset:48\n"
    "ds_read_b64_tr_b16 v[32:33], v116 offset:56\n"
    "s_waitcnt lgkmcnt(0)\n"
    "s_waitcnt vmcnt(12)\n"
    "s_barrier\n"
    "s_mul_i32 s38, s61, 4160\n"
    "s_add_u32 m0, s38, 89600\n"
    "s_mov_b32 s39, 67584\n"
    "buffer_load_dwordx4 v113, s[80:83], s39 offen lds\n"
    "s_add_u32 m0, s38, 90640\n"
    "s_mov_b32 s39, 68608\n"
    "buffer_load_dwordx4 v113, s[80:83], s39 offen lds\n"
    "s_add_u32 m0, s38, 91680\n"
    "s_mov_b32 s39, 71680\n"
    "buffer_load_dwordx4 v113, s[80:83], s39 offen lds\n"
    "s_add_u32 m0, s38, 92720\n"
    "s_mov_b32 s39, 72704\n"
    "buffer_load_dwordx4 v113, s[80:83], s39 offen lds\n"
    "v_add_u32 v116, 17920, v115\n"
    "ds_read_b64_tr_b16 v[170:171], v116 offset:0\n"
    "ds_read_b64_tr_b16 v[186:187], v116 offset:8\n"
    "ds_read_b64_tr_b16 v[202:203], v116 offset:16\n"
    "ds_read_b64_tr_b16 v[218:219], v116 offset:24\n"
    "ds_read_b64_tr_b16 v[234:235], v116 offset:32\n"
    "ds_read_b64_tr_b16 v[250:251], v116 offset:40\n"
    "ds_read_b64_tr_b16 v[106:107], v116 offset:48\n"
    "ds_read_b64_tr_b16 v[34:35], v116 offset:56\n"
    "s_waitcnt lgkmcnt(0)\n"
    "s_waitcnt vmcnt(12)\n"
    "s_barrier\n"
    "s_mul_i32 s38, s61, 4160\n"
    "s_add_u32 m0, s38, 0\n"
    "s_mov_b32 s39, 98304\n"
    "buffer_load_dwordx4 v113, s[80:83], s39 offen lds\n"
    "s_add_u32 m0, s38, 1040\n"
    "s_mov_b32 s39, 99328\n"
    "buffer_load_dwordx4 v113, s[80:83], s39 offen lds\n"
    "s_add_u32 m0, s38, 2080\n"
    "s_mov_b32 s39, 102400\n"
    "buffer_load_dwordx4 v113, s[80:83], s39 offen lds\n"
    "s_add_u32 m0, s38, 3120\n"
    "s_mov_b32 s39, 103424\n"
    "buffer_load_dwordx4 v113, s[80:83], s39 offen lds\n"
    "v_add_u32 v116, 35840, v115\n"
    "ds_read_b64_tr_b16 v[172:173], v116 offset:0\n"
    "ds_read_b64_tr_b16 v[188:189], v116 offset:8\n"
    "ds_read_b64_tr_b16 v[204:205], v116 offset:16\n"
    "ds_read_b64_tr_b16 v[220:221], v116 offset:24\n"
    "ds_read_b64_tr_b16 v[236:237], v116 offset:32\n"
    "ds_read_b64_tr_b16 v[252:253], v116 offset:40\n"
    "ds_read_b64_tr_b16 v[108:109], v116 offset:48\n"
    "ds_read_b64_tr_b16 v[36:37], v116 offset:56\n"
    "s_waitcnt lgkmcnt(0)\n"
    "s_waitcnt vmcnt(12)\n"
    "s_barrier\n"
    "s_mul_i32 s38, s61, 4160\n"
    "s_add_u32 m0, s38, 17920\n"
    "s_mov_b32 s39, 100352\n"
    "buffer_load_dwordx4 v113, s[80:83], s39 offen lds\n"
    "s_add_u32 m0, s38, 18960\n"
    "s_mov_b32 s39, 101376\n"
    "buffer_load_dwordx4 v113, s[80:83], s39 offen lds\n"
    "s_add_u32 m0, s38, 20000\n"
    "s_mov_b32 s39, 104448\n"
    "buffer_load_dwordx4 v113, s[80:83], s39 offen lds\n"
    "s_add_u32 m0, s38, 21040\n"
    "s_mov_b32 s39, 105472\n"
    "buffer_load_dwordx4 v113, s[80:83], s39 offen lds\n"
    "v_add_u32 v116, 53760, v115\n"
    "ds_read_b64_tr_b16 v[174:175], v116 offset:0\n"
    "ds_read_b64_tr_b16 v[190:191], v116 offset:8\n"
    "ds_read_b64_tr_b16 v[206:207], v116 offset:16\n"
    "ds_read_b64_tr_b16 v[222:223], v116 offset:24\n"
    "ds_read_b64_tr_b16 v[238:239], v116 offset:32\n"
    "ds_read_b64_tr_b16 v[254:255], v116 offset:40\n"
    "ds_read_b64_tr_b16 v[110:111], v116 offset:48\n"
    "ds_read_b64_tr_b16 v[38:39], v116 offset:56\n"
    "s_waitcnt lgkmcnt(0)\n"
    "s_waitcnt vmcnt(12)\n"
    "s_barrier\n"
    "s_mov_b32 s80, s86\n"
    "s_mov_b32 s81, s87\n"
    "s_mul_i32 s38, s61, 4480\n"
    "s_add_u32 m0, s38, 35840\n"
    "s_mov_b32 s39, 0\n"
    "buffer_load_dwordx4 v112, s[80:83], s39 offen lds\n"
    "s_add_u32 m0, s38, 36928\n"
    "s_mov_b32 s39, 32768\n"
    "buffer_load_dwordx4 v112, s[80:83], s39 offen lds\n"
    "s_add_u32 m0, s38, 38016\n"
    "s_mov_b32 s39, 65536\n"
    "buffer_load_dwordx4 v112, s[80:83], s39 offen lds\n"
    "s_add_u32 m0, s38, 39104\n"
    "s_mov_b32 s39, 98304\n"
    "buffer_load_dwordx4 v112, s[80:83], s39 offen lds\n"
    "v_add_u32 v116, 71680, v115\n"
    "ds_read_b64_tr_b16 v[176:177], v116 offset:0\n"
    "ds_read_b64_tr_b16 v[192:193], v116 offset:8\n"
    "ds_read_b64_tr_b16 v[208:209], v116 offset:16\n"
    "ds_read_b64_tr_b16 v[224:225], v116 offset:24\n"
    "ds_read_b64_tr_b16 v[240:241], v116 offset:32\n"
    "ds_read_b64_tr_b16 v[96:97], v116 offset:40\n"
    "ds_read_b64_tr_b16 v[24:25], v116 offset:48\n"
    "ds_read_b64_tr_b16 v[40:41], v116 offset:56\n"
    "s_waitcnt lgkmcnt(0)\n"
    "s_waitcnt vmcnt(12)\n"
    "s_barrier\n"
    "s_mul_i32 s38, s61, 4480\n"
    "s_add_u32 m0, s38, 53760\n"
    "s_mov_b32 s39, 524288\n"
    "buffer_load_dwordx4 v112, s[80:83], s39 offen lds\n"
    "s_add_u32 m0, s38, 54848\n"
    "s_mov_b32 s39, 557056\n"
    "buffer_load_dwordx4 v112, s[80:83], s39 offen lds\n"
    "s_add_u32 m0, s38, 55936\n"
    "s_mov_b32 s39, 589824\n"
    "buffer_load_dwordx4 v112, s[80:83], s39 offen lds\n"
    "s_add_u32 m0, s38, 57024\n"
    "s_mov_b32 s39, 622592\n"
    "buffer_load_dwordx4 v112, s[80:83], s39 offen lds\n"
    "v_add_u32 v116, 89600, v115\n"
    "ds_read_b64_tr_b16 v[178:179], v116 offset:0\n"
    "ds_read_b64_tr_b16 v[194:195], v116 offset:8\n"
    "ds_read_b64_tr_b16 v[210:211], v116 offset:16\n"
    "ds_read_b64_tr_b16 v[226:227], v116 offset:24\n"
    "ds_read_b64_tr_b16 v[242:243], v116 offset:32\n"
    "ds_read_b64_tr_b16 v[98:99], v116 offset:40\n"
    "ds_read_b64_tr_b16 v[26:27], v116 offset:48\n"
    "ds_read_b64_tr_b16 v[42:43], v116 offset:56\n"
    "s_waitcnt lgkmcnt(0)\n"
    "s_waitcnt vmcnt(12)\n"
    "s_barrier\n"
    "s_mul_i32 s38, s61, 4480\n"
    "s_add_u32 m0, s38, 71680\n"
    "s_mov_b32 s39, 1048576\n"
    "buffer_load_dwordx4 v112, s[80:83], s39 offen lds\n"
    "s_add_u32 m0, s38, 72768\n"
    "s_mov_b32 s39, 1081344\n"
    "buffer_load_dwordx4 v112, s[80:83], s39 offen lds\n"
    "s_add_u32 m0, s38, 73856\n"
    "s_mov_b32 s39, 1114112\n"
    "buffer_load_dwordx4 v112, s[80:83], s39 offen lds\n"
    "s_add_u32 m0, s38, 74944\n"
    "s_mov_b32 s39, 1146880\n"
    "buffer_load_dwordx4 v112, s[80:83], s39 offen lds\n"
    "v_add_u32 v116, 0, v115\n"
    "ds_read_b64_tr_b16 v[180:181], v116 offset:0\n"
    "ds_read_b64_tr_b16 v[196:197], v116 offset:8\n"
    "ds_read_b64_tr_b16 v[212:213], v116 offset:16\n"
    "ds_read_b64_tr_b16 v[228:229], v116 offset:24\n"
    "ds_read_b64_tr_b16 v[244:245], v116 offset:32\n"
    "ds_read_b64_tr_b16 v[100:101], v116 offset:40\n"
    "ds_read_b64_tr_b16 v[28:29], v116 offset:48\n"
    "ds_read_b64_tr_b16 v[44:45], v116 offset:56\n"
    "s_waitcnt lgkmcnt(0)\n"
    "s_waitcnt vmcnt(12)\n"
    "s_barrier\n"
    "s_mul_i32 s38, s61, 4480\n"
    "s_add_u32 m0, s38, 89600\n"
    "s_mov_b32 s39, 1572864\n"
    "buffer_load_dwordx4 v112, s[80:83], s39 offen lds\n"
    "s_add_u32 m0, s38, 90688\n"
    "s_mov_b32 s39, 1605632\n"
    "buffer_load_dwordx4 v112, s[80:83], s39 offen lds\n"
    "s_add_u32 m0, s38, 91776\n"
    "s_mov_b32 s39, 1638400\n"
    "buffer_load_dwordx4 v112, s[80:83], s39 offen lds\n"
    "s_add_u32 m0, s38, 92864\n"
    "s_mov_b32 s39, 1671168\n"
    "buffer_load_dwordx4 v112, s[80:83], s39 offen lds\n"
    "v_add_u32 v116, 17920, v115\n"
    "ds_read_b64_tr_b16 v[182:183], v116 offset:0\n"
    "ds_read_b64_tr_b16 v[198:199], v116 offset:8\n"
    "ds_read_b64_tr_b16 v[214:215], v116 offset:16\n"
    "ds_read_b64_tr_b16 v[230:231], v116 offset:24\n"
    "ds_read_b64_tr_b16 v[246:247], v116 offset:32\n"
    "ds_read_b64_tr_b16 v[102:103], v116 offset:40\n"
    "ds_read_b64_tr_b16 v[30:31], v116 offset:48\n"
    "ds_read_b64_tr_b16 v[46:47], v116 offset:56\n"
    "ds_write_b128 v9, v[24:27] offset:0\n"
    "s_waitcnt lgkmcnt(2)\n"
    "ds_write_b128 v9, v[28:31] offset:1024\n"
    "ds_write_b128 v9, v[32:35] offset:8192\n"
    "ds_write_b128 v9, v[36:39] offset:9216\n"
    "ds_write_b128 v9, v[40:43] offset:16384\n"
    "s_waitcnt lgkmcnt(5)\n"
    "ds_write_b128 v9, v[44:47] offset:17408\n"
    "s_waitcnt lgkmcnt(0)\n"
    "s_waitcnt vmcnt(12)\n"
    "s_barrier\n"
    "s_mul_i32 s38, s61, 4480\n"
    "s_add_u32 m0, s38, 0\n"
    "s_mov_b32 s39, 2097152\n"
    "buffer_load_dwordx4 v112, s[80:83], s39 offen lds\n"
    "s_add_u32 m0, s38, 1088\n"
    "s_mov_b32 s39, 2129920\n"
    "buffer_load_dwordx4 v112, s[80:83], s39 offen lds\n"
    "s_add_u32 m0, s38, 2176\n"
    "s_mov_b32 s39, 2162688\n"
    "buffer_load_dwordx4 v112, s[80:83], s39 offen lds\n"
    "s_add_u32 m0, s38, 3264\n"
    "s_mov_b32 s39, 2195456\n"
    "buffer_load_dwordx4 v112, s[80:83], s39 offen lds\n"
    "v_add_u32 v116, 35840, v114\n"
    "ds_read_b64_tr_b16 v[64:65], v116 offset:0\n"
    "ds_read_b64_tr_b16 v[66:67], v116 offset:2176\n"
    "ds_read_b64_tr_b16 v[68:69], v116 offset:8960\n"
    "ds_read_b64_tr_b16 v[70:71], v116 offset:11136\n"
    "v_xor_b32 v116, 32, v116\n"
    "ds_read_b64_tr_b16 v[72:73], v116 offset:0\n"
    "ds_read_b64_tr_b16 v[74:75], v116 offset:2176\n"
    "ds_read_b64_tr_b16 v[76:77], v116 offset:8960\n"
    "ds_read_b64_tr_b16 v[78:79], v116 offset:11136\n"
    "s_waitcnt lgkmcnt(0)\n"
    "s_waitcnt vmcnt(12)\n"
    "s_barrier\n"
    "s_mul_i32 s38, s61, 4480\n"
    "s_add_u32 m0, s38, 17920\n"
    "s_mov_b32 s39, 2621440\n"
    "buffer_load_dwordx4 v112, s[80:83], s39 offen lds\n"
    "s_add_u32 m0, s38, 19008\n"
    "s_mov_b32 s39, 2654208\n"
    "buffer_load_dwordx4 v112, s[80:83], s39 offen lds\n"
    "s_add_u32 m0, s38, 20096\n"
    "s_mov_b32 s39, 2686976\n"
    "buffer_load_dwordx4 v112, s[80:83], s39 offen lds\n"
    "s_add_u32 m0, s38, 21184\n"
    "s_mov_b32 s39, 2719744\n"
    "buffer_load_dwordx4 v112, s[80:83], s39 offen lds\n"
    "v_add_u32 v116, 53760, v114\n"
    "ds_read_b64_tr_b16 v[80:81], v116 offset:0\n"
    "ds_read_b64_tr_b16 v[82:83], v116 offset:2176\n"
    "ds_read_b64_tr_b16 v[84:85], v116 offset:8960\n"
    "ds_read_b64_tr_b16 v[86:87], v116 offset:11136\n"
    "v_xor_b32 v116, 32, v116\n"
    "ds_read_b64_tr_b16 v[88:89], v116 offset:0\n"
    "ds_read_b64_tr_b16 v[90:91], v116 offset:2176\n"
    "ds_read_b64_tr_b16 v[92:93], v116 offset:8960\n"
    "ds_read_b64_tr_b16 v[94:95], v116 offset:11136\n"
    "s_waitcnt lgkmcnt(0)\n"
    "s_waitcnt vmcnt(12)\n"
    "s_barrier\n"
    "s_mul_i32 s38, s61, 4480\n"
    "s_add_u32 m0, s38, 35840\n"
    "s_mov_b32 s39, 3145728\n"
    "buffer_load_dwordx4 v112, s[80:83], s39 offen lds\n"
    "s_add_u32 m0, s38, 36928\n"
    "s_mov_b32 s39, 3178496\n"
    "buffer_load_dwordx4 v112, s[80:83], s39 offen lds\n"
    "s_add_u32 m0, s38, 38016\n"
    "s_mov_b32 s39, 3211264\n"
    "buffer_load_dwordx4 v112, s[80:83], s39 offen lds\n"
    "s_add_u32 m0, s38, 39104\n"
    "s_mov_b32 s39, 3244032\n"
    "buffer_load_dwordx4 v112, s[80:83], s39 offen lds\n"
    "v_add_u32 v116, 71680, v114\n"
    "ds_read_b64_tr_b16 a[80:81], v116 offset:0\n"
    "ds_read_b64_tr_b16 a[82:83], v116 offset:2176\n"
    "ds_read_b64_tr_b16 a[84:85], v116 offset:8960\n"
    "ds_read_b64_tr_b16 a[86:87], v116 offset:11136\n"
    "v_xor_b32 v116, 32, v116\n"
    "ds_read_b64_tr_b16 a[88:89], v116 offset:0\n"
    "ds_read_b64_tr_b16 a[90:91], v116 offset:2176\n"
    "ds_read_b64_tr_b16 a[92:93], v116 offset:8960\n"
    "ds_read_b64_tr_b16 a[94:95], v116 offset:11136\n"
    "s_waitcnt lgkmcnt(0)\n"
    "s_waitcnt vmcnt(12)\n"
    "s_barrier\n"
    "s_mul_i32 s38, s61, 4480\n"
    "s_add_u32 m0, s38, 53760\n"
    "s_mov_b32 s39, 3670016\n"
    "buffer_load_dwordx4 v112, s[80:83], s39 offen lds\n"
    "s_add_u32 m0, s38, 54848\n"
    "s_mov_b32 s39, 3702784\n"
    "buffer_load_dwordx4 v112, s[80:83], s39 offen lds\n"
    "s_add_u32 m0, s38, 55936\n"
    "s_mov_b32 s39, 3735552\n"
    "buffer_load_dwordx4 v112, s[80:83], s39 offen lds\n"
    "s_add_u32 m0, s38, 57024\n"
    "s_mov_b32 s39, 3768320\n"
    "buffer_load_dwordx4 v112, s[80:83], s39 offen lds\n"
    "v_add_u32 v116, 89600, v114\n"
    "ds_read_b64_tr_b16 a[96:97], v116 offset:0\n"
    "ds_read_b64_tr_b16 a[98:99], v116 offset:2176\n"
    "ds_read_b64_tr_b16 a[100:101], v116 offset:8960\n"
    "ds_read_b64_tr_b16 a[102:103], v116 offset:11136\n"
    "v_xor_b32 v116, 32, v116\n"
    "ds_read_b64_tr_b16 a[104:105], v116 offset:0\n"
    "ds_read_b64_tr_b16 a[106:107], v116 offset:2176\n"
    "ds_read_b64_tr_b16 a[108:109], v116 offset:8960\n"
    "ds_read_b64_tr_b16 a[110:111], v116 offset:11136\n"
    "s_waitcnt lgkmcnt(0)\n"
    "s_waitcnt vmcnt(12)\n"
    "s_barrier\n"
    "s_mov_b32 s80, s84\n"
    "s_mov_b32 s81, s85\n"
    "s_mul_i32 s38, s61, 4480\n"
    "s_add_u32 m0, s38, 71680\n"
    "s_mov_b32 s39, 0\n"
    "buffer_load_dwordx4 v112, s[80:83], s39 offen lds\n"
    "s_add_u32 m0, s38, 72768\n"
    "s_mov_b32 s39, 32768\n"
    "buffer_load_dwordx4 v112, s[80:83], s39 offen lds\n"
    "s_add_u32 m0, s38, 73856\n"
    "s_mov_b32 s39, 65536\n"
    "buffer_load_dwordx4 v112, s[80:83], s39 offen lds\n"
    "s_add_u32 m0, s38, 74944\n"
    "s_mov_b32 s39, 98304\n"
    "buffer_load_dwordx4 v112, s[80:83], s39 offen lds\n"
    "v_add_u32 v116, 0, v114\n"
    "ds_read_b64_tr_b16 a[112:113], v116 offset:0\n"
    "ds_read_b64_tr_b16 a[114:115], v116 offset:2176\n"
    "ds_read_b64_tr_b16 a[116:117], v116 offset:8960\n"
    "ds_read_b64_tr_b16 a[118:119], v116 offset:11136\n"
    "v_xor_b32 v116, 32, v116\n"
    "ds_read_b64_tr_b16 a[120:121], v116 offset:0\n"
    "ds_read_b64_tr_b16 a[122:123], v116 offset:2176\n"
    "ds_read_b64_tr_b16 a[124:125], v116 offset:8960\n"
    "ds_read_b64_tr_b16 a[126:127], v116 offset:11136\n"
    "s_waitcnt lgkmcnt(0)\n"
    "s_waitcnt vmcnt(12)\n"
    "s_barrier\n"
    "s_mul_i32 s38, s61, 4480\n"
    "s_add_u32 m0, s38, 89600\n"
    "s_mov_b32 s39, 524288\n"
    "buffer_load_dwordx4 v112, s[80:83], s39 offen lds\n"
    "s_add_u32 m0, s38, 90688\n"
    "s_mov_b32 s39, 557056\n"
    "buffer_load_dwordx4 v112, s[80:83], s39 offen lds\n"
    "s_add_u32 m0, s38, 91776\n"
    "s_mov_b32 s39, 589824\n"
    "buffer_load_dwordx4 v112, s[80:83], s39 offen lds\n"
    "s_add_u32 m0, s38, 92864\n"
    "s_mov_b32 s39, 622592\n"
    "buffer_load_dwordx4 v112, s[80:83], s39 offen lds\n"
    "v_add_u32 v116, 17920, v114\n"
    "ds_read_b64_tr_b16 a[128:129], v116 offset:0\n"
    "ds_read_b64_tr_b16 a[130:131], v116 offset:2176\n"
    "ds_read_b64_tr_b16 a[132:133], v116 offset:8960\n"
    "ds_read_b64_tr_b16 a[134:135], v116 offset:11136\n"
    "v_xor_b32 v116, 32, v116\n"
    "ds_read_b64_tr_b16 a[136:137], v116 offset:0\n"
    "ds_read_b64_tr_b16 a[138:139], v116 offset:2176\n"
    "ds_read_b64_tr_b16 a[140:141], v116 offset:8960\n"
    "ds_read_b64_tr_b16 a[142:143], v116 offset:11136\n"
    "s_waitcnt lgkmcnt(0)\n"
    "s_waitcnt vmcnt(12)\n"
    "s_barrier\n"
    "s_mul_i32 s38, s61, 4480\n"
    "s_add_u32 m0, s38, 0\n"
    "s_mov_b32 s39, 1048576\n"
    "buffer_load_dwordx4 v112, s[80:83], s39 offen lds\n"
    "s_add_u32 m0, s38, 1088\n"
    "s_mov_b32 s39, 1081344\n"
    "buffer_load_dwordx4 v112, s[80:83], s39 offen lds\n"
    "s_add_u32 m0, s38, 2176\n"
    "s_mov_b32 s39, 1114112\n"
    "buffer_load_dwordx4 v112, s[80:83], s39 offen lds\n"
    "s_add_u32 m0, s38, 3264\n"
    "s_mov_b32 s39, 1146880\n"
    "buffer_load_dwordx4 v112, s[80:83], s39 offen lds\n"
    "v_add_u32 v116, 35840, v114\n"
    "ds_read_b64_tr_b16 a[144:145], v116 offset:0\n"
    "ds_read_b64_tr_b16 a[146:147], v116 offset:2176\n"
    "ds_read_b64_tr_b16 a[148:149], v116 offset:8960\n"
    "ds_read_b64_tr_b16 a[150:151], v116 offset:11136\n"
    "v_xor_b32 v116, 32, v116\n"
    "ds_read_b64_tr_b16 a[152:153], v116 offset:0\n"
    "ds_read_b64_tr_b16 a[154:155], v116 offset:2176\n"
    "ds_read_b64_tr_b16 a[156:157], v116 offset:8960\n"
    "ds_read_b64_tr_b16 a[158:159], v116 offset:11136\n"
    "s_waitcnt lgkmcnt(0)\n"
    "s_waitcnt vmcnt(12)\n"
    "s_barrier\n"
    "s_mul_i32 s38, s61, 4480\n"
    "s_add_u32 m0, s38, 17920\n"
    "s_mov_b32 s39, 1572864\n"
    "buffer_load_dwordx4 v112, s[80:83], s39 offen lds\n"
    "s_add_u32 m0, s38, 19008\n"
    "s_mov_b32 s39, 1605632\n"
    "buffer_load_dwordx4 v112, s[80:83], s39 offen lds\n"
    "s_add_u32 m0, s38, 20096\n"
    "s_mov_b32 s39, 1638400\n"
    "buffer_load_dwordx4 v112, s[80:83], s39 offen lds\n"
    "s_add_u32 m0, s38, 21184\n"
    "s_mov_b32 s39, 1671168\n"
    "buffer_load_dwordx4 v112, s[80:83], s39 offen lds\n"
    "v_add_u32 v116, 53760, v114\n"
    "ds_read_b64_tr_b16 a[160:161], v116 offset:0\n"
    "ds_read_b64_tr_b16 a[162:163], v116 offset:2176\n"
    "ds_read_b64_tr_b16 a[164:165], v116 offset:8960\n"
    "ds_read_b64_tr_b16 a[166:167], v116 offset:11136\n"
    "v_xor_b32 v116, 32, v116\n"
    "ds_read_b64_tr_b16 a[168:169], v116 offset:0\n"
    "ds_read_b64_tr_b16 a[170:171], v116 offset:2176\n"
    "ds_read_b64_tr_b16 a[172:173], v116 offset:8960\n"
    "ds_read_b64_tr_b16 a[174:175], v116 offset:11136\n"
    "s_waitcnt lgkmcnt(0)\n"
    "s_waitcnt vmcnt(12)\n"
    "s_barrier\n"
    "s_mul_i32 s38, s61, 4480\n"
    "s_add_u32 m0, s38, 35840\n"
    "s_mov_b32 s39, 2097152\n"
    "buffer_load_dwordx4 v112, s[80:83], s39 offen lds\n"
    "s_add_u32 m0, s38, 36928\n"
    "s_mov_b32 s39, 2129920\n"
    "buffer_load_dwordx4 v112, s[80:83], s39 offen lds\n"
    "s_add_u32 m0, s38, 38016\n"
    "s_mov_b32 s39, 2162688\n"
    "buffer_load_dwordx4 v112, s[80:83], s39 offen lds\n"
    "s_add_u32 m0, s38, 39104\n"
    "s_mov_b32 s39, 2195456\n"
    "buffer_load_dwordx4 v112, s[80:83], s39 offen lds\n"
    "v_add_u32 v116, 71680, v114\n"
    "ds_read_b64_tr_b16 v[16:17], v116 offset:0\n"
    "ds_read_b64_tr_b16 v[18:19], v116 offset:2176\n"
    "ds_read_b64_tr_b16 v[20:21], v116 offset:8960\n"
    "ds_read_b64_tr_b16 v[22:23], v116 offset:11136\n"
    "v_xor_b32 v116, 32, v116\n"
    "ds_read_b64_tr_b16 v[24:25], v116 offset:0\n"
    "ds_read_b64_tr_b16 v[26:27], v116 offset:2176\n"
    "ds_read_b64_tr_b16 v[28:29], v116 offset:8960\n"
    "ds_read_b64_tr_b16 v[30:31], v116 offset:11136\n"
    "s_waitcnt lgkmcnt(0)\n"
    "s_waitcnt vmcnt(12)\n"
    "s_barrier\n"
    "s_mul_i32 s38, s61, 4480\n"
    "s_add_u32 m0, s38, 53760\n"
    "s_mov_b32 s39, 2621440\n"
    "buffer_load_dwordx4 v112, s[80:83], s39 offen lds\n"
    "s_add_u32 m0, s38, 54848\n"
    "s_mov_b32 s39, 2654208\n"
    "buffer_load_dwordx4 v112, s[80:83], s39 offen lds\n"
    "s_add_u32 m0, s38, 55936\n"
    "s_mov_b32 s39, 2686976\n"
    "buffer_load_dwordx4 v112, s[80:83], s39 offen lds\n"
    "s_add_u32 m0, s38, 57024\n"
    "s_mov_b32 s39, 2719744\n"
    "buffer_load_dwordx4 v112, s[80:83], s39 offen lds\n"
    "v_add_u32 v116, 89600, v114\n"
    "ds_read_b64_tr_b16 v[32:33], v116 offset:0\n"
    "ds_read_b64_tr_b16 v[34:35], v116 offset:2176\n"
    "ds_read_b64_tr_b16 v[36:37], v116 offset:8960\n"
    "ds_read_b64_tr_b16 v[38:39], v116 offset:11136\n"
    "v_xor_b32 v116, 32, v116\n"
    "ds_read_b64_tr_b16 v[40:41], v116 offset:0\n"
    "ds_read_b64_tr_b16 v[42:43], v116 offset:2176\n"
    "ds_read_b64_tr_b16 v[44:45], v116 offset:8960\n"
    "ds_read_b64_tr_b16 v[46:47], v116 offset:11136\n"
    "s_waitcnt lgkmcnt(0)\n"
    "s_waitcnt vmcnt(12)\n"
    "s_barrier\n"
    "s_mul_i32 s38, s61, 4480\n"
    "s_add_u32 m0, s38, 71680\n"
    "s_mov_b32 s39, 3145728\n"
    "buffer_load_dwordx4 v112, s[80:83], s39 offen lds\n"
    "s_add_u32 m0, s38, 72768\n"
    "s_mov_b32 s39, 3178496\n"
    "buffer_load_dwordx4 v112, s[80:83], s39 offen lds\n"
    "s_add_u32 m0, s38, 73856\n"
    "s_mov_b32 s39, 3211264\n"
    "buffer_load_dwordx4 v112, s[80:83], s39 offen lds\n"
    "s_add_u32 m0, s38, 74944\n"
    "s_mov_b32 s39, 3244032\n"
    "buffer_load_dwordx4 v112, s[80:83], s39 offen lds\n"
    "v_add_u32 v116, 0, v114\n"
    "ds_read_b64_tr_b16 v[48:49], v116 offset:0\n"
    "ds_read_b64_tr_b16 v[50:51], v116 offset:2176\n"
    "ds_read_b64_tr_b16 v[52:53], v116 offset:8960\n"
    "ds_read_b64_tr_b16 v[54:55], v116 offset:11136\n"
    "v_xor_b32 v116, 32, v116\n"
    "ds_read_b64_tr_b16 v[56:57], v116 offset:0\n"
    "ds_read_b64_tr_b16 v[58:59], v116 offset:2176\n"
    "ds_read_b64_tr_b16 v[60:61], v116 offset:8960\n"
    "ds_read_b64_tr_b16 v[62:63], v116 offset:11136\n"
    "s_waitcnt lgkmcnt(0)\n"
    "s_waitcnt vmcnt(12)\n"
    "s_barrier\n"
    "s_mul_i32 s38, s61, 4480\n"
    "s_add_u32 m0, s38, 89600\n"
    "s_mov_b32 s39, 3670016\n"
    "buffer_load_dwordx4 v112, s[80:83], s39 offen lds\n"
    "s_add_u32 m0, s38, 90688\n"
    "s_mov_b32 s39, 3702784\n"
    "buffer_load_dwordx4 v112, s[80:83], s39 offen lds\n"
    "s_add_u32 m0, s38, 91776\n"
    "s_mov_b32 s39, 3735552\n"
    "buffer_load_dwordx4 v112, s[80:83], s39 offen lds\n"
    "s_add_u32 m0, s38, 92864\n"
    "s_mov_b32 s39, 3768320\n"
    "buffer_load_dwordx4 v112, s[80:83], s39 offen lds\n"
    "v_add_u32 v116, 17920, v114\n"
    "ds_read_b64_tr_b16 a[0:1], v116 offset:0\n"
    "ds_read_b64_tr_b16 a[2:3], v116 offset:2176\n"
    "ds_read_b64_tr_b16 a[4:5], v116 offset:8960\n"
    "ds_read_b64_tr_b16 a[6:7], v116 offset:11136\n"
    "v_xor_b32 v116, 32, v116\n"
    "ds_read_b64_tr_b16 a[8:9], v116 offset:0\n"
    "ds_read_b64_tr_b16 a[10:11], v116 offset:2176\n"
    "ds_read_b64_tr_b16 a[12:13], v116 offset:8960\n"
    "ds_read_b64_tr_b16 a[14:15], v116 offset:11136\n"
    "s_waitcnt lgkmcnt(0)\n"
    "s_waitcnt vmcnt(12)\n"
    "s_barrier\n"
    "s_mov_b32 s80, s88\n"
    "s_mov_b32 s81, s89\n"
    "s_mul_i32 s38, s61, 4480\n"
    "s_add_u32 m0, s38, 0\n"
    "s_mov_b32 s39, 0\n"
    "buffer_load_dwordx4 v112, s[80:83], s39 offen lds\n"
    "s_add_u32 m0, s38, 1088\n"
    "s_mov_b32 s39, 32768\n"
    "buffer_load_dwordx4 v112, s[80:83], s39 offen lds\n"
    "s_add_u32 m0, s38, 2176\n"
    "s_mov_b32 s39, 65536\n"
    "buffer_load_dwordx4 v112, s[80:83], s39 offen lds\n"
    "s_add_u32 m0, s38, 3264\n"
    "s_mov_b32 s39, 98304\n"
    "buffer_load_dwordx4 v112, s[80:83], s39 offen lds\n"
    "v_add_u32 v116, 35840, v114\n"
    "ds_read_b64_tr_b16 a[16:17], v116 offset:0\n"
    "ds_read_b64_tr_b16 a[18:19], v116 offset:2176\n"
    "ds_read_b64_tr_b16 a[20:21], v116 offset:8960\n"
    "ds_read_b64_tr_b16 a[22:23], v116 offset:11136\n"
    "v_xor_b32 v116, 32, v116\n"
    "ds_read_b64_tr_b16 a[24:25], v116 offset:0\n"
    "ds_read_b64_tr_b16 a[26:27], v116 offset:2176\n"
    "ds_read_b64_tr_b16 a[28:29], v116 offset:8960\n"
    "ds_read_b64_tr_b16 a[30:31], v116 offset:11136\n"
    "s_waitcnt lgkmcnt(0)\n"
    "s_waitcnt vmcnt(12)\n"
    "s_barrier\n"
    "s_mul_i32 s38, s61, 4480\n"
    "s_add_u32 m0, s38, 17920\n"
    "s_mov_b32 s39, 524288\n"
    "buffer_load_dwordx4 v112, s[80:83], s39 offen lds\n"
    "s_add_u32 m0, s38, 19008\n"
    "s_mov_b32 s39, 557056\n"
    "buffer_load_dwordx4 v112, s[80:83], s39 offen lds\n"
    "s_add_u32 m0, s38, 20096\n"
    "s_mov_b32 s39, 589824\n"
    "buffer_load_dwordx4 v112, s[80:83], s39 offen lds\n"
    "s_add_u32 m0, s38, 21184\n"
    "s_mov_b32 s39, 622592\n"
    "buffer_load_dwordx4 v112, s[80:83], s39 offen lds\n"
    "v_add_u32 v116, 53760, v114\n"
    "ds_read_b64_tr_b16 a[32:33], v116 offset:0\n"
    "ds_read_b64_tr_b16 a[34:35], v116 offset:2176\n"
    "ds_read_b64_tr_b16 a[36:37], v116 offset:8960\n"
    "ds_read_b64_tr_b16 a[38:39], v116 offset:11136\n"
    "v_xor_b32 v116, 32, v116\n"
    "ds_read_b64_tr_b16 a[40:41], v116 offset:0\n"
    "ds_read_b64_tr_b16 a[42:43], v116 offset:2176\n"
    "ds_read_b64_tr_b16 a[44:45], v116 offset:8960\n"
    "ds_read_b64_tr_b16 a[46:47], v116 offset:11136\n"
    "s_waitcnt lgkmcnt(0)\n"
    "s_waitcnt vmcnt(12)\n"
    "s_barrier\n"
    "global_load_dword v117, v118, s[92:93] sc1\n"
    "s_mul_i32 s38, s61, 4480\n"
    "s_add_u32 m0, s38, 35840\n"
    "s_mov_b32 s39, 1048576\n"
    "buffer_load_dwordx4 v112, s[80:83], s39 offen lds\n"
    "s_add_u32 m0, s38, 36928\n"
    "s_mov_b32 s39, 1081344\n"
    "buffer_load_dwordx4 v112, s[80:83], s39 offen lds\n"
    "s_add_u32 m0, s38, 38016\n"
    "s_mov_b32 s39, 1114112\n"
    "buffer_load_dwordx4 v112, s[80:83], s39 offen lds\n"
    "s_add_u32 m0, s38, 39104\n"
    "s_mov_b32 s39, 1146880\n"
    "buffer_load_dwordx4 v112, s[80:83], s39 offen lds\n"
    "v_add_u32 v116, 71680, v114\n"
    "ds_read_b64_tr_b16 a[48:49], v116 offset:0\n"
    "ds_read_b64_tr_b16 a[50:51], v116 offset:2176\n"
    "ds_read_b64_tr_b16 a[52:53], v116 offset:8960\n"
    "ds_read_b64_tr_b16 a[54:55], v116 offset:11136\n"
    "v_xor_b32 v116, 32, v116\n"
    "ds_read_b64_tr_b16 a[56:57], v116 offset:0\n"
    "ds_read_b64_tr_b16 a[58:59], v116 offset:2176\n"
    "ds_read_b64_tr_b16 a[60:61], v116 offset:8960\n"
    "ds_read_b64_tr_b16 a[62:63], v116 offset:11136\n"
    "s_waitcnt lgkmcnt(0)\n"
    "s_waitcnt vmcnt(13)\n"
    "s_barrier\n"
    "s_mul_i32 s38, s61, 4480\n"
    "s_add_u32 m0, s38, 53760\n"
    "s_mov_b32 s39, 1572864\n"
    "buffer_load_dwordx4 v112, s[80:83], s39 offen lds\n"
    "s_add_u32 m0, s38, 54848\n"
    "s_mov_b32 s39, 1605632\n"
    "buffer_load_dwordx4 v112, s[80:83], s39 offen lds\n"
    "s_add_u32 m0, s38, 55936\n"
    "s_mov_b32 s39, 1638400\n"
    "buffer_load_dwordx4 v112, s[80:83], s39 offen lds\n"
    "s_add_u32 m0, s38, 57024\n"
    "s_mov_b32 s39, 1671168\n"
    "buffer_load_dwordx4 v112, s[80:83], s39 offen lds\n"
    "v_add_u32 v116, 89600, v114\n"
    "ds_read_b64_tr_b16 a[64:65], v116 offset:0\n"
    "ds_read_b64_tr_b16 a[66:67], v116 offset:2176\n"
    "ds_read_b64_tr_b16 a[68:69], v116 offset:8960\n"
    "ds_read_b64_tr_b16 a[70:71], v116 offset:11136\n"
    "v_xor_b32 v116, 32, v116\n"
    "ds_read_b64_tr_b16 a[72:73], v116 offset:0\n"
    "ds_read_b64_tr_b16 a[74:75], v116 offset:2176\n"
    "ds_read_b64_tr_b16 a[76:77], v116 offset:8960\n"
    "ds_read_b64_tr_b16 a[78:79], v116 offset:11136\n"
    "s_waitcnt lgkmcnt(0)\n"
    "s_waitcnt vmcnt(13)\n"
    "s_barrier\n"
    "s_mul_i32 s38, s61, 4480\n"
    "s_add_u32 m0, s38, 71680\n"
    "s_mov_b32 s39, 2097152\n"
    "buffer_load_dwordx4 v112, s[80:83], s39 offen lds\n"
    "s_add_u32 m0, s38, 72768\n"
    "s_mov_b32 s39, 2129920\n"
    "buffer_load_dwordx4 v112, s[80:83], s39 offen lds\n"
    "s_add_u32 m0, s38, 73856\n"
    "s_mov_b32 s39, 2162688\n"
    "buffer_load_dwordx4 v112, s[80:83], s39 offen lds\n"
    "s_add_u32 m0, s38, 74944\n"
    "s_mov_b32 s39, 2195456\n"
    "buffer_load_dwordx4 v112, s[80:83], s39 offen lds\n"
    "v_add_u32 v116, 0, v114\n"
    "ds_read_b64_tr_b16 a[176:177], v116 offset:0\n"
    "ds_read_b64_tr_b16 a[178:179], v116 offset:2176\n"
    "ds_read_b64_tr_b16 a[180:181], v116 offset:8960\n"
    "ds_read_b64_tr_b16 a[182:183], v116 offset:11136\n"
    "v_xor_b32 v116, 32, v116\n"
    "ds_read_b64_tr_b16 a[184:185], v116 offset:0\n"
    "ds_read_b64_tr_b16 a[186:187], v116 offset:2176\n"
    "ds_read_b64_tr_b16 a[188:189], v116 offset:8960\n"
    "ds_read_b64_tr_b16 a[190:191], v116 offset:11136\n"
    "s_waitcnt lgkmcnt(0)\n"
    "s_waitcnt vmcnt(13)\n"
    "s_barrier\n"
    "s_mul_i32 s38, s61, 4480\n"
    "s_add_u32 m0, s38, 89600\n"
    "s_mov_b32 s39, 2621440\n"
    "buffer_load_dwordx4 v112, s[80:83], s39 offen lds\n"
    "s_add_u32 m0, s38, 90688\n"
    "s_mov_b32 s39, 2654208\n"
    "buffer_load_dwordx4 v112, s[80:83], s39 offen lds\n"
    "s_add_u32 m0, s38, 91776\n"
    "s_mov_b32 s39, 2686976\n"
    "buffer_load_dwordx4 v112, s[80:83], s39 offen lds\n"
    "s_add_u32 m0, s38, 92864\n"
    "s_mov_b32 s39, 2719744\n"
    "buffer_load_dwordx4 v112, s[80:83], s39 offen lds\n"
    "v_add_u32 v116, 17920, v114\n"
    "ds_read_b64_tr_b16 a[192:193], v116 offset:0\n"
    "ds_read_b64_tr_b16 a[194:195], v116 offset:2176\n"
    "ds_read_b64_tr_b16 a[196:197], v116 offset:8960\n"
    "ds_read_b64_tr_b16 a[198:199], v116 offset:11136\n"
    "v_xor_b32 v116, 32, v116\n"
    "ds_read_b64_tr_b16 a[200:201], v116 offset:0\n"
    "ds_read_b64_tr_b16 a[202:203], v116 offset:2176\n"
    "ds_read_b64_tr_b16 a[204:205], v116 offset:8960\n"
    "ds_read_b64_tr_b16 a[206:207], v116 offset:11136\n"
    "s_waitcnt lgkmcnt(0)\n"
    "s_waitcnt vmcnt(12)\n"
    "s_barrier\n"
    "s_mul_i32 s38, s61, 4480\n"
    "s_add_u32 m0, s38, 0\n"
    "s_mov_b32 s39, 3145728\n"
    "buffer_load_dwordx4 v112, s[80:83], s39 offen lds\n"
    "s_add_u32 m0, s38, 1088\n"
    "s_mov_b32 s39, 3178496\n"
    "buffer_load_dwordx4 v112, s[80:83], s39 offen lds\n"
    "s_add_u32 m0, s38, 2176\n"
    "s_mov_b32 s39, 3211264\n"
    "buffer_load_dwordx4 v112, s[80:83], s39 offen lds\n"
    "s_add_u32 m0, s38, 3264\n"
    "s_mov_b32 s39, 3244032\n"
    "buffer_load_dwordx4 v112, s[80:83], s39 offen lds\n"
    "v_add_u32 v116, 35840, v114\n"
    "ds_read_b64_tr_b16 a[208:209], v116 offset:0\n"
    "ds_read_b64_tr_b16 a[210:211], v116 offset:2176\n"
    "ds_read_b64_tr_b16 a[212:213], v116 offset:8960\n"
    "ds_read_b64_tr_b16 a[214:215], v116 offset:11136\n"
    "v_xor_b32 v116, 32, v116\n"
    "ds_read_b64_tr_b16 a[216:217], v116 offset:0\n"
    "ds_read_b64_tr_b16 a[218:219], v116 offset:2176\n"
    "ds_read_b64_tr_b16 a[220:221], v116 offset:8960\n"
    "ds_read_b64_tr_b16 a[222:223], v116 offset:11136\n"
    "s_waitcnt lgkmcnt(0)\n"
    "s_waitcnt vmcnt(12)\n"
    "s_barrier\n"
    "s_mul_i32 s38, s61, 4480\n"
    "s_add_u32 m0, s38, 17920\n"
    "s_mov_b32 s39, 3670016\n"
    "buffer_load_dwordx4 v112, s[80:83], s39 offen lds\n"
    "s_add_u32 m0, s38, 19008\n"
    "s_mov_b32 s39, 3702784\n"
    "buffer_load_dwordx4 v112, s[80:83], s39 offen lds\n"
    "s_add_u32 m0, s38, 20096\n"
    "s_mov_b32 s39, 3735552\n"
    "buffer_load_dwordx4 v112, s[80:83], s39 offen lds\n"
    "s_add_u32 m0, s38, 21184\n"
    "s_mov_b32 s39, 3768320\n"
    "buffer_load_dwordx4 v112, s[80:83], s39 offen lds\n"
    "v_add_u32 v116, 53760, v114\n"
    "ds_read_b64_tr_b16 a[224:225], v116 offset:0\n"
    "ds_read_b64_tr_b16 a[226:227], v116 offset:2176\n"
    "ds_read_b64_tr_b16 a[228:229], v116 offset:8960\n"
    "ds_read_b64_tr_b16 a[230:231], v116 offset:11136\n"
    "v_xor_b32 v116, 32, v116\n"
    "ds_read_b64_tr_b16 a[232:233], v116 offset:0\n"
    "ds_read_b64_tr_b16 a[234:235], v116 offset:2176\n"
    "ds_read_b64_tr_b16 a[236:237], v116 offset:8960\n"
    "ds_read_b64_tr_b16 a[238:239], v116 offset:11136\n"
    "s_waitcnt lgkmcnt(0)\n"
    "s_waitcnt vmcnt(12)\n"
    "s_barrier\n"
    "v_add_u32 v116, 71680, v114\n"
    "ds_read_b64_tr_b16 a[240:241], v116 offset:0\n"
    "ds_read_b64_tr_b16 a[242:243], v116 offset:2176\n"
    "ds_read_b64_tr_b16 a[244:245], v116 offset:8960\n"
    "ds_read_b64_tr_b16 a[246:247], v116 offset:11136\n"
    "v_xor_b32 v116, 32, v116\n"
    "ds_read_b64_tr_b16 a[248:249], v116 offset:0\n"
    "ds_read_b64_tr_b16 a[250:251], v116 offset:2176\n"
    "ds_read_b64_tr_b16 a[252:253], v116 offset:8960\n"
    "ds_read_b64_tr_b16 a[254:255], v116 offset:11136\n"
    "s_waitcnt lgkmcnt(0)\n"
    "s_waitcnt vmcnt(8)\n"
    "s_barrier\n"
    "v_add_u32 v116, 89600, v114\n"
    "ds_read_b64_tr_b16 v[120:121], v116 offset:0\n"
    "ds_read_b64_tr_b16 v[122:123], v116 offset:2176\n"
    "ds_read_b64_tr_b16 v[124:125], v116 offset:8960\n"
    "ds_read_b64_tr_b16 v[126:127], v116 offset:11136\n"
    "v_xor_b32 v116, 32, v116\n"
    "ds_read_b64_tr_b16 v[128:129], v116 offset:0\n"
    "ds_read_b64_tr_b16 v[130:131], v116 offset:2176\n"
    "ds_read_b64_tr_b16 v[132:133], v116 offset:8960\n"
    "ds_read_b64_tr_b16 v[134:135], v116 offset:11136\n"
    "s_waitcnt lgkmcnt(0)\n"
    "s_mov_b32 s37, 0x80000\n"
    "LRY%=:\n"
    "v_readfirstlane_b32 s44, v117\n"
    "s_cmp_lg_u32 s44, 0\n"
    "s_cbranch_scc1 LRZ%=\n"
    "global_load_dword v117, v118, s[92:93] sc1\n"
    "s_waitcnt vmcnt(0)\n"
    "s_sleep 1\n"
    "s_sub_u32 s37, s37, 1\n"
    "s_cbranch_scc0 LRY%=\n"
    "s_branch LFAIL%=\n"
    "LRZ%=:\n"
    "s_lshl_b32 s38, s44, 13\n"
    "s_and_b32 s38, s38, 0x2000\n"
    "s_xor_b32 s46, s46, s38\n"
    "s_xor_b32 s47, s47, s38\n"
    "s_xor_b32 s48, s48, s38\n"
    "s_xor_b32 s56, s56, s38\n"
    "s_xor_b32 s74, s74, s38\n"
    "s_xor_b32 s58, s58, s38\n"
    "s_xor_b32 s59, s59, s38\n"
    "s_xor_b32 s67, s67, s38\n"
    "s_xor_b32 s69, s69, s38\n"
    "s_sub_u32 s58, s58, 4\n"
    "s_mov_b64 exec, 7\n"
    "buffer_load_dword v119, v0, s[16:19], s59 offen sc1\n"
    "s_mov_b64 exec, -1\n"
    "s_waitcnt vmcnt(5)\n"
    "s_barrier\n"
    "v_add_u32 v116, 0, v114\n"
    "ds_read_b64_tr_b16 v[136:137], v116 offset:0\n"
    "ds_read_b64_tr_b16 v[138:139], v116 offset:2176\n"
    "ds_read_b64_tr_b16 v[140:141], v116 offset:8960\n"
    "ds_read_b64_tr_b16 v[142:143], v116 offset:11136\n"
    "v_xor_b32 v116, 32, v116\n"
    "ds_read_b64_tr_b16 v[144:145], v116 offset:0\n"
    "ds_read_b64_tr_b16 v[146:147], v116 offset:2176\n"
    "ds_read_b64_tr_b16 v[148:149], v116 offset:8960\n"
    "ds_read_b64_tr_b16 v[150:151], v116 offset:11136\n"
    "s_waitcnt lgkmcnt(0)\n"
    "s_waitcnt vmcnt(1)\n"
    "s_barrier\n"
    "v_add_u32 v116, 17920, v114\n"
    "ds_read_b64_tr_b16 v[152:153], v116 offset:0\n"
    "ds_read_b64_tr_b16 v[154:155], v116 offset:2176\n"
    "ds_read_b64_tr_b16 v[156:157], v116 offset:8960\n"
    "ds_read_b64_tr_b16 v[158:159], v116 offset:11136\n"
    "v_xor_b32 v116, 32, v116\n"
    "ds_read_b64_tr_b16 v[160:161], v116 offset:0\n"
    "ds_read_b64_tr_b16 v[162:163], v116 offset:2176\n"
    "ds_read_b64_tr_b16 v[164:165], v116 offset:8960\n"
    "ds_read_b64_tr_b16 v[166:167], v116 offset:11136\n"
    "s_waitcnt lgkmcnt(0)\n"
    "s_barrier\n"
    "ds_write_b128 v7, v[16:19] offset:0\n"
    "ds_write_b128 v7, v[20:23] offset:1024\n"
    "ds_write_b128 v7, v[24:27] offset:2048\n"
    "ds_write_b128 v7, v[28:31] offset:3072\n"
    "ds_write_b128 v7, v[32:35] offset:4096\n"
    "ds_write_b128 v7, v[36:39] offset:5120\n"
    "ds_write_b128 v7, v[40:43] offset:6144\n"
    "ds_write_b128 v7, v[44:47] offset:7168\n"
    "ds_write_b128 v7, v[48:51] offset:8192\n"
    "ds_write_b128 v7, v[52:55] offset:9216\n"
    "ds_write_b128 v7, v[56:59] offset:10240\n"
    "ds_write_b128 v7, v[60:63] offset:11264\n"
    "ds_write_b128 v7, v[64:67] offset:12288\n"
    "ds_write_b128 v7, v[68:71] offset:13312\n"
    "ds_write_b128 v7, v[72:75] offset:14336\n"
    "s_waitcnt lgkmcnt(11)\n"
    "ds_write_b128 v7, v[76:79] offset:15360\n"
    "ds_write_b128 v7, v[80:83] offset:16384\n"
    "ds_write_b128 v7, v[84:87] offset:17408\n"
    "ds_write_b128 v7, v[88:91] offset:18432\n"
    "s_waitcnt lgkmcnt(11)\n"
    "ds_write_b128 v7, v[92:95] offset:19456\n"
    "ds_write_b128 v7, v[96:99] offset:20480\n"
    "ds_write_b128 v7, v[100:103] offset:21504\n"
    "ds_write_b128 v7, v[104:107] offset:22528\n"
    "s_waitcnt lgkmcnt(11)\n"
    "ds_write_b128 v7, v[108:111] offset:23552\n"
    "s_waitcnt lgkmcnt(0)\n"
    "ds_read_b128 v[16:19], v9 offset:0\n"
    "ds_read_b128 v[20:23], v9 offset:1024\n"
    "ds_read_b128 v[24:27], v9 offset:8192\n"
    "ds_read_b128 v[28:31], v9 offset:9216\n"
    "ds_read_b128 v[32:35], v9 offset:16384\n"
    "ds_read_b128 v[36:39], v9 offset:17408\n"
    "s_waitcnt lgkmcnt(5)\n"
    "ds_write_b128 v7, v[16:19] offset:24576\n"
    "s_waitcnt lgkmcnt(5)\n"
    "ds_write_b128 v7, v[20:23] offset:25600\n"
    "s_waitcnt lgkmcnt(5)\n"
    "ds_write_b128 v7, v[24:27] offset:26624\n"
    "s_waitcnt lgkmcnt(5)\n"
    "ds_write_b128 v7, v[28:31] offset:27648\n"
    "s_waitcnt lgkmcnt(5)\n"
    "ds_write_b128 v7, v[32:35] offset:28672\n"
    "s_waitcnt lgkmcnt(5)\n"
    "ds_write_b128 v7, v[36:39] offset:29696\n"
    "v_lshrrev_b32 v64, 4, v0\n"
    "v_lshrrev_b32 v65, 4, v64\n"
    "v_and_b32 v64, 15, v64\n"
    "v_lshlrev_b32 v65, 2, v65\n"
    "v_sub_u32 v64, v64, v65\n"
    "s_and_b32 s38, s61, 1\n"
    "s_lshl_b32 s38, s38, 4\n"
    "v_add_u32 v64, s38, v64\n"
    "v_cmp_le_i32 s[80:81], 0, v64\n"
    "v_cmp_le_i32 s[82:83], 1, v64\n"
    "v_cmp_le_i32 s[84:85], 2, v64\n"
    "v_cmp_le_i32 s[86:87], 3, v64\n"
    "v_cmp_le_i32 s[88:89], 16, v64\n"
    "v_cmp_le_i32 s[90:91], 17, v64\n"
    "v_cmp_le_i32 s[92:93], 18, v64\n"
    "v_cmp_le_i32 s[94:95], 19, v64\n"
    "v_mov_b32 v64, 158224\n"
    "v_mov_b64 v[66:67], 0\n"
    "ds_write_b64 v64, v[66:67] offset:16\n"
    "s_mov_b32 s54, 0x80000000\n"
    "s_xor_b32 s46, s46, 0x80000\n"
    "s_mov_b32 s44, s45\n"
    "s_waitcnt vmcnt(0)\n"
    "v_mov_b32 v116, v119\n"
    "v_cmp_ne_u32 vcc, 0, v116\n"
    "v_readfirstlane_b32 s70, v116\n"
    "s_nop 0\n"
    "s_andn2_b32 s38, 7, vcc_lo\n"
    "s_cbranch_scc0 LS0K%=\n"
    "s_mov_b32 s37, 0x80000\n"
    "LS0W%=:\n"
    "s_mov_b64 exec, 7\n"
    "buffer_load_dword v116, v0, s[16:19], s59 offen sc1\n"
    "s_mov_b64 exec, -1\n"
    "s_waitcnt vmcnt(0)\n"
    "v_cmp_ne_u32 vcc, 0, v116\n"
    "v_readfirstlane_b32 s70, v116\n"
    "s_nop 0\n"
    "s_andn2_b32 s38, 7, vcc_lo\n"
    "s_cbranch_scc0 LS0K%=\n"
    "s_sleep 2\n"
    "s_sub_u32 s37, s37, 1\n"
    "s_cbranch_scc0 LS0W%=\n"
    "s_branch LFAIL%=\n"
    "LS0K%=:\n"
    "s_lshr_b32 s38, s70, 16\n"
    "s_cmp_gt_i32 s38, s68\n"
    "s_cbranch_scc1 LREC%=\n"
    "s_add_u32 s53, s50, s55\n"
    "s_add_u32 s49, s48, s62\n"
    "s_add_u32 s51, s53, 0\n"
    "s_lshr_b32 s38, s51, 8\n"
    "s_xor_b32 s38, s38, s56\n"
    "s_and_b32 s38, s38, 0x2000\n"
    "s_or_b32 s51, s51, s38\n"
    "s_add_u32 s52, s53, 524288\n"
    "s_lshr_b32 s38, s52, 8\n"
    "s_xor_b32 s38, s38, s56\n"
    "s_and_b32 s38, s38, 0x2000\n"
    "s_or_b32 s52, s52, s38\n"
    "s_add_u32 s53, s53, 1048576\n"
    "s_lshr_b32 s38, s53, 8\n"
    "s_xor_b32 s38, s38, s56\n"
    "s_and_b32 s38, s38, 0x2000\n"
    "s_or_b32 s53, s53, s38\n"
    "s_add_u32 s59, s59, 128\n"
    "s_waitcnt lgkmcnt(0)\n"
    "s_barrier\n"
    "s_cmp_lg_u32 s61, 1\n"
    "s_cbranch_scc1 LBM%=\n"
    "s_sub_u32 s51, s51, 2048\n"
    "s_mov_b32 m0, 122880\n"
    "s_nop 0\n"
    "buffer_load_dwordx4 v0, s[16:19], s51 offen offset:0 sc1 lds\n"
    "buffer_load_dwordx4 v0, s[16:19], s51 offen offset:1024 sc1 lds\n"
    "buffer_load_dwordx4 v0, s[16:19], s51 offen offset:2048 sc1 lds\n"
    "buffer_load_dwordx4 v0, s[16:19], s51 offen offset:3072 sc1 lds\n"
    "s_mov_b32 m0, 126976\n"
    "s_add_u32 s51, s51, 4096\n"
    "buffer_load_dwordx4 v0, s[16:19], s51 offen offset:0 sc1 lds\n"
    "buffer_load_dwordx4 v0, s[16:19], s51 offen offset:1024 sc1 lds\n"
    "buffer_load_dwordx4 v0, s[16:19], s51 offen offset:2048 sc1 lds\n"
    "buffer_load_dwordx4 v0, s[16:19], s51 offen offset:3072 sc1 lds\n"
    "s_mov_b32 s44, s45\n"
    "v_mov_b32 v112, 158224\n"
    "v_mov_b32 v113, s44\n"
    "ds_write_b32 v112, v113\n"
    "s_waitcnt lgkmcnt(0)\n"
    "s_branch LMO%=\n"
    "LBM%=:\n"
    "s_waitcnt lgkmcnt(0)\n"
    "s_barrier\n"
    "s_add_u32 m0, s62, 147456\n"
    "s_nop 0\n"
    "buffer_load_dwordx4 v0, s[16:19], s49 offen offset:0 sc1 lds\n"
    "buffer_load_dwordx4 v0, s[16:19], s49 offen offset:1024 sc1 lds\n"
    "v_mov_b32 v112, 158224\n"
    "ds_read_b64 v[114:115], v112 offset:0\n"
    "ds_read_b128 v[16:19], v9 offset:16384\n"
    "ds_read_b128 v[20:23], v9 offset:17408\n"
    "s_waitcnt lgkmcnt(2)\n"
    "v_readfirstlane_b32 s39, v114\n"
    "v_readfirstlane_b32 s44, v115\n"
    "s_sub_u32 s38, s39, s44\n"
    "s_cmp_gt_i32 s38, 0\n"
    "s_cbranch_scc1 LSL%=\n"
    "s_cmp_lg_u32 s32, 0\n"
    "s_cbranch_scc1 LSV%=\n"
    "s_add_u32 m0, s62, 131072\n"
    "s_nop 0\n"
    "buffer_load_dwordx4 v0, s[16:19], s52 offen offset:0 sc1 lds\n"
    "buffer_load_dwordx4 v0, s[16:19], s52 offen offset:1024 sc1 lds\n"
    "LSV%=:\n"
    "v_lshrrev_b32 v113, 4, v0\n"
    "v_and_b32 v113, 0x30, v113\n"
    "v_lshl_add_u32 v113, v113, 4, v0\n"
    "ds_read_b128 v[64:67], v7 offset:0\n"
    "ds_read_b128 v[68:71], v7 offset:1024\n"
    "ds_read_b128 v[72:75], v7 offset:2048\n"
    "ds_read_b128 v[76:79], v7 offset:3072\n"
    "ds_read_b128 v[80:83], v7 offset:4096\n"
    "ds_read_b128 v[84:87], v7 offset:5120\n"
    "ds_read_b128 v[88:91], v7 offset:6144\n"
    "ds_read_b128 v[92:95], v7 offset:7168\n"
    "ds_read_b128 v[96:99], v7 offset:8192\n"
    "ds_read_b128 v[100:103], v7 offset:9216\n"
    "ds_read_b128 v[104:107], v7 offset:10240\n"
    "ds_read_b128 v[108:111], v7 offset:11264\n"
    "v_mov_b64 v[114:115], 0\n"
    "s_nop 1\n"
    "v_mfma_f32_32x32x8_bf16 v[48:63], v[114:115], v[114:115], 0\n"
    "s_waitcnt lgkmcnt(13)\n"
    "buffer_store_dwordx4 v[16:19], v113, s[16:19], s54 offen offset:0 sc1\n"
    "s_waitcnt lgkmcnt(12)\n"
    "buffer_store_dwordx4 v[20:23], v113, s[16:19], s54 offen offset:2048 sc1\n"
    "s_cmp_eq_u32 s32, 0\n"
    "s_cbranch_scc1 LSW%=\n"
    "s_waitcnt vmcnt(0)\n"
    "LSW%=:\n"
    "s_waitcnt vmcnt(4)\n"
    "s_waitcnt lgkmcnt(0)\n"
    "LC%=:\n"
    "s_barrier\n"
    "ds_read_b128 v[16:19], v2 offset:0\n"
    "ds_read_b128 v[20:23], v2 offset:1024\n"
    "ds_read_b128 v[24:27], v2 offset:2048\n"
    "s_cmp_lg_u32 s32, 0\n"
    "s_cbranch_scc1 LSQ%=\n"
    "s_add_u32 m0, s62, 139264\n"
    "s_nop 0\n"
    "buffer_load_dwordx4 v0, s[16:19], s53 offen offset:0 sc1 lds\n"
    "buffer_load_dwordx4 v0, s[16:19], s53 offen offset:1024 sc1 lds\n"
    "LSQ%=:\n"
    "s_waitcnt lgkmcnt(2)\n"
    "v_smfmac_f32_16x16x64_bf16 v[48:51], v[16:19], v[64:71], v1\n"
    "ds_read_b128 v[28:31], v2 offset:3072\n"
    "v_smfmac_f32_16x16x64_bf16 v[52:55], v[16:19], v[72:79], v1\n"
    "ds_read_b128 v[32:35], v2 offset:4096\n"
    "s_waitcnt lgkmcnt(3)\n"
    "v_smfmac_f32_16x16x64_bf16 v[48:51], v[20:23], v[80:87], v1\n"
    "ds_read_b128 v[36:39], v2 offset:5120\n"
    "v_smfmac_f32_16x16x64_bf16 v[52:55], v[20:23], v[88:95], v1\n"
    "ds_read_b128 v[40:43], v2 offset:6144\n"
    "s_waitcnt lgkmcnt(4)\n"
    "v_smfmac_f32_16x16x64_bf16 v[48:51], v[24:27], v[96:103], v1\n"
    "ds_read_b128 v[44:47], v2 offset:7168\n"
    "v_max3_u32 v112, v19, v23, v27\n"
    "v_smfmac_f32_16x16x64_bf16 v[52:55], v[24:27], v[104:111], v1\n"
    "ds_read_b128 v[64:67], v7 offset:12288\n"
    "s_waitcnt lgkmcnt(5)\n"
    "v_smfmac_f32_16x16x64_bf16 v[48:51], v[28:31], a[0:7], v1\n"
    "ds_read_b128 v[68:71], v7 offset:13312\n"
    "s_waitcnt lgkmcnt(5)\n"
    "v_max3_u32 v112, v112, v31, v35\n"
    "v_smfmac_f32_16x16x64_bf16 v[52:55], v[28:31], a[8:15], v1\n"
    "ds_read_b128 v[72:75], v7 offset:14336\n"
    "v_smfmac_f32_16x16x64_bf16 v[48:51], v[32:35], a[16:23], v1\n"
    "ds_read_b128 v[76:79], v7 offset:15360\n"
    "s_waitcnt lgkmcnt(5)\n"
    "v_max3_u32 v112, v112, v39, v43\n"
    "v_smfmac_f32_16x16x64_bf16 v[52:55], v[32:35], a[24:31], v1\n"
    "ds_read_b128 v[80:83], v7 offset:16384\n"
    "v_smfmac_f32_16x16x64_bf16 v[48:51], v[36:39], a[32:39], v1\n"
    "ds_read_b128 v[84:87], v7 offset:17408\n"
    "s_waitcnt lgkmcnt(6)\n"
    "v_max_u32 v112, v112, v47\n"
    "v_smfmac_f32_16x16x64_bf16 v[52:55], v[36:39], a[40:47], v1\n"
    "ds_read_b128 v[88:91], v7 offset:18432\n"
    "v_smfmac_f32_16x16x64_bf16 v[48:51], v[40:43], a[48:55], v1\n"
    "ds_read_b128 v[92:95], v7 offset:19456\n"
    "v_cmp_eq_u32 vcc, -1, v112\n"
    "v_smfmac_f32_16x16x64_bf16 v[52:55], v[40:43], a[56:63], v1\n"
    "v_smfmac_f32_16x16x64_bf16 v[48:51], v[44:47], a[64:71], v1\n"
    "s_cbranch_vccnz LMR%=\n"
    "v_smfmac_f32_16x16x64_bf16 v[52:55], v[44:47], a[72:79], v1\n"
    "s_waitcnt lgkmcnt(6)\n"
    "v_smfmac_f32_16x16x64_bf16 v[56:59], v[16:19], v[64:71], v1\n"
    "s_waitcnt lgkmcnt(4)\n"
    "v_smfmac_f32_16x16x64_bf16 v[60:63], v[16:19], v[72:79], v1\n"
    "s_waitcnt lgkmcnt(2)\n"
    "v_add_f32 v48, v48, v50\n"
    "v_add_f32 v52, v52, v54\n"
    "v_smfmac_f32_16x16x64_bf16 v[56:59], v[20:23], v[80:87], v1\n"
    "s_waitcnt lgkmcnt(0)\n"
    "v_add_f32 v49, v49, v51\n"
    "v_add_f32 v53, v53, v55\n"
    "v_smfmac_f32_16x16x64_bf16 v[60:63], v[20:23], v[88:95], v1\n"
    "v_cvt_pk_bf16_f32 v48, v48, v52\n"
    "v_smfmac_f32_16x16x64_bf16 v[56:59], v[24:27], a[80:87], v1\n"
    "v_cvt_pk_bf16_f32 v49, v49, v53\n"
    "v_smfmac_f32_16x16x64_bf16 v[60:63], v[24:27], a[88:95], v1\n"
    "ds_write2_b32 v3, v48, v49 offset0:0 offset1:72\n"
    "v_add_u32 v117, s20, v5\n"
    "v_smfmac_f32_16x16x64_bf16 v[56:59], v[28:31], a[96:103], v1\n"
    "v_mov_b64 v[50:51], 0\n"
    "v_smfmac_f32_16x16x64_bf16 v[60:63], v[28:31], a[104:111], v1\n"
    "v_mov_b64 v[52:53], 0\n"
    "v_smfmac_f32_16x16x64_bf16 v[56:59], v[32:35], a[112:119], v1\n"
    "v_mov_b64 v[54:55], 0\n"
    "v_smfmac_f32_16x16x64_bf16 v[60:63], v[32:35], a[120:127], v1\n"
    "s_waitcnt lgkmcnt(0)\n"
    "s_barrier\n"
    "v_smfmac_f32_16x16x64_bf16 v[56:59], v[36:39], a[128:135], v1\n"
    "ds_read_b128 v[80:83], v117 offset:0\n"
    "ds_read_b128 v[84:87], v117 offset:64\n"
    "v_smfmac_f32_16x16x64_bf16 v[60:63], v[36:39], a[136:143], v1\n"
    "ds_read_b128 v[88:91], v9 offset:0\n"
    "ds_read_b128 v[92:95], v9 offset:1024\n"
    "v_smfmac_f32_16x16x64_bf16 v[56:59], v[40:43], a[144:151], v1\n"
    "ds_read_b128 v[96:99], v5 offset:0\n"
    "v_mov_b64 v[48:49], 0\n"
    "v_smfmac_f32_16x16x64_bf16 v[56:59], v[44:47], a[160:167], v1\n"
    "ds_read_b128 v[100:103], v5 offset:64\n"
    "v_smfmac_f32_16x16x64_bf16 v[60:63], v[40:43], a[152:159], v1\n"
    "ds_read_b128 v[104:107], v5 offset:128\n"
    "v_smfmac_f32_16x16x64_bf16 v[60:63], v[44:47], a[168:175], v1\n"
    "ds_read_b128 v[108:111], v5 offset:192\n"
    "s_waitcnt lgkmcnt(5)\n"
    "v_add_f32 v56, v56, v58\n"
    "v_mfma_f32_16x16x32_bf16 v[64:67], v[88:91], v[80:83], 0\n"
    "s_waitcnt lgkmcnt(4)\n"
    "v_add_f32 v57, v57, v59\n"
    "v_mfma_f32_16x16x32_bf16 v[64:67], v[92:95], v[84:87], v[64:67]\n"
    "v_add_f32 v60, v60, v62\n"
    "v_add_f32 v61, v61, v63\n"
    "v_cvt_pk_bf16_f32 v56, v56, v60\n"
    "v_cvt_pk_bf16_f32 v57, v57, v61\n"
    "v_smfmac_f32_16x16x64_bf16 v[48:51], v[16:19], a[176:183], v1\n"
    "ds_write2_b32 v4, v56, v57 offset0:0 offset1:72\n"
    "v_smfmac_f32_16x16x64_bf16 v[52:55], v[16:19], a[184:191], v1\n"
    "ds_write_b128 v8, v[64:67]\n"
    "v_smfmac_f32_16x16x64_bf16 v[48:51], v[20:23], a[192:199], v1\n"
    "v_mov_b64 v[60:61], s[14:15]\n"
    "v_smfmac_f32_16x16x64_bf16 v[52:55], v[20:23], a[200:207], v1\n"
    "v_mov_b64 v[62:63], s[14:15]\n"
    "v_smfmac_f32_16x16x64_bf16 v[48:51], v[24:27], a[208:215], v1\n"
    "v_smfmac_f32_16x16x64_bf16 v[52:55], v[24:27], a[216:223], v1\n"
    "s_waitcnt lgkmcnt(0)\n"
    "s_barrier\n"
    "ds_read_b128 v[64:67], v6 offset:0\n"
    "ds_read_b128 v[88:91], v15 offset:0\n"
    "v_smfmac_f32_16x16x64_bf16 v[48:51], v[28:31], a[224:231], v1\n"
    "ds_read_b128 v[92:95], v15 offset:512\n"
    "ds_read_b128 v[68:71], v6 offset:64\n"
    "v_smfmac_f32_16x16x64_bf16 v[52:55], v[28:31], a[232:239], v1\n"
    "ds_read_b128 v[72:75], v6 offset:128\n"
    "v_smfmac_f32_16x16x64_bf16 v[48:51], v[32:35], a[240:247], v1\n"
    "ds_read_b128 v[76:79], v6 offset:192\n"
    "s_waitcnt lgkmcnt(5)\n"
    "v_mfma_f32_16x16x32_bf16 v[112:115], v[64:67], v[96:99], 0\n"
    "s_waitcnt lgkmcnt(3)\n"
    "s_waitcnt lgkmcnt(2)\n"
    "v_add_f32 v88, v88, v92\n"
    "v_add_f32 v89, v89, v93\n"
    "v_mfma_f32_16x16x32_bf16 v[112:115], v[68:71], v[100:103], v[112:115]\n"
    "s_waitcnt lgkmcnt(1)\n"
    "v_add_f32 v90, v90, v94\n"
    "v_add_f32 v91, v91, v95\n"
    "v_mfma_f32_16x16x32_bf16 v[112:115], v[72:75], v[104:107], v[112:115]\n"
    "s_waitcnt lgkmcnt(0)\n"
    "v_max3_f32 v117, v88, v89, v90\n"
    "v_max_f32 v117, v117, v91\n"
    "v_mfma_f32_16x16x32_bf16 v[112:115], v[76:79], v[108:111], v[112:115]\n"
    "s_nop 0\n"
    "v_max_f32_dpp v116, v117, v117 row_ror:8 row_mask:0xf bank_mask:0xf\n"
    "v_mov_b32 v117, v116\n"
    "v_smfmac_f32_16x16x64_bf16 v[52:55], v[32:35], a[248:255], v1\n"
    "s_nop 0\n"
    "v_permlane32_swap_b32 v116, v117\n"
    "v_smfmac_f32_16x16x64_bf16 v[48:51], v[36:39], v[120:127], v1\n"
    "v_max_f32 v116, v116, v117\n"
    "v_smfmac_f32_16x16x64_bf16 v[48:51], v[40:43], v[136:143], v1\n"
    "v_max3_f32 v117, v112, v113, v116\n"
    "v_smfmac_f32_16x16x64_bf16 v[48:51], v[44:47], v[152:159], v1\n"
    "v_max3_f32 v117, v114, v115, v117\n"
    "v_smfmac_f32_16x16x64_bf16 v[52:55], v[36:39], v[128:135], v1\n"
    "v_mov_b32 v118, v117\n"
    "v_smfmac_f32_16x16x64_bf16 v[52:55], v[40:43], v[144:151], v1\n"
    "s_waitcnt vmcnt(4)\n"
    "ds_read_b128 v[80:83], v9 offset:8192\n"
    "v_permlane16_swap_b32 v117, v118\n"
    "v_smfmac_f32_16x16x64_bf16 v[52:55], v[44:47], v[160:167], v1\n"
    "ds_read_b128 v[84:87], v9 offset:9216\n"
    "v_max_f32 v117, v117, v118\n"
    "v_mul_f32 v117, 0xbe0293ee, v117\n"
    "v_fma_f32 v88, v88, s65, v117\n"
    "v_fma_f32 v89, v89, s65, v117\n"
    "v_fma_f32 v90, v90, s65, v117\n"
    "v_fma_f32 v91, v91, s65, v117\n"
    "v_fma_f32 v112, v112, s65, v117\n"
    "v_fma_f32 v113, v113, s65, v117\n"
    "v_exp_f32 v88, v88\n"
    "v_exp_f32 v89, v89\n"
    "v_exp_f32 v90, v90\n"
    "v_exp_f32 v91, v91\n"
    "v_cvt_pk_bf16_f32 v56, v88, v89\n"
    "v_cvt_pk_bf16_f32 v57, v90, v91\n"
    "v_exp_f32 v112, v112\n"
    "v_mov_b32_dpp v58, v56 row_shl:8 row_mask:0xf bank_mask:0xf bound_ctrl:1\n"
    "v_mov_b32_dpp v59, v57 row_shl:8 row_mask:0xf bank_mask:0xf bound_ctrl:1\n"
    "v_exp_f32 v113, v113\n"
    "v_mov_b32 v93, 0\n"
    "v_mfma_f32_16x16x32_bf16 v[104:107], v[60:63], v[56:59], 0\n"
    "s_waitcnt lgkmcnt(1)\n"
    "v_cvt_pk_bf16_f32 v92, v112, v113\n"
    "v_add_f32 v48, v48, v50\n"
    "v_mfma_f32_16x16x32_bf16 v[96:99], v[80:83], v[56:59], 0\n"
    "v_add_f32 v49, v49, v51\n"
    "v_cvt_pk_bf16_f32 v48, v48, v49\n"
    "v_mfma_f32_16x16x16_bf16 v[104:107], v[60:61], v[92:93], v[104:107]\n"
    "s_waitcnt lgkmcnt(0)\n"
    "v_add_f32 v52, v52, v54\n"
    "v_add_f32 v53, v53, v55\n"
    "v_mfma_f32_16x16x32_bf16 v[100:103], v[84:87], v[56:59], 0\n"
    "v_cvt_pk_bf16_f32 v49, v52, v53\n"
    "v_cvt_pk_bf16_f32 v95, v112, v113\n"
    "v_mov_b32 v94, 0\n"
    "v_mfma_f32_16x16x16_bf16 v[96:99], v[48:49], v[92:93], v[96:99]\n"
    "v_rcp_f32 v104, v104\n"
    "v_mfma_f32_16x16x16_bf16 v[100:103], v[48:49], v[94:95], v[100:103]\n"
    "ds_write_b64 v10, v[48:49]\n"
    "v_mov_b64 v[114:115], 0\n"
    "s_mov_b64 exec, s[78:79]\n"
    "s_nop 0\n"
    "v_pk_mul_f32 v[96:97], v[96:97], v[104:105] op_sel_hi:[1,0]\n"
    "v_pk_mul_f32 v[98:99], v[98:99], v[104:105] op_sel_hi:[1,0]\n"
    "v_cvt_pk_bf16_f32 v96, v96, v97\n"
    "v_cvt_pk_bf16_f32 v97, v98, v99\n"
    "ds_write_b64 v11, v[96:97] offset:0\n"
    "v_pk_mul_f32 v[100:101], v[100:101], v[104:105] op_sel_hi:[1,0]\n"
    "v_pk_mul_f32 v[102:103], v[102:103], v[104:105] op_sel_hi:[1,0]\n"
    "v_cvt_pk_bf16_f32 v98, v100, v101\n"
    "v_cvt_pk_bf16_f32 v99, v102, v103\n"
    "ds_write_b64 v11, v[98:99] offset:8\n"
    "s_mov_b64 exec, 7\n"
    "buffer_load_dword v116, v0, s[16:19], s59 offen sc1\n"
    "s_mov_b64 exec, -1\n"
    "v_mfma_f32_32x32x8_bf16 v[72:87], v[114:115], v[114:115], 0\n"
    "s_add_u32 s36, s36, 1\n"
    "s_cmp_eq_u32 s36, 64\n"
    "s_waitcnt lgkmcnt(0)\n"
    "s_waitcnt vmcnt(1)\n"
    "s_barrier\n"
    "ds_read_b128 v[104:107], v12 offset:0\n"
    "ds_read_b128 v[108:111], v12 offset:1024\n"
    "v_mfma_f32_32x32x8_bf16 v[48:63], v[114:115], v[114:115], 0\n"
    "s_cbranch_scc1 LEND%=\n"
    "s_waitcnt lgkmcnt(1)\n"
    "v_smfmac_f32_16x16x64_bf16 v[72:75], v[104:107], v[168:175], v1\n"
    "ds_read_b128 v[64:67], v7 offset:20480\n"
    "s_waitcnt lgkmcnt(1)\n"
    "v_smfmac_f32_16x16x64_bf16 v[72:75], v[108:111], v[176:183], v1\n"
    "ds_read_b128 v[68:71], v7 offset:21504\n"
    "v_smfmac_f32_16x16x64_bf16 v[76:79], v[104:107], v[184:191], v1\n"
    "ds_read_b128 v[16:19], v7 offset:22528\n"
    "v_smfmac_f32_16x16x64_bf16 v[76:79], v[108:111], v[192:199], v1\n"
    "ds_read_b128 v[20:23], v7 offset:23552\n"
    "v_add_f32 v72, v72, v74\n"
    "v_smfmac_f32_16x16x64_bf16 v[80:83], v[104:107], v[200:207], v1\n"
    "ds_read_b128 v[24:27], v7 offset:24576\n"
    "v_add_f32 v73, v73, v75\n"
    "v_smfmac_f32_16x16x64_bf16 v[80:83], v[108:111], v[208:215], v1\n"
    "ds_read_b128 v[28:31], v7 offset:25600\n"
    "v_add_f32 v76, v76, v78\n"
    "v_smfmac_f32_16x16x64_bf16 v[84:87], v[104:107], v[216:223], v1\n"
    "ds_read_b128 v[32:35], v7 offset:26624\n"
    "v_add_f32 v77, v77, v79\n"
    "v_smfmac_f32_16x16x64_bf16 v[84:87], v[108:111], v[224:231], v1\n"
    "ds_read_b128 v[36:39], v7 offset:27648\n"
    "v_cvt_pk_bf16_f32 v88, v72, v76\n"
    "v_cvt_pk_bf16_f32 v90, v73, v77\n"
    "v_smfmac_f32_16x16x64_bf16 v[48:51], v[104:107], v[232:239], v1\n"
    "ds_read_b128 v[40:43], v7 offset:28672\n"
    "v_add_f32 v80, v80, v82\n"
    "v_add_f32 v81, v81, v83\n"
    "v_smfmac_f32_16x16x64_bf16 v[48:51], v[108:111], v[240:247], v1\n"
    "ds_read_b128 v[44:47], v7 offset:29696\n"
    "v_add_f32 v85, v85, v87\n"
    "v_add_f32 v84, v84, v86\n"
    "v_cvt_pk_bf16_f32 v91, v81, v85\n"
    "v_cvt_pk_bf16_f32 v89, v80, v84\n"
    "v_and_or_b32 v91, v91, -2, s64\n"
    "buffer_store_dwordx4 v[88:91], v13, s[16:19], s46 offen sc1\n"
    "v_smfmac_f32_16x16x64_bf16 v[52:55], v[104:107], v[248:255], v1\n"
    "s_waitcnt lgkmcnt(8)\n"
    "v_smfmac_f32_16x16x64_bf16 v[52:55], v[108:111], v[64:71], v1\n"
    "s_waitcnt lgkmcnt(6)\n"
    "v_add_f32 v48, v48, v50\n"
    "v_smfmac_f32_16x16x64_bf16 v[56:59], v[104:107], v[16:23], v1\n"
    "s_waitcnt lgkmcnt(4)\n"
    "v_add_f32 v49, v49, v51\n"
    "v_smfmac_f32_16x16x64_bf16 v[56:59], v[108:111], v[24:31], v1\n"
    "s_waitcnt lgkmcnt(2)\n"
    "v_add_f32 v52, v52, v54\n"
    "v_add_f32 v53, v53, v55\n"
    "v_smfmac_f32_16x16x64_bf16 v[60:63], v[104:107], v[32:39], v1\n"
    "s_waitcnt lgkmcnt(0)\n"
    "v_cvt_pk_bf16_f32 v92, v48, v52\n"
    "v_cvt_pk_bf16_f32 v94, v49, v53\n"
    "v_smfmac_f32_16x16x64_bf16 v[60:63], v[108:111], v[40:47], v1\n"
    "v_add_f32 v56, v56, v58\n"
    "v_add_f32 v57, v57, v59\n"
    "s_nop 5\n"
    "v_add_f32 v61, v61, v63\n"
    "v_add_f32 v60, v60, v62\n"
    "v_cvt_pk_bf16_f32 v95, v57, v61\n"
    "v_cvt_pk_bf16_f32 v93, v56, v60\n"
    "v_and_or_b32 v95, v95, -2, s64\n"
    "buffer_store_dwordx4 v[92:95], v13, s[16:19], s47 offen sc1\n"
    "s_add_u32 s54, s50, s57\n"
    "s_lshr_b32 s38, s54, 8\n"
    "s_xor_b32 s38, s38, s74\n"
    "s_and_b32 s38, s38, 0x2000\n"
    "s_or_b32 s54, s54, s38\n"
    "s_add_u32 s50, s50, 2621440\n"
    "s_add_u32 s48, s48, 16384\n"
    "s_cmp_eq_u32 s61, 1\n"
    "s_cbranch_scc1 LW1%=\n"
    "s_cmp_gt_u32 s61, 1\n"
    "s_cbranch_scc1 LW2%=\n"
    "s_waitcnt vmcnt(2)\n"
    "v_cmp_ne_u32 vcc, 0, v116\n"
    "v_readfirstlane_b32 s70, v116\n"
    "s_nop 0\n"
    "s_andn2_b32 s38, 7, vcc_lo\n"
    "s_cbranch_scc1 LE2%=\n"
    "s_lshr_b32 s38, s70, 16\n"
    "s_cmp_gt_i32 s38, s68\n"
    "s_cbranch_scc1 LREC%=\n"
    "LE2%=:\n"
    "s_add_u32 s53, s50, s55\n"
    "s_add_u32 s49, s48, s62\n"
    "s_add_u32 s51, s53, 0\n"
    "s_lshr_b32 s38, s51, 8\n"
    "s_xor_b32 s38, s38, s56\n"
    "s_and_b32 s38, s38, 0x2000\n"
    "s_or_b32 s51, s51, s38\n"
    "s_add_u32 s52, s53, 524288\n"
    "s_lshr_b32 s38, s52, 8\n"
    "s_xor_b32 s38, s38, s56\n"
    "s_and_b32 s38, s38, 0x2000\n"
    "s_or_b32 s52, s52, s38\n"
    "s_add_u32 s53, s53, 1048576\n"
    "s_lshr_b32 s38, s53, 8\n"
    "s_xor_b32 s38, s38, s56\n"
    "s_and_b32 s38, s38, 0x2000\n"
    "s_or_b32 s53, s53, s38\n"
    "ds_read_b128 v[64:67], v9 offset:35584\n"
    "s_add_u32 s73, s46, s63\n"
    "s_add_u32 s66, s73, 4096\n"
    "s_add_u32 s39, s44, s9\n"
    "s_waitcnt lgkmcnt(0)\n"
    "LUG%=:\n"
    "s_memtime s[40:41]\n"
    "s_waitcnt lgkmcnt(0)\n"
    "s_sub_u32 s40, s39, s40\n"
    "s_sub_u32 s41, s40, 253\n"
    "s_cmp_lt_u32 s41, 16384\n"
    "s_cbranch_scc1 LUG%=\n"
    "s_cmp_lt_u32 s40, 253\n"
    "s_cselect_b32 s40, s40, 0\n"
    "s_and_b32 s40, s40, 0xfc\n"
    "s_getpc_b64 s[42:43]\n"
    "s_sub_u32 s40, 272, s40\n"
    "s_add_u32 s42, s42, s40\n"
    "s_addc_u32 s43, s43, 0\n"
    "s_setpc_b64 s[42:43]\n"
    ".rept 63\n"
    "s_nop 0\n"
    ".endr\n"
    "buffer_load_dwordx4 v[16:19], v0, s[16:19], s73 offen offset:0 sc1\n"
    "buffer_load_dwordx4 v[20:23], v0, s[16:19], s73 offen offset:1024 sc1\n"
    "buffer_load_dwordx4 v[24:27], v0, s[16:19], s73 offen offset:2048 sc1\n"
    "buffer_load_dwordx4 v[28:31], v0, s[16:19], s73 offen offset:3072 sc1\n"
    "buffer_load_dwordx4 v[32:35], v0, s[16:19], s66 offen offset:0 sc1\n"
    "buffer_load_dwordx4 v[36:39], v0, s[16:19], s66 offen offset:1024 sc1\n"
    "buffer_load_dwordx4 v[40:43], v0, s[16:19], s66 offen offset:2048 sc1\n"
    "buffer_load_dwordx4 v[44:47], v0, s[16:19], s66 offen offset:3072 sc1\n"
    "s_waitcnt vmcnt(7)\n"
    "v_mfma_f32_16x16x32_bf16 v[68:71], v[64:67], v[16:19], 0\n"
    "s_waitcnt vmcnt(6)\n"
    "v_mfma_f32_16x16x32_bf16 v[68:71], v[64:67], v[20:23], v[68:71]\n"
    "s_waitcnt vmcnt(5)\n"
    "v_bitop3_b32 v76, v19, v23, s64 bitop3:0x7e\n"
    "v_mfma_f32_16x16x32_bf16 v[68:71], v[64:67], v[24:27], v[68:71]\n"
    "s_waitcnt vmcnt(4)\n"
    "v_mfma_f32_16x16x32_bf16 v[68:71], v[64:67], v[28:31], v[68:71]\n"
    "s_waitcnt vmcnt(3)\n"
    "v_bitop3_b32 v77, v27, v31, s64 bitop3:0x7e\n"
    "v_mfma_f32_16x16x32_bf16 v[72:75], v[64:67], v[32:35], 0\n"
    "s_waitcnt vmcnt(2)\n"
    "v_mfma_f32_16x16x32_bf16 v[72:75], v[64:67], v[36:39], v[72:75]\n"
    "v_bitop3_b32 v78, v35, v39, s64 bitop3:0x7e\n"
    "v_or3_b32 v76, v76, v77, v78\n"
    "v_add_f32_dpp v68, v68, v68 row_ror:8 row_mask:0xf bank_mask:0xf\n"
    "v_add_f32_dpp v69, v69, v69 row_ror:8 row_mask:0xf bank_mask:0xf\n"
    "v_add_f32_dpp v70, v70, v70 row_ror:8 row_mask:0xf bank_mask:0xf\n"
    "v_add_f32_dpp v71, v71, v71 row_ror:8 row_mask:0xf bank_mask:0xf\n"
    "v_cvt_pk_bf16_f32 v81, v70, v71\n"
    "v_cvt_pk_bf16_f32 v80, v68, v69\n"
    "s_waitcnt vmcnt(1)\n"
    "v_mfma_f32_16x16x32_bf16 v[72:75], v[64:67], v[40:43], v[72:75]\n"
    "s_waitcnt vmcnt(0)\n"
    "v_mfma_f32_16x16x32_bf16 v[72:75], v[64:67], v[44:47], v[72:75]\n"
    "v_bitop3_b32 v79, v43, v47, s64 bitop3:0x7e\n"
    "v_or_b32 v76, v76, v79\n"
    "v_and_b32 v77, 1, v76\n"
    "v_cmp_ne_u32 vcc, 0, v77\n"
    "s_cbranch_vccnz LGR%=\n"
    "s_nop 2\n"
    "v_add_f32_dpp v72, v72, v72 row_ror:8 row_mask:0xf bank_mask:0xf\n"
    "v_add_f32_dpp v73, v73, v73 row_ror:8 row_mask:0xf bank_mask:0xf\n"
    "v_add_f32_dpp v74, v74, v74 row_ror:8 row_mask:0xf bank_mask:0xf\n"
    "v_add_f32_dpp v75, v75, v75 row_ror:8 row_mask:0xf bank_mask:0xf\n"
    "v_cvt_pk_bf16_f32 v83, v74, v75\n"
    "v_cvt_pk_bf16_f32 v82, v72, v73\n"
    "v_min_u32 v83, -2, v83\n"
    "buffer_store_dwordx4 v[80:83], v14, s[16:19], s48 offen sc1\n"
    "v_cmp_ne_u32 vcc, 0, v116\n"
    "v_readfirstlane_b32 s70, v116\n"
    "s_nop 0\n"
    "s_andn2_b32 s38, 7, vcc_lo\n"
    "s_cbranch_scc0 LS1K%=\n"
    "s_mov_b32 s37, 0x80000\n"
    "LS1W%=:\n"
    "s_mov_b64 exec, 7\n"
    "buffer_load_dword v116, v0, s[16:19], s59 offen sc1\n"
    "s_mov_b64 exec, -1\n"
    "s_waitcnt vmcnt(0)\n"
    "v_cmp_ne_u32 vcc, 0, v116\n"
    "v_readfirstlane_b32 s70, v116\n"
    "s_nop 0\n"
    "s_andn2_b32 s38, 7, vcc_lo\n"
    "s_cbranch_scc0 LS1K%=\n"
    "s_sleep 2\n"
    "s_sub_u32 s37, s37, 1\n"
    "s_cbranch_scc0 LS1W%=\n"
    "s_branch LFAIL%=\n"
    "LS1K%=:\n"
    "s_lshr_b32 s38, s70, 16\n"
    "s_cmp_gt_i32 s38, s68\n"
    "s_cbranch_scc1 LREC%=\n"
    "s_mov_b32 s32, 0\n"
    "s_xor_b32 s46, s46, 0x80000\n"
    "s_add_u32 s47, s46, 4096\n"
    "s_bfe_u32 s64, s36, 0x10001\n"
    "s_add_u32 s44, s44, s8\n"
    "s_add_u32 s59, s59, 128\n"
    "s_branch LBM%=\n"
    "LGR%=:\n"
    "s_add_u32 s33, s33, 1\n"
    "s_cmp_gt_u32 s33, 0x40000\n"
    "s_cbranch_scc1 LFAIL%=\n"
    "s_and_b32 s38, s33, 63\n"
    "s_cbranch_scc1 LE4%=\n"
    "s_mov_b64 exec, 7\n"
    "buffer_load_dword v116, v0, s[16:19], s59 offen sc1\n"
    "s_mov_b64 exec, -1\n"
    "s_waitcnt vmcnt(0)\n"
    "v_cmp_ne_u32 vcc, 0, v116\n"
    "v_readfirstlane_b32 s70, v116\n"
    "s_nop 0\n"
    "s_andn2_b32 s38, 7, vcc_lo\n"
    "s_cbranch_scc1 LE4%=\n"
    "s_lshr_b32 s38, s70, 16\n"
    "s_cmp_gt_i32 s38, s68\n"
    "s_cbranch_scc1 LREC%=\n"
    "LE4%=:\n"
    "s_add_u32 s44, s44, s13\n"
    "LGM%=:\n"
    "s_memtime s[40:41]\n"
    "s_add_u32 s42, s21, s9\n"
    "s_waitcnt lgkmcnt(0)\n"
    "s_sub_u32 s40, s40, s42\n"
    "s_sub_u32 s41, s44, s40\n"
    "s_cmp_ge_i32 s41, 0\n"
    "s_cbranch_scc1 LGMD%=\n"
    "LGMI%=:\n"
    "s_add_u32 s44, s44, s13\n"
    "s_add_u32 s41, s41, s13\n"
    "s_cbranch_scc0 LGMI%=\n"
    "s_branch LGM%=\n"
    "LGMD%=:\n"
    "s_add_u32 s39, s44, s9\n"
    "s_add_u32 s38, s44, s8\n"
    "v_mov_b32 v76, s72\n"
    "v_mov_b32 v77, s38\n"
    "ds_write_b32 v76, v77 offset:16\n"
    "s_branch LUG%=\n"
    "LMR%=:\n"
    "s_add_u32 s33, s33, 1\n"
    "s_cmp_gt_u32 s33, 0x40000\n"
    "s_cbranch_scc1 LFAIL%=\n"
    "s_add_u32 s39, s44, s13\n"
    "s_mov_b32 s54, 0x80000000\n"
    "LSL%=:\n"
    "s_mov_b32 s44, s39\n"
    "s_waitcnt lgkmcnt(0)\n"
    "s_barrier\n"
    "s_cmp_lg_u32 s61, 1\n"
    "s_cbranch_scc1 LBM%=\n"
    "LMO%=:\n"
    "LMP%=:\n"
    "s_memtime s[40:41]\n"
    "s_add_u32 s42, s21, 0\n"
    "s_waitcnt lgkmcnt(0)\n"
    "s_sub_u32 s40, s40, s42\n"
    "s_sub_u32 s41, s44, s40\n"
    "s_cmp_ge_i32 s41, 0\n"
    "s_cbranch_scc1 LMPD%=\n"
    "LMPI%=:\n"
    "s_add_u32 s44, s44, s13\n"
    "s_add_u32 s41, s41, s13\n"
    "s_cbranch_scc0 LMPI%=\n"
    "s_branch LMP%=\n"
    "LMPD%=:\n"
    "v_mov_b32 v64, s72\n"
    "v_mov_b32 v65, s44\n"
    "ds_write_b32 v64, v65 offset:16\n"
    "s_waitcnt lgkmcnt(0)\n"
    "s_branch LUM%=\n"
    "LW1%=:\n"
    "buffer_store_dword v1, off, s[16:19], s58 sc1\n"
    "s_add_u32 s58, s58, 4\n"
    "s_bitset0_b32 s58, 31\n"
    "s_waitcnt vmcnt(3)\n"
    "v_cmp_ne_u32 vcc, 0, v116\n"
    "v_readfirstlane_b32 s70, v116\n"
    "s_nop 0\n"
    "s_andn2_b32 s38, 7, vcc_lo\n"
    "s_cbranch_scc0 LS2K%=\n"
    "s_mov_b32 s37, 0x80000\n"
    "LS2W%=:\n"
    "s_mov_b64 exec, 7\n"
    "buffer_load_dword v116, v0, s[16:19], s59 offen sc1\n"
    "s_mov_b64 exec, -1\n"
    "s_waitcnt vmcnt(0)\n"
    "v_cmp_ne_u32 vcc, 0, v116\n"
    "v_readfirstlane_b32 s70, v116\n"
    "s_nop 0\n"
    "s_andn2_b32 s38, 7, vcc_lo\n"
    "s_cbranch_scc0 LS2K%=\n"
    "s_sleep 2\n"
    "s_sub_u32 s37, s37, 1\n"
    "s_cbranch_scc0 LS2W%=\n"
    "s_branch LFAIL%=\n"
    "LS2K%=:\n"
    "s_lshr_b32 s38, s70, 16\n"
    "s_cmp_gt_i32 s38, s68\n"
    "s_cbranch_scc1 LREC%=\n"
    "s_add_u32 s53, s50, s55\n"
    "s_add_u32 s49, s48, s62\n"
    "s_add_u32 s51, s53, 0\n"
    "s_lshr_b32 s38, s51, 8\n"
    "s_xor_b32 s38, s38, s56\n"
    "s_and_b32 s38, s38, 0x2000\n"
    "s_or_b32 s51, s51, s38\n"
    "s_add_u32 s52, s53, 524288\n"
    "s_lshr_b32 s38, s52, 8\n"
    "s_xor_b32 s38, s38, s56\n"
    "s_and_b32 s38, s38, 0x2000\n"
    "s_or_b32 s52, s52, s38\n"
    "s_add_u32 s53, s53, 1048576\n"
    "s_lshr_b32 s38, s53, 8\n"
    "s_xor_b32 s38, s38, s56\n"
    "s_and_b32 s38, s38, 0x2000\n"
    "s_or_b32 s53, s53, s38\n"
    "v_mov_b32 v64, 158240\n"
    "s_mov_b32 s37, 0x80000\n"
    "LKW%=:\n"
    "ds_read_b64 v[66:67], v64\n"
    "s_waitcnt lgkmcnt(0)\n"
    "v_min_u32 v66, v66, v67\n"
    "v_cmp_eq_u32 vcc, s36, v66\n"
    "s_cbranch_vccnz LKK%=\n"
    "s_sleep 1\n"
    "s_sub_u32 s37, s37, 1\n"
    "s_cbranch_scc0 LKW%=\n"
    "s_branch LFAIL%=\n"
    "LKK%=:\n"
    "s_add_u32 s39, s44, s12\n"
    "LUF%=:\n"
    "s_memtime s[40:41]\n"
    "s_waitcnt lgkmcnt(0)\n"
    "s_sub_u32 s40, s39, s40\n"
    "s_sub_u32 s41, s40, 253\n"
    "s_cmp_lt_u32 s41, 16384\n"
    "s_cbranch_scc1 LUF%=\n"
    "s_cmp_lt_u32 s40, 253\n"
    "s_cselect_b32 s40, s40, 0\n"
    "s_and_b32 s40, s40, 0xfc\n"
    "s_getpc_b64 s[42:43]\n"
    "s_sub_u32 s40, 272, s40\n"
    "s_add_u32 s42, s42, s40\n"
    "s_addc_u32 s43, s43, 0\n"
    "s_setpc_b64 s[42:43]\n"
    ".rept 63\n"
    "s_nop 0\n"
    ".endr\n"
    "s_sub_u32 s51, s51, 2048\n"
    "s_mov_b32 m0, 122880\n"
    "s_nop 0\n"
    "buffer_load_dwordx4 v0, s[16:19], s51 offen offset:0 sc1 lds\n"
    "buffer_load_dwordx4 v0, s[16:19], s51 offen offset:1024 sc1 lds\n"
    "buffer_load_dwordx4 v0, s[16:19], s51 offen offset:2048 sc1 lds\n"
    "buffer_load_dwordx4 v0, s[16:19], s51 offen offset:3072 sc1 lds\n"
    "s_mov_b32 m0, 126976\n"
    "s_add_u32 s51, s51, 4096\n"
    "buffer_load_dwordx4 v0, s[16:19], s51 offen offset:0 sc1 lds\n"
    "buffer_load_dwordx4 v0, s[16:19], s51 offen offset:1024 sc1 lds\n"
    "buffer_load_dwordx4 v0, s[16:19], s51 offen offset:2048 sc1 lds\n"
    "buffer_load_dwordx4 v0, s[16:19], s51 offen offset:3072 sc1 lds\n"
    "s_mov_b32 s32, 0\n"
    "s_xor_b32 s46, s46, 0x80000\n"
    "s_add_u32 s47, s46, 4096\n"
    "s_bfe_u32 s64, s36, 0x10001\n"
    "s_add_u32 s44, s44, s8\n"
    "s_add_u32 s59, s59, 128\n"
    "LMN%=:\n"
    "LMM%=:\n"
    "s_memtime s[40:41]\n"
    "s_add_u32 s42, s21, 0\n"
    "s_waitcnt lgkmcnt(0)\n"
    "s_sub_u32 s40, s40, s42\n"
    "s_sub_u32 s41, s44, s40\n"
    "s_cmp_ge_i32 s41, 0\n"
    "s_cbranch_scc1 LMMD%=\n"
    "LMMI%=:\n"
    "s_add_u32 s44, s44, s13\n"
    "s_add_u32 s41, s41, s13\n"
    "s_cbranch_scc0 LMMI%=\n"
    "s_branch LMM%=\n"
    "LMMD%=:\n"
    "v_mov_b32 v64, s72\n"
    "v_mov_b32 v65, s44\n"
    "ds_write_b32 v64, v65 offset:16\n"
    "s_sub_u32 s39, s44, 330\n"
    "s_waitcnt lgkmcnt(0)\n"
    "LUL%=:\n"
    "s_memtime s[40:41]\n"
    "s_waitcnt lgkmcnt(0)\n"
    "s_sub_u32 s40, s39, s40\n"
    "s_sub_u32 s41, s40, 253\n"
    "s_cmp_lt_u32 s41, 16384\n"
    "s_cbranch_scc1 LUL%=\n"
    "s_cmp_lt_u32 s40, 253\n"
    "s_cselect_b32 s40, s40, 0\n"
    "s_and_b32 s40, s40, 0xfc\n"
    "s_getpc_b64 s[42:43]\n"
    "s_sub_u32 s40, 272, s40\n"
    "s_add_u32 s42, s42, s40\n"
    "s_addc_u32 s43, s43, 0\n"
    "s_setpc_b64 s[42:43]\n"
    ".rept 63\n"
    "s_nop 0\n"
    ".endr\n"
    "ds_read_b32 v65, v64 offset:12\n"
    "s_waitcnt lgkmcnt(0)\n"
    "v_readfirstlane_b32 s39, v65\n"
    "s_sub_u32 s38, s39, s44\n"
    "s_cmp_gt_i32 s38, 0\n"
    "s_cselect_b32 s44, s39, s44\n"
    "s_cbranch_scc1 LMN%=\n"
    "LUM%=:\n"
    "s_memtime s[40:41]\n"
    "s_waitcnt lgkmcnt(0)\n"
    "s_sub_u32 s40, s44, s40\n"
    "s_sub_u32 s41, s40, 253\n"
    "s_cmp_lt_u32 s41, 16384\n"
    "s_cbranch_scc1 LUM%=\n"
    "s_cmp_lt_u32 s40, 253\n"
    "s_cselect_b32 s40, s40, 0\n"
    "s_and_b32 s40, s40, 0xfc\n"
    "s_getpc_b64 s[42:43]\n"
    "s_sub_u32 s40, 272, s40\n"
    "s_add_u32 s42, s42, s40\n"
    "s_addc_u32 s43, s43, 0\n"
    "s_setpc_b64 s[42:43]\n"
    ".rept 63\n"
    "s_nop 0\n"
    ".endr\n"
    "s_branch LBM%=\n"
    "LW2%=:\n"
    "s_waitcnt vmcnt(2)\n"
    "v_cmp_ne_u32 vcc, 0, v116\n"
    "v_readfirstlane_b32 s70, v116\n"
    "s_nop 0\n"
    "s_andn2_b32 s38, 7, vcc_lo\n"
    "s_cbranch_scc1 LE3%=\n"
    "s_lshr_b32 s38, s70, 16\n"
    "s_cmp_gt_i32 s38, s68\n"
    "s_cbranch_scc1 LREC%=\n"
    "LE3%=:\n"
    "v_add_u32 v118, 122880, v0\n"
    "ds_read_b128 v[16:19], v14 offset:0\n"
    "ds_read_b128 v[32:35], v118 offset:0\n"
    "ds_read_b128 v[48:51], v118 offset:4096\n"
    "ds_read_b128 v[20:23], v14 offset:1024\n"
    "ds_read_b128 v[36:39], v118 offset:1024\n"
    "ds_read_b128 v[52:55], v118 offset:5120\n"
    "ds_read_b128 v[24:27], v14 offset:2048\n"
    "ds_read_b128 v[40:43], v118 offset:2048\n"
    "ds_read_b128 v[56:59], v118 offset:6144\n"
    "ds_read_b128 v[28:31], v14 offset:3072\n"
    "ds_read_b128 v[44:47], v118 offset:3072\n"
    "ds_read_b128 v[60:63], v118 offset:7168\n"
    "s_waitcnt lgkmcnt(10)\n"
    "v_lshrrev_b32 v112, 1, v0\n"
    "v_add_u32 v112, 149760, v112\n"
    "v_mov_b32 v111, 0xd0289000\n"
    "v_mfma_f32_16x16x32_bf16 v[88:91], v[32:35], v[16:19], 0\n"
    "ds_read_b128 v[64:67], v6 offset:0\n"
    "s_waitcnt lgkmcnt(10)\n"
    "v_mfma_f32_16x16x32_bf16 v[92:95], v[48:51], v[16:19], 0\n"
    "ds_read_b128 v[68:71], v6 offset:64\n"
    "s_waitcnt lgkmcnt(9)\n"
    "v_mfma_f32_16x16x32_bf16 v[88:91], v[36:39], v[20:23], v[88:91]\n"
    "ds_read_b128 v[72:75], v6 offset:128\n"
    "s_waitcnt lgkmcnt(9)\n"
    "v_mfma_f32_16x16x32_bf16 v[92:95], v[52:55], v[20:23], v[92:95]\n"
    "ds_read_b128 v[76:79], v6 offset:192\n"
    "s_waitcnt lgkmcnt(8)\n"
    "v_mfma_f32_16x16x32_bf16 v[88:91], v[40:43], v[24:27], v[88:91]\n"
    "ds_read_b64 v[80:81], v112 offset:0\n"
    "s_waitcnt lgkmcnt(8)\n"
    "v_mfma_f32_16x16x32_bf16 v[92:95], v[56:59], v[24:27], v[92:95]\n"
    "ds_read_b64 v[82:83], v112 offset:512\n"
    "s_waitcnt lgkmcnt(7)\n"
    "v_mfma_f32_16x16x32_bf16 v[88:91], v[44:47], v[28:31], v[88:91]\n"
    "ds_read_b64 v[84:85], v112 offset:1024\n"
    "s_waitcnt lgkmcnt(7)\n"
    "v_mfma_f32_16x16x32_bf16 v[92:95], v[60:63], v[28:31], v[92:95]\n"
    "ds_read_b64 v[86:87], v112 offset:1536\n"
    "v_mov_b32 v108, s72\n"
    "v_mov_b32 v109, s36\n"
    "ds_write_b32 v108, v109 offset:24\n"
    "s_waitcnt lgkmcnt(8)\n"
    "v_mfma_f32_16x16x32_bf16 v[96:99], v[64:67], v[16:19], 0\n"
    "ds_read_b128 v[32:35], v118 offset:8192\n"
    "ds_read_b128 v[36:39], v118 offset:9216\n"
    "s_waitcnt lgkmcnt(9)\n"
    "v_mfma_f32_16x16x32_bf16 v[96:99], v[68:71], v[20:23], v[96:99]\n"
    "ds_read_b128 v[40:43], v118 offset:10240\n"
    "ds_read_b128 v[44:47], v118 offset:11264\n"
    "s_waitcnt lgkmcnt(10)\n"
    "v_cndmask_b32 v88, v111, v88, s[80:81]\n"
    "v_cndmask_b32 v89, v111, v89, s[82:83]\n"
    "v_mfma_f32_16x16x32_bf16 v[96:99], v[72:75], v[24:27], v[96:99]\n"
    "ds_read_b128 v[48:51], v118 offset:12288\n"
    "ds_read_b128 v[52:55], v118 offset:13312\n"
    "s_waitcnt lgkmcnt(11)\n"
    "v_cndmask_b32 v90, v111, v90, s[84:85]\n"
    "v_cndmask_b32 v91, v111, v91, s[86:87]\n"
    "v_mfma_f32_16x16x32_bf16 v[96:99], v[76:79], v[28:31], v[96:99]\n"
    "ds_read_b128 v[56:59], v118 offset:14336\n"
    "ds_read_b128 v[60:63], v118 offset:15360\n"
    "v_cndmask_b32 v92, v111, v92, s[88:89]\n"
    "v_cndmask_b32 v93, v111, v93, s[90:91]\n"
    "v_cndmask_b32 v94, v111, v94, s[92:93]\n"
    "v_cndmask_b32 v95, v111, v95, s[94:95]\n"
    "v_max3_f32 v108, v88, v89, v90\n"
    "v_max3_f32 v109, v91, v92, v93\n"
    "v_max3_f32 v108, v108, v94, v95\n"
    "v_max3_f32 v109, v109, v96, v97\n"
    "v_max_f32 v108, v108, v109\n"
    "v_mov_b32 v109, v108\n"
    "s_nop 1\n"
    "v_permlane16_swap_b32 v108, v109\n"
    "v_max_f32 v108, v108, v109\n"
    "v_mov_b32 v109, v108\n"
    "s_nop 1\n"
    "v_permlane32_swap_b32 v108, v109\n"
    "v_max_f32 v108, v108, v109\n"
    "v_mul_f32 v108, 0xbe0293ee, v108\n"
    "v_fma_f32 v88, v88, s65, v108\n"
    "v_fma_f32 v89, v89, s65, v108\n"
    "v_fma_f32 v90, v90, s65, v108\n"
    "v_fma_f32 v91, v91, s65, v108\n"
    "v_fma_f32 v92, v92, s65, v108\n"
    "v_fma_f32 v93, v93, s65, v108\n"
    "v_fma_f32 v94, v94, s65, v108\n"
    "v_fma_f32 v95, v95, s65, v108\n"
    "v_fma_f32 v96, v96, s65, v108\n"
    "v_fma_f32 v97, v97, s65, v108\n"
    "v_exp_f32 v88, v88\n"
    "v_exp_f32 v89, v89\n"
    "v_exp_f32 v90, v90\n"
    "v_exp_f32 v91, v91\n"
    "v_exp_f32 v92, v92\n"
    "v_exp_f32 v93, v93\n"
    "v_exp_f32 v94, v94\n"
    "v_exp_f32 v95, v95\n"
    "v_exp_f32 v96, v96\n"
    "v_exp_f32 v97, v97\n"
    "v_add_f32 v108, v88, v89\n"
    "v_add_f32 v110, v90, v91\n"
    "v_add_f32 v108, v108, v92\n"
    "v_add_f32 v110, v110, v93\n"
    "v_add_f32 v108, v108, v94\n"
    "v_add_f32 v110, v110, v95\n"
    "v_add_f32 v108, v108, v96\n"
    "v_add_f32 v110, v110, v97\n"
    "v_add_f32 v108, v108, v110\n"
    "v_mov_b32 v109, v108\n"
    "s_nop 1\n"
    "v_permlane16_swap_b32 v108, v109\n"
    "v_add_f32 v108, v108, v109\n"
    "v_mov_b32 v109, v108\n"
    "s_nop 1\n"
    "v_permlane32_swap_b32 v108, v109\n"
    "v_add_f32 v108, v108, v109\n"
    "v_rcp_f32 v108, v108\n"
    "v_mov_b32 v105, 0\n"
    "v_mov_b32 v106, 0\n"
    "v_mul_f32 v88, v88, v108\n"
    "v_mul_f32 v89, v89, v108\n"
    "v_mul_f32 v90, v90, v108\n"
    "v_mul_f32 v91, v91, v108\n"
    "v_mul_f32 v92, v92, v108\n"
    "v_mul_f32 v93, v93, v108\n"
    "v_mul_f32 v94, v94, v108\n"
    "v_mul_f32 v95, v95, v108\n"
    "v_mul_f32 v96, v96, v108\n"
    "v_mul_f32 v97, v97, v108\n"
    "v_cvt_pk_bf16_f32 v100, v88, v89\n"
    "v_cvt_pk_bf16_f32 v101, v90, v91\n"
    "v_cvt_pk_bf16_f32 v102, v92, v93\n"
    "v_cvt_pk_bf16_f32 v103, v94, v95\n"
    "v_cvt_pk_bf16_f32 v104, v96, v97\n"
    "v_cvt_pk_bf16_f32 v107, v96, v97\n"
    "s_waitcnt lgkmcnt(7)\n"
    "v_mfma_f32_16x16x32_bf16 v[64:67], v[32:35], v[100:103], 0\n"
    "s_waitcnt lgkmcnt(6)\n"
    "v_mfma_f32_16x16x32_bf16 v[68:71], v[36:39], v[100:103], 0\n"
    "v_mfma_f32_16x16x16_bf16 v[64:67], v[80:81], v[104:105], v[64:67]\n"
    "v_mfma_f32_16x16x16_bf16 v[68:71], v[80:81], v[106:107], v[68:71]\n"
    "s_waitcnt lgkmcnt(5)\n"
    "v_mfma_f32_16x16x32_bf16 v[72:75], v[40:43], v[100:103], 0\n"
    "s_waitcnt lgkmcnt(4)\n"
    "v_mfma_f32_16x16x32_bf16 v[76:79], v[44:47], v[100:103], 0\n"
    "v_cvt_pk_bf16_f32 v88, v64, v65\n"
    "v_cvt_pk_bf16_f32 v89, v66, v67\n"
    "v_cvt_pk_bf16_f32 v90, v68, v69\n"
    "v_mfma_f32_16x16x16_bf16 v[72:75], v[82:83], v[104:105], v[72:75]\n"
    "v_cvt_pk_bf16_f32 v91, v70, v71\n"
    "ds_write_b128 v14, v[88:91] offset:0\n"
    "v_mfma_f32_16x16x16_bf16 v[76:79], v[82:83], v[106:107], v[76:79]\n"
    "s_waitcnt lgkmcnt(4)\n"
    "v_mfma_f32_16x16x32_bf16 v[16:19], v[48:51], v[100:103], 0\n"
    "s_waitcnt lgkmcnt(3)\n"
    "v_mfma_f32_16x16x32_bf16 v[20:23], v[52:55], v[100:103], 0\n"
    "v_cvt_pk_bf16_f32 v92, v72, v73\n"
    "v_cvt_pk_bf16_f32 v93, v74, v75\n"
    "v_cvt_pk_bf16_f32 v94, v76, v77\n"
    "v_mfma_f32_16x16x16_bf16 v[16:19], v[84:85], v[104:105], v[16:19]\n"
    "v_cvt_pk_bf16_f32 v95, v78, v79\n"
    "ds_write_b128 v14, v[92:95] offset:1024\n"
    "v_mfma_f32_16x16x16_bf16 v[20:23], v[84:85], v[106:107], v[20:23]\n"
    "s_waitcnt lgkmcnt(3)\n"
    "v_mfma_f32_16x16x32_bf16 v[24:27], v[56:59], v[100:103], 0\n"
    "s_waitcnt lgkmcnt(2)\n"
    "v_mfma_f32_16x16x32_bf16 v[28:31], v[60:63], v[100:103], 0\n"
    "v_cvt_pk_bf16_f32 v88, v16, v17\n"
    "v_cvt_pk_bf16_f32 v89, v18, v19\n"
    "v_cvt_pk_bf16_f32 v90, v20, v21\n"
    "v_mfma_f32_16x16x16_bf16 v[24:27], v[86:87], v[104:105], v[24:27]\n"
    "v_cvt_pk_bf16_f32 v91, v22, v23\n"
    "ds_write_b128 v14, v[88:91] offset:2048\n"
    "v_mfma_f32_16x16x16_bf16 v[28:31], v[86:87], v[106:107], v[28:31]\n"
    "s_nop 3\n"
    "v_cvt_pk_bf16_f32 v92, v24, v25\n"
    "v_cvt_pk_bf16_f32 v93, v26, v27\n"
    "s_nop 1\n"
    "v_cvt_pk_bf16_f32 v94, v28, v29\n"
    "v_cvt_pk_bf16_f32 v95, v30, v31\n"
    "ds_write_b128 v14, v[92:95] offset:3072\n"
    "v_cmp_ne_u32 vcc, 0, v116\n"
    "v_readfirstlane_b32 s70, v116\n"
    "s_nop 0\n"
    "s_andn2_b32 s38, 7, vcc_lo\n"
    "s_cbranch_scc0 LS3K%=\n"
    "s_mov_b32 s37, 0x80000\n"
    "LS3W%=:\n"
    "s_mov_b64 exec, 7\n"
    "buffer_load_dword v116, v0, s[16:19], s59 offen sc1\n"
    "s_mov_b64 exec, -1\n"
    "s_waitcnt vmcnt(0)\n"
    "v_cmp_ne_u32 vcc, 0, v116\n"
    "v_readfirstlane_b32 s70, v116\n"
    "s_nop 0\n"
    "s_andn2_b32 s38, 7, vcc_lo\n"
    "s_cbranch_scc0 LS3K%=\n"
    "s_sleep 2\n"
    "s_sub_u32 s37, s37, 1\n"
    "s_cbranch_scc0 LS3W%=\n"
    "s_branch LFAIL%=\n"
    "LS3K%=:\n"
    "s_lshr_b32 s38, s70, 16\n"
    "s_cmp_gt_i32 s38, s68\n"
    "s_cbranch_scc1 LREC%=\n"
    "s_add_u32 s53, s50, s55\n"
    "s_add_u32 s49, s48, s62\n"
    "s_add_u32 s51, s53, 0\n"
    "s_lshr_b32 s38, s51, 8\n"
    "s_xor_b32 s38, s38, s56\n"
    "s_and_b32 s38, s38, 0x2000\n"
    "s_or_b32 s51, s51, s38\n"
    "s_add_u32 s52, s53, 524288\n"
    "s_lshr_b32 s38, s52, 8\n"
    "s_xor_b32 s38, s38, s56\n"
    "s_and_b32 s38, s38, 0x2000\n"
    "s_or_b32 s52, s52, s38\n"
    "s_add_u32 s53, s53, 1048576\n"
    "s_lshr_b32 s38, s53, 8\n"
    "s_xor_b32 s38, s38, s56\n"
    "s_and_b32 s38, s38, 0x2000\n"
    "s_or_b32 s53, s53, s38\n"
    "s_mov_b32 s32, 0\n"
    "s_xor_b32 s46, s46, 0x80000\n"
    "s_add_u32 s47, s46, 4096\n"
    "s_bfe_u32 s64, s36, 0x10001\n"
    "s_add_u32 s44, s44, s8\n"
    "s_add_u32 s59, s59, 128\n"
    "s_branch LBM%=\n"
    "LREC%=:\n"
    "s_mov_b32 s68, s38\n"
    "s_andn2_b32 s39, 31, s70\n"
    "s_add_u32 s53, s50, s55\n"
    "s_add_u32 s49, s48, s62\n"
    "s_add_u32 s51, s53, 0\n"
    "s_lshr_b32 s38, s51, 8\n"
    "s_xor_b32 s38, s38, s56\n"
    "s_and_b32 s38, s38, 0x2000\n"
    "s_or_b32 s51, s51, s38\n"
    "s_add_u32 s52, s53, 524288\n"
    "s_lshr_b32 s38, s52, 8\n"
    "s_xor_b32 s38, s38, s56\n"
    "s_and_b32 s38, s38, 0x2000\n"
    "s_or_b32 s52, s52, s38\n"
    "s_add_u32 s53, s53, 1048576\n"
    "s_lshr_b32 s38, s53, 8\n"
    "s_xor_b32 s38, s38, s56\n"
    "s_and_b32 s38, s38, 0x2000\n"
    "s_or_b32 s53, s53, s38\n"
    "s_mov_b32 s32, 1\n"
    "s_xor_b32 s46, s46, 0x80000\n"
    "s_add_u32 s47, s46, 4096\n"
    "s_bfe_u32 s64, s36, 0x10001\n"
    "s_add_u32 s44, s44, s13\n"
    "s_add_u32 s59, s59, 128\n"
    "s_lshl_b32 s49, s36, 14\n"
    "s_add_u32 s49, s49, s67\n"
    "s_add_u32 s49, s49, s62\n"
    "s_mov_b32 s54, 0x80000000\n"
    "s_andn2_b32 s70, s36, 7\n"
    "s_add_u32 s70, s70, s36\n"
    "s_lshl_b32 s70, s70, 10\n"
    "s_add_u32 s70, s70, s69\n"
    "s_nop 7\n"
    "s_waitcnt lgkmcnt(0)\n"
    "buffer_load_dwordx4 v[16:19], v0, s[16:19], s70 offen sc1\n"
    "s_barrier\n"
    "s_add_u32 m0, s62, 122880\n"
    "s_nop 0\n"
    "buffer_load_dwordx4 v0, s[16:19], s51 offen offset:0 sc1 lds\n"
    "buffer_load_dwordx4 v0, s[16:19], s51 offen offset:1024 sc1 lds\n"
    "s_add_u32 m0, s62, 131072\n"
    "s_nop 0\n"
    "buffer_load_dwordx4 v0, s[16:19], s52 offen offset:0 sc1 lds\n"
    "buffer_load_dwordx4 v0, s[16:19], s52 offen offset:1024 sc1 lds\n"
    "s_add_u32 m0, s62, 139264\n"
    "s_nop 0\n"
    "buffer_load_dwordx4 v0, s[16:19], s53 offen offset:0 sc1 lds\n"
    "buffer_load_dwordx4 v0, s[16:19], s53 offen offset:1024 sc1 lds\n"
    "ds_read_b128 v[64:67], v7 offset:0\n"
    "ds_read_b128 v[68:71], v7 offset:1024\n"
    "ds_read_b128 v[72:75], v7 offset:2048\n"
    "ds_read_b128 v[76:79], v7 offset:3072\n"
    "ds_read_b128 v[80:83], v7 offset:4096\n"
    "ds_read_b128 v[84:87], v7 offset:5120\n"
    "ds_read_b128 v[88:91], v7 offset:6144\n"
    "ds_read_b128 v[92:95], v7 offset:7168\n"
    "ds_read_b128 v[96:99], v7 offset:8192\n"
    "ds_read_b128 v[100:103], v7 offset:9216\n"
    "ds_read_b128 v[104:107], v7 offset:10240\n"
    "ds_read_b128 v[108:111], v7 offset:11264\n"
    "v_mov_b64 v[114:115], 0\n"
    "s_nop 1\n"
    "v_mfma_f32_32x32x8_bf16 v[48:63], v[114:115], v[114:115], 0\n"
    "v_mov_b32 v1, 0x4444\n"
    "s_mov_b32 vcc_lo, 0xaaaaaaaa\n"
    "s_mov_b32 vcc_hi, 0xaaaaaaaa\n"
    "s_mov_b64 exec, vcc\n"
    "v_mov_b32 v1, 0xeeee\n"
    "s_mov_b64 exec, -1\n"
    "v_lshrrev_b32 v112, 3, v0\n"
    "v_and_b32 v112, 0x58, v112\n"
    "v_add_u32 v112, s62, v112\n"
    "v_add_u32 v112, 147456, v112\n"
    "s_mov_b32 s37, 0x80000\n"
    "s_waitcnt vmcnt(6)\n"
    "LXW%=:\n"
    "v_max3_u32 v36, v16, v17, v18\n"
    "v_max_u32 v36, v36, v19\n"
    "v_cmp_eq_u32 vcc, -1, v36\n"
    "s_cbranch_vccz LXK%=\n"
    "s_sleep 2\n"
    "buffer_load_dwordx4 v[16:19], v0, s[16:19], s70 offen sc1\n"
    "s_waitcnt vmcnt(0)\n"
    "s_sub_u32 s37, s37, 1\n"
    "s_cbranch_scc0 LXW%=\n"
    "s_branch LFAIL%=\n"
    "LXK%=:\n"
    "v_lshlrev_b32 v20, 16, v16\n"
    "v_and_b32 v21, 0xffff0000, v16\n"
    "v_readfirstlane_b32 s40, v20\n"
    "v_lshlrev_b32 v22, 16, v17\n"
    "v_and_b32 v23, 0xffff0000, v17\n"
    "v_lshlrev_b32 v24, 16, v18\n"
    "v_and_b32 v25, 0xffff0000, v18\n"
    "v_lshlrev_b32 v26, 16, v19\n"
    "v_and_b32 v27, 0xffff0000, v19\n"
    "v_pk_add_f32 v[28:29], v[20:21], s[40:41] op_sel_hi:[1,0] neg_lo:[0,1] neg_hi:[0,1]\n"
    "v_pk_add_f32 v[30:31], v[22:23], s[40:41] op_sel_hi:[1,0] neg_lo:[0,1] neg_hi:[0,1]\n"
    "v_pk_add_f32 v[32:33], v[24:25], s[40:41] op_sel_hi:[1,0] neg_lo:[0,1] neg_hi:[0,1]\n"
    "v_pk_add_f32 v[34:35], v[26:27], s[40:41] op_sel_hi:[1,0] neg_lo:[0,1] neg_hi:[0,1]\n"
    "v_pk_add_f32 v[36:37], v[28:29], v[30:31]\n"
    "v_pk_mul_f32 v[38:39], v[28:29], v[28:29]\n"
    "v_pk_add_f32 v[40:41], v[32:33], v[34:35]\n"
    "v_pk_fma_f32 v[38:39], v[30:31], v[30:31], v[38:39]\n"
    "v_pk_add_f32 v[36:37], v[36:37], v[40:41]\n"
    "v_pk_fma_f32 v[38:39], v[32:33], v[32:33], v[38:39]\n"
    "v_add_f32 v36, v36, v37\n"
    "v_pk_fma_f32 v[38:39], v[34:35], v[34:35], v[38:39]\n"
    "v_mov_b32 v41, 0x3b000000\n"
    "v_add_f32 v37, v38, v39\n"
    "v_add_f32_dpp v36, v36, v36 row_ror:8 row_mask:0xf bank_mask:0xf\n"
    "s_nop 0\n"
    "v_add_f32_dpp v37, v37, v37 row_ror:8 row_mask:0xf bank_mask:0xf\n"
    "v_add_f32_dpp v36, v36, v36 row_ror:4 row_mask:0xf bank_mask:0xf\n"
    "s_nop 0\n"
    "v_add_f32_dpp v37, v37, v37 row_ror:4 row_mask:0xf bank_mask:0xf\n"
    "v_add_f32_dpp v36, v36, v36 row_ror:2 row_mask:0xf bank_mask:0xf\n"
    "s_nop 0\n"
    "v_add_f32_dpp v37, v37, v37 row_ror:2 row_mask:0xf bank_mask:0xf\n"
    "v_add_f32_dpp v36, v36, v36 row_ror:1 row_mask:0xf bank_mask:0xf\n"
    "s_nop 0\n"
    "v_add_f32_dpp v37, v37, v37 row_ror:1 row_mask:0xf bank_mask:0xf\n"
    "s_nop 1\n"
    "v_permlane16_swap_b32 v36, v37\n"
    "v_add_f32 v36, v36, v37\n"
    "v_mov_b32 v37, v36\n"
    "s_nop 1\n"
    "v_permlane32_swap_b32 v36, v37\n"
    "v_add_f32 v36, v36, v37\n"
    "s_nop 0\n"
    "v_readlane_b32 s42, v36, 0\n"
    "v_readlane_b32 s43, v36, 16\n"
    "s_nop 0\n"
    "v_mul_f32 v36, s42, v41\n"
    "v_mul_f32 v37, s43, v41\n"
    "v_fma_f32 v37, -v36, v36, v37\n"
    "v_add_f32 v40, s40, v36\n"
    "v_max_f32 v37, 0, v37\n"
    "v_cvt_pk_bf16_f32 v36, v40, v37\n"
    "v_and_b32 v37, 0xffff0000, v36\n"
    "v_add_f32 v37, 0x3727c5ac, v37\n"
    "v_cvt_pk_bf16_f32 v37, v37, v37\n"
    "v_lshlrev_b32 v37, 16, v37\n"
    "v_sqrt_f32 v38, v37\n"
    "v_lshlrev_b32 v36, 16, v36\n"
    "v_cvt_pk_bf16_f32 v38, v38, v38\n"
    "v_lshlrev_b32 v38, 16, v38\n"
    "v_rcp_f32 v38, v38\n"
    "v_pk_add_f32 v[28:29], v[20:21], v[36:37] op_sel_hi:[1,0] neg_lo:[0,1] neg_hi:[0,1]\n"
    "v_pk_add_f32 v[30:31], v[22:23], v[36:37] op_sel_hi:[1,0] neg_lo:[0,1] neg_hi:[0,1]\n"
    "v_pk_add_f32 v[32:33], v[24:25], v[36:37] op_sel_hi:[1,0] neg_lo:[0,1] neg_hi:[0,1]\n"
    "v_pk_add_f32 v[34:35], v[26:27], v[36:37] op_sel_hi:[1,0] neg_lo:[0,1] neg_hi:[0,1]\n"
    "v_pk_mul_f32 v[28:29], v[28:29], v[38:39] op_sel_hi:[1,0]\n"
    "v_pk_mul_f32 v[30:31], v[30:31], v[38:39] op_sel_hi:[1,0]\n"
    "v_pk_mul_f32 v[32:33], v[32:33], v[38:39] op_sel_hi:[1,0]\n"
    "v_pk_mul_f32 v[34:35], v[34:35], v[38:39] op_sel_hi:[1,0]\n"
    "v_cvt_pk_bf16_f32 v116, v28, v29\n"
    "v_cvt_pk_bf16_f32 v117, v30, v31\n"
    "v_cvt_pk_bf16_f32 v118, v32, v33\n"
    "v_cvt_pk_bf16_f32 v119, v34, v35\n"
    "ds_write_b128 v9, v[116:119] offset:24576\n"
    "ds_read_b64_tr_b16 v[16:17], v112 offset:0\n"
    "ds_read_b64_tr_b16 v[18:19], v112 offset:32\n"
    "s_waitcnt lgkmcnt(11)\n"
    "ds_read_b64_tr_b16 v[20:21], v112 offset:128\n"
    "ds_read_b64_tr_b16 v[22:23], v112 offset:160\n"
    "ds_read_b64_tr_b16 v[24:25], v112 offset:256\n"
    "ds_read_b64_tr_b16 v[26:27], v112 offset:288\n"
    "s_waitcnt lgkmcnt(11)\n"
    "ds_read_b64_tr_b16 v[28:29], v112 offset:384\n"
    "ds_read_b64_tr_b16 v[30:31], v112 offset:416\n"
    "ds_read_b64_tr_b16 v[32:33], v112 offset:512\n"
    "ds_read_b64_tr_b16 v[34:35], v112 offset:544\n"
    "s_waitcnt lgkmcnt(11)\n"
    "ds_read_b64_tr_b16 v[36:37], v112 offset:640\n"
    "ds_read_b64_tr_b16 v[38:39], v112 offset:672\n"
    "ds_read_b64_tr_b16 v[40:41], v112 offset:768\n"
    "ds_read_b64_tr_b16 v[42:43], v112 offset:800\n"
    "s_waitcnt lgkmcnt(11)\n"
    "ds_read_b64_tr_b16 v[44:45], v112 offset:896\n"
    "ds_read_b64_tr_b16 v[46:47], v112 offset:928\n"
    "s_waitcnt lgkmcnt(0)\n"
    "s_mov_b32 exec_lo, 0x3333cccc\n"
    "s_mov_b32 exec_hi, 0x3333cccc\n"
    "v_mov_b64 v[16:17], 0\n"
    "v_mov_b64 v[18:19], 0\n"
    "v_mov_b64 v[20:21], 0\n"
    "v_mov_b64 v[22:23], 0\n"
    "v_mov_b64 v[24:25], 0\n"
    "v_mov_b64 v[26:27], 0\n"
    "v_mov_b64 v[28:29], 0\n"
    "v_mov_b64 v[30:31], 0\n"
    "v_mov_b64 v[32:33], 0\n"
    "v_mov_b64 v[34:35], 0\n"
    "v_mov_b64 v[36:37], 0\n"
    "v_mov_b64 v[38:39], 0\n"
    "v_mov_b64 v[40:41], 0\n"
    "v_mov_b64 v[42:43], 0\n"
    "v_mov_b64 v[44:45], 0\n"
    "v_mov_b64 v[46:47], 0\n"
    "s_mov_b64 exec, -1\n"
    "v_mov_b64 v[112:113], 0\n"
    "v_mov_b64 v[114:115], 0\n"
    "v_mov_b64 v[116:117], 0\n"
    "v_mov_b64 v[118:119], 0\n"
    "v_smfmac_f32_16x16x64_bf16 v[48:51], v[16:19], v[64:71], v1\n"
    "v_smfmac_f32_16x16x64_bf16 v[52:55], v[16:19], v[72:79], v1\n"
    "v_smfmac_f32_16x16x64_bf16 v[48:51], v[20:23], v[80:87], v1\n"
    "v_smfmac_f32_16x16x64_bf16 v[52:55], v[20:23], v[88:95], v1\n"
    "v_smfmac_f32_16x16x64_bf16 v[48:51], v[24:27], v[96:103], v1\n"
    "ds_read_b128 v[64:67], v7 offset:12288\n"
    "v_smfmac_f32_16x16x64_bf16 v[52:55], v[24:27], v[104:111], v1\n"
    "ds_read_b128 v[68:71], v7 offset:13312\n"
    "v_smfmac_f32_16x16x64_bf16 v[48:51], v[28:31], a[0:7], v1\n"
    "ds_read_b128 v[72:75], v7 offset:14336\n"
    "v_smfmac_f32_16x16x64_bf16 v[52:55], v[28:31], a[8:15], v1\n"
    "ds_read_b128 v[76:79], v7 offset:15360\n"
    "v_smfmac_f32_16x16x64_bf16 v[48:51], v[32:35], a[16:23], v1\n"
    "ds_read_b128 v[80:83], v7 offset:16384\n"
    "v_smfmac_f32_16x16x64_bf16 v[52:55], v[32:35], a[24:31], v1\n"
    "ds_read_b128 v[84:87], v7 offset:17408\n"
    "v_smfmac_f32_16x16x64_bf16 v[48:51], v[36:39], a[32:39], v1\n"
    "ds_read_b128 v[88:91], v7 offset:18432\n"
    "v_smfmac_f32_16x16x64_bf16 v[52:55], v[36:39], a[40:47], v1\n"
    "ds_read_b128 v[92:95], v7 offset:19456\n"
    "v_smfmac_f32_16x16x64_bf16 v[48:51], v[40:43], a[48:55], v1\n"
    "v_smfmac_f32_16x16x64_bf16 v[52:55], v[40:43], a[56:63], v1\n"
    "v_smfmac_f32_16x16x64_bf16 v[48:51], v[44:47], a[64:71], v1\n"
    "v_smfmac_f32_16x16x64_bf16 v[52:55], v[44:47], a[72:79], v1\n"
    "v_smfmac_f32_16x16x64_bf16 v[112:115], v[16:19], a[176:183], v1\n"
    "v_smfmac_f32_16x16x64_bf16 v[116:119], v[16:19], a[184:191], v1\n"
    "v_smfmac_f32_16x16x64_bf16 v[112:115], v[20:23], a[192:199], v1\n"
    "v_smfmac_f32_16x16x64_bf16 v[116:119], v[20:23], a[200:207], v1\n"
    "v_smfmac_f32_16x16x64_bf16 v[112:115], v[24:27], a[208:215], v1\n"
    "v_smfmac_f32_16x16x64_bf16 v[116:119], v[24:27], a[216:223], v1\n"
    "v_smfmac_f32_16x16x64_bf16 v[112:115], v[28:31], a[224:231], v1\n"
    "v_smfmac_f32_16x16x64_bf16 v[116:119], v[28:31], a[232:239], v1\n"
    "v_smfmac_f32_16x16x64_bf16 v[112:115], v[32:35], a[240:247], v1\n"
    "v_smfmac_f32_16x16x64_bf16 v[116:119], v[32:35], a[248:255], v1\n"
    "v_smfmac_f32_16x16x64_bf16 v[112:115], v[36:39], v[120:127], v1\n"
    "v_smfmac_f32_16x16x64_bf16 v[116:119], v[36:39], v[128:135], v1\n"
    "v_smfmac_f32_16x16x64_bf16 v[112:115], v[40:43], v[136:143], v1\n"
    "v_smfmac_f32_16x16x64_bf16 v[116:119], v[40:43], v[144:151], v1\n"
    "v_smfmac_f32_16x16x64_bf16 v[112:115], v[44:47], v[152:159], v1\n"
    "v_smfmac_f32_16x16x64_bf16 v[116:119], v[44:47], v[160:167], v1\n"
    "s_waitcnt lgkmcnt(6)\n"
    "v_smfmac_f32_16x16x64_bf16 v[56:59], v[16:19], v[64:71], v1\n"
    "s_waitcnt lgkmcnt(4)\n"
    "v_smfmac_f32_16x16x64_bf16 v[60:63], v[16:19], v[72:79], v1\n"
    "s_waitcnt lgkmcnt(2)\n"
    "v_smfmac_f32_16x16x64_bf16 v[56:59], v[20:23], v[80:87], v1\n"
    "s_waitcnt lgkmcnt(0)\n"
    "v_smfmac_f32_16x16x64_bf16 v[60:63], v[20:23], v[88:95], v1\n"
    "v_smfmac_f32_16x16x64_bf16 v[56:59], v[24:27], a[80:87], v1\n"
    "v_smfmac_f32_16x16x64_bf16 v[60:63], v[24:27], a[88:95], v1\n"
    "v_smfmac_f32_16x16x64_bf16 v[56:59], v[28:31], a[96:103], v1\n"
    "v_smfmac_f32_16x16x64_bf16 v[60:63], v[28:31], a[104:111], v1\n"
    "v_smfmac_f32_16x16x64_bf16 v[56:59], v[32:35], a[112:119], v1\n"
    "v_smfmac_f32_16x16x64_bf16 v[60:63], v[32:35], a[120:127], v1\n"
    "v_smfmac_f32_16x16x64_bf16 v[56:59], v[36:39], a[128:135], v1\n"
    "v_smfmac_f32_16x16x64_bf16 v[60:63], v[36:39], a[136:143], v1\n"
    "v_smfmac_f32_16x16x64_bf16 v[56:59], v[40:43], a[144:151], v1\n"
    "v_smfmac_f32_16x16x64_bf16 v[60:63], v[40:43], a[152:159], v1\n"
    "v_smfmac_f32_16x16x64_bf16 v[56:59], v[44:47], a[160:167], v1\n"
    "v_smfmac_f32_16x16x64_bf16 v[60:63], v[44:47], a[168:175], v1\n"
    "v_lshrrev_b32 v104, 2, v0\n"
    "v_and_b32 v104, 12, v104\n"
    "v_lshlrev_b32 v105, 2, v0\n"
    "v_and_b32 v105, 0x300, v105\n"
    "v_or_b32 v104, v104, v105\n"
    "s_lshr_b32 s40, s39, 4\n"
    "s_lshl_b32 s41, s40, 12\n"
    "s_and_b32 s42, s39, 15\n"
    "s_lshl_b32 s42, s42, 4\n"
    "s_add_u32 s41, s41, s42\n"
    "s_lshr_b32 s42, s62, 1\n"
    "s_add_u32 s41, s41, s42\n"
    "s_add_u32 s41, s41, 122880\n"
    "v_add_u32 v104, s41, v104\n"
    "s_lshl_b32 s40, s40, 3\n"
    "s_and_b32 s41, s39, 3\n"
    "s_lshl_b32 s41, s41, 1\n"
    "s_add_u32 s40, s40, s41\n"
    "s_bfe_u32 s41, s39, 0x20002\n"
    "s_lshl_b32 s41, s41, 8\n"
    "s_add_u32 s40, s40, s41\n"
    "s_add_u32 s40, s40, s62\n"
    "s_add_u32 s40, s40, 131072\n"
    "v_and_b32 v105, 0xf0, v0\n"
    "v_add_u32 v105, s40, v105\n"
    "v_add_f32 v48, v48, v49\n"
    "v_add_f32 v50, v50, v51\n"
    "v_add_f32 v48, v48, v50\n"
    "v_add_f32 v52, v52, v53\n"
    "v_add_f32 v54, v54, v55\n"
    "v_add_f32 v52, v52, v54\n"
    "v_cvt_pk_bf16_f32 v48, v48, v52\n"
    "v_add_f32 v112, v112, v113\n"
    "v_add_f32 v114, v114, v115\n"
    "v_add_f32 v112, v112, v114\n"
    "v_add_f32 v116, v116, v117\n"
    "v_add_f32 v118, v118, v119\n"
    "v_add_f32 v116, v116, v118\n"
    "v_cvt_pk_bf16_f32 v112, v112, v116\n"
    "v_add_f32 v56, v56, v57\n"
    "v_add_f32 v58, v58, v59\n"
    "v_add_f32 v56, v56, v58\n"
    "v_add_f32 v60, v60, v61\n"
    "v_add_f32 v62, v62, v63\n"
    "v_add_f32 v60, v60, v62\n"
    "v_cvt_pk_bf16_f32 v56, v56, v60\n"
    "v_mov_b32 v1, 0x4444\n"
    "s_mov_b32 vcc_lo, 0xcccccccc\n"
    "s_mov_b32 vcc_hi, 0xcccccccc\n"
    "s_mov_b64 exec, vcc\n"
    "v_mov_b32 v1, 0xeeee\n"
    "s_mov_b64 exec, -1\n"
    "s_waitcnt vmcnt(0)\n"
    "s_barrier\n"
    "ds_write_b32 v104, v56\n"
    "ds_write_b32 v104, v48 offset:16384\n"
    "ds_write_b16 v105, v112\n"
    "ds_write_b16_d16_hi v105, v112 offset:1024\n"
    "s_cmp_lg_u32 s61, 1\n"
    "s_cbranch_scc1 LBM%=\n"
    "v_mov_b32 v112, 158224\n"
    "v_mov_b32 v113, s44\n"
    "ds_write_b32 v112, v113\n"
    "s_waitcnt lgkmcnt(0)\n"
    "s_branch LMO%=\n"
    "LEND%=:\n"
    "s_waitcnt lgkmcnt(0)\n"
    "s_cmp_lg_u32 s61, 1\n"
    "s_cbranch_scc1 LE0%=\n"
    "buffer_store_dword v1, off, s[16:19], s58 sc1\n"
    "s_add_u32 s58, s58, 4\n"
    "s_bitset0_b32 s58, 31\n"
    "LE0%=:\n"
    "s_add_u32 s54, s50, s57\n"
    "s_lshr_b32 s38, s54, 8\n"
    "s_xor_b32 s38, s38, s74\n"
    "s_and_b32 s38, s38, 0x2000\n"
    "s_or_b32 s54, s54, s38\n"
    "s_cmp_lt_u32 s61, 2\n"
    "s_cbranch_scc1 LE1%=\n"
    "v_add_u32 v118, 122880, v0\n"
    "ds_read_b128 v[16:19], v14 offset:0\n"
    "ds_read_b128 v[32:35], v118 offset:0\n"
    "ds_read_b128 v[48:51], v118 offset:4096\n"
    "ds_read_b128 v[20:23], v14 offset:1024\n"
    "ds_read_b128 v[36:39], v118 offset:1024\n"
    "ds_read_b128 v[52:55], v118 offset:5120\n"
    "ds_read_b128 v[24:27], v14 offset:2048\n"
    "ds_read_b128 v[40:43], v118 offset:2048\n"
    "ds_read_b128 v[56:59], v118 offset:6144\n"
    "ds_read_b128 v[28:31], v14 offset:3072\n"
    "ds_read_b128 v[44:47], v118 offset:3072\n"
    "ds_read_b128 v[60:63], v118 offset:7168\n"
    "s_waitcnt lgkmcnt(10)\n"
    "v_lshrrev_b32 v112, 1, v0\n"
    "v_add_u32 v112, 149760, v112\n"
    "v_mov_b32 v111, 0xd0289000\n"
    "v_mfma_f32_16x16x32_bf16 v[88:91], v[32:35], v[16:19], 0\n"
    "ds_read_b128 v[64:67], v6 offset:0\n"
    "s_waitcnt lgkmcnt(10)\n"
    "v_mfma_f32_16x16x32_bf16 v[92:95], v[48:51], v[16:19], 0\n"
    "ds_read_b128 v[68:71], v6 offset:64\n"
    "s_waitcnt lgkmcnt(9)\n"
    "v_mfma_f32_16x16x32_bf16 v[88:91], v[36:39], v[20:23], v[88:91]\n"
    "ds_read_b128 v[72:75], v6 offset:128\n"
    "s_waitcnt lgkmcnt(9)\n"
    "v_mfma_f32_16x16x32_bf16 v[92:95], v[52:55], v[20:23], v[92:95]\n"
    "ds_read_b128 v[76:79], v6 offset:192\n"
    "s_waitcnt lgkmcnt(8)\n"
    "v_mfma_f32_16x16x32_bf16 v[88:91], v[40:43], v[24:27], v[88:91]\n"
    "ds_read_b64 v[80:81], v112 offset:0\n"
    "s_waitcnt lgkmcnt(8)\n"
    "v_mfma_f32_16x16x32_bf16 v[92:95], v[56:59], v[24:27], v[92:95]\n"
    "ds_read_b64 v[82:83], v112 offset:512\n"
    "s_waitcnt lgkmcnt(7)\n"
    "v_mfma_f32_16x16x32_bf16 v[88:91], v[44:47], v[28:31], v[88:91]\n"
    "ds_read_b64 v[84:85], v112 offset:1024\n"
    "s_waitcnt lgkmcnt(7)\n"
    "v_mfma_f32_16x16x32_bf16 v[92:95], v[60:63], v[28:31], v[92:95]\n"
    "ds_read_b64 v[86:87], v112 offset:1536\n"
    "v_mov_b32 v108, s72\n"
    "v_mov_b32 v109, s36\n"
    "ds_write_b32 v108, v109 offset:24\n"
    "s_waitcnt lgkmcnt(8)\n"
    "v_mfma_f32_16x16x32_bf16 v[96:99], v[64:67], v[16:19], 0\n"
    "ds_read_b128 v[32:35], v118 offset:8192\n"
    "ds_read_b128 v[36:39], v118 offset:9216\n"
    "s_waitcnt lgkmcnt(9)\n"
    "v_mfma_f32_16x16x32_bf16 v[96:99], v[68:71], v[20:23], v[96:99]\n"
    "ds_read_b128 v[40:43], v118 offset:10240\n"
    "ds_read_b128 v[44:47], v118 offset:11264\n"
    "s_waitcnt lgkmcnt(10)\n"
    "v_cndmask_b32 v88, v111, v88, s[80:81]\n"
    "v_cndmask_b32 v89, v111, v89, s[82:83]\n"
    "v_mfma_f32_16x16x32_bf16 v[96:99], v[72:75], v[24:27], v[96:99]\n"
    "ds_read_b128 v[48:51], v118 offset:12288\n"
    "ds_read_b128 v[52:55], v118 offset:13312\n"
    "s_waitcnt lgkmcnt(11)\n"
    "v_cndmask_b32 v90, v111, v90, s[84:85]\n"
    "v_cndmask_b32 v91, v111, v91, s[86:87]\n"
    "v_mfma_f32_16x16x32_bf16 v[96:99], v[76:79], v[28:31], v[96:99]\n"
    "ds_read_b128 v[56:59], v118 offset:14336\n"
    "ds_read_b128 v[60:63], v118 offset:15360\n"
    "v_cndmask_b32 v92, v111, v92, s[88:89]\n"
    "v_cndmask_b32 v93, v111, v93, s[90:91]\n"
    "v_cndmask_b32 v94, v111, v94, s[92:93]\n"
    "v_cndmask_b32 v95, v111, v95, s[94:95]\n"
    "v_max3_f32 v108, v88, v89, v90\n"
    "v_max3_f32 v109, v91, v92, v93\n"
    "v_max3_f32 v108, v108, v94, v95\n"
    "v_max3_f32 v109, v109, v96, v97\n"
    "v_max_f32 v108, v108, v109\n"
    "v_mov_b32 v109, v108\n"
    "s_nop 1\n"
    "v_permlane16_swap_b32 v108, v109\n"
    "v_max_f32 v108, v108, v109\n"
    "v_mov_b32 v109, v108\n"
    "s_nop 1\n"
    "v_permlane32_swap_b32 v108, v109\n"
    "v_max_f32 v108, v108, v109\n"
    "v_mul_f32 v108, 0xbe0293ee, v108\n"
    "v_fma_f32 v88, v88, s65, v108\n"
    "v_fma_f32 v89, v89, s65, v108\n"
    "v_fma_f32 v90, v90, s65, v108\n"
    "v_fma_f32 v91, v91, s65, v108\n"
    "v_fma_f32 v92, v92, s65, v108\n"
    "v_fma_f32 v93, v93, s65, v108\n"
    "v_fma_f32 v94, v94, s65, v108\n"
    "v_fma_f32 v95, v95, s65, v108\n"
    "v_fma_f32 v96, v96, s65, v108\n"
    "v_fma_f32 v97, v97, s65, v108\n"
    "v_exp_f32 v88, v88\n"
    "v_exp_f32 v89, v89\n"
    "v_exp_f32 v90, v90\n"
    "v_exp_f32 v91, v91\n"
    "v_exp_f32 v92, v92\n"
    "v_exp_f32 v93, v93\n"
    "v_exp_f32 v94, v94\n"
    "v_exp_f32 v95, v95\n"
    "v_exp_f32 v96, v96\n"
    "v_exp_f32 v97, v97\n"
    "v_add_f32 v108, v88, v89\n"
    "v_add_f32 v110, v90, v91\n"
    "v_add_f32 v108, v108, v92\n"
    "v_add_f32 v110, v110, v93\n"
    "v_add_f32 v108, v108, v94\n"
    "v_add_f32 v110, v110, v95\n"
    "v_add_f32 v108, v108, v96\n"
    "v_add_f32 v110, v110, v97\n"
    "v_add_f32 v108, v108, v110\n"
    "v_mov_b32 v109, v108\n"
    "s_nop 1\n"
    "v_permlane16_swap_b32 v108, v109\n"
    "v_add_f32 v108, v108, v109\n"
    "v_mov_b32 v109, v108\n"
    "s_nop 1\n"
    "v_permlane32_swap_b32 v108, v109\n"
    "v_add_f32 v108, v108, v109\n"
    "v_rcp_f32 v108, v108\n"
    "v_mov_b32 v105, 0\n"
    "v_mov_b32 v106, 0\n"
    "v_mul_f32 v88, v88, v108\n"
    "v_mul_f32 v89, v89, v108\n"
    "v_mul_f32 v90, v90, v108\n"
    "v_mul_f32 v91, v91, v108\n"
    "v_mul_f32 v92, v92, v108\n"
    "v_mul_f32 v93, v93, v108\n"
    "v_mul_f32 v94, v94, v108\n"
    "v_mul_f32 v95, v95, v108\n"
    "v_mul_f32 v96, v96, v108\n"
    "v_mul_f32 v97, v97, v108\n"
    "v_cvt_pk_bf16_f32 v100, v88, v89\n"
    "v_cvt_pk_bf16_f32 v101, v90, v91\n"
    "v_cvt_pk_bf16_f32 v102, v92, v93\n"
    "v_cvt_pk_bf16_f32 v103, v94, v95\n"
    "v_cvt_pk_bf16_f32 v104, v96, v97\n"
    "v_cvt_pk_bf16_f32 v107, v96, v97\n"
    "s_waitcnt lgkmcnt(7)\n"
    "v_mfma_f32_16x16x32_bf16 v[64:67], v[32:35], v[100:103], 0\n"
    "s_waitcnt lgkmcnt(6)\n"
    "v_mfma_f32_16x16x32_bf16 v[68:71], v[36:39], v[100:103], 0\n"
    "v_mfma_f32_16x16x16_bf16 v[64:67], v[80:81], v[104:105], v[64:67]\n"
    "v_mfma_f32_16x16x16_bf16 v[68:71], v[80:81], v[106:107], v[68:71]\n"
    "s_waitcnt lgkmcnt(5)\n"
    "v_mfma_f32_16x16x32_bf16 v[72:75], v[40:43], v[100:103], 0\n"
    "s_waitcnt lgkmcnt(4)\n"
    "v_mfma_f32_16x16x32_bf16 v[76:79], v[44:47], v[100:103], 0\n"
    "v_cvt_pk_bf16_f32 v88, v64, v65\n"
    "v_cvt_pk_bf16_f32 v89, v66, v67\n"
    "v_cvt_pk_bf16_f32 v90, v68, v69\n"
    "v_mfma_f32_16x16x16_bf16 v[72:75], v[82:83], v[104:105], v[72:75]\n"
    "v_cvt_pk_bf16_f32 v91, v70, v71\n"
    "ds_write_b128 v14, v[88:91] offset:0\n"
    "v_mfma_f32_16x16x16_bf16 v[76:79], v[82:83], v[106:107], v[76:79]\n"
    "s_waitcnt lgkmcnt(4)\n"
    "v_mfma_f32_16x16x32_bf16 v[16:19], v[48:51], v[100:103], 0\n"
    "s_waitcnt lgkmcnt(3)\n"
    "v_mfma_f32_16x16x32_bf16 v[20:23], v[52:55], v[100:103], 0\n"
    "v_cvt_pk_bf16_f32 v92, v72, v73\n"
    "v_cvt_pk_bf16_f32 v93, v74, v75\n"
    "v_cvt_pk_bf16_f32 v94, v76, v77\n"
    "v_mfma_f32_16x16x16_bf16 v[16:19], v[84:85], v[104:105], v[16:19]\n"
    "v_cvt_pk_bf16_f32 v95, v78, v79\n"
    "ds_write_b128 v14, v[92:95] offset:1024\n"
    "v_mfma_f32_16x16x16_bf16 v[20:23], v[84:85], v[106:107], v[20:23]\n"
    "s_waitcnt lgkmcnt(3)\n"
    "v_mfma_f32_16x16x32_bf16 v[24:27], v[56:59], v[100:103], 0\n"
    "s_waitcnt lgkmcnt(2)\n"
    "v_mfma_f32_16x16x32_bf16 v[28:31], v[60:63], v[100:103], 0\n"
    "v_cvt_pk_bf16_f32 v88, v16, v17\n"
    "v_cvt_pk_bf16_f32 v89, v18, v19\n"
    "v_cvt_pk_bf16_f32 v90, v20, v21\n"
    "v_mfma_f32_16x16x16_bf16 v[24:27], v[86:87], v[104:105], v[24:27]\n"
    "v_cvt_pk_bf16_f32 v91, v22, v23\n"
    "ds_write_b128 v14, v[88:91] offset:2048\n"
    "v_mfma_f32_16x16x16_bf16 v[28:31], v[86:87], v[106:107], v[28:31]\n"
    "s_nop 3\n"
    "v_cvt_pk_bf16_f32 v92, v24, v25\n"
    "v_cvt_pk_bf16_f32 v93, v26, v27\n"
    "s_nop 1\n"
    "v_cvt_pk_bf16_f32 v94, v28, v29\n"
    "v_cvt_pk_bf16_f32 v95, v30, v31\n"
    "ds_write_b128 v14, v[92:95] offset:3072\n"
    "s_waitcnt lgkmcnt(0)\n"
    "LE1%=:\n"
    "s_barrier\n"
    "ds_read_b128 v[16:19], v9 offset:16384\n"
    "ds_read_b128 v[20:23], v9 offset:17408\n"
    "v_lshrrev_b32 v113, 4, v0\n"
    "v_and_b32 v113, 0x30, v113\n"
    "v_lshl_add_u32 v113, v113, 4, v0\n"
    "s_waitcnt lgkmcnt(1)\n"
    "buffer_store_dwordx4 v[16:19], v113, s[16:19], s54 offen offset:0 sc1\n"
    "s_waitcnt lgkmcnt(0)\n"
    "buffer_store_dwordx4 v[20:23], v113, s[16:19], s54 offen offset:2048 sc1\n"
    "s_waitcnt vmcnt(0)\n"
    "s_barrier\n"
    "buffer_store_dword v1, off, s[16:19], s58 sc1\n"
    "s_endpgm\n"
    "LFAIL%=:\n"
    "buffer_store_dword v1, off, s[16:19], s35 sc1\n"
    "s_endpgm\n"
    :
    : "{v0}"(hv0), "{v1}"(hv1), "{v2}"(hv2), "{v3}"(hv3), "{v4}"(hv4), "{v5}"(hv5), "{v6}"(hv6), "{v7}"(hv7), "{v8}"(hv8), "{v9}"(hv9), "{v10}"(hv10), "{v11}"(hv11), "{v12}"(hv12), "{v13}"(hv13), "{v14}"(hv14), "{v15}"(hv15), "{v16}"(hv16), "{v17}"(hv17), "{v18}"(hv18), "{v19}"(hv19), "{v20}"(hv20)
    : "v255", "a255", "s101", "vcc", "scc", "memory");
  } else {
  asm volatile(
    "s_mov_b32 s33, 0\n"
    "s_nop 1\n"
    "v_mov_b32 v119, v20\n"
    "s_nop 0\n"
    "v_readlane_b32 s61, v119, 0\n"
    "v_readlane_b32 s46, v119, 1\n"
    "v_readlane_b32 s63, v119, 2\n"
    "v_readlane_b32 s48, v119, 3\n"
    "v_readlane_b32 s55, v119, 4\n"
    "v_readlane_b32 s56, v119, 5\n"
    "v_readlane_b32 s57, v119, 6\n"
    "v_readlane_b32 s74, v119, 7\n"
    "v_readlane_b32 s58, v119, 8\n"
    "v_readlane_b32 s59, v119, 9\n"
    "v_readlane_b32 s60, v119, 10\n"
    "v_readlane_b32 s67, v119, 11\n"
    "v_readlane_b32 s68, v119, 12\n"
    "v_readlane_b32 s69, v119, 13\n"
    "v_readlane_b32 s70, v119, 14\n"
    "v_readlane_b32 s71, v119, 15\n"
    "v_readlane_b32 s73, v119, 16\n"
    "v_readlane_b32 s37, v119, 17\n"
    "v_readlane_b32 s92, v119, 36\n"
    "v_readlane_b32 s93, v119, 37\n"
    "v_readlane_b32 s35, v119, 18\n"
    "v_readlane_b32 s75, v119, 19\n"
    "v_readlane_b32 s16, v119, 28\n"
    "v_readlane_b32 s17, v119, 29\n"
    "v_readlane_b32 s18, v119, 30\n"
    "v_readlane_b32 s8, v119, 31\n"
    "v_readlane_b32 s9, v119, 32\n"
    "v_readlane_b32 s10, v119, 33\n"
    "v_readlane_b32 s11, v119, 34\n"
    "v_readlane_b32 s12, v119, 35\n"
    "v_readlane_b32 s13, v119, 38\n"
    "v_readlane_b32 s21, v119, 39\n"
    "s_mov_b32 s19, 0x20000\n"
    "v_readlane_b32 s84, v119, 20\n"
    "v_readlane_b32 s85, v119, 21\n"
    "v_readlane_b32 s86, v119, 22\n"
    "v_readlane_b32 s87, v119, 23\n"
    "v_readlane_b32 s88, v119, 24\n"
    "v_readlane_b32 s89, v119, 25\n"
    "v_readlane_b32 s90, v119, 26\n"
    "v_readlane_b32 s91, v119, 27\n"
    "s_mov_b32 s76, 0xff00ff00\n"
    "s_mov_b32 s77, 0xff00ff00\n"
    "s_mov_b32 s78, 0x00ff00ff\n"
    "s_mov_b32 s79, 0x00ff00ff\n"
    "s_mov_b32 s65, 0x3e0293ee\n"
    "s_mov_b32 s66, 0xfffefffe\n"
    "s_lshl_b32 s62, s61, 11\n"
    "s_mov_b32 s36, 0\n"
    "s_mov_b32 s64, 0\n"
    "s_mov_b32 s50, 0\n"
    "s_lshl_b32 s72, s61, 2\n"
    "s_add_u32 s72, s72, 158208\n"
    "s_add_u32 s47, s46, 4096\n"
    "s_and_b32 s20, s61, 1\n"
    "s_lshl_b32 s20, s20, 7\n"
    "s_mov_b32 s14, 0x3f803f80\n"
    "s_mov_b32 s15, 0x3f803f80\n"
    "s_mov_b32 s82, 0x400000\n"
    "s_mov_b32 s83, 0x20000\n"
    "v_mov_b32 v112, v16\n"
    "v_mov_b32 v113, v17\n"
    "v_mov_b32 v114, v18\n"
    "v_mov_b32 v115, v19\n"
    "v_mov_b32 v117, 0\n"
    "v_mov_b32 v118, 0\n"
    "s_memrealtime s[42:43]\n"
    "s_cmp_eq_u32 s61, 1\n"
    "s_cselect_b64 exec, 1, 0\n"
    "s_waitcnt lgkmcnt(0)\n"
    "s_add_u32 s44, s42, s37\n"
    "s_or_b32 s44, s44, 3\n"
    "v_mov_b32 v16, s44\n"
    "v_mov_b32 v17, 0\n"
    "s_cbranch_execz LCS%=\n"
    "global_atomic_cmpswap v16, v118, v[16:17], s[92:93] offset:1024 sc0 sc1\n"
    "LCS%=:\n"
    "s_mov_b64 exec, -1\n"
    "s_mov_b32 s80, s90\n"
    "s_mov_b32 s81, s91\n"
    "s_mul_i32 s38, s61, 4160\n"
    "s_add_u32 m0, s38, 0\n"
    "s_mov_b32 s39, 0\n"
    "buffer_load_dwordx4 v113, s[80:83], s39 offen lds\n"
    "s_add_u32 m0, s38, 1040\n"
    "s_mov_b32 s39, 1024\n"
    "buffer_load_dwordx4 v113, s[80:83], s39 offen lds\n"
    "s_add_u32 m0, s38, 2080\n"
    "s_mov_b32 s39, 4096\n"
    "buffer_load_dwordx4 v113, s[80:83], s39 offen lds\n"
    "s_add_u32 m0, s38, 3120\n"
    "s_mov_b32 s39, 5120\n"
    "buffer_load_dwordx4 v113, s[80:83], s39 offen lds\n"
    "s_mul_i32 s38, s61, 4160\n"
    "s_add_u32 m0, s38, 17920\n"
    "s_mov_b32 s39, 2048\n"
    "buffer_load_dwordx4 v113, s[80:83], s39 offen lds\n"
    "s_add_u32 m0, s38, 18960\n"
    "s_mov_b32 s39, 3072\n"
    "buffer_load_dwordx4 v113, s[80:83], s39 offen lds\n"
    "s_add_u32 m0, s38, 20000\n"
    "s_mov_b32 s39, 6144\n"
    "buffer_load_dwordx4 v113, s[80:83], s39 offen lds\n"
    "s_add_u32 m0, s38, 21040\n"
    "s_mov_b32 s39, 7168\n"
    "buffer_load_dwordx4 v113, s[80:83], s39 offen lds\n"
    "s_cmp_lg_u32 s61, 1\n"
    "s_cbranch_scc1 LCE%=\n"
    "s_waitcnt vmcnt(8)\n"
    "v_readfirstlane_b32 s38, v16\n"
    "s_cmp_eq_u32 s38, 0\n"
    "s_cselect_b32 s44, s44, s38\n"
    "s_add_u32 s44, s44, 1\n"
    "s_mov_b32 s45, 0\n"
    "s_mov_b32 s37, 8\n"
    "LCB%=:\n"
    "s_memrealtime s[42:43]\n"
    "s_memtime s[40:41]\n"
    "s_waitcnt lgkmcnt(0)\n"
    "s_sub_u32 s42, s42, s44\n"
    "s_cmp_gt_u32 s42, -2000\n"
    "s_cbranch_scc1 LCB%=\n"
    "s_mul_i32 s42, s42, 24\n"
    "s_sub_u32 s40, s40, s42\n"
    "s_cmp_eq_u32 s37, 8\n"
    "s_cselect_b32 s54, s40, s54\n"
    "s_sub_u32 s40, s40, s54\n"
    "s_add_u32 s45, s45, s40\n"
    "s_add_u32 s44, s44, 8\n"
    "s_sub_u32 s37, s37, 1\n"
    "s_cmp_lg_u32 s37, 0\n"
    "s_cbranch_scc1 LCB%=\n"
    "s_ashr_i32 s45, s45, 3\n"
    "s_add_u32 s45, s45, s54\n"
    "s_waitcnt vmcnt(0)\n"
    "s_branch LCG%=\n"
    "LCE%=:\n"
    "s_cmp_lg_u32 s61, 0\n"
    "s_cbranch_scc1 LCF2_%=\n"
    "s_mul_i32 s38, s61, 4160\n"
    "s_add_u32 m0, s38, 40000\n"
    "s_mov_b32 s39, 49152\n"
    "buffer_load_dwordx4 v113, s[80:83], s39 offen lds\n"
    "s_add_u32 m0, s38, 41040\n"
    "s_mov_b32 s39, 50176\n"
    "buffer_load_dwordx4 v113, s[80:83], s39 offen lds\n"
    "s_add_u32 m0, s38, 42080\n"
    "s_mov_b32 s39, 53248\n"
    "buffer_load_dwordx4 v113, s[80:83], s39 offen lds\n"
    "s_add_u32 m0, s38, 43120\n"
    "s_mov_b32 s39, 54272\n"
    "buffer_load_dwordx4 v113, s[80:83], s39 offen lds\n"
    "LCF2_%=:\n"
    "s_mul_i32 s38, s61, 4160\n"
    "s_add_u32 m0, s38, 35840\n"
    "s_mov_b32 s39, 32768\n"
    "buffer_load_dwordx4 v113, s[80:83], s39 offen lds\n"
    "s_add_u32 m0, s38, 36880\n"
    "s_mov_b32 s39, 33792\n"
    "buffer_load_dwordx4 v113, s[80:83], s39 offen lds\n"
    "s_add_u32 m0, s38, 37920\n"
    "s_mov_b32 s39, 36864\n"
    "buffer_load_dwordx4 v113, s[80:83], s39 offen lds\n"
    "s_add_u32 m0, s38, 38960\n"
    "s_mov_b32 s39, 37888\n"
    "buffer_load_dwordx4 v113, s[80:83], s39 offen lds\n"
    "s_cmp_lg_u32 s61, 0\n"
    "s_cbranch_scc1 LCF3_%=\n"
    "s_mul_i32 s38, s61, 4160\n"
    "s_add_u32 m0, s38, 57920\n"
    "s_mov_b32 s39, 51200\n"
    "buffer_load_dwordx4 v113, s[80:83], s39 offen lds\n"
    "s_add_u32 m0, s38, 58960\n"
    "s_mov_b32 s39, 52224\n"
    "buffer_load_dwordx4 v113, s[80:83], s39 offen lds\n"
    "s_add_u32 m0, s38, 60000\n"
    "s_mov_b32 s39, 55296\n"
    "buffer_load_dwordx4 v113, s[80:83], s39 offen lds\n"
    "s_add_u32 m0, s38, 61040\n"
    "s_mov_b32 s39, 56320\n"
    "buffer_load_dwordx4 v113, s[80:83], s39 offen lds\n"
    "LCF3_%=:\n"
    "s_mul_i32 s38, s61, 4160\n"
    "s_add_u32 m0, s38, 53760\n"
    "s_mov_b32 s39, 34816\n"
    "buffer_load_dwordx4 v113, s[80:83], s39 offen lds\n"
    "s_add_u32 m0, s38, 54800\n"
    "s_mov_b32 s39, 35840\n"
    "buffer_load_dwordx4 v113, s[80:83], s39 offen lds\n"
    "s_add_u32 m0, s38, 55840\n"
    "s_mov_b32 s39, 38912\n"
    "buffer_load_dwordx4 v113, s[80:83], s39 offen lds\n"
    "s_add_u32 m0, s38, 56880\n"
    "s_mov_b32 s39, 39936\n"
    "buffer_load_dwordx4 v113, s[80:83], s39 offen lds\n"
    "LCG%=:\n"
    "s_waitcnt vmcnt(12)\n"
    "s_barrier\n"
    "s_mul_i32 s38, s61, 4160\n"
    "s_add_u32 m0, s38, 71680\n"
    "s_mov_b32 s39, 65536\n"
    "buffer_load_dwordx4 v113, s[80:83], s39 offen lds\n"
    "s_add_u32 m0, s38, 72720\n"
    "s_mov_b32 s39, 66560\n"
    "buffer_load_dwordx4 v113, s[80:83], s39 offen lds\n"
    "s_add_u32 m0, s38, 73760\n"
    "s_mov_b32 s39, 69632\n"
    "buffer_load_dwordx4 v113, s[80:83], s39 offen lds\n"
    "s_add_u32 m0, s38, 74800\n"
    "s_mov_b32 s39, 70656\n"
    "buffer_load_dwordx4 v113, s[80:83], s39 offen lds\n"
    "v_add_u32 v116, 0, v115\n"
    "ds_read_b64_tr_b16 v[168:169], v116 offset:0\n"
    "ds_read_b64_tr_b16 v[184:185], v116 offset:8\n"
    "ds_read_b64_tr_b16 v[200:201], v116 offset:16\n"
    "ds_read_b64_tr_b16 v[216:217], v116 offset:24\n"
    "ds_read_b64_tr_b16 v[232:233], v116 offset:32\n"
    "ds_read_b64_tr_b16 v[248:249], v116 offset:40\n"
    "ds_read_b64_tr_b16 v[104:105], v116 offset:48\n"
    "ds_read_b64_tr_b16 v[32:33], v116 offset:56\n"
    "s_waitcnt lgkmcnt(0)\n"
    "s_waitcnt vmcnt(12)\n"
    "s_barrier\n"
    "s_mul_i32 s38, s61, 4160\n"
    "s_add_u32 m0, s38, 89600\n"
    "s_mov_b32 s39, 67584\n"
    "buffer_load_dwordx4 v113, s[80:83], s39 offen lds\n"
    "s_add_u32 m0, s38, 90640\n"
    "s_mov_b32 s39, 68608\n"
    "buffer_load_dwordx4 v113, s[80:83], s39 offen lds\n"
    "s_add_u32 m0, s38, 91680\n"
    "s_mov_b32 s39, 71680\n"
    "buffer_load_dwordx4 v113, s[80:83], s39 offen lds\n"
    "s_add_u32 m0, s38, 92720\n"
    "s_mov_b32 s39, 72704\n"
    "buffer_load_dwordx4 v113, s[80:83], s39 offen lds\n"
    "v_add_u32 v116, 17920, v115\n"
    "ds_read_b64_tr_b16 v[170:171], v116 offset:0\n"
    "ds_read_b64_tr_b16 v[186:187], v116 offset:8\n"
    "ds_read_b64_tr_b16 v[202:203], v116 offset:16\n"
    "ds_read_b64_tr_b16 v[218:219], v116 offset:24\n"
    "ds_read_b64_tr_b16 v[234:235], v116 offset:32\n"
    "ds_read_b64_tr_b16 v[250:251], v116 offset:40\n"
    "ds_read_b64_tr_b16 v[106:107], v116 offset:48\n"
    "ds_read_b64_tr_b16 v[34:35], v116 offset:56\n"
    "s_waitcnt lgkmcnt(0)\n"
    "s_waitcnt vmcnt(12)\n"
    "s_barrier\n"
    "s_mul_i32 s38, s61, 4160\n"
    "s_add_u32 m0, s38, 0\n"
    "s_mov_b32 s39, 98304\n"
    "buffer_load_dwordx4 v113, s[80:83], s39 offen lds\n"
    "s_add_u32 m0, s38, 1040\n"
    "s_mov_b32 s39, 99328\n"
    "buffer_load_dwordx4 v113, s[80:83], s39 offen lds\n"
    "s_add_u32 m0, s38, 2080\n"
    "s_mov_b32 s39, 102400\n"
    "buffer_load_dwordx4 v113, s[80:83], s39 offen lds\n"
    "s_add_u32 m0, s38, 3120\n"
    "s_mov_b32 s39, 103424\n"
    "buffer_load_dwordx4 v113, s[80:83], s39 offen lds\n"
    "v_add_u32 v116, 35840, v115\n"
    "ds_read_b64_tr_b16 v[172:173], v116 offset:0\n"
    "ds_read_b64_tr_b16 v[188:189], v116 offset:8\n"
    "ds_read_b64_tr_b16 v[204:205], v116 offset:16\n"
    "ds_read_b64_tr_b16 v[220:221], v116 offset:24\n"
    "ds_read_b64_tr_b16 v[236:237], v116 offset:32\n"
    "ds_read_b64_tr_b16 v[252:253], v116 offset:40\n"
    "ds_read_b64_tr_b16 v[108:109], v116 offset:48\n"
    "ds_read_b64_tr_b16 v[36:37], v116 offset:56\n"
    "s_waitcnt lgkmcnt(0)\n"
    "s_waitcnt vmcnt(12)\n"
    "s_barrier\n"
    "s_mul_i32 s38, s61, 4160\n"
    "s_add_u32 m0, s38, 17920\n"
    "s_mov_b32 s39, 100352\n"
    "buffer_load_dwordx4 v113, s[80:83], s39 offen lds\n"
    "s_add_u32 m0, s38, 18960\n"
    "s_mov_b32 s39, 101376\n"
    "buffer_load_dwordx4 v113, s[80:83], s39 offen lds\n"
    "s_add_u32 m0, s38, 20000\n"
    "s_mov_b32 s39, 104448\n"
    "buffer_load_dwordx4 v113, s[80:83], s39 offen lds\n"
    "s_add_u32 m0, s38, 21040\n"
    "s_mov_b32 s39, 105472\n"
    "buffer_load_dwordx4 v113, s[80:83], s39 offen lds\n"
    "v_add_u32 v116, 53760, v115\n"
    "ds_read_b64_tr_b16 v[174:175], v116 offset:0\n"
    "ds_read_b64_tr_b16 v[190:191], v116 offset:8\n"
    "ds_read_b64_tr_b16 v[206:207], v116 offset:16\n"
    "ds_read_b64_tr_b16 v[222:223], v116 offset:24\n"
    "ds_read_b64_tr_b16 v[238:239], v116 offset:32\n"
    "ds_read_b64_tr_b16 v[254:255], v116 offset:40\n"
    "ds_read_b64_tr_b16 v[110:111], v116 offset:48\n"
    "ds_read_b64_tr_b16 v[38:39], v116 offset:56\n"
    "s_waitcnt lgkmcnt(0)\n"
    "s_waitcnt vmcnt(12)\n"
    "s_barrier\n"
    "s_mov_b32 s80, s88\n"
    "s_mov_b32 s81, s89\n"
    "s_mul_i32 s38, s61, 4480\n"
    "s_add_u32 m0, s38, 35840\n"
    "s_mov_b32 s39, 0\n"
    "buffer_load_dwordx4 v112, s[80:83], s39 offen lds\n"
    "s_add_u32 m0, s38, 36928\n"
    "s_mov_b32 s39, 32768\n"
    "buffer_load_dwordx4 v112, s[80:83], s39 offen lds\n"
    "s_add_u32 m0, s38, 38016\n"
    "s_mov_b32 s39, 65536\n"
    "buffer_load_dwordx4 v112, s[80:83], s39 offen lds\n"
    "s_add_u32 m0, s38, 39104\n"
    "s_mov_b32 s39, 98304\n"
    "buffer_load_dwordx4 v112, s[80:83], s39 offen lds\n"
    "v_add_u32 v116, 71680, v115\n"
    "ds_read_b64_tr_b16 v[176:177], v116 offset:0\n"
    "ds_read_b64_tr_b16 v[192:193], v116 offset:8\n"
    "ds_read_b64_tr_b16 v[208:209], v116 offset:16\n"
    "ds_read_b64_tr_b16 v[224:225], v116 offset:24\n"
    "ds_read_b64_tr_b16 v[240:241], v116 offset:32\n"
    "ds_read_b64_tr_b16 v[96:97], v116 offset:40\n"
    "ds_read_b64_tr_b16 v[24:25], v116 offset:48\n"
    "ds_read_b64_tr_b16 v[40:41], v116 offset:56\n"
    "s_waitcnt lgkmcnt(0)\n"
    "s_waitcnt vmcnt(12)\n"
    "s_barrier\n"
    "s_mul_i32 s38, s61, 4480\n"
    "s_add_u32 m0, s38, 53760\n"
    "s_mov_b32 s39, 524288\n"
    "buffer_load_dwordx4 v112, s[80:83], s39 offen lds\n"
    "s_add_u32 m0, s38, 54848\n"
    "s_mov_b32 s39, 557056\n"
    "buffer_load_dwordx4 v112, s[80:83], s39 offen lds\n"
    "s_add_u32 m0, s38, 55936\n"
    "s_mov_b32 s39, 589824\n"
    "buffer_load_dwordx4 v112, s[80:83], s39 offen lds\n"
    "s_add_u32 m0, s38, 57024\n"
    "s_mov_b32 s39, 622592\n"
    "buffer_load_dwordx4 v112, s[80:83], s39 offen lds\n"
    "v_add_u32 v116, 89600, v115\n"
    "ds_read_b64_tr_b16 v[178:179], v116 offset:0\n"
    "ds_read_b64_tr_b16 v[194:195], v116 offset:8\n"
    "ds_read_b64_tr_b16 v[210:211], v116 offset:16\n"
    "ds_read_b64_tr_b16 v[226:227], v116 offset:24\n"
    "ds_read_b64_tr_b16 v[242:243], v116 offset:32\n"
    "ds_read_b64_tr_b16 v[98:99], v116 offset:40\n"
    "ds_read_b64_tr_b16 v[26:27], v116 offset:48\n"
    "ds_read_b64_tr_b16 v[42:43], v116 offset:56\n"
    "s_waitcnt lgkmcnt(0)\n"
    "s_waitcnt vmcnt(12)\n"
    "s_barrier\n"
    "s_mul_i32 s38, s61, 4480\n"
    "s_add_u32 m0, s38, 71680\n"
    "s_mov_b32 s39, 1048576\n"
    "buffer_load_dwordx4 v112, s[80:83], s39 offen lds\n"
    "s_add_u32 m0, s38, 72768\n"
    "s_mov_b32 s39, 1081344\n"
    "buffer_load_dwordx4 v112, s[80:83], s39 offen lds\n"
    "s_add_u32 m0, s38, 73856\n"
    "s_mov_b32 s39, 1114112\n"
    "buffer_load_dwordx4 v112, s[80:83], s39 offen lds\n"
    "s_add_u32 m0, s38, 74944\n"
    "s_mov_b32 s39, 1146880\n"
    "buffer_load_dwordx4 v112, s[80:83], s39 offen lds\n"
    "v_add_u32 v116, 0, v115\n"
    "ds_read_b64_tr_b16 v[180:181], v116 offset:0\n"
    "ds_read_b64_tr_b16 v[196:197], v116 offset:8\n"
    "ds_read_b64_tr_b16 v[212:213], v116 offset:16\n"
    "ds_read_b64_tr_b16 v[228:229], v116 offset:24\n"
    "ds_read_b64_tr_b16 v[244:245], v116 offset:32\n"
    "ds_read_b64_tr_b16 v[100:101], v116 offset:40\n"
    "ds_read_b64_tr_b16 v[28:29], v116 offset:48\n"
    "ds_read_b64_tr_b16 v[44:45], v116 offset:56\n"
    "s_waitcnt lgkmcnt(0)\n"
    "s_waitcnt vmcnt(12)\n"
    "s_barrier\n"
    "s_mul_i32 s38, s61, 4480\n"
    "s_add_u32 m0, s38, 89600\n"
    "s_mov_b32 s39, 1572864\n"
    "buffer_load_dwordx4 v112, s[80:83], s39 offen lds\n"
    "s_add_u32 m0, s38, 90688\n"
    "s_mov_b32 s39, 1605632\n"
    "buffer_load_dwordx4 v112, s[80:83], s39 offen lds\n"
    "s_add_u32 m0, s38, 91776\n"
    "s_mov_b32 s39, 1638400\n"
    "buffer_load_dwordx4 v112, s[80:83], s39 offen lds\n"
    "s_add_u32 m0, s38, 92864\n"
    "s_mov_b32 s39, 1671168\n"
    "buffer_load_dwordx4 v112, s[80:83], s39 offen lds\n"
    "v_add_u32 v116, 17920, v115\n"
    "ds_read_b64_tr_b16 v[182:183], v116 offset:0\n"
    "ds_read_b64_tr_b16 v[198:199], v116 offset:8\n"
    "ds_read_b64_tr_b16 v[214:215], v116 offset:16\n"
    "ds_read_b64_tr_b16 v[230:231], v116 offset:24\n"
    "ds_read_b64_tr_b16 v[246:247], v116 offset:32\n"
    "ds_read_b64_tr_b16 v[102:103], v116 offset:40\n"
    "ds_read_b64_tr_b16 v[30:31], v116 offset:48\n"
    "ds_read_b64_tr_b16 v[46:47], v116 offset:56\n"
    "ds_write_b128 v9, v[24:27] offset:0\n"
    "s_waitcnt lgkmcnt(2)\n"
    "ds_write_b128 v9, v[28:31] offset:1024\n"
    "ds_write_b128 v9, v[32:35] offset:8192\n"
    "ds_write_b128 v9, v[36:39] offset:9216\n"
    "ds_write_b128 v9, v[40:43] offset:16384\n"
    "s_waitcnt lgkmcnt(5)\n"
    "ds_write_b128 v9, v[44:47] offset:17408\n"
    "s_waitcnt lgkmcnt(0)\n"
    "s_waitcnt vmcnt(12)\n"
    "s_barrier\n"
    "s_mul_i32 s38, s61, 4480\n"
    "s_add_u32 m0, s38, 0\n"
    "s_mov_b32 s39, 2097152\n"
    "buffer_load_dwordx4 v112, s[80:83], s39 offen lds\n"
    "s_add_u32 m0, s38, 1088\n"
    "s_mov_b32 s39, 2129920\n"
    "buffer_load_dwordx4 v112, s[80:83], s39 offen lds\n"
    "s_add_u32 m0, s38, 2176\n"
    "s_mov_b32 s39, 2162688\n"
    "buffer_load_dwordx4 v112, s[80:83], s39 offen lds\n"
    "s_add_u32 m0, s38, 3264\n"
    "s_mov_b32 s39, 2195456\n"
    "buffer_load_dwordx4 v112, s[80:83], s39 offen lds\n"
    "v_add_u32 v116, 35840, v114\n"
    "ds_read_b64_tr_b16 a[176:177], v116 offset:0\n"
    "ds_read_b64_tr_b16 a[178:179], v116 offset:2176\n"
    "ds_read_b64_tr_b16 a[180:181], v116 offset:8960\n"
    "ds_read_b64_tr_b16 a[182:183], v116 offset:11136\n"
    "v_xor_b32 v116, 32, v116\n"
    "ds_read_b64_tr_b16 a[184:185], v116 offset:0\n"
    "ds_read_b64_tr_b16 a[186:187], v116 offset:2176\n"
    "ds_read_b64_tr_b16 a[188:189], v116 offset:8960\n"
    "ds_read_b64_tr_b16 a[190:191], v116 offset:11136\n"
    "s_waitcnt lgkmcnt(0)\n"
    "s_waitcnt vmcnt(12)\n"
    "s_barrier\n"
    "s_mul_i32 s38, s61, 4480\n"
    "s_add_u32 m0, s38, 17920\n"
    "s_mov_b32 s39, 2621440\n"
    "buffer_load_dwordx4 v112, s[80:83], s39 offen lds\n"
    "s_add_u32 m0, s38, 19008\n"
    "s_mov_b32 s39, 2654208\n"
    "buffer_load_dwordx4 v112, s[80:83], s39 offen lds\n"
    "s_add_u32 m0, s38, 20096\n"
    "s_mov_b32 s39, 2686976\n"
    "buffer_load_dwordx4 v112, s[80:83], s39 offen lds\n"
    "s_add_u32 m0, s38, 21184\n"
    "s_mov_b32 s39, 2719744\n"
    "buffer_load_dwordx4 v112, s[80:83], s39 offen lds\n"
    "v_add_u32 v116, 53760, v114\n"
    "ds_read_b64_tr_b16 a[192:193], v116 offset:0\n"
    "ds_read_b64_tr_b16 a[194:195], v116 offset:2176\n"
    "ds_read_b64_tr_b16 a[196:197], v116 offset:8960\n"
    "ds_read_b64_tr_b16 a[198:199], v116 offset:11136\n"
    "v_xor_b32 v116, 32, v116\n"
    "ds_read_b64_tr_b16 a[200:201], v116 offset:0\n"
    "ds_read_b64_tr_b16 a[202:203], v116 offset:2176\n"
    "ds_read_b64_tr_b16 a[204:205], v116 offset:8960\n"
    "ds_read_b64_tr_b16 a[206:207], v116 offset:11136\n"
    "s_waitcnt lgkmcnt(0)\n"
    "s_waitcnt vmcnt(12)\n"
    "s_barrier\n"
    "s_mul_i32 s38, s61, 4480\n"
    "s_add_u32 m0, s38, 35840\n"
    "s_mov_b32 s39, 3145728\n"
    "buffer_load_dwordx4 v112, s[80:83], s39 offen lds\n"
    "s_add_u32 m0, s38, 36928\n"
    "s_mov_b32 s39, 3178496\n"
    "buffer_load_dwordx4 v112, s[80:83], s39 offen lds\n"
    "s_add_u32 m0, s38, 38016\n"
    "s_mov_b32 s39, 3211264\n"
    "buffer_load_dwordx4 v112, s[80:83], s39 offen lds\n"
    "s_add_u32 m0, s38, 39104\n"
    "s_mov_b32 s39, 3244032\n"
    "buffer_load_dwordx4 v112, s[80:83], s39 offen lds\n"
    "v_add_u32 v116, 71680, v114\n"
    "ds_read_b64_tr_b16 a[208:209], v116 offset:0\n"
    "ds_read_b64_tr_b16 a[210:211], v116 offset:2176\n"
    "ds_read_b64_tr_b16 a[212:213], v116 offset:8960\n"
    "ds_read_b64_tr_b16 a[214:215], v116 offset:11136\n"
    "v_xor_b32 v116, 32, v116\n"
    "ds_read_b64_tr_b16 a[216:217], v116 offset:0\n"
    "ds_read_b64_tr_b16 a[218:219], v116 offset:2176\n"
    "ds_read_b64_tr_b16 a[220:221], v116 offset:8960\n"
    "ds_read_b64_tr_b16 a[222:223], v116 offset:11136\n"
    "s_waitcnt lgkmcnt(0)\n"
    "s_waitcnt vmcnt(12)\n"
    "s_barrier\n"
    "s_mul_i32 s38, s61, 4480\n"
    "s_add_u32 m0, s38, 53760\n"
    "s_mov_b32 s39, 3670016\n"
    "buffer_load_dwordx4 v112, s[80:83], s39 offen lds\n"
    "s_add_u32 m0, s38, 54848\n"
    "s_mov_b32 s39, 3702784\n"
    "buffer_load_dwordx4 v112, s[80:83], s39 offen lds\n"
    "s_add_u32 m0, s38, 55936\n"
    "s_mov_b32 s39, 3735552\n"
    "buffer_load_dwordx4 v112, s[80:83], s39 offen lds\n"
    "s_add_u32 m0, s38, 57024\n"
    "s_mov_b32 s39, 3768320\n"
    "buffer_load_dwordx4 v112, s[80:83], s39 offen lds\n"
    "v_add_u32 v116, 89600, v114\n"
    "ds_read_b64_tr_b16 a[224:225], v116 offset:0\n"
    "ds_read_b64_tr_b16 a[226:227], v116 offset:2176\n"
    "ds_read_b64_tr_b16 a[228:229], v116 offset:8960\n"
    "ds_read_b64_tr_b16 a[230:231], v116 offset:11136\n"
    "v_xor_b32 v116, 32, v116\n"
    "ds_read_b64_tr_b16 a[232:233], v116 offset:0\n"
    "ds_read_b64_tr_b16 a[234:235], v116 offset:2176\n"
    "ds_read_b64_tr_b16 a[236:237], v116 offset:8960\n"
    "ds_read_b64_tr_b16 a[238:239], v116 offset:11136\n"
    "s_waitcnt lgkmcnt(0)\n"
    "s_waitcnt vmcnt(12)\n"
    "s_barrier\n"
    "s_mov_b32 s80, s86\n"
    "s_mov_b32 s81, s87\n"
    "s_mul_i32 s38, s61, 4480\n"
    "s_add_u32 m0, s38, 71680\n"
    "s_mov_b32 s39, 0\n"
    "buffer_load_dwordx4 v112, s[80:83], s39 offen lds\n"
    "s_add_u32 m0, s38, 72768\n"
    "s_mov_b32 s39, 32768\n"
    "buffer_load_dwordx4 v112, s[80:83], s39 offen lds\n"
    "s_add_u32 m0, s38, 73856\n"
    "s_mov_b32 s39, 65536\n"
    "buffer_load_dwordx4 v112, s[80:83], s39 offen lds\n"
    "s_add_u32 m0, s38, 74944\n"
    "s_mov_b32 s39, 98304\n"
    "buffer_load_dwordx4 v112, s[80:83], s39 offen lds\n"
    "v_add_u32 v116, 0, v114\n"
    "ds_read_b64_tr_b16 a[240:241], v116 offset:0\n"
    "ds_read_b64_tr_b16 a[242:243], v116 offset:2176\n"
    "ds_read_b64_tr_b16 a[244:245], v116 offset:8960\n"
    "ds_read_b64_tr_b16 a[246:247], v116 offset:11136\n"
    "v_xor_b32 v116, 32, v116\n"
    "ds_read_b64_tr_b16 a[248:249], v116 offset:0\n"
    "ds_read_b64_tr_b16 a[250:251], v116 offset:2176\n"
    "ds_read_b64_tr_b16 a[252:253], v116 offset:8960\n"
    "ds_read_b64_tr_b16 a[254:255], v116 offset:11136\n"
    "s_waitcnt lgkmcnt(0)\n"
    "s_waitcnt vmcnt(12)\n"
    "s_barrier\n"
    "s_mul_i32 s38, s61, 4480\n"
    "s_add_u32 m0, s38, 89600\n"
    "s_mov_b32 s39, 524288\n"
    "buffer_load_dwordx4 v112, s[80:83], s39 offen lds\n"
    "s_add_u32 m0, s38, 90688\n"
    "s_mov_b32 s39, 557056\n"
    "buffer_load_dwordx4 v112, s[80:83], s39 offen lds\n"
    "s_add_u32 m0, s38, 91776\n"
    "s_mov_b32 s39, 589824\n"
    "buffer_load_dwordx4 v112, s[80:83], s39 offen lds\n"
    "s_add_u32 m0, s38, 92864\n"
    "s_mov_b32 s39, 622592\n"
    "buffer_load_dwordx4 v112, s[80:83], s39 offen lds\n"
    "v_add_u32 v116, 17920, v114\n"
    "ds_read_b64_tr_b16 v[120:121], v116 offset:0\n"
    "ds_read_b64_tr_b16 v[122:123], v116 offset:2176\n"
    "ds_read_b64_tr_b16 v[124:125], v116 offset:8960\n"
    "ds_read_b64_tr_b16 v[126:127], v116 offset:11136\n"
    "v_xor_b32 v116, 32, v116\n"
    "ds_read_b64_tr_b16 v[128:129], v116 offset:0\n"
    "ds_read_b64_tr_b16 v[130:131], v116 offset:2176\n"
    "ds_read_b64_tr_b16 v[132:133], v116 offset:8960\n"
    "ds_read_b64_tr_b16 v[134:135], v116 offset:11136\n"
    "s_waitcnt lgkmcnt(0)\n"
    "s_waitcnt vmcnt(12)\n"
    "s_barrier\n"
    "s_mul_i32 s38, s61, 4480\n"
    "s_add_u32 m0, s38, 0\n"
    "s_mov_b32 s39, 1048576\n"
    "buffer_load_dwordx4 v112, s[80:83], s39 offen lds\n"
    "s_add_u32 m0, s38, 1088\n"
    "s_mov_b32 s39, 1081344\n"
    "buffer_load_dwordx4 v112, s[80:83], s39 offen lds\n"
    "s_add_u32 m0, s38, 2176\n"
    "s_mov_b32 s39, 1114112\n"
    "buffer_load_dwordx4 v112, s[80:83], s39 offen lds\n"
    "s_add_u32 m0, s38, 3264\n"
    "s_mov_b32 s39, 1146880\n"
    "buffer_load_dwordx4 v112, s[80:83], s39 offen lds\n"
    "v_add_u32 v116, 35840, v114\n"
    "ds_read_b64_tr_b16 v[136:137], v116 offset:0\n"
    "ds_read_b64_tr_b16 v[138:139], v116 offset:2176\n"
    "ds_read_b64_tr_b16 v[140:141], v116 offset:8960\n"
    "ds_read_b64_tr_b16 v[142:143], v116 offset:11136\n"
    "v_xor_b32 v116, 32, v116\n"
    "ds_read_b64_tr_b16 v[144:145], v116 offset:0\n"
    "ds_read_b64_tr_b16 v[146:147], v116 offset:2176\n"
    "ds_read_b64_tr_b16 v[148:149], v116 offset:8960\n"
    "ds_read_b64_tr_b16 v[150:151], v116 offset:11136\n"
    "s_waitcnt lgkmcnt(0)\n"
    "s_waitcnt vmcnt(12)\n"
    "s_barrier\n"
    "s_mul_i32 s38, s61, 4480\n"
    "s_add_u32 m0, s38, 17920\n"
    "s_mov_b32 s39, 1572864\n"
    "buffer_load_dwordx4 v112, s[80:83], s39 offen lds\n"
    "s_add_u32 m0, s38, 19008\n"
    "s_mov_b32 s39, 1605632\n"
    "buffer_load_dwordx4 v112, s[80:83], s39 offen lds\n"
    "s_add_u32 m0, s38, 20096\n"
    "s_mov_b32 s39, 1638400\n"
    "buffer_load_dwordx4 v112, s[80:83], s39 offen lds\n"
    "s_add_u32 m0, s38, 21184\n"
    "s_mov_b32 s39, 1671168\n"
    "buffer_load_dwordx4 v112, s[80:83], s39 offen lds\n"
    "v_add_u32 v116, 53760, v114\n"
    "ds_read_b64_tr_b16 v[152:153], v116 offset:0\n"
    "ds_read_b64_tr_b16 v[154:155], v116 offset:2176\n"
    "ds_read_b64_tr_b16 v[156:157], v116 offset:8960\n"
    "ds_read_b64_tr_b16 v[158:159], v116 offset:11136\n"
    "v_xor_b32 v116, 32, v116\n"
    "ds_read_b64_tr_b16 v[160:161], v116 offset:0\n"
    "ds_read_b64_tr_b16 v[162:163], v116 offset:2176\n"
    "ds_read_b64_tr_b16 v[164:165], v116 offset:8960\n"
    "ds_read_b64_tr_b16 v[166:167], v116 offset:11136\n"
    "s_waitcnt lgkmcnt(0)\n"
    "s_waitcnt vmcnt(12)\n"
    "s_barrier\n"
    "s_mul_i32 s38, s61, 4480\n"
    "s_add_u32 m0, s38, 35840\n"
    "s_mov_b32 s39, 2097152\n"
    "buffer_load_dwordx4 v112, s[80:83], s39 offen lds\n"
    "s_add_u32 m0, s38, 36928\n"
    "s_mov_b32 s39, 2129920\n"
    "buffer_load_dwordx4 v112, s[80:83], s39 offen lds\n"
    "s_add_u32 m0, s38, 38016\n"
    "s_mov_b32 s39, 2162688\n"
    "buffer_load_dwordx4 v112, s[80:83], s39 offen lds\n"
    "s_add_u32 m0, s38, 39104\n"
    "s_mov_b32 s39, 2195456\n"
    "buffer_load_dwordx4 v112, s[80:83], s39 offen lds\n"
    "v_add_u32 v116, 71680, v114\n"
    "ds_read_b64_tr_b16 v[64:65], v116 offset:0\n"
    "ds_read_b64_tr_b16 v[66:67], v116 offset:2176\n"
    "ds_read_b64_tr_b16 v[68:69], v116 offset:8960\n"
    "ds_read_b64_tr_b16 v[70:71], v116 offset:11136\n"
    "v_xor_b32 v116, 32, v116\n"
    "ds_read_b64_tr_b16 v[72:73], v116 offset:0\n"
    "ds_read_b64_tr_b16 v[74:75], v116 offset:2176\n"
    "ds_read_b64_tr_b16 v[76:77], v116 offset:8960\n"
    "ds_read_b64_tr_b16 v[78:79], v116 offset:11136\n"
    "s_waitcnt lgkmcnt(0)\n"
    "s_waitcnt vmcnt(12)\n"
    "s_barrier\n"
    "s_mul_i32 s38, s61, 4480\n"
    "s_add_u32 m0, s38, 53760\n"
    "s_mov_b32 s39, 2621440\n"
    "buffer_load_dwordx4 v112, s[80:83], s39 offen lds\n"
    "s_add_u32 m0, s38, 54848\n"
    "s_mov_b32 s39, 2654208\n"
    "buffer_load_dwordx4 v112, s[80:83], s39 offen lds\n"
    "s_add_u32 m0, s38, 55936\n"
    "s_mov_b32 s39, 2686976\n"
    "buffer_load_dwordx4 v112, s[80:83], s39 offen lds\n"
    "s_add_u32 m0, s38, 57024\n"
    "s_mov_b32 s39, 2719744\n"
    "buffer_load_dwordx4 v112, s[80:83], s39 offen lds\n"
    "v_add_u32 v116, 89600, v114\n"
    "ds_read_b64_tr_b16 v[80:81], v116 offset:0\n"
    "ds_read_b64_tr_b16 v[82:83], v116 offset:2176\n"
    "ds_read_b64_tr_b16 v[84:85], v116 offset:8960\n"
    "ds_read_b64_tr_b16 v[86:87], v116 offset:11136\n"
    "v_xor_b32 v116, 32, v116\n"
    "ds_read_b64_tr_b16 v[88:89], v116 offset:0\n"
    "ds_read_b64_tr_b16 v[90:91], v116 offset:2176\n"
    "ds_read_b64_tr_b16 v[92:93], v116 offset:8960\n"
    "ds_read_b64_tr_b16 v[94:95], v116 offset:11136\n"
    "s_waitcnt lgkmcnt(0)\n"
    "s_waitcnt vmcnt(12)\n"
    "s_barrier\n"
    "s_mul_i32 s38, s61, 4480\n"
    "s_add_u32 m0, s38, 71680\n"
    "s_mov_b32 s39, 3145728\n"
    "buffer_load_dwordx4 v112, s[80:83], s39 offen lds\n"
    "s_add_u32 m0, s38, 72768\n"
    "s_mov_b32 s39, 3178496\n"
    "buffer_load_dwordx4 v112, s[80:83], s39 offen lds\n"
    "s_add_u32 m0, s38, 73856\n"
    "s_mov_b32 s39, 3211264\n"
    "buffer_load_dwordx4 v112, s[80:83], s39 offen lds\n"
    "s_add_u32 m0, s38, 74944\n"
    "s_mov_b32 s39, 3244032\n"
    "buffer_load_dwordx4 v112, s[80:83], s39 offen lds\n"
    "v_add_u32 v116, 0, v114\n"
    "ds_read_b64_tr_b16 a[80:81], v116 offset:0\n"
    "ds_read_b64_tr_b16 a[82:83], v116 offset:2176\n"
    "ds_read_b64_tr_b16 a[84:85], v116 offset:8960\n"
    "ds_read_b64_tr_b16 a[86:87], v116 offset:11136\n"
    "v_xor_b32 v116, 32, v116\n"
    "ds_read_b64_tr_b16 a[88:89], v116 offset:0\n"
    "ds_read_b64_tr_b16 a[90:91], v116 offset:2176\n"
    "ds_read_b64_tr_b16 a[92:93], v116 offset:8960\n"
    "ds_read_b64_tr_b16 a[94:95], v116 offset:11136\n"
    "s_waitcnt lgkmcnt(0)\n"
    "s_waitcnt vmcnt(12)\n"
    "s_barrier\n"
    "s_mul_i32 s38, s61, 4480\n"
    "s_add_u32 m0, s38, 89600\n"
    "s_mov_b32 s39, 3670016\n"
    "buffer_load_dwordx4 v112, s[80:83], s39 offen lds\n"
    "s_add_u32 m0, s38, 90688\n"
    "s_mov_b32 s39, 3702784\n"
    "buffer_load_dwordx4 v112, s[80:83], s39 offen lds\n"
    "s_add_u32 m0, s38, 91776\n"
    "s_mov_b32 s39, 3735552\n"
    "buffer_load_dwordx4 v112, s[80:83], s39 offen lds\n"
    "s_add_u32 m0, s38, 92864\n"
    "s_mov_b32 s39, 3768320\n"
    "buffer_load_dwordx4 v112, s[80:83], s39 offen lds\n"
    "v_add_u32 v116, 17920, v114\n"
    "ds_read_b64_tr_b16 a[96:97], v116 offset:0\n"
    "ds_read_b64_tr_b16 a[98:99], v116 offset:2176\n"
    "ds_read_b64_tr_b16 a[100:101], v116 offset:8960\n"
    "ds_read_b64_tr_b16 a[102:103], v116 offset:11136\n"
    "v_xor_b32 v116, 32, v116\n"
    "ds_read_b64_tr_b16 a[104:105], v116 offset:0\n"
    "ds_read_b64_tr_b16 a[106:107], v116 offset:2176\n"
    "ds_read_b64_tr_b16 a[108:109], v116 offset:8960\n"
    "ds_read_b64_tr_b16 a[110:111], v116 offset:11136\n"
    "s_waitcnt lgkmcnt(0)\n"
    "s_waitcnt vmcnt(12)\n"
    "s_barrier\n"
    "s_mov_b32 s80, s84\n"
    "s_mov_b32 s81, s85\n"
    "s_mul_i32 s38, s61, 4480\n"
    "s_add_u32 m0, s38, 0\n"
    "s_mov_b32 s39, 0\n"
    "buffer_load_dwordx4 v112, s[80:83], s39 offen lds\n"
    "s_add_u32 m0, s38, 1088\n"
    "s_mov_b32 s39, 32768\n"
    "buffer_load_dwordx4 v112, s[80:83], s39 offen lds\n"
    "s_add_u32 m0, s38, 2176\n"
    "s_mov_b32 s39, 65536\n"
    "buffer_load_dwordx4 v112, s[80:83], s39 offen lds\n"
    "s_add_u32 m0, s38, 3264\n"
    "s_mov_b32 s39, 98304\n"
    "buffer_load_dwordx4 v112, s[80:83], s39 offen lds\n"
    "v_add_u32 v116, 35840, v114\n"
    "ds_read_b64_tr_b16 a[112:113], v116 offset:0\n"
    "ds_read_b64_tr_b16 a[114:115], v116 offset:2176\n"
    "ds_read_b64_tr_b16 a[116:117], v116 offset:8960\n"
    "ds_read_b64_tr_b16 a[118:119], v116 offset:11136\n"
    "v_xor_b32 v116, 32, v116\n"
    "ds_read_b64_tr_b16 a[120:121], v116 offset:0\n"
    "ds_read_b64_tr_b16 a[122:123], v116 offset:2176\n"
    "ds_read_b64_tr_b16 a[124:125], v116 offset:8960\n"
    "ds_read_b64_tr_b16 a[126:127], v116 offset:11136\n"
    "s_waitcnt lgkmcnt(0)\n"
    "s_waitcnt vmcnt(12)\n"
    "s_barrier\n"
    "s_mul_i32 s38, s61, 4480\n"
    "s_add_u32 m0, s38, 17920\n"
    "s_mov_b32 s39, 524288\n"
    "buffer_load_dwordx4 v112, s[80:83], s39 offen lds\n"
    "s_add_u32 m0, s38, 19008\n"
    "s_mov_b32 s39, 557056\n"
    "buffer_load_dwordx4 v112, s[80:83], s39 offen lds\n"
    "s_add_u32 m0, s38, 20096\n"
    "s_mov_b32 s39, 589824\n"
    "buffer_load_dwordx4 v112, s[80:83], s39 offen lds\n"
    "s_add_u32 m0, s38, 21184\n"
    "s_mov_b32 s39, 622592\n"
    "buffer_load_dwordx4 v112, s[80:83], s39 offen lds\n"
    "v_add_u32 v116, 53760, v114\n"
    "ds_read_b64_tr_b16 a[128:129], v116 offset:0\n"
    "ds_read_b64_tr_b16 a[130:131], v116 offset:2176\n"
    "ds_read_b64_tr_b16 a[132:133], v116 offset:8960\n"
    "ds_read_b64_tr_b16 a[134:135], v116 offset:11136\n"
    "v_xor_b32 v116, 32, v116\n"
    "ds_read_b64_tr_b16 a[136:137], v116 offset:0\n"
    "ds_read_b64_tr_b16 a[138:139], v116 offset:2176\n"
    "ds_read_b64_tr_b16 a[140:141], v116 offset:8960\n"
    "ds_read_b64_tr_b16 a[142:143], v116 offset:11136\n"
    "s_waitcnt lgkmcnt(0)\n"
    "s_waitcnt vmcnt(12)\n"
    "s_barrier\n"
    "global_load_dword v117, v118, s[92:93] sc1\n"
    "s_mul_i32 s38, s61, 4480\n"
    "s_add_u32 m0, s38, 35840\n"
    "s_mov_b32 s39, 1048576\n"
    "buffer_load_dwordx4 v112, s[80:83], s39 offen lds\n"
    "s_add_u32 m0, s38, 36928\n"
    "s_mov_b32 s39, 1081344\n"
    "buffer_load_dwordx4 v112, s[80:83], s39 offen lds\n"
    "s_add_u32 m0, s38, 38016\n"
    "s_mov_b32 s39, 1114112\n"
    "buffer_load_dwordx4 v112, s[80:83], s39 offen lds\n"
    "s_add_u32 m0, s38, 39104\n"
    "s_mov_b32 s39, 1146880\n"
    "buffer_load_dwordx4 v112, s[80:83], s39 offen lds\n"
    "v_add_u32 v116, 71680, v114\n"
    "ds_read_b64_tr_b16 a[144:145], v116 offset:0\n"
    "ds_read_b64_tr_b16 a[146:147], v116 offset:2176\n"
    "ds_read_b64_tr_b16 a[148:149], v116 offset:8960\n"
    "ds_read_b64_tr_b16 a[150:151], v116 offset:11136\n"
    "v_xor_b32 v116, 32, v116\n"
    "ds_read_b64_tr_b16 a[152:153], v116 offset:0\n"
    "ds_read_b64_tr_b16 a[154:155], v116 offset:2176\n"
    "ds_read_b64_tr_b16 a[156:157], v116 offset:8960\n"
    "ds_read_b64_tr_b16 a[158:159], v116 offset:11136\n"
    "s_waitcnt lgkmcnt(0)\n"
    "s_waitcnt vmcnt(13)\n"
    "s_barrier\n"
    "s_mul_i32 s38, s61, 4480\n"
    "s_add_u32 m0, s38, 53760\n"
    "s_mov_b32 s39, 1572864\n"
    "buffer_load_dwordx4 v112, s[80:83], s39 offen lds\n"
    "s_add_u32 m0, s38, 54848\n"
    "s_mov_b32 s39, 1605632\n"
    "buffer_load_dwordx4 v112, s[80:83], s39 offen lds\n"
    "s_add_u32 m0, s38, 55936\n"
    "s_mov_b32 s39, 1638400\n"
    "buffer_load_dwordx4 v112, s[80:83], s39 offen lds\n"
    "s_add_u32 m0, s38, 57024\n"
    "s_mov_b32 s39, 1671168\n"
    "buffer_load_dwordx4 v112, s[80:83], s39 offen lds\n"
    "v_add_u32 v116, 89600, v114\n"
    "ds_read_b64_tr_b16 a[160:161], v116 offset:0\n"
    "ds_read_b64_tr_b16 a[162:163], v116 offset:2176\n"
    "ds_read_b64_tr_b16 a[164:165], v116 offset:8960\n"
    "ds_read_b64_tr_b16 a[166:167], v116 offset:11136\n"
    "v_xor_b32 v116, 32, v116\n"
    "ds_read_b64_tr_b16 a[168:169], v116 offset:0\n"
    "ds_read_b64_tr_b16 a[170:171], v116 offset:2176\n"
    "ds_read_b64_tr_b16 a[172:173], v116 offset:8960\n"
    "ds_read_b64_tr_b16 a[174:175], v116 offset:11136\n"
    "s_waitcnt lgkmcnt(0)\n"
    "s_waitcnt vmcnt(13)\n"
    "s_barrier\n"
    "s_mul_i32 s38, s61, 4480\n"
    "s_add_u32 m0, s38, 71680\n"
    "s_mov_b32 s39, 2097152\n"
    "buffer_load_dwordx4 v112, s[80:83], s39 offen lds\n"
    "s_add_u32 m0, s38, 72768\n"
    "s_mov_b32 s39, 2129920\n"
    "buffer_load_dwordx4 v112, s[80:83], s39 offen lds\n"
    "s_add_u32 m0, s38, 73856\n"
    "s_mov_b32 s39, 2162688\n"
    "buffer_load_dwordx4 v112, s[80:83], s39 offen lds\n"
    "s_add_u32 m0, s38, 74944\n"
    "s_mov_b32 s39, 2195456\n"
    "buffer_load_dwordx4 v112, s[80:83], s39 offen lds\n"
    "v_add_u32 v116, 0, v114\n"
    "ds_read_b64_tr_b16 v[16:17], v116 offset:0\n"
    "ds_read_b64_tr_b16 v[18:19], v116 offset:2176\n"
    "ds_read_b64_tr_b16 v[20:21], v116 offset:8960\n"
    "ds_read_b64_tr_b16 v[22:23], v116 offset:11136\n"
    "v_xor_b32 v116, 32, v116\n"
    "ds_read_b64_tr_b16 v[24:25], v116 offset:0\n"
    "ds_read_b64_tr_b16 v[26:27], v116 offset:2176\n"
    "ds_read_b64_tr_b16 v[28:29], v116 offset:8960\n"
    "ds_read_b64_tr_b16 v[30:31], v116 offset:11136\n"
    "s_waitcnt lgkmcnt(0)\n"
    "s_waitcnt vmcnt(13)\n"
    "s_barrier\n"
    "s_mul_i32 s38, s61, 4480\n"
    "s_add_u32 m0, s38, 89600\n"
    "s_mov_b32 s39, 2621440\n"
    "buffer_load_dwordx4 v112, s[80:83], s39 offen lds\n"
    "s_add_u32 m0, s38, 90688\n"
    "s_mov_b32 s39, 2654208\n"
    "buffer_load_dwordx4 v112, s[80:83], s39 offen lds\n"
    "s_add_u32 m0, s38, 91776\n"
    "s_mov_b32 s39, 2686976\n"
    "buffer_load_dwordx4 v112, s[80:83], s39 offen lds\n"
    "s_add_u32 m0, s38, 92864\n"
    "s_mov_b32 s39, 2719744\n"
    "buffer_load_dwordx4 v112, s[80:83], s39 offen lds\n"
    "v_add_u32 v116, 17920, v114\n"
    "ds_read_b64_tr_b16 v[32:33], v116 offset:0\n"
    "ds_read_b64_tr_b16 v[34:35], v116 offset:2176\n"
    "ds_read_b64_tr_b16 v[36:37], v116 offset:8960\n"
    "ds_read_b64_tr_b16 v[38:39], v116 offset:11136\n"
    "v_xor_b32 v116, 32, v116\n"
    "ds_read_b64_tr_b16 v[40:41], v116 offset:0\n"
    "ds_read_b64_tr_b16 v[42:43], v116 offset:2176\n"
    "ds_read_b64_tr_b16 v[44:45], v116 offset:8960\n"
    "ds_read_b64_tr_b16 v[46:47], v116 offset:11136\n"
    "s_waitcnt lgkmcnt(0)\n"
    "s_waitcnt vmcnt(12)\n"
    "s_barrier\n"
    "s_mul_i32 s38, s61, 4480\n"
    "s_add_u32 m0, s38, 0\n"
    "s_mov_b32 s39, 3145728\n"
    "buffer_load_dwordx4 v112, s[80:83], s39 offen lds\n"
    "s_add_u32 m0, s38, 1088\n"
    "s_mov_b32 s39, 3178496\n"
    "buffer_load_dwordx4 v112, s[80:83], s39 offen lds\n"
    "s_add_u32 m0, s38, 2176\n"
    "s_mov_b32 s39, 3211264\n"
    "buffer_load_dwordx4 v112, s[80:83], s39 offen lds\n"
    "s_add_u32 m0, s38, 3264\n"
    "s_mov_b32 s39, 3244032\n"
    "buffer_load_dwordx4 v112, s[80:83], s39 offen lds\n"
    "v_add_u32 v116, 35840, v114\n"
    "ds_read_b64_tr_b16 v[48:49], v116 offset:0\n"
    "ds_read_b64_tr_b16 v[50:51], v116 offset:2176\n"
    "ds_read_b64_tr_b16 v[52:53], v116 offset:8960\n"
    "ds_read_b64_tr_b16 v[54:55], v116 offset:11136\n"
    "v_xor_b32 v116, 32, v116\n"
    "ds_read_b64_tr_b16 v[56:57], v116 offset:0\n"
    "ds_read_b64_tr_b16 v[58:59], v116 offset:2176\n"
    "ds_read_b64_tr_b16 v[60:61], v116 offset:8960\n"
    "ds_read_b64_tr_b16 v[62:63], v116 offset:11136\n"
    "s_waitcnt lgkmcnt(0)\n"
    "s_waitcnt vmcnt(12)\n"
    "s_barrier\n"
    "s_mul_i32 s38, s61, 4480\n"
    "s_add_u32 m0, s38, 17920\n"
    "s_mov_b32 s39, 3670016\n"
    "buffer_load_dwordx4 v112, s[80:83], s39 offen lds\n"
    "s_add_u32 m0, s38, 19008\n"
    "s_mov_b32 s39, 3702784\n"
    "buffer_load_dwordx4 v112, s[80:83], s39 offen lds\n"
    "s_add_u32 m0, s38, 20096\n"
    "s_mov_b32 s39, 3735552\n"
    "buffer_load_dwordx4 v112, s[80:83], s39 offen lds\n"
    "s_add_u32 m0, s38, 21184\n"
    "s_mov_b32 s39, 3768320\n"
    "buffer_load_dwordx4 v112, s[80:83], s39 offen lds\n"
    "v_add_u32 v116, 53760, v114\n"
    "ds_read_b64_tr_b16 a[0:1], v116 offset:0\n"
    "ds_read_b64_tr_b16 a[2:3], v116 offset:2176\n"
    "ds_read_b64_tr_b16 a[4:5], v116 offset:8960\n"
    "ds_read_b64_tr_b16 a[6:7], v116 offset:11136\n"
    "v_xor_b32 v116, 32, v116\n"
    "ds_read_b64_tr_b16 a[8:9], v116 offset:0\n"
    "ds_read_b64_tr_b16 a[10:11], v116 offset:2176\n"
    "ds_read_b64_tr_b16 a[12:13], v116 offset:8960\n"
    "ds_read_b64_tr_b16 a[14:15], v116 offset:11136\n"
    "s_waitcnt lgkmcnt(0)\n"
    "s_waitcnt vmcnt(12)\n"
    "s_barrier\n"
    "v_add_u32 v116, 71680, v114\n"
    "ds_read_b64_tr_b16 a[16:17], v116 offset:0\n"
    "ds_read_b64_tr_b16 a[18:19], v116 offset:2176\n"
    "ds_read_b64_tr_b16 a[20:21], v116 offset:8960\n"
    "ds_read_b64_tr_b16 a[22:23], v116 offset:11136\n"
    "v_xor_b32 v116, 32, v116\n"
    "ds_read_b64_tr_b16 a[24:25], v116 offset:0\n"
    "ds_read_b64_tr_b16 a[26:27], v116 offset:2176\n"
    "ds_read_b64_tr_b16 a[28:29], v116 offset:8960\n"
    "ds_read_b64_tr_b16 a[30:31], v116 offset:11136\n"
    "s_waitcnt lgkmcnt(0)\n"
    "s_waitcnt vmcnt(8)\n"
    "s_barrier\n"
    "v_add_u32 v116, 89600, v114\n"
    "ds_read_b64_tr_b16 a[32:33], v116 offset:0\n"
    "ds_read_b64_tr_b16 a[34:35], v116 offset:2176\n"
    "ds_read_b64_tr_b16 a[36:37], v116 offset:8960\n"
    "ds_read_b64_tr_b16 a[38:39], v116 offset:11136\n"
    "v_xor_b32 v116, 32, v116\n"
    "ds_read_b64_tr_b16 a[40:41], v116 offset:0\n"
    "ds_read_b64_tr_b16 a[42:43], v116 offset:2176\n"
    "ds_read_b64_tr_b16 a[44:45], v116 offset:8960\n"
    "ds_read_b64_tr_b16 a[46:47], v116 offset:11136\n"
    "s_waitcnt lgkmcnt(0)\n"
    "s_mov_b32 s37, 0x80000\n"
    "LRY%=:\n"
    "v_readfirstlane_b32 s44, v117\n"
    "s_cmp_lg_u32 s44, 0\n"
    "s_cbranch_scc1 LRZ%=\n"
    "global_load_dword v117, v118, s[92:93] sc1\n"
    "s_waitcnt vmcnt(0)\n"
    "s_sleep 1\n"
    "s_sub_u32 s37, s37, 1\n"
    "s_cbranch_scc0 LRY%=\n"
    "s_branch LFAIL%=\n"
    "LRZ%=:\n"
    "s_lshl_b32 s38, s44, 13\n"
    "s_and_b32 s38, s38, 0x2000\n"
    "s_xor_b32 s46, s46, s38\n"
    "s_xor_b32 s47, s47, s38\n"
    "s_xor_b32 s48, s48, s38\n"
    "s_xor_b32 s56, s56, s38\n"
    "s_xor_b32 s74, s74, s38\n"
    "s_xor_b32 s58, s58, s38\n"
    "s_xor_b32 s59, s59, s38\n"
    "s_xor_b32 s67, s67, s38\n"
    "s_xor_b32 s69, s69, s38\n"
    "s_sub_u32 s58, s58, 4\n"
    "s_mov_b64 exec, 7\n"
    "buffer_load_dword v119, v0, s[16:19], s59 offen sc1\n"
    "s_mov_b64 exec, -1\n"
    "s_waitcnt vmcnt(5)\n"
    "s_barrier\n"
    "v_add_u32 v116, 0, v114\n"
    "ds_read_b64_tr_b16 a[48:49], v116 offset:0\n"
    "ds_read_b64_tr_b16 a[50:51], v116 offset:2176\n"
    "ds_read_b64_tr_b16 a[52:53], v116 offset:8960\n"
    "ds_read_b64_tr_b16 a[54:55], v116 offset:11136\n"
    "v_xor_b32 v116, 32, v116\n"
    "ds_read_b64_tr_b16 a[56:57], v116 offset:0\n"
    "ds_read_b64_tr_b16 a[58:59], v116 offset:2176\n"
    "ds_read_b64_tr_b16 a[60:61], v116 offset:8960\n"
    "ds_read_b64_tr_b16 a[62:63], v116 offset:11136\n"
    "s_waitcnt lgkmcnt(0)\n"
    "s_waitcnt vmcnt(1)\n"
    "s_barrier\n"
    "v_add_u32 v116, 17920, v114\n"
    "ds_read_b64_tr_b16 a[64:65], v116 offset:0\n"
    "ds_read_b64_tr_b16 a[66:67], v116 offset:2176\n"
    "ds_read_b64_tr_b16 a[68:69], v116 offset:8960\n"
    "ds_read_b64_tr_b16 a[70:71], v116 offset:11136\n"
    "v_xor_b32 v116, 32, v116\n"
    "ds_read_b64_tr_b16 a[72:73], v116 offset:0\n"
    "ds_read_b64_tr_b16 a[74:75], v116 offset:2176\n"
    "ds_read_b64_tr_b16 a[76:77], v116 offset:8960\n"
    "ds_read_b64_tr_b16 a[78:79], v116 offset:11136\n"
    "s_waitcnt lgkmcnt(0)\n"
    "s_barrier\n"
    "ds_write_b128 v7, v[16:19] offset:0\n"
    "ds_write_b128 v7, v[20:23] offset:1024\n"
    "ds_write_b128 v7, v[24:27] offset:2048\n"
    "ds_write_b128 v7, v[28:31] offset:3072\n"
    "ds_write_b128 v7, v[32:35] offset:4096\n"
    "ds_write_b128 v7, v[36:39] offset:5120\n"
    "ds_write_b128 v7, v[40:43] offset:6144\n"
    "ds_write_b128 v7, v[44:47] offset:7168\n"
    "ds_write_b128 v7, v[48:51] offset:8192\n"
    "ds_write_b128 v7, v[52:55] offset:9216\n"
    "ds_write_b128 v7, v[56:59] offset:10240\n"
    "ds_write_b128 v7, v[60:63] offset:11264\n"
    "ds_write_b128 v7, v[64:67] offset:12288\n"
    "ds_write_b128 v7, v[68:71] offset:13312\n"
    "ds_write_b128 v7, v[72:75] offset:14336\n"
    "s_waitcnt lgkmcnt(11)\n"
    "ds_write_b128 v7, v[76:79] offset:15360\n"
    "ds_write_b128 v7, v[80:83] offset:16384\n"
    "ds_write_b128 v7, v[84:87] offset:17408\n"
    "ds_write_b128 v7, v[88:91] offset:18432\n"
    "s_waitcnt lgkmcnt(11)\n"
    "ds_write_b128 v7, v[92:95] offset:19456\n"
    "ds_write_b128 v7, v[96:99] offset:20480\n"
    "ds_write_b128 v7, v[100:103] offset:21504\n"
    "ds_write_b128 v7, v[104:107] offset:22528\n"
    "s_waitcnt lgkmcnt(11)\n"
    "ds_write_b128 v7, v[108:111] offset:23552\n"
    "s_waitcnt lgkmcnt(0)\n"
    "ds_read_b128 v[16:19], v9 offset:0\n"
    "ds_read_b128 v[20:23], v9 offset:1024\n"
    "ds_read_b128 v[24:27], v9 offset:8192\n"
    "ds_read_b128 v[28:31], v9 offset:9216\n"
    "ds_read_b128 v[32:35], v9 offset:16384\n"
    "ds_read_b128 v[36:39], v9 offset:17408\n"
    "s_waitcnt lgkmcnt(5)\n"
    "ds_write_b128 v7, v[16:19] offset:24576\n"
    "s_waitcnt lgkmcnt(5)\n"
    "ds_write_b128 v7, v[20:23] offset:25600\n"
    "s_waitcnt lgkmcnt(5)\n"
    "ds_write_b128 v7, v[24:27] offset:26624\n"
    "s_waitcnt lgkmcnt(5)\n"
    "ds_write_b128 v7, v[28:31] offset:27648\n"
    "s_waitcnt lgkmcnt(5)\n"
    "ds_write_b128 v7, v[32:35] offset:28672\n"
    "s_waitcnt lgkmcnt(5)\n"
    "ds_write_b128 v7, v[36:39] offset:29696\n"
    "v_lshrrev_b32 v64, 4, v0\n"
    "v_lshrrev_b32 v65, 4, v64\n"
    "v_and_b32 v64, 15, v64\n"
    "v_lshlrev_b32 v65, 2, v65\n"
    "v_sub_u32 v64, v64, v65\n"
    "s_and_b32 s38, s61, 1\n"
    "s_lshl_b32 s38, s38, 4\n"
    "v_add_u32 v64, s38, v64\n"
    "v_cmp_le_i32 s[80:81], 0, v64\n"
    "v_cmp_le_i32 s[82:83], 1, v64\n"
    "v_cmp_le_i32 s[84:85], 2, v64\n"
    "v_cmp_le_i32 s[86:87], 3, v64\n"
    "v_cmp_le_i32 s[88:89], 16, v64\n"
    "v_cmp_le_i32 s[90:91], 17, v64\n"
    "v_cmp_le_i32 s[92:93], 18, v64\n"
    "v_cmp_le_i32 s[94:95], 19, v64\n"
    "v_mov_b32 v64, 158224\n"
    "v_mov_b64 v[66:67], 0\n"
    "ds_write_b64 v64, v[66:67] offset:16\n"
    "s_mov_b32 s54, 0x80000000\n"
    "s_waitcnt vmcnt(0)\n"
    "v_mov_b32 v116, v119\n"
    "v_cmp_ne_u32 vcc, 0, v116\n"
    "v_readfirstlane_b32 s70, v116\n"
    "s_nop 0\n"
    "s_andn2_b32 s38, 7, vcc_lo\n"
    "s_cbranch_scc0 LS0K%=\n"
    "s_mov_b32 s37, 0x80000\n"
    "LS0W%=:\n"
    "s_mov_b64 exec, 7\n"
    "buffer_load_dword v116, v0, s[16:19], s59 offen sc1\n"
    "s_mov_b64 exec, -1\n"
    "s_waitcnt vmcnt(0)\n"
    "v_cmp_ne_u32 vcc, 0, v116\n"
    "v_readfirstlane_b32 s70, v116\n"
    "s_nop 0\n"
    "s_andn2_b32 s38, 7, vcc_lo\n"
    "s_cbranch_scc0 LS0K%=\n"
    "s_sleep 2\n"
    "s_sub_u32 s37, s37, 1\n"
    "s_cbranch_scc0 LS0W%=\n"
    "s_branch LFAIL%=\n"
    "LS0K%=:\n"
    "s_add_u32 s53, s50, s55\n"
    "s_add_u32 s49, s48, s62\n"
    "s_add_u32 s51, s53, 0\n"
    "s_lshr_b32 s38, s51, 8\n"
    "s_xor_b32 s38, s38, s56\n"
    "s_and_b32 s38, s38, 0x2000\n"
    "s_or_b32 s51, s51, s38\n"
    "s_add_u32 s52, s53, 524288\n"
    "s_lshr_b32 s38, s52, 8\n"
    "s_xor_b32 s38, s38, s56\n"
    "s_and_b32 s38, s38, 0x2000\n"
    "s_or_b32 s52, s52, s38\n"
    "s_add_u32 s53, s53, 1048576\n"
    "s_lshr_b32 s38, s53, 8\n"
    "s_xor_b32 s38, s38, s56\n"
    "s_and_b32 s38, s38, 0x2000\n"
    "s_or_b32 s53, s53, s38\n"
    "s_add_u32 s59, s59, 128\n"
    "s_waitcnt lgkmcnt(0)\n"
    "s_barrier\n"
    "s_cmp_lg_u32 s61, 1\n"
    "s_cbranch_scc1 LBM%=\n"
    "s_sub_u32 s51, s51, 2048\n"
    "s_mov_b32 m0, 122880\n"
    "s_nop 0\n"
    "buffer_load_dwordx4 v0, s[16:19], s51 offen offset:0 sc1 lds\n"
    "buffer_load_dwordx4 v0, s[16:19], s51 offen offset:1024 sc1 lds\n"
    "buffer_load_dwordx4 v0, s[16:19], s51 offen offset:2048 sc1 lds\n"
    "buffer_load_dwordx4 v0, s[16:19], s51 offen offset:3072 sc1 lds\n"
    "s_mov_b32 m0, 126976\n"
    "s_add_u32 s51, s51, 4096\n"
    "buffer_load_dwordx4 v0, s[16:19], s51 offen offset:0 sc1 lds\n"
    "buffer_load_dwordx4 v0, s[16:19], s51 offen offset:1024 sc1 lds\n"
    "buffer_load_dwordx4 v0, s[16:19], s51 offen offset:2048 sc1 lds\n"
    "buffer_load_dwordx4 v0, s[16:19], s51 offen offset:3072 sc1 lds\n"
    "s_mov_b32 s44, s45\n"
    "v_mov_b32 v112, 158224\n"
    "v_mov_b32 v113, s44\n"
    "ds_write_b32 v112, v113\n"
    "s_waitcnt lgkmcnt(0)\n"
    "s_branch LMO%=\n"
    "LBM%=:\n"
    "s_waitcnt lgkmcnt(0)\n"
    "s_barrier\n"
    "s_add_u32 m0, s62, 147456\n"
    "s_nop 0\n"
    "buffer_load_dwordx4 v0, s[16:19], s49 offen offset:0 sc1 lds\n"
    "buffer_load_dwordx4 v0, s[16:19], s49 offen offset:1024 sc1 lds\n"
    "v_mov_b32 v112, 158224\n"
    "ds_read_b64 v[114:115], v112 offset:0\n"
    "ds_read_b128 v[16:19], v9 offset:16384\n"
    "ds_read_b128 v[20:23], v9 offset:17408\n"
    "s_waitcnt lgkmcnt(2)\n"
    "v_readfirstlane_b32 s39, v114\n"
    "v_readfirstlane_b32 s44, v115\n"
    "s_sub_u32 s38, s39, s44\n"
    "s_cmp_gt_i32 s38, 0\n"
    "s_cbranch_scc1 LSL%=\n"
    "s_add_u32 m0, s62, 131072\n"
    "s_nop 0\n"
    "buffer_load_dwordx4 v0, s[16:19], s52 offen offset:0 sc1 lds\n"
    "buffer_load_dwordx4 v0, s[16:19], s52 offen offset:1024 sc1 lds\n"
    "v_lshrrev_b32 v113, 4, v0\n"
    "v_and_b32 v113, 0x30, v113\n"
    "v_lshl_add_u32 v113, v113, 4, v0\n"
    "ds_read_b128 v[64:67], v7 offset:0\n"
    "ds_read_b128 v[68:71], v7 offset:1024\n"
    "ds_read_b128 v[72:75], v7 offset:2048\n"
    "ds_read_b128 v[76:79], v7 offset:3072\n"
    "ds_read_b128 v[80:83], v7 offset:4096\n"
    "ds_read_b128 v[84:87], v7 offset:5120\n"
    "ds_read_b128 v[88:91], v7 offset:6144\n"
    "ds_read_b128 v[92:95], v7 offset:7168\n"
    "ds_read_b128 v[96:99], v7 offset:8192\n"
    "ds_read_b128 v[100:103], v7 offset:9216\n"
    "ds_read_b128 v[104:107], v7 offset:10240\n"
    "ds_read_b128 v[108:111], v7 offset:11264\n"
    "v_mov_b64 v[114:115], 0\n"
    "s_nop 1\n"
    "v_mfma_f32_32x32x8_bf16 v[48:63], v[114:115], v[114:115], 0\n"
    "s_waitcnt lgkmcnt(13)\n"
    "buffer_store_dwordx4 v[16:19], v113, s[16:19], s54 offen offset:0 sc1\n"
    "s_waitcnt lgkmcnt(12)\n"
    "buffer_store_dwordx4 v[20:23], v113, s[16:19], s54 offen offset:2048 sc1\n"
    "s_waitcnt vmcnt(4)\n"
    "s_waitcnt lgkmcnt(0)\n"
    "LC%=:\n"
    "s_barrier\n"
    "ds_read_b128 v[16:19], v2 offset:0\n"
    "ds_read_b128 v[20:23], v2 offset:1024\n"
    "ds_read_b128 v[24:27], v2 offset:2048\n"
    "s_add_u32 m0, s62, 139264\n"
    "s_nop 0\n"
    "buffer_load_dwordx4 v0, s[16:19], s53 offen offset:0 sc1 lds\n"
    "buffer_load_dwordx4 v0, s[16:19], s53 offen offset:1024 sc1 lds\n"
    "s_waitcnt lgkmcnt(2)\n"
    "v_smfmac_f32_16x16x64_bf16 v[48:51], v[16:19], v[64:71], v1\n"
    "ds_read_b128 v[28:31], v2 offset:3072\n"
    "v_smfmac_f32_16x16x64_bf16 v[52:55], v[16:19], v[72:79], v1\n"
    "ds_read_b128 v[32:35], v2 offset:4096\n"
    "s_waitcnt lgkmcnt(3)\n"
    "v_smfmac_f32_16x16x64_bf16 v[48:51], v[20:23], v[80:87], v1\n"
    "ds_read_b128 v[36:39], v2 offset:5120\n"
    "v_smfmac_f32_16x16x64_bf16 v[52:55], v[20:23], v[88:95], v1\n"
    "ds_read_b128 v[40:43], v2 offset:6144\n"
    "s_waitcnt lgkmcnt(4)\n"
    "v_smfmac_f32_16x16x64_bf16 v[48:51], v[24:27], v[96:103], v1\n"
    "ds_read_b128 v[44:47], v2 offset:7168\n"
    "v_max3_u32 v112, v19, v23, v27\n"
    "v_smfmac_f32_16x16x64_bf16 v[52:55], v[24:27], v[104:111], v1\n"
    "ds_read_b128 v[64:67], v7 offset:12288\n"
    "s_waitcnt lgkmcnt(5)\n"
    "v_smfmac_f32_16x16x64_bf16 v[48:51], v[28:31], a[0:7], v1\n"
    "ds_read_b128 v[68:71], v7 offset:13312\n"
    "s_waitcnt lgkmcnt(5)\n"
    "v_max3_u32 v112, v112, v31, v35\n"
    "v_smfmac_f32_16x16x64_bf16 v[52:55], v[28:31], a[8:15], v1\n"
    "ds_read_b128 v[72:75], v7 offset:14336\n"
    "v_smfmac_f32_16x16x64_bf16 v[48:51], v[32:35], a[16:23], v1\n"
    "ds_read_b128 v[76:79], v7 offset:15360\n"
    "s_waitcnt lgkmcnt(5)\n"
    "v_max3_u32 v112, v112, v39, v43\n"
    "v_smfmac_f32_16x16x64_bf16 v[52:55], v[32:35], a[24:31], v1\n"
    "ds_read_b128 v[80:83], v7 offset:16384\n"
    "v_smfmac_f32_16x16x64_bf16 v[48:51], v[36:39], a[32:39], v1\n"
    "ds_read_b128 v[84:87], v7 offset:17408\n"
    "s_waitcnt lgkmcnt(6)\n"
    "v_max_u32 v112, v112, v47\n"
    "v_smfmac_f32_16x16x64_bf16 v[52:55], v[36:39], a[40:47], v1\n"
    "ds_read_b128 v[88:91], v7 offset:18432\n"
    "v_smfmac_f32_16x16x64_bf16 v[48:51], v[40:43], a[48:55], v1\n"
    "ds_read_b128 v[92:95], v7 offset:19456\n"
    "v_cmp_eq_u32 vcc, -1, v112\n"
    "v_smfmac_f32_16x16x64_bf16 v[52:55], v[40:43], a[56:63], v1\n"
    "v_smfmac_f32_16x16x64_bf16 v[48:51], v[44:47], a[64:71], v1\n"
    "s_cbranch_vccnz LMR%=\n"
    "v_smfmac_f32_16x16x64_bf16 v[52:55], v[44:47], a[72:79], v1\n"
    "s_waitcnt lgkmcnt(6)\n"
    "v_smfmac_f32_16x16x64_bf16 v[56:59], v[16:19], v[64:71], v1\n"
    "s_waitcnt lgkmcnt(4)\n"
    "v_smfmac_f32_16x16x64_bf16 v[60:63], v[16:19], v[72:79], v1\n"
    "s_waitcnt lgkmcnt(2)\n"
    "v_add_f32 v48, v48, v50\n"
    "v_add_f32 v52, v52, v54\n"
    "v_smfmac_f32_16x16x64_bf16 v[56:59], v[20:23], v[80:87], v1\n"
    "s_waitcnt lgkmcnt(0)\n"
    "v_add_f32 v49, v49, v51\n"
    "v_add_f32 v53, v53, v55\n"
    "v_smfmac_f32_16x16x64_bf16 v[60:63], v[20:23], v[88:95], v1\n"
    "v_cvt_pk_bf16_f32 v48, v48, v52\n"
    "v_smfmac_f32_16x16x64_bf16 v[56:59], v[24:27], a[80:87], v1\n"
    "v_cvt_pk_bf16_f32 v49, v49, v53\n"
    "v_smfmac_f32_16x16x64_bf16 v[60:63], v[24:27], a[88:95], v1\n"
    "ds_write2_b32 v3, v48, v49 offset0:0 offset1:72\n"
    "v_add_u32 v117, s20, v5\n"
    "v_smfmac_f32_16x16x64_bf16 v[56:59], v[28:31], a[96:103], v1\n"
    "v_mov_b64 v[50:51], 0\n"
    "v_smfmac_f32_16x16x64_bf16 v[60:63], v[28:31], a[104:111], v1\n"
    "v_mov_b64 v[52:53], 0\n"
    "v_smfmac_f32_16x16x64_bf16 v[56:59], v[32:35], a[112:119], v1\n"
    "v_mov_b64 v[54:55], 0\n"
    "v_smfmac_f32_16x16x64_bf16 v[60:63], v[32:35], a[120:127], v1\n"
    "s_waitcnt lgkmcnt(0)\n"
    "s_barrier\n"
    "v_smfmac_f32_16x16x64_bf16 v[56:59], v[36:39], a[128:135], v1\n"
    "ds_read_b128 v[80:83], v117 offset:0\n"
    "ds_read_b128 v[84:87], v117 offset:64\n"
    "v_smfmac_f32_16x16x64_bf16 v[60:63], v[36:39], a[136:143], v1\n"
    "ds_read_b128 v[88:91], v9 offset:0\n"
    "ds_read_b128 v[92:95], v9 offset:1024\n"
    "v_smfmac_f32_16x16x64_bf16 v[56:59], v[40:43], a[144:151], v1\n"
    "ds_read_b128 v[96:99], v5 offset:0\n"
    "v_mov_b64 v[48:49], 0\n"
    "v_smfmac_f32_16x16x64_bf16 v[56:59], v[44:47], a[160:167], v1\n"
    "ds_read_b128 v[100:103], v5 offset:64\n"
    "v_smfmac_f32_16x16x64_bf16 v[60:63], v[40:43], a[152:159], v1\n"
    "ds_read_b128 v[104:107], v5 offset:128\n"
    "v_smfmac_f32_16x16x64_bf16 v[60:63], v[44:47], a[168:175], v1\n"
    "ds_read_b128 v[108:111], v5 offset:192\n"
    "s_waitcnt lgkmcnt(5)\n"
    "v_add_f32 v56, v56, v58\n"
    "v_mfma_f32_16x16x32_bf16 v[64:67], v[88:91], v[80:83], 0\n"
    "s_waitcnt lgkmcnt(4)\n"
    "v_add_f32 v57, v57, v59\n"
    "v_mfma_f32_16x16x32_bf16 v[64:67], v[92:95], v[84:87], v[64:67]\n"
    "v_add_f32 v60, v60, v62\n"
    "v_add_f32 v61, v61, v63\n"
    "v_cvt_pk_bf16_f32 v56, v56, v60\n"
    "v_cvt_pk_bf16_f32 v57, v57, v61\n"
    "v_smfmac_f32_16x16x64_bf16 v[48:51], v[16:19], a[176:183], v1\n"
    "ds_write2_b32 v4, v56, v57 offset0:0 offset1:72\n"
    "v_smfmac_f32_16x16x64_bf16 v[52:55], v[16:19], a[184:191], v1\n"
    "ds_write_b128 v8, v[64:67]\n"
    "v_smfmac_f32_16x16x64_bf16 v[48:51], v[20:23], a[192:199], v1\n"
    "v_mov_b64 v[60:61], s[14:15]\n"
    "v_smfmac_f32_16x16x64_bf16 v[52:55], v[20:23], a[200:207], v1\n"
    "v_mov_b64 v[62:63], s[14:15]\n"
    "v_smfmac_f32_16x16x64_bf16 v[48:51], v[24:27], a[208:215], v1\n"
    "v_smfmac_f32_16x16x64_bf16 v[52:55], v[24:27], a[216:223], v1\n"
    "s_waitcnt lgkmcnt(0)\n"
    "s_barrier\n"
    "ds_read_b128 v[64:67], v6 offset:0\n"
    "ds_read_b128 v[88:91], v15 offset:0\n"
    "v_smfmac_f32_16x16x64_bf16 v[48:51], v[28:31], a[224:231], v1\n"
    "ds_read_b128 v[92:95], v15 offset:512\n"
    "ds_read_b128 v[68:71], v6 offset:64\n"
    "v_smfmac_f32_16x16x64_bf16 v[52:55], v[28:31], a[232:239], v1\n"
    "ds_read_b128 v[72:75], v6 offset:128\n"
    "v_smfmac_f32_16x16x64_bf16 v[48:51], v[32:35], a[240:247], v1\n"
    "ds_read_b128 v[76:79], v6 offset:192\n"
    "s_waitcnt lgkmcnt(5)\n"
    "v_mfma_f32_16x16x32_bf16 v[112:115], v[64:67], v[96:99], 0\n"
    "s_waitcnt lgkmcnt(3)\n"
    "s_waitcnt lgkmcnt(2)\n"
    "v_add_f32 v88, v88, v92\n"
    "v_add_f32 v89, v89, v93\n"
    "v_mfma_f32_16x16x32_bf16 v[112:115], v[68:71], v[100:103], v[112:115]\n"
    "s_waitcnt lgkmcnt(1)\n"
    "v_add_f32 v90, v90, v94\n"
    "v_add_f32 v91, v91, v95\n"
    "v_mfma_f32_16x16x32_bf16 v[112:115], v[72:75], v[104:107], v[112:115]\n"
    "s_waitcnt lgkmcnt(0)\n"
    "v_max3_f32 v117, v88, v89, v90\n"
    "v_max_f32 v117, v117, v91\n"
    "v_mfma_f32_16x16x32_bf16 v[112:115], v[76:79], v[108:111], v[112:115]\n"
    "s_nop 0\n"
    "v_max_f32_dpp v116, v117, v117 row_ror:8 row_mask:0xf bank_mask:0xf\n"
    "v_mov_b32 v117, v116\n"
    "v_smfmac_f32_16x16x64_bf16 v[52:55], v[32:35], a[248:255], v1\n"
    "s_nop 0\n"
    "v_permlane32_swap_b32 v116, v117\n"
    "v_smfmac_f32_16x16x64_bf16 v[48:51], v[36:39], v[120:127], v1\n"
    "v_max_f32 v116, v116, v117\n"
    "v_smfmac_f32_16x16x64_bf16 v[48:51], v[40:43], v[136:143], v1\n"
    "v_max3_f32 v117, v112, v113, v116\n"
    "v_smfmac_f32_16x16x64_bf16 v[48:51], v[44:47], v[152:159], v1\n"
    "v_max3_f32 v117, v114, v115, v117\n"
    "v_smfmac_f32_16x16x64_bf16 v[52:55], v[36:39], v[128:135], v1\n"
    "v_mov_b32 v118, v117\n"
    "v_smfmac_f32_16x16x64_bf16 v[52:55], v[40:43], v[144:151], v1\n"
    "s_waitcnt vmcnt(4)\n"
    "ds_read_b128 v[80:83], v9 offset:8192\n"
    "v_permlane16_swap_b32 v117, v118\n"
    "v_smfmac_f32_16x16x64_bf16 v[52:55], v[44:47], v[160:167], v1\n"
    "ds_read_b128 v[84:87], v9 offset:9216\n"
    "v_max_f32 v117, v117, v118\n"
    "v_mul_f32 v117, 0xbe0293ee, v117\n"
    "v_fma_f32 v88, v88, s65, v117\n"
    "v_fma_f32 v89, v89, s65, v117\n"
    "v_fma_f32 v90, v90, s65, v117\n"
    "v_fma_f32 v91, v91, s65, v117\n"
    "v_fma_f32 v112, v112, s65, v117\n"
    "v_fma_f32 v113, v113, s65, v117\n"
    "v_exp_f32 v88, v88\n"
    "v_exp_f32 v89, v89\n"
    "v_exp_f32 v90, v90\n"
    "v_exp_f32 v91, v91\n"
    "v_cvt_pk_bf16_f32 v56, v88, v89\n"
    "v_cvt_pk_bf16_f32 v57, v90, v91\n"
    "v_exp_f32 v112, v112\n"
    "v_mov_b32_dpp v58, v56 row_shl:8 row_mask:0xf bank_mask:0xf bound_ctrl:1\n"
    "v_mov_b32_dpp v59, v57 row_shl:8 row_mask:0xf bank_mask:0xf bound_ctrl:1\n"
    "v_exp_f32 v113, v113\n"
    "v_mov_b32 v93, 0\n"
    "v_mfma_f32_16x16x32_bf16 v[104:107], v[60:63], v[56:59], 0\n"
    "s_waitcnt lgkmcnt(1)\n"
    "v_cvt_pk_bf16_f32 v92, v112, v113\n"
    "v_add_f32 v48, v48, v50\n"
    "v_mfma_f32_16x16x32_bf16 v[96:99], v[80:83], v[56:59], 0\n"
    "v_add_f32 v49, v49, v51\n"
    "v_cvt_pk_bf16_f32 v48, v48, v49\n"
    "v_mfma_f32_16x16x16_bf16 v[104:107], v[60:61], v[92:93], v[104:107]\n"
    "s_waitcnt lgkmcnt(0)\n"
    "v_add_f32 v52, v52, v54\n"
    "v_add_f32 v53, v53, v55\n"
    "v_mfma_f32_16x16x32_bf16 v[100:103], v[84:87], v[56:59], 0\n"
    "v_cvt_pk_bf16_f32 v49, v52, v53\n"
    "v_cvt_pk_bf16_f32 v95, v112, v113\n"
    "v_mov_b32 v94, 0\n"
    "v_mfma_f32_16x16x16_bf16 v[96:99], v[48:49], v[92:93], v[96:99]\n"
    "v_rcp_f32 v104, v104\n"
    "v_mfma_f32_16x16x16_bf16 v[100:103], v[48:49], v[94:95], v[100:103]\n"
    "ds_write_b64 v10, v[48:49]\n"
    "v_mov_b64 v[114:115], 0\n"
    "s_mov_b64 exec, s[78:79]\n"
    "s_nop 0\n"
    "v_pk_mul_f32 v[96:97], v[96:97], v[104:105] op_sel_hi:[1,0]\n"
    "v_pk_mul_f32 v[98:99], v[98:99], v[104:105] op_sel_hi:[1,0]\n"
    "v_cvt_pk_bf16_f32 v96, v96, v97\n"
    "v_cvt_pk_bf16_f32 v97, v98, v99\n"
    "ds_write_b64 v11, v[96:97] offset:0\n"
    "v_pk_mul_f32 v[100:101], v[100:101], v[104:105] op_sel_hi:[1,0]\n"
    "v_pk_mul_f32 v[102:103], v[102:103], v[104:105] op_sel_hi:[1,0]\n"
    "v_cvt_pk_bf16_f32 v98, v100, v101\n"
    "v_cvt_pk_bf16_f32 v99, v102, v103\n"
    "ds_write_b64 v11, v[98:99] offset:8\n"
    "s_mov_b64 exec, 7\n"
    "buffer_load_dword v116, v0, s[16:19], s59 offen sc1\n"
    "s_mov_b64 exec, -1\n"
    "v_mfma_f32_32x32x8_bf16 v[72:87], v[114:115], v[114:115], 0\n"
    "s_add_u32 s36, s36, 1\n"
    "s_cmp_eq_u32 s36, 64\n"
    "s_waitcnt lgkmcnt(0)\n"
    "s_waitcnt vmcnt(1)\n"
    "s_barrier\n"
    "ds_read_b128 v[104:107], v12 offset:0\n"
    "ds_read_b128 v[108:111], v12 offset:1024\n"
    "v_mfma_f32_32x32x8_bf16 v[48:63], v[114:115], v[114:115], 0\n"
    "s_cbranch_scc1 LEND%=\n"
    "s_waitcnt lgkmcnt(1)\n"
    "v_smfmac_f32_16x16x64_bf16 v[72:75], v[104:107], v[168:175], v1\n"
    "ds_read_b128 v[64:67], v7 offset:20480\n"
    "s_waitcnt lgkmcnt(1)\n"
    "v_smfmac_f32_16x16x64_bf16 v[72:75], v[108:111], v[176:183], v1\n"
    "ds_read_b128 v[68:71], v7 offset:21504\n"
    "v_smfmac_f32_16x16x64_bf16 v[76:79], v[104:107], v[184:191], v1\n"
    "ds_read_b128 v[16:19], v7 offset:22528\n"
    "v_smfmac_f32_16x16x64_bf16 v[76:79], v[108:111], v[192:199], v1\n"
    "ds_read_b128 v[20:23], v7 offset:23552\n"
    "v_add_f32 v72, v72, v74\n"
    "v_smfmac_f32_16x16x64_bf16 v[80:83], v[104:107], v[200:207], v1\n"
    "ds_read_b128 v[24:27], v7 offset:24576\n"
    "v_add_f32 v73, v73, v75\n"
    "v_smfmac_f32_16x16x64_bf16 v[80:83], v[108:111], v[208:215], v1\n"
    "ds_read_b128 v[28:31], v7 offset:25600\n"
    "v_add_f32 v76, v76, v78\n"
    "v_smfmac_f32_16x16x64_bf16 v[84:87], v[104:107], v[216:223], v1\n"
    "ds_read_b128 v[32:35], v7 offset:26624\n"
    "v_add_f32 v77, v77, v79\n"
    "v_smfmac_f32_16x16x64_bf16 v[84:87], v[108:111], v[224:231], v1\n"
    "ds_read_b128 v[36:39], v7 offset:27648\n"
    "v_cvt_pk_bf16_f32 v88, v72, v76\n"
    "v_cvt_pk_bf16_f32 v90, v73, v77\n"
    "v_smfmac_f32_16x16x64_bf16 v[48:51], v[104:107], v[232:239], v1\n"
    "ds_read_b128 v[40:43], v7 offset:28672\n"
    "v_add_f32 v80, v80, v82\n"
    "v_add_f32 v81, v81, v83\n"
    "v_smfmac_f32_16x16x64_bf16 v[48:51], v[108:111], v[240:247], v1\n"
    "ds_read_b128 v[44:47], v7 offset:29696\n"
    "v_add_f32 v85, v85, v87\n"
    "v_add_f32 v84, v84, v86\n"
    "v_cvt_pk_bf16_f32 v91, v81, v85\n"
    "v_cvt_pk_bf16_f32 v89, v80, v84\n"
    "v_and_or_b32 v91, v91, -2, s64\n"
    "buffer_store_dwordx4 v[88:91], v13, s[16:19], s46 offen sc1\n"
    "v_smfmac_f32_16x16x64_bf16 v[52:55], v[104:107], v[248:255], v1\n"
    "s_waitcnt lgkmcnt(8)\n"
    "v_smfmac_f32_16x16x64_bf16 v[52:55], v[108:111], v[64:71], v1\n"
    "s_waitcnt lgkmcnt(6)\n"
    "v_add_f32 v48, v48, v50\n"
    "v_smfmac_f32_16x16x64_bf16 v[56:59], v[104:107], v[16:23], v1\n"
    "s_waitcnt lgkmcnt(4)\n"
    "v_add_f32 v49, v49, v51\n"
    "v_smfmac_f32_16x16x64_bf16 v[56:59], v[108:111], v[24:31], v1\n"
    "s_waitcnt lgkmcnt(2)\n"
    "v_add_f32 v52, v52, v54\n"
    "v_add_f32 v53, v53, v55\n"
    "v_smfmac_f32_16x16x64_bf16 v[60:63], v[104:107], v[32:39], v1\n"
    "s_waitcnt lgkmcnt(0)\n"
    "v_cvt_pk_bf16_f32 v92, v48, v52\n"
    "v_cvt_pk_bf16_f32 v94, v49, v53\n"
    "v_smfmac_f32_16x16x64_bf16 v[60:63], v[108:111], v[40:47], v1\n"
    "v_add_f32 v56, v56, v58\n"
    "v_add_f32 v57, v57, v59\n"
    "s_nop 5\n"
    "v_add_f32 v61, v61, v63\n"
    "v_add_f32 v60, v60, v62\n"
    "v_cvt_pk_bf16_f32 v95, v57, v61\n"
    "v_cvt_pk_bf16_f32 v93, v56, v60\n"
    "v_and_or_b32 v95, v95, -2, s64\n"
    "buffer_store_dwordx4 v[92:95], v13, s[16:19], s47 offen sc1\n"
    "s_add_u32 s54, s50, s57\n"
    "s_lshr_b32 s38, s54, 8\n"
    "s_xor_b32 s38, s38, s74\n"
    "s_and_b32 s38, s38, 0x2000\n"
    "s_or_b32 s54, s54, s38\n"
    "s_add_u32 s50, s50, 2621440\n"
    "s_add_u32 s48, s48, 16384\n"
    "s_cmp_eq_u32 s61, 1\n"
    "s_cbranch_scc1 LW1%=\n"
    "s_cmp_gt_u32 s61, 1\n"
    "s_cbranch_scc1 LW2%=\n"
    "s_add_u32 s53, s50, s55\n"
    "s_add_u32 s49, s48, s62\n"
    "s_add_u32 s51, s53, 0\n"
    "s_lshr_b32 s38, s51, 8\n"
    "s_xor_b32 s38, s38, s56\n"
    "s_and_b32 s38, s38, 0x2000\n"
    "s_or_b32 s51, s51, s38\n"
    "s_add_u32 s52, s53, 524288\n"
    "s_lshr_b32 s38, s52, 8\n"
    "s_xor_b32 s38, s38, s56\n"
    "s_and_b32 s38, s38, 0x2000\n"
    "s_or_b32 s52, s52, s38\n"
    "s_add_u32 s53, s53, 1048576\n"
    "s_lshr_b32 s38, s53, 8\n"
    "s_xor_b32 s38, s38, s56\n"
    "s_and_b32 s38, s38, 0x2000\n"
    "s_or_b32 s53, s53, s38\n"
    "ds_read_b128 v[64:67], v9 offset:35584\n"
    "s_add_u32 s73, s46, s63\n"
    "s_add_u32 s66, s73, 4096\n"
    "s_add_u32 s39, s44, s9\n"
    "s_waitcnt lgkmcnt(0)\n"
    "LUG%=:\n"
    "s_memtime s[40:41]\n"
    "s_waitcnt lgkmcnt(0)\n"
    "s_sub_u32 s40, s39, s40\n"
    "s_sub_u32 s41, s40, 253\n"
    "s_cmp_lt_u32 s41, 16384\n"
    "s_cbranch_scc1 LUG%=\n"
    "s_cmp_lt_u32 s40, 253\n"
    "s_cselect_b32 s40, s40, 0\n"
    "s_and_b32 s40, s40, 0xfc\n"
    "s_getpc_b64 s[42:43]\n"
    "s_sub_u32 s40, 272, s40\n"
    "s_add_u32 s42, s42, s40\n"
    "s_addc_u32 s43, s43, 0\n"
    "s_setpc_b64 s[42:43]\n"
    ".rept 63\n"
    "s_nop 0\n"
    ".endr\n"
    "buffer_load_dwordx4 v[16:19], v0, s[16:19], s73 offen offset:0 sc1\n"
    "buffer_load_dwordx4 v[20:23], v0, s[16:19], s73 offen offset:1024 sc1\n"
    "buffer_load_dwordx4 v[24:27], v0, s[16:19], s73 offen offset:2048 sc1\n"
    "buffer_load_dwordx4 v[28:31], v0, s[16:19], s73 offen offset:3072 sc1\n"
    "buffer_load_dwordx4 v[32:35], v0, s[16:19], s66 offen offset:0 sc1\n"
    "buffer_load_dwordx4 v[36:39], v0, s[16:19], s66 offen offset:1024 sc1\n"
    "buffer_load_dwordx4 v[40:43], v0, s[16:19], s66 offen offset:2048 sc1\n"
    "buffer_load_dwordx4 v[44:47], v0, s[16:19], s66 offen offset:3072 sc1\n"
    "s_waitcnt vmcnt(7)\n"
    "v_mfma_f32_16x16x32_bf16 v[68:71], v[64:67], v[16:19], 0\n"
    "s_waitcnt vmcnt(6)\n"
    "v_mfma_f32_16x16x32_bf16 v[68:71], v[64:67], v[20:23], v[68:71]\n"
    "s_waitcnt vmcnt(5)\n"
    "v_bitop3_b32 v76, v19, v23, s64 bitop3:0x7e\n"
    "v_mfma_f32_16x16x32_bf16 v[68:71], v[64:67], v[24:27], v[68:71]\n"
    "s_waitcnt vmcnt(4)\n"
    "v_mfma_f32_16x16x32_bf16 v[68:71], v[64:67], v[28:31], v[68:71]\n"
    "s_waitcnt vmcnt(3)\n"
    "v_bitop3_b32 v77, v27, v31, s64 bitop3:0x7e\n"
    "v_mfma_f32_16x16x32_bf16 v[72:75], v[64:67], v[32:35], 0\n"
    "s_waitcnt vmcnt(2)\n"
    "v_mfma_f32_16x16x32_bf16 v[72:75], v[64:67], v[36:39], v[72:75]\n"
    "v_bitop3_b32 v78, v35, v39, s64 bitop3:0x7e\n"
    "v_or3_b32 v76, v76, v77, v78\n"
    "v_add_f32_dpp v68, v68, v68 row_ror:8 row_mask:0xf bank_mask:0xf\n"
    "v_add_f32_dpp v69, v69, v69 row_ror:8 row_mask:0xf bank_mask:0xf\n"
    "v_add_f32_dpp v70, v70, v70 row_ror:8 row_mask:0xf bank_mask:0xf\n"
    "v_add_f32_dpp v71, v71, v71 row_ror:8 row_mask:0xf bank_mask:0xf\n"
    "v_cvt_pk_bf16_f32 v81, v70, v71\n"
    "v_cvt_pk_bf16_f32 v80, v68, v69\n"
    "s_waitcnt vmcnt(1)\n"
    "v_mfma_f32_16x16x32_bf16 v[72:75], v[64:67], v[40:43], v[72:75]\n"
    "s_waitcnt vmcnt(0)\n"
    "v_mfma_f32_16x16x32_bf16 v[72:75], v[64:67], v[44:47], v[72:75]\n"
    "v_bitop3_b32 v79, v43, v47, s64 bitop3:0x7e\n"
    "v_or_b32 v76, v76, v79\n"
    "v_and_b32 v77, 1, v76\n"
    "v_cmp_ne_u32 vcc, 0, v77\n"
    "s_cbranch_vccnz LGR%=\n"
    "s_nop 2\n"
    "v_add_f32_dpp v72, v72, v72 row_ror:8 row_mask:0xf bank_mask:0xf\n"
    "v_add_f32_dpp v73, v73, v73 row_ror:8 row_mask:0xf bank_mask:0xf\n"
    "v_add_f32_dpp v74, v74, v74 row_ror:8 row_mask:0xf bank_mask:0xf\n"
    "v_add_f32_dpp v75, v75, v75 row_ror:8 row_mask:0xf bank_mask:0xf\n"
    "v_cvt_pk_bf16_f32 v83, v74, v75\n"
    "v_cvt_pk_bf16_f32 v82, v72, v73\n"
    "v_min_u32 v83, -2, v83\n"
    "buffer_store_dwordx4 v[80:83], v14, s[16:19], s48 offen sc1\n"
    "s_xor_b32 s46, s46, 0x80000\n"
    "s_add_u32 s47, s46, 4096\n"
    "s_bfe_u32 s64, s36, 0x10001\n"
    "s_add_u32 s44, s44, s8\n"
    "s_add_u32 s59, s59, 128\n"
    "s_branch LBM%=\n"
    "LGR%=:\n"
    "s_add_u32 s33, s33, 1\n"
    "s_cmp_gt_u32 s33, 0x40000\n"
    "s_cbranch_scc1 LFAIL%=\n"
    "s_add_u32 s44, s44, s13\n"
    "LGM%=:\n"
    "s_memtime s[40:41]\n"
    "s_add_u32 s42, s21, s9\n"
    "s_waitcnt lgkmcnt(0)\n"
    "s_sub_u32 s40, s40, s42\n"
    "s_sub_u32 s41, s44, s40\n"
    "s_cmp_ge_i32 s41, 0\n"
    "s_cbranch_scc1 LGMD%=\n"
    "LGMI%=:\n"
    "s_add_u32 s44, s44, s13\n"
    "s_add_u32 s41, s41, s13\n"
    "s_cbranch_scc0 LGMI%=\n"
    "s_branch LGM%=\n"
    "LGMD%=:\n"
    "s_add_u32 s39, s44, s9\n"
    "s_add_u32 s38, s44, s8\n"
    "v_mov_b32 v76, s72\n"
    "v_mov_b32 v77, s38\n"
    "ds_write_b32 v76, v77 offset:16\n"
    "s_branch LUG%=\n"
    "LMR%=:\n"
    "s_add_u32 s33, s33, 1\n"
    "s_cmp_gt_u32 s33, 0x40000\n"
    "s_cbranch_scc1 LFAIL%=\n"
    "s_add_u32 s39, s44, s13\n"
    "s_mov_b32 s54, 0x80000000\n"
    "LSL%=:\n"
    "s_mov_b32 s44, s39\n"
    "s_waitcnt lgkmcnt(0)\n"
    "s_barrier\n"
    "s_cmp_lg_u32 s61, 1\n"
    "s_cbranch_scc1 LBM%=\n"
    "LMO%=:\n"
    "LMP%=:\n"
    "s_memtime s[40:41]\n"
    "s_add_u32 s42, s21, 0\n"
    "s_waitcnt lgkmcnt(0)\n"
    "s_sub_u32 s40, s40, s42\n"
    "s_sub_u32 s41, s44, s40\n"
    "s_cmp_ge_i32 s41, 0\n"
    "s_cbranch_scc1 LMPD%=\n"
    "LMPI%=:\n"
    "s_add_u32 s44, s44, s13\n"
    "s_add_u32 s41, s41, s13\n"
    "s_cbranch_scc0 LMPI%=\n"
    "s_branch LMP%=\n"
    "LMPD%=:\n"
    "v_mov_b32 v64, s72\n"
    "v_mov_b32 v65, s44\n"
    "ds_write_b32 v64, v65 offset:16\n"
    "s_waitcnt lgkmcnt(0)\n"
    "s_branch LUM%=\n"
    "LW1%=:\n"
    "buffer_store_dword v1, off, s[16:19], s58 sc1\n"
    "s_add_u32 s58, s58, 4\n"
    "s_bitset0_b32 s58, 31\n"
    "s_waitcnt vmcnt(3)\n"
    "v_cmp_ne_u32 vcc, 0, v116\n"
    "v_readfirstlane_b32 s70, v116\n"
    "s_nop 0\n"
    "s_andn2_b32 s38, 7, vcc_lo\n"
    "s_cbranch_scc0 LS2K%=\n"
    "s_mov_b32 s37, 0x80000\n"
    "LS2W%=:\n"
    "s_mov_b64 exec, 7\n"
    "buffer_load_dword v116, v0, s[16:19], s59 offen sc1\n"
    "s_mov_b64 exec, -1\n"
    "s_waitcnt vmcnt(0)\n"
    "v_cmp_ne_u32 vcc, 0, v116\n"
    "v_readfirstlane_b32 s70, v116\n"
    "s_nop 0\n"
    "s_andn2_b32 s38, 7, vcc_lo\n"
    "s_cbranch_scc0 LS2K%=\n"
    "s_sleep 2\n"
    "s_sub_u32 s37, s37, 1\n"
    "s_cbranch_scc0 LS2W%=\n"
    "s_branch LFAIL%=\n"
    "LS2K%=:\n"
    "s_add_u32 s53, s50, s55\n"
    "s_add_u32 s49, s48, s62\n"
    "s_add_u32 s51, s53, 0\n"
    "s_lshr_b32 s38, s51, 8\n"
    "s_xor_b32 s38, s38, s56\n"
    "s_and_b32 s38, s38, 0x2000\n"
    "s_or_b32 s51, s51, s38\n"
    "s_add_u32 s52, s53, 524288\n"
    "s_lshr_b32 s38, s52, 8\n"
    "s_xor_b32 s38, s38, s56\n"
    "s_and_b32 s38, s38, 0x2000\n"
    "s_or_b32 s52, s52, s38\n"
    "s_add_u32 s53, s53, 1048576\n"
    "s_lshr_b32 s38, s53, 8\n"
    "s_xor_b32 s38, s38, s56\n"
    "s_and_b32 s38, s38, 0x2000\n"
    "s_or_b32 s53, s53, s38\n"
    "v_mov_b32 v64, 158240\n"
    "s_mov_b32 s37, 0x80000\n"
    "LKW%=:\n"
    "ds_read_b64 v[66:67], v64\n"
    "s_waitcnt lgkmcnt(0)\n"
    "v_min_u32 v66, v66, v67\n"
    "v_cmp_eq_u32 vcc, s36, v66\n"
    "s_cbranch_vccnz LKK%=\n"
    "s_sleep 1\n"
    "s_sub_u32 s37, s37, 1\n"
    "s_cbranch_scc0 LKW%=\n"
    "s_branch LFAIL%=\n"
    "LKK%=:\n"
    "s_add_u32 s39, s44, s12\n"
    "LUF%=:\n"
    "s_memtime s[40:41]\n"
    "s_waitcnt lgkmcnt(0)\n"
    "s_sub_u32 s40, s39, s40\n"
    "s_sub_u32 s41, s40, 253\n"
    "s_cmp_lt_u32 s41, 16384\n"
    "s_cbranch_scc1 LUF%=\n"
    "s_cmp_lt_u32 s40, 253\n"
    "s_cselect_b32 s40, s40, 0\n"
    "s_and_b32 s40, s40, 0xfc\n"
    "s_getpc_b64 s[42:43]\n"
    "s_sub_u32 s40, 272, s40\n"
    "s_add_u32 s42, s42, s40\n"
    "s_addc_u32 s43, s43, 0\n"
    "s_setpc_b64 s[42:43]\n"
    ".rept 63\n"
    "s_nop 0\n"
    ".endr\n"
    "s_sub_u32 s51, s51, 2048\n"
    "s_mov_b32 m0, 122880\n"
    "s_nop 0\n"
    "buffer_load_dwordx4 v0, s[16:19], s51 offen offset:0 sc1 lds\n"
    "buffer_load_dwordx4 v0, s[16:19], s51 offen offset:1024 sc1 lds\n"
    "buffer_load_dwordx4 v0, s[16:19], s51 offen offset:2048 sc1 lds\n"
    "buffer_load_dwordx4 v0, s[16:19], s51 offen offset:3072 sc1 lds\n"
    "s_mov_b32 m0, 126976\n"
    "s_add_u32 s51, s51, 4096\n"
    "buffer_load_dwordx4 v0, s[16:19], s51 offen offset:0 sc1 lds\n"
    "buffer_load_dwordx4 v0, s[16:19], s51 offen offset:1024 sc1 lds\n"
    "buffer_load_dwordx4 v0, s[16:19], s51 offen offset:2048 sc1 lds\n"
    "buffer_load_dwordx4 v0, s[16:19], s51 offen offset:3072 sc1 lds\n"
    "s_xor_b32 s46, s46, 0x80000\n"
    "s_add_u32 s47, s46, 4096\n"
    "s_bfe_u32 s64, s36, 0x10001\n"
    "s_add_u32 s44, s44, s8\n"
    "s_add_u32 s59, s59, 128\n"
    "LMN%=:\n"
    "LMM%=:\n"
    "s_memtime s[40:41]\n"
    "s_add_u32 s42, s21, 0\n"
    "s_waitcnt lgkmcnt(0)\n"
    "s_sub_u32 s40, s40, s42\n"
    "s_sub_u32 s41, s44, s40\n"
    "s_cmp_ge_i32 s41, 0\n"
    "s_cbranch_scc1 LMMD%=\n"
    "LMMI%=:\n"
    "s_add_u32 s44, s44, s13\n"
    "s_add_u32 s41, s41, s13\n"
    "s_cbranch_scc0 LMMI%=\n"
    "s_branch LMM%=\n"
    "LMMD%=:\n"
    "v_mov_b32 v64, s72\n"
    "v_mov_b32 v65, s44\n"
    "ds_write_b32 v64, v65 offset:16\n"
    "s_sub_u32 s39, s44, 330\n"
    "s_waitcnt lgkmcnt(0)\n"
    "LUL%=:\n"
    "s_memtime s[40:41]\n"
    "s_waitcnt lgkmcnt(0)\n"
    "s_sub_u32 s40, s39, s40\n"
    "s_sub_u32 s41, s40, 253\n"
    "s_cmp_lt_u32 s41, 16384\n"
    "s_cbranch_scc1 LUL%=\n"
    "s_cmp_lt_u32 s40, 253\n"
    "s_cselect_b32 s40, s40, 0\n"
    "s_and_b32 s40, s40, 0xfc\n"
    "s_getpc_b64 s[42:43]\n"
    "s_sub_u32 s40, 272, s40\n"
    "s_add_u32 s42, s42, s40\n"
    "s_addc_u32 s43, s43, 0\n"
    "s_setpc_b64 s[42:43]\n"
    ".rept 63\n"
    "s_nop 0\n"
    ".endr\n"
    "ds_read_b32 v65, v64 offset:12\n"
    "s_waitcnt lgkmcnt(0)\n"
    "v_readfirstlane_b32 s39, v65\n"
    "s_sub_u32 s38, s39, s44\n"
    "s_cmp_gt_i32 s38, 0\n"
    "s_cselect_b32 s44, s39, s44\n"
    "s_cbranch_scc1 LMN%=\n"
    "LUM%=:\n"
    "s_memtime s[40:41]\n"
    "s_waitcnt lgkmcnt(0)\n"
    "s_sub_u32 s40, s44, s40\n"
    "s_sub_u32 s41, s40, 253\n"
    "s_cmp_lt_u32 s41, 16384\n"
    "s_cbranch_scc1 LUM%=\n"
    "s_cmp_lt_u32 s40, 253\n"
    "s_cselect_b32 s40, s40, 0\n"
    "s_and_b32 s40, s40, 0xfc\n"
    "s_getpc_b64 s[42:43]\n"
    "s_sub_u32 s40, 272, s40\n"
    "s_add_u32 s42, s42, s40\n"
    "s_addc_u32 s43, s43, 0\n"
    "s_setpc_b64 s[42:43]\n"
    ".rept 63\n"
    "s_nop 0\n"
    ".endr\n"
    "s_branch LBM%=\n"
    "LW2%=:\n"
    "v_add_u32 v118, 122880, v0\n"
    "ds_read_b128 v[16:19], v14 offset:0\n"
    "ds_read_b128 v[32:35], v118 offset:0\n"
    "ds_read_b128 v[48:51], v118 offset:4096\n"
    "ds_read_b128 v[20:23], v14 offset:1024\n"
    "ds_read_b128 v[36:39], v118 offset:1024\n"
    "ds_read_b128 v[52:55], v118 offset:5120\n"
    "ds_read_b128 v[24:27], v14 offset:2048\n"
    "ds_read_b128 v[40:43], v118 offset:2048\n"
    "ds_read_b128 v[56:59], v118 offset:6144\n"
    "ds_read_b128 v[28:31], v14 offset:3072\n"
    "ds_read_b128 v[44:47], v118 offset:3072\n"
    "ds_read_b128 v[60:63], v118 offset:7168\n"
    "s_waitcnt lgkmcnt(10)\n"
    "v_lshrrev_b32 v112, 1, v0\n"
    "v_add_u32 v112, 149760, v112\n"
    "v_mov_b32 v111, 0xd0289000\n"
    "v_mfma_f32_16x16x32_bf16 v[88:91], v[32:35], v[16:19], 0\n"
    "ds_read_b128 v[64:67], v6 offset:0\n"
    "s_waitcnt lgkmcnt(10)\n"
    "v_mfma_f32_16x16x32_bf16 v[92:95], v[48:51], v[16:19], 0\n"
    "ds_read_b128 v[68:71], v6 offset:64\n"
    "s_waitcnt lgkmcnt(9)\n"
    "v_mfma_f32_16x16x32_bf16 v[88:91], v[36:39], v[20:23], v[88:91]\n"
    "ds_read_b128 v[72:75], v6 offset:128\n"
    "s_waitcnt lgkmcnt(9)\n"
    "v_mfma_f32_16x16x32_bf16 v[92:95], v[52:55], v[20:23], v[92:95]\n"
    "ds_read_b128 v[76:79], v6 offset:192\n"
    "s_waitcnt lgkmcnt(8)\n"
    "v_mfma_f32_16x16x32_bf16 v[88:91], v[40:43], v[24:27], v[88:91]\n"
    "ds_read_b64 v[80:81], v112 offset:0\n"
    "s_waitcnt lgkmcnt(8)\n"
    "v_mfma_f32_16x16x32_bf16 v[92:95], v[56:59], v[24:27], v[92:95]\n"
    "ds_read_b64 v[82:83], v112 offset:512\n"
    "s_waitcnt lgkmcnt(7)\n"
    "v_mfma_f32_16x16x32_bf16 v[88:91], v[44:47], v[28:31], v[88:91]\n"
    "ds_read_b64 v[84:85], v112 offset:1024\n"
    "s_waitcnt lgkmcnt(7)\n"
    "v_mfma_f32_16x16x32_bf16 v[92:95], v[60:63], v[28:31], v[92:95]\n"
    "ds_read_b64 v[86:87], v112 offset:1536\n"
    "v_mov_b32 v108, s72\n"
    "v_mov_b32 v109, s36\n"
    "ds_write_b32 v108, v109 offset:24\n"
    "s_waitcnt lgkmcnt(8)\n"
    "v_mfma_f32_16x16x32_bf16 v[96:99], v[64:67], v[16:19], 0\n"
    "ds_read_b128 v[32:35], v118 offset:8192\n"
    "ds_read_b128 v[36:39], v118 offset:9216\n"
    "s_waitcnt lgkmcnt(9)\n"
    "v_mfma_f32_16x16x32_bf16 v[96:99], v[68:71], v[20:23], v[96:99]\n"
    "ds_read_b128 v[40:43], v118 offset:10240\n"
    "ds_read_b128 v[44:47], v118 offset:11264\n"
    "s_waitcnt lgkmcnt(10)\n"
    "v_cndmask_b32 v88, v111, v88, s[80:81]\n"
    "v_cndmask_b32 v89, v111, v89, s[82:83]\n"
    "v_mfma_f32_16x16x32_bf16 v[96:99], v[72:75], v[24:27], v[96:99]\n"
    "ds_read_b128 v[48:51], v118 offset:12288\n"
    "ds_read_b128 v[52:55], v118 offset:13312\n"
    "s_waitcnt lgkmcnt(11)\n"
    "v_cndmask_b32 v90, v111, v90, s[84:85]\n"
    "v_cndmask_b32 v91, v111, v91, s[86:87]\n"
    "v_mfma_f32_16x16x32_bf16 v[96:99], v[76:79], v[28:31], v[96:99]\n"
    "ds_read_b128 v[56:59], v118 offset:14336\n"
    "ds_read_b128 v[60:63], v118 offset:15360\n"
    "v_cndmask_b32 v92, v111, v92, s[88:89]\n"
    "v_cndmask_b32 v93, v111, v93, s[90:91]\n"
    "v_cndmask_b32 v94, v111, v94, s[92:93]\n"
    "v_cndmask_b32 v95, v111, v95, s[94:95]\n"
    "v_max3_f32 v108, v88, v89, v90\n"
    "v_max3_f32 v109, v91, v92, v93\n"
    "v_max3_f32 v108, v108, v94, v95\n"
    "v_max3_f32 v109, v109, v96, v97\n"
    "v_max_f32 v108, v108, v109\n"
    "v_mov_b32 v109, v108\n"
    "s_nop 1\n"
    "v_permlane16_swap_b32 v108, v109\n"
    "v_max_f32 v108, v108, v109\n"
    "v_mov_b32 v109, v108\n"
    "s_nop 1\n"
    "v_permlane32_swap_b32 v108, v109\n"
    "v_max_f32 v108, v108, v109\n"
    "v_mul_f32 v108, 0xbe0293ee, v108\n"
    "v_fma_f32 v88, v88, s65, v108\n"
    "v_fma_f32 v89, v89, s65, v108\n"
    "v_fma_f32 v90, v90, s65, v108\n"
    "v_fma_f32 v91, v91, s65, v108\n"
    "v_fma_f32 v92, v92, s65, v108\n"
    "v_fma_f32 v93, v93, s65, v108\n"
    "v_fma_f32 v94, v94, s65, v108\n"
    "v_fma_f32 v95, v95, s65, v108\n"
    "v_fma_f32 v96, v96, s65, v108\n"
    "v_fma_f32 v97, v97, s65, v108\n"
    "v_exp_f32 v88, v88\n"
    "v_exp_f32 v89, v89\n"
    "v_exp_f32 v90, v90\n"
    "v_exp_f32 v91, v91\n"
    "v_exp_f32 v92, v92\n"
    "v_exp_f32 v93, v93\n"
    "v_exp_f32 v94, v94\n"
    "v_exp_f32 v95, v95\n"
    "v_exp_f32 v96, v96\n"
    "v_exp_f32 v97, v97\n"
    "v_add_f32 v108, v88, v89\n"
    "v_add_f32 v110, v90, v91\n"
    "v_add_f32 v108, v108, v92\n"
    "v_add_f32 v110, v110, v93\n"
    "v_add_f32 v108, v108, v94\n"
    "v_add_f32 v110, v110, v95\n"
    "v_add_f32 v108, v108, v96\n"
    "v_add_f32 v110, v110, v97\n"
    "v_add_f32 v108, v108, v110\n"
    "v_mov_b32 v109, v108\n"
    "s_nop 1\n"
    "v_permlane16_swap_b32 v108, v109\n"
    "v_add_f32 v108, v108, v109\n"
    "v_mov_b32 v109, v108\n"
    "s_nop 1\n"
    "v_permlane32_swap_b32 v108, v109\n"
    "v_add_f32 v108, v108, v109\n"
    "v_rcp_f32 v108, v108\n"
    "v_mov_b32 v105, 0\n"
    "v_mov_b32 v106, 0\n"
    "v_mul_f32 v88, v88, v108\n"
    "v_mul_f32 v89, v89, v108\n"
    "v_mul_f32 v90, v90, v108\n"
    "v_mul_f32 v91, v91, v108\n"
    "v_mul_f32 v92, v92, v108\n"
    "v_mul_f32 v93, v93, v108\n"
    "v_mul_f32 v94, v94, v108\n"
    "v_mul_f32 v95, v95, v108\n"
    "v_mul_f32 v96, v96, v108\n"
    "v_mul_f32 v97, v97, v108\n"
    "v_cvt_pk_bf16_f32 v100, v88, v89\n"
    "v_cvt_pk_bf16_f32 v101, v90, v91\n"
    "v_cvt_pk_bf16_f32 v102, v92, v93\n"
    "v_cvt_pk_bf16_f32 v103, v94, v95\n"
    "v_cvt_pk_bf16_f32 v104, v96, v97\n"
    "v_cvt_pk_bf16_f32 v107, v96, v97\n"
    "s_waitcnt lgkmcnt(7)\n"
    "v_mfma_f32_16x16x32_bf16 v[64:67], v[32:35], v[100:103], 0\n"
    "s_waitcnt lgkmcnt(6)\n"
    "v_mfma_f32_16x16x32_bf16 v[68:71], v[36:39], v[100:103], 0\n"
    "v_mfma_f32_16x16x16_bf16 v[64:67], v[80:81], v[104:105], v[64:67]\n"
    "v_mfma_f32_16x16x16_bf16 v[68:71], v[80:81], v[106:107], v[68:71]\n"
    "s_waitcnt lgkmcnt(5)\n"
    "v_mfma_f32_16x16x32_bf16 v[72:75], v[40:43], v[100:103], 0\n"
    "s_waitcnt lgkmcnt(4)\n"
    "v_mfma_f32_16x16x32_bf16 v[76:79], v[44:47], v[100:103], 0\n"
    "v_cvt_pk_bf16_f32 v88, v64, v65\n"
    "v_cvt_pk_bf16_f32 v89, v66, v67\n"
    "v_cvt_pk_bf16_f32 v90, v68, v69\n"
    "v_mfma_f32_16x16x16_bf16 v[72:75], v[82:83], v[104:105], v[72:75]\n"
    "v_cvt_pk_bf16_f32 v91, v70, v71\n"
    "ds_write_b128 v14, v[88:91] offset:0\n"
    "v_mfma_f32_16x16x16_bf16 v[76:79], v[82:83], v[106:107], v[76:79]\n"
    "s_waitcnt lgkmcnt(4)\n"
    "v_mfma_f32_16x16x32_bf16 v[16:19], v[48:51], v[100:103], 0\n"
    "s_waitcnt lgkmcnt(3)\n"
    "v_mfma_f32_16x16x32_bf16 v[20:23], v[52:55], v[100:103], 0\n"
    "v_cvt_pk_bf16_f32 v92, v72, v73\n"
    "v_cvt_pk_bf16_f32 v93, v74, v75\n"
    "v_cvt_pk_bf16_f32 v94, v76, v77\n"
    "v_mfma_f32_16x16x16_bf16 v[16:19], v[84:85], v[104:105], v[16:19]\n"
    "v_cvt_pk_bf16_f32 v95, v78, v79\n"
    "ds_write_b128 v14, v[92:95] offset:1024\n"
    "v_mfma_f32_16x16x16_bf16 v[20:23], v[84:85], v[106:107], v[20:23]\n"
    "s_waitcnt lgkmcnt(3)\n"
    "v_mfma_f32_16x16x32_bf16 v[24:27], v[56:59], v[100:103], 0\n"
    "s_waitcnt lgkmcnt(2)\n"
    "v_mfma_f32_16x16x32_bf16 v[28:31], v[60:63], v[100:103], 0\n"
    "v_cvt_pk_bf16_f32 v88, v16, v17\n"
    "v_cvt_pk_bf16_f32 v89, v18, v19\n"
    "v_cvt_pk_bf16_f32 v90, v20, v21\n"
    "v_mfma_f32_16x16x16_bf16 v[24:27], v[86:87], v[104:105], v[24:27]\n"
    "v_cvt_pk_bf16_f32 v91, v22, v23\n"
    "ds_write_b128 v14, v[88:91] offset:2048\n"
    "v_mfma_f32_16x16x16_bf16 v[28:31], v[86:87], v[106:107], v[28:31]\n"
    "s_nop 3\n"
    "v_cvt_pk_bf16_f32 v92, v24, v25\n"
    "v_cvt_pk_bf16_f32 v93, v26, v27\n"
    "s_nop 1\n"
    "v_cvt_pk_bf16_f32 v94, v28, v29\n"
    "v_cvt_pk_bf16_f32 v95, v30, v31\n"
    "ds_write_b128 v14, v[92:95] offset:3072\n"
    "s_add_u32 s53, s50, s55\n"
    "s_add_u32 s49, s48, s62\n"
    "s_add_u32 s51, s53, 0\n"
    "s_lshr_b32 s38, s51, 8\n"
    "s_xor_b32 s38, s38, s56\n"
    "s_and_b32 s38, s38, 0x2000\n"
    "s_or_b32 s51, s51, s38\n"
    "s_add_u32 s52, s53, 524288\n"
    "s_lshr_b32 s38, s52, 8\n"
    "s_xor_b32 s38, s38, s56\n"
    "s_and_b32 s38, s38, 0x2000\n"
    "s_or_b32 s52, s52, s38\n"
    "s_add_u32 s53, s53, 1048576\n"
    "s_lshr_b32 s38, s53, 8\n"
    "s_xor_b32 s38, s38, s56\n"
    "s_and_b32 s38, s38, 0x2000\n"
    "s_or_b32 s53, s53, s38\n"
    "s_xor_b32 s46, s46, 0x80000\n"
    "s_add_u32 s47, s46, 4096\n"
    "s_bfe_u32 s64, s36, 0x10001\n"
    "s_add_u32 s44, s44, s8\n"
    "s_add_u32 s59, s59, 128\n"
    "s_branch LBM%=\n"
    "LEND%=:\n"
    "s_waitcnt lgkmcnt(0)\n"
    "s_cmp_lg_u32 s61, 1\n"
    "s_cbranch_scc1 LE0%=\n"
    "buffer_store_dword v1, off, s[16:19], s58 sc1\n"
    "s_add_u32 s58, s58, 4\n"
    "s_bitset0_b32 s58, 31\n"
    "LE0%=:\n"
    "s_add_u32 s54, s50, s57\n"
    "s_lshr_b32 s38, s54, 8\n"
    "s_xor_b32 s38, s38, s74\n"
    "s_and_b32 s38, s38, 0x2000\n"
    "s_or_b32 s54, s54, s38\n"
    "s_cmp_lt_u32 s61, 2\n"
    "s_cbranch_scc1 LE1%=\n"
    "v_add_u32 v118, 122880, v0\n"
    "ds_read_b128 v[16:19], v14 offset:0\n"
    "ds_read_b128 v[32:35], v118 offset:0\n"
    "ds_read_b128 v[48:51], v118 offset:4096\n"
    "ds_read_b128 v[20:23], v14 offset:1024\n"
    "ds_read_b128 v[36:39], v118 offset:1024\n"
    "ds_read_b128 v[52:55], v118 offset:5120\n"
    "ds_read_b128 v[24:27], v14 offset:2048\n"
    "ds_read_b128 v[40:43], v118 offset:2048\n"
    "ds_read_b128 v[56:59], v118 offset:6144\n"
    "ds_read_b128 v[28:31], v14 offset:3072\n"
    "ds_read_b128 v[44:47], v118 offset:3072\n"
    "ds_read_b128 v[60:63], v118 offset:7168\n"
    "s_waitcnt lgkmcnt(10)\n"
    "v_lshrrev_b32 v112, 1, v0\n"
    "v_add_u32 v112, 149760, v112\n"
    "v_mov_b32 v111, 0xd0289000\n"
    "v_mfma_f32_16x16x32_bf16 v[88:91], v[32:35], v[16:19], 0\n"
    "ds_read_b128 v[64:67], v6 offset:0\n"
    "s_waitcnt lgkmcnt(10)\n"
    "v_mfma_f32_16x16x32_bf16 v[92:95], v[48:51], v[16:19], 0\n"
    "ds_read_b128 v[68:71], v6 offset:64\n"
    "s_waitcnt lgkmcnt(9)\n"
    "v_mfma_f32_16x16x32_bf16 v[88:91], v[36:39], v[20:23], v[88:91]\n"
    "ds_read_b128 v[72:75], v6 offset:128\n"
    "s_waitcnt lgkmcnt(9)\n"
    "v_mfma_f32_16x16x32_bf16 v[92:95], v[52:55], v[20:23], v[92:95]\n"
    "ds_read_b128 v[76:79], v6 offset:192\n"
    "s_waitcnt lgkmcnt(8)\n"
    "v_mfma_f32_16x16x32_bf16 v[88:91], v[40:43], v[24:27], v[88:91]\n"
    "ds_read_b64 v[80:81], v112 offset:0\n"
    "s_waitcnt lgkmcnt(8)\n"
    "v_mfma_f32_16x16x32_bf16 v[92:95], v[56:59], v[24:27], v[92:95]\n"
    "ds_read_b64 v[82:83], v112 offset:512\n"
    "s_waitcnt lgkmcnt(7)\n"
    "v_mfma_f32_16x16x32_bf16 v[88:91], v[44:47], v[28:31], v[88:91]\n"
    "ds_read_b64 v[84:85], v112 offset:1024\n"
    "s_waitcnt lgkmcnt(7)\n"
    "v_mfma_f32_16x16x32_bf16 v[92:95], v[60:63], v[28:31], v[92:95]\n"
    "ds_read_b64 v[86:87], v112 offset:1536\n"
    "v_mov_b32 v108, s72\n"
    "v_mov_b32 v109, s36\n"
    "ds_write_b32 v108, v109 offset:24\n"
    "s_waitcnt lgkmcnt(8)\n"
    "v_mfma_f32_16x16x32_bf16 v[96:99], v[64:67], v[16:19], 0\n"
    "ds_read_b128 v[32:35], v118 offset:8192\n"
    "ds_read_b128 v[36:39], v118 offset:9216\n"
    "s_waitcnt lgkmcnt(9)\n"
    "v_mfma_f32_16x16x32_bf16 v[96:99], v[68:71], v[20:23], v[96:99]\n"
    "ds_read_b128 v[40:43], v118 offset:10240\n"
    "ds_read_b128 v[44:47], v118 offset:11264\n"
    "s_waitcnt lgkmcnt(10)\n"
    "v_cndmask_b32 v88, v111, v88, s[80:81]\n"
    "v_cndmask_b32 v89, v111, v89, s[82:83]\n"
    "v_mfma_f32_16x16x32_bf16 v[96:99], v[72:75], v[24:27], v[96:99]\n"
    "ds_read_b128 v[48:51], v118 offset:12288\n"
    "ds_read_b128 v[52:55], v118 offset:13312\n"
    "s_waitcnt lgkmcnt(11)\n"
    "v_cndmask_b32 v90, v111, v90, s[84:85]\n"
    "v_cndmask_b32 v91, v111, v91, s[86:87]\n"
    "v_mfma_f32_16x16x32_bf16 v[96:99], v[76:79], v[28:31], v[96:99]\n"
    "ds_read_b128 v[56:59], v118 offset:14336\n"
    "ds_read_b128 v[60:63], v118 offset:15360\n"
    "v_cndmask_b32 v92, v111, v92, s[88:89]\n"
    "v_cndmask_b32 v93, v111, v93, s[90:91]\n"
    "v_cndmask_b32 v94, v111, v94, s[92:93]\n"
    "v_cndmask_b32 v95, v111, v95, s[94:95]\n"
    "v_max3_f32 v108, v88, v89, v90\n"
    "v_max3_f32 v109, v91, v92, v93\n"
    "v_max3_f32 v108, v108, v94, v95\n"
    "v_max3_f32 v109, v109, v96, v97\n"
    "v_max_f32 v108, v108, v109\n"
    "v_mov_b32 v109, v108\n"
    "s_nop 1\n"
    "v_permlane16_swap_b32 v108, v109\n"
    "v_max_f32 v108, v108, v109\n"
    "v_mov_b32 v109, v108\n"
    "s_nop 1\n"
    "v_permlane32_swap_b32 v108, v109\n"
    "v_max_f32 v108, v108, v109\n"
    "v_mul_f32 v108, 0xbe0293ee, v108\n"
    "v_fma_f32 v88, v88, s65, v108\n"
    "v_fma_f32 v89, v89, s65, v108\n"
    "v_fma_f32 v90, v90, s65, v108\n"
    "v_fma_f32 v91, v91, s65, v108\n"
    "v_fma_f32 v92, v92, s65, v108\n"
    "v_fma_f32 v93, v93, s65, v108\n"
    "v_fma_f32 v94, v94, s65, v108\n"
    "v_fma_f32 v95, v95, s65, v108\n"
    "v_fma_f32 v96, v96, s65, v108\n"
    "v_fma_f32 v97, v97, s65, v108\n"
    "v_exp_f32 v88, v88\n"
    "v_exp_f32 v89, v89\n"
    "v_exp_f32 v90, v90\n"
    "v_exp_f32 v91, v91\n"
    "v_exp_f32 v92, v92\n"
    "v_exp_f32 v93, v93\n"
    "v_exp_f32 v94, v94\n"
    "v_exp_f32 v95, v95\n"
    "v_exp_f32 v96, v96\n"
    "v_exp_f32 v97, v97\n"
    "v_add_f32 v108, v88, v89\n"
    "v_add_f32 v110, v90, v91\n"
    "v_add_f32 v108, v108, v92\n"
    "v_add_f32 v110, v110, v93\n"
    "v_add_f32 v108, v108, v94\n"
    "v_add_f32 v110, v110, v95\n"
    "v_add_f32 v108, v108, v96\n"
    "v_add_f32 v110, v110, v97\n"
    "v_add_f32 v108, v108, v110\n"
    "v_mov_b32 v109, v108\n"
    "s_nop 1\n"
    "v_permlane16_swap_b32 v108, v109\n"
    "v_add_f32 v108, v108, v109\n"
    "v_mov_b32 v109, v108\n"
    "s_nop 1\n"
    "v_permlane32_swap_b32 v108, v109\n"
    "v_add_f32 v108, v108, v109\n"
    "v_rcp_f32 v108, v108\n"
    "v_mov_b32 v105, 0\n"
    "v_mov_b32 v106, 0\n"
    "v_mul_f32 v88, v88, v108\n"
    "v_mul_f32 v89, v89, v108\n"
    "v_mul_f32 v90, v90, v108\n"
    "v_mul_f32 v91, v91, v108\n"
    "v_mul_f32 v92, v92, v108\n"
    "v_mul_f32 v93, v93, v108\n"
    "v_mul_f32 v94, v94, v108\n"
    "v_mul_f32 v95, v95, v108\n"
    "v_mul_f32 v96, v96, v108\n"
    "v_mul_f32 v97, v97, v108\n"
    "v_cvt_pk_bf16_f32 v100, v88, v89\n"
    "v_cvt_pk_bf16_f32 v101, v90, v91\n"
    "v_cvt_pk_bf16_f32 v102, v92, v93\n"
    "v_cvt_pk_bf16_f32 v103, v94, v95\n"
    "v_cvt_pk_bf16_f32 v104, v96, v97\n"
    "v_cvt_pk_bf16_f32 v107, v96, v97\n"
    "s_waitcnt lgkmcnt(7)\n"
    "v_mfma_f32_16x16x32_bf16 v[64:67], v[32:35], v[100:103], 0\n"
    "s_waitcnt lgkmcnt(6)\n"
    "v_mfma_f32_16x16x32_bf16 v[68:71], v[36:39], v[100:103], 0\n"
    "v_mfma_f32_16x16x16_bf16 v[64:67], v[80:81], v[104:105], v[64:67]\n"
    "v_mfma_f32_16x16x16_bf16 v[68:71], v[80:81], v[106:107], v[68:71]\n"
    "s_waitcnt lgkmcnt(5)\n"
    "v_mfma_f32_16x16x32_bf16 v[72:75], v[40:43], v[100:103], 0\n"
    "s_waitcnt lgkmcnt(4)\n"
    "v_mfma_f32_16x16x32_bf16 v[76:79], v[44:47], v[100:103], 0\n"
    "v_cvt_pk_bf16_f32 v88, v64, v65\n"
    "v_cvt_pk_bf16_f32 v89, v66, v67\n"
    "v_cvt_pk_bf16_f32 v90, v68, v69\n"
    "v_mfma_f32_16x16x16_bf16 v[72:75], v[82:83], v[104:105], v[72:75]\n"
    "v_cvt_pk_bf16_f32 v91, v70, v71\n"
    "ds_write_b128 v14, v[88:91] offset:0\n"
    "v_mfma_f32_16x16x16_bf16 v[76:79], v[82:83], v[106:107], v[76:79]\n"
    "s_waitcnt lgkmcnt(4)\n"
    "v_mfma_f32_16x16x32_bf16 v[16:19], v[48:51], v[100:103], 0\n"
    "s_waitcnt lgkmcnt(3)\n"
    "v_mfma_f32_16x16x32_bf16 v[20:23], v[52:55], v[100:103], 0\n"
    "v_cvt_pk_bf16_f32 v92, v72, v73\n"
    "v_cvt_pk_bf16_f32 v93, v74, v75\n"
    "v_cvt_pk_bf16_f32 v94, v76, v77\n"
    "v_mfma_f32_16x16x16_bf16 v[16:19], v[84:85], v[104:105], v[16:19]\n"
    "v_cvt_pk_bf16_f32 v95, v78, v79\n"
    "ds_write_b128 v14, v[92:95] offset:1024\n"
    "v_mfma_f32_16x16x16_bf16 v[20:23], v[84:85], v[106:107], v[20:23]\n"
    "s_waitcnt lgkmcnt(3)\n"
    "v_mfma_f32_16x16x32_bf16 v[24:27], v[56:59], v[100:103], 0\n"
    "s_waitcnt lgkmcnt(2)\n"
    "v_mfma_f32_16x16x32_bf16 v[28:31], v[60:63], v[100:103], 0\n"
    "v_cvt_pk_bf16_f32 v88, v16, v17\n"
    "v_cvt_pk_bf16_f32 v89, v18, v19\n"
    "v_cvt_pk_bf16_f32 v90, v20, v21\n"
    "v_mfma_f32_16x16x16_bf16 v[24:27], v[86:87], v[104:105], v[24:27]\n"
    "v_cvt_pk_bf16_f32 v91, v22, v23\n"
    "ds_write_b128 v14, v[88:91] offset:2048\n"
    "v_mfma_f32_16x16x16_bf16 v[28:31], v[86:87], v[106:107], v[28:31]\n"
    "s_nop 3\n"
    "v_cvt_pk_bf16_f32 v92, v24, v25\n"
    "v_cvt_pk_bf16_f32 v93, v26, v27\n"
    "s_nop 1\n"
    "v_cvt_pk_bf16_f32 v94, v28, v29\n"
    "v_cvt_pk_bf16_f32 v95, v30, v31\n"
    "ds_write_b128 v14, v[92:95] offset:3072\n"
    "s_waitcnt lgkmcnt(0)\n"
    "LE1%=:\n"
    "s_barrier\n"
    "ds_read_b128 v[16:19], v9 offset:16384\n"
    "ds_read_b128 v[20:23], v9 offset:17408\n"
    "v_lshrrev_b32 v113, 4, v0\n"
    "v_and_b32 v113, 0x30, v113\n"
    "v_lshl_add_u32 v113, v113, 4, v0\n"
    "s_waitcnt lgkmcnt(1)\n"
    "buffer_store_dwordx4 v[16:19], v113, s[16:19], s54 offen offset:0 sc1\n"
    "s_waitcnt lgkmcnt(0)\n"
    "buffer_store_dwordx4 v[20:23], v113, s[16:19], s54 offen offset:2048 sc1\n"
    "s_waitcnt vmcnt(0)\n"
    "s_barrier\n"
    "buffer_store_dword v1, off, s[16:19], s58 sc1\n"
    "s_endpgm\n"
    "LFAIL%=:\n"
    "buffer_store_dword v1, off, s[16:19], s35 sc1\n"
    "s_endpgm\n"
    :
    : "{v0}"(hv0), "{v1}"(hv1), "{v2}"(hv2), "{v3}"(hv3), "{v4}"(hv4), "{v5}"(hv5), "{v6}"(hv6), "{v7}"(hv7), "{v8}"(hv8), "{v9}"(hv9), "{v10}"(hv10), "{v11}"(hv11), "{v12}"(hv12), "{v13}"(hv13), "{v14}"(hv14), "{v15}"(hv15), "{v16}"(hv16), "{v17}"(hv17), "{v18}"(hv18), "{v19}"(hv19), "{v20}"(hv20)
    : "v255", "a255", "s101", "vcc", "scc", "memory");
  }
  __builtin_unreachable();
}
#define ODL 9
#define ROLL 2
#define MEMFENCE() asm volatile("" ::: "memory")
__device__ __forceinline__ void t_role8(int w, int m, int i, const unsigned* rdy, char* ws, const u16* W) {
  const int lane = lane_id(), g = lane >> 4, q = (lane >> 2) & 3, p = lane & 3;
  const unsigned me = w ? OOR : 0u, c1 = 0u, c2 = 0x2000u;
  const int h = i & 30, par = i & 1, ob = m * 524288 + h * 16384 + (m == 1 ? w * 2048 : w * 1024);
  const int pv[24] = {w, (int)Z_XN + par * 65536 + w * 16384, (int)Z_XN + w * 16384, ob, (int)c1, (int)nearof(Z_FT + h * 16384, c1) + m * 16, (int)nearof(Z_FT + h * 16384, c2) + m * 16,
                      (int)((nearof(Z_DEC, c1) + 4096 + m * 512 + par * 4) | me), (int)((nearof(Z_DEC, c1) + 2048 + m * 512) | me), 0, 0, (int)(nearof(Z_FO + 32768, c2) + h * 256), (int)Z_ERR, m, par,
                      (int)(size_t)W, (int)((size_t)W >> 32) & 0xffff, (int)(size_t)ws, (int)((size_t)ws >> 32) & 0xffff, (int)ZWS_BYTES, (int)((Z_DBG + MBY(1) + (m * 32 + i) * 4096) | (w == 2 ? 0u : OOR)),
                      (int)(size_t)rdy, (int)((size_t)rdy >> 32), (int)((nearof(Z_DEC, c2) + 1024) | (w == 1 ? 0u : OOR))};
  int hv0 = lane * 16, hv1 = (lane & 15) * 1056 + g * 16, hv2 = 0;
  int hv3 = (w * 8 + (lane >> 5)) * 8192 + h * 256 + ((lane & 31) ^ ((lane >> 5) * 2)) * 16, hv4 = g * 4480 + (q >> 1) * 1088 + (q & 1) * 544 + 64 * w + 8 * p, hv5 = 0;
#pragma unroll
  for (int j = 0; j < 24; ++j) hv5 = lane == j ? pv[j] : hv5;
  asm volatile(
    "s_nop 1\n"
    "v_readlane_b32 s36, v5, 0\n"
    "v_readlane_b32 s45, v5, 1\n"
    "v_readlane_b32 s46, v5, 2\n"
    "v_readlane_b32 s47, v5, 3\n"
    "v_readlane_b32 s48, v5, 4\n"
    "v_readlane_b32 s50, v5, 5\n"
    "v_readlane_b32 s51, v5, 6\n"
    "v_readlane_b32 s53, v5, 7\n"
    "v_readlane_b32 s54, v5, 8\n"
    "v_readlane_b32 s55, v5, 9\n"
    "v_readlane_b32 s56, v5, 10\n"
    "v_readlane_b32 s57, v5, 11\n"
    "v_readlane_b32 s58, v5, 12\n"
    "v_readlane_b32 s61, v5, 13\n"
    "v_readlane_b32 s40, v5, 14\n"
    "v_readlane_b32 s20, v5, 15\n"
    "v_readlane_b32 s21, v5, 16\n"
    "v_readlane_b32 s16, v5, 17\n"
    "v_readlane_b32 s17, v5, 18\n"
    "v_readlane_b32 s18, v5, 19\n"
    "v_readlane_b32 s65, v5, 20\n"
    "v_readlane_b32 s24, v5, 21\n"
    "v_readlane_b32 s25, v5, 22\n"
    "v_readlane_b32 s26, v5, 23\n"
    "v_mov_b32 v5, 1\n"
    "s_mov_b32 s19, 0x20000\n"
    "s_mov_b32 s22, 0x400000\n"
    "s_mov_b32 s23, 0x20000\n"
    "s_xor_b32 s49, s48, 0x2000\n"
    "s_mul_i32 s44, s36, 8448\n"
    "s_mul_i32 s39, s36, 4480\n"
    "s_mov_b32 s43, 0\n"
    "s_mov_b32 s27, 0\n"
    "s_mov_b32 s41, s40\n"
    "s_mov_b32 s42, s40\n"
    "s_mov_b32 s52, 0x80000000\n"
    "s_cmp_eq_u32 s36, 1\n"
    "s_cselect_b32 s68, 0, 0x80000000\n"
    "s_add_u32 m0, s39, 0\n"
    "s_mov_b32 s38, 0\n"
    "buffer_load_dwordx4 v3, s[20:23], s38 offen lds\n"
    "s_add_u32 m0, s39, 1088\n"
    "s_mov_b32 s38, 16384\n"
    "buffer_load_dwordx4 v3, s[20:23], s38 offen lds\n"
    "s_add_u32 m0, s39, 2176\n"
    "s_mov_b32 s38, 32768\n"
    "buffer_load_dwordx4 v3, s[20:23], s38 offen lds\n"
    "s_add_u32 m0, s39, 3264\n"
    "s_mov_b32 s38, 49152\n"
    "buffer_load_dwordx4 v3, s[20:23], s38 offen lds\n"
    "s_add_u32 m0, s39, 17920\n"
    "s_mov_b32 s38, 262144\n"
    "buffer_load_dwordx4 v3, s[20:23], s38 offen lds\n"
    "s_add_u32 m0, s39, 19008\n"
    "s_mov_b32 s38, 278528\n"
    "buffer_load_dwordx4 v3, s[20:23], s38 offen lds\n"
    "s_add_u32 m0, s39, 20096\n"
    "s_mov_b32 s38, 294912\n"
    "buffer_load_dwordx4 v3, s[20:23], s38 offen lds\n"
    "s_add_u32 m0, s39, 21184\n"
    "s_mov_b32 s38, 311296\n"
    "buffer_load_dwordx4 v3, s[20:23], s38 offen lds\n"
    "s_add_u32 m0, s39, 35840\n"
    "s_mov_b32 s38, 524288\n"
    "buffer_load_dwordx4 v3, s[20:23], s38 offen lds\n"
    "s_add_u32 m0, s39, 36928\n"
    "s_mov_b32 s38, 540672\n"
    "buffer_load_dwordx4 v3, s[20:23], s38 offen lds\n"
    "s_add_u32 m0, s39, 38016\n"
    "s_mov_b32 s38, 557056\n"
    "buffer_load_dwordx4 v3, s[20:23], s38 offen lds\n"
    "s_add_u32 m0, s39, 39104\n"
    "s_mov_b32 s38, 573440\n"
    "buffer_load_dwordx4 v3, s[20:23], s38 offen lds\n"
    "s_add_u32 m0, s39, 53760\n"
    "s_mov_b32 s38, 786432\n"
    "buffer_load_dwordx4 v3, s[20:23], s38 offen lds\n"
    "s_add_u32 m0, s39, 54848\n"
    "s_mov_b32 s38, 802816\n"
    "buffer_load_dwordx4 v3, s[20:23], s38 offen lds\n"
    "s_add_u32 m0, s39, 55936\n"
    "s_mov_b32 s38, 819200\n"
    "buffer_load_dwordx4 v3, s[20:23], s38 offen lds\n"
    "s_add_u32 m0, s39, 57024\n"
    "s_mov_b32 s38, 835584\n"
    "buffer_load_dwordx4 v3, s[20:23], s38 offen lds\n"
    "s_add_u32 m0, s39, 71680\n"
    "s_mov_b32 s38, 1048576\n"
    "buffer_load_dwordx4 v3, s[20:23], s38 offen lds\n"
    "s_add_u32 m0, s39, 72768\n"
    "s_mov_b32 s38, 1064960\n"
    "buffer_load_dwordx4 v3, s[20:23], s38 offen lds\n"
    "s_add_u32 m0, s39, 73856\n"
    "s_mov_b32 s38, 1081344\n"
    "buffer_load_dwordx4 v3, s[20:23], s38 offen lds\n"
    "s_add_u32 m0, s39, 74944\n"
    "s_mov_b32 s38, 1097728\n"
    "buffer_load_dwordx4 v3, s[20:23], s38 offen lds\n"
    "s_add_u32 m0, s39, 89600\n"
    "s_mov_b32 s38, 1310720\n"
    "buffer_load_dwordx4 v3, s[20:23], s38 offen lds\n"
    "s_add_u32 m0, s39, 90688\n"
    "s_mov_b32 s38, 1327104\n"
    "buffer_load_dwordx4 v3, s[20:23], s38 offen lds\n"
    "s_add_u32 m0, s39, 91776\n"
    "s_mov_b32 s38, 1343488\n"
    "buffer_load_dwordx4 v3, s[20:23], s38 offen lds\n"
    "s_add_u32 m0, s39, 92864\n"
    "s_mov_b32 s38, 1359872\n"
    "buffer_load_dwordx4 v3, s[20:23], s38 offen lds\n"
    "s_add_u32 m0, s39, 107520\n"
    "s_mov_b32 s38, 1572864\n"
    "buffer_load_dwordx4 v3, s[20:23], s38 offen lds\n"
    "s_add_u32 m0, s39, 108608\n"
    "s_mov_b32 s38, 1589248\n"
    "buffer_load_dwordx4 v3, s[20:23], s38 offen lds\n"
    "s_add_u32 m0, s39, 109696\n"
    "s_mov_b32 s38, 1605632\n"
    "buffer_load_dwordx4 v3, s[20:23], s38 offen lds\n"
    "s_add_u32 m0, s39, 110784\n"
    "s_mov_b32 s38, 1622016\n"
    "buffer_load_dwordx4 v3, s[20:23], s38 offen lds\n"
    "s_waitcnt vmcnt(24)\n"
    "s_barrier\n"
    "s_add_u32 m0, s39, 125440\n"
    "s_mov_b32 s38, 1835008\n"
    "buffer_load_dwordx4 v3, s[20:23], s38 offen lds\n"
    "s_add_u32 m0, s39, 126528\n"
    "s_mov_b32 s38, 1851392\n"
    "buffer_load_dwordx4 v3, s[20:23], s38 offen lds\n"
    "s_add_u32 m0, s39, 127616\n"
    "s_mov_b32 s38, 1867776\n"
    "buffer_load_dwordx4 v3, s[20:23], s38 offen lds\n"
    "s_add_u32 m0, s39, 128704\n"
    "s_mov_b32 s38, 1884160\n"
    "buffer_load_dwordx4 v3, s[20:23], s38 offen lds\n"
    "v_add_u32 v8, 0, v4\n"
    "v_xor_b32 v9, 32, v8\n"
    "ds_read_b64_tr_b16 a[0:1], v8 offset:0\n"
    "ds_read_b64_tr_b16 a[2:3], v8 offset:2176\n"
    "ds_read_b64_tr_b16 a[4:5], v9 offset:0\n"
    "ds_read_b64_tr_b16 a[6:7], v9 offset:2176\n"
    "ds_read_b64_tr_b16 a[128:129], v8 offset:256\n"
    "ds_read_b64_tr_b16 a[130:131], v8 offset:2432\n"
    "ds_read_b64_tr_b16 a[132:133], v9 offset:256\n"
    "ds_read_b64_tr_b16 a[134:135], v9 offset:2432\n"
    "s_waitcnt lgkmcnt(0)\n"
    "s_waitcnt vmcnt(24)\n"
    "s_barrier\n"
    "s_add_u32 m0, s39, 0\n"
    "s_mov_b32 s38, 2097152\n"
    "buffer_load_dwordx4 v3, s[20:23], s38 offen lds\n"
    "s_add_u32 m0, s39, 1088\n"
    "s_mov_b32 s38, 2113536\n"
    "buffer_load_dwordx4 v3, s[20:23], s38 offen lds\n"
    "s_add_u32 m0, s39, 2176\n"
    "s_mov_b32 s38, 2129920\n"
    "buffer_load_dwordx4 v3, s[20:23], s38 offen lds\n"
    "s_add_u32 m0, s39, 3264\n"
    "s_mov_b32 s38, 2146304\n"
    "buffer_load_dwordx4 v3, s[20:23], s38 offen lds\n"
    "v_add_u32 v8, 17920, v4\n"
    "v_xor_b32 v9, 32, v8\n"
    "ds_read_b64_tr_b16 a[8:9], v8 offset:0\n"
    "ds_read_b64_tr_b16 a[10:11], v8 offset:2176\n"
    "ds_read_b64_tr_b16 a[12:13], v9 offset:0\n"
    "ds_read_b64_tr_b16 a[14:15], v9 offset:2176\n"
    "ds_read_b64_tr_b16 a[136:137], v8 offset:256\n"
    "ds_read_b64_tr_b16 a[138:139], v8 offset:2432\n"
    "ds_read_b64_tr_b16 a[140:141], v9 offset:256\n"
    "ds_read_b64_tr_b16 a[142:143], v9 offset:2432\n"
    "s_waitcnt lgkmcnt(0)\n"
    "s_waitcnt vmcnt(24)\n"
    "s_barrier\n"
    "s_add_u32 m0, s39, 17920\n"
    "s_mov_b32 s38, 2359296\n"
    "buffer_load_dwordx4 v3, s[20:23], s38 offen lds\n"
    "s_add_u32 m0, s39, 19008\n"
    "s_mov_b32 s38, 2375680\n"
    "buffer_load_dwordx4 v3, s[20:23], s38 offen lds\n"
    "s_add_u32 m0, s39, 20096\n"
    "s_mov_b32 s38, 2392064\n"
    "buffer_load_dwordx4 v3, s[20:23], s38 offen lds\n"
    "s_add_u32 m0, s39, 21184\n"
    "s_mov_b32 s38, 2408448\n"
    "buffer_load_dwordx4 v3, s[20:23], s38 offen lds\n"
    "v_add_u32 v8, 35840, v4\n"
    "v_xor_b32 v9, 32, v8\n"
    "ds_read_b64_tr_b16 a[16:17], v8 offset:0\n"
    "ds_read_b64_tr_b16 a[18:19], v8 offset:2176\n"
    "ds_read_b64_tr_b16 a[20:21], v9 offset:0\n"
    "ds_read_b64_tr_b16 a[22:23], v9 offset:2176\n"
    "ds_read_b64_tr_b16 a[144:145], v8 offset:256\n"
    "ds_read_b64_tr_b16 a[146:147], v8 offset:2432\n"
    "ds_read_b64_tr_b16 a[148:149], v9 offset:256\n"
    "ds_read_b64_tr_b16 a[150:151], v9 offset:2432\n"
    "s_waitcnt lgkmcnt(0)\n"
    "s_waitcnt vmcnt(24)\n"
    "s_barrier\n"
    "s_add_u32 m0, s39, 35840\n"
    "s_mov_b32 s38, 2621440\n"
    "buffer_load_dwordx4 v3, s[20:23], s38 offen lds\n"
    "s_add_u32 m0, s39, 36928\n"
    "s_mov_b32 s38, 2637824\n"
    "buffer_load_dwordx4 v3, s[20:23], s38 offen lds\n"
    "s_add_u32 m0, s39, 38016\n"
    "s_mov_b32 s38, 2654208\n"
    "buffer_load_dwordx4 v3, s[20:23], s38 offen lds\n"
    "s_add_u32 m0, s39, 39104\n"
    "s_mov_b32 s38, 2670592\n"
    "buffer_load_dwordx4 v3, s[20:23], s38 offen lds\n"
    "v_add_u32 v8, 53760, v4\n"
    "v_xor_b32 v9, 32, v8\n"
    "ds_read_b64_tr_b16 a[24:25], v8 offset:0\n"
    "ds_read_b64_tr_b16 a[26:27], v8 offset:2176\n"
    "ds_read_b64_tr_b16 a[28:29], v9 offset:0\n"
    "ds_read_b64_tr_b16 a[30:31], v9 offset:2176\n"
    "ds_read_b64_tr_b16 a[152:153], v8 offset:256\n"
    "ds_read_b64_tr_b16 a[154:155], v8 offset:2432\n"
    "ds_read_b64_tr_b16 a[156:157], v9 offset:256\n"
    "ds_read_b64_tr_b16 a[158:159], v9 offset:2432\n"
    "s_waitcnt lgkmcnt(0)\n"
    "s_waitcnt vmcnt(24)\n"
    "s_barrier\n"
    "s_add_u32 m0, s39, 53760\n"
    "s_mov_b32 s38, 2883584\n"
    "buffer_load_dwordx4 v3, s[20:23], s38 offen lds\n"
    "s_add_u32 m0, s39, 54848\n"
    "s_mov_b32 s38, 2899968\n"
    "buffer_load_dwordx4 v3, s[20:23], s38 offen lds\n"
    "s_add_u32 m0, s39, 55936\n"
    "s_mov_b32 s38, 2916352\n"
    "buffer_load_dwordx4 v3, s[20:23], s38 offen lds\n"
    "s_add_u32 m0, s39, 57024\n"
    "s_mov_b32 s38, 2932736\n"
    "buffer_load_dwordx4 v3, s[20:23], s38 offen lds\n"
    "v_add_u32 v8, 71680, v4\n"
    "v_xor_b32 v9, 32, v8\n"
    "ds_read_b64_tr_b16 a[32:33], v8 offset:0\n"
    "ds_read_b64_tr_b16 a[34:35], v8 offset:2176\n"
    "ds_read_b64_tr_b16 a[36:37], v9 offset:0\n"
    "ds_read_b64_tr_b16 a[38:39], v9 offset:2176\n"
    "ds_read_b64_tr_b16 a[160:161], v8 offset:256\n"
    "ds_read_b64_tr_b16 a[162:163], v8 offset:2432\n"
    "ds_read_b64_tr_b16 a[164:165], v9 offset:256\n"
    "ds_read_b64_tr_b16 a[166:167], v9 offset:2432\n"
    "s_waitcnt lgkmcnt(0)\n"
    "s_waitcnt vmcnt(24)\n"
    "s_barrier\n"
    "s_add_u32 m0, s39, 71680\n"
    "s_mov_b32 s38, 3145728\n"
    "buffer_load_dwordx4 v3, s[20:23], s38 offen lds\n"
    "s_add_u32 m0, s39, 72768\n"
    "s_mov_b32 s38, 3162112\n"
    "buffer_load_dwordx4 v3, s[20:23], s38 offen lds\n"
    "s_add_u32 m0, s39, 73856\n"
    "s_mov_b32 s38, 3178496\n"
    "buffer_load_dwordx4 v3, s[20:23], s38 offen lds\n"
    "s_add_u32 m0, s39, 74944\n"
    "s_mov_b32 s38, 3194880\n"
    "buffer_load_dwordx4 v3, s[20:23], s38 offen lds\n"
    "v_add_u32 v8, 89600, v4\n"
    "v_xor_b32 v9, 32, v8\n"
    "ds_read_b64_tr_b16 a[40:41], v8 offset:0\n"
    "ds_read_b64_tr_b16 a[42:43], v8 offset:2176\n"
    "ds_read_b64_tr_b16 a[44:45], v9 offset:0\n"
    "ds_read_b64_tr_b16 a[46:47], v9 offset:2176\n"
    "ds_read_b64_tr_b16 a[168:169], v8 offset:256\n"
    "ds_read_b64_tr_b16 a[170:171], v8 offset:2432\n"
    "ds_read_b64_tr_b16 a[172:173], v9 offset:256\n"
    "ds_read_b64_tr_b16 a[174:175], v9 offset:2432\n"
    "s_waitcnt lgkmcnt(0)\n"
    "s_waitcnt vmcnt(24)\n"
    "s_barrier\n"
    "s_add_u32 m0, s39, 89600\n"
    "s_mov_b32 s38, 3407872\n"
    "buffer_load_dwordx4 v3, s[20:23], s38 offen lds\n"
    "s_add_u32 m0, s39, 90688\n"
    "s_mov_b32 s38, 3424256\n"
    "buffer_load_dwordx4 v3, s[20:23], s38 offen lds\n"
    "s_add_u32 m0, s39, 91776\n"
    "s_mov_b32 s38, 3440640\n"
    "buffer_load_dwordx4 v3, s[20:23], s38 offen lds\n"
    "s_add_u32 m0, s39, 92864\n"
    "s_mov_b32 s38, 3457024\n"
    "buffer_load_dwordx4 v3, s[20:23], s38 offen lds\n"
    "v_add_u32 v8, 107520, v4\n"
    "v_xor_b32 v9, 32, v8\n"
    "ds_read_b64_tr_b16 a[48:49], v8 offset:0\n"
    "ds_read_b64_tr_b16 a[50:51], v8 offset:2176\n"
    "ds_read_b64_tr_b16 a[52:53], v9 offset:0\n"
    "ds_read_b64_tr_b16 a[54:55], v9 offset:2176\n"
    "ds_read_b64_tr_b16 a[176:177], v8 offset:256\n"
    "ds_read_b64_tr_b16 a[178:179], v8 offset:2432\n"
    "ds_read_b64_tr_b16 a[180:181], v9 offset:256\n"
    "ds_read_b64_tr_b16 a[182:183], v9 offset:2432\n"
    "s_waitcnt lgkmcnt(0)\n"
    "s_waitcnt vmcnt(24)\n"
    "s_barrier\n"
    "s_add_u32 m0, s39, 107520\n"
    "s_mov_b32 s38, 3670016\n"
    "buffer_load_dwordx4 v3, s[20:23], s38 offen lds\n"
    "s_add_u32 m0, s39, 108608\n"
    "s_mov_b32 s38, 3686400\n"
    "buffer_load_dwordx4 v3, s[20:23], s38 offen lds\n"
    "s_add_u32 m0, s39, 109696\n"
    "s_mov_b32 s38, 3702784\n"
    "buffer_load_dwordx4 v3, s[20:23], s38 offen lds\n"
    "s_add_u32 m0, s39, 110784\n"
    "s_mov_b32 s38, 3719168\n"
    "buffer_load_dwordx4 v3, s[20:23], s38 offen lds\n"
    "v_add_u32 v8, 125440, v4\n"
    "v_xor_b32 v9, 32, v8\n"
    "ds_read_b64_tr_b16 a[56:57], v8 offset:0\n"
    "ds_read_b64_tr_b16 a[58:59], v8 offset:2176\n"
    "ds_read_b64_tr_b16 a[60:61], v9 offset:0\n"
    "ds_read_b64_tr_b16 a[62:63], v9 offset:2176\n"
    "ds_read_b64_tr_b16 a[184:185], v8 offset:256\n"
    "ds_read_b64_tr_b16 a[186:187], v8 offset:2432\n"
    "ds_read_b64_tr_b16 a[188:189], v9 offset:256\n"
    "ds_read_b64_tr_b16 a[190:191], v9 offset:2432\n"
    "s_waitcnt lgkmcnt(0)\n"
    "s_waitcnt vmcnt(24)\n"
    "s_barrier\n"
    "s_add_u32 m0, s39, 125440\n"
    "s_mov_b32 s38, 3932160\n"
    "buffer_load_dwordx4 v3, s[20:23], s38 offen lds\n"
    "s_add_u32 m0, s39, 126528\n"
    "s_mov_b32 s38, 3948544\n"
    "buffer_load_dwordx4 v3, s[20:23], s38 offen lds\n"
    "s_add_u32 m0, s39, 127616\n"
    "s_mov_b32 s38, 3964928\n"
    "buffer_load_dwordx4 v3, s[20:23], s38 offen lds\n"
    "s_add_u32 m0, s39, 128704\n"
    "s_mov_b32 s38, 3981312\n"
    "buffer_load_dwordx4 v3, s[20:23], s38 offen lds\n"
    "v_add_u32 v8, 0, v4\n"
    "v_xor_b32 v9, 32, v8\n"
    "ds_read_b64_tr_b16 a[64:65], v8 offset:0\n"
    "ds_read_b64_tr_b16 a[66:67], v8 offset:2176\n"
    "ds_read_b64_tr_b16 a[68:69], v9 offset:0\n"
    "ds_read_b64_tr_b16 a[70:71], v9 offset:2176\n"
    "ds_read_b64_tr_b16 a[192:193], v8 offset:256\n"
    "ds_read_b64_tr_b16 a[194:195], v8 offset:2432\n"
    "ds_read_b64_tr_b16 a[196:197], v9 offset:256\n"
    "ds_read_b64_tr_b16 a[198:199], v9 offset:2432\n"
    "s_waitcnt lgkmcnt(0)\n"
    "s_waitcnt vmcnt(24)\n"
    "s_barrier\n"
    "v_add_u32 v8, 17920, v4\n"
    "v_xor_b32 v9, 32, v8\n"
    "ds_read_b64_tr_b16 a[72:73], v8 offset:0\n"
    "ds_read_b64_tr_b16 a[74:75], v8 offset:2176\n"
    "ds_read_b64_tr_b16 a[76:77], v9 offset:0\n"
    "ds_read_b64_tr_b16 a[78:79], v9 offset:2176\n"
    "ds_read_b64_tr_b16 a[200:201], v8 offset:256\n"
    "ds_read_b64_tr_b16 a[202:203], v8 offset:2432\n"
    "ds_read_b64_tr_b16 a[204:205], v9 offset:256\n"
    "ds_read_b64_tr_b16 a[206:207], v9 offset:2432\n"
    "s_waitcnt lgkmcnt(0)\n"
    "s_waitcnt vmcnt(20)\n"
    "s_barrier\n"
    "v_add_u32 v8, 35840, v4\n"
    "v_xor_b32 v9, 32, v8\n"
    "ds_read_b64_tr_b16 a[80:81], v8 offset:0\n"
    "ds_read_b64_tr_b16 a[82:83], v8 offset:2176\n"
    "ds_read_b64_tr_b16 a[84:85], v9 offset:0\n"
    "ds_read_b64_tr_b16 a[86:87], v9 offset:2176\n"
    "ds_read_b64_tr_b16 a[208:209], v8 offset:256\n"
    "ds_read_b64_tr_b16 a[210:211], v8 offset:2432\n"
    "ds_read_b64_tr_b16 a[212:213], v9 offset:256\n"
    "ds_read_b64_tr_b16 a[214:215], v9 offset:2432\n"
    "s_waitcnt lgkmcnt(0)\n"
    "s_waitcnt vmcnt(16)\n"
    "s_barrier\n"
    "v_add_u32 v8, 53760, v4\n"
    "v_xor_b32 v9, 32, v8\n"
    "ds_read_b64_tr_b16 a[88:89], v8 offset:0\n"
    "ds_read_b64_tr_b16 a[90:91], v8 offset:2176\n"
    "ds_read_b64_tr_b16 a[92:93], v9 offset:0\n"
    "ds_read_b64_tr_b16 a[94:95], v9 offset:2176\n"
    "ds_read_b64_tr_b16 a[216:217], v8 offset:256\n"
    "ds_read_b64_tr_b16 a[218:219], v8 offset:2432\n"
    "ds_read_b64_tr_b16 a[220:221], v9 offset:256\n"
    "ds_read_b64_tr_b16 a[222:223], v9 offset:2432\n"
    "s_waitcnt lgkmcnt(0)\n"
    "s_waitcnt vmcnt(12)\n"
    "s_barrier\n"
    "v_add_u32 v8, 71680, v4\n"
    "v_xor_b32 v9, 32, v8\n"
    "ds_read_b64_tr_b16 a[96:97], v8 offset:0\n"
    "ds_read_b64_tr_b16 a[98:99], v8 offset:2176\n"
    "ds_read_b64_tr_b16 a[100:101], v9 offset:0\n"
    "ds_read_b64_tr_b16 a[102:103], v9 offset:2176\n"
    "ds_read_b64_tr_b16 a[224:225], v8 offset:256\n"
    "ds_read_b64_tr_b16 a[226:227], v8 offset:2432\n"
    "ds_read_b64_tr_b16 a[228:229], v9 offset:256\n"
    "ds_read_b64_tr_b16 a[230:231], v9 offset:2432\n"
    "s_waitcnt lgkmcnt(0)\n"
    "s_waitcnt vmcnt(8)\n"
    "s_barrier\n"
    "v_add_u32 v8, 89600, v4\n"
    "v_xor_b32 v9, 32, v8\n"
    "ds_read_b64_tr_b16 a[104:105], v8 offset:0\n"
    "ds_read_b64_tr_b16 a[106:107], v8 offset:2176\n"
    "ds_read_b64_tr_b16 a[108:109], v9 offset:0\n"
    "ds_read_b64_tr_b16 a[110:111], v9 offset:2176\n"
    "ds_read_b64_tr_b16 a[232:233], v8 offset:256\n"
    "ds_read_b64_tr_b16 a[234:235], v8 offset:2432\n"
    "ds_read_b64_tr_b16 a[236:237], v9 offset:256\n"
    "ds_read_b64_tr_b16 a[238:239], v9 offset:2432\n"
    "s_waitcnt lgkmcnt(0)\n"
    "s_waitcnt vmcnt(4)\n"
    "s_barrier\n"
    "v_add_u32 v8, 107520, v4\n"
    "v_xor_b32 v9, 32, v8\n"
    "ds_read_b64_tr_b16 a[112:113], v8 offset:0\n"
    "ds_read_b64_tr_b16 a[114:115], v8 offset:2176\n"
    "ds_read_b64_tr_b16 a[116:117], v9 offset:0\n"
    "ds_read_b64_tr_b16 a[118:119], v9 offset:2176\n"
    "ds_read_b64_tr_b16 a[240:241], v8 offset:256\n"
    "ds_read_b64_tr_b16 a[242:243], v8 offset:2432\n"
    "ds_read_b64_tr_b16 a[244:245], v9 offset:256\n"
    "ds_read_b64_tr_b16 a[246:247], v9 offset:2432\n"
    "s_waitcnt lgkmcnt(0)\n"
    "s_waitcnt vmcnt(0)\n"
    "s_barrier\n"
    "v_add_u32 v8, 125440, v4\n"
    "v_xor_b32 v9, 32, v8\n"
    "ds_read_b64_tr_b16 a[120:121], v8 offset:0\n"
    "ds_read_b64_tr_b16 a[122:123], v8 offset:2176\n"
    "ds_read_b64_tr_b16 a[124:125], v9 offset:0\n"
    "ds_read_b64_tr_b16 a[126:127], v9 offset:2176\n"
    "ds_read_b64_tr_b16 a[248:249], v8 offset:256\n"
    "ds_read_b64_tr_b16 a[250:251], v8 offset:2432\n"
    "ds_read_b64_tr_b16 a[252:253], v9 offset:256\n"
    "ds_read_b64_tr_b16 a[254:255], v9 offset:2432\n"
    "s_waitcnt lgkmcnt(0)\n"
    "s_lshl_b32 s38, s61, 7\n"
    "v_mov_b32 v8, s38\n"
    "s_mov_b32 s37, 0x80000\n"
    "LRY%=:\n"
    "global_load_dword v6, v8, s[24:25] offset:640 sc1\n"
    "s_waitcnt vmcnt(0)\n"
    "v_readfirstlane_b32 s60, v6\n"
    "s_cmp_lg_u32 s60, 0\n"
    "s_cbranch_scc1 LRZ%=\n"
    "s_sleep 2\n"
    "s_sub_u32 s37, s37, 1\n"
    "s_cbranch_scc0 LRY%=\n"
    "s_endpgm\n"
    "LRZ%=:\n"
    "s_lshl_b32 s38, s60, 13\n"
    "s_and_b32 s38, s38, 0x2000\n"
    "s_xor_b32 s48, s48, s38\n"
    "s_xor_b32 s49, s49, s38\n"
    "s_xor_b32 s50, s50, s38\n"
    "s_xor_b32 s51, s51, s38\n"
    "s_xor_b32 s53, s53, s38\n"
    "s_xor_b32 s26, s26, s38\n"
    "s_barrier\n"
    "v_bfe_u32 v9, v0, 4, 1\n"
    "v_lshlrev_b32 v4, 8, v9\n"
    "v_lshlrev_b32 v9, 14, v9\n"
    "s_mov_b32 s63, s45\n"
    "s_lshr_b32 s38, s63, 8\n"
    "s_xor_b32 s38, s38, s48\n"
    "s_and_b32 s38, s38, 0x2000\n"
    "s_or_b32 s63, s63, s38\n"
    "s_add_u32 s39, s44, s43\n"
    "s_add_u32 s69, s63, 4096\n"
    "s_add_u32 m0, s39, 0\n"
    "s_nop 0\n"
    "buffer_load_dwordx4 v0, s[16:19], s63 offen offset:0 sc1 lds\n"
    "s_add_u32 m0, s39, 32\n"
    "s_nop 0\n"
    "buffer_load_dwordx4 v0, s[16:19], s63 offen offset:1024 sc1 lds\n"
    "s_add_u32 m0, s39, 64\n"
    "s_nop 0\n"
    "buffer_load_dwordx4 v0, s[16:19], s63 offen offset:2048 sc1 lds\n"
    "s_add_u32 m0, s39, 96\n"
    "s_nop 0\n"
    "buffer_load_dwordx4 v0, s[16:19], s63 offen offset:3072 sc1 lds\n"
    "s_add_u32 m0, s39, 4224\n"
    "s_nop 0\n"
    "buffer_load_dwordx4 v0, s[16:19], s69 offen offset:0 sc1 lds\n"
    "s_add_u32 m0, s39, 4256\n"
    "s_nop 0\n"
    "buffer_load_dwordx4 v0, s[16:19], s69 offen offset:1024 sc1 lds\n"
    "s_add_u32 m0, s39, 4288\n"
    "s_nop 0\n"
    "buffer_load_dwordx4 v0, s[16:19], s69 offen offset:2048 sc1 lds\n"
    "s_add_u32 m0, s39, 4320\n"
    "s_nop 0\n"
    "buffer_load_dwordx4 v0, s[16:19], s69 offen offset:3072 sc1 lds\n"
    "s_add_u32 s45, s45, 131072\n"
    "v_mov_b32 v10, 101376\n"
    "v_lshl_add_u32 v11, s36, 3, v10\n"
    "LT%=:\n"
    "s_cmp_lg_u32 s36, 0\n"
    "s_cbranch_scc1 LB%=\n"
    "s_mov_b32 s37, 0x80000\n"
    "LP%=:\n"
    "buffer_load_dword v6, off, s[16:19], s53 sc1\n"
    "s_waitcnt vmcnt(0)\n"
    "v_readfirstlane_b32 s38, v6\n"
    "s_mov_b32 s60, 3\n"
    "s_cmp_ge_u32 s41, 64\n"
    "s_cbranch_scc1 LD%=\n"
    "s_mov_b32 s60, 2\n"
    "s_cmp_lg_u32 s38, 0\n"
    "s_cbranch_scc1 LD%=\n"
    "s_add_u32 s38, s41, 8\n"
    "s_cmp_lt_u32 s41, 2\n"
    "s_cselect_b32 s38, 12, s38\n"
    "s_min_u32 s38, s38, 64\n"
    "s_mov_b32 s60, 1\n"
    "s_cmp_lt_u32 s40, s38\n"
    "s_cbranch_scc1 LD%=\n"
    "s_mov_b32 s60, 0\n"
    "s_cmp_lt_u32 s52, 0x80000000\n"
    "s_cbranch_scc1 LD%=\n"
    "s_sleep 1\n"
    "s_sub_u32 s37, s37, 1\n"
    "s_cbranch_scc0 LP%=\n"
    "buffer_store_dword v10, off, s[16:19], s58 sc1\n"
    "s_mov_b32 s60, 3\n"
    "LD%=:\n"
    "v_mov_b32 v6, s60\n"
    "ds_write_b32 v10, v6\n"
    "LB%=:\n"
    "s_waitcnt vmcnt(0) lgkmcnt(0)\n"
    "s_barrier\n"
    "ds_read_b32 v6, v10\n"
    "v_readfirstlane_b32 s38, v5\n"
    "s_or_b32 s52, s52, s68\n"
    "s_or_b32 s38, s38, s68\n"
    "s_cbranch_scc0 LK0_%=\n"
    "LJ0_%=:\n"
    "buffer_store_dword v5, v9, s[16:19], s52 offen sc1\n"
    "s_mov_b32 s52, 0x80000000\n"
    "s_lshl_b32 s63, s41, 16\n"
    "s_add_u32 s63, s63, s46\n"
    "s_lshr_b32 s38, s63, 8\n"
    "s_xor_b32 s38, s38, s49\n"
    "s_and_b32 s38, s38, 0x2000\n"
    "s_or_b32 s63, s63, s38\n"
    "s_waitcnt lgkmcnt(0)\n"
    "v_readfirstlane_b32 s60, v6\n"
    "s_cmp_eq_u32 s60, 1\n"
    "s_cbranch_scc1 L1%=\n"
    "s_cmp_eq_u32 s60, 2\n"
    "s_cbranch_scc0 LX%=\n"
    "s_add_u32 s39, s44, 67584\n"
    "s_add_u32 s69, s63, 4096\n"
    "s_add_u32 m0, s39, 0\n"
    "s_nop 0\n"
    "buffer_load_dwordx4 v0, s[16:19], s63 offen offset:0 sc1 lds\n"
    "s_add_u32 m0, s39, 32\n"
    "s_nop 0\n"
    "buffer_load_dwordx4 v0, s[16:19], s63 offen offset:1024 sc1 lds\n"
    "s_add_u32 m0, s39, 64\n"
    "s_nop 0\n"
    "buffer_load_dwordx4 v0, s[16:19], s63 offen offset:2048 sc1 lds\n"
    "s_add_u32 m0, s39, 96\n"
    "s_nop 0\n"
    "buffer_load_dwordx4 v0, s[16:19], s63 offen offset:3072 sc1 lds\n"
    "s_add_u32 m0, s39, 4224\n"
    "s_nop 0\n"
    "buffer_load_dwordx4 v0, s[16:19], s69 offen offset:0 sc1 lds\n"
    "s_add_u32 m0, s39, 4256\n"
    "s_nop 0\n"
    "buffer_load_dwordx4 v0, s[16:19], s69 offen offset:1024 sc1 lds\n"
    "s_add_u32 m0, s39, 4288\n"
    "s_nop 0\n"
    "buffer_load_dwordx4 v0, s[16:19], s69 offen offset:2048 sc1 lds\n"
    "s_add_u32 m0, s39, 4320\n"
    "s_nop 0\n"
    "buffer_load_dwordx4 v0, s[16:19], s69 offen offset:3072 sc1 lds\n"
    "s_waitcnt vmcnt(0)\n"
    "L2%=:\n"
    "s_mul_i32 s59, s41, 2621440\n"
    "s_add_u32 s59, s59, s47\n"
    "s_lshr_b32 s38, s59, 8\n"
    "s_xor_b32 s38, s38, s49\n"
    "s_and_b32 s38, s38, 0x2000\n"
    "s_or_b32 s59, s59, s38\n"
    "s_lshl_b32 s64, s41, 7\n"
    "s_add_u32 s64, s64, s51\n"
    "s_lshl_b32 s67, s41, 2\n"
    "s_add_u32 s67, s67, s26\n"
    "s_add_u32 s41, s41, 2\n"
    "s_add_u32 s53, s53, 8\n"
    "s_mov_b32 s60, 2\n"
    "LG%=:\n"
    "v_add_u32 v8, 67584, v1\n"
    "s_waitcnt vmcnt(2)\n"
    "s_barrier\n"
    "s_cmp_eq_u32 s61, 1\n"
    "s_cbranch_scc1 LHV%=\n"
    "s_add_u32 s62, s59, 4096\n"
    "ds_read_b128 v[20:23], v8 offset:0\n"
    "ds_read_b128 v[24:27], v8 offset:16896\n"
    "ds_read_b128 v[28:31], v8 offset:64\n"
    "ds_read_b128 v[32:35], v8 offset:16960\n"
    "ds_read_b128 v[36:39], v8 offset:128\n"
    "ds_read_b128 v[40:43], v8 offset:17024\n"
    "ds_read_b128 v[44:47], v8 offset:192\n"
    "ds_read_b128 v[48:51], v8 offset:17088\n"
    "s_waitcnt lgkmcnt(7)\n"
    "v_mfma_f32_16x16x32_bf16 v[64:67], a[0:3], v[20:23], 0\n"
    "ds_read_b128 v[52:55], v8 offset:256\n"
    "s_waitcnt lgkmcnt(7)\n"
    "v_mfma_f32_16x16x32_bf16 v[68:71], a[0:3], v[24:27], 0\n"
    "ds_read_b128 v[56:59], v8 offset:17152\n"
    "v_max3_u32 v6, v23, v27, v23\n"
    "v_mfma_f32_16x16x32_bf16 v[76:79], a[4:7], v[24:27], 0\n"
    "v_mfma_f32_16x16x32_bf16 v[72:75], a[4:7], v[20:23], 0\n"
    "s_waitcnt lgkmcnt(7)\n"
    "v_mfma_f32_16x16x32_bf16 v[64:67], a[8:11], v[28:31], v[64:67]\n"
    "ds_read_b128 v[20:23], v8 offset:320\n"
    "s_waitcnt lgkmcnt(7)\n"
    "v_mfma_f32_16x16x32_bf16 v[68:71], a[8:11], v[32:35], v[68:71]\n"
    "ds_read_b128 v[24:27], v8 offset:17216\n"
    "v_max3_u32 v6, v31, v35, v6\n"
    "v_mfma_f32_16x16x32_bf16 v[76:79], a[12:15], v[32:35], v[76:79]\n"
    "v_mfma_f32_16x16x32_bf16 v[72:75], a[12:15], v[28:31], v[72:75]\n"
    "s_waitcnt lgkmcnt(7)\n"
    "v_mfma_f32_16x16x32_bf16 v[64:67], a[16:19], v[36:39], v[64:67]\n"
    "ds_read_b128 v[28:31], v8 offset:384\n"
    "s_waitcnt lgkmcnt(7)\n"
    "v_mfma_f32_16x16x32_bf16 v[68:71], a[16:19], v[40:43], v[68:71]\n"
    "ds_read_b128 v[32:35], v8 offset:17280\n"
    "v_max3_u32 v6, v39, v43, v6\n"
    "v_mfma_f32_16x16x32_bf16 v[76:79], a[20:23], v[40:43], v[76:79]\n"
    "v_mfma_f32_16x16x32_bf16 v[72:75], a[20:23], v[36:39], v[72:75]\n"
    "s_waitcnt lgkmcnt(7)\n"
    "v_mfma_f32_16x16x32_bf16 v[64:67], a[24:27], v[44:47], v[64:67]\n"
    "ds_read_b128 v[36:39], v8 offset:448\n"
    "s_waitcnt lgkmcnt(7)\n"
    "v_mfma_f32_16x16x32_bf16 v[68:71], a[24:27], v[48:51], v[68:71]\n"
    "ds_read_b128 v[40:43], v8 offset:17344\n"
    "v_max3_u32 v6, v47, v51, v6\n"
    "v_mfma_f32_16x16x32_bf16 v[76:79], a[28:31], v[48:51], v[76:79]\n"
    "v_mfma_f32_16x16x32_bf16 v[72:75], a[28:31], v[44:47], v[72:75]\n"
    "s_waitcnt lgkmcnt(7)\n"
    "v_mfma_f32_16x16x32_bf16 v[64:67], a[32:35], v[52:55], v[64:67]\n"
    "ds_read_b128 v[44:47], v8 offset:512\n"
    "s_waitcnt lgkmcnt(7)\n"
    "v_mfma_f32_16x16x32_bf16 v[68:71], a[32:35], v[56:59], v[68:71]\n"
    "ds_read_b128 v[48:51], v8 offset:17408\n"
    "v_max3_u32 v6, v55, v59, v6\n"
    "v_mfma_f32_16x16x32_bf16 v[76:79], a[36:39], v[56:59], v[76:79]\n"
    "v_mfma_f32_16x16x32_bf16 v[72:75], a[36:39], v[52:55], v[72:75]\n"
    "s_waitcnt lgkmcnt(7)\n"
    "v_mfma_f32_16x16x32_bf16 v[64:67], a[40:43], v[20:23], v[64:67]\n"
    "ds_read_b128 v[52:55], v8 offset:576\n"
    "s_waitcnt lgkmcnt(7)\n"
    "v_mfma_f32_16x16x32_bf16 v[68:71], a[40:43], v[24:27], v[68:71]\n"
    "ds_read_b128 v[56:59], v8 offset:17472\n"
    "v_max3_u32 v6, v23, v27, v6\n"
    "v_mfma_f32_16x16x32_bf16 v[76:79], a[44:47], v[24:27], v[76:79]\n"
    "v_mfma_f32_16x16x32_bf16 v[72:75], a[44:47], v[20:23], v[72:75]\n"
    "s_waitcnt lgkmcnt(7)\n"
    "v_mfma_f32_16x16x32_bf16 v[64:67], a[48:51], v[28:31], v[64:67]\n"
    "ds_read_b128 v[20:23], v8 offset:640\n"
    "s_waitcnt lgkmcnt(7)\n"
    "v_mfma_f32_16x16x32_bf16 v[68:71], a[48:51], v[32:35], v[68:71]\n"
    "ds_read_b128 v[24:27], v8 offset:17536\n"
    "v_max3_u32 v6, v31, v35, v6\n"
    "v_mfma_f32_16x16x32_bf16 v[76:79], a[52:55], v[32:35], v[76:79]\n"
    "v_mfma_f32_16x16x32_bf16 v[72:75], a[52:55], v[28:31], v[72:75]\n"
    "s_waitcnt lgkmcnt(7)\n"
    "v_mfma_f32_16x16x32_bf16 v[64:67], a[56:59], v[36:39], v[64:67]\n"
    "ds_read_b128 v[28:31], v8 offset:704\n"
    "s_waitcnt lgkmcnt(7)\n"
    "v_mfma_f32_16x16x32_bf16 v[68:71], a[56:59], v[40:43], v[68:71]\n"
    "ds_read_b128 v[32:35], v8 offset:17600\n"
    "v_max3_u32 v6, v39, v43, v6\n"
    "v_mfma_f32_16x16x32_bf16 v[76:79], a[60:63], v[40:43], v[76:79]\n"
    "v_mfma_f32_16x16x32_bf16 v[72:75], a[60:63], v[36:39], v[72:75]\n"
    "s_waitcnt lgkmcnt(7)\n"
    "v_mfma_f32_16x16x32_bf16 v[64:67], a[64:67], v[44:47], v[64:67]\n"
    "ds_read_b128 v[36:39], v8 offset:768\n"
    "s_waitcnt lgkmcnt(7)\n"
    "v_mfma_f32_16x16x32_bf16 v[68:71], a[64:67], v[48:51], v[68:71]\n"
    "ds_read_b128 v[40:43], v8 offset:17664\n"
    "v_max3_u32 v6, v47, v51, v6\n"
    "v_mfma_f32_16x16x32_bf16 v[76:79], a[68:71], v[48:51], v[76:79]\n"
    "v_mfma_f32_16x16x32_bf16 v[72:75], a[68:71], v[44:47], v[72:75]\n"
    "s_waitcnt lgkmcnt(7)\n"
    "v_mfma_f32_16x16x32_bf16 v[64:67], a[72:75], v[52:55], v[64:67]\n"
    "ds_read_b128 v[44:47], v8 offset:832\n"
    "s_waitcnt lgkmcnt(7)\n"
    "v_mfma_f32_16x16x32_bf16 v[68:71], a[72:75], v[56:59], v[68:71]\n"
    "ds_read_b128 v[48:51], v8 offset:17728\n"
    "v_max3_u32 v6, v55, v59, v6\n"
    "v_mfma_f32_16x16x32_bf16 v[76:79], a[76:79], v[56:59], v[76:79]\n"
    "v_mfma_f32_16x16x32_bf16 v[72:75], a[76:79], v[52:55], v[72:75]\n"
    "s_waitcnt lgkmcnt(7)\n"
    "v_mfma_f32_16x16x32_bf16 v[64:67], a[80:83], v[20:23], v[64:67]\n"
    "ds_read_b128 v[52:55], v8 offset:896\n"
    "s_waitcnt lgkmcnt(7)\n"
    "v_mfma_f32_16x16x32_bf16 v[68:71], a[80:83], v[24:27], v[68:71]\n"
    "ds_read_b128 v[56:59], v8 offset:17792\n"
    "v_max3_u32 v6, v23, v27, v6\n"
    "v_mfma_f32_16x16x32_bf16 v[76:79], a[84:87], v[24:27], v[76:79]\n"
    "v_mfma_f32_16x16x32_bf16 v[72:75], a[84:87], v[20:23], v[72:75]\n"
    "s_waitcnt lgkmcnt(7)\n"
    "v_mfma_f32_16x16x32_bf16 v[64:67], a[88:91], v[28:31], v[64:67]\n"
    "ds_read_b128 v[20:23], v8 offset:960\n"
    "s_waitcnt lgkmcnt(7)\n"
    "v_mfma_f32_16x16x32_bf16 v[68:71], a[88:91], v[32:35], v[68:71]\n"
    "ds_read_b128 v[24:27], v8 offset:17856\n"
    "v_max3_u32 v6, v31, v35, v6\n"
    "v_mfma_f32_16x16x32_bf16 v[76:79], a[92:95], v[32:35], v[76:79]\n"
    "v_mfma_f32_16x16x32_bf16 v[72:75], a[92:95], v[28:31], v[72:75]\n"
    "s_waitcnt lgkmcnt(7)\n"
    "v_mfma_f32_16x16x32_bf16 v[64:67], a[96:99], v[36:39], v[64:67]\n"
    "ds_read_b128 v[28:31], v8 offset:0\n"
    "s_waitcnt lgkmcnt(7)\n"
    "v_mfma_f32_16x16x32_bf16 v[68:71], a[96:99], v[40:43], v[68:71]\n"
    "ds_read_b128 v[32:35], v8 offset:16896\n"
    "s_waitcnt vmcnt(0)\n"
    "s_barrier\n"
    "v_readfirstlane_b32 s38, v5\n"
    "s_or_b32 s52, s52, s68\n"
    "s_or_b32 s38, s38, s68\n"
    "s_cbranch_scc0 LK1_%=\n"
    "LJ1_%=:\n"
    "buffer_store_dword v5, v9, s[16:19], s52 offen sc1\n"
    "s_mov_b32 s52, 0x80000000\n"
    "v_mfma_f32_16x16x32_bf16 v[76:79], a[100:103], v[40:43], v[76:79]\n"
    "v_max3_u32 v6, v39, v43, v6\n"
    "v_mfma_f32_16x16x32_bf16 v[72:75], a[100:103], v[36:39], v[72:75]\n"
    "s_waitcnt lgkmcnt(7)\n"
    "v_mfma_f32_16x16x32_bf16 v[64:67], a[104:107], v[44:47], v[64:67]\n"
    "ds_read_b128 v[36:39], v8 offset:64\n"
    "s_waitcnt lgkmcnt(7)\n"
    "v_mfma_f32_16x16x32_bf16 v[68:71], a[104:107], v[48:51], v[68:71]\n"
    "ds_read_b128 v[40:43], v8 offset:16960\n"
    "v_max3_u32 v6, v47, v51, v6\n"
    "v_mfma_f32_16x16x32_bf16 v[76:79], a[108:111], v[48:51], v[76:79]\n"
    "v_mfma_f32_16x16x32_bf16 v[72:75], a[108:111], v[44:47], v[72:75]\n"
    "s_waitcnt lgkmcnt(7)\n"
    "v_mfma_f32_16x16x32_bf16 v[64:67], a[112:115], v[52:55], v[64:67]\n"
    "ds_read_b128 v[44:47], v8 offset:128\n"
    "s_waitcnt lgkmcnt(7)\n"
    "v_mfma_f32_16x16x32_bf16 v[68:71], a[112:115], v[56:59], v[68:71]\n"
    "ds_read_b128 v[48:51], v8 offset:17024\n"
    "v_max3_u32 v6, v55, v59, v6\n"
    "v_mfma_f32_16x16x32_bf16 v[76:79], a[116:119], v[56:59], v[76:79]\n"
    "v_mfma_f32_16x16x32_bf16 v[72:75], a[116:119], v[52:55], v[72:75]\n"
    "s_waitcnt lgkmcnt(7)\n"
    "v_mfma_f32_16x16x32_bf16 v[64:67], a[120:123], v[20:23], v[64:67]\n"
    "ds_read_b128 v[52:55], v8 offset:192\n"
    "s_waitcnt lgkmcnt(7)\n"
    "v_mfma_f32_16x16x32_bf16 v[68:71], a[120:123], v[24:27], v[68:71]\n"
    "ds_read_b128 v[56:59], v8 offset:17088\n"
    "v_max3_u32 v6, v23, v27, v6\n"
    "v_mfma_f32_16x16x32_bf16 v[76:79], a[124:127], v[24:27], v[76:79]\n"
    "v_mfma_f32_16x16x32_bf16 v[72:75], a[124:127], v[20:23], v[72:75]\n"
    "s_waitcnt lgkmcnt(7)\n"
    "v_mfma_f32_16x16x32_bf16 v[80:83], a[128:131], v[28:31], 0\n"
    "ds_read_b128 v[20:23], v8 offset:256\n"
    "s_waitcnt lgkmcnt(7)\n"
    "v_mfma_f32_16x16x32_bf16 v[84:87], a[128:131], v[32:35], 0\n"
    "ds_read_b128 v[24:27], v8 offset:17152\n"
    "v_cmp_eq_u32 vcc, -1, v6\n"
    "v_mfma_f32_16x16x32_bf16 v[92:95], a[132:135], v[32:35], 0\n"
    "s_cbranch_vccnz LRD%=\n"
    "v_mfma_f32_16x16x32_bf16 v[88:91], a[132:135], v[28:31], 0\n"
    "s_waitcnt lgkmcnt(7)\n"
    "v_mfma_f32_16x16x32_bf16 v[80:83], a[136:139], v[36:39], v[80:83]\n"
    "ds_read_b128 v[28:31], v8 offset:320\n"
    "s_waitcnt lgkmcnt(7)\n"
    "v_mfma_f32_16x16x32_bf16 v[84:87], a[136:139], v[40:43], v[84:87]\n"
    "ds_read_b128 v[32:35], v8 offset:17216\n"
    "v_cvt_pk_bf16_f32 v12, v64, v72\n"
    "v_mfma_f32_16x16x32_bf16 v[92:95], a[140:143], v[40:43], v[92:95]\n"
    "v_cvt_pk_bf16_f32 v13, v65, v73\n"
    "v_mfma_f32_16x16x32_bf16 v[88:91], a[140:143], v[36:39], v[88:91]\n"
    "s_waitcnt lgkmcnt(7)\n"
    "v_cvt_pk_bf16_f32 v14, v66, v74\n"
    "v_mfma_f32_16x16x32_bf16 v[80:83], a[144:147], v[44:47], v[80:83]\n"
    "ds_read_b128 v[36:39], v8 offset:384\n"
    "s_waitcnt lgkmcnt(7)\n"
    "v_mfma_f32_16x16x32_bf16 v[84:87], a[144:147], v[48:51], v[84:87]\n"
    "ds_read_b128 v[40:43], v8 offset:17280\n"
    "v_cvt_pk_bf16_f32 v15, v67, v75\n"
    "v_mfma_f32_16x16x32_bf16 v[92:95], a[148:151], v[48:51], v[92:95]\n"
    "buffer_store_dwordx4 v[12:15], v0, s[16:19], s59 offen sc1\n"
    "v_mfma_f32_16x16x32_bf16 v[88:91], a[148:151], v[44:47], v[88:91]\n"
    "s_waitcnt lgkmcnt(7)\n"
    "v_cvt_pk_bf16_f32 v16, v68, v76\n"
    "v_mfma_f32_16x16x32_bf16 v[80:83], a[152:155], v[52:55], v[80:83]\n"
    "ds_read_b128 v[44:47], v8 offset:448\n"
    "s_waitcnt lgkmcnt(7)\n"
    "v_mfma_f32_16x16x32_bf16 v[84:87], a[152:155], v[56:59], v[84:87]\n"
    "ds_read_b128 v[48:51], v8 offset:17344\n"
    "v_cvt_pk_bf16_f32 v17, v69, v77\n"
    "v_mfma_f32_16x16x32_bf16 v[92:95], a[156:159], v[56:59], v[92:95]\n"
    "v_cvt_pk_bf16_f32 v18, v70, v78\n"
    "v_mfma_f32_16x16x32_bf16 v[88:91], a[156:159], v[52:55], v[88:91]\n"
    "s_waitcnt lgkmcnt(7)\n"
    "v_cvt_pk_bf16_f32 v19, v71, v79\n"
    "v_mfma_f32_16x16x32_bf16 v[80:83], a[160:163], v[20:23], v[80:83]\n"
    "ds_read_b128 v[52:55], v8 offset:512\n"
    "s_waitcnt lgkmcnt(7)\n"
    "v_mfma_f32_16x16x32_bf16 v[84:87], a[160:163], v[24:27], v[84:87]\n"
    "ds_read_b128 v[56:59], v8 offset:17408\n"
    "buffer_store_dwordx4 v[16:19], v0, s[16:19], s62 offen sc1\n"
    "v_mfma_f32_16x16x32_bf16 v[92:95], a[164:167], v[24:27], v[92:95]\n"
    "s_add_u32 s59, s59, 16384\n"
    "s_add_u32 s62, s62, 16384\n"
    "v_mfma_f32_16x16x32_bf16 v[88:91], a[164:167], v[20:23], v[88:91]\n"
    "s_waitcnt lgkmcnt(7)\n"
    "v_mfma_f32_16x16x32_bf16 v[80:83], a[168:171], v[28:31], v[80:83]\n"
    "ds_read_b128 v[20:23], v8 offset:576\n"
    "s_waitcnt lgkmcnt(7)\n"
    "v_mfma_f32_16x16x32_bf16 v[84:87], a[168:171], v[32:35], v[84:87]\n"
    "ds_read_b128 v[24:27], v8 offset:17472\n"
    "v_mfma_f32_16x16x32_bf16 v[92:95], a[172:175], v[32:35], v[92:95]\n"
    "v_mfma_f32_16x16x32_bf16 v[88:91], a[172:175], v[28:31], v[88:91]\n"
    "s_waitcnt lgkmcnt(7)\n"
    "v_mfma_f32_16x16x32_bf16 v[80:83], a[176:179], v[36:39], v[80:83]\n"
    "ds_read_b128 v[28:31], v8 offset:640\n"
    "s_waitcnt lgkmcnt(7)\n"
    "v_mfma_f32_16x16x32_bf16 v[84:87], a[176:179], v[40:43], v[84:87]\n"
    "ds_read_b128 v[32:35], v8 offset:17536\n"
    "v_mfma_f32_16x16x32_bf16 v[92:95], a[180:183], v[40:43], v[92:95]\n"
    "v_mfma_f32_16x16x32_bf16 v[88:91], a[180:183], v[36:39], v[88:91]\n"
    "s_waitcnt lgkmcnt(7)\n"
    "v_mfma_f32_16x16x32_bf16 v[80:83], a[184:187], v[44:47], v[80:83]\n"
    "ds_read_b128 v[36:39], v8 offset:704\n"
    "s_waitcnt lgkmcnt(7)\n"
    "v_mfma_f32_16x16x32_bf16 v[84:87], a[184:187], v[48:51], v[84:87]\n"
    "ds_read_b128 v[40:43], v8 offset:17600\n"
    "v_mfma_f32_16x16x32_bf16 v[92:95], a[188:191], v[48:51], v[92:95]\n"
    "v_mfma_f32_16x16x32_bf16 v[88:91], a[188:191], v[44:47], v[88:91]\n"
    "s_waitcnt lgkmcnt(7)\n"
    "v_mfma_f32_16x16x32_bf16 v[80:83], a[192:195], v[52:55], v[80:83]\n"
    "ds_read_b128 v[44:47], v8 offset:768\n"
    "s_waitcnt lgkmcnt(7)\n"
    "v_mfma_f32_16x16x32_bf16 v[84:87], a[192:195], v[56:59], v[84:87]\n"
    "ds_read_b128 v[48:51], v8 offset:17664\n"
    "buffer_load_dword v5, off, s[16:19], s67 sc1\n"
    "v_mfma_f32_16x16x32_bf16 v[92:95], a[196:199], v[56:59], v[92:95]\n"
    "v_mfma_f32_16x16x32_bf16 v[88:91], a[196:199], v[52:55], v[88:91]\n"
    "s_waitcnt lgkmcnt(7)\n"
    "v_mfma_f32_16x16x32_bf16 v[80:83], a[200:203], v[20:23], v[80:83]\n"
    "ds_read_b128 v[52:55], v8 offset:832\n"
    "s_waitcnt lgkmcnt(7)\n"
    "v_mfma_f32_16x16x32_bf16 v[84:87], a[200:203], v[24:27], v[84:87]\n"
    "ds_read_b128 v[56:59], v8 offset:17728\n"
    "v_mfma_f32_16x16x32_bf16 v[92:95], a[204:207], v[24:27], v[92:95]\n"
    "v_mfma_f32_16x16x32_bf16 v[88:91], a[204:207], v[20:23], v[88:91]\n"
    "s_waitcnt lgkmcnt(7)\n"
    "v_mfma_f32_16x16x32_bf16 v[80:83], a[208:211], v[28:31], v[80:83]\n"
    "ds_read_b128 v[20:23], v8 offset:896\n"
    "s_waitcnt lgkmcnt(7)\n"
    "v_mfma_f32_16x16x32_bf16 v[84:87], a[208:211], v[32:35], v[84:87]\n"
    "ds_read_b128 v[24:27], v8 offset:17792\n"
    "v_mfma_f32_16x16x32_bf16 v[92:95], a[212:215], v[32:35], v[92:95]\n"
    "v_mfma_f32_16x16x32_bf16 v[88:91], a[212:215], v[28:31], v[88:91]\n"
    "s_waitcnt lgkmcnt(7)\n"
    "v_mfma_f32_16x16x32_bf16 v[80:83], a[216:219], v[36:39], v[80:83]\n"
    "ds_read_b128 v[28:31], v8 offset:960\n"
    "s_waitcnt lgkmcnt(7)\n"
    "v_mfma_f32_16x16x32_bf16 v[84:87], a[216:219], v[40:43], v[84:87]\n"
    "ds_read_b128 v[32:35], v8 offset:17856\n"
    "v_mfma_f32_16x16x32_bf16 v[92:95], a[220:223], v[40:43], v[92:95]\n"
    "v_mfma_f32_16x16x32_bf16 v[88:91], a[220:223], v[36:39], v[88:91]\n"
    "s_waitcnt lgkmcnt(7)\n"
    "v_mfma_f32_16x16x32_bf16 v[80:83], a[224:227], v[44:47], v[80:83]\n"
    "s_waitcnt lgkmcnt(6)\n"
    "v_mfma_f32_16x16x32_bf16 v[84:87], a[224:227], v[48:51], v[84:87]\n"
    "v_mfma_f32_16x16x32_bf16 v[92:95], a[228:231], v[48:51], v[92:95]\n"
    "v_mfma_f32_16x16x32_bf16 v[88:91], a[228:231], v[44:47], v[88:91]\n"
    "s_waitcnt lgkmcnt(5)\n"
    "v_mfma_f32_16x16x32_bf16 v[80:83], a[232:235], v[52:55], v[80:83]\n"
    "s_waitcnt lgkmcnt(4)\n"
    "v_mfma_f32_16x16x32_bf16 v[84:87], a[232:235], v[56:59], v[84:87]\n"
    "v_mfma_f32_16x16x32_bf16 v[92:95], a[236:239], v[56:59], v[92:95]\n"
    "v_mfma_f32_16x16x32_bf16 v[88:91], a[236:239], v[52:55], v[88:91]\n"
    "s_waitcnt lgkmcnt(3)\n"
    "v_mfma_f32_16x16x32_bf16 v[80:83], a[240:243], v[20:23], v[80:83]\n"
    "s_waitcnt lgkmcnt(2)\n"
    "v_mfma_f32_16x16x32_bf16 v[84:87], a[240:243], v[24:27], v[84:87]\n"
    "v_mfma_f32_16x16x32_bf16 v[92:95], a[244:247], v[24:27], v[92:95]\n"
    "v_mfma_f32_16x16x32_bf16 v[88:91], a[244:247], v[20:23], v[88:91]\n"
    "s_waitcnt lgkmcnt(1)\n"
    "v_mfma_f32_16x16x32_bf16 v[80:83], a[248:251], v[28:31], v[80:83]\n"
    "s_waitcnt lgkmcnt(0)\n"
    "v_mfma_f32_16x16x32_bf16 v[84:87], a[248:251], v[32:35], v[84:87]\n"
    "v_mfma_f32_16x16x32_bf16 v[92:95], a[252:255], v[32:35], v[92:95]\n"
    "v_mfma_f32_16x16x32_bf16 v[88:91], a[252:255], v[28:31], v[88:91]\n"
    "s_nop 7\n"
    "v_cvt_pk_bf16_f32 v12, v80, v88\n"
    "v_cvt_pk_bf16_f32 v13, v81, v89\n"
    "v_cvt_pk_bf16_f32 v14, v82, v90\n"
    "v_cvt_pk_bf16_f32 v15, v83, v91\n"
    "buffer_store_dwordx4 v[12:15], v0, s[16:19], s59 offen sc1\n"
    "v_cvt_pk_bf16_f32 v16, v84, v92\n"
    "v_cvt_pk_bf16_f32 v17, v85, v93\n"
    "v_cvt_pk_bf16_f32 v18, v86, v94\n"
    "v_cvt_pk_bf16_f32 v19, v87, v95\n"
    "buffer_store_dwordx4 v[16:19], v0, s[16:19], s62 offen sc1\n"
    "s_mov_b32 s52, s64\n"
    "s_branch LHE%=\n"
    "LHV%=:\n"
    "s_add_u32 s62, s59, 1024\n"
    "ds_read_b128 v[20:23], v8 offset:0\n"
    "ds_read_b128 v[24:27], v8 offset:16896\n"
    "ds_read_b128 v[28:31], v8 offset:64\n"
    "ds_read_b128 v[32:35], v8 offset:16960\n"
    "ds_read_b128 v[36:39], v8 offset:128\n"
    "ds_read_b128 v[40:43], v8 offset:17024\n"
    "ds_read_b128 v[44:47], v8 offset:192\n"
    "ds_read_b128 v[48:51], v8 offset:17088\n"
    "s_waitcnt lgkmcnt(7)\n"
    "v_mfma_f32_16x16x32_bf16 v[64:67], v[20:23], a[0:3], 0\n"
    "ds_read_b128 v[52:55], v8 offset:256\n"
    "s_waitcnt lgkmcnt(7)\n"
    "v_mfma_f32_16x16x32_bf16 v[68:71], v[24:27], a[0:3], 0\n"
    "ds_read_b128 v[56:59], v8 offset:17152\n"
    "v_max3_u32 v6, v23, v27, v23\n"
    "v_mfma_f32_16x16x32_bf16 v[76:79], v[24:27], a[4:7], 0\n"
    "v_mfma_f32_16x16x32_bf16 v[72:75], v[20:23], a[4:7], 0\n"
    "s_waitcnt lgkmcnt(7)\n"
    "v_mfma_f32_16x16x32_bf16 v[64:67], v[28:31], a[8:11], v[64:67]\n"
    "ds_read_b128 v[20:23], v8 offset:320\n"
    "s_waitcnt lgkmcnt(7)\n"
    "v_mfma_f32_16x16x32_bf16 v[68:71], v[32:35], a[8:11], v[68:71]\n"
    "ds_read_b128 v[24:27], v8 offset:17216\n"
    "v_max3_u32 v6, v31, v35, v6\n"
    "v_mfma_f32_16x16x32_bf16 v[76:79], v[32:35], a[12:15], v[76:79]\n"
    "v_mfma_f32_16x16x32_bf16 v[72:75], v[28:31], a[12:15], v[72:75]\n"
    "s_waitcnt lgkmcnt(7)\n"
    "v_mfma_f32_16x16x32_bf16 v[64:67], v[36:39], a[16:19], v[64:67]\n"
    "ds_read_b128 v[28:31], v8 offset:384\n"
    "s_waitcnt lgkmcnt(7)\n"
    "v_mfma_f32_16x16x32_bf16 v[68:71], v[40:43], a[16:19], v[68:71]\n"
    "ds_read_b128 v[32:35], v8 offset:17280\n"
    "v_max3_u32 v6, v39, v43, v6\n"
    "v_mfma_f32_16x16x32_bf16 v[76:79], v[40:43], a[20:23], v[76:79]\n"
    "v_mfma_f32_16x16x32_bf16 v[72:75], v[36:39], a[20:23], v[72:75]\n"
    "s_waitcnt lgkmcnt(7)\n"
    "v_mfma_f32_16x16x32_bf16 v[64:67], v[44:47], a[24:27], v[64:67]\n"
    "ds_read_b128 v[36:39], v8 offset:448\n"
    "s_waitcnt lgkmcnt(7)\n"
    "v_mfma_f32_16x16x32_bf16 v[68:71], v[48:51], a[24:27], v[68:71]\n"
    "ds_read_b128 v[40:43], v8 offset:17344\n"
    "v_max3_u32 v6, v47, v51, v6\n"
    "v_mfma_f32_16x16x32_bf16 v[76:79], v[48:51], a[28:31], v[76:79]\n"
    "v_mfma_f32_16x16x32_bf16 v[72:75], v[44:47], a[28:31], v[72:75]\n"
    "s_waitcnt lgkmcnt(7)\n"
    "v_mfma_f32_16x16x32_bf16 v[64:67], v[52:55], a[32:35], v[64:67]\n"
    "ds_read_b128 v[44:47], v8 offset:512\n"
    "s_waitcnt lgkmcnt(7)\n"
    "v_mfma_f32_16x16x32_bf16 v[68:71], v[56:59], a[32:35], v[68:71]\n"
    "ds_read_b128 v[48:51], v8 offset:17408\n"
    "v_max3_u32 v6, v55, v59, v6\n"
    "v_mfma_f32_16x16x32_bf16 v[76:79], v[56:59], a[36:39], v[76:79]\n"
    "v_mfma_f32_16x16x32_bf16 v[72:75], v[52:55], a[36:39], v[72:75]\n"
    "s_waitcnt lgkmcnt(7)\n"
    "v_mfma_f32_16x16x32_bf16 v[64:67], v[20:23], a[40:43], v[64:67]\n"
    "ds_read_b128 v[52:55], v8 offset:576\n"
    "s_waitcnt lgkmcnt(7)\n"
    "v_mfma_f32_16x16x32_bf16 v[68:71], v[24:27], a[40:43], v[68:71]\n"
    "ds_read_b128 v[56:59], v8 offset:17472\n"
    "v_max3_u32 v6, v23, v27, v6\n"
    "v_mfma_f32_16x16x32_bf16 v[76:79], v[24:27], a[44:47], v[76:79]\n"
    "v_mfma_f32_16x16x32_bf16 v[72:75], v[20:23], a[44:47], v[72:75]\n"
    "s_waitcnt lgkmcnt(7)\n"
    "v_mfma_f32_16x16x32_bf16 v[64:67], v[28:31], a[48:51], v[64:67]\n"
    "ds_read_b128 v[20:23], v8 offset:640\n"
    "s_waitcnt lgkmcnt(7)\n"
    "v_mfma_f32_16x16x32_bf16 v[68:71], v[32:35], a[48:51], v[68:71]\n"
    "ds_read_b128 v[24:27], v8 offset:17536\n"
    "v_max3_u32 v6, v31, v35, v6\n"
    "v_mfma_f32_16x16x32_bf16 v[76:79], v[32:35], a[52:55], v[76:79]\n"
    "v_mfma_f32_16x16x32_bf16 v[72:75], v[28:31], a[52:55], v[72:75]\n"
    "s_waitcnt lgkmcnt(7)\n"
    "v_mfma_f32_16x16x32_bf16 v[64:67], v[36:39], a[56:59], v[64:67]\n"
    "ds_read_b128 v[28:31], v8 offset:704\n"
    "s_waitcnt lgkmcnt(7)\n"
    "v_mfma_f32_16x16x32_bf16 v[68:71], v[40:43], a[56:59], v[68:71]\n"
    "ds_read_b128 v[32:35], v8 offset:17600\n"
    "v_max3_u32 v6, v39, v43, v6\n"
    "v_mfma_f32_16x16x32_bf16 v[76:79], v[40:43], a[60:63], v[76:79]\n"
    "v_mfma_f32_16x16x32_bf16 v[72:75], v[36:39], a[60:63], v[72:75]\n"
    "s_waitcnt lgkmcnt(7)\n"
    "v_mfma_f32_16x16x32_bf16 v[64:67], v[44:47], a[64:67], v[64:67]\n"
    "ds_read_b128 v[36:39], v8 offset:768\n"
    "s_waitcnt lgkmcnt(7)\n"
    "v_mfma_f32_16x16x32_bf16 v[68:71], v[48:51], a[64:67], v[68:71]\n"
    "ds_read_b128 v[40:43], v8 offset:17664\n"
    "v_max3_u32 v6, v47, v51, v6\n"
    "v_mfma_f32_16x16x32_bf16 v[76:79], v[48:51], a[68:71], v[76:79]\n"
    "v_mfma_f32_16x16x32_bf16 v[72:75], v[44:47], a[68:71], v[72:75]\n"
    "s_waitcnt lgkmcnt(7)\n"
    "v_mfma_f32_16x16x32_bf16 v[64:67], v[52:55], a[72:75], v[64:67]\n"
    "ds_read_b128 v[44:47], v8 offset:832\n"
    "s_waitcnt lgkmcnt(7)\n"
    "v_mfma_f32_16x16x32_bf16 v[68:71], v[56:59], a[72:75], v[68:71]\n"
    "ds_read_b128 v[48:51], v8 offset:17728\n"
    "v_max3_u32 v6, v55, v59, v6\n"
    "v_mfma_f32_16x16x32_bf16 v[76:79], v[56:59], a[76:79], v[76:79]\n"
    "v_mfma_f32_16x16x32_bf16 v[72:75], v[52:55], a[76:79], v[72:75]\n"
    "s_waitcnt lgkmcnt(7)\n"
    "v_mfma_f32_16x16x32_bf16 v[64:67], v[20:23], a[80:83], v[64:67]\n"
    "ds_read_b128 v[52:55], v8 offset:896\n"
    "s_waitcnt lgkmcnt(7)\n"
    "v_mfma_f32_16x16x32_bf16 v[68:71], v[24:27], a[80:83], v[68:71]\n"
    "ds_read_b128 v[56:59], v8 offset:17792\n"
    "v_max3_u32 v6, v23, v27, v6\n"
    "v_mfma_f32_16x16x32_bf16 v[76:79], v[24:27], a[84:87], v[76:79]\n"
    "v_mfma_f32_16x16x32_bf16 v[72:75], v[20:23], a[84:87], v[72:75]\n"
    "s_waitcnt lgkmcnt(7)\n"
    "v_mfma_f32_16x16x32_bf16 v[64:67], v[28:31], a[88:91], v[64:67]\n"
    "ds_read_b128 v[20:23], v8 offset:960\n"
    "s_waitcnt lgkmcnt(7)\n"
    "v_mfma_f32_16x16x32_bf16 v[68:71], v[32:35], a[88:91], v[68:71]\n"
    "ds_read_b128 v[24:27], v8 offset:17856\n"
    "v_max3_u32 v6, v31, v35, v6\n"
    "v_mfma_f32_16x16x32_bf16 v[76:79], v[32:35], a[92:95], v[76:79]\n"
    "v_mfma_f32_16x16x32_bf16 v[72:75], v[28:31], a[92:95], v[72:75]\n"
    "s_waitcnt lgkmcnt(7)\n"
    "v_mfma_f32_16x16x32_bf16 v[64:67], v[36:39], a[96:99], v[64:67]\n"
    "ds_read_b128 v[28:31], v8 offset:0\n"
    "s_waitcnt lgkmcnt(7)\n"
    "v_mfma_f32_16x16x32_bf16 v[68:71], v[40:43], a[96:99], v[68:71]\n"
    "ds_read_b128 v[32:35], v8 offset:16896\n"
    "s_waitcnt vmcnt(0)\n"
    "s_barrier\n"
    "v_readfirstlane_b32 s38, v5\n"
    "s_or_b32 s52, s52, s68\n"
    "s_or_b32 s38, s38, s68\n"
    "s_cbranch_scc0 LK2_%=\n"
    "LJ2_%=:\n"
    "buffer_store_dword v5, v9, s[16:19], s52 offen sc1\n"
    "s_mov_b32 s52, 0x80000000\n"
    "v_mfma_f32_16x16x32_bf16 v[76:79], v[40:43], a[100:103], v[76:79]\n"
    "v_max3_u32 v6, v39, v43, v6\n"
    "v_mfma_f32_16x16x32_bf16 v[72:75], v[36:39], a[100:103], v[72:75]\n"
    "s_waitcnt lgkmcnt(7)\n"
    "v_mfma_f32_16x16x32_bf16 v[64:67], v[44:47], a[104:107], v[64:67]\n"
    "ds_read_b128 v[36:39], v8 offset:64\n"
    "s_waitcnt lgkmcnt(7)\n"
    "v_mfma_f32_16x16x32_bf16 v[68:71], v[48:51], a[104:107], v[68:71]\n"
    "ds_read_b128 v[40:43], v8 offset:16960\n"
    "v_max3_u32 v6, v47, v51, v6\n"
    "v_mfma_f32_16x16x32_bf16 v[76:79], v[48:51], a[108:111], v[76:79]\n"
    "v_mfma_f32_16x16x32_bf16 v[72:75], v[44:47], a[108:111], v[72:75]\n"
    "s_waitcnt lgkmcnt(7)\n"
    "v_mfma_f32_16x16x32_bf16 v[64:67], v[52:55], a[112:115], v[64:67]\n"
    "ds_read_b128 v[44:47], v8 offset:128\n"
    "s_waitcnt lgkmcnt(7)\n"
    "v_mfma_f32_16x16x32_bf16 v[68:71], v[56:59], a[112:115], v[68:71]\n"
    "ds_read_b128 v[48:51], v8 offset:17024\n"
    "v_max3_u32 v6, v55, v59, v6\n"
    "v_mfma_f32_16x16x32_bf16 v[76:79], v[56:59], a[116:119], v[76:79]\n"
    "v_mfma_f32_16x16x32_bf16 v[72:75], v[52:55], a[116:119], v[72:75]\n"
    "s_waitcnt lgkmcnt(7)\n"
    "v_mfma_f32_16x16x32_bf16 v[64:67], v[20:23], a[120:123], v[64:67]\n"
    "ds_read_b128 v[52:55], v8 offset:192\n"
    "s_waitcnt lgkmcnt(7)\n"
    "v_mfma_f32_16x16x32_bf16 v[68:71], v[24:27], a[120:123], v[68:71]\n"
    "ds_read_b128 v[56:59], v8 offset:17088\n"
    "v_max3_u32 v6, v23, v27, v6\n"
    "v_mfma_f32_16x16x32_bf16 v[76:79], v[24:27], a[124:127], v[76:79]\n"
    "v_mfma_f32_16x16x32_bf16 v[72:75], v[20:23], a[124:127], v[72:75]\n"
    "s_waitcnt lgkmcnt(7)\n"
    "v_mfma_f32_16x16x32_bf16 v[80:83], v[28:31], a[128:131], 0\n"
    "ds_read_b128 v[20:23], v8 offset:256\n"
    "s_waitcnt lgkmcnt(7)\n"
    "v_mfma_f32_16x16x32_bf16 v[84:87], v[32:35], a[128:131], 0\n"
    "ds_read_b128 v[24:27], v8 offset:17152\n"
    "v_cmp_eq_u32 vcc, -1, v6\n"
    "v_mfma_f32_16x16x32_bf16 v[92:95], v[32:35], a[132:135], 0\n"
    "s_cbranch_vccnz LRD%=\n"
    "v_mfma_f32_16x16x32_bf16 v[88:91], v[28:31], a[132:135], 0\n"
    "s_waitcnt lgkmcnt(7)\n"
    "v_mfma_f32_16x16x32_bf16 v[80:83], v[36:39], a[136:139], v[80:83]\n"
    "ds_read_b128 v[28:31], v8 offset:320\n"
    "s_waitcnt lgkmcnt(7)\n"
    "v_mfma_f32_16x16x32_bf16 v[84:87], v[40:43], a[136:139], v[84:87]\n"
    "ds_read_b128 v[32:35], v8 offset:17216\n"
    "v_cvt_pk_bf16_f32 v12, v64, v65\n"
    "v_mfma_f32_16x16x32_bf16 v[92:95], v[40:43], a[140:143], v[92:95]\n"
    "v_cvt_pk_bf16_f32 v13, v66, v67\n"
    "v_mfma_f32_16x16x32_bf16 v[88:91], v[36:39], a[140:143], v[88:91]\n"
    "s_waitcnt lgkmcnt(7)\n"
    "v_cvt_pk_bf16_f32 v14, v68, v69\n"
    "v_mfma_f32_16x16x32_bf16 v[80:83], v[44:47], a[144:147], v[80:83]\n"
    "ds_read_b128 v[36:39], v8 offset:384\n"
    "s_waitcnt lgkmcnt(7)\n"
    "v_mfma_f32_16x16x32_bf16 v[84:87], v[48:51], a[144:147], v[84:87]\n"
    "ds_read_b128 v[40:43], v8 offset:17280\n"
    "v_cvt_pk_bf16_f32 v15, v70, v71\n"
    "v_mfma_f32_16x16x32_bf16 v[92:95], v[48:51], a[148:151], v[92:95]\n"
    "buffer_store_dwordx4 v[12:15], v0, s[16:19], s59 offen sc1\n"
    "v_mfma_f32_16x16x32_bf16 v[88:91], v[44:47], a[148:151], v[88:91]\n"
    "s_waitcnt lgkmcnt(7)\n"
    "v_cvt_pk_bf16_f32 v16, v72, v73\n"
    "v_mfma_f32_16x16x32_bf16 v[80:83], v[52:55], a[152:155], v[80:83]\n"
    "ds_read_b128 v[44:47], v8 offset:448\n"
    "s_waitcnt lgkmcnt(7)\n"
    "v_mfma_f32_16x16x32_bf16 v[84:87], v[56:59], a[152:155], v[84:87]\n"
    "ds_read_b128 v[48:51], v8 offset:17344\n"
    "v_cvt_pk_bf16_f32 v17, v74, v75\n"
    "v_mfma_f32_16x16x32_bf16 v[92:95], v[56:59], a[156:159], v[92:95]\n"
    "v_cvt_pk_bf16_f32 v18, v76, v77\n"
    "v_mfma_f32_16x16x32_bf16 v[88:91], v[52:55], a[156:159], v[88:91]\n"
    "s_waitcnt lgkmcnt(7)\n"
    "v_cvt_pk_bf16_f32 v19, v78, v79\n"
    "v_mfma_f32_16x16x32_bf16 v[80:83], v[20:23], a[160:163], v[80:83]\n"
    "ds_read_b128 v[52:55], v8 offset:512\n"
    "s_waitcnt lgkmcnt(7)\n"
    "v_mfma_f32_16x16x32_bf16 v[84:87], v[24:27], a[160:163], v[84:87]\n"
    "ds_read_b128 v[56:59], v8 offset:17408\n"
    "buffer_store_dwordx4 v[16:19], v0, s[16:19], s62 offen sc1\n"
    "v_mfma_f32_16x16x32_bf16 v[92:95], v[24:27], a[164:167], v[92:95]\n"
    "s_add_u32 s59, s59, 16384\n"
    "s_add_u32 s62, s62, 16384\n"
    "v_mfma_f32_16x16x32_bf16 v[88:91], v[20:23], a[164:167], v[88:91]\n"
    "s_waitcnt lgkmcnt(7)\n"
    "v_mfma_f32_16x16x32_bf16 v[80:83], v[28:31], a[168:171], v[80:83]\n"
    "ds_read_b128 v[20:23], v8 offset:576\n"
    "s_waitcnt lgkmcnt(7)\n"
    "v_mfma_f32_16x16x32_bf16 v[84:87], v[32:35], a[168:171], v[84:87]\n"
    "ds_read_b128 v[24:27], v8 offset:17472\n"
    "v_mfma_f32_16x16x32_bf16 v[92:95], v[32:35], a[172:175], v[92:95]\n"
    "v_mfma_f32_16x16x32_bf16 v[88:91], v[28:31], a[172:175], v[88:91]\n"
    "s_waitcnt lgkmcnt(7)\n"
    "v_mfma_f32_16x16x32_bf16 v[80:83], v[36:39], a[176:179], v[80:83]\n"
    "ds_read_b128 v[28:31], v8 offset:640\n"
    "s_waitcnt lgkmcnt(7)\n"
    "v_mfma_f32_16x16x32_bf16 v[84:87], v[40:43], a[176:179], v[84:87]\n"
    "ds_read_b128 v[32:35], v8 offset:17536\n"
    "v_mfma_f32_16x16x32_bf16 v[92:95], v[40:43], a[180:183], v[92:95]\n"
    "v_mfma_f32_16x16x32_bf16 v[88:91], v[36:39], a[180:183], v[88:91]\n"
    "s_waitcnt lgkmcnt(7)\n"
    "v_mfma_f32_16x16x32_bf16 v[80:83], v[44:47], a[184:187], v[80:83]\n"
    "ds_read_b128 v[36:39], v8 offset:704\n"
    "s_waitcnt lgkmcnt(7)\n"
    "v_mfma_f32_16x16x32_bf16 v[84:87], v[48:51], a[184:187], v[84:87]\n"
    "ds_read_b128 v[40:43], v8 offset:17600\n"
    "v_mfma_f32_16x16x32_bf16 v[92:95], v[48:51], a[188:191], v[92:95]\n"
    "v_mfma_f32_16x16x32_bf16 v[88:91], v[44:47], a[188:191], v[88:91]\n"
    "s_waitcnt lgkmcnt(7)\n"
    "v_mfma_f32_16x16x32_bf16 v[80:83], v[52:55], a[192:195], v[80:83]\n"
    "ds_read_b128 v[44:47], v8 offset:768\n"
    "s_waitcnt lgkmcnt(7)\n"
    "v_mfma_f32_16x16x32_bf16 v[84:87], v[56:59], a[192:195], v[84:87]\n"
    "ds_read_b128 v[48:51], v8 offset:17664\n"
    "buffer_load_dword v5, off, s[16:19], s67 sc1\n"
    "v_mfma_f32_16x16x32_bf16 v[92:95], v[56:59], a[196:199], v[92:95]\n"
    "v_mfma_f32_16x16x32_bf16 v[88:91], v[52:55], a[196:199], v[88:91]\n"
    "s_waitcnt lgkmcnt(7)\n"
    "v_mfma_f32_16x16x32_bf16 v[80:83], v[20:23], a[200:203], v[80:83]\n"
    "ds_read_b128 v[52:55], v8 offset:832\n"
    "s_waitcnt lgkmcnt(7)\n"
    "v_mfma_f32_16x16x32_bf16 v[84:87], v[24:27], a[200:203], v[84:87]\n"
    "ds_read_b128 v[56:59], v8 offset:17728\n"
    "v_mfma_f32_16x16x32_bf16 v[92:95], v[24:27], a[204:207], v[92:95]\n"
    "v_mfma_f32_16x16x32_bf16 v[88:91], v[20:23], a[204:207], v[88:91]\n"
    "s_waitcnt lgkmcnt(7)\n"
    "v_mfma_f32_16x16x32_bf16 v[80:83], v[28:31], a[208:211], v[80:83]\n"
    "ds_read_b128 v[20:23], v8 offset:896\n"
    "s_waitcnt lgkmcnt(7)\n"
    "v_mfma_f32_16x16x32_bf16 v[84:87], v[32:35], a[208:211], v[84:87]\n"
    "ds_read_b128 v[24:27], v8 offset:17792\n"
    "v_mfma_f32_16x16x32_bf16 v[92:95], v[32:35], a[212:215], v[92:95]\n"
    "v_mfma_f32_16x16x32_bf16 v[88:91], v[28:31], a[212:215], v[88:91]\n"
    "s_waitcnt lgkmcnt(7)\n"
    "v_mfma_f32_16x16x32_bf16 v[80:83], v[36:39], a[216:219], v[80:83]\n"
    "ds_read_b128 v[28:31], v8 offset:960\n"
    "s_waitcnt lgkmcnt(7)\n"
    "v_mfma_f32_16x16x32_bf16 v[84:87], v[40:43], a[216:219], v[84:87]\n"
    "ds_read_b128 v[32:35], v8 offset:17856\n"
    "v_mfma_f32_16x16x32_bf16 v[92:95], v[40:43], a[220:223], v[92:95]\n"
    "v_mfma_f32_16x16x32_bf16 v[88:91], v[36:39], a[220:223], v[88:91]\n"
    "s_waitcnt lgkmcnt(7)\n"
    "v_mfma_f32_16x16x32_bf16 v[80:83], v[44:47], a[224:227], v[80:83]\n"
    "s_waitcnt lgkmcnt(6)\n"
    "v_mfma_f32_16x16x32_bf16 v[84:87], v[48:51], a[224:227], v[84:87]\n"
    "v_mfma_f32_16x16x32_bf16 v[92:95], v[48:51], a[228:231], v[92:95]\n"
    "v_mfma_f32_16x16x32_bf16 v[88:91], v[44:47], a[228:231], v[88:91]\n"
    "s_waitcnt lgkmcnt(5)\n"
    "v_mfma_f32_16x16x32_bf16 v[80:83], v[52:55], a[232:235], v[80:83]\n"
    "s_waitcnt lgkmcnt(4)\n"
    "v_mfma_f32_16x16x32_bf16 v[84:87], v[56:59], a[232:235], v[84:87]\n"
    "v_mfma_f32_16x16x32_bf16 v[92:95], v[56:59], a[236:239], v[92:95]\n"
    "v_mfma_f32_16x16x32_bf16 v[88:91], v[52:55], a[236:239], v[88:91]\n"
    "s_waitcnt lgkmcnt(3)\n"
    "v_mfma_f32_16x16x32_bf16 v[80:83], v[20:23], a[240:243], v[80:83]\n"
    "s_waitcnt lgkmcnt(2)\n"
    "v_mfma_f32_16x16x32_bf16 v[84:87], v[24:27], a[240:243], v[84:87]\n"
    "v_mfma_f32_16x16x32_bf16 v[92:95], v[24:27], a[244:247], v[92:95]\n"
    "v_mfma_f32_16x16x32_bf16 v[88:91], v[20:23], a[244:247], v[88:91]\n"
    "s_waitcnt lgkmcnt(1)\n"
    "v_mfma_f32_16x16x32_bf16 v[80:83], v[28:31], a[248:251], v[80:83]\n"
    "s_waitcnt lgkmcnt(0)\n"
    "v_mfma_f32_16x16x32_bf16 v[84:87], v[32:35], a[248:251], v[84:87]\n"
    "v_mfma_f32_16x16x32_bf16 v[92:95], v[32:35], a[252:255], v[92:95]\n"
    "v_mfma_f32_16x16x32_bf16 v[88:91], v[28:31], a[252:255], v[88:91]\n"
    "v_cvt_pk_bf16_f32 v12, v80, v81\n"
    "v_cvt_pk_bf16_f32 v13, v82, v83\n"
    "v_cvt_pk_bf16_f32 v14, v84, v85\n"
    "v_cvt_pk_bf16_f32 v15, v86, v87\n"
    "buffer_store_dwordx4 v[12:15], v0, s[16:19], s59 offen sc1\n"
    "s_nop 2\n"
    "v_cvt_pk_bf16_f32 v16, v88, v89\n"
    "v_cvt_pk_bf16_f32 v17, v90, v91\n"
    "v_cvt_pk_bf16_f32 v18, v92, v93\n"
    "v_cvt_pk_bf16_f32 v19, v94, v95\n"
    "buffer_store_dwordx4 v[16:19], v0, s[16:19], s62 offen sc1\n"
    "s_mov_b32 s52, s64\n"
    "LHE%=:\n"
    "s_cmp_ge_u32 s40, 64\n"
    "s_cbranch_scc1 LT%=\n"
    "L1%=:\n"
    "v_add_u32 v8, s43, v1\n"
    "s_xor_b32 s43, s43, 33792\n"
    "s_mov_b32 s63, s45\n"
    "s_lshr_b32 s38, s63, 8\n"
    "s_xor_b32 s38, s38, s48\n"
    "s_and_b32 s38, s38, 0x2000\n"
    "s_or_b32 s63, s63, s38\n"
    "s_add_u32 s39, s44, s43\n"
    "s_add_u32 s69, s63, 4096\n"
    "s_add_u32 m0, s39, 0\n"
    "s_nop 0\n"
    "buffer_load_dwordx4 v0, s[16:19], s63 offen offset:0 sc1 lds\n"
    "s_add_u32 m0, s39, 32\n"
    "s_nop 0\n"
    "buffer_load_dwordx4 v0, s[16:19], s63 offen offset:1024 sc1 lds\n"
    "s_add_u32 m0, s39, 64\n"
    "s_nop 0\n"
    "buffer_load_dwordx4 v0, s[16:19], s63 offen offset:2048 sc1 lds\n"
    "s_add_u32 m0, s39, 96\n"
    "s_nop 0\n"
    "buffer_load_dwordx4 v0, s[16:19], s63 offen offset:3072 sc1 lds\n"
    "s_add_u32 m0, s39, 4224\n"
    "s_nop 0\n"
    "buffer_load_dwordx4 v0, s[16:19], s69 offen offset:0 sc1 lds\n"
    "s_add_u32 m0, s39, 4256\n"
    "s_nop 0\n"
    "buffer_load_dwordx4 v0, s[16:19], s69 offen offset:1024 sc1 lds\n"
    "s_add_u32 m0, s39, 4288\n"
    "s_nop 0\n"
    "buffer_load_dwordx4 v0, s[16:19], s69 offen offset:2048 sc1 lds\n"
    "s_add_u32 m0, s39, 4320\n"
    "s_nop 0\n"
    "buffer_load_dwordx4 v0, s[16:19], s69 offen offset:3072 sc1 lds\n"
    "s_add_u32 s45, s45, 131072\n"
    "s_mul_i32 s59, s40, 2621440\n"
    "s_add_u32 s59, s59, s47\n"
    "s_lshr_b32 s38, s59, 8\n"
    "s_xor_b32 s38, s38, s48\n"
    "s_and_b32 s38, s38, 0x2000\n"
    "s_or_b32 s59, s59, s38\n"
    "s_lshl_b32 s64, s40, 7\n"
    "s_add_u32 s64, s64, s50\n"
    "s_add_u32 s40, s40, 2\n"
    "s_cmp_eq_u32 s61, 1\n"
    "s_cbranch_scc1 LIV%=\n"
    "s_add_u32 s62, s59, 4096\n"
    "ds_read_b128 v[20:23], v8 offset:0\n"
    "ds_read_b128 v[24:27], v8 offset:16896\n"
    "ds_read_b128 v[28:31], v8 offset:64\n"
    "ds_read_b128 v[32:35], v8 offset:16960\n"
    "ds_read_b128 v[36:39], v8 offset:128\n"
    "ds_read_b128 v[40:43], v8 offset:17024\n"
    "ds_read_b128 v[44:47], v8 offset:192\n"
    "ds_read_b128 v[48:51], v8 offset:17088\n"
    "s_waitcnt lgkmcnt(7)\n"
    "v_mfma_f32_16x16x32_bf16 v[64:67], a[0:3], v[20:23], 0\n"
    "ds_read_b128 v[52:55], v8 offset:256\n"
    "s_waitcnt lgkmcnt(7)\n"
    "v_mfma_f32_16x16x32_bf16 v[68:71], a[0:3], v[24:27], 0\n"
    "ds_read_b128 v[56:59], v8 offset:17152\n"
    "v_mfma_f32_16x16x32_bf16 v[76:79], a[4:7], v[24:27], 0\n"
    "v_mfma_f32_16x16x32_bf16 v[72:75], a[4:7], v[20:23], 0\n"
    "s_waitcnt lgkmcnt(7)\n"
    "v_mfma_f32_16x16x32_bf16 v[64:67], a[8:11], v[28:31], v[64:67]\n"
    "ds_read_b128 v[20:23], v8 offset:320\n"
    "s_waitcnt lgkmcnt(7)\n"
    "v_mfma_f32_16x16x32_bf16 v[68:71], a[8:11], v[32:35], v[68:71]\n"
    "ds_read_b128 v[24:27], v8 offset:17216\n"
    "v_mfma_f32_16x16x32_bf16 v[76:79], a[12:15], v[32:35], v[76:79]\n"
    "v_mfma_f32_16x16x32_bf16 v[72:75], a[12:15], v[28:31], v[72:75]\n"
    "s_waitcnt lgkmcnt(7)\n"
    "v_mfma_f32_16x16x32_bf16 v[64:67], a[16:19], v[36:39], v[64:67]\n"
    "ds_read_b128 v[28:31], v8 offset:384\n"
    "s_waitcnt lgkmcnt(7)\n"
    "v_mfma_f32_16x16x32_bf16 v[68:71], a[16:19], v[40:43], v[68:71]\n"
    "ds_read_b128 v[32:35], v8 offset:17280\n"
    "v_mfma_f32_16x16x32_bf16 v[76:79], a[20:23], v[40:43], v[76:79]\n"
    "v_mfma_f32_16x16x32_bf16 v[72:75], a[20:23], v[36:39], v[72:75]\n"
    "s_waitcnt lgkmcnt(7)\n"
    "v_mfma_f32_16x16x32_bf16 v[64:67], a[24:27], v[44:47], v[64:67]\n"
    "ds_read_b128 v[36:39], v8 offset:448\n"
    "s_waitcnt lgkmcnt(7)\n"
    "v_mfma_f32_16x16x32_bf16 v[68:71], a[24:27], v[48:51], v[68:71]\n"
    "ds_read_b128 v[40:43], v8 offset:17344\n"
    "v_mfma_f32_16x16x32_bf16 v[76:79], a[28:31], v[48:51], v[76:79]\n"
    "v_mfma_f32_16x16x32_bf16 v[72:75], a[28:31], v[44:47], v[72:75]\n"
    "s_waitcnt lgkmcnt(7)\n"
    "v_mfma_f32_16x16x32_bf16 v[64:67], a[32:35], v[52:55], v[64:67]\n"
    "ds_read_b128 v[44:47], v8 offset:512\n"
    "s_waitcnt lgkmcnt(7)\n"
    "v_mfma_f32_16x16x32_bf16 v[68:71], a[32:35], v[56:59], v[68:71]\n"
    "ds_read_b128 v[48:51], v8 offset:17408\n"
    "v_mfma_f32_16x16x32_bf16 v[76:79], a[36:39], v[56:59], v[76:79]\n"
    "v_mfma_f32_16x16x32_bf16 v[72:75], a[36:39], v[52:55], v[72:75]\n"
    "s_waitcnt lgkmcnt(7)\n"
    "v_mfma_f32_16x16x32_bf16 v[64:67], a[40:43], v[20:23], v[64:67]\n"
    "ds_read_b128 v[52:55], v8 offset:576\n"
    "s_waitcnt lgkmcnt(7)\n"
    "v_mfma_f32_16x16x32_bf16 v[68:71], a[40:43], v[24:27], v[68:71]\n"
    "ds_read_b128 v[56:59], v8 offset:17472\n"
    "v_mfma_f32_16x16x32_bf16 v[76:79], a[44:47], v[24:27], v[76:79]\n"
    "v_mfma_f32_16x16x32_bf16 v[72:75], a[44:47], v[20:23], v[72:75]\n"
    "s_waitcnt lgkmcnt(7)\n"
    "v_mfma_f32_16x16x32_bf16 v[64:67], a[48:51], v[28:31], v[64:67]\n"
    "ds_read_b128 v[20:23], v8 offset:640\n"
    "s_waitcnt lgkmcnt(7)\n"
    "v_mfma_f32_16x16x32_bf16 v[68:71], a[48:51], v[32:35], v[68:71]\n"
    "ds_read_b128 v[24:27], v8 offset:17536\n"
    "v_mfma_f32_16x16x32_bf16 v[76:79], a[52:55], v[32:35], v[76:79]\n"
    "v_mfma_f32_16x16x32_bf16 v[72:75], a[52:55], v[28:31], v[72:75]\n"
    "s_waitcnt lgkmcnt(7)\n"
    "v_mfma_f32_16x16x32_bf16 v[64:67], a[56:59], v[36:39], v[64:67]\n"
    "ds_read_b128 v[28:31], v8 offset:704\n"
    "s_waitcnt lgkmcnt(7)\n"
    "v_mfma_f32_16x16x32_bf16 v[68:71], a[56:59], v[40:43], v[68:71]\n"
    "ds_read_b128 v[32:35], v8 offset:17600\n"
    "v_mfma_f32_16x16x32_bf16 v[76:79], a[60:63], v[40:43], v[76:79]\n"
    "v_mfma_f32_16x16x32_bf16 v[72:75], a[60:63], v[36:39], v[72:75]\n"
    "s_waitcnt lgkmcnt(7)\n"
    "v_mfma_f32_16x16x32_bf16 v[64:67], a[64:67], v[44:47], v[64:67]\n"
    "ds_read_b128 v[36:39], v8 offset:768\n"
    "s_waitcnt lgkmcnt(7)\n"
    "v_mfma_f32_16x16x32_bf16 v[68:71], a[64:67], v[48:51], v[68:71]\n"
    "ds_read_b128 v[40:43], v8 offset:17664\n"
    "v_mfma_f32_16x16x32_bf16 v[76:79], a[68:71], v[48:51], v[76:79]\n"
    "v_mfma_f32_16x16x32_bf16 v[72:75], a[68:71], v[44:47], v[72:75]\n"
    "s_waitcnt lgkmcnt(7)\n"
    "v_mfma_f32_16x16x32_bf16 v[64:67], a[72:75], v[52:55], v[64:67]\n"
    "ds_read_b128 v[44:47], v8 offset:832\n"
    "s_waitcnt lgkmcnt(7)\n"
    "v_mfma_f32_16x16x32_bf16 v[68:71], a[72:75], v[56:59], v[68:71]\n"
    "ds_read_b128 v[48:51], v8 offset:17728\n"
    "v_mfma_f32_16x16x32_bf16 v[76:79], a[76:79], v[56:59], v[76:79]\n"
    "v_mfma_f32_16x16x32_bf16 v[72:75], a[76:79], v[52:55], v[72:75]\n"
    "s_waitcnt lgkmcnt(7)\n"
    "v_mfma_f32_16x16x32_bf16 v[64:67], a[80:83], v[20:23], v[64:67]\n"
    "ds_read_b128 v[52:55], v8 offset:896\n"
    "s_waitcnt lgkmcnt(7)\n"
    "v_mfma_f32_16x16x32_bf16 v[68:71], a[80:83], v[24:27], v[68:71]\n"
    "ds_read_b128 v[56:59], v8 offset:17792\n"
    "v_mfma_f32_16x16x32_bf16 v[76:79], a[84:87], v[24:27], v[76:79]\n"
    "v_mfma_f32_16x16x32_bf16 v[72:75], a[84:87], v[20:23], v[72:75]\n"
    "s_waitcnt lgkmcnt(7)\n"
    "v_mfma_f32_16x16x32_bf16 v[64:67], a[88:91], v[28:31], v[64:67]\n"
    "ds_read_b128 v[20:23], v8 offset:960\n"
    "s_waitcnt lgkmcnt(7)\n"
    "v_mfma_f32_16x16x32_bf16 v[68:71], a[88:91], v[32:35], v[68:71]\n"
    "ds_read_b128 v[24:27], v8 offset:17856\n"
    "v_mfma_f32_16x16x32_bf16 v[76:79], a[92:95], v[32:35], v[76:79]\n"
    "v_mfma_f32_16x16x32_bf16 v[72:75], a[92:95], v[28:31], v[72:75]\n"
    "s_waitcnt lgkmcnt(7)\n"
    "v_mfma_f32_16x16x32_bf16 v[64:67], a[96:99], v[36:39], v[64:67]\n"
    "ds_read_b128 v[28:31], v8 offset:0\n"
    "s_waitcnt lgkmcnt(7)\n"
    "v_mfma_f32_16x16x32_bf16 v[68:71], a[96:99], v[40:43], v[68:71]\n"
    "ds_read_b128 v[32:35], v8 offset:16896\n"
    "s_waitcnt vmcnt(8)\n"
    "s_barrier\n"
    "v_readfirstlane_b32 s38, v5\n"
    "s_or_b32 s52, s52, s68\n"
    "s_or_b32 s38, s38, s68\n"
    "s_cbranch_scc0 LK3_%=\n"
    "LJ3_%=:\n"
    "buffer_store_dword v5, v9, s[16:19], s52 offen sc1\n"
    "s_mov_b32 s52, 0x80000000\n"
    "v_mfma_f32_16x16x32_bf16 v[76:79], a[100:103], v[40:43], v[76:79]\n"
    "v_mfma_f32_16x16x32_bf16 v[72:75], a[100:103], v[36:39], v[72:75]\n"
    "s_waitcnt lgkmcnt(7)\n"
    "v_mfma_f32_16x16x32_bf16 v[64:67], a[104:107], v[44:47], v[64:67]\n"
    "ds_read_b128 v[36:39], v8 offset:64\n"
    "s_waitcnt lgkmcnt(7)\n"
    "v_mfma_f32_16x16x32_bf16 v[68:71], a[104:107], v[48:51], v[68:71]\n"
    "ds_read_b128 v[40:43], v8 offset:16960\n"
    "v_mfma_f32_16x16x32_bf16 v[76:79], a[108:111], v[48:51], v[76:79]\n"
    "v_mfma_f32_16x16x32_bf16 v[72:75], a[108:111], v[44:47], v[72:75]\n"
    "s_waitcnt lgkmcnt(7)\n"
    "v_mfma_f32_16x16x32_bf16 v[64:67], a[112:115], v[52:55], v[64:67]\n"
    "ds_read_b128 v[44:47], v8 offset:128\n"
    "s_waitcnt lgkmcnt(7)\n"
    "v_mfma_f32_16x16x32_bf16 v[68:71], a[112:115], v[56:59], v[68:71]\n"
    "ds_read_b128 v[48:51], v8 offset:17024\n"
    "v_mfma_f32_16x16x32_bf16 v[76:79], a[116:119], v[56:59], v[76:79]\n"
    "v_mfma_f32_16x16x32_bf16 v[72:75], a[116:119], v[52:55], v[72:75]\n"
    "s_waitcnt lgkmcnt(7)\n"
    "v_mfma_f32_16x16x32_bf16 v[64:67], a[120:123], v[20:23], v[64:67]\n"
    "ds_read_b128 v[52:55], v8 offset:192\n"
    "s_waitcnt lgkmcnt(7)\n"
    "v_mfma_f32_16x16x32_bf16 v[68:71], a[120:123], v[24:27], v[68:71]\n"
    "ds_read_b128 v[56:59], v8 offset:17088\n"
    "v_mfma_f32_16x16x32_bf16 v[76:79], a[124:127], v[24:27], v[76:79]\n"
    "v_mfma_f32_16x16x32_bf16 v[72:75], a[124:127], v[20:23], v[72:75]\n"
    "s_waitcnt lgkmcnt(7)\n"
    "v_mfma_f32_16x16x32_bf16 v[80:83], a[128:131], v[28:31], 0\n"
    "ds_read_b128 v[20:23], v8 offset:256\n"
    "s_waitcnt lgkmcnt(7)\n"
    "v_mfma_f32_16x16x32_bf16 v[84:87], a[128:131], v[32:35], 0\n"
    "ds_read_b128 v[24:27], v8 offset:17152\n"
    "buffer_load_dword v6, off, s[16:19], s53 sc1\n"
    "v_mfma_f32_16x16x32_bf16 v[92:95], a[132:135], v[32:35], 0\n"
    "v_mfma_f32_16x16x32_bf16 v[88:91], a[132:135], v[28:31], 0\n"
    "s_waitcnt lgkmcnt(7)\n"
    "v_mfma_f32_16x16x32_bf16 v[80:83], a[136:139], v[36:39], v[80:83]\n"
    "ds_read_b128 v[28:31], v8 offset:320\n"
    "s_waitcnt lgkmcnt(7)\n"
    "v_mfma_f32_16x16x32_bf16 v[84:87], a[136:139], v[40:43], v[84:87]\n"
    "ds_read_b128 v[32:35], v8 offset:17216\n"
    "v_cvt_pk_bf16_f32 v12, v64, v72\n"
    "v_mfma_f32_16x16x32_bf16 v[92:95], a[140:143], v[40:43], v[92:95]\n"
    "v_cvt_pk_bf16_f32 v13, v65, v73\n"
    "v_mfma_f32_16x16x32_bf16 v[88:91], a[140:143], v[36:39], v[88:91]\n"
    "s_waitcnt lgkmcnt(7)\n"
    "v_cvt_pk_bf16_f32 v14, v66, v74\n"
    "v_mfma_f32_16x16x32_bf16 v[80:83], a[144:147], v[44:47], v[80:83]\n"
    "ds_read_b128 v[36:39], v8 offset:384\n"
    "s_waitcnt lgkmcnt(7)\n"
    "v_mfma_f32_16x16x32_bf16 v[84:87], a[144:147], v[48:51], v[84:87]\n"
    "ds_read_b128 v[40:43], v8 offset:17280\n"
    "v_cvt_pk_bf16_f32 v15, v67, v75\n"
    "v_mfma_f32_16x16x32_bf16 v[92:95], a[148:151], v[48:51], v[92:95]\n"
    "buffer_store_dwordx4 v[12:15], v0, s[16:19], s59 offen sc1\n"
    "v_mfma_f32_16x16x32_bf16 v[88:91], a[148:151], v[44:47], v[88:91]\n"
    "s_waitcnt lgkmcnt(7)\n"
    "v_cvt_pk_bf16_f32 v16, v68, v76\n"
    "v_mfma_f32_16x16x32_bf16 v[80:83], a[152:155], v[52:55], v[80:83]\n"
    "ds_read_b128 v[44:47], v8 offset:448\n"
    "s_waitcnt lgkmcnt(7)\n"
    "v_mfma_f32_16x16x32_bf16 v[84:87], a[152:155], v[56:59], v[84:87]\n"
    "ds_read_b128 v[48:51], v8 offset:17344\n"
    "v_cvt_pk_bf16_f32 v17, v69, v77\n"
    "v_mfma_f32_16x16x32_bf16 v[92:95], a[156:159], v[56:59], v[92:95]\n"
    "v_cvt_pk_bf16_f32 v18, v70, v78\n"
    "v_mfma_f32_16x16x32_bf16 v[88:91], a[156:159], v[52:55], v[88:91]\n"
    "s_waitcnt lgkmcnt(7)\n"
    "v_cvt_pk_bf16_f32 v19, v71, v79\n"
    "v_mfma_f32_16x16x32_bf16 v[80:83], a[160:163], v[20:23], v[80:83]\n"
    "ds_read_b128 v[52:55], v8 offset:512\n"
    "s_waitcnt lgkmcnt(7)\n"
    "v_mfma_f32_16x16x32_bf16 v[84:87], a[160:163], v[24:27], v[84:87]\n"
    "ds_read_b128 v[56:59], v8 offset:17408\n"
    "buffer_store_dwordx4 v[16:19], v0, s[16:19], s62 offen sc1\n"
    "v_mfma_f32_16x16x32_bf16 v[92:95], a[164:167], v[24:27], v[92:95]\n"
    "s_add_u32 s59, s59, 16384\n"
    "s_add_u32 s62, s62, 16384\n"
    "v_mfma_f32_16x16x32_bf16 v[88:91], a[164:167], v[20:23], v[88:91]\n"
    "s_waitcnt lgkmcnt(7)\n"
    "v_mfma_f32_16x16x32_bf16 v[80:83], a[168:171], v[28:31], v[80:83]\n"
    "ds_read_b128 v[20:23], v8 offset:576\n"
    "s_waitcnt lgkmcnt(7)\n"
    "v_mfma_f32_16x16x32_bf16 v[84:87], a[168:171], v[32:35], v[84:87]\n"
    "ds_read_b128 v[24:27], v8 offset:17472\n"
    "v_mfma_f32_16x16x32_bf16 v[92:95], a[172:175], v[32:35], v[92:95]\n"
    "v_mfma_f32_16x16x32_bf16 v[88:91], a[172:175], v[28:31], v[88:91]\n"
    "s_waitcnt lgkmcnt(7)\n"
    "v_mfma_f32_16x16x32_bf16 v[80:83], a[176:179], v[36:39], v[80:83]\n"
    "ds_read_b128 v[28:31], v8 offset:640\n"
    "s_waitcnt lgkmcnt(7)\n"
    "v_mfma_f32_16x16x32_bf16 v[84:87], a[176:179], v[40:43], v[84:87]\n"
    "ds_read_b128 v[32:35], v8 offset:17536\n"
    "v_mfma_f32_16x16x32_bf16 v[92:95], a[180:183], v[40:43], v[92:95]\n"
    "v_mfma_f32_16x16x32_bf16 v[88:91], a[180:183], v[36:39], v[88:91]\n"
    "s_waitcnt lgkmcnt(7)\n"
    "v_mfma_f32_16x16x32_bf16 v[80:83], a[184:187], v[44:47], v[80:83]\n"
    "ds_read_b128 v[36:39], v8 offset:704\n"
    "s_waitcnt lgkmcnt(7)\n"
    "v_mfma_f32_16x16x32_bf16 v[84:87], a[184:187], v[48:51], v[84:87]\n"
    "ds_read_b128 v[40:43], v8 offset:17600\n"
    "s_waitcnt vmcnt(2)\n"
    "ds_write_b32 v11, v6 offset:64\n"
    "v_mfma_f32_16x16x32_bf16 v[92:95], a[188:191], v[48:51], v[92:95]\n"
    "v_mfma_f32_16x16x32_bf16 v[88:91], a[188:191], v[44:47], v[88:91]\n"
    "s_waitcnt lgkmcnt(8)\n"
    "v_mfma_f32_16x16x32_bf16 v[80:83], a[192:195], v[52:55], v[80:83]\n"
    "ds_read_b128 v[44:47], v8 offset:768\n"
    "s_waitcnt lgkmcnt(8)\n"
    "v_mfma_f32_16x16x32_bf16 v[84:87], a[192:195], v[56:59], v[84:87]\n"
    "ds_read_b128 v[48:51], v8 offset:17664\n"
    "v_mfma_f32_16x16x32_bf16 v[92:95], a[196:199], v[56:59], v[92:95]\n"
    "v_mfma_f32_16x16x32_bf16 v[88:91], a[196:199], v[52:55], v[88:91]\n"
    "s_waitcnt lgkmcnt(8)\n"
    "v_mfma_f32_16x16x32_bf16 v[80:83], a[200:203], v[20:23], v[80:83]\n"
    "ds_read_b128 v[52:55], v8 offset:832\n"
    "s_waitcnt lgkmcnt(8)\n"
    "v_mfma_f32_16x16x32_bf16 v[84:87], a[200:203], v[24:27], v[84:87]\n"
    "ds_read_b128 v[56:59], v8 offset:17728\n"
    "s_waitcnt lgkmcnt(2)\n"
    "s_barrier\n"
    "ds_read_b32 v6, v10 offset:64\n"
    "v_mfma_f32_16x16x32_bf16 v[92:95], a[204:207], v[24:27], v[92:95]\n"
    "v_mfma_f32_16x16x32_bf16 v[88:91], a[204:207], v[20:23], v[88:91]\n"
    "v_mfma_f32_16x16x32_bf16 v[80:83], a[208:211], v[28:31], v[80:83]\n"
    "ds_read_b128 v[20:23], v8 offset:896\n"
    "v_mfma_f32_16x16x32_bf16 v[84:87], a[208:211], v[32:35], v[84:87]\n"
    "ds_read_b128 v[24:27], v8 offset:17792\n"
    "s_lshl_b32 s63, s41, 16\n"
    "s_add_u32 s63, s63, s46\n"
    "s_lshr_b32 s38, s63, 8\n"
    "s_xor_b32 s38, s38, s49\n"
    "s_and_b32 s38, s38, 0x2000\n"
    "s_or_b32 s63, s63, s38\n"
    "v_mfma_f32_16x16x32_bf16 v[92:95], a[212:215], v[32:35], v[92:95]\n"
    "v_mfma_f32_16x16x32_bf16 v[88:91], a[212:215], v[28:31], v[88:91]\n"
    "s_waitcnt lgkmcnt(2)\n"
    "v_readfirstlane_b32 s60, v6\n"
    "s_cmp_lg_u32 s60, 0\n"
    "s_cbranch_scc1 LIKA%=\n"
    "v_mfma_f32_16x16x32_bf16 v[80:83], a[216:219], v[36:39], v[80:83]\n"
    "ds_read_b128 v[28:31], v8 offset:960\n"
    "v_mfma_f32_16x16x32_bf16 v[84:87], a[216:219], v[40:43], v[84:87]\n"
    "ds_read_b128 v[32:35], v8 offset:17856\n"
    "v_mfma_f32_16x16x32_bf16 v[92:95], a[220:223], v[40:43], v[92:95]\n"
    "v_mfma_f32_16x16x32_bf16 v[88:91], a[220:223], v[36:39], v[88:91]\n"
    "v_mfma_f32_16x16x32_bf16 v[80:83], a[224:227], v[44:47], v[80:83]\n"
    "v_mfma_f32_16x16x32_bf16 v[84:87], a[224:227], v[48:51], v[84:87]\n"
    "v_mfma_f32_16x16x32_bf16 v[92:95], a[228:231], v[48:51], v[92:95]\n"
    "v_mfma_f32_16x16x32_bf16 v[88:91], a[228:231], v[44:47], v[88:91]\n"
    "v_mfma_f32_16x16x32_bf16 v[80:83], a[232:235], v[52:55], v[80:83]\n"
    "v_mfma_f32_16x16x32_bf16 v[84:87], a[232:235], v[56:59], v[84:87]\n"
    "v_mfma_f32_16x16x32_bf16 v[92:95], a[236:239], v[56:59], v[92:95]\n"
    "v_mfma_f32_16x16x32_bf16 v[88:91], a[236:239], v[52:55], v[88:91]\n"
    "s_waitcnt lgkmcnt(3)\n"
    "v_mfma_f32_16x16x32_bf16 v[80:83], a[240:243], v[20:23], v[80:83]\n"
    "s_waitcnt lgkmcnt(2)\n"
    "v_mfma_f32_16x16x32_bf16 v[84:87], a[240:243], v[24:27], v[84:87]\n"
    "v_mfma_f32_16x16x32_bf16 v[92:95], a[244:247], v[24:27], v[92:95]\n"
    "v_mfma_f32_16x16x32_bf16 v[88:91], a[244:247], v[20:23], v[88:91]\n"
    "s_waitcnt lgkmcnt(1)\n"
    "v_mfma_f32_16x16x32_bf16 v[80:83], a[248:251], v[28:31], v[80:83]\n"
    "s_waitcnt lgkmcnt(0)\n"
    "v_mfma_f32_16x16x32_bf16 v[84:87], a[248:251], v[32:35], v[84:87]\n"
    "v_mfma_f32_16x16x32_bf16 v[92:95], a[252:255], v[32:35], v[92:95]\n"
    "v_mfma_f32_16x16x32_bf16 v[88:91], a[252:255], v[28:31], v[88:91]\n"
    "s_branch LIKB%=\n"
    "LIKA%=:\n"
    "v_mfma_f32_16x16x32_bf16 v[80:83], a[216:219], v[36:39], v[80:83]\n"
    "ds_read_b128 v[28:31], v8 offset:960\n"
    "v_mfma_f32_16x16x32_bf16 v[84:87], a[216:219], v[40:43], v[84:87]\n"
    "ds_read_b128 v[32:35], v8 offset:17856\n"
    "s_add_u32 s39, s44, 67584\n"
    "s_add_u32 s69, s63, 4096\n"
    "v_mfma_f32_16x16x32_bf16 v[92:95], a[220:223], v[40:43], v[92:95]\n"
    "s_add_u32 m0, s39, 0\n"
    "s_nop 0\n"
    "buffer_load_dwordx4 v0, s[16:19], s63 offen offset:0 sc1 lds\n"
    "v_mfma_f32_16x16x32_bf16 v[88:91], a[220:223], v[36:39], v[88:91]\n"
    "v_mfma_f32_16x16x32_bf16 v[80:83], a[224:227], v[44:47], v[80:83]\n"
    "v_mfma_f32_16x16x32_bf16 v[84:87], a[224:227], v[48:51], v[84:87]\n"
    "s_add_u32 m0, s39, 32\n"
    "s_nop 0\n"
    "buffer_load_dwordx4 v0, s[16:19], s63 offen offset:1024 sc1 lds\n"
    "v_mfma_f32_16x16x32_bf16 v[92:95], a[228:231], v[48:51], v[92:95]\n"
    "s_add_u32 m0, s39, 64\n"
    "s_nop 0\n"
    "buffer_load_dwordx4 v0, s[16:19], s63 offen offset:2048 sc1 lds\n"
    "v_mfma_f32_16x16x32_bf16 v[88:91], a[228:231], v[44:47], v[88:91]\n"
    "v_mfma_f32_16x16x32_bf16 v[80:83], a[232:235], v[52:55], v[80:83]\n"
    "v_mfma_f32_16x16x32_bf16 v[84:87], a[232:235], v[56:59], v[84:87]\n"
    "s_add_u32 m0, s39, 96\n"
    "s_nop 0\n"
    "buffer_load_dwordx4 v0, s[16:19], s63 offen offset:3072 sc1 lds\n"
    "v_mfma_f32_16x16x32_bf16 v[92:95], a[236:239], v[56:59], v[92:95]\n"
    "s_add_u32 m0, s39, 4224\n"
    "s_nop 0\n"
    "buffer_load_dwordx4 v0, s[16:19], s69 offen offset:0 sc1 lds\n"
    "v_mfma_f32_16x16x32_bf16 v[88:91], a[236:239], v[52:55], v[88:91]\n"
    "s_waitcnt lgkmcnt(3)\n"
    "v_mfma_f32_16x16x32_bf16 v[80:83], a[240:243], v[20:23], v[80:83]\n"
    "s_waitcnt lgkmcnt(2)\n"
    "v_mfma_f32_16x16x32_bf16 v[84:87], a[240:243], v[24:27], v[84:87]\n"
    "s_add_u32 m0, s39, 4256\n"
    "s_nop 0\n"
    "buffer_load_dwordx4 v0, s[16:19], s69 offen offset:1024 sc1 lds\n"
    "v_mfma_f32_16x16x32_bf16 v[92:95], a[244:247], v[24:27], v[92:95]\n"
    "s_add_u32 m0, s39, 4288\n"
    "s_nop 0\n"
    "buffer_load_dwordx4 v0, s[16:19], s69 offen offset:2048 sc1 lds\n"
    "v_mfma_f32_16x16x32_bf16 v[88:91], a[244:247], v[20:23], v[88:91]\n"
    "s_waitcnt lgkmcnt(1)\n"
    "v_mfma_f32_16x16x32_bf16 v[80:83], a[248:251], v[28:31], v[80:83]\n"
    "s_waitcnt lgkmcnt(0)\n"
    "v_mfma_f32_16x16x32_bf16 v[84:87], a[248:251], v[32:35], v[84:87]\n"
    "s_add_u32 m0, s39, 4320\n"
    "s_nop 0\n"
    "buffer_load_dwordx4 v0, s[16:19], s69 offen offset:3072 sc1 lds\n"
    "v_mfma_f32_16x16x32_bf16 v[92:95], a[252:255], v[32:35], v[92:95]\n"
    "v_mfma_f32_16x16x32_bf16 v[88:91], a[252:255], v[28:31], v[88:91]\n"
    "LIKB%=:\n"
    "s_nop 7\n"
    "v_cvt_pk_bf16_f32 v12, v80, v88\n"
    "v_cvt_pk_bf16_f32 v13, v81, v89\n"
    "v_cvt_pk_bf16_f32 v14, v82, v90\n"
    "v_cvt_pk_bf16_f32 v15, v83, v91\n"
    "buffer_store_dwordx4 v[12:15], v0, s[16:19], s59 offen sc1\n"
    "v_cvt_pk_bf16_f32 v16, v84, v92\n"
    "v_cvt_pk_bf16_f32 v17, v85, v93\n"
    "v_cvt_pk_bf16_f32 v18, v86, v94\n"
    "v_cvt_pk_bf16_f32 v19, v87, v95\n"
    "buffer_store_dwordx4 v[16:19], v0, s[16:19], s62 offen sc1\n"
    "s_mov_b32 s52, s64\n"
    "s_branch LIE%=\n"
    "LIV%=:\n"
    "s_add_u32 s62, s59, 1024\n"
    "ds_read_b128 v[20:23], v8 offset:0\n"
    "ds_read_b128 v[24:27], v8 offset:16896\n"
    "ds_read_b128 v[28:31], v8 offset:64\n"
    "ds_read_b128 v[32:35], v8 offset:16960\n"
    "ds_read_b128 v[36:39], v8 offset:128\n"
    "ds_read_b128 v[40:43], v8 offset:17024\n"
    "ds_read_b128 v[44:47], v8 offset:192\n"
    "ds_read_b128 v[48:51], v8 offset:17088\n"
    "s_waitcnt lgkmcnt(7)\n"
    "v_mfma_f32_16x16x32_bf16 v[64:67], v[20:23], a[0:3], 0\n"
    "ds_read_b128 v[52:55], v8 offset:256\n"
    "s_waitcnt lgkmcnt(7)\n"
    "v_mfma_f32_16x16x32_bf16 v[68:71], v[24:27], a[0:3], 0\n"
    "ds_read_b128 v[56:59], v8 offset:17152\n"
    "v_mfma_f32_16x16x32_bf16 v[76:79], v[24:27], a[4:7], 0\n"
    "v_mfma_f32_16x16x32_bf16 v[72:75], v[20:23], a[4:7], 0\n"
    "s_waitcnt lgkmcnt(7)\n"
    "v_mfma_f32_16x16x32_bf16 v[64:67], v[28:31], a[8:11], v[64:67]\n"
    "ds_read_b128 v[20:23], v8 offset:320\n"
    "s_waitcnt lgkmcnt(7)\n"
    "v_mfma_f32_16x16x32_bf16 v[68:71], v[32:35], a[8:11], v[68:71]\n"
    "ds_read_b128 v[24:27], v8 offset:17216\n"
    "v_mfma_f32_16x16x32_bf16 v[76:79], v[32:35], a[12:15], v[76:79]\n"
    "v_mfma_f32_16x16x32_bf16 v[72:75], v[28:31], a[12:15], v[72:75]\n"
    "s_waitcnt lgkmcnt(7)\n"
    "v_mfma_f32_16x16x32_bf16 v[64:67], v[36:39], a[16:19], v[64:67]\n"
    "ds_read_b128 v[28:31], v8 offset:384\n"
    "s_waitcnt lgkmcnt(7)\n"
    "v_mfma_f32_16x16x32_bf16 v[68:71], v[40:43], a[16:19], v[68:71]\n"
    "ds_read_b128 v[32:35], v8 offset:17280\n"
    "v_mfma_f32_16x16x32_bf16 v[76:79], v[40:43], a[20:23], v[76:79]\n"
    "v_mfma_f32_16x16x32_bf16 v[72:75], v[36:39], a[20:23], v[72:75]\n"
    "s_waitcnt lgkmcnt(7)\n"
    "v_mfma_f32_16x16x32_bf16 v[64:67], v[44:47], a[24:27], v[64:67]\n"
    "ds_read_b128 v[36:39], v8 offset:448\n"
    "s_waitcnt lgkmcnt(7)\n"
    "v_mfma_f32_16x16x32_bf16 v[68:71], v[48:51], a[24:27], v[68:71]\n"
    "ds_read_b128 v[40:43], v8 offset:17344\n"
    "v_mfma_f32_16x16x32_bf16 v[76:79], v[48:51], a[28:31], v[76:79]\n"
    "v_mfma_f32_16x16x32_bf16 v[72:75], v[44:47], a[28:31], v[72:75]\n"
    "s_waitcnt lgkmcnt(7)\n"
    "v_mfma_f32_16x16x32_bf16 v[64:67], v[52:55], a[32:35], v[64:67]\n"
    "ds_read_b128 v[44:47], v8 offset:512\n"
    "s_waitcnt lgkmcnt(7)\n"
    "v_mfma_f32_16x16x32_bf16 v[68:71], v[56:59], a[32:35], v[68:71]\n"
    "ds_read_b128 v[48:51], v8 offset:17408\n"
    "v_mfma_f32_16x16x32_bf16 v[76:79], v[56:59], a[36:39], v[76:79]\n"
    "v_mfma_f32_16x16x32_bf16 v[72:75], v[52:55], a[36:39], v[72:75]\n"
    "s_waitcnt lgkmcnt(7)\n"
    "v_mfma_f32_16x16x32_bf16 v[64:67], v[20:23], a[40:43], v[64:67]\n"
    "ds_read_b128 v[52:55], v8 offset:576\n"
    "s_waitcnt lgkmcnt(7)\n"
    "v_mfma_f32_16x16x32_bf16 v[68:71], v[24:27], a[40:43], v[68:71]\n"
    "ds_read_b128 v[56:59], v8 offset:17472\n"
    "v_mfma_f32_16x16x32_bf16 v[76:79], v[24:27], a[44:47], v[76:79]\n"
    "v_mfma_f32_16x16x32_bf16 v[72:75], v[20:23], a[44:47], v[72:75]\n"
    "s_waitcnt lgkmcnt(7)\n"
    "v_mfma_f32_16x16x32_bf16 v[64:67], v[28:31], a[48:51], v[64:67]\n"
    "ds_read_b128 v[20:23], v8 offset:640\n"
    "s_waitcnt lgkmcnt(7)\n"
    "v_mfma_f32_16x16x32_bf16 v[68:71], v[32:35], a[48:51], v[68:71]\n"
    "ds_read_b128 v[24:27], v8 offset:17536\n"
    "v_mfma_f32_16x16x32_bf16 v[76:79], v[32:35], a[52:55], v[76:79]\n"
    "v_mfma_f32_16x16x32_bf16 v[72:75], v[28:31], a[52:55], v[72:75]\n"
    "s_waitcnt lgkmcnt(7)\n"
    "v_mfma_f32_16x16x32_bf16 v[64:67], v[36:39], a[56:59], v[64:67]\n"
    "ds_read_b128 v[28:31], v8 offset:704\n"
    "s_waitcnt lgkmcnt(7)\n"
    "v_mfma_f32_16x16x32_bf16 v[68:71], v[40:43], a[56:59], v[68:71]\n"
    "ds_read_b128 v[32:35], v8 offset:17600\n"
    "v_mfma_f32_16x16x32_bf16 v[76:79], v[40:43], a[60:63], v[76:79]\n"
    "v_mfma_f32_16x16x32_bf16 v[72:75], v[36:39], a[60:63], v[72:75]\n"
    "s_waitcnt lgkmcnt(7)\n"
    "v_mfma_f32_16x16x32_bf16 v[64:67], v[44:47], a[64:67], v[64:67]\n"
    "ds_read_b128 v[36:39], v8 offset:768\n"
    "s_waitcnt lgkmcnt(7)\n"
    "v_mfma_f32_16x16x32_bf16 v[68:71], v[48:51], a[64:67], v[68:71]\n"
    "ds_read_b128 v[40:43], v8 offset:17664\n"
    "v_mfma_f32_16x16x32_bf16 v[76:79], v[48:51], a[68:71], v[76:79]\n"
    "v_mfma_f32_16x16x32_bf16 v[72:75], v[44:47], a[68:71], v[72:75]\n"
    "s_waitcnt lgkmcnt(7)\n"
    "v_mfma_f32_16x16x32_bf16 v[64:67], v[52:55], a[72:75], v[64:67]\n"
    "ds_read_b128 v[44:47], v8 offset:832\n"
    "s_waitcnt lgkmcnt(7)\n"
    "v_mfma_f32_16x16x32_bf16 v[68:71], v[56:59], a[72:75], v[68:71]\n"
    "ds_read_b128 v[48:51], v8 offset:17728\n"
    "v_mfma_f32_16x16x32_bf16 v[76:79], v[56:59], a[76:79], v[76:79]\n"
    "v_mfma_f32_16x16x32_bf16 v[72:75], v[52:55], a[76:79], v[72:75]\n"
    "s_waitcnt lgkmcnt(7)\n"
    "v_mfma_f32_16x16x32_bf16 v[64:67], v[20:23], a[80:83], v[64:67]\n"
    "ds_read_b128 v[52:55], v8 offset:896\n"
    "s_waitcnt lgkmcnt(7)\n"
    "v_mfma_f32_16x16x32_bf16 v[68:71], v[24:27], a[80:83], v[68:71]\n"
    "ds_read_b128 v[56:59], v8 offset:17792\n"
    "v_mfma_f32_16x16x32_bf16 v[76:79], v[24:27], a[84:87], v[76:79]\n"
    "v_mfma_f32_16x16x32_bf16 v[72:75], v[20:23], a[84:87], v[72:75]\n"
    "s_waitcnt lgkmcnt(7)\n"
    "v_mfma_f32_16x16x32_bf16 v[64:67], v[28:31], a[88:91], v[64:67]\n"
    "ds_read_b128 v[20:23], v8 offset:960\n"
    "s_waitcnt lgkmcnt(7)\n"
    "v_mfma_f32_16x16x32_bf16 v[68:71], v[32:35], a[88:91], v[68:71]\n"
    "ds_read_b128 v[24:27], v8 offset:17856\n"
    "v_mfma_f32_16x16x32_bf16 v[76:79], v[32:35], a[92:95], v[76:79]\n"
    "v_mfma_f32_16x16x32_bf16 v[72:75], v[28:31], a[92:95], v[72:75]\n"
    "s_waitcnt lgkmcnt(7)\n"
    "v_mfma_f32_16x16x32_bf16 v[64:67], v[36:39], a[96:99], v[64:67]\n"
    "ds_read_b128 v[28:31], v8 offset:0\n"
    "s_waitcnt lgkmcnt(7)\n"
    "v_mfma_f32_16x16x32_bf16 v[68:71], v[40:43], a[96:99], v[68:71]\n"
    "ds_read_b128 v[32:35], v8 offset:16896\n"
    "s_waitcnt vmcnt(8)\n"
    "s_barrier\n"
    "v_readfirstlane_b32 s38, v5\n"
    "s_or_b32 s52, s52, s68\n"
    "s_or_b32 s38, s38, s68\n"
    "s_cbranch_scc0 LK4_%=\n"
    "LJ4_%=:\n"
    "buffer_store_dword v5, v9, s[16:19], s52 offen sc1\n"
    "s_mov_b32 s52, 0x80000000\n"
    "v_mfma_f32_16x16x32_bf16 v[76:79], v[40:43], a[100:103], v[76:79]\n"
    "v_mfma_f32_16x16x32_bf16 v[72:75], v[36:39], a[100:103], v[72:75]\n"
    "s_waitcnt lgkmcnt(7)\n"
    "v_mfma_f32_16x16x32_bf16 v[64:67], v[44:47], a[104:107], v[64:67]\n"
    "ds_read_b128 v[36:39], v8 offset:64\n"
    "s_waitcnt lgkmcnt(7)\n"
    "v_mfma_f32_16x16x32_bf16 v[68:71], v[48:51], a[104:107], v[68:71]\n"
    "ds_read_b128 v[40:43], v8 offset:16960\n"
    "v_mfma_f32_16x16x32_bf16 v[76:79], v[48:51], a[108:111], v[76:79]\n"
    "v_mfma_f32_16x16x32_bf16 v[72:75], v[44:47], a[108:111], v[72:75]\n"
    "s_waitcnt lgkmcnt(7)\n"
    "v_mfma_f32_16x16x32_bf16 v[64:67], v[52:55], a[112:115], v[64:67]\n"
    "ds_read_b128 v[44:47], v8 offset:128\n"
    "s_waitcnt lgkmcnt(7)\n"
    "v_mfma_f32_16x16x32_bf16 v[68:71], v[56:59], a[112:115], v[68:71]\n"
    "ds_read_b128 v[48:51], v8 offset:17024\n"
    "v_mfma_f32_16x16x32_bf16 v[76:79], v[56:59], a[116:119], v[76:79]\n"
    "v_mfma_f32_16x16x32_bf16 v[72:75], v[52:55], a[116:119], v[72:75]\n"
    "s_waitcnt lgkmcnt(7)\n"
    "v_mfma_f32_16x16x32_bf16 v[64:67], v[20:23], a[120:123], v[64:67]\n"
    "ds_read_b128 v[52:55], v8 offset:192\n"
    "s_waitcnt lgkmcnt(7)\n"
    "v_mfma_f32_16x16x32_bf16 v[68:71], v[24:27], a[120:123], v[68:71]\n"
    "ds_read_b128 v[56:59], v8 offset:17088\n"
    "v_mfma_f32_16x16x32_bf16 v[76:79], v[24:27], a[124:127], v[76:79]\n"
    "v_mfma_f32_16x16x32_bf16 v[72:75], v[20:23], a[124:127], v[72:75]\n"
    "s_waitcnt lgkmcnt(7)\n"
    "v_mfma_f32_16x16x32_bf16 v[80:83], v[28:31], a[128:131], 0\n"
    "ds_read_b128 v[20:23], v8 offset:256\n"
    "s_waitcnt lgkmcnt(7)\n"
    "v_mfma_f32_16x16x32_bf16 v[84:87], v[32:35], a[128:131], 0\n"
    "ds_read_b128 v[24:27], v8 offset:17152\n"
    "buffer_load_dword v6, off, s[16:19], s53 sc1\n"
    "v_mfma_f32_16x16x32_bf16 v[92:95], v[32:35], a[132:135], 0\n"
    "v_mfma_f32_16x16x32_bf16 v[88:91], v[28:31], a[132:135], 0\n"
    "s_waitcnt lgkmcnt(7)\n"
    "v_mfma_f32_16x16x32_bf16 v[80:83], v[36:39], a[136:139], v[80:83]\n"
    "ds_read_b128 v[28:31], v8 offset:320\n"
    "s_waitcnt lgkmcnt(7)\n"
    "v_mfma_f32_16x16x32_bf16 v[84:87], v[40:43], a[136:139], v[84:87]\n"
    "ds_read_b128 v[32:35], v8 offset:17216\n"
    "v_cvt_pk_bf16_f32 v12, v64, v65\n"
    "v_mfma_f32_16x16x32_bf16 v[92:95], v[40:43], a[140:143], v[92:95]\n"
    "v_cvt_pk_bf16_f32 v13, v66, v67\n"
    "v_mfma_f32_16x16x32_bf16 v[88:91], v[36:39], a[140:143], v[88:91]\n"
    "s_waitcnt lgkmcnt(7)\n"
    "v_cvt_pk_bf16_f32 v14, v68, v69\n"
    "v_mfma_f32_16x16x32_bf16 v[80:83], v[44:47], a[144:147], v[80:83]\n"
    "ds_read_b128 v[36:39], v8 offset:384\n"
    "s_waitcnt lgkmcnt(7)\n"
    "v_mfma_f32_16x16x32_bf16 v[84:87], v[48:51], a[144:147], v[84:87]\n"
    "ds_read_b128 v[40:43], v8 offset:17280\n"
    "v_cvt_pk_bf16_f32 v15, v70, v71\n"
    "v_mfma_f32_16x16x32_bf16 v[92:95], v[48:51], a[148:151], v[92:95]\n"
    "buffer_store_dwordx4 v[12:15], v0, s[16:19], s59 offen sc1\n"
    "v_mfma_f32_16x16x32_bf16 v[88:91], v[44:47], a[148:151], v[88:91]\n"
    "s_waitcnt lgkmcnt(7)\n"
    "v_cvt_pk_bf16_f32 v16, v72, v73\n"
    "v_mfma_f32_16x16x32_bf16 v[80:83], v[52:55], a[152:155], v[80:83]\n"
    "ds_read_b128 v[44:47], v8 offset:448\n"
    "s_waitcnt lgkmcnt(7)\n"
    "v_mfma_f32_16x16x32_bf16 v[84:87], v[56:59], a[152:155], v[84:87]\n"
    "ds_read_b128 v[48:51], v8 offset:17344\n"
    "v_cvt_pk_bf16_f32 v17, v74, v75\n"
    "v_mfma_f32_16x16x32_bf16 v[92:95], v[56:59], a[156:159], v[92:95]\n"
    "v_cvt_pk_bf16_f32 v18, v76, v77\n"
    "v_mfma_f32_16x16x32_bf16 v[88:91], v[52:55], a[156:159], v[88:91]\n"
    "s_waitcnt lgkmcnt(7)\n"
    "v_cvt_pk_bf16_f32 v19, v78, v79\n"
    "v_mfma_f32_16x16x32_bf16 v[80:83], v[20:23], a[160:163], v[80:83]\n"
    "ds_read_b128 v[52:55], v8 offset:512\n"
    "s_waitcnt lgkmcnt(7)\n"
    "v_mfma_f32_16x16x32_bf16 v[84:87], v[24:27], a[160:163], v[84:87]\n"
    "ds_read_b128 v[56:59], v8 offset:17408\n"
    "buffer_store_dwordx4 v[16:19], v0, s[16:19], s62 offen sc1\n"
    "v_mfma_f32_16x16x32_bf16 v[92:95], v[24:27], a[164:167], v[92:95]\n"
    "s_add_u32 s59, s59, 16384\n"
    "s_add_u32 s62, s62, 16384\n"
    "v_mfma_f32_16x16x32_bf16 v[88:91], v[20:23], a[164:167], v[88:91]\n"
    "s_waitcnt lgkmcnt(7)\n"
    "v_mfma_f32_16x16x32_bf16 v[80:83], v[28:31], a[168:171], v[80:83]\n"
    "ds_read_b128 v[20:23], v8 offset:576\n"
    "s_waitcnt lgkmcnt(7)\n"
    "v_mfma_f32_16x16x32_bf16 v[84:87], v[32:35], a[168:171], v[84:87]\n"
    "ds_read_b128 v[24:27], v8 offset:17472\n"
    "v_mfma_f32_16x16x32_bf16 v[92:95], v[32:35], a[172:175], v[92:95]\n"
    "v_mfma_f32_16x16x32_bf16 v[88:91], v[28:31], a[172:175], v[88:91]\n"
    "s_waitcnt lgkmcnt(7)\n"
    "v_mfma_f32_16x16x32_bf16 v[80:83], v[36:39], a[176:179], v[80:83]\n"
    "ds_read_b128 v[28:31], v8 offset:640\n"
    "s_waitcnt lgkmcnt(7)\n"
    "v_mfma_f32_16x16x32_bf16 v[84:87], v[40:43], a[176:179], v[84:87]\n"
    "ds_read_b128 v[32:35], v8 offset:17536\n"
    "v_mfma_f32_16x16x32_bf16 v[92:95], v[40:43], a[180:183], v[92:95]\n"
    "v_mfma_f32_16x16x32_bf16 v[88:91], v[36:39], a[180:183], v[88:91]\n"
    "s_waitcnt lgkmcnt(7)\n"
    "v_mfma_f32_16x16x32_bf16 v[80:83], v[44:47], a[184:187], v[80:83]\n"
    "ds_read_b128 v[36:39], v8 offset:704\n"
    "s_waitcnt lgkmcnt(7)\n"
    "v_mfma_f32_16x16x32_bf16 v[84:87], v[48:51], a[184:187], v[84:87]\n"
    "ds_read_b128 v[40:43], v8 offset:17600\n"
    "s_waitcnt vmcnt(2)\n"
    "ds_write_b32 v11, v6 offset:64\n"
    "v_mfma_f32_16x16x32_bf16 v[92:95], v[48:51], a[188:191], v[92:95]\n"
    "v_mfma_f32_16x16x32_bf16 v[88:91], v[44:47], a[188:191], v[88:91]\n"
    "s_waitcnt lgkmcnt(8)\n"
    "v_mfma_f32_16x16x32_bf16 v[80:83], v[52:55], a[192:195], v[80:83]\n"
    "ds_read_b128 v[44:47], v8 offset:768\n"
    "s_waitcnt lgkmcnt(8)\n"
    "v_mfma_f32_16x16x32_bf16 v[84:87], v[56:59], a[192:195], v[84:87]\n"
    "ds_read_b128 v[48:51], v8 offset:17664\n"
    "v_mfma_f32_16x16x32_bf16 v[92:95], v[56:59], a[196:199], v[92:95]\n"
    "v_mfma_f32_16x16x32_bf16 v[88:91], v[52:55], a[196:199], v[88:91]\n"
    "s_waitcnt lgkmcnt(8)\n"
    "v_mfma_f32_16x16x32_bf16 v[80:83], v[20:23], a[200:203], v[80:83]\n"
    "ds_read_b128 v[52:55], v8 offset:832\n"
    "s_waitcnt lgkmcnt(8)\n"
    "v_mfma_f32_16x16x32_bf16 v[84:87], v[24:27], a[200:203], v[84:87]\n"
    "ds_read_b128 v[56:59], v8 offset:17728\n"
    "s_waitcnt lgkmcnt(2)\n"
    "s_barrier\n"
    "ds_read_b32 v6, v10 offset:64\n"
    "v_mfma_f32_16x16x32_bf16 v[92:95], v[24:27], a[204:207], v[92:95]\n"
    "v_mfma_f32_16x16x32_bf16 v[88:91], v[20:23], a[204:207], v[88:91]\n"
    "v_mfma_f32_16x16x32_bf16 v[80:83], v[28:31], a[208:211], v[80:83]\n"
    "ds_read_b128 v[20:23], v8 offset:896\n"
    "v_mfma_f32_16x16x32_bf16 v[84:87], v[32:35], a[208:211], v[84:87]\n"
    "ds_read_b128 v[24:27], v8 offset:17792\n"
    "s_lshl_b32 s63, s41, 16\n"
    "s_add_u32 s63, s63, s46\n"
    "s_lshr_b32 s38, s63, 8\n"
    "s_xor_b32 s38, s38, s49\n"
    "s_and_b32 s38, s38, 0x2000\n"
    "s_or_b32 s63, s63, s38\n"
    "v_mfma_f32_16x16x32_bf16 v[92:95], v[32:35], a[212:215], v[92:95]\n"
    "v_mfma_f32_16x16x32_bf16 v[88:91], v[28:31], a[212:215], v[88:91]\n"
    "s_waitcnt lgkmcnt(2)\n"
    "v_readfirstlane_b32 s60, v6\n"
    "s_cmp_lg_u32 s60, 0\n"
    "s_cbranch_scc1 LITA%=\n"
    "v_mfma_f32_16x16x32_bf16 v[80:83], v[36:39], a[216:219], v[80:83]\n"
    "ds_read_b128 v[28:31], v8 offset:960\n"
    "v_mfma_f32_16x16x32_bf16 v[84:87], v[40:43], a[216:219], v[84:87]\n"
    "ds_read_b128 v[32:35], v8 offset:17856\n"
    "v_mfma_f32_16x16x32_bf16 v[92:95], v[40:43], a[220:223], v[92:95]\n"
    "v_mfma_f32_16x16x32_bf16 v[88:91], v[36:39], a[220:223], v[88:91]\n"
    "v_mfma_f32_16x16x32_bf16 v[80:83], v[44:47], a[224:227], v[80:83]\n"
    "v_mfma_f32_16x16x32_bf16 v[84:87], v[48:51], a[224:227], v[84:87]\n"
    "v_mfma_f32_16x16x32_bf16 v[92:95], v[48:51], a[228:231], v[92:95]\n"
    "v_mfma_f32_16x16x32_bf16 v[88:91], v[44:47], a[228:231], v[88:91]\n"
    "v_mfma_f32_16x16x32_bf16 v[80:83], v[52:55], a[232:235], v[80:83]\n"
    "v_mfma_f32_16x16x32_bf16 v[84:87], v[56:59], a[232:235], v[84:87]\n"
    "v_mfma_f32_16x16x32_bf16 v[92:95], v[56:59], a[236:239], v[92:95]\n"
    "v_mfma_f32_16x16x32_bf16 v[88:91], v[52:55], a[236:239], v[88:91]\n"
    "s_waitcnt lgkmcnt(3)\n"
    "v_mfma_f32_16x16x32_bf16 v[80:83], v[20:23], a[240:243], v[80:83]\n"
    "s_waitcnt lgkmcnt(2)\n"
    "v_mfma_f32_16x16x32_bf16 v[84:87], v[24:27], a[240:243], v[84:87]\n"
    "v_mfma_f32_16x16x32_bf16 v[92:95], v[24:27], a[244:247], v[92:95]\n"
    "v_mfma_f32_16x16x32_bf16 v[88:91], v[20:23], a[244:247], v[88:91]\n"
    "s_waitcnt lgkmcnt(1)\n"
    "v_mfma_f32_16x16x32_bf16 v[80:83], v[28:31], a[248:251], v[80:83]\n"
    "s_waitcnt lgkmcnt(0)\n"
    "v_mfma_f32_16x16x32_bf16 v[84:87], v[32:35], a[248:251], v[84:87]\n"
    "v_mfma_f32_16x16x32_bf16 v[92:95], v[32:35], a[252:255], v[92:95]\n"
    "v_mfma_f32_16x16x32_bf16 v[88:91], v[28:31], a[252:255], v[88:91]\n"
    "s_branch LITB%=\n"
    "LITA%=:\n"
    "v_mfma_f32_16x16x32_bf16 v[80:83], v[36:39], a[216:219], v[80:83]\n"
    "ds_read_b128 v[28:31], v8 offset:960\n"
    "v_mfma_f32_16x16x32_bf16 v[84:87], v[40:43], a[216:219], v[84:87]\n"
    "ds_read_b128 v[32:35], v8 offset:17856\n"
    "s_add_u32 s39, s44, 67584\n"
    "s_add_u32 s69, s63, 4096\n"
    "v_mfma_f32_16x16x32_bf16 v[92:95], v[40:43], a[220:223], v[92:95]\n"
    "s_add_u32 m0, s39, 0\n"
    "s_nop 0\n"
    "buffer_load_dwordx4 v0, s[16:19], s63 offen offset:0 sc1 lds\n"
    "v_mfma_f32_16x16x32_bf16 v[88:91], v[36:39], a[220:223], v[88:91]\n"
    "v_mfma_f32_16x16x32_bf16 v[80:83], v[44:47], a[224:227], v[80:83]\n"
    "v_mfma_f32_16x16x32_bf16 v[84:87], v[48:51], a[224:227], v[84:87]\n"
    "s_add_u32 m0, s39, 32\n"
    "s_nop 0\n"
    "buffer_load_dwordx4 v0, s[16:19], s63 offen offset:1024 sc1 lds\n"
    "v_mfma_f32_16x16x32_bf16 v[92:95], v[48:51], a[228:231], v[92:95]\n"
    "s_add_u32 m0, s39, 64\n"
    "s_nop 0\n"
    "buffer_load_dwordx4 v0, s[16:19], s63 offen offset:2048 sc1 lds\n"
    "v_mfma_f32_16x16x32_bf16 v[88:91], v[44:47], a[228:231], v[88:91]\n"
    "v_mfma_f32_16x16x32_bf16 v[80:83], v[52:55], a[232:235], v[80:83]\n"
    "v_mfma_f32_16x16x32_bf16 v[84:87], v[56:59], a[232:235], v[84:87]\n"
    "s_add_u32 m0, s39, 96\n"
    "s_nop 0\n"
    "buffer_load_dwordx4 v0, s[16:19], s63 offen offset:3072 sc1 lds\n"
    "v_mfma_f32_16x16x32_bf16 v[92:95], v[56:59], a[236:239], v[92:95]\n"
    "s_add_u32 m0, s39, 4224\n"
    "s_nop 0\n"
    "buffer_load_dwordx4 v0, s[16:19], s69 offen offset:0 sc1 lds\n"
    "v_mfma_f32_16x16x32_bf16 v[88:91], v[52:55], a[236:239], v[88:91]\n"
    "s_waitcnt lgkmcnt(3)\n"
    "v_mfma_f32_16x16x32_bf16 v[80:83], v[20:23], a[240:243], v[80:83]\n"
    "s_waitcnt lgkmcnt(2)\n"
    "v_mfma_f32_16x16x32_bf16 v[84:87], v[24:27], a[240:243], v[84:87]\n"
    "s_add_u32 m0, s39, 4256\n"
    "s_nop 0\n"
    "buffer_load_dwordx4 v0, s[16:19], s69 offen offset:1024 sc1 lds\n"
    "v_mfma_f32_16x16x32_bf16 v[92:95], v[24:27], a[244:247], v[92:95]\n"
    "s_add_u32 m0, s39, 4288\n"
    "s_nop 0\n"
    "buffer_load_dwordx4 v0, s[16:19], s69 offen offset:2048 sc1 lds\n"
    "v_mfma_f32_16x16x32_bf16 v[88:91], v[20:23], a[244:247], v[88:91]\n"
    "s_waitcnt lgkmcnt(1)\n"
    "v_mfma_f32_16x16x32_bf16 v[80:83], v[28:31], a[248:251], v[80:83]\n"
    "s_waitcnt lgkmcnt(0)\n"
    "v_mfma_f32_16x16x32_bf16 v[84:87], v[32:35], a[248:251], v[84:87]\n"
    "s_add_u32 m0, s39, 4320\n"
    "s_nop 0\n"
    "buffer_load_dwordx4 v0, s[16:19], s69 offen offset:3072 sc1 lds\n"
    "v_mfma_f32_16x16x32_bf16 v[92:95], v[32:35], a[252:255], v[92:95]\n"
    "v_mfma_f32_16x16x32_bf16 v[88:91], v[28:31], a[252:255], v[88:91]\n"
    "LITB%=:\n"
    "s_nop 7\n"
    "v_cvt_pk_bf16_f32 v12, v80, v81\n"
    "v_cvt_pk_bf16_f32 v13, v82, v83\n"
    "v_cvt_pk_bf16_f32 v14, v84, v85\n"
    "v_cvt_pk_bf16_f32 v15, v86, v87\n"
    "buffer_store_dwordx4 v[12:15], v0, s[16:19], s59 offen sc1\n"
    "v_cvt_pk_bf16_f32 v16, v88, v89\n"
    "v_cvt_pk_bf16_f32 v17, v90, v91\n"
    "v_cvt_pk_bf16_f32 v18, v92, v93\n"
    "v_cvt_pk_bf16_f32 v19, v94, v95\n"
    "buffer_store_dwordx4 v[16:19], v0, s[16:19], s62 offen sc1\n"
    "s_mov_b32 s52, s64\n"
    "LIE%=:\n"
    "s_cmp_lg_u32 s60, 0\n"
    "s_cbranch_scc1 L2%=\n"
    "s_branch LT%=\n"
    "LX%=:\n"
    "s_barrier\n"
    "s_cmp_eq_u32 s60, 0\n"
    "s_cbranch_scc1 LT%=\n"
    "s_branch LE%=\n"
    "LRD%=:\n"
    "s_waitcnt lgkmcnt(0)\n"
    "s_barrier\n"
    "s_add_u32 s27, s27, 1\n"
    "s_cmp_gt_u32 s27, 0x40000\n"
    "s_cbranch_scc1 LE%=\n"
    "s_add_u32 s39, s44, 67584\n"
    "s_add_u32 s69, s63, 4096\n"
    "s_add_u32 m0, s39, 0\n"
    "s_nop 0\n"
    "buffer_load_dwordx4 v0, s[16:19], s63 offen offset:0 sc1 lds\n"
    "s_add_u32 m0, s39, 32\n"
    "s_nop 0\n"
    "buffer_load_dwordx4 v0, s[16:19], s63 offen offset:1024 sc1 lds\n"
    "s_add_u32 m0, s39, 64\n"
    "s_nop 0\n"
    "buffer_load_dwordx4 v0, s[16:19], s63 offen offset:2048 sc1 lds\n"
    "s_add_u32 m0, s39, 96\n"
    "s_nop 0\n"
    "buffer_load_dwordx4 v0, s[16:19], s63 offen offset:3072 sc1 lds\n"
    "s_add_u32 m0, s39, 4224\n"
    "s_nop 0\n"
    "buffer_load_dwordx4 v0, s[16:19], s69 offen offset:0 sc1 lds\n"
    "s_add_u32 m0, s39, 4256\n"
    "s_nop 0\n"
    "buffer_load_dwordx4 v0, s[16:19], s69 offen offset:1024 sc1 lds\n"
    "s_add_u32 m0, s39, 4288\n"
    "s_nop 0\n"
    "buffer_load_dwordx4 v0, s[16:19], s69 offen offset:2048 sc1 lds\n"
    "s_add_u32 m0, s39, 4320\n"
    "s_nop 0\n"
    "buffer_load_dwordx4 v0, s[16:19], s69 offen offset:3072 sc1 lds\n"
    "s_waitcnt vmcnt(0)\n"
    "s_branch LG%=\n"
    "LK0_%=:\n"
    "s_mov_b32 s37, 0x80000\n"
    "LL0_%=:\n"
    "buffer_load_dword v5, off, s[16:19], s67 sc1\n"
    "s_waitcnt vmcnt(0)\n"
    "v_readfirstlane_b32 s38, v5\n"
    "s_cmp_lg_u32 s38, 0\n"
    "s_cbranch_scc1 LJ0_%=\n"
    "s_sleep 1\n"
    "s_sub_u32 s37, s37, 1\n"
    "s_cbranch_scc0 LL0_%=\n"
    "s_endpgm\n"
    "LK1_%=:\n"
    "s_mov_b32 s37, 0x80000\n"
    "LL1_%=:\n"
    "buffer_load_dword v5, off, s[16:19], s67 sc1\n"
    "s_waitcnt vmcnt(0)\n"
    "v_readfirstlane_b32 s38, v5\n"
    "s_cmp_lg_u32 s38, 0\n"
    "s_cbranch_scc1 LJ1_%=\n"
    "s_sleep 1\n"
    "s_sub_u32 s37, s37, 1\n"
    "s_cbranch_scc0 LL1_%=\n"
    "s_endpgm\n"
    "LK2_%=:\n"
    "s_mov_b32 s37, 0x80000\n"
    "LL2_%=:\n"
    "buffer_load_dword v5, off, s[16:19], s67 sc1\n"
    "s_waitcnt vmcnt(0)\n"
    "v_readfirstlane_b32 s38, v5\n"
    "s_cmp_lg_u32 s38, 0\n"
    "s_cbranch_scc1 LJ2_%=\n"
    "s_sleep 1\n"
    "s_sub_u32 s37, s37, 1\n"
    "s_cbranch_scc0 LL2_%=\n"
    "s_endpgm\n"
    "LK3_%=:\n"
    "s_mov_b32 s37, 0x80000\n"
    "LL3_%=:\n"
    "buffer_load_dword v5, off, s[16:19], s67 sc1\n"
    "s_waitcnt vmcnt(0)\n"
    "v_readfirstlane_b32 s38, v5\n"
    "s_cmp_lg_u32 s38, 0\n"
    "s_cbranch_scc1 LJ3_%=\n"
    "s_sleep 1\n"
    "s_sub_u32 s37, s37, 1\n"
    "s_cbranch_scc0 LL3_%=\n"
    "s_endpgm\n"
    "LK4_%=:\n"
    "s_mov_b32 s37, 0x80000\n"
    "LL4_%=:\n"
    "buffer_load_dword v5, off, s[16:19], s67 sc1\n"
    "s_waitcnt vmcnt(0)\n"
    "v_readfirstlane_b32 s38, v5\n"
    "s_cmp_lg_u32 s38, 0\n"
    "s_cbranch_scc1 LJ4_%=\n"
    "s_sleep 1\n"
    "s_sub_u32 s37, s37, 1\n"
    "s_cbranch_scc0 LL4_%=\n"
    "s_endpgm\n"
    "LE%=:\n"
    "s_endpgm\n"
    :
    : "{v0}"(hv0), "{v1}"(hv1), "{v2}"(hv2), "{v3}"(hv3), "{v4}"(hv4), "{v5}"(hv5)
    : "v255", "a255", "s101", "vcc", "scc", "memory");
  __builtin_unreachable();
}
__device__ __forceinline__ void o_role8(int w, int V, int u, const unsigned* rdy, char* ws, const u16* Wo) {
  const int lane = lane_id(), g = lane >> 4, q = (lane >> 2) & 3, p = lane & 3, hg = u >> 2, cs = u & 3;
  const unsigned me = w == 1 ? 0u : OOR, c1 = 0u;
  const int pv[19] = {w, (V == 1 ? 4 : 3) * 524288 + hg * 131072 + w * 2048, (int)(V ? c1 ^ 0x2000u : c1), (int)(c1 ^ 0x2000u), (int)nearof(Z_FO + V * 16384, c1 ^ 0x2000u) + hg * 2048, (int)((nearof(Z_FP + V * 16384, c1 ^ 0x2000u) + u * 256) | me),
                      (int)(Z_PART + V * MBY(16)) + u * 16384 + w * 2048, (int)Z_ERR, (int)(size_t)Wo, (int)((size_t)Wo >> 32) & 0xffff, (int)(size_t)ws, (int)((size_t)ws >> 32) & 0xffff, (int)ZWS_BYTES,
                      (int)((Z_DBG + MBY(2) + (V * 16 + u) * 1024) | me), (int)(size_t)rdy, (int)((size_t)rdy >> 32), V ? ODL : 0, (int)(u ? OOR & ~me : 4096u), V < ROLL ? 3 : 63};
  int hv0 = lane * 16, hv1 = 131072 + (lane & 31) * 272 + w * 64 + (lane >> 5) * 16, hv2 = 131072 + (8 * w + g) * 272 + (lane & 15) * 16;
  int hv3 = (hg * 1024 + w * 128 + g) * 1024 + cs * 256 + ((lane & 15) ^ (g * 4)) * 16, hv4 = (4 * (g >> 1) + q) * 256 + 64 * (w ^ q) + 32 * (g & 1) + 16 * (p & 1) + 8 * (p >> 1), hv5 = 0;
#pragma unroll
  for (int i = 0; i < 19; ++i) hv5 = lane == i ? pv[i] : hv5;
  asm volatile(
    "s_nop 1\n"
    "v_readlane_b32 s36, v5, 0\n"
    "v_readlane_b32 s41, v5, 1\n"
    "v_readlane_b32 s42, v5, 2\n"
    "v_readlane_b32 s43, v5, 3\n"
    "v_readlane_b32 s44, v5, 4\n"
    "v_readlane_b32 s45, v5, 5\n"
    "v_readlane_b32 s46, v5, 6\n"
    "v_readlane_b32 s47, v5, 7\n"
    "v_readlane_b32 s20, v5, 8\n"
    "v_readlane_b32 s21, v5, 9\n"
    "v_readlane_b32 s16, v5, 10\n"
    "v_readlane_b32 s17, v5, 11\n"
    "v_readlane_b32 s18, v5, 12\n"
    "v_readlane_b32 s51, v5, 13\n"
    "v_readlane_b32 s24, v5, 14\n"
    "v_readlane_b32 s25, v5, 15\n"
    "v_readlane_b32 s53, v5, 16\n"
    "v_readlane_b32 s52, v5, 17\n"
    "v_readlane_b32 s55, v5, 18\n"
    "v_mov_b32 v5, 1\n"
    "s_mov_b32 s19, 0x20000\n"
    "s_mov_b32 s22, 0x400000\n"
    "s_mov_b32 s23, 0x20000\n"
    "s_lshl_b32 s39, s36, 15\n"
    "s_lshl_b32 s49, s36, 11\n"
    "s_mov_b32 s40, 0\n"
    "s_add_u32 m0, s39, 0\n"
    "s_mov_b32 s38, 0\n"
    "buffer_load_dwordx4 v3, s[20:23], s38 offen lds\n"
    "s_add_u32 m0, s39, 1024\n"
    "s_mov_b32 s38, 4096\n"
    "buffer_load_dwordx4 v3, s[20:23], s38 offen lds\n"
    "s_add_u32 m0, s39, 2048\n"
    "s_mov_b32 s38, 8192\n"
    "buffer_load_dwordx4 v3, s[20:23], s38 offen lds\n"
    "s_add_u32 m0, s39, 3072\n"
    "s_mov_b32 s38, 12288\n"
    "buffer_load_dwordx4 v3, s[20:23], s38 offen lds\n"
    "s_add_u32 m0, s39, 4096\n"
    "s_mov_b32 s38, 16384\n"
    "buffer_load_dwordx4 v3, s[20:23], s38 offen lds\n"
    "s_add_u32 m0, s39, 5120\n"
    "s_mov_b32 s38, 20480\n"
    "buffer_load_dwordx4 v3, s[20:23], s38 offen lds\n"
    "s_add_u32 m0, s39, 6144\n"
    "s_mov_b32 s38, 24576\n"
    "buffer_load_dwordx4 v3, s[20:23], s38 offen lds\n"
    "s_add_u32 m0, s39, 7168\n"
    "s_mov_b32 s38, 28672\n"
    "buffer_load_dwordx4 v3, s[20:23], s38 offen lds\n"
    "s_add_u32 m0, s39, 8192\n"
    "s_mov_b32 s38, 32768\n"
    "buffer_load_dwordx4 v3, s[20:23], s38 offen lds\n"
    "s_add_u32 m0, s39, 9216\n"
    "s_mov_b32 s38, 36864\n"
    "buffer_load_dwordx4 v3, s[20:23], s38 offen lds\n"
    "s_add_u32 m0, s39, 10240\n"
    "s_mov_b32 s38, 40960\n"
    "buffer_load_dwordx4 v3, s[20:23], s38 offen lds\n"
    "s_add_u32 m0, s39, 11264\n"
    "s_mov_b32 s38, 45056\n"
    "buffer_load_dwordx4 v3, s[20:23], s38 offen lds\n"
    "s_add_u32 m0, s39, 12288\n"
    "s_mov_b32 s38, 49152\n"
    "buffer_load_dwordx4 v3, s[20:23], s38 offen lds\n"
    "s_add_u32 m0, s39, 13312\n"
    "s_mov_b32 s38, 53248\n"
    "buffer_load_dwordx4 v3, s[20:23], s38 offen lds\n"
    "s_add_u32 m0, s39, 14336\n"
    "s_mov_b32 s38, 57344\n"
    "buffer_load_dwordx4 v3, s[20:23], s38 offen lds\n"
    "s_add_u32 m0, s39, 15360\n"
    "s_mov_b32 s38, 61440\n"
    "buffer_load_dwordx4 v3, s[20:23], s38 offen lds\n"
    "s_add_u32 m0, s39, 16384\n"
    "s_mov_b32 s38, 65536\n"
    "buffer_load_dwordx4 v3, s[20:23], s38 offen lds\n"
    "s_add_u32 m0, s39, 17408\n"
    "s_mov_b32 s38, 69632\n"
    "buffer_load_dwordx4 v3, s[20:23], s38 offen lds\n"
    "s_add_u32 m0, s39, 18432\n"
    "s_mov_b32 s38, 73728\n"
    "buffer_load_dwordx4 v3, s[20:23], s38 offen lds\n"
    "s_add_u32 m0, s39, 19456\n"
    "s_mov_b32 s38, 77824\n"
    "buffer_load_dwordx4 v3, s[20:23], s38 offen lds\n"
    "s_add_u32 m0, s39, 20480\n"
    "s_mov_b32 s38, 81920\n"
    "buffer_load_dwordx4 v3, s[20:23], s38 offen lds\n"
    "s_add_u32 m0, s39, 21504\n"
    "s_mov_b32 s38, 86016\n"
    "buffer_load_dwordx4 v3, s[20:23], s38 offen lds\n"
    "s_add_u32 m0, s39, 22528\n"
    "s_mov_b32 s38, 90112\n"
    "buffer_load_dwordx4 v3, s[20:23], s38 offen lds\n"
    "s_add_u32 m0, s39, 23552\n"
    "s_mov_b32 s38, 94208\n"
    "buffer_load_dwordx4 v3, s[20:23], s38 offen lds\n"
    "s_add_u32 m0, s39, 24576\n"
    "s_mov_b32 s38, 98304\n"
    "buffer_load_dwordx4 v3, s[20:23], s38 offen lds\n"
    "s_add_u32 m0, s39, 25600\n"
    "s_mov_b32 s38, 102400\n"
    "buffer_load_dwordx4 v3, s[20:23], s38 offen lds\n"
    "s_add_u32 m0, s39, 26624\n"
    "s_mov_b32 s38, 106496\n"
    "buffer_load_dwordx4 v3, s[20:23], s38 offen lds\n"
    "s_add_u32 m0, s39, 27648\n"
    "s_mov_b32 s38, 110592\n"
    "buffer_load_dwordx4 v3, s[20:23], s38 offen lds\n"
    "s_add_u32 m0, s39, 28672\n"
    "s_mov_b32 s38, 114688\n"
    "buffer_load_dwordx4 v3, s[20:23], s38 offen lds\n"
    "s_add_u32 m0, s39, 29696\n"
    "s_mov_b32 s38, 118784\n"
    "buffer_load_dwordx4 v3, s[20:23], s38 offen lds\n"
    "s_add_u32 m0, s39, 30720\n"
    "s_mov_b32 s38, 122880\n"
    "buffer_load_dwordx4 v3, s[20:23], s38 offen lds\n"
    "s_add_u32 m0, s39, 31744\n"
    "s_mov_b32 s38, 126976\n"
    "buffer_load_dwordx4 v3, s[20:23], s38 offen lds\n"
    "s_waitcnt vmcnt(0)\n"
    "s_barrier\n"
    "v_add_u32 v7, 0, v4\n"
    "ds_read_b64_tr_b16 a[0:1], v7 offset:0\n"
    "ds_read_b64_tr_b16 a[2:3], v7 offset:4096\n"
    "ds_read_b64_tr_b16 a[4:5], v7 offset:2048\n"
    "ds_read_b64_tr_b16 a[6:7], v7 offset:6144\n"
    "ds_read_b64_tr_b16 a[8:9], v7 offset:8192\n"
    "ds_read_b64_tr_b16 a[10:11], v7 offset:12288\n"
    "ds_read_b64_tr_b16 a[12:13], v7 offset:10240\n"
    "ds_read_b64_tr_b16 a[14:15], v7 offset:14336\n"
    "ds_read_b64_tr_b16 a[16:17], v7 offset:16384\n"
    "ds_read_b64_tr_b16 a[18:19], v7 offset:20480\n"
    "ds_read_b64_tr_b16 a[20:21], v7 offset:18432\n"
    "ds_read_b64_tr_b16 a[22:23], v7 offset:22528\n"
    "ds_read_b64_tr_b16 a[24:25], v7 offset:24576\n"
    "ds_read_b64_tr_b16 a[26:27], v7 offset:28672\n"
    "ds_read_b64_tr_b16 a[28:29], v7 offset:26624\n"
    "s_waitcnt lgkmcnt(11)\n"
    "ds_read_b64_tr_b16 a[30:31], v7 offset:30720\n"
    "ds_read_b64_tr_b16 a[32:33], v7 offset:32768\n"
    "ds_read_b64_tr_b16 a[34:35], v7 offset:36864\n"
    "ds_read_b64_tr_b16 a[36:37], v7 offset:34816\n"
    "s_waitcnt lgkmcnt(11)\n"
    "ds_read_b64_tr_b16 a[38:39], v7 offset:38912\n"
    "ds_read_b64_tr_b16 a[40:41], v7 offset:40960\n"
    "ds_read_b64_tr_b16 a[42:43], v7 offset:45056\n"
    "ds_read_b64_tr_b16 a[44:45], v7 offset:43008\n"
    "s_waitcnt lgkmcnt(11)\n"
    "ds_read_b64_tr_b16 a[46:47], v7 offset:47104\n"
    "ds_read_b64_tr_b16 a[48:49], v7 offset:49152\n"
    "ds_read_b64_tr_b16 a[50:51], v7 offset:53248\n"
    "ds_read_b64_tr_b16 a[52:53], v7 offset:51200\n"
    "s_waitcnt lgkmcnt(11)\n"
    "ds_read_b64_tr_b16 a[54:55], v7 offset:55296\n"
    "ds_read_b64_tr_b16 a[56:57], v7 offset:57344\n"
    "ds_read_b64_tr_b16 a[58:59], v7 offset:61440\n"
    "ds_read_b64_tr_b16 a[60:61], v7 offset:59392\n"
    "s_waitcnt lgkmcnt(11)\n"
    "ds_read_b64_tr_b16 a[62:63], v7 offset:63488\n"
    "s_waitcnt lgkmcnt(0)\n"
    "v_add_u32 v7, 65536, v4\n"
    "ds_read_b64_tr_b16 a[64:65], v7 offset:0\n"
    "ds_read_b64_tr_b16 a[66:67], v7 offset:4096\n"
    "ds_read_b64_tr_b16 a[68:69], v7 offset:2048\n"
    "ds_read_b64_tr_b16 a[70:71], v7 offset:6144\n"
    "ds_read_b64_tr_b16 a[72:73], v7 offset:8192\n"
    "ds_read_b64_tr_b16 a[74:75], v7 offset:12288\n"
    "ds_read_b64_tr_b16 a[76:77], v7 offset:10240\n"
    "ds_read_b64_tr_b16 a[78:79], v7 offset:14336\n"
    "ds_read_b64_tr_b16 a[80:81], v7 offset:16384\n"
    "ds_read_b64_tr_b16 a[82:83], v7 offset:20480\n"
    "ds_read_b64_tr_b16 a[84:85], v7 offset:18432\n"
    "ds_read_b64_tr_b16 a[86:87], v7 offset:22528\n"
    "ds_read_b64_tr_b16 a[88:89], v7 offset:24576\n"
    "ds_read_b64_tr_b16 a[90:91], v7 offset:28672\n"
    "ds_read_b64_tr_b16 a[92:93], v7 offset:26624\n"
    "s_waitcnt lgkmcnt(11)\n"
    "ds_read_b64_tr_b16 a[94:95], v7 offset:30720\n"
    "ds_read_b64_tr_b16 a[96:97], v7 offset:32768\n"
    "ds_read_b64_tr_b16 a[98:99], v7 offset:36864\n"
    "ds_read_b64_tr_b16 a[100:101], v7 offset:34816\n"
    "s_waitcnt lgkmcnt(11)\n"
    "ds_read_b64_tr_b16 a[102:103], v7 offset:38912\n"
    "ds_read_b64_tr_b16 a[104:105], v7 offset:40960\n"
    "ds_read_b64_tr_b16 a[106:107], v7 offset:45056\n"
    "ds_read_b64_tr_b16 a[108:109], v7 offset:43008\n"
    "s_waitcnt lgkmcnt(11)\n"
    "ds_read_b64_tr_b16 a[110:111], v7 offset:47104\n"
    "ds_read_b64_tr_b16 a[112:113], v7 offset:49152\n"
    "ds_read_b64_tr_b16 a[114:115], v7 offset:53248\n"
    "ds_read_b64_tr_b16 a[116:117], v7 offset:51200\n"
    "s_waitcnt lgkmcnt(11)\n"
    "ds_read_b64_tr_b16 a[118:119], v7 offset:55296\n"
    "ds_read_b64_tr_b16 a[120:121], v7 offset:57344\n"
    "ds_read_b64_tr_b16 a[122:123], v7 offset:61440\n"
    "ds_read_b64_tr_b16 a[124:125], v7 offset:59392\n"
    "s_waitcnt lgkmcnt(11)\n"
    "ds_read_b64_tr_b16 a[126:127], v7 offset:63488\n"
    "s_waitcnt lgkmcnt(0)\n"
    "s_barrier\n"
    "s_add_u32 m0, s39, 0\n"
    "s_mov_b32 s38, 524288\n"
    "buffer_load_dwordx4 v3, s[20:23], s38 offen lds\n"
    "s_add_u32 m0, s39, 1024\n"
    "s_mov_b32 s38, 528384\n"
    "buffer_load_dwordx4 v3, s[20:23], s38 offen lds\n"
    "s_add_u32 m0, s39, 2048\n"
    "s_mov_b32 s38, 532480\n"
    "buffer_load_dwordx4 v3, s[20:23], s38 offen lds\n"
    "s_add_u32 m0, s39, 3072\n"
    "s_mov_b32 s38, 536576\n"
    "buffer_load_dwordx4 v3, s[20:23], s38 offen lds\n"
    "s_add_u32 m0, s39, 4096\n"
    "s_mov_b32 s38, 540672\n"
    "buffer_load_dwordx4 v3, s[20:23], s38 offen lds\n"
    "s_add_u32 m0, s39, 5120\n"
    "s_mov_b32 s38, 544768\n"
    "buffer_load_dwordx4 v3, s[20:23], s38 offen lds\n"
    "s_add_u32 m0, s39, 6144\n"
    "s_mov_b32 s38, 548864\n"
    "buffer_load_dwordx4 v3, s[20:23], s38 offen lds\n"
    "s_add_u32 m0, s39, 7168\n"
    "s_mov_b32 s38, 552960\n"
    "buffer_load_dwordx4 v3, s[20:23], s38 offen lds\n"
    "s_add_u32 m0, s39, 8192\n"
    "s_mov_b32 s38, 557056\n"
    "buffer_load_dwordx4 v3, s[20:23], s38 offen lds\n"
    "s_add_u32 m0, s39, 9216\n"
    "s_mov_b32 s38, 561152\n"
    "buffer_load_dwordx4 v3, s[20:23], s38 offen lds\n"
    "s_add_u32 m0, s39, 10240\n"
    "s_mov_b32 s38, 565248\n"
    "buffer_load_dwordx4 v3, s[20:23], s38 offen lds\n"
    "s_add_u32 m0, s39, 11264\n"
    "s_mov_b32 s38, 569344\n"
    "buffer_load_dwordx4 v3, s[20:23], s38 offen lds\n"
    "s_add_u32 m0, s39, 12288\n"
    "s_mov_b32 s38, 573440\n"
    "buffer_load_dwordx4 v3, s[20:23], s38 offen lds\n"
    "s_add_u32 m0, s39, 13312\n"
    "s_mov_b32 s38, 577536\n"
    "buffer_load_dwordx4 v3, s[20:23], s38 offen lds\n"
    "s_add_u32 m0, s39, 14336\n"
    "s_mov_b32 s38, 581632\n"
    "buffer_load_dwordx4 v3, s[20:23], s38 offen lds\n"
    "s_add_u32 m0, s39, 15360\n"
    "s_mov_b32 s38, 585728\n"
    "buffer_load_dwordx4 v3, s[20:23], s38 offen lds\n"
    "s_add_u32 m0, s39, 16384\n"
    "s_mov_b32 s38, 589824\n"
    "buffer_load_dwordx4 v3, s[20:23], s38 offen lds\n"
    "s_add_u32 m0, s39, 17408\n"
    "s_mov_b32 s38, 593920\n"
    "buffer_load_dwordx4 v3, s[20:23], s38 offen lds\n"
    "s_add_u32 m0, s39, 18432\n"
    "s_mov_b32 s38, 598016\n"
    "buffer_load_dwordx4 v3, s[20:23], s38 offen lds\n"
    "s_add_u32 m0, s39, 19456\n"
    "s_mov_b32 s38, 602112\n"
    "buffer_load_dwordx4 v3, s[20:23], s38 offen lds\n"
    "s_add_u32 m0, s39, 20480\n"
    "s_mov_b32 s38, 606208\n"
    "buffer_load_dwordx4 v3, s[20:23], s38 offen lds\n"
    "s_add_u32 m0, s39, 21504\n"
    "s_mov_b32 s38, 610304\n"
    "buffer_load_dwordx4 v3, s[20:23], s38 offen lds\n"
    "s_add_u32 m0, s39, 22528\n"
    "s_mov_b32 s38, 614400\n"
    "buffer_load_dwordx4 v3, s[20:23], s38 offen lds\n"
    "s_add_u32 m0, s39, 23552\n"
    "s_mov_b32 s38, 618496\n"
    "buffer_load_dwordx4 v3, s[20:23], s38 offen lds\n"
    "s_add_u32 m0, s39, 24576\n"
    "s_mov_b32 s38, 622592\n"
    "buffer_load_dwordx4 v3, s[20:23], s38 offen lds\n"
    "s_add_u32 m0, s39, 25600\n"
    "s_mov_b32 s38, 626688\n"
    "buffer_load_dwordx4 v3, s[20:23], s38 offen lds\n"
    "s_add_u32 m0, s39, 26624\n"
    "s_mov_b32 s38, 630784\n"
    "buffer_load_dwordx4 v3, s[20:23], s38 offen lds\n"
    "s_add_u32 m0, s39, 27648\n"
    "s_mov_b32 s38, 634880\n"
    "buffer_load_dwordx4 v3, s[20:23], s38 offen lds\n"
    "s_add_u32 m0, s39, 28672\n"
    "s_mov_b32 s38, 638976\n"
    "buffer_load_dwordx4 v3, s[20:23], s38 offen lds\n"
    "s_add_u32 m0, s39, 29696\n"
    "s_mov_b32 s38, 643072\n"
    "buffer_load_dwordx4 v3, s[20:23], s38 offen lds\n"
    "s_add_u32 m0, s39, 30720\n"
    "s_mov_b32 s38, 647168\n"
    "buffer_load_dwordx4 v3, s[20:23], s38 offen lds\n"
    "s_add_u32 m0, s39, 31744\n"
    "s_mov_b32 s38, 651264\n"
    "buffer_load_dwordx4 v3, s[20:23], s38 offen lds\n"
    "s_waitcnt vmcnt(0)\n"
    "s_barrier\n"
    "v_add_u32 v7, 0, v4\n"
    "ds_read_b64_tr_b16 a[128:129], v7 offset:0\n"
    "ds_read_b64_tr_b16 a[130:131], v7 offset:4096\n"
    "ds_read_b64_tr_b16 a[132:133], v7 offset:2048\n"
    "ds_read_b64_tr_b16 a[134:135], v7 offset:6144\n"
    "ds_read_b64_tr_b16 a[136:137], v7 offset:8192\n"
    "ds_read_b64_tr_b16 a[138:139], v7 offset:12288\n"
    "ds_read_b64_tr_b16 a[140:141], v7 offset:10240\n"
    "ds_read_b64_tr_b16 a[142:143], v7 offset:14336\n"
    "ds_read_b64_tr_b16 a[144:145], v7 offset:16384\n"
    "ds_read_b64_tr_b16 a[146:147], v7 offset:20480\n"
    "ds_read_b64_tr_b16 a[148:149], v7 offset:18432\n"
    "ds_read_b64_tr_b16 a[150:151], v7 offset:22528\n"
    "ds_read_b64_tr_b16 a[152:153], v7 offset:24576\n"
    "ds_read_b64_tr_b16 a[154:155], v7 offset:28672\n"
    "ds_read_b64_tr_b16 a[156:157], v7 offset:26624\n"
    "s_waitcnt lgkmcnt(11)\n"
    "ds_read_b64_tr_b16 a[158:159], v7 offset:30720\n"
    "ds_read_b64_tr_b16 a[160:161], v7 offset:32768\n"
    "ds_read_b64_tr_b16 a[162:163], v7 offset:36864\n"
    "ds_read_b64_tr_b16 a[164:165], v7 offset:34816\n"
    "s_waitcnt lgkmcnt(11)\n"
    "ds_read_b64_tr_b16 a[166:167], v7 offset:38912\n"
    "ds_read_b64_tr_b16 a[168:169], v7 offset:40960\n"
    "ds_read_b64_tr_b16 a[170:171], v7 offset:45056\n"
    "ds_read_b64_tr_b16 a[172:173], v7 offset:43008\n"
    "s_waitcnt lgkmcnt(11)\n"
    "ds_read_b64_tr_b16 a[174:175], v7 offset:47104\n"
    "ds_read_b64_tr_b16 a[176:177], v7 offset:49152\n"
    "ds_read_b64_tr_b16 a[178:179], v7 offset:53248\n"
    "ds_read_b64_tr_b16 a[180:181], v7 offset:51200\n"
    "s_waitcnt lgkmcnt(11)\n"
    "ds_read_b64_tr_b16 a[182:183], v7 offset:55296\n"
    "ds_read_b64_tr_b16 a[184:185], v7 offset:57344\n"
    "ds_read_b64_tr_b16 a[186:187], v7 offset:61440\n"
    "ds_read_b64_tr_b16 a[188:189], v7 offset:59392\n"
    "s_waitcnt lgkmcnt(11)\n"
    "ds_read_b64_tr_b16 a[190:191], v7 offset:63488\n"
    "s_waitcnt lgkmcnt(0)\n"
    "v_add_u32 v7, 65536, v4\n"
    "ds_read_b64_tr_b16 a[192:193], v7 offset:0\n"
    "ds_read_b64_tr_b16 a[194:195], v7 offset:4096\n"
    "ds_read_b64_tr_b16 a[196:197], v7 offset:2048\n"
    "ds_read_b64_tr_b16 a[198:199], v7 offset:6144\n"
    "ds_read_b64_tr_b16 a[200:201], v7 offset:8192\n"
    "ds_read_b64_tr_b16 a[202:203], v7 offset:12288\n"
    "ds_read_b64_tr_b16 a[204:205], v7 offset:10240\n"
    "ds_read_b64_tr_b16 a[206:207], v7 offset:14336\n"
    "ds_read_b64_tr_b16 a[208:209], v7 offset:16384\n"
    "ds_read_b64_tr_b16 a[210:211], v7 offset:20480\n"
    "ds_read_b64_tr_b16 a[212:213], v7 offset:18432\n"
    "ds_read_b64_tr_b16 a[214:215], v7 offset:22528\n"
    "ds_read_b64_tr_b16 a[216:217], v7 offset:24576\n"
    "ds_read_b64_tr_b16 a[218:219], v7 offset:28672\n"
    "ds_read_b64_tr_b16 a[220:221], v7 offset:26624\n"
    "s_waitcnt lgkmcnt(11)\n"
    "ds_read_b64_tr_b16 a[222:223], v7 offset:30720\n"
    "ds_read_b64_tr_b16 a[224:225], v7 offset:32768\n"
    "ds_read_b64_tr_b16 a[226:227], v7 offset:36864\n"
    "ds_read_b64_tr_b16 a[228:229], v7 offset:34816\n"
    "s_waitcnt lgkmcnt(11)\n"
    "ds_read_b64_tr_b16 a[230:231], v7 offset:38912\n"
    "ds_read_b64_tr_b16 a[232:233], v7 offset:40960\n"
    "ds_read_b64_tr_b16 a[234:235], v7 offset:45056\n"
    "ds_read_b64_tr_b16 a[236:237], v7 offset:43008\n"
    "s_waitcnt lgkmcnt(11)\n"
    "ds_read_b64_tr_b16 a[238:239], v7 offset:47104\n"
    "ds_read_b64_tr_b16 a[240:241], v7 offset:49152\n"
    "ds_read_b64_tr_b16 a[242:243], v7 offset:53248\n"
    "ds_read_b64_tr_b16 a[244:245], v7 offset:51200\n"
    "s_waitcnt lgkmcnt(11)\n"
    "ds_read_b64_tr_b16 a[246:247], v7 offset:55296\n"
    "ds_read_b64_tr_b16 a[248:249], v7 offset:57344\n"
    "ds_read_b64_tr_b16 a[250:251], v7 offset:61440\n"
    "ds_read_b64_tr_b16 a[252:253], v7 offset:59392\n"
    "s_waitcnt lgkmcnt(11)\n"
    "ds_read_b64_tr_b16 a[254:255], v7 offset:63488\n"
    "s_waitcnt lgkmcnt(0)\n"
    "s_barrier\n"
    "v_mov_b32 v7, 0\n"
    "s_mov_b32 s37, 0x80000\n"
    "LRY%=:\n"
    "global_load_dword v6, v7, s[24:25] sc1\n"
    "s_waitcnt vmcnt(0)\n"
    "v_readfirstlane_b32 s38, v6\n"
    "s_cmp_lg_u32 s38, 0\n"
    "s_cbranch_scc1 LRZ%=\n"
    "s_sleep 8\n"
    "s_sub_u32 s37, s37, 1\n"
    "s_cbranch_scc0 LRY%=\n"
    "s_endpgm\n"
    "LRZ%=:\n"
    "s_lshl_b32 s38, s38, 13\n"
    "s_and_b32 s38, s38, 0x2000\n"
    "s_xor_b32 s42, s42, s38\n"
    "s_xor_b32 s43, s43, s38\n"
    "s_xor_b32 s44, s44, s38\n"
    "s_xor_b32 s45, s45, s38\n"
    "LDL%=:\n"
    "s_sub_u32 s53, s53, 1\n"
    "s_cbranch_scc1 LDM%=\n"
    "s_sleep 37\n"
    "s_branch LDL%=\n"
    "LDM%=:\n"
    "v_mov_b32 v80, -1\n"
    "v_mov_b32 v81, -1\n"
    "v_mov_b32 v82, -1\n"
    "v_mov_b32 v83, -1\n"
    "s_mov_b32 s54, s46\n"
    "s_mov_b32 s37, s55\n"
    "LF%=:\n"
    "s_lshr_b32 s38, s54, 8\n"
    "s_xor_b32 s38, s38, s43\n"
    "s_and_b32 s38, s38, 0x2000\n"
    "s_or_b32 s50, s54, s38\n"
    "buffer_store_dwordx4 v[80:83], v0, s[16:19], s50 offen offset:0 sc1\n"
    "buffer_store_dwordx4 v[80:83], v0, s[16:19], s50 offen offset:1024 sc1\n"
    "s_waitcnt vmcnt(24)\n"
    "s_add_u32 s54, s54, 262144\n"
    "s_sub_u32 s37, s37, 1\n"
    "s_cbranch_scc0 LF%=\n"
    "s_waitcnt vmcnt(0)\n"
    "s_barrier\n"
    "buffer_store_dword v5, off, s[16:19], s45 sc1\n"
    "s_add_u32 s45, s45, s52\n"
    "v_lshlrev_b32 v7, 4, v0\n"
    "v_and_b32 v7, 0x700, v7\n"
    "v_mov_b32 v9, 139776\n"
    "v_lshl_add_u32 v10, s36, 2, v9\n"
    "s_lshr_b32 s38, s41, 8\n"
    "s_xor_b32 s38, s38, s42\n"
    "s_and_b32 s38, s38, 0x2000\n"
    "s_or_b32 s48, s41, s38\n"
    "LT%=:\n"
    "s_cmp_lg_u32 s36, 0\n"
    "s_cbranch_scc1 LB%=\n"
    "s_mov_b32 s37, 0x80000\n"
    "LP%=:\n"
    "buffer_load_dword v6, v7, s[16:19], s44 offen sc1\n"
    "s_waitcnt vmcnt(0)\n"
    "v_cmp_eq_u32 vcc, 0, v6\n"
    "s_cbranch_vccz LB%=\n"
    "s_sleep 1\n"
    "s_sub_u32 s37, s37, 1\n"
    "s_cbranch_scc0 LP%=\n"
    "buffer_store_dword v5, off, s[16:19], s47 sc1\n"
    "s_endpgm\n"
    "LB%=:\n"
    "s_barrier\n"
    "LC%=:\n"
    "s_add_u32 m0, s49, 0\n"
    "s_nop 0\n"
    "buffer_load_dwordx4 v0, s[16:19], s48 offen offset:0 sc1 lds\n"
    "buffer_load_dwordx4 v0, s[16:19], s48 offen offset:1024 sc1 lds\n"
    "s_add_u32 s48, s48, 16384\n"
    "s_add_u32 m0, s49, 8192\n"
    "s_nop 0\n"
    "buffer_load_dwordx4 v0, s[16:19], s48 offen offset:0 sc1 lds\n"
    "buffer_load_dwordx4 v0, s[16:19], s48 offen offset:1024 sc1 lds\n"
    "s_add_u32 s48, s48, 16384\n"
    "s_add_u32 m0, s49, 16384\n"
    "s_nop 0\n"
    "buffer_load_dwordx4 v0, s[16:19], s48 offen offset:0 sc1 lds\n"
    "buffer_load_dwordx4 v0, s[16:19], s48 offen offset:1024 sc1 lds\n"
    "s_add_u32 s48, s48, 16384\n"
    "LG%=:\n"
    "s_lshr_b32 s38, s46, 8\n"
    "s_xor_b32 s38, s38, s43\n"
    "s_and_b32 s38, s38, 0x2000\n"
    "s_or_b32 s50, s46, s38\n"
    "s_add_u32 s41, s41, 2621440\n"
    "s_add_u32 s46, s46, 262144\n"
    "s_add_u32 s44, s44, 4\n"
    "s_add_u32 s40, s40, 1\n"
    "s_waitcnt vmcnt(4)\n"
    "s_barrier\n"
    "ds_read_b128 v[20:23], v0 offset:0\n"
    "ds_read_b128 v[24:27], v0 offset:1024\n"
    "ds_read_b128 v[28:31], v0 offset:2048\n"
    "ds_read_b128 v[32:35], v0 offset:3072\n"
    "s_waitcnt lgkmcnt(3)\n"
    "v_mfma_f32_32x32x16_bf16 v[64:79], a[0:3], v[20:23], 0\n"
    "s_add_u32 m0, s49, 24576\n"
    "s_nop 0\n"
    "buffer_load_dwordx4 v0, s[16:19], s48 offen offset:0 sc1 lds\n"
    "buffer_load_dwordx4 v0, s[16:19], s48 offen offset:1024 sc1 lds\n"
    "s_add_u32 s48, s48, 16384\n"
    "ds_read_b128 v[36:39], v0 offset:4096\n"
    "s_waitcnt lgkmcnt(3)\n"
    "v_mfma_f32_32x32x16_bf16 v[64:79], a[4:7], v[24:27], v[64:79]\n"
    "ds_read_b128 v[40:43], v0 offset:5120\n"
    "s_waitcnt lgkmcnt(3)\n"
    "v_mfma_f32_32x32x16_bf16 v[64:79], a[8:11], v[28:31], v[64:79]\n"
    "ds_read_b128 v[44:47], v0 offset:6144\n"
    "s_waitcnt lgkmcnt(3)\n"
    "v_mfma_f32_32x32x16_bf16 v[64:79], a[12:15], v[32:35], v[64:79]\n"
    "ds_read_b128 v[48:51], v0 offset:7168\n"
    "s_waitcnt lgkmcnt(3)\n"
    "v_mfma_f32_32x32x16_bf16 v[64:79], a[16:19], v[36:39], v[64:79]\n"
    "s_waitcnt vmcnt(4)\n"
    "s_barrier\n"
    "ds_read_b128 v[20:23], v0 offset:8192\n"
    "s_waitcnt lgkmcnt(3)\n"
    "v_mfma_f32_32x32x16_bf16 v[64:79], a[20:23], v[40:43], v[64:79]\n"
    "ds_read_b128 v[24:27], v0 offset:9216\n"
    "s_waitcnt lgkmcnt(3)\n"
    "v_mfma_f32_32x32x16_bf16 v[64:79], a[24:27], v[44:47], v[64:79]\n"
    "s_add_u32 m0, s49, 32768\n"
    "s_nop 0\n"
    "buffer_load_dwordx4 v0, s[16:19], s48 offen offset:0 sc1 lds\n"
    "buffer_load_dwordx4 v0, s[16:19], s48 offen offset:1024 sc1 lds\n"
    "s_add_u32 s48, s48, 16384\n"
    "ds_read_b128 v[28:31], v0 offset:10240\n"
    "s_waitcnt lgkmcnt(3)\n"
    "v_mfma_f32_32x32x16_bf16 v[64:79], a[28:31], v[48:51], v[64:79]\n"
    "ds_read_b128 v[32:35], v0 offset:11264\n"
    "s_waitcnt lgkmcnt(3)\n"
    "v_mfma_f32_32x32x16_bf16 v[64:79], a[32:35], v[20:23], v[64:79]\n"
    "ds_read_b128 v[36:39], v0 offset:12288\n"
    "s_waitcnt lgkmcnt(3)\n"
    "v_mfma_f32_32x32x16_bf16 v[64:79], a[36:39], v[24:27], v[64:79]\n"
    "ds_read_b128 v[40:43], v0 offset:13312\n"
    "s_waitcnt lgkmcnt(3)\n"
    "v_mfma_f32_32x32x16_bf16 v[64:79], a[40:43], v[28:31], v[64:79]\n"
    "ds_read_b128 v[44:47], v0 offset:14336\n"
    "s_waitcnt lgkmcnt(3)\n"
    "v_mfma_f32_32x32x16_bf16 v[64:79], a[44:47], v[32:35], v[64:79]\n"
    "ds_read_b128 v[48:51], v0 offset:15360\n"
    "s_waitcnt lgkmcnt(3)\n"
    "v_mfma_f32_32x32x16_bf16 v[64:79], a[48:51], v[36:39], v[64:79]\n"
    "s_add_u32 m0, s49, 40960\n"
    "s_nop 0\n"
    "buffer_load_dwordx4 v0, s[16:19], s48 offen offset:0 sc1 lds\n"
    "buffer_load_dwordx4 v0, s[16:19], s48 offen offset:1024 sc1 lds\n"
    "s_add_u32 s48, s48, 16384\n"
    "s_waitcnt vmcnt(6)\n"
    "s_barrier\n"
    "ds_read_b128 v[20:23], v0 offset:16384\n"
    "s_waitcnt lgkmcnt(3)\n"
    "v_mfma_f32_32x32x16_bf16 v[64:79], a[52:55], v[40:43], v[64:79]\n"
    "ds_read_b128 v[24:27], v0 offset:17408\n"
    "s_waitcnt lgkmcnt(3)\n"
    "v_mfma_f32_32x32x16_bf16 v[64:79], a[56:59], v[44:47], v[64:79]\n"
    "ds_read_b128 v[28:31], v0 offset:18432\n"
    "s_waitcnt lgkmcnt(3)\n"
    "v_mfma_f32_32x32x16_bf16 v[64:79], a[60:63], v[48:51], v[64:79]\n"
    "ds_read_b128 v[32:35], v0 offset:19456\n"
    "s_waitcnt lgkmcnt(3)\n"
    "v_mfma_f32_32x32x16_bf16 v[64:79], a[64:67], v[20:23], v[64:79]\n"
    "ds_read_b128 v[36:39], v0 offset:20480\n"
    "s_waitcnt lgkmcnt(3)\n"
    "v_mfma_f32_32x32x16_bf16 v[64:79], a[68:71], v[24:27], v[64:79]\n"
    "ds_read_b128 v[40:43], v0 offset:21504\n"
    "s_waitcnt lgkmcnt(3)\n"
    "v_mfma_f32_32x32x16_bf16 v[64:79], a[72:75], v[28:31], v[64:79]\n"
    "s_add_u32 m0, s49, 49152\n"
    "s_nop 0\n"
    "buffer_load_dwordx4 v0, s[16:19], s48 offen offset:0 sc1 lds\n"
    "buffer_load_dwordx4 v0, s[16:19], s48 offen offset:1024 sc1 lds\n"
    "s_add_u32 s48, s48, 16384\n"
    "ds_read_b128 v[44:47], v0 offset:22528\n"
    "s_waitcnt lgkmcnt(3)\n"
    "v_mfma_f32_32x32x16_bf16 v[64:79], a[76:79], v[32:35], v[64:79]\n"
    "ds_read_b128 v[48:51], v0 offset:23552\n"
    "s_waitcnt lgkmcnt(3)\n"
    "v_mfma_f32_32x32x16_bf16 v[64:79], a[80:83], v[36:39], v[64:79]\n"
    "s_waitcnt vmcnt(6)\n"
    "s_barrier\n"
    "ds_read_b128 v[20:23], v0 offset:24576\n"
    "s_waitcnt lgkmcnt(3)\n"
    "v_mfma_f32_32x32x16_bf16 v[64:79], a[84:87], v[40:43], v[64:79]\n"
    "ds_read_b128 v[24:27], v0 offset:25600\n"
    "s_waitcnt lgkmcnt(3)\n"
    "v_mfma_f32_32x32x16_bf16 v[64:79], a[88:91], v[44:47], v[64:79]\n"
    "ds_read_b128 v[28:31], v0 offset:26624\n"
    "s_waitcnt lgkmcnt(3)\n"
    "v_mfma_f32_32x32x16_bf16 v[64:79], a[92:95], v[48:51], v[64:79]\n"
    "ds_read_b128 v[32:35], v0 offset:27648\n"
    "s_waitcnt lgkmcnt(3)\n"
    "v_mfma_f32_32x32x16_bf16 v[64:79], a[96:99], v[20:23], v[64:79]\n"
    "s_add_u32 m0, s49, 57344\n"
    "s_nop 0\n"
    "buffer_load_dwordx4 v0, s[16:19], s48 offen offset:0 sc1 lds\n"
    "buffer_load_dwordx4 v0, s[16:19], s48 offen offset:1024 sc1 lds\n"
    "s_add_u32 s48, s48, 16384\n"
    "s_lshr_b32 s38, s41, 8\n"
    "s_xor_b32 s38, s38, s42\n"
    "s_and_b32 s38, s38, 0x2000\n"
    "s_or_b32 s48, s41, s38\n"
    "ds_read_b128 v[36:39], v0 offset:28672\n"
    "s_waitcnt lgkmcnt(3)\n"
    "v_mfma_f32_32x32x16_bf16 v[64:79], a[100:103], v[24:27], v[64:79]\n"
    "ds_read_b128 v[40:43], v0 offset:29696\n"
    "s_waitcnt lgkmcnt(3)\n"
    "v_mfma_f32_32x32x16_bf16 v[64:79], a[104:107], v[28:31], v[64:79]\n"
    "ds_read_b128 v[44:47], v0 offset:30720\n"
    "s_waitcnt lgkmcnt(3)\n"
    "v_mfma_f32_32x32x16_bf16 v[64:79], a[108:111], v[32:35], v[64:79]\n"
    "ds_read_b128 v[48:51], v0 offset:31744\n"
    "s_waitcnt lgkmcnt(3)\n"
    "v_mfma_f32_32x32x16_bf16 v[64:79], a[112:115], v[36:39], v[64:79]\n"
    "s_add_u32 s55, s55, 1\n"
    "s_lshr_b32 s38, s54, 8\n"
    "s_xor_b32 s38, s38, s43\n"
    "s_and_b32 s38, s38, 0x2000\n"
    "s_or_b32 s39, s54, s38\n"
    "s_cmp_lt_u32 s55, 64\n"
    "s_cselect_b32 s39, s39, 0x80000000\n"
    "s_add_u32 s54, s54, 262144\n"
    "s_waitcnt vmcnt(6)\n"
    "s_barrier\n"
    "ds_read_b128 v[20:23], v0 offset:32768\n"
    "s_waitcnt lgkmcnt(3)\n"
    "v_mfma_f32_32x32x16_bf16 v[64:79], a[116:119], v[40:43], v[64:79]\n"
    "buffer_store_dwordx4 v[80:83], v0, s[16:19], s39 offen offset:0 sc1\n"
    "ds_read_b128 v[24:27], v0 offset:33792\n"
    "s_waitcnt lgkmcnt(3)\n"
    "v_mfma_f32_32x32x16_bf16 v[64:79], a[120:123], v[44:47], v[64:79]\n"
    "ds_read_b128 v[28:31], v0 offset:34816\n"
    "s_waitcnt lgkmcnt(3)\n"
    "v_mfma_f32_32x32x16_bf16 v[64:79], a[124:127], v[48:51], v[64:79]\n"
    "buffer_store_dwordx4 v[80:83], v0, s[16:19], s39 offen offset:1024 sc1\n"
    "ds_read_b128 v[32:35], v0 offset:35840\n"
    "s_waitcnt lgkmcnt(3)\n"
    "v_mfma_f32_32x32x16_bf16 v[64:79], a[128:131], v[20:23], v[64:79]\n"
    "ds_read_b128 v[36:39], v0 offset:36864\n"
    "s_waitcnt lgkmcnt(3)\n"
    "v_mfma_f32_32x32x16_bf16 v[64:79], a[132:135], v[24:27], v[64:79]\n"
    "ds_read_b128 v[40:43], v0 offset:37888\n"
    "s_waitcnt lgkmcnt(3)\n"
    "v_mfma_f32_32x32x16_bf16 v[64:79], a[136:139], v[28:31], v[64:79]\n"
    "buffer_load_dword v6, v7, s[16:19], s44 offen sc1\n"
    "ds_read_b128 v[44:47], v0 offset:38912\n"
    "s_waitcnt lgkmcnt(3)\n"
    "v_mfma_f32_32x32x16_bf16 v[64:79], a[140:143], v[32:35], v[64:79]\n"
    "ds_read_b128 v[48:51], v0 offset:39936\n"
    "s_waitcnt lgkmcnt(3)\n"
    "v_mfma_f32_32x32x16_bf16 v[64:79], a[144:147], v[36:39], v[64:79]\n"
    "s_waitcnt vmcnt(7)\n"
    "s_barrier\n"
    "ds_read_b128 v[20:23], v0 offset:40960\n"
    "s_waitcnt lgkmcnt(3)\n"
    "v_mfma_f32_32x32x16_bf16 v[64:79], a[148:151], v[40:43], v[64:79]\n"
    "ds_read_b128 v[24:27], v0 offset:41984\n"
    "s_waitcnt lgkmcnt(3)\n"
    "v_mfma_f32_32x32x16_bf16 v[64:79], a[152:155], v[44:47], v[64:79]\n"
    "ds_read_b128 v[28:31], v0 offset:43008\n"
    "s_waitcnt lgkmcnt(3)\n"
    "v_mfma_f32_32x32x16_bf16 v[64:79], a[156:159], v[48:51], v[64:79]\n"
    "ds_read_b128 v[32:35], v0 offset:44032\n"
    "s_waitcnt lgkmcnt(3)\n"
    "v_mfma_f32_32x32x16_bf16 v[64:79], a[160:163], v[20:23], v[64:79]\n"
    "ds_read_b128 v[36:39], v0 offset:45056\n"
    "s_waitcnt lgkmcnt(3)\n"
    "v_mfma_f32_32x32x16_bf16 v[64:79], a[164:167], v[24:27], v[64:79]\n"
    "ds_read_b128 v[40:43], v0 offset:46080\n"
    "s_waitcnt lgkmcnt(3)\n"
    "v_mfma_f32_32x32x16_bf16 v[64:79], a[168:171], v[28:31], v[64:79]\n"
    "ds_read_b128 v[44:47], v0 offset:47104\n"
    "s_waitcnt lgkmcnt(3)\n"
    "v_mfma_f32_32x32x16_bf16 v[64:79], a[172:175], v[32:35], v[64:79]\n"
    "ds_read_b128 v[48:51], v0 offset:48128\n"
    "s_waitcnt lgkmcnt(3)\n"
    "v_mfma_f32_32x32x16_bf16 v[64:79], a[176:179], v[36:39], v[64:79]\n"
    "s_waitcnt vmcnt(5)\n"
    "s_barrier\n"
    "ds_read_b128 v[20:23], v0 offset:49152\n"
    "s_waitcnt lgkmcnt(3)\n"
    "v_mfma_f32_32x32x16_bf16 v[64:79], a[180:183], v[40:43], v[64:79]\n"
    "ds_read_b128 v[24:27], v0 offset:50176\n"
    "s_waitcnt lgkmcnt(3)\n"
    "v_mfma_f32_32x32x16_bf16 v[64:79], a[184:187], v[44:47], v[64:79]\n"
    "ds_read_b128 v[28:31], v0 offset:51200\n"
    "s_waitcnt lgkmcnt(3)\n"
    "v_mfma_f32_32x32x16_bf16 v[64:79], a[188:191], v[48:51], v[64:79]\n"
    "s_waitcnt vmcnt(0)\n"
    "v_cmp_eq_u32 vcc, 0, v6\n"
    "s_cmp_eq_u32 s40, 64\n"
    "s_cselect_b32 vcc_hi, 1, 0\n"
    "s_or_b32 s38, vcc_lo, vcc_hi\n"
    "v_mov_b32 v6, s38\n"
    "ds_write_b32 v10, v6\n"
    "ds_read_b128 v[32:35], v0 offset:52224\n"
    "s_waitcnt lgkmcnt(4)\n"
    "v_mfma_f32_32x32x16_bf16 v[64:79], a[192:195], v[20:23], v[64:79]\n"
    "ds_read_b128 v[36:39], v0 offset:53248\n"
    "s_waitcnt lgkmcnt(4)\n"
    "v_mfma_f32_32x32x16_bf16 v[64:79], a[196:199], v[24:27], v[64:79]\n"
    "ds_read_b128 v[40:43], v0 offset:54272\n"
    "s_waitcnt lgkmcnt(4)\n"
    "v_mfma_f32_32x32x16_bf16 v[64:79], a[200:203], v[28:31], v[64:79]\n"
    "ds_read_b128 v[44:47], v0 offset:55296\n"
    "s_waitcnt lgkmcnt(3)\n"
    "v_mfma_f32_32x32x16_bf16 v[64:79], a[204:207], v[32:35], v[64:79]\n"
    "ds_read_b128 v[48:51], v0 offset:56320\n"
    "s_waitcnt lgkmcnt(3)\n"
    "v_mfma_f32_32x32x16_bf16 v[64:79], a[208:211], v[36:39], v[64:79]\n"
    "s_barrier\n"
    "ds_read_b128 v[20:23], v0 offset:57344\n"
    "ds_read_b32 v6, v9\n"
    "s_waitcnt lgkmcnt(4)\n"
    "v_mfma_f32_32x32x16_bf16 v[64:79], a[212:215], v[40:43], v[64:79]\n"
    "ds_read_b128 v[24:27], v0 offset:58368\n"
    "s_waitcnt lgkmcnt(4)\n"
    "v_mfma_f32_32x32x16_bf16 v[64:79], a[216:219], v[44:47], v[64:79]\n"
    "ds_read_b128 v[28:31], v0 offset:59392\n"
    "s_waitcnt lgkmcnt(4)\n"
    "v_mfma_f32_32x32x16_bf16 v[64:79], a[220:223], v[48:51], v[64:79]\n"
    "ds_read_b128 v[32:35], v0 offset:60416\n"
    "s_waitcnt lgkmcnt(3)\n"
    "v_readfirstlane_b32 s52, v6\n"
    "s_cmp_eq_u32 s52, 0\n"
    "s_cbranch_scc1 LH%=\n"
    "v_mfma_f32_32x32x16_bf16 v[64:79], a[224:227], v[20:23], v[64:79]\n"
    "ds_read_b128 v[36:39], v0 offset:61440\n"
    "s_waitcnt lgkmcnt(3)\n"
    "v_mfma_f32_32x32x16_bf16 v[64:79], a[228:231], v[24:27], v[64:79]\n"
    "ds_read_b128 v[40:43], v0 offset:62464\n"
    "s_waitcnt lgkmcnt(3)\n"
    "v_mfma_f32_32x32x16_bf16 v[64:79], a[232:235], v[28:31], v[64:79]\n"
    "ds_read_b128 v[44:47], v0 offset:63488\n"
    "s_waitcnt lgkmcnt(3)\n"
    "v_mfma_f32_32x32x16_bf16 v[64:79], a[236:239], v[32:35], v[64:79]\n"
    "ds_read_b128 v[48:51], v0 offset:64512\n"
    "s_waitcnt lgkmcnt(3)\n"
    "v_mfma_f32_32x32x16_bf16 v[64:79], a[240:243], v[36:39], v[64:79]\n"
    "s_waitcnt lgkmcnt(2)\n"
    "v_mfma_f32_32x32x16_bf16 v[64:79], a[244:247], v[40:43], v[64:79]\n"
    "s_waitcnt lgkmcnt(1)\n"
    "v_mfma_f32_32x32x16_bf16 v[64:79], a[248:251], v[44:47], v[64:79]\n"
    "s_waitcnt lgkmcnt(0)\n"
    "v_mfma_f32_32x32x16_bf16 v[64:79], a[252:255], v[48:51], v[64:79]\n"
    "s_branch LI%=\n"
    "LH%=:\n"
    "v_mfma_f32_32x32x16_bf16 v[64:79], a[224:227], v[20:23], v[64:79]\n"
    "ds_read_b128 v[36:39], v0 offset:61440\n"
    "s_add_u32 m0, s49, 0\n"
    "s_nop 0\n"
    "buffer_load_dwordx4 v0, s[16:19], s48 offen offset:0 sc1 lds\n"
    "buffer_load_dwordx4 v0, s[16:19], s48 offen offset:1024 sc1 lds\n"
    "s_add_u32 s48, s48, 16384\n"
    "s_waitcnt lgkmcnt(3)\n"
    "v_mfma_f32_32x32x16_bf16 v[64:79], a[228:231], v[24:27], v[64:79]\n"
    "ds_read_b128 v[40:43], v0 offset:62464\n"
    "s_add_u32 m0, s49, 8192\n"
    "s_nop 0\n"
    "buffer_load_dwordx4 v0, s[16:19], s48 offen offset:0 sc1 lds\n"
    "buffer_load_dwordx4 v0, s[16:19], s48 offen offset:1024 sc1 lds\n"
    "s_add_u32 s48, s48, 16384\n"
    "s_waitcnt lgkmcnt(3)\n"
    "v_mfma_f32_32x32x16_bf16 v[64:79], a[232:235], v[28:31], v[64:79]\n"
    "ds_read_b128 v[44:47], v0 offset:63488\n"
    "s_add_u32 m0, s49, 16384\n"
    "s_nop 0\n"
    "buffer_load_dwordx4 v0, s[16:19], s48 offen offset:0 sc1 lds\n"
    "buffer_load_dwordx4 v0, s[16:19], s48 offen offset:1024 sc1 lds\n"
    "s_add_u32 s48, s48, 16384\n"
    "s_waitcnt lgkmcnt(3)\n"
    "v_mfma_f32_32x32x16_bf16 v[64:79], a[236:239], v[32:35], v[64:79]\n"
    "ds_read_b128 v[48:51], v0 offset:64512\n"
    "s_waitcnt lgkmcnt(3)\n"
    "v_mfma_f32_32x32x16_bf16 v[64:79], a[240:243], v[36:39], v[64:79]\n"
    "s_waitcnt lgkmcnt(2)\n"
    "v_mfma_f32_32x32x16_bf16 v[64:79], a[244:247], v[40:43], v[64:79]\n"
    "s_waitcnt lgkmcnt(1)\n"
    "v_mfma_f32_32x32x16_bf16 v[64:79], a[248:251], v[44:47], v[64:79]\n"
    "s_waitcnt lgkmcnt(0)\n"
    "v_mfma_f32_32x32x16_bf16 v[64:79], a[252:255], v[48:51], v[64:79]\n"
    "LI%=:\n"
    "s_nop 7\n"
    "s_nop 3\n"
    "v_cvt_pk_bf16_f32 v12, v64, v65\n"
    "v_cvt_pk_bf16_f32 v13, v66, v67\n"
    "v_cvt_pk_bf16_f32 v14, v68, v69\n"
    "v_cvt_pk_bf16_f32 v15, v70, v71\n"
    "v_min_u32 v15, -2, v15\n"
    "ds_write_b128 v1, v[12:15] offset:0\n"
    "v_cvt_pk_bf16_f32 v16, v72, v73\n"
    "v_cvt_pk_bf16_f32 v17, v74, v75\n"
    "v_cvt_pk_bf16_f32 v18, v76, v77\n"
    "v_cvt_pk_bf16_f32 v19, v78, v79\n"
    "v_min_u32 v19, -2, v19\n"
    "ds_write_b128 v1, v[16:19] offset:32\n"
    "buffer_load_dword v6, v7, s[16:19], s44 offen sc1\n"
    "s_waitcnt lgkmcnt(0)\n"
    "s_barrier\n"
    "ds_read_b128 v[12:15], v2 offset:0\n"
    "ds_read_b128 v[16:19], v2 offset:1088\n"
    "s_waitcnt lgkmcnt(1)\n"
    "buffer_store_dwordx4 v[12:15], v0, s[16:19], s50 offen offset:0 sc1\n"
    "s_waitcnt lgkmcnt(0)\n"
    "buffer_store_dwordx4 v[16:19], v0, s[16:19], s50 offen offset:1024 sc1\n"
    "buffer_store_dword v5, off, s[16:19], s45 sc1\n"
    "s_add_u32 s45, s45, 4\n"
    "s_waitcnt vmcnt(3)\n"
    "s_cmp_eq_u32 s52, 0\n"
    "s_cbranch_scc1 LG%=\n"
    "s_cmp_eq_u32 s40, 64\n"
    "s_cbranch_scc1 LE%=\n"
    "s_cmp_lg_u32 s36, 0\n"
    "s_cbranch_scc1 LB%=\n"
    "v_cmp_eq_u32 vcc, 0, v6\n"
    "s_cbranch_vccz LB%=\n"
    "s_branch LT%=\n"
    "LE%=:\n"
    "s_endpgm\n"
    :
    : "{v0}"(hv0), "{v1}"(hv1), "{v2}"(hv2), "{v3}"(hv3), "{v4}"(hv4), "{v5}"(hv5)
    : "v255", "a255", "s101", "vcc", "scc", "memory");
  __builtin_unreachable();
}

typedef float f2 __attribute__((ext_vector_type(2)));
__device__ __forceinline__ unsigned cvt2(f2 v) { unsigned p; asm("v_cvt_pk_bf16_f32 %0, %1, %2" : "=v"(p) : "v"(v[0]), "v"(v[1])); return p; }
__device__ __forceinline__ f2 up2(unsigned p) { return (f2){__uint_as_float(p << 16), __uint_as_float(p & 0xffff0000u)}; }
__device__ __forceinline__ void row_sum2(float& x, float& y) {
  x = ror_add<8>(x); y = ror_add<8>(y); x = ror_add<4>(x); y = ror_add<4>(y); x = ror_add<2>(x); y = ror_add<2>(y); x = ror_add<1>(x); y = ror_add<1>(y);
  auto q = __builtin_amdgcn_permlane16_swap(__float_as_uint(x), __float_as_uint(y), false, false);
  float t = __uint_as_float(q[0]) + __uint_as_float(q[1]);
  auto s = __builtin_amdgcn_permlane32_swap(__float_as_uint(t), __float_as_uint(t), false, false);
  t = __uint_as_float(s[0]) + __uint_as_float(s[1]);
  x = __int_as_float(__builtin_amdgcn_readlane(__float_as_int(t), 0)); y = __int_as_float(__builtin_amdgcn_readlane(__float_as_int(t), 16));
}
__device__ __forceinline__ void ln8(const f2 (&x)[4], f2 (&y)[4]) {
  const float c0 = __int_as_float(__builtin_amdgcn_readfirstlane(__float_as_int(x[0][0])));
  const f2 d0 = x[0] - c0, d1 = x[1] - c0, d2 = x[2] - c0, d3 = x[3] - c0;
  const f2 s2 = (d0 + d1) + (d2 + d3), v2 = d3 * d3 + (d2 * d2 + (d1 * d1 + d0 * d0));
  float s = s2[0] + s2[1], vs = v2[0] + v2[1];
  row_sum2(s, vs);
  s *= 1.f / DM;
  const f2 mv = up2(cvt2((f2){c0 + s, fmaxf(__builtin_fmaf(-s, s, vs * (1.f / DM)), 0.f)}));
  const float rc = __builtin_amdgcn_rcpf(rbf(__builtin_amdgcn_sqrtf(rbf(mv[1] + 1e-5f))));
#pragma unroll
  for (int i = 0; i < 4; ++i) y[i] = (x[i] - mv[0]) * rc;
}
__device__ __forceinline__ v4u ln_row(v4u q) {
  f2 x[4], y[4];
#pragma unroll
  for (int i = 0; i < 4; ++i) x[i] = up2(q[i]);
  ln8(x, y);
  return (v4u){cvt2(y[0]), cvt2(y[1]), cvt2(y[2]), cvt2(y[3])};
}
__device__ __forceinline__ void dma16(rsrc_t r, char* lp, unsigned vo, unsigned so) { __builtin_amdgcn_raw_ptr_buffer_load_lds(r, (__attribute__((address_space(3))) void*)lp, 16, vo, so, 0, 0); }
#define PTR(p) (int)(size_t)(p), (int)((size_t)(p) >> 32) & 0xffff
template <bool L1>
__device__ __forceinline__ void l_role8(int w, int u, const unsigned* rdy, char* ws, const u16* x, const u16* w_r, const u16* wg, const u16* wu, const u16* wd, u16* out) {
  const int lane = lane_id(), c = lane & 15, g = lane >> 4, q = (lane >> 2) & 3, r = u * 4 + w, t0 = L1 ? (w < 2 ? 3 * w : 2 * w + 2) : w;
  const unsigned c1 = 0u, c2 = 0x2000u;
  const unsigned zd1 = nearof(Z_DEC, c1), zd2 = nearof(Z_DEC, c2), fp = nearof(Z_FP + (L1 ? 0 : 16384 + (g & 1) * 16384), c2);
  const int pv[22] = {w, r, (int)nearof(Z_R1, c2), (int)Z_ERR, PTR(ws), (int)ZWS_BYTES, PTR(x), PTR(out), PTR(wg), PTR(wu), PTR(wd), PTR(rdy), PTR(w_r), (int)(Z_DBG + MBY(3)) + ((L1 ? 0 : 32) + r) * 16};
  const int one = (c >> 2) != g ? 0 : ((c & 1) ? 0x3f800000 : 0x3f80), trow = 16 * (g >> 1) + 8 * (q >> 1) + 4 * (g & 1) + (q & 1);
  int hv0 = lane * 16, hv1 = g * 16384 + r * 256 + c * 16, hv2 = lane < 32 ? fp + 4096 + (L1 ? g * 4 : 0) : zd2 + (L1 ? 512 : 0), hv3 = g < (L1 ? 2 : 1) ? 4 : 0, hv4 = g == 1 ? 4 : 0, hv5 = g >= 2 ? 4 : 0;
  int hv6 = (c & 2) ? 0xEEEE : 0x4444, hv7 = w * 4096 + g * 32 + (c & 2) * 8, hv8 = w * 4096 + lane * 16;
  int hv9 = !(c & 1) && g < (L1 ? (w < 2 ? 3 : 2) : 1) ? (L1 ? u * 320 : 2560 + u * 128) + (t0 + g) * 32 + c * 2 : OOR, hv10 = (lane & 0x31) == 0 ? r * 32 + c * 2 : OOR;
  int hv11 = lane < 6 ? (lane < 3 ? zd1 + lane * 512 : zd2 + (lane - 3) * 512) : OOR, hv12 = (c & 2) ? 0 : one, hv13 = (c & 2) ? one : 0;
  int hv14 = trow * (L1 ? 320 : 128) + t0 * 32 + (lane & 3) * 8, hv15 = 0, hv21 = fp + c * 256;
#define VO(i) ((w * 16 + (i * 64 + lane) / 20) * (FF * 2) + u * 320 + ((i * 64 + lane) % 20) * 16)
  int hv16 = L1 ? VO(0) : (w * 16 + (lane >> 3)) * (FF * 2) + 2560 + u * 128 + (lane & 7) * 16, hv17 = L1 ? VO(1) : (w * 16 + (lane >> 3)) * (DM * 2) + u * 128 + (lane & 7) * 16;
  int hv18 = L1 ? VO(2) : r * 32 + c * 2, hv19 = VO(3), hv20 = VO(4);
#pragma unroll
  for (int i = 0; i < 22; ++i) hv15 = lane == i ? pv[i] : hv15;
  if constexpr (L1) {
  asm volatile(
    "s_nop 1\n"
    "v_readlane_b32 s36, v15, 0\n"
    "v_readlane_b32 s55, v15, 1\n"
    "v_readlane_b32 s58, v15, 2\n"
    "v_readlane_b32 s59, v15, 3\n"
    "v_readlane_b32 s16, v15, 4\n"
    "v_readlane_b32 s17, v15, 5\n"
    "v_readlane_b32 s18, v15, 6\n"
    "v_readlane_b32 s20, v15, 7\n"
    "v_readlane_b32 s21, v15, 8\n"
    "v_readlane_b32 s24, v15, 9\n"
    "v_readlane_b32 s25, v15, 10\n"
    "v_readlane_b32 s90, v15, 11\n"
    "v_readlane_b32 s91, v15, 12\n"
    "v_readlane_b32 s92, v15, 13\n"
    "v_readlane_b32 s93, v15, 14\n"
    "v_readlane_b32 s94, v15, 15\n"
    "v_readlane_b32 s95, v15, 16\n"
    "v_readlane_b32 s96, v15, 17\n"
    "v_readlane_b32 s97, v15, 18\n"
    "v_readlane_b32 s98, v15, 19\n"
    "v_readlane_b32 s99, v15, 20\n"
    "v_readlane_b32 s72, v15, 21\n"
    "s_mov_b32 s19, 0x20000\n"
    "s_mov_b32 s23, 0x20000\n"
    "s_mov_b32 s27, 0x20000\n"
    "s_mov_b32 s31, 0x20000\n"
    "s_mov_b32 s22, 0x200000\n"
    "s_mov_b32 s26, 0x200000\n"
    "s_mov_b32 s30, 1835008\n"
    "s_nop 3\n"
    "global_load_dwordx4 v[116:119], v0, s[98:99]\n"
    "s_mul_i32 s62, s36, 5120\n"
    "s_mov_b32 s28, s90\n"
    "s_mov_b32 s29, s91\n"
    "s_add_u32 m0, s62, 0\n"
    "s_mov_b32 s38, 0\n"
    "buffer_load_dwordx4 v16, s[28:31], s38 offen lds\n"
    "s_add_u32 m0, s62, 1024\n"
    "s_mov_b32 s38, 0\n"
    "buffer_load_dwordx4 v17, s[28:31], s38 offen lds\n"
    "s_add_u32 m0, s62, 2048\n"
    "s_mov_b32 s38, 0\n"
    "buffer_load_dwordx4 v18, s[28:31], s38 offen lds\n"
    "s_add_u32 m0, s62, 3072\n"
    "s_mov_b32 s38, 0\n"
    "buffer_load_dwordx4 v19, s[28:31], s38 offen lds\n"
    "s_add_u32 m0, s62, 4096\n"
    "s_mov_b32 s38, 0\n"
    "buffer_load_dwordx4 v20, s[28:31], s38 offen lds\n"
    "s_mov_b32 s28, s92\n"
    "s_mov_b32 s29, s93\n"
    "s_add_u32 m0, s62, 20480\n"
    "s_mov_b32 s38, 0\n"
    "buffer_load_dwordx4 v16, s[28:31], s38 offen lds\n"
    "s_add_u32 m0, s62, 21504\n"
    "s_mov_b32 s38, 0\n"
    "buffer_load_dwordx4 v17, s[28:31], s38 offen lds\n"
    "s_add_u32 m0, s62, 22528\n"
    "s_mov_b32 s38, 0\n"
    "buffer_load_dwordx4 v18, s[28:31], s38 offen lds\n"
    "s_add_u32 m0, s62, 23552\n"
    "s_mov_b32 s38, 0\n"
    "buffer_load_dwordx4 v19, s[28:31], s38 offen lds\n"
    "s_add_u32 m0, s62, 24576\n"
    "s_mov_b32 s38, 0\n"
    "buffer_load_dwordx4 v20, s[28:31], s38 offen lds\n"
    "s_mov_b32 s28, s90\n"
    "s_mov_b32 s29, s91\n"
    "s_add_u32 m0, s62, 40960\n"
    "s_mov_b32 s38, 229376\n"
    "buffer_load_dwordx4 v16, s[28:31], s38 offen lds\n"
    "s_add_u32 m0, s62, 41984\n"
    "s_mov_b32 s38, 229376\n"
    "buffer_load_dwordx4 v17, s[28:31], s38 offen lds\n"
    "s_add_u32 m0, s62, 43008\n"
    "s_mov_b32 s38, 229376\n"
    "buffer_load_dwordx4 v18, s[28:31], s38 offen lds\n"
    "s_add_u32 m0, s62, 44032\n"
    "s_mov_b32 s38, 229376\n"
    "buffer_load_dwordx4 v19, s[28:31], s38 offen lds\n"
    "s_add_u32 m0, s62, 45056\n"
    "s_mov_b32 s38, 229376\n"
    "buffer_load_dwordx4 v20, s[28:31], s38 offen lds\n"
    "s_mov_b32 s28, s92\n"
    "s_mov_b32 s29, s93\n"
    "s_add_u32 m0, s62, 61440\n"
    "s_mov_b32 s38, 229376\n"
    "buffer_load_dwordx4 v16, s[28:31], s38 offen lds\n"
    "s_add_u32 m0, s62, 62464\n"
    "s_mov_b32 s38, 229376\n"
    "buffer_load_dwordx4 v17, s[28:31], s38 offen lds\n"
    "s_add_u32 m0, s62, 63488\n"
    "s_mov_b32 s38, 229376\n"
    "buffer_load_dwordx4 v18, s[28:31], s38 offen lds\n"
    "s_add_u32 m0, s62, 64512\n"
    "s_mov_b32 s38, 229376\n"
    "buffer_load_dwordx4 v19, s[28:31], s38 offen lds\n"
    "s_add_u32 m0, s62, 65536\n"
    "s_mov_b32 s38, 229376\n"
    "buffer_load_dwordx4 v20, s[28:31], s38 offen lds\n"
    "s_mov_b32 s28, s90\n"
    "s_mov_b32 s29, s91\n"
    "s_add_u32 m0, s62, 81920\n"
    "s_mov_b32 s38, 458752\n"
    "buffer_load_dwordx4 v16, s[28:31], s38 offen lds\n"
    "s_add_u32 m0, s62, 82944\n"
    "s_mov_b32 s38, 458752\n"
    "buffer_load_dwordx4 v17, s[28:31], s38 offen lds\n"
    "s_add_u32 m0, s62, 83968\n"
    "s_mov_b32 s38, 458752\n"
    "buffer_load_dwordx4 v18, s[28:31], s38 offen lds\n"
    "s_add_u32 m0, s62, 84992\n"
    "s_mov_b32 s38, 458752\n"
    "buffer_load_dwordx4 v19, s[28:31], s38 offen lds\n"
    "s_add_u32 m0, s62, 86016\n"
    "s_mov_b32 s38, 458752\n"
    "buffer_load_dwordx4 v20, s[28:31], s38 offen lds\n"
    "s_mov_b32 s28, s92\n"
    "s_mov_b32 s29, s93\n"
    "s_add_u32 m0, s62, 102400\n"
    "s_mov_b32 s38, 458752\n"
    "buffer_load_dwordx4 v16, s[28:31], s38 offen lds\n"
    "s_add_u32 m0, s62, 103424\n"
    "s_mov_b32 s38, 458752\n"
    "buffer_load_dwordx4 v17, s[28:31], s38 offen lds\n"
    "s_add_u32 m0, s62, 104448\n"
    "s_mov_b32 s38, 458752\n"
    "buffer_load_dwordx4 v18, s[28:31], s38 offen lds\n"
    "s_add_u32 m0, s62, 105472\n"
    "s_mov_b32 s38, 458752\n"
    "buffer_load_dwordx4 v19, s[28:31], s38 offen lds\n"
    "s_add_u32 m0, s62, 106496\n"
    "s_mov_b32 s38, 458752\n"
    "buffer_load_dwordx4 v20, s[28:31], s38 offen lds\n"
    "s_waitcnt vmcnt(25)\n"
    "s_barrier\n"
    "s_mov_b32 s28, s90\n"
    "s_mov_b32 s29, s91\n"
    "s_add_u32 m0, s62, 122880\n"
    "s_mov_b32 s38, 688128\n"
    "buffer_load_dwordx4 v16, s[28:31], s38 offen lds\n"
    "s_add_u32 m0, s62, 123904\n"
    "s_mov_b32 s38, 688128\n"
    "buffer_load_dwordx4 v17, s[28:31], s38 offen lds\n"
    "s_add_u32 m0, s62, 124928\n"
    "s_mov_b32 s38, 688128\n"
    "buffer_load_dwordx4 v18, s[28:31], s38 offen lds\n"
    "s_add_u32 m0, s62, 125952\n"
    "s_mov_b32 s38, 688128\n"
    "buffer_load_dwordx4 v19, s[28:31], s38 offen lds\n"
    "s_add_u32 m0, s62, 126976\n"
    "s_mov_b32 s38, 688128\n"
    "buffer_load_dwordx4 v20, s[28:31], s38 offen lds\n"
    "v_add_u32 v48, 0, v14\n"
    "ds_read_b64_tr_b16 a[0:1], v48 offset:0\n"
    "ds_read_b64_tr_b16 a[2:3], v48 offset:640\n"
    "ds_read_b64_tr_b16 a[4:5], v48 offset:10240\n"
    "ds_read_b64_tr_b16 a[6:7], v48 offset:10880\n"
    "ds_read_b64_tr_b16 a[16:17], v48 offset:32\n"
    "ds_read_b64_tr_b16 a[18:19], v48 offset:672\n"
    "ds_read_b64_tr_b16 a[20:21], v48 offset:10272\n"
    "ds_read_b64_tr_b16 a[22:23], v48 offset:10912\n"
    "ds_read_b64_tr_b16 v[128:129], v48 offset:64\n"
    "ds_read_b64_tr_b16 v[130:131], v48 offset:704\n"
    "ds_read_b64_tr_b16 v[132:133], v48 offset:10304\n"
    "ds_read_b64_tr_b16 v[134:135], v48 offset:10944\n"
    "s_waitcnt lgkmcnt(0)\n"
    "s_waitcnt vmcnt(25)\n"
    "s_barrier\n"
    "s_mov_b32 s28, s92\n"
    "s_mov_b32 s29, s93\n"
    "s_add_u32 m0, s62, 0\n"
    "s_mov_b32 s38, 688128\n"
    "buffer_load_dwordx4 v16, s[28:31], s38 offen lds\n"
    "s_add_u32 m0, s62, 1024\n"
    "s_mov_b32 s38, 688128\n"
    "buffer_load_dwordx4 v17, s[28:31], s38 offen lds\n"
    "s_add_u32 m0, s62, 2048\n"
    "s_mov_b32 s38, 688128\n"
    "buffer_load_dwordx4 v18, s[28:31], s38 offen lds\n"
    "s_add_u32 m0, s62, 3072\n"
    "s_mov_b32 s38, 688128\n"
    "buffer_load_dwordx4 v19, s[28:31], s38 offen lds\n"
    "s_add_u32 m0, s62, 4096\n"
    "s_mov_b32 s38, 688128\n"
    "buffer_load_dwordx4 v20, s[28:31], s38 offen lds\n"
    "v_add_u32 v48, 20480, v14\n"
    "ds_read_b64_tr_b16 a[8:9], v48 offset:0\n"
    "ds_read_b64_tr_b16 a[10:11], v48 offset:640\n"
    "ds_read_b64_tr_b16 a[12:13], v48 offset:10240\n"
    "ds_read_b64_tr_b16 a[14:15], v48 offset:10880\n"
    "ds_read_b64_tr_b16 a[24:25], v48 offset:32\n"
    "ds_read_b64_tr_b16 a[26:27], v48 offset:672\n"
    "ds_read_b64_tr_b16 a[28:29], v48 offset:10272\n"
    "ds_read_b64_tr_b16 a[30:31], v48 offset:10912\n"
    "ds_read_b64_tr_b16 v[136:137], v48 offset:64\n"
    "ds_read_b64_tr_b16 v[138:139], v48 offset:704\n"
    "ds_read_b64_tr_b16 v[140:141], v48 offset:10304\n"
    "ds_read_b64_tr_b16 v[142:143], v48 offset:10944\n"
    "s_waitcnt lgkmcnt(0)\n"
    "s_waitcnt vmcnt(25)\n"
    "s_barrier\n"
    "s_mov_b32 s28, s90\n"
    "s_mov_b32 s29, s91\n"
    "s_add_u32 m0, s62, 20480\n"
    "s_mov_b32 s38, 917504\n"
    "buffer_load_dwordx4 v16, s[28:31], s38 offen lds\n"
    "s_add_u32 m0, s62, 21504\n"
    "s_mov_b32 s38, 917504\n"
    "buffer_load_dwordx4 v17, s[28:31], s38 offen lds\n"
    "s_add_u32 m0, s62, 22528\n"
    "s_mov_b32 s38, 917504\n"
    "buffer_load_dwordx4 v18, s[28:31], s38 offen lds\n"
    "s_add_u32 m0, s62, 23552\n"
    "s_mov_b32 s38, 917504\n"
    "buffer_load_dwordx4 v19, s[28:31], s38 offen lds\n"
    "s_add_u32 m0, s62, 24576\n"
    "s_mov_b32 s38, 917504\n"
    "buffer_load_dwordx4 v20, s[28:31], s38 offen lds\n"
    "v_add_u32 v48, 40960, v14\n"
    "ds_read_b64_tr_b16 a[32:33], v48 offset:0\n"
    "ds_read_b64_tr_b16 a[34:35], v48 offset:640\n"
    "ds_read_b64_tr_b16 a[36:37], v48 offset:10240\n"
    "ds_read_b64_tr_b16 a[38:39], v48 offset:10880\n"
    "ds_read_b64_tr_b16 a[48:49], v48 offset:32\n"
    "ds_read_b64_tr_b16 a[50:51], v48 offset:672\n"
    "ds_read_b64_tr_b16 a[52:53], v48 offset:10272\n"
    "ds_read_b64_tr_b16 a[54:55], v48 offset:10912\n"
    "ds_read_b64_tr_b16 v[144:145], v48 offset:64\n"
    "ds_read_b64_tr_b16 v[146:147], v48 offset:704\n"
    "ds_read_b64_tr_b16 v[148:149], v48 offset:10304\n"
    "ds_read_b64_tr_b16 v[150:151], v48 offset:10944\n"
    "s_waitcnt lgkmcnt(0)\n"
    "s_waitcnt vmcnt(25)\n"
    "s_barrier\n"
    "s_mov_b32 s28, s92\n"
    "s_mov_b32 s29, s93\n"
    "s_add_u32 m0, s62, 40960\n"
    "s_mov_b32 s38, 917504\n"
    "buffer_load_dwordx4 v16, s[28:31], s38 offen lds\n"
    "s_add_u32 m0, s62, 41984\n"
    "s_mov_b32 s38, 917504\n"
    "buffer_load_dwordx4 v17, s[28:31], s38 offen lds\n"
    "s_add_u32 m0, s62, 43008\n"
    "s_mov_b32 s38, 917504\n"
    "buffer_load_dwordx4 v18, s[28:31], s38 offen lds\n"
    "s_add_u32 m0, s62, 44032\n"
    "s_mov_b32 s38, 917504\n"
    "buffer_load_dwordx4 v19, s[28:31], s38 offen lds\n"
    "s_add_u32 m0, s62, 45056\n"
    "s_mov_b32 s38, 917504\n"
    "buffer_load_dwordx4 v20, s[28:31], s38 offen lds\n"
    "v_add_u32 v48, 61440, v14\n"
    "ds_read_b64_tr_b16 a[40:41], v48 offset:0\n"
    "ds_read_b64_tr_b16 a[42:43], v48 offset:640\n"
    "ds_read_b64_tr_b16 a[44:45], v48 offset:10240\n"
    "ds_read_b64_tr_b16 a[46:47], v48 offset:10880\n"
    "ds_read_b64_tr_b16 a[56:57], v48 offset:32\n"
    "ds_read_b64_tr_b16 a[58:59], v48 offset:672\n"
    "ds_read_b64_tr_b16 a[60:61], v48 offset:10272\n"
    "ds_read_b64_tr_b16 a[62:63], v48 offset:10912\n"
    "ds_read_b64_tr_b16 v[152:153], v48 offset:64\n"
    "ds_read_b64_tr_b16 v[154:155], v48 offset:704\n"
    "ds_read_b64_tr_b16 v[156:157], v48 offset:10304\n"
    "ds_read_b64_tr_b16 v[158:159], v48 offset:10944\n"
    "s_waitcnt lgkmcnt(0)\n"
    "s_waitcnt vmcnt(25)\n"
    "s_barrier\n"
    "s_mov_b32 s28, s90\n"
    "s_mov_b32 s29, s91\n"
    "s_add_u32 m0, s62, 61440\n"
    "s_mov_b32 s38, 1146880\n"
    "buffer_load_dwordx4 v16, s[28:31], s38 offen lds\n"
    "s_add_u32 m0, s62, 62464\n"
    "s_mov_b32 s38, 1146880\n"
    "buffer_load_dwordx4 v17, s[28:31], s38 offen lds\n"
    "s_add_u32 m0, s62, 63488\n"
    "s_mov_b32 s38, 1146880\n"
    "buffer_load_dwordx4 v18, s[28:31], s38 offen lds\n"
    "s_add_u32 m0, s62, 64512\n"
    "s_mov_b32 s38, 1146880\n"
    "buffer_load_dwordx4 v19, s[28:31], s38 offen lds\n"
    "s_add_u32 m0, s62, 65536\n"
    "s_mov_b32 s38, 1146880\n"
    "buffer_load_dwordx4 v20, s[28:31], s38 offen lds\n"
    "v_add_u32 v48, 81920, v14\n"
    "ds_read_b64_tr_b16 a[64:65], v48 offset:0\n"
    "ds_read_b64_tr_b16 a[66:67], v48 offset:640\n"
    "ds_read_b64_tr_b16 a[68:69], v48 offset:10240\n"
    "ds_read_b64_tr_b16 a[70:71], v48 offset:10880\n"
    "ds_read_b64_tr_b16 a[80:81], v48 offset:32\n"
    "ds_read_b64_tr_b16 a[82:83], v48 offset:672\n"
    "ds_read_b64_tr_b16 a[84:85], v48 offset:10272\n"
    "ds_read_b64_tr_b16 a[86:87], v48 offset:10912\n"
    "ds_read_b64_tr_b16 v[160:161], v48 offset:64\n"
    "ds_read_b64_tr_b16 v[162:163], v48 offset:704\n"
    "ds_read_b64_tr_b16 v[164:165], v48 offset:10304\n"
    "ds_read_b64_tr_b16 v[166:167], v48 offset:10944\n"
    "s_waitcnt lgkmcnt(0)\n"
    "s_waitcnt vmcnt(25)\n"
    "s_barrier\n"
    "s_mov_b32 s28, s92\n"
    "s_mov_b32 s29, s93\n"
    "s_add_u32 m0, s62, 81920\n"
    "s_mov_b32 s38, 1146880\n"
    "buffer_load_dwordx4 v16, s[28:31], s38 offen lds\n"
    "s_add_u32 m0, s62, 82944\n"
    "s_mov_b32 s38, 1146880\n"
    "buffer_load_dwordx4 v17, s[28:31], s38 offen lds\n"
    "s_add_u32 m0, s62, 83968\n"
    "s_mov_b32 s38, 1146880\n"
    "buffer_load_dwordx4 v18, s[28:31], s38 offen lds\n"
    "s_add_u32 m0, s62, 84992\n"
    "s_mov_b32 s38, 1146880\n"
    "buffer_load_dwordx4 v19, s[28:31], s38 offen lds\n"
    "s_add_u32 m0, s62, 86016\n"
    "s_mov_b32 s38, 1146880\n"
    "buffer_load_dwordx4 v20, s[28:31], s38 offen lds\n"
    "v_add_u32 v48, 102400, v14\n"
    "ds_read_b64_tr_b16 a[72:73], v48 offset:0\n"
    "ds_read_b64_tr_b16 a[74:75], v48 offset:640\n"
    "ds_read_b64_tr_b16 a[76:77], v48 offset:10240\n"
    "ds_read_b64_tr_b16 a[78:79], v48 offset:10880\n"
    "ds_read_b64_tr_b16 a[88:89], v48 offset:32\n"
    "ds_read_b64_tr_b16 a[90:91], v48 offset:672\n"
    "ds_read_b64_tr_b16 a[92:93], v48 offset:10272\n"
    "ds_read_b64_tr_b16 a[94:95], v48 offset:10912\n"
    "ds_read_b64_tr_b16 v[168:169], v48 offset:64\n"
    "ds_read_b64_tr_b16 v[170:171], v48 offset:704\n"
    "ds_read_b64_tr_b16 v[172:173], v48 offset:10304\n"
    "ds_read_b64_tr_b16 v[174:175], v48 offset:10944\n"
    "s_waitcnt lgkmcnt(0)\n"
    "s_waitcnt vmcnt(25)\n"
    "s_barrier\n"
    "s_mov_b32 s28, s90\n"
    "s_mov_b32 s29, s91\n"
    "s_add_u32 m0, s62, 102400\n"
    "s_mov_b32 s38, 1376256\n"
    "buffer_load_dwordx4 v16, s[28:31], s38 offen lds\n"
    "s_add_u32 m0, s62, 103424\n"
    "s_mov_b32 s38, 1376256\n"
    "buffer_load_dwordx4 v17, s[28:31], s38 offen lds\n"
    "s_add_u32 m0, s62, 104448\n"
    "s_mov_b32 s38, 1376256\n"
    "buffer_load_dwordx4 v18, s[28:31], s38 offen lds\n"
    "s_add_u32 m0, s62, 105472\n"
    "s_mov_b32 s38, 1376256\n"
    "buffer_load_dwordx4 v19, s[28:31], s38 offen lds\n"
    "s_add_u32 m0, s62, 106496\n"
    "s_mov_b32 s38, 1376256\n"
    "buffer_load_dwordx4 v20, s[28:31], s38 offen lds\n"
    "v_add_u32 v48, 122880, v14\n"
    "ds_read_b64_tr_b16 a[96:97], v48 offset:0\n"
    "ds_read_b64_tr_b16 a[98:99], v48 offset:640\n"
    "ds_read_b64_tr_b16 a[100:101], v48 offset:10240\n"
    "ds_read_b64_tr_b16 a[102:103], v48 offset:10880\n"
    "ds_read_b64_tr_b16 a[112:113], v48 offset:32\n"
    "ds_read_b64_tr_b16 a[114:115], v48 offset:672\n"
    "ds_read_b64_tr_b16 a[116:117], v48 offset:10272\n"
    "ds_read_b64_tr_b16 a[118:119], v48 offset:10912\n"
    "ds_read_b64_tr_b16 v[176:177], v48 offset:64\n"
    "ds_read_b64_tr_b16 v[178:179], v48 offset:704\n"
    "ds_read_b64_tr_b16 v[180:181], v48 offset:10304\n"
    "ds_read_b64_tr_b16 v[182:183], v48 offset:10944\n"
    "s_waitcnt lgkmcnt(0)\n"
    "s_waitcnt vmcnt(25)\n"
    "s_barrier\n"
    "s_mov_b32 s28, s92\n"
    "s_mov_b32 s29, s93\n"
    "s_add_u32 m0, s62, 122880\n"
    "s_mov_b32 s38, 1376256\n"
    "buffer_load_dwordx4 v16, s[28:31], s38 offen lds\n"
    "s_add_u32 m0, s62, 123904\n"
    "s_mov_b32 s38, 1376256\n"
    "buffer_load_dwordx4 v17, s[28:31], s38 offen lds\n"
    "s_add_u32 m0, s62, 124928\n"
    "s_mov_b32 s38, 1376256\n"
    "buffer_load_dwordx4 v18, s[28:31], s38 offen lds\n"
    "s_add_u32 m0, s62, 125952\n"
    "s_mov_b32 s38, 1376256\n"
    "buffer_load_dwordx4 v19, s[28:31], s38 offen lds\n"
    "s_add_u32 m0, s62, 126976\n"
    "s_mov_b32 s38, 1376256\n"
    "buffer_load_dwordx4 v20, s[28:31], s38 offen lds\n"
    "v_add_u32 v48, 0, v14\n"
    "ds_read_b64_tr_b16 a[104:105], v48 offset:0\n"
    "ds_read_b64_tr_b16 a[106:107], v48 offset:640\n"
    "ds_read_b64_tr_b16 a[108:109], v48 offset:10240\n"
    "ds_read_b64_tr_b16 a[110:111], v48 offset:10880\n"
    "ds_read_b64_tr_b16 a[120:121], v48 offset:32\n"
    "ds_read_b64_tr_b16 a[122:123], v48 offset:672\n"
    "ds_read_b64_tr_b16 a[124:125], v48 offset:10272\n"
    "ds_read_b64_tr_b16 a[126:127], v48 offset:10912\n"
    "ds_read_b64_tr_b16 v[184:185], v48 offset:64\n"
    "ds_read_b64_tr_b16 v[186:187], v48 offset:704\n"
    "ds_read_b64_tr_b16 v[188:189], v48 offset:10304\n"
    "ds_read_b64_tr_b16 v[190:191], v48 offset:10944\n"
    "s_waitcnt lgkmcnt(0)\n"
    "s_waitcnt vmcnt(25)\n"
    "s_barrier\n"
    "s_mov_b32 s28, s90\n"
    "s_mov_b32 s29, s91\n"
    "s_add_u32 m0, s62, 0\n"
    "s_mov_b32 s38, 1605632\n"
    "buffer_load_dwordx4 v16, s[28:31], s38 offen lds\n"
    "s_add_u32 m0, s62, 1024\n"
    "s_mov_b32 s38, 1605632\n"
    "buffer_load_dwordx4 v17, s[28:31], s38 offen lds\n"
    "s_add_u32 m0, s62, 2048\n"
    "s_mov_b32 s38, 1605632\n"
    "buffer_load_dwordx4 v18, s[28:31], s38 offen lds\n"
    "s_add_u32 m0, s62, 3072\n"
    "s_mov_b32 s38, 1605632\n"
    "buffer_load_dwordx4 v19, s[28:31], s38 offen lds\n"
    "s_add_u32 m0, s62, 4096\n"
    "s_mov_b32 s38, 1605632\n"
    "buffer_load_dwordx4 v20, s[28:31], s38 offen lds\n"
    "v_add_u32 v48, 20480, v14\n"
    "ds_read_b64_tr_b16 a[128:129], v48 offset:0\n"
    "ds_read_b64_tr_b16 a[130:131], v48 offset:640\n"
    "ds_read_b64_tr_b16 a[132:133], v48 offset:10240\n"
    "ds_read_b64_tr_b16 a[134:135], v48 offset:10880\n"
    "ds_read_b64_tr_b16 a[144:145], v48 offset:32\n"
    "ds_read_b64_tr_b16 a[146:147], v48 offset:672\n"
    "ds_read_b64_tr_b16 a[148:149], v48 offset:10272\n"
    "ds_read_b64_tr_b16 a[150:151], v48 offset:10912\n"
    "ds_read_b64_tr_b16 v[192:193], v48 offset:64\n"
    "ds_read_b64_tr_b16 v[194:195], v48 offset:704\n"
    "ds_read_b64_tr_b16 v[196:197], v48 offset:10304\n"
    "ds_read_b64_tr_b16 v[198:199], v48 offset:10944\n"
    "s_waitcnt lgkmcnt(0)\n"
    "s_waitcnt vmcnt(25)\n"
    "s_barrier\n"
    "s_mov_b32 s28, s92\n"
    "s_mov_b32 s29, s93\n"
    "s_add_u32 m0, s62, 20480\n"
    "s_mov_b32 s38, 1605632\n"
    "buffer_load_dwordx4 v16, s[28:31], s38 offen lds\n"
    "s_add_u32 m0, s62, 21504\n"
    "s_mov_b32 s38, 1605632\n"
    "buffer_load_dwordx4 v17, s[28:31], s38 offen lds\n"
    "s_add_u32 m0, s62, 22528\n"
    "s_mov_b32 s38, 1605632\n"
    "buffer_load_dwordx4 v18, s[28:31], s38 offen lds\n"
    "s_add_u32 m0, s62, 23552\n"
    "s_mov_b32 s38, 1605632\n"
    "buffer_load_dwordx4 v19, s[28:31], s38 offen lds\n"
    "s_add_u32 m0, s62, 24576\n"
    "s_mov_b32 s38, 1605632\n"
    "buffer_load_dwordx4 v20, s[28:31], s38 offen lds\n"
    "v_add_u32 v48, 40960, v14\n"
    "ds_read_b64_tr_b16 a[136:137], v48 offset:0\n"
    "ds_read_b64_tr_b16 a[138:139], v48 offset:640\n"
    "ds_read_b64_tr_b16 a[140:141], v48 offset:10240\n"
    "ds_read_b64_tr_b16 a[142:143], v48 offset:10880\n"
    "ds_read_b64_tr_b16 a[152:153], v48 offset:32\n"
    "ds_read_b64_tr_b16 a[154:155], v48 offset:672\n"
    "ds_read_b64_tr_b16 a[156:157], v48 offset:10272\n"
    "ds_read_b64_tr_b16 a[158:159], v48 offset:10912\n"
    "ds_read_b64_tr_b16 v[200:201], v48 offset:64\n"
    "ds_read_b64_tr_b16 v[202:203], v48 offset:704\n"
    "ds_read_b64_tr_b16 v[204:205], v48 offset:10304\n"
    "ds_read_b64_tr_b16 v[206:207], v48 offset:10944\n"
    "s_waitcnt lgkmcnt(0)\n"
    "s_waitcnt vmcnt(25)\n"
    "s_barrier\n"
    "v_add_u32 v48, 61440, v14\n"
    "ds_read_b64_tr_b16 a[160:161], v48 offset:0\n"
    "ds_read_b64_tr_b16 a[162:163], v48 offset:640\n"
    "ds_read_b64_tr_b16 a[164:165], v48 offset:10240\n"
    "ds_read_b64_tr_b16 a[166:167], v48 offset:10880\n"
    "ds_read_b64_tr_b16 a[176:177], v48 offset:32\n"
    "ds_read_b64_tr_b16 a[178:179], v48 offset:672\n"
    "ds_read_b64_tr_b16 a[180:181], v48 offset:10272\n"
    "ds_read_b64_tr_b16 a[182:183], v48 offset:10912\n"
    "ds_read_b64_tr_b16 v[208:209], v48 offset:64\n"
    "ds_read_b64_tr_b16 v[210:211], v48 offset:704\n"
    "ds_read_b64_tr_b16 v[212:213], v48 offset:10304\n"
    "ds_read_b64_tr_b16 v[214:215], v48 offset:10944\n"
    "s_waitcnt lgkmcnt(0)\n"
    "s_waitcnt vmcnt(20)\n"
    "s_barrier\n"
    "v_add_u32 v48, 81920, v14\n"
    "ds_read_b64_tr_b16 a[168:169], v48 offset:0\n"
    "ds_read_b64_tr_b16 a[170:171], v48 offset:640\n"
    "ds_read_b64_tr_b16 a[172:173], v48 offset:10240\n"
    "ds_read_b64_tr_b16 a[174:175], v48 offset:10880\n"
    "ds_read_b64_tr_b16 a[184:185], v48 offset:32\n"
    "ds_read_b64_tr_b16 a[186:187], v48 offset:672\n"
    "ds_read_b64_tr_b16 a[188:189], v48 offset:10272\n"
    "ds_read_b64_tr_b16 a[190:191], v48 offset:10912\n"
    "ds_read_b64_tr_b16 v[216:217], v48 offset:64\n"
    "ds_read_b64_tr_b16 v[218:219], v48 offset:704\n"
    "ds_read_b64_tr_b16 v[220:221], v48 offset:10304\n"
    "ds_read_b64_tr_b16 v[222:223], v48 offset:10944\n"
    "s_waitcnt lgkmcnt(0)\n"
    "s_waitcnt vmcnt(15)\n"
    "s_barrier\n"
    "v_add_u32 v48, 102400, v14\n"
    "ds_read_b64_tr_b16 a[192:193], v48 offset:0\n"
    "ds_read_b64_tr_b16 a[194:195], v48 offset:640\n"
    "ds_read_b64_tr_b16 a[196:197], v48 offset:10240\n"
    "ds_read_b64_tr_b16 a[198:199], v48 offset:10880\n"
    "ds_read_b64_tr_b16 a[208:209], v48 offset:32\n"
    "ds_read_b64_tr_b16 a[210:211], v48 offset:672\n"
    "ds_read_b64_tr_b16 a[212:213], v48 offset:10272\n"
    "ds_read_b64_tr_b16 a[214:215], v48 offset:10912\n"
    "ds_read_b64_tr_b16 v[224:225], v48 offset:64\n"
    "ds_read_b64_tr_b16 v[226:227], v48 offset:704\n"
    "ds_read_b64_tr_b16 v[228:229], v48 offset:10304\n"
    "ds_read_b64_tr_b16 v[230:231], v48 offset:10944\n"
    "s_waitcnt lgkmcnt(0)\n"
    "s_waitcnt vmcnt(10)\n"
    "s_barrier\n"
    "v_add_u32 v48, 122880, v14\n"
    "ds_read_b64_tr_b16 a[200:201], v48 offset:0\n"
    "ds_read_b64_tr_b16 a[202:203], v48 offset:640\n"
    "ds_read_b64_tr_b16 a[204:205], v48 offset:10240\n"
    "ds_read_b64_tr_b16 a[206:207], v48 offset:10880\n"
    "ds_read_b64_tr_b16 a[216:217], v48 offset:32\n"
    "ds_read_b64_tr_b16 a[218:219], v48 offset:672\n"
    "ds_read_b64_tr_b16 a[220:221], v48 offset:10272\n"
    "ds_read_b64_tr_b16 a[222:223], v48 offset:10912\n"
    "ds_read_b64_tr_b16 v[232:233], v48 offset:64\n"
    "ds_read_b64_tr_b16 v[234:235], v48 offset:704\n"
    "ds_read_b64_tr_b16 v[236:237], v48 offset:10304\n"
    "ds_read_b64_tr_b16 v[238:239], v48 offset:10944\n"
    "s_waitcnt lgkmcnt(0)\n"
    "s_waitcnt vmcnt(5)\n"
    "s_barrier\n"
    "v_add_u32 v48, 0, v14\n"
    "ds_read_b64_tr_b16 a[224:225], v48 offset:0\n"
    "ds_read_b64_tr_b16 a[226:227], v48 offset:640\n"
    "ds_read_b64_tr_b16 a[228:229], v48 offset:10240\n"
    "ds_read_b64_tr_b16 a[230:231], v48 offset:10880\n"
    "ds_read_b64_tr_b16 a[240:241], v48 offset:32\n"
    "ds_read_b64_tr_b16 a[242:243], v48 offset:672\n"
    "ds_read_b64_tr_b16 a[244:245], v48 offset:10272\n"
    "ds_read_b64_tr_b16 a[246:247], v48 offset:10912\n"
    "ds_read_b64_tr_b16 v[240:241], v48 offset:64\n"
    "ds_read_b64_tr_b16 v[242:243], v48 offset:704\n"
    "ds_read_b64_tr_b16 v[244:245], v48 offset:10304\n"
    "ds_read_b64_tr_b16 v[246:247], v48 offset:10944\n"
    "s_waitcnt lgkmcnt(0)\n"
    "s_waitcnt vmcnt(0)\n"
    "s_barrier\n"
    "v_add_u32 v48, 20480, v14\n"
    "ds_read_b64_tr_b16 a[232:233], v48 offset:0\n"
    "ds_read_b64_tr_b16 a[234:235], v48 offset:640\n"
    "ds_read_b64_tr_b16 a[236:237], v48 offset:10240\n"
    "ds_read_b64_tr_b16 a[238:239], v48 offset:10880\n"
    "ds_read_b64_tr_b16 a[248:249], v48 offset:32\n"
    "ds_read_b64_tr_b16 a[250:251], v48 offset:672\n"
    "ds_read_b64_tr_b16 a[252:253], v48 offset:10272\n"
    "ds_read_b64_tr_b16 a[254:255], v48 offset:10912\n"
    "ds_read_b64_tr_b16 v[248:249], v48 offset:64\n"
    "ds_read_b64_tr_b16 v[250:251], v48 offset:704\n"
    "ds_read_b64_tr_b16 v[252:253], v48 offset:10304\n"
    "ds_read_b64_tr_b16 v[254:255], v48 offset:10944\n"
    "s_waitcnt lgkmcnt(0)\n"
    "s_barrier\n"
    "v_lshlrev_b32 v32, 16, v116\n"
    "v_and_b32 v33, 0xffff0000, v116\n"
    "v_lshlrev_b32 v34, 16, v117\n"
    "v_and_b32 v35, 0xffff0000, v117\n"
    "v_lshlrev_b32 v36, 16, v118\n"
    "v_and_b32 v37, 0xffff0000, v118\n"
    "v_lshlrev_b32 v38, 16, v119\n"
    "v_and_b32 v39, 0xffff0000, v119\n"
    "v_mov_b32 v24, v12\n"
    "v_mov_b32 v25, v13\n"
    "v_mov_b32 v30, v12\n"
    "v_mov_b32 v31, v13\n"
    "v_mov_b32 v26, 0\n"
    "v_mov_b32 v27, 0\n"
    "v_mov_b32 v28, 0\n"
    "v_mov_b32 v29, 0\n"
    "v_mov_b32 v42, 0\n"
    "v_mov_b32 v43, 0\n"
    "v_mov_b32 v48, 0\n"
    "v_mov_b32 v44, 0x3b000000\n"
    "s_mov_b32 s40, 0\n"
    "s_mov_b32 s41, 0\n"
    "s_mov_b32 s42, 64\n"
    "s_mov_b32 s43, 0\n"
    "s_mov_b32 s44, 0\n"
    "s_mov_b32 s45, 0\n"
    "s_mov_b32 s46, -1\n"
    "s_mov_b32 s47, -1\n"
    "s_mov_b32 s48, 0\n"
    "s_mov_b32 s51, 0\n"
    "s_mov_b32 s49, 0\n"
    "s_mov_b32 s37, 0\n"
    "s_mov_b64 s[64:65], 0\n"
    "s_mov_b32 s79, 0\n"
    "s_mov_b32 s74, 0xffff0000\n"
    "s_mov_b32 s75, 0\n"
    "s_mov_b32 s76, 0xaaaaaaaa\n"
    "s_mov_b32 s77, 0xaaaaaaaa\n"
    "LRY%=:\n"
    "global_load_dword v40, v48, s[96:97] sc1\n"
    "s_waitcnt vmcnt(0)\n"
    "v_readfirstlane_b32 s56, v40\n"
    "s_cmp_lg_u32 s56, 0\n"
    "s_cbranch_scc1 LRZ%=\n"
    "s_sleep 8\n"
    "s_add_u32 s37, s37, 1\n"
    "s_cmp_lt_u32 s37, 0x80000\n"
    "s_cbranch_scc1 LRY%=\n"
    "s_branch LFAIL%=\n"
    "LRZ%=:\n"
    "s_lshl_b32 s56, s56, 13\n"
    "s_and_b32 s56, s56, 0x2000\n"
    "s_xor_b32 s57, s56, 0x2000\n"
    "s_xor_b32 s58, s58, s56\n"
    "v_xor_b32 v2, s56, v2\n"
    "v_xor_b32 v11, s56, v11\n"
    "v_xor_b32 v12, s56, v21\n"
    "s_cmp_eq_u32 s55, 0\n"
    "s_cselect_b32 s38, 0, 0x80000000\n"
    "s_add_u32 s38, s38, 4096\n"
    "v_add_u32 v13, s38, v11\n"
    "LPR%=:\n"
    "buffer_load_dword v40, v12, s[16:19], 0 offen sc1\n"
    "s_waitcnt vmcnt(0)\n"
    "v_cmp_eq_u32 vcc, 0, v40\n"
    "s_cbranch_vccz LPOLL%=\n"
    "s_sleep 8\n"
    "s_add_u32 s37, s37, 1\n"
    "s_cmp_lt_u32 s37, 0x80000\n"
    "s_cbranch_scc1 LPR%=\n"
    "s_branch LFAIL%=\n"
    "LIDLE%=:\n"
    "s_add_u32 s37, s37, 1\n"
    "s_cmp_gt_u32 s37, 0x100000\n"
    "s_cbranch_scc1 LFAIL%=\n"
    "s_sleep 1\n"
    "LPOLL%=:\n"
    "v_mad_u32_u24 v41, v3, s41, v2\n"
    "v_mad_u32_u24 v41, v5, s40, v41\n"
    "buffer_load_dword v40, v41, s[16:19], 0 offen sc1\n"
    "s_waitcnt vmcnt(0)\n"
    "LTOP%=:\n"
    "v_readlane_b32 s52, v40, 32\n"
    "v_readlane_b32 s53, v40, 0\n"
    "v_readlane_b32 s54, v40, 16\n"
    "s_cmp_lg_u32 s52, 0\n"
    "s_cbranch_scc1 LDEC%=\n"
    "LPICK%=:\n"
    "s_cmp_eq_u32 s53, 0\n"
    "s_mov_b32 s50, -1\n"
    "s_cbranch_scc1 LNONE%=\n"
    "s_mov_b32 s50, s41\n"
    "s_add_u32 s41, s41, 1\n"
    "s_lshl_b32 s62, s50, 18\n"
    "s_add_u32 s62, s62, 180355072\n"
    "s_lshl_b32 s63, s50, 5\n"
    "s_add_u32 s63, s63, s55\n"
    "s_lshl_b32 s63, s63, 10\n"
    "s_lshr_b32 s38, s62, 8\n"
    "s_xor_b32 s38, s38, s57\n"
    "s_and_b32 s38, s38, 0x2000\n"
    "s_or_b32 s62, s62, s38\n"
    "buffer_load_dwordx4 v[116:119], v0, s[20:23], s63 offen\n"
    "buffer_load_dwordx4 v[100:103], v1, s[16:19], s62 offen sc1\n"
    "s_add_u32 s62, s62, 0x10000\n"
    "buffer_load_dwordx4 v[104:107], v1, s[16:19], s62 offen sc1\n"
    "s_add_u32 s62, s62, 0x10000\n"
    "buffer_load_dwordx4 v[108:111], v1, s[16:19], s62 offen sc1\n"
    "s_add_u32 s62, s62, 0x10000\n"
    "buffer_load_dwordx4 v[112:115], v1, s[16:19], s62 offen sc1\n"
    "s_cmp_ge_i32 s47, 0\n"
    "s_cbranch_scc1 LJOB%=\n"
    "s_lshl_b32 s60, s50, 5\n"
    "s_add_u32 s60, s60, s55\n"
    "s_lshl_b32 s39, s60, 11\n"
    "s_andn2_b32 s32, s39, 0x1fff\n"
    "s_add_u32 s32, s32, s39\n"
    "s_add_u32 s32, s32, 245366784\n"
    "s_lshr_b32 s38, s32, 8\n"
    "s_xor_b32 s38, s38, s57\n"
    "s_and_b32 s38, s38, 0x2000\n"
    "s_or_b32 s32, s32, s38\n"
    "s_lshl_b32 s39, s60, 10\n"
    "s_andn2_b32 s78, s39, 0x1fff\n"
    "s_add_u32 s78, s78, s39\n"
    "s_add_u32 s78, s78, 236978176\n"
    "s_lshr_b32 s38, s78, 8\n"
    "s_xor_b32 s38, s38, s57\n"
    "s_and_b32 s38, s38, 0x2000\n"
    "s_or_b32 s78, s78, s38\n"
    "s_mov_b32 s33, s60\n"
    "s_waitcnt vmcnt(4)\n"
    "v_lshlrev_b32 v64, 16, v116\n"
    "v_and_b32 v65, 0xffff0000, v116\n"
    "v_lshlrev_b32 v66, 16, v117\n"
    "v_and_b32 v67, 0xffff0000, v117\n"
    "v_lshlrev_b32 v68, 16, v118\n"
    "v_and_b32 v69, 0xffff0000, v118\n"
    "v_lshlrev_b32 v70, 16, v119\n"
    "v_and_b32 v71, 0xffff0000, v119\n"
    "s_waitcnt vmcnt(3)\n"
    "v_mfma_f32_16x16x32_bf16 v[64:67], v[24:27], v[100:103], v[64:67]\n"
    "v_mfma_f32_16x16x32_bf16 v[68:71], v[28:31], v[100:103], v[68:71]\n"
    "s_waitcnt vmcnt(2)\n"
    "v_mfma_f32_16x16x32_bf16 v[64:67], v[24:27], v[104:107], v[64:67]\n"
    "v_mfma_f32_16x16x32_bf16 v[68:71], v[28:31], v[104:107], v[68:71]\n"
    "s_waitcnt vmcnt(1)\n"
    "v_mfma_f32_16x16x32_bf16 v[64:67], v[24:27], v[108:111], v[64:67]\n"
    "v_mfma_f32_16x16x32_bf16 v[68:71], v[28:31], v[108:111], v[68:71]\n"
    "v_max3_u32 v48, v103, v107, v111\n"
    "s_waitcnt vmcnt(0)\n"
    "v_mfma_f32_16x16x32_bf16 v[64:67], v[24:27], v[112:115], v[64:67]\n"
    "v_mfma_f32_16x16x32_bf16 v[68:71], v[28:31], v[112:115], v[68:71]\n"
    "v_max_u32 v48, v48, v115\n"
    "v_cmp_eq_u32 vcc, -1, v48\n"
    "s_mov_b32 s47, s50\n"
    "s_mov_b32 s48, s51\n"
    "s_mov_b32 s50, -1\n"
    "s_cbranch_vccnz LEARLY%=\n"
    "LNXT%=:\n"
    "s_mov_b32 s53, s54\n"
    "s_cmp_lg_u32 s54, 0\n"
    "s_cbranch_scc1 LPICK%=\n"
    "LJOB%=:\n"
    "v_mad_u32_u24 v41, v3, s41, v2\n"
    "v_mad_u32_u24 v41, v5, s40, v41\n"
    "buffer_load_dword v40, v41, s[16:19], 0 offen sc1\n"
    "v_cvt_pk_bf16_f32 v124, v64, v65\n"
    "v_lshlrev_b32 v72, 16, v124\n"
    "v_cvt_pk_bf16_f32 v125, v66, v67\n"
    "v_readfirstlane_b32 s68, v72\n"
    "v_cvt_pk_bf16_f32 v126, v68, v69\n"
    "v_cvt_pk_bf16_f32 v127, v70, v71\n"
    "v_and_b32 v73, 0xffff0000, v124\n"
    "v_lshlrev_b32 v74, 16, v125\n"
    "v_and_b32 v75, 0xffff0000, v125\n"
    "v_lshlrev_b32 v76, 16, v126\n"
    "v_and_b32 v77, 0xffff0000, v126\n"
    "v_lshlrev_b32 v78, 16, v127\n"
    "v_and_b32 v79, 0xffff0000, v127\n"
    "v_pk_add_f32 v[92:93], v[72:73], s[68:69] op_sel_hi:[1,0] neg_lo:[0,1] neg_hi:[0,1]\n"
    "v_pk_add_f32 v[94:95], v[74:75], s[68:69] op_sel_hi:[1,0] neg_lo:[0,1] neg_hi:[0,1]\n"
    "v_pk_add_f32 v[96:97], v[76:77], s[68:69] op_sel_hi:[1,0] neg_lo:[0,1] neg_hi:[0,1]\n"
    "v_pk_add_f32 v[98:99], v[78:79], s[68:69] op_sel_hi:[1,0] neg_lo:[0,1] neg_hi:[0,1]\n"
    "v_pk_add_f32 v[48:49], v[92:93], v[94:95]\n"
    "v_pk_mul_f32 v[50:51], v[92:93], v[92:93]\n"
    "v_pk_add_f32 v[52:53], v[96:97], v[98:99]\n"
    "v_pk_fma_f32 v[50:51], v[94:95], v[94:95], v[50:51]\n"
    "v_pk_add_f32 v[48:49], v[48:49], v[52:53]\n"
    "v_pk_fma_f32 v[50:51], v[96:97], v[96:97], v[50:51]\n"
    "v_add_f32 v48, v48, v49\n"
    "v_pk_fma_f32 v[50:51], v[98:99], v[98:99], v[50:51]\n"
    "v_mov_b32 v52, s68\n"
    "v_add_f32 v49, v50, v51\n"
    "v_add_f32_dpp v48, v48, v48 row_ror:8 row_mask:0xf bank_mask:0xf\n"
    "s_nop 0\n"
    "v_add_f32_dpp v49, v49, v49 row_ror:8 row_mask:0xf bank_mask:0xf\n"
    "v_add_f32_dpp v48, v48, v48 row_ror:4 row_mask:0xf bank_mask:0xf\n"
    "s_nop 0\n"
    "v_add_f32_dpp v49, v49, v49 row_ror:4 row_mask:0xf bank_mask:0xf\n"
    "v_add_f32_dpp v48, v48, v48 row_ror:2 row_mask:0xf bank_mask:0xf\n"
    "s_nop 0\n"
    "v_add_f32_dpp v49, v49, v49 row_ror:2 row_mask:0xf bank_mask:0xf\n"
    "v_add_f32_dpp v48, v48, v48 row_ror:1 row_mask:0xf bank_mask:0xf\n"
    "s_nop 0\n"
    "v_add_f32_dpp v49, v49, v49 row_ror:1 row_mask:0xf bank_mask:0xf\n"
    "s_nop 1\n"
    "v_permlane16_swap_b32 v48, v49\n"
    "v_add_f32 v48, v48, v49\n"
    "v_mov_b32 v49, v48\n"
    "s_nop 1\n"
    "v_permlane32_swap_b32 v48, v49\n"
    "v_add_f32 v48, v48, v49\n"
    "s_nop 0\n"
    "v_readlane_b32 s62, v48, 0\n"
    "v_readlane_b32 s63, v48, 16\n"
    "s_nop 0\n"
    "v_mul_f32 v48, s62, v44\n"
    "v_mul_f32 v49, s63, v44\n"
    "v_fma_f32 v49, -v48, v48, v49\n"
    "v_add_f32 v52, v52, v48\n"
    "v_max_f32 v49, 0, v49\n"
    "v_cvt_pk_bf16_f32 v48, v52, v49\n"
    "v_and_b32 v49, 0xffff0000, v48\n"
    "v_add_f32 v49, 0x3727c5ac, v49\n"
    "v_cvt_pk_bf16_f32 v49, v49, v49\n"
    "v_lshlrev_b32 v49, 16, v49\n"
    "v_sqrt_f32 v50, v49\n"
    "v_lshlrev_b32 v48, 16, v48\n"
    "v_cvt_pk_bf16_f32 v50, v50, v50\n"
    "v_lshlrev_b32 v50, 16, v50\n"
    "v_rcp_f32 v50, v50\n"
    "v_pk_add_f32 v[92:93], v[72:73], v[48:49] op_sel_hi:[1,0] neg_lo:[0,1] neg_hi:[0,1]\n"
    "v_pk_add_f32 v[94:95], v[74:75], v[48:49] op_sel_hi:[1,0] neg_lo:[0,1] neg_hi:[0,1]\n"
    "v_pk_add_f32 v[96:97], v[76:77], v[48:49] op_sel_hi:[1,0] neg_lo:[0,1] neg_hi:[0,1]\n"
    "v_pk_add_f32 v[98:99], v[78:79], v[48:49] op_sel_hi:[1,0] neg_lo:[0,1] neg_hi:[0,1]\n"
    "v_cvt_pk_bf16_f32 v52, v92, v93\n"
    "v_cvt_pk_bf16_f32 v53, v94, v95\n"
    "v_cvt_pk_bf16_f32 v54, v96, v97\n"
    "v_cvt_pk_bf16_f32 v55, v98, v99\n"
    "v_lshlrev_b32 v92, 16, v52\n"
    "v_and_b32 v93, 0xffff0000, v52\n"
    "v_lshlrev_b32 v94, 16, v53\n"
    "v_and_b32 v95, 0xffff0000, v53\n"
    "v_lshlrev_b32 v96, 16, v54\n"
    "v_and_b32 v97, 0xffff0000, v54\n"
    "v_lshlrev_b32 v98, 16, v55\n"
    "v_and_b32 v99, 0xffff0000, v55\n"
    "v_pk_mul_f32 v[80:81], v[92:93], v[50:51] op_sel_hi:[1,0]\n"
    "v_pk_mul_f32 v[82:83], v[94:95], v[50:51] op_sel_hi:[1,0]\n"
    "v_pk_mul_f32 v[84:85], v[96:97], v[50:51] op_sel_hi:[1,0]\n"
    "v_pk_mul_f32 v[86:87], v[98:99], v[50:51] op_sel_hi:[1,0]\n"
    "s_waitcnt vmcnt(0)\n"
    "v_readlane_b32 s52, v40, 32\n"
    "s_lshr_b32 s38, s52, 16\n"
    "s_cmp_eq_u32 s52, 0\n"
    "s_cselect_b32 s38, -1, s38\n"
    "s_cmp_gt_i32 s38, s46\n"
    "s_cbranch_scc1 LMID%=\n"
    "v_cvt_pk_bf16_f32 v88, v80, v81\n"
    "v_cvt_pk_bf16_f32 v89, v82, v83\n"
    "v_cvt_pk_bf16_f32 v90, v84, v85\n"
    "v_cvt_pk_bf16_f32 v91, v86, v87\n"
    "v_lshlrev_b32 v80, 16, v88\n"
    "v_and_b32 v81, 0xffff0000, v88\n"
    "v_lshlrev_b32 v82, 16, v89\n"
    "v_and_b32 v83, 0xffff0000, v89\n"
    "v_lshlrev_b32 v84, 16, v90\n"
    "v_and_b32 v85, 0xffff0000, v90\n"
    "v_lshlrev_b32 v86, 16, v91\n"
    "v_and_b32 v87, 0xffff0000, v91\n"
    "v_pk_add_f32 v[72:73], v[72:73], v[80:81]\n"
    "v_pk_add_f32 v[74:75], v[74:75], v[82:83]\n"
    "v_pk_add_f32 v[76:77], v[76:77], v[84:85]\n"
    "v_pk_add_f32 v[78:79], v[78:79], v[86:87]\n"
    "v_cvt_pk_bf16_f32 v60, v72, v73\n"
    "v_cvt_pk_bf16_f32 v61, v74, v75\n"
    "v_cvt_pk_bf16_f32 v62, v76, v77\n"
    "v_cvt_pk_bf16_f32 v63, v78, v79\n"
    "v_lshlrev_b32 v72, 16, v60\n"
    "v_and_b32 v73, 0xffff0000, v60\n"
    "v_lshlrev_b32 v74, 16, v61\n"
    "v_and_b32 v75, 0xffff0000, v61\n"
    "v_lshlrev_b32 v76, 16, v62\n"
    "v_and_b32 v77, 0xffff0000, v62\n"
    "v_lshlrev_b32 v78, 16, v63\n"
    "v_and_b32 v79, 0xffff0000, v63\n"
    "s_lshl_b32 s39, s47, 2\n"
    "s_mov_b64 exec, 7\n"
    "buffer_store_dword v6, v13, s[16:19], s39 offen sc1\n"
    "s_mov_b64 exec, -1\n"
    "v_pk_add_f32 v[92:93], v[72:73], s[68:69] op_sel_hi:[1,0] neg_lo:[0,1] neg_hi:[0,1]\n"
    "v_pk_add_f32 v[94:95], v[74:75], s[68:69] op_sel_hi:[1,0] neg_lo:[0,1] neg_hi:[0,1]\n"
    "v_pk_add_f32 v[96:97], v[76:77], s[68:69] op_sel_hi:[1,0] neg_lo:[0,1] neg_hi:[0,1]\n"
    "v_pk_add_f32 v[98:99], v[78:79], s[68:69] op_sel_hi:[1,0] neg_lo:[0,1] neg_hi:[0,1]\n"
    "v_pk_add_f32 v[48:49], v[92:93], v[94:95]\n"
    "v_pk_mul_f32 v[50:51], v[92:93], v[92:93]\n"
    "v_pk_add_f32 v[52:53], v[96:97], v[98:99]\n"
    "v_pk_fma_f32 v[50:51], v[94:95], v[94:95], v[50:51]\n"
    "v_pk_add_f32 v[48:49], v[48:49], v[52:53]\n"
    "v_pk_fma_f32 v[50:51], v[96:97], v[96:97], v[50:51]\n"
    "v_add_f32 v48, v48, v49\n"
    "v_pk_fma_f32 v[50:51], v[98:99], v[98:99], v[50:51]\n"
    "v_mov_b32 v52, s68\n"
    "v_add_f32 v49, v50, v51\n"
    "v_add_f32_dpp v48, v48, v48 row_ror:8 row_mask:0xf bank_mask:0xf\n"
    "s_nop 0\n"
    "v_add_f32_dpp v49, v49, v49 row_ror:8 row_mask:0xf bank_mask:0xf\n"
    "v_pk_mul_f32 v[56:57], v[80:81], v[32:33]\n"
    "v_add_f32_dpp v48, v48, v48 row_ror:4 row_mask:0xf bank_mask:0xf\n"
    "v_add_f32_dpp v49, v49, v49 row_ror:4 row_mask:0xf bank_mask:0xf\n"
    "v_pk_fma_f32 v[56:57], v[82:83], v[34:35], v[56:57]\n"
    "v_add_f32_dpp v48, v48, v48 row_ror:2 row_mask:0xf bank_mask:0xf\n"
    "v_add_f32_dpp v49, v49, v49 row_ror:2 row_mask:0xf bank_mask:0xf\n"
    "v_pk_fma_f32 v[56:57], v[84:85], v[36:37], v[56:57]\n"
    "v_add_f32_dpp v48, v48, v48 row_ror:1 row_mask:0xf bank_mask:0xf\n"
    "v_add_f32_dpp v49, v49, v49 row_ror:1 row_mask:0xf bank_mask:0xf\n"
    "v_pk_fma_f32 v[56:57], v[86:87], v[38:39], v[56:57]\n"
    "s_nop 0\n"
    "v_permlane16_swap_b32 v48, v49\n"
    "v_add_f32 v48, v48, v49\n"
    "v_mov_b32 v49, v48\n"
    "s_nop 1\n"
    "v_permlane32_swap_b32 v48, v49\n"
    "v_add_f32 v48, v48, v49\n"
    "s_nop 0\n"
    "v_readlane_b32 s62, v48, 0\n"
    "v_readlane_b32 s63, v48, 16\n"
    "s_nop 0\n"
    "v_mul_f32 v48, s62, v44\n"
    "v_mul_f32 v49, s63, v44\n"
    "v_fma_f32 v49, -v48, v48, v49\n"
    "v_add_f32 v52, v52, v48\n"
    "v_max_f32 v49, 0, v49\n"
    "v_cvt_pk_bf16_f32 v48, v52, v49\n"
    "v_and_b32 v49, 0xffff0000, v48\n"
    "v_add_f32 v49, 0x3727c5ac, v49\n"
    "v_cvt_pk_bf16_f32 v49, v49, v49\n"
    "v_lshlrev_b32 v49, 16, v49\n"
    "v_sqrt_f32 v50, v49\n"
    "v_lshlrev_b32 v48, 16, v48\n"
    "v_cvt_pk_bf16_f32 v50, v50, v50\n"
    "v_lshlrev_b32 v50, 16, v50\n"
    "v_rcp_f32 v50, v50\n"
    "v_pk_add_f32 v[92:93], v[72:73], v[48:49] op_sel_hi:[1,0] neg_lo:[0,1] neg_hi:[0,1]\n"
    "v_pk_add_f32 v[94:95], v[74:75], v[48:49] op_sel_hi:[1,0] neg_lo:[0,1] neg_hi:[0,1]\n"
    "v_pk_add_f32 v[96:97], v[76:77], v[48:49] op_sel_hi:[1,0] neg_lo:[0,1] neg_hi:[0,1]\n"
    "v_pk_add_f32 v[98:99], v[78:79], v[48:49] op_sel_hi:[1,0] neg_lo:[0,1] neg_hi:[0,1]\n"
    "v_pk_mul_f32 v[64:65], v[92:93], v[50:51] op_sel_hi:[1,0]\n"
    "v_pk_mul_f32 v[66:67], v[94:95], v[50:51] op_sel_hi:[1,0]\n"
    "v_pk_mul_f32 v[68:69], v[96:97], v[50:51] op_sel_hi:[1,0]\n"
    "v_pk_mul_f32 v[70:71], v[98:99], v[50:51] op_sel_hi:[1,0]\n"
    "v_cvt_pk_bf16_f32 v95, v70, v71\n"
    "v_cvt_pk_bf16_f32 v92, v64, v65\n"
    "v_cvt_pk_bf16_f32 v93, v66, v67\n"
    "v_cvt_pk_bf16_f32 v94, v68, v69\n"
    "v_min_u32 v95, -2, v95\n"
    "buffer_store_dwordx4 v[92:95], v0, s[16:19], s78 offen offset:0 sc1\n"
    "s_lshl_b32 s70, s47, 6\n"
    "s_add_u32 s70, s70, s58\n"
    "v_add_f32_dpp v56, v56, v56 row_ror:8 row_mask:0xf bank_mask:0xf\n"
    "v_add_f32_dpp v57, v57, v57 row_ror:8 row_mask:0xf bank_mask:0xf\n"
    "s_nop 0\n"
    "v_add_f32_dpp v56, v56, v56 row_ror:4 row_mask:0xf bank_mask:0xf\n"
    "v_add_f32_dpp v57, v57, v57 row_ror:4 row_mask:0xf bank_mask:0xf\n"
    "s_nop 0\n"
    "v_add_f32_dpp v56, v56, v56 row_ror:2 row_mask:0xf bank_mask:0xf\n"
    "v_add_f32_dpp v57, v57, v57 row_ror:2 row_mask:0xf bank_mask:0xf\n"
    "s_nop 0\n"
    "v_add_f32_dpp v56, v56, v56 row_ror:1 row_mask:0xf bank_mask:0xf\n"
    "v_add_f32_dpp v57, v57, v57 row_ror:1 row_mask:0xf bank_mask:0xf\n"
    "s_nop 1\n"
    "v_permlane16_swap_b32 v56, v57\n"
    "v_add_f32 v56, v56, v57\n"
    "v_mov_b32 v57, v56\n"
    "s_nop 1\n"
    "v_permlane32_swap_b32 v56, v57\n"
    "v_add_f32 v56, v56, v57\n"
    "s_nop 0\n"
    "v_readlane_b32 s62, v56, 0\n"
    "v_readlane_b32 s63, v56, 16\n"
    "s_sub_u32 s39, 0xffff, s33\n"
    "v_mov_b32 v56, s62\n"
    "v_add_f32 v56, s63, v56\n"
    "v_cvt_pk_bf16_f32 v56, v56, v56\n"
    "v_lshlrev_b32 v56, 16, v56\n"
    "v_cmp_o_f32 vcc, v56, v56\n"
    "v_ashrrev_i32 v57, 31, v56\n"
    "v_or_b32 v57, 0x80000000, v57\n"
    "v_xor_b32 v56, v56, v57\n"
    "v_cndmask_b32 v56, 0, v56, vcc\n"
    "v_and_b32 v56, 0xffff0000, v56\n"
    "v_or_b32 v56, s39, v56\n"
    "s_mov_b64 exec, 1\n"
    "buffer_atomic_umax v56, off, s[16:19], s70 offset:4 sc1\n"
    "s_mov_b64 exec, -1\n"
    "buffer_store_dwordx4 v[88:91], v0, s[16:19], s32 offen offset:1024 sc1\n"
    "buffer_store_dwordx4 v[124:127], v0, s[16:19], s32 offen offset:0 sc1\n"
    "s_add_u32 s78, s78, 4194304\n"
    "buffer_store_dwordx4 v[60:63], v0, s[16:19], s78 offen offset:0 sc1\n"
    "v_mov_b64 v[120:121], 1\n"
    "v_mad_u32_u24 v41, v3, s41, v2\n"
    "v_mad_u32_u24 v41, v5, s40, v41\n"
    "s_nop 7\n"
    "s_nop 7\n"
    "s_nop 7\n"
    "buffer_load_dword v40, v41, s[16:19], 0 offen sc1\n"
    "s_mov_b32 s73, s47\n"
    "s_mov_b64 exec, 1\n"
    "s_waitcnt vmcnt(1)\n"
    "buffer_atomic_add_x2 v[120:121], off, s[16:19], s70 sc0 sc1\n"
    "s_mov_b64 exec, -1\n"
    "s_mov_b32 s47, -1\n"
    "s_mov_b32 s37, 0\n"
    "s_cmp_ge_i32 s50, 0\n"
    "s_cbranch_scc1 LAB%=\n"
    "s_waitcnt vmcnt(1)\n"
    "v_readlane_b32 s52, v40, 32\n"
    "v_readlane_b32 s53, v40, 0\n"
    "v_readlane_b32 s54, v40, 16\n"
    "s_cmp_eq_u32 s52, 0\n"
    "s_cselect_b32 s38, s53, 0\n"
    "s_cmp_lg_u32 s38, 0\n"
    "s_cbranch_scc1 LAC%=\n"
    "buffer_load_dword v40, v41, s[16:19], 0 offen sc1\n"
    "s_waitcnt vmcnt(1)\n"
    "v_readfirstlane_b32 s38, v120\n"
    "v_readfirstlane_b32 s52, v121\n"
    "s_lshl_b32 s39, s73, 2\n"
    "s_cmp_eq_u32 s38, 31\n"
    "v_mov_b32 v120, s52\n"
    "s_cselect_b32 s39, s39, 0x80000000\n"
    "s_cselect_b32 s73, s73, -1\n"
    "s_mov_b64 exec, 0x38\n"
    "buffer_store_dword v120, v11, s[16:19], s39 offen sc1\n"
    "s_mov_b64 exec, -1\n"
    "s_waitcnt vmcnt(1)\n"
    "LAD%=:\n"
    "s_cmp_lg_u32 s40, s73\n"
    "s_cbranch_scc1 LTOP%=\n"
    "v_writelane_b32 v40, s52, 32\n"
    "s_nop 0\n"
    "s_branch LTOP%=\n"
    "LAC%=:\n"
    "s_mov_b32 s50, s41\n"
    "s_add_u32 s41, s41, 1\n"
    "s_lshl_b32 s62, s50, 18\n"
    "s_add_u32 s62, s62, 180355072\n"
    "s_lshl_b32 s63, s50, 5\n"
    "s_add_u32 s63, s63, s55\n"
    "s_lshl_b32 s63, s63, 10\n"
    "s_lshr_b32 s38, s62, 8\n"
    "s_xor_b32 s38, s38, s57\n"
    "s_and_b32 s38, s38, 0x2000\n"
    "s_or_b32 s62, s62, s38\n"
    "buffer_load_dwordx4 v[116:119], v0, s[20:23], s63 offen\n"
    "buffer_load_dwordx4 v[100:103], v1, s[16:19], s62 offen sc1\n"
    "s_add_u32 s62, s62, 0x10000\n"
    "buffer_load_dwordx4 v[104:107], v1, s[16:19], s62 offen sc1\n"
    "s_add_u32 s62, s62, 0x10000\n"
    "buffer_load_dwordx4 v[108:111], v1, s[16:19], s62 offen sc1\n"
    "s_add_u32 s62, s62, 0x10000\n"
    "buffer_load_dwordx4 v[112:115], v1, s[16:19], s62 offen sc1\n"
    "s_waitcnt vmcnt(5)\n"
    "v_readfirstlane_b32 s38, v120\n"
    "v_readfirstlane_b32 s52, v121\n"
    "s_lshl_b32 s39, s73, 2\n"
    "s_cmp_eq_u32 s38, 31\n"
    "v_mov_b32 v120, s52\n"
    "s_cselect_b32 s39, s39, 0x80000000\n"
    "s_cselect_b32 s73, s73, -1\n"
    "s_mov_b64 exec, 0x38\n"
    "buffer_store_dword v120, v11, s[16:19], s39 offen sc1\n"
    "s_mov_b64 exec, -1\n"
    "s_cmp_lg_u32 s40, s73\n"
    "s_cbranch_scc1 LAE%=\n"
    "s_lshr_b32 s38, s52, 16\n"
    "s_cmp_gt_i32 s38, s46\n"
    "s_cbranch_scc0 LAE%=\n"
    "s_sub_u32 s41, s41, 1\n"
    "s_waitcnt vmcnt(1)\n"
    "s_branch LDEC%=\n"
    "LAE%=:\n"
    "s_lshl_b32 s60, s50, 5\n"
    "s_add_u32 s60, s60, s55\n"
    "s_lshl_b32 s39, s60, 11\n"
    "s_andn2_b32 s32, s39, 0x1fff\n"
    "s_add_u32 s32, s32, s39\n"
    "s_add_u32 s32, s32, 245366784\n"
    "s_lshr_b32 s38, s32, 8\n"
    "s_xor_b32 s38, s38, s57\n"
    "s_and_b32 s38, s38, 0x2000\n"
    "s_or_b32 s32, s32, s38\n"
    "s_lshl_b32 s39, s60, 10\n"
    "s_andn2_b32 s78, s39, 0x1fff\n"
    "s_add_u32 s78, s78, s39\n"
    "s_add_u32 s78, s78, 236978176\n"
    "s_lshr_b32 s38, s78, 8\n"
    "s_xor_b32 s38, s38, s57\n"
    "s_and_b32 s38, s38, 0x2000\n"
    "s_or_b32 s78, s78, s38\n"
    "s_mov_b32 s33, s60\n"
    "s_waitcnt vmcnt(5)\n"
    "v_lshlrev_b32 v64, 16, v116\n"
    "v_and_b32 v65, 0xffff0000, v116\n"
    "v_lshlrev_b32 v66, 16, v117\n"
    "v_and_b32 v67, 0xffff0000, v117\n"
    "v_lshlrev_b32 v68, 16, v118\n"
    "v_and_b32 v69, 0xffff0000, v118\n"
    "v_lshlrev_b32 v70, 16, v119\n"
    "v_and_b32 v71, 0xffff0000, v119\n"
    "s_waitcnt vmcnt(4)\n"
    "v_mfma_f32_16x16x32_bf16 v[64:67], v[24:27], v[100:103], v[64:67]\n"
    "v_mfma_f32_16x16x32_bf16 v[68:71], v[28:31], v[100:103], v[68:71]\n"
    "s_waitcnt vmcnt(3)\n"
    "v_mfma_f32_16x16x32_bf16 v[64:67], v[24:27], v[104:107], v[64:67]\n"
    "v_mfma_f32_16x16x32_bf16 v[68:71], v[28:31], v[104:107], v[68:71]\n"
    "s_waitcnt vmcnt(2)\n"
    "v_mfma_f32_16x16x32_bf16 v[64:67], v[24:27], v[108:111], v[64:67]\n"
    "v_mfma_f32_16x16x32_bf16 v[68:71], v[28:31], v[108:111], v[68:71]\n"
    "v_max3_u32 v48, v103, v107, v111\n"
    "s_waitcnt vmcnt(1)\n"
    "v_mfma_f32_16x16x32_bf16 v[64:67], v[24:27], v[112:115], v[64:67]\n"
    "v_mfma_f32_16x16x32_bf16 v[68:71], v[28:31], v[112:115], v[68:71]\n"
    "v_max_u32 v48, v48, v115\n"
    "v_cmp_eq_u32 vcc, -1, v48\n"
    "s_mov_b32 s47, s50\n"
    "s_mov_b32 s48, s51\n"
    "s_mov_b32 s50, -1\n"
    "s_cbranch_vccnz LEARLY%=\n"
    "s_branch LNXT%=\n"
    "LAB%=:\n"
    "s_waitcnt vmcnt(0)\n"
    "v_readfirstlane_b32 s38, v120\n"
    "v_readfirstlane_b32 s52, v121\n"
    "s_lshl_b32 s39, s73, 2\n"
    "s_cmp_eq_u32 s38, 31\n"
    "v_mov_b32 v120, s52\n"
    "s_cselect_b32 s39, s39, 0x80000000\n"
    "s_cselect_b32 s73, s73, -1\n"
    "s_mov_b64 exec, 0x38\n"
    "buffer_store_dword v120, v11, s[16:19], s39 offen sc1\n"
    "s_mov_b64 exec, -1\n"
    "s_lshl_b32 s60, s50, 5\n"
    "s_add_u32 s60, s60, s55\n"
    "s_lshl_b32 s39, s60, 11\n"
    "s_andn2_b32 s32, s39, 0x1fff\n"
    "s_add_u32 s32, s32, s39\n"
    "s_add_u32 s32, s32, 245366784\n"
    "s_lshr_b32 s38, s32, 8\n"
    "s_xor_b32 s38, s38, s57\n"
    "s_and_b32 s38, s38, 0x2000\n"
    "s_or_b32 s32, s32, s38\n"
    "s_lshl_b32 s39, s60, 10\n"
    "s_andn2_b32 s78, s39, 0x1fff\n"
    "s_add_u32 s78, s78, s39\n"
    "s_add_u32 s78, s78, 236978176\n"
    "s_lshr_b32 s38, s78, 8\n"
    "s_xor_b32 s38, s38, s57\n"
    "s_and_b32 s38, s38, 0x2000\n"
    "s_or_b32 s78, s78, s38\n"
    "s_mov_b32 s33, s60\n"
    "v_lshlrev_b32 v64, 16, v116\n"
    "v_and_b32 v65, 0xffff0000, v116\n"
    "v_lshlrev_b32 v66, 16, v117\n"
    "v_and_b32 v67, 0xffff0000, v117\n"
    "v_lshlrev_b32 v68, 16, v118\n"
    "v_and_b32 v69, 0xffff0000, v118\n"
    "v_lshlrev_b32 v70, 16, v119\n"
    "v_and_b32 v71, 0xffff0000, v119\n"
    "v_mfma_f32_16x16x32_bf16 v[64:67], v[24:27], v[100:103], v[64:67]\n"
    "s_nop 0\n"
    "v_mfma_f32_16x16x32_bf16 v[68:71], v[28:31], v[100:103], v[68:71]\n"
    "v_mfma_f32_16x16x32_bf16 v[64:67], v[24:27], v[104:107], v[64:67]\n"
    "v_mfma_f32_16x16x32_bf16 v[68:71], v[28:31], v[104:107], v[68:71]\n"
    "v_mfma_f32_16x16x32_bf16 v[64:67], v[24:27], v[108:111], v[64:67]\n"
    "v_mfma_f32_16x16x32_bf16 v[68:71], v[28:31], v[108:111], v[68:71]\n"
    "v_max3_u32 v48, v103, v107, v111\n"
    "v_mfma_f32_16x16x32_bf16 v[64:67], v[24:27], v[112:115], v[64:67]\n"
    "v_mfma_f32_16x16x32_bf16 v[68:71], v[28:31], v[112:115], v[68:71]\n"
    "v_max_u32 v48, v48, v115\n"
    "v_cmp_eq_u32 vcc, -1, v48\n"
    "s_mov_b32 s47, s50\n"
    "s_mov_b32 s48, s51\n"
    "s_mov_b32 s50, -1\n"
    "s_cbranch_vccnz LEARLY%=\n"
    "s_branch LAD%=\n"
    "LMID%=:\n"
    "s_cmp_ge_i32 s50, 0\n"
    "s_subb_u32 s41, s41, 0\n"
    "s_branch LDEC%=\n"
    "LNONE%=:\n"
    "s_cmp_ge_i32 s47, 0\n"
    "s_cbranch_scc1 LJOB%=\n"
    "s_branch LIDLE%=\n"
    "LEARLY%=:\n"
    "s_mov_b32 s41, s47\n"
    "s_mov_b32 s53, 1\n"
    "s_mov_b32 s47, -1\n"
    "s_and_b32 s38, s37, 3\n"
    "s_cmp_eq_u32 s38, 3\n"
    "s_cbranch_scc1 LIDLE%=\n"
    "s_sleep 2\n"
    "s_add_u32 s37, s37, 1\n"
    "s_cmp_lt_u32 s37, 0x80000\n"
    "s_cbranch_scc1 LPICK%=\n"
    "s_branch LFAIL%=\n"
    "LDEC%=:\n"
    "s_mov_b32 s70, s40\n"
    "s_cmp_eq_u32 s40, 64\n"
    "s_cbranch_scc1 LMLP%=\n"
    "s_add_u32 s40, s40, 1\n"
    "s_mov_b32 s37, 0\n"
    "s_lshr_b32 s38, s52, 16\n"
    "s_cmp_gt_i32 s38, s46\n"
    "s_cselect_b32 s70, s70, -1\n"
    "s_cselect_b32 s46, s38, s46\n"
    "s_addc_u32 s45, s45, 0\n"
    "s_cmp_lt_i32 s70, 0\n"
    "s_cbranch_scc1 LPICK%=\n"
    "s_mov_b32 s43, s70\n"
    "s_andn2_b32 s44, 0x7ff, s52\n"
    "LMLP%=:\n"
    "s_andn2_b32 s71, 0x7ff, s52\n"
    "s_lshr_b32 s38, s71, 5\n"
    "s_cmp_ge_u32 s38, s43\n"
    "s_cselect_b32 s63, s57, s56\n"
    "s_cselect_b32 s38, s70, 0\n"
    "s_cmp_eq_u32 s38, 64\n"
    "s_cselect_b32 s62, 8388608, 0\n"
    "s_add_u32 s62, s62, 245366784\n"
    "s_lshl_b32 s39, s71, 11\n"
    "s_andn2_b32 s61, s39, 0x1fff\n"
    "s_add_u32 s61, s61, s39\n"
    "s_add_u32 s61, s61, s62\n"
    "s_lshr_b32 s38, s61, 8\n"
    "s_xor_b32 s38, s38, s63\n"
    "s_and_b32 s38, s38, 0x2000\n"
    "s_or_b32 s61, s61, s38\n"
    "buffer_load_dwordx4 v[116:119], v0, s[16:19], s61 offen offset:1024 sc1\n"
    "s_lshl_b32 s39, s70, 12\n"
    "s_andn2_b32 s61, s39, 0x1fff\n"
    "s_add_u32 s61, s61, s39\n"
    "s_add_u32 s61, s61, 263323648\n"
    "s_lshr_b32 s38, s61, 8\n"
    "s_xor_b32 s38, s38, s57\n"
    "s_and_b32 s38, s38, 0x2000\n"
    "s_or_b32 s61, s61, s38\n"
    "v_mov_b64 v[48:49], 0\n"
    "v_mov_b64 v[50:51], 0\n"
    "v_mov_b64 v[52:53], 0\n"
    "v_mov_b64 v[54:55], 0\n"
    "v_mov_b64 v[56:57], 0\n"
    "v_mov_b64 v[58:59], 0\n"
    "v_mov_b64 v[60:61], 0\n"
    "v_mov_b64 v[62:63], 0\n"
    "v_mov_b64 v[104:105], 0\n"
    "v_mov_b64 v[106:107], 0\n"
    "v_mov_b64 v[108:109], 0\n"
    "v_mov_b64 v[110:111], 0\n"
    "s_waitcnt vmcnt(0)\n"
    "ds_write_b128 v8, v[116:119]\n"
    "ds_read_b128 v[72:75], v7 offset:0\n"
    "ds_read_b128 v[76:79], v7 offset:128\n"
    "ds_read_b128 v[80:83], v7 offset:256\n"
    "ds_read_b128 v[84:87], v7 offset:384\n"
    "ds_read_b128 v[88:91], v7 offset:512\n"
    "ds_read_b128 v[92:95], v7 offset:640\n"
    "ds_read_b128 v[96:99], v7 offset:768\n"
    "ds_read_b128 v[100:103], v7 offset:896\n"
    "s_waitcnt lgkmcnt(7)\n"
    "v_smfmac_f32_16x16x64_bf16 v[48:51], v[72:75], a[0:7], v6\n"
    "v_smfmac_f32_16x16x64_bf16 v[52:55], v[72:75], a[8:15], v6\n"
    "v_smfmac_f32_16x16x64_bf16 v[56:59], v[72:75], a[16:23], v6\n"
    "v_smfmac_f32_16x16x64_bf16 v[60:63], v[72:75], a[24:31], v6\n"
    "v_smfmac_f32_16x16x64_bf16 v[104:107], v[72:75], v[128:135], v6\n"
    "v_smfmac_f32_16x16x64_bf16 v[108:111], v[72:75], v[136:143], v6\n"
    "s_waitcnt lgkmcnt(6)\n"
    "v_smfmac_f32_16x16x64_bf16 v[48:51], v[76:79], a[32:39], v6\n"
    "v_smfmac_f32_16x16x64_bf16 v[52:55], v[76:79], a[40:47], v6\n"
    "v_smfmac_f32_16x16x64_bf16 v[56:59], v[76:79], a[48:55], v6\n"
    "v_smfmac_f32_16x16x64_bf16 v[60:63], v[76:79], a[56:63], v6\n"
    "v_smfmac_f32_16x16x64_bf16 v[104:107], v[76:79], v[144:151], v6\n"
    "v_smfmac_f32_16x16x64_bf16 v[108:111], v[76:79], v[152:159], v6\n"
    "s_waitcnt lgkmcnt(5)\n"
    "v_smfmac_f32_16x16x64_bf16 v[48:51], v[80:83], a[64:71], v6\n"
    "v_smfmac_f32_16x16x64_bf16 v[52:55], v[80:83], a[72:79], v6\n"
    "v_smfmac_f32_16x16x64_bf16 v[56:59], v[80:83], a[80:87], v6\n"
    "v_smfmac_f32_16x16x64_bf16 v[60:63], v[80:83], a[88:95], v6\n"
    "v_smfmac_f32_16x16x64_bf16 v[104:107], v[80:83], v[160:167], v6\n"
    "v_smfmac_f32_16x16x64_bf16 v[108:111], v[80:83], v[168:175], v6\n"
    "s_waitcnt lgkmcnt(4)\n"
    "v_smfmac_f32_16x16x64_bf16 v[48:51], v[84:87], a[96:103], v6\n"
    "v_smfmac_f32_16x16x64_bf16 v[52:55], v[84:87], a[104:111], v6\n"
    "v_smfmac_f32_16x16x64_bf16 v[56:59], v[84:87], a[112:119], v6\n"
    "v_smfmac_f32_16x16x64_bf16 v[60:63], v[84:87], a[120:127], v6\n"
    "v_smfmac_f32_16x16x64_bf16 v[104:107], v[84:87], v[176:183], v6\n"
    "v_smfmac_f32_16x16x64_bf16 v[108:111], v[84:87], v[184:191], v6\n"
    "s_waitcnt lgkmcnt(3)\n"
    "v_smfmac_f32_16x16x64_bf16 v[48:51], v[88:91], a[128:135], v6\n"
    "v_smfmac_f32_16x16x64_bf16 v[52:55], v[88:91], a[136:143], v6\n"
    "v_smfmac_f32_16x16x64_bf16 v[56:59], v[88:91], a[144:151], v6\n"
    "v_smfmac_f32_16x16x64_bf16 v[60:63], v[88:91], a[152:159], v6\n"
    "v_smfmac_f32_16x16x64_bf16 v[104:107], v[88:91], v[192:199], v6\n"
    "v_smfmac_f32_16x16x64_bf16 v[108:111], v[88:91], v[200:207], v6\n"
    "s_waitcnt lgkmcnt(2)\n"
    "v_smfmac_f32_16x16x64_bf16 v[48:51], v[92:95], a[160:167], v6\n"
    "v_smfmac_f32_16x16x64_bf16 v[52:55], v[92:95], a[168:175], v6\n"
    "v_smfmac_f32_16x16x64_bf16 v[56:59], v[92:95], a[176:183], v6\n"
    "v_smfmac_f32_16x16x64_bf16 v[60:63], v[92:95], a[184:191], v6\n"
    "v_smfmac_f32_16x16x64_bf16 v[104:107], v[92:95], v[208:215], v6\n"
    "v_smfmac_f32_16x16x64_bf16 v[108:111], v[92:95], v[216:223], v6\n"
    "s_waitcnt lgkmcnt(1)\n"
    "v_smfmac_f32_16x16x64_bf16 v[48:51], v[96:99], a[192:199], v6\n"
    "v_smfmac_f32_16x16x64_bf16 v[52:55], v[96:99], a[200:207], v6\n"
    "v_smfmac_f32_16x16x64_bf16 v[56:59], v[96:99], a[208:215], v6\n"
    "v_smfmac_f32_16x16x64_bf16 v[60:63], v[96:99], a[216:223], v6\n"
    "v_smfmac_f32_16x16x64_bf16 v[104:107], v[96:99], v[224:231], v6\n"
    "v_smfmac_f32_16x16x64_bf16 v[108:111], v[96:99], v[232:239], v6\n"
    "s_waitcnt lgkmcnt(0)\n"
    "v_smfmac_f32_16x16x64_bf16 v[48:51], v[100:103], a[224:231], v6\n"
    "v_smfmac_f32_16x16x64_bf16 v[52:55], v[100:103], a[232:239], v6\n"
    "v_smfmac_f32_16x16x64_bf16 v[56:59], v[100:103], a[240:247], v6\n"
    "v_smfmac_f32_16x16x64_bf16 v[60:63], v[100:103], a[248:255], v6\n"
    "v_smfmac_f32_16x16x64_bf16 v[104:107], v[100:103], v[240:247], v6\n"
    "v_smfmac_f32_16x16x64_bf16 v[108:111], v[100:103], v[248:255], v6\n"
    "v_add_f32 v48, v48, v50\n"
    "v_add_f32 v52, v52, v54\n"
    "v_add_f32 v56, v56, v58\n"
    "v_add_f32 v60, v60, v62\n"
    "v_cndmask_b32 v48, v48, v56, s[74:75]\n"
    "v_cndmask_b32 v52, v52, v60, s[74:75]\n"
    "s_mov_b32 vcc_lo, -1\n"
    "s_mov_b32 vcc_hi, 0\n"
    "v_add_f32 v104, v104, v106\n"
    "v_add_f32 v108, v108, v110\n"
    "v_cndmask_b32 v104, v104, v48, vcc\n"
    "v_cndmask_b32 v108, v108, v52, vcc\n"
    "v_cvt_pk_bf16_f32 v108, v104, v108\n"
    "v_lshlrev_b32 v104, 16, v108\n"
    "v_mul_f32 v105, 0xbfb8aa3b, v104\n"
    "v_exp_f32 v105, v105\n"
    "v_and_b32 v108, 0xffff0000, v108\n"
    "v_add_f32 v105, 1.0, v105\n"
    "v_rcp_f32 v105, v105\n"
    "s_nop 0\n"
    "v_mul_f32 v104, v104, v105\n"
    "v_cvt_pk_bf16_f32 v104, v104, v104\n"
    "v_lshlrev_b32 v104, 16, v104\n"
    "v_mul_f32 v104, v104, v108\n"
    "s_nop 1\n"
    "v_mov_b32_dpp v105, v104 quad_perm:[1,1,3,3] row_mask:0xf bank_mask:0xf\n"
    "v_cvt_pk_bf16_f32 v104, v104, v105\n"
    "v_min_u32 v104, -2, v104\n"
    "buffer_store_dword v104, v9, s[16:19], s61 offen sc1\n"
    "s_cmp_lg_u32 s70, 64\n"
    "s_cbranch_scc1 LPOLL%=\n"
    "s_branch LEND%=\n"
    "LFAIL%=:\n"
    "buffer_store_dword v6, off, s[16:19], s59 sc1\n"
    "LEND%=:\n"
    "s_endpgm\n"
    :
    : "{v0}"(hv0), "{v1}"(hv1), "{v2}"(hv2), "{v3}"(hv3), "{v4}"(hv4), "{v5}"(hv5), "{v6}"(hv6), "{v7}"(hv7), "{v8}"(hv8), "{v9}"(hv9), "{v10}"(hv10), "{v11}"(hv11), "{v12}"(hv12), "{v13}"(hv13), "{v14}"(hv14), "{v15}"(hv15), "{v16}"(hv16), "{v17}"(hv17), "{v18}"(hv18), "{v19}"(hv19), "{v20}"(hv20), "{v21}"(hv21)
    : "v255", "a255", "s101", "vcc", "scc", "memory");
  } else {
  asm volatile(
    "s_nop 1\n"
    "v_readlane_b32 s36, v15, 0\n"
    "v_readlane_b32 s55, v15, 1\n"
    "v_readlane_b32 s58, v15, 2\n"
    "v_readlane_b32 s59, v15, 3\n"
    "v_readlane_b32 s16, v15, 4\n"
    "v_readlane_b32 s17, v15, 5\n"
    "v_readlane_b32 s18, v15, 6\n"
    "v_readlane_b32 s20, v15, 7\n"
    "v_readlane_b32 s21, v15, 8\n"
    "v_readlane_b32 s24, v15, 9\n"
    "v_readlane_b32 s25, v15, 10\n"
    "v_readlane_b32 s90, v15, 11\n"
    "v_readlane_b32 s91, v15, 12\n"
    "v_readlane_b32 s92, v15, 13\n"
    "v_readlane_b32 s93, v15, 14\n"
    "v_readlane_b32 s94, v15, 15\n"
    "v_readlane_b32 s95, v15, 16\n"
    "v_readlane_b32 s96, v15, 17\n"
    "v_readlane_b32 s97, v15, 18\n"
    "v_readlane_b32 s98, v15, 19\n"
    "v_readlane_b32 s99, v15, 20\n"
    "v_readlane_b32 s72, v15, 21\n"
    "s_mov_b32 s19, 0x20000\n"
    "s_mov_b32 s23, 0x20000\n"
    "s_mov_b32 s27, 0x20000\n"
    "s_mov_b32 s31, 0x20000\n"
    "s_mov_b32 s22, 0x200000\n"
    "s_mov_b32 s26, 0x200000\n"
    "s_mov_b32 s30, 1835008\n"
    "s_nop 3\n"
    "global_load_dwordx4 v[116:119], v0, s[98:99]\n"
    "s_lshl_b32 s62, s36, 11\n"
    "s_mov_b32 s28, s90\n"
    "s_mov_b32 s29, s91\n"
    "s_add_u32 m0, s62, 0\n"
    "s_mov_b32 s38, 0\n"
    "buffer_load_dwordx4 v16, s[28:31], s38 offen lds\n"
    "s_add_u32 m0, s62, 1024\n"
    "s_mov_b32 s38, 28672\n"
    "buffer_load_dwordx4 v16, s[28:31], s38 offen lds\n"
    "s_add_u32 m0, s62, 8192\n"
    "s_mov_b32 s38, 229376\n"
    "buffer_load_dwordx4 v16, s[28:31], s38 offen lds\n"
    "s_add_u32 m0, s62, 9216\n"
    "s_mov_b32 s38, 258048\n"
    "buffer_load_dwordx4 v16, s[28:31], s38 offen lds\n"
    "s_mov_b32 s28, s90\n"
    "s_mov_b32 s29, s91\n"
    "s_add_u32 m0, s62, 16384\n"
    "s_mov_b32 s38, 458752\n"
    "buffer_load_dwordx4 v16, s[28:31], s38 offen lds\n"
    "s_add_u32 m0, s62, 17408\n"
    "s_mov_b32 s38, 487424\n"
    "buffer_load_dwordx4 v16, s[28:31], s38 offen lds\n"
    "s_add_u32 m0, s62, 24576\n"
    "s_mov_b32 s38, 688128\n"
    "buffer_load_dwordx4 v16, s[28:31], s38 offen lds\n"
    "s_add_u32 m0, s62, 25600\n"
    "s_mov_b32 s38, 716800\n"
    "buffer_load_dwordx4 v16, s[28:31], s38 offen lds\n"
    "s_mov_b32 s28, s90\n"
    "s_mov_b32 s29, s91\n"
    "s_add_u32 m0, s62, 32768\n"
    "s_mov_b32 s38, 917504\n"
    "buffer_load_dwordx4 v16, s[28:31], s38 offen lds\n"
    "s_add_u32 m0, s62, 33792\n"
    "s_mov_b32 s38, 946176\n"
    "buffer_load_dwordx4 v16, s[28:31], s38 offen lds\n"
    "s_add_u32 m0, s62, 40960\n"
    "s_mov_b32 s38, 1146880\n"
    "buffer_load_dwordx4 v16, s[28:31], s38 offen lds\n"
    "s_add_u32 m0, s62, 41984\n"
    "s_mov_b32 s38, 1175552\n"
    "buffer_load_dwordx4 v16, s[28:31], s38 offen lds\n"
    "s_mov_b32 s28, s90\n"
    "s_mov_b32 s29, s91\n"
    "s_add_u32 m0, s62, 49152\n"
    "s_mov_b32 s38, 1376256\n"
    "buffer_load_dwordx4 v16, s[28:31], s38 offen lds\n"
    "s_add_u32 m0, s62, 50176\n"
    "s_mov_b32 s38, 1404928\n"
    "buffer_load_dwordx4 v16, s[28:31], s38 offen lds\n"
    "s_add_u32 m0, s62, 57344\n"
    "s_mov_b32 s38, 1605632\n"
    "buffer_load_dwordx4 v16, s[28:31], s38 offen lds\n"
    "s_add_u32 m0, s62, 58368\n"
    "s_mov_b32 s38, 1634304\n"
    "buffer_load_dwordx4 v16, s[28:31], s38 offen lds\n"
    "s_mov_b32 s28, s92\n"
    "s_mov_b32 s29, s93\n"
    "s_add_u32 m0, s62, 65536\n"
    "s_mov_b32 s38, 0\n"
    "buffer_load_dwordx4 v16, s[28:31], s38 offen lds\n"
    "s_add_u32 m0, s62, 66560\n"
    "s_mov_b32 s38, 28672\n"
    "buffer_load_dwordx4 v16, s[28:31], s38 offen lds\n"
    "s_add_u32 m0, s62, 73728\n"
    "s_mov_b32 s38, 229376\n"
    "buffer_load_dwordx4 v16, s[28:31], s38 offen lds\n"
    "s_add_u32 m0, s62, 74752\n"
    "s_mov_b32 s38, 258048\n"
    "buffer_load_dwordx4 v16, s[28:31], s38 offen lds\n"
    "s_mov_b32 s28, s92\n"
    "s_mov_b32 s29, s93\n"
    "s_add_u32 m0, s62, 81920\n"
    "s_mov_b32 s38, 458752\n"
    "buffer_load_dwordx4 v16, s[28:31], s38 offen lds\n"
    "s_add_u32 m0, s62, 82944\n"
    "s_mov_b32 s38, 487424\n"
    "buffer_load_dwordx4 v16, s[28:31], s38 offen lds\n"
    "s_add_u32 m0, s62, 90112\n"
    "s_mov_b32 s38, 688128\n"
    "buffer_load_dwordx4 v16, s[28:31], s38 offen lds\n"
    "s_add_u32 m0, s62, 91136\n"
    "s_mov_b32 s38, 716800\n"
    "buffer_load_dwordx4 v16, s[28:31], s38 offen lds\n"
    "s_mov_b32 s28, s92\n"
    "s_mov_b32 s29, s93\n"
    "s_add_u32 m0, s62, 98304\n"
    "s_mov_b32 s38, 917504\n"
    "buffer_load_dwordx4 v16, s[28:31], s38 offen lds\n"
    "s_add_u32 m0, s62, 99328\n"
    "s_mov_b32 s38, 946176\n"
    "buffer_load_dwordx4 v16, s[28:31], s38 offen lds\n"
    "s_add_u32 m0, s62, 106496\n"
    "s_mov_b32 s38, 1146880\n"
    "buffer_load_dwordx4 v16, s[28:31], s38 offen lds\n"
    "s_add_u32 m0, s62, 107520\n"
    "s_mov_b32 s38, 1175552\n"
    "buffer_load_dwordx4 v16, s[28:31], s38 offen lds\n"
    "s_waitcnt vmcnt(24)\n"
    "s_barrier\n"
    "s_mov_b32 s28, s92\n"
    "s_mov_b32 s29, s93\n"
    "s_add_u32 m0, s62, 114688\n"
    "s_mov_b32 s38, 1376256\n"
    "buffer_load_dwordx4 v16, s[28:31], s38 offen lds\n"
    "s_add_u32 m0, s62, 115712\n"
    "s_mov_b32 s38, 1404928\n"
    "buffer_load_dwordx4 v16, s[28:31], s38 offen lds\n"
    "s_add_u32 m0, s62, 122880\n"
    "s_mov_b32 s38, 1605632\n"
    "buffer_load_dwordx4 v16, s[28:31], s38 offen lds\n"
    "s_add_u32 m0, s62, 123904\n"
    "s_mov_b32 s38, 1634304\n"
    "buffer_load_dwordx4 v16, s[28:31], s38 offen lds\n"
    "v_add_u32 v48, 0, v14\n"
    "ds_read_b64_tr_b16 v[128:129], v48 offset:0\n"
    "ds_read_b64_tr_b16 v[130:131], v48 offset:256\n"
    "ds_read_b64_tr_b16 v[132:133], v48 offset:4096\n"
    "ds_read_b64_tr_b16 v[134:135], v48 offset:4352\n"
    "ds_read_b64_tr_b16 v[144:145], v48 offset:8192\n"
    "ds_read_b64_tr_b16 v[146:147], v48 offset:8448\n"
    "ds_read_b64_tr_b16 v[148:149], v48 offset:12288\n"
    "ds_read_b64_tr_b16 v[150:151], v48 offset:12544\n"
    "s_waitcnt lgkmcnt(0)\n"
    "s_waitcnt vmcnt(24)\n"
    "s_barrier\n"
    "s_mov_b32 s28, s94\n"
    "s_mov_b32 s29, s95\n"
    "s_add_u32 m0, s62, 0\n"
    "s_mov_b32 s38, 0\n"
    "buffer_load_dwordx4 v17, s[28:31], s38 offen lds\n"
    "s_add_u32 m0, s62, 1024\n"
    "s_mov_b32 s38, 8192\n"
    "buffer_load_dwordx4 v17, s[28:31], s38 offen lds\n"
    "s_add_u32 m0, s62, 8192\n"
    "s_mov_b32 s38, 65536\n"
    "buffer_load_dwordx4 v17, s[28:31], s38 offen lds\n"
    "s_add_u32 m0, s62, 9216\n"
    "s_mov_b32 s38, 73728\n"
    "buffer_load_dwordx4 v17, s[28:31], s38 offen lds\n"
    "v_add_u32 v48, 16384, v14\n"
    "ds_read_b64_tr_b16 v[160:161], v48 offset:0\n"
    "ds_read_b64_tr_b16 v[162:163], v48 offset:256\n"
    "ds_read_b64_tr_b16 v[164:165], v48 offset:4096\n"
    "ds_read_b64_tr_b16 v[166:167], v48 offset:4352\n"
    "ds_read_b64_tr_b16 v[176:177], v48 offset:8192\n"
    "ds_read_b64_tr_b16 v[178:179], v48 offset:8448\n"
    "ds_read_b64_tr_b16 v[180:181], v48 offset:12288\n"
    "ds_read_b64_tr_b16 v[182:183], v48 offset:12544\n"
    "s_waitcnt lgkmcnt(0)\n"
    "s_waitcnt vmcnt(24)\n"
    "s_barrier\n"
    "s_mov_b32 s28, s94\n"
    "s_mov_b32 s29, s95\n"
    "s_add_u32 m0, s62, 16384\n"
    "s_mov_b32 s38, 131072\n"
    "buffer_load_dwordx4 v17, s[28:31], s38 offen lds\n"
    "s_add_u32 m0, s62, 17408\n"
    "s_mov_b32 s38, 139264\n"
    "buffer_load_dwordx4 v17, s[28:31], s38 offen lds\n"
    "s_add_u32 m0, s62, 24576\n"
    "s_mov_b32 s38, 196608\n"
    "buffer_load_dwordx4 v17, s[28:31], s38 offen lds\n"
    "s_add_u32 m0, s62, 25600\n"
    "s_mov_b32 s38, 204800\n"
    "buffer_load_dwordx4 v17, s[28:31], s38 offen lds\n"
    "v_add_u32 v48, 32768, v14\n"
    "ds_read_b64_tr_b16 v[192:193], v48 offset:0\n"
    "ds_read_b64_tr_b16 v[194:195], v48 offset:256\n"
    "ds_read_b64_tr_b16 v[196:197], v48 offset:4096\n"
    "ds_read_b64_tr_b16 v[198:199], v48 offset:4352\n"
    "ds_read_b64_tr_b16 v[208:209], v48 offset:8192\n"
    "ds_read_b64_tr_b16 v[210:211], v48 offset:8448\n"
    "ds_read_b64_tr_b16 v[212:213], v48 offset:12288\n"
    "ds_read_b64_tr_b16 v[214:215], v48 offset:12544\n"
    "s_waitcnt lgkmcnt(0)\n"
    "s_waitcnt vmcnt(24)\n"
    "s_barrier\n"
    "s_mov_b32 s28, s94\n"
    "s_mov_b32 s29, s95\n"
    "s_add_u32 m0, s62, 32768\n"
    "s_mov_b32 s38, 262144\n"
    "buffer_load_dwordx4 v17, s[28:31], s38 offen lds\n"
    "s_add_u32 m0, s62, 33792\n"
    "s_mov_b32 s38, 270336\n"
    "buffer_load_dwordx4 v17, s[28:31], s38 offen lds\n"
    "s_add_u32 m0, s62, 40960\n"
    "s_mov_b32 s38, 327680\n"
    "buffer_load_dwordx4 v17, s[28:31], s38 offen lds\n"
    "s_add_u32 m0, s62, 41984\n"
    "s_mov_b32 s38, 335872\n"
    "buffer_load_dwordx4 v17, s[28:31], s38 offen lds\n"
    "v_add_u32 v48, 49152, v14\n"
    "ds_read_b64_tr_b16 v[224:225], v48 offset:0\n"
    "ds_read_b64_tr_b16 v[226:227], v48 offset:256\n"
    "ds_read_b64_tr_b16 v[228:229], v48 offset:4096\n"
    "ds_read_b64_tr_b16 v[230:231], v48 offset:4352\n"
    "ds_read_b64_tr_b16 v[240:241], v48 offset:8192\n"
    "ds_read_b64_tr_b16 v[242:243], v48 offset:8448\n"
    "ds_read_b64_tr_b16 v[244:245], v48 offset:12288\n"
    "ds_read_b64_tr_b16 v[246:247], v48 offset:12544\n"
    "s_waitcnt lgkmcnt(0)\n"
    "s_waitcnt vmcnt(24)\n"
    "s_barrier\n"
    "s_mov_b32 s28, s94\n"
    "s_mov_b32 s29, s95\n"
    "s_add_u32 m0, s62, 49152\n"
    "s_mov_b32 s38, 393216\n"
    "buffer_load_dwordx4 v17, s[28:31], s38 offen lds\n"
    "s_add_u32 m0, s62, 50176\n"
    "s_mov_b32 s38, 401408\n"
    "buffer_load_dwordx4 v17, s[28:31], s38 offen lds\n"
    "s_add_u32 m0, s62, 57344\n"
    "s_mov_b32 s38, 458752\n"
    "buffer_load_dwordx4 v17, s[28:31], s38 offen lds\n"
    "s_add_u32 m0, s62, 58368\n"
    "s_mov_b32 s38, 466944\n"
    "buffer_load_dwordx4 v17, s[28:31], s38 offen lds\n"
    "v_add_u32 v48, 65536, v14\n"
    "ds_read_b64_tr_b16 v[136:137], v48 offset:0\n"
    "ds_read_b64_tr_b16 v[138:139], v48 offset:256\n"
    "ds_read_b64_tr_b16 v[140:141], v48 offset:4096\n"
    "ds_read_b64_tr_b16 v[142:143], v48 offset:4352\n"
    "ds_read_b64_tr_b16 v[152:153], v48 offset:8192\n"
    "ds_read_b64_tr_b16 v[154:155], v48 offset:8448\n"
    "ds_read_b64_tr_b16 v[156:157], v48 offset:12288\n"
    "ds_read_b64_tr_b16 v[158:159], v48 offset:12544\n"
    "s_waitcnt lgkmcnt(0)\n"
    "s_waitcnt vmcnt(24)\n"
    "s_barrier\n"
    "s_mov_b32 s28, s94\n"
    "s_mov_b32 s29, s95\n"
    "s_add_u32 m0, s62, 65536\n"
    "s_mov_b32 s38, 524288\n"
    "buffer_load_dwordx4 v17, s[28:31], s38 offen lds\n"
    "s_add_u32 m0, s62, 66560\n"
    "s_mov_b32 s38, 532480\n"
    "buffer_load_dwordx4 v17, s[28:31], s38 offen lds\n"
    "s_add_u32 m0, s62, 73728\n"
    "s_mov_b32 s38, 589824\n"
    "buffer_load_dwordx4 v17, s[28:31], s38 offen lds\n"
    "s_add_u32 m0, s62, 74752\n"
    "s_mov_b32 s38, 598016\n"
    "buffer_load_dwordx4 v17, s[28:31], s38 offen lds\n"
    "v_add_u32 v48, 81920, v14\n"
    "ds_read_b64_tr_b16 v[168:169], v48 offset:0\n"
    "ds_read_b64_tr_b16 v[170:171], v48 offset:256\n"
    "ds_read_b64_tr_b16 v[172:173], v48 offset:4096\n"
    "ds_read_b64_tr_b16 v[174:175], v48 offset:4352\n"
    "ds_read_b64_tr_b16 v[184:185], v48 offset:8192\n"
    "ds_read_b64_tr_b16 v[186:187], v48 offset:8448\n"
    "ds_read_b64_tr_b16 v[188:189], v48 offset:12288\n"
    "ds_read_b64_tr_b16 v[190:191], v48 offset:12544\n"
    "s_waitcnt lgkmcnt(0)\n"
    "s_waitcnt vmcnt(24)\n"
    "s_barrier\n"
    "s_mov_b32 s28, s94\n"
    "s_mov_b32 s29, s95\n"
    "s_add_u32 m0, s62, 81920\n"
    "s_mov_b32 s38, 655360\n"
    "buffer_load_dwordx4 v17, s[28:31], s38 offen lds\n"
    "s_add_u32 m0, s62, 82944\n"
    "s_mov_b32 s38, 663552\n"
    "buffer_load_dwordx4 v17, s[28:31], s38 offen lds\n"
    "s_add_u32 m0, s62, 90112\n"
    "s_mov_b32 s38, 720896\n"
    "buffer_load_dwordx4 v17, s[28:31], s38 offen lds\n"
    "s_add_u32 m0, s62, 91136\n"
    "s_mov_b32 s38, 729088\n"
    "buffer_load_dwordx4 v17, s[28:31], s38 offen lds\n"
    "v_add_u32 v48, 98304, v14\n"
    "ds_read_b64_tr_b16 v[200:201], v48 offset:0\n"
    "ds_read_b64_tr_b16 v[202:203], v48 offset:256\n"
    "ds_read_b64_tr_b16 v[204:205], v48 offset:4096\n"
    "ds_read_b64_tr_b16 v[206:207], v48 offset:4352\n"
    "ds_read_b64_tr_b16 v[216:217], v48 offset:8192\n"
    "ds_read_b64_tr_b16 v[218:219], v48 offset:8448\n"
    "ds_read_b64_tr_b16 v[220:221], v48 offset:12288\n"
    "ds_read_b64_tr_b16 v[222:223], v48 offset:12544\n"
    "s_waitcnt lgkmcnt(0)\n"
    "s_waitcnt vmcnt(24)\n"
    "s_barrier\n"
    "s_mov_b32 s28, s94\n"
    "s_mov_b32 s29, s95\n"
    "s_add_u32 m0, s62, 98304\n"
    "s_mov_b32 s38, 786432\n"
    "buffer_load_dwordx4 v17, s[28:31], s38 offen lds\n"
    "s_add_u32 m0, s62, 99328\n"
    "s_mov_b32 s38, 794624\n"
    "buffer_load_dwordx4 v17, s[28:31], s38 offen lds\n"
    "s_add_u32 m0, s62, 106496\n"
    "s_mov_b32 s38, 851968\n"
    "buffer_load_dwordx4 v17, s[28:31], s38 offen lds\n"
    "s_add_u32 m0, s62, 107520\n"
    "s_mov_b32 s38, 860160\n"
    "buffer_load_dwordx4 v17, s[28:31], s38 offen lds\n"
    "v_add_u32 v48, 114688, v14\n"
    "ds_read_b64_tr_b16 v[232:233], v48 offset:0\n"
    "ds_read_b64_tr_b16 v[234:235], v48 offset:256\n"
    "ds_read_b64_tr_b16 v[236:237], v48 offset:4096\n"
    "ds_read_b64_tr_b16 v[238:239], v48 offset:4352\n"
    "ds_read_b64_tr_b16 v[248:249], v48 offset:8192\n"
    "ds_read_b64_tr_b16 v[250:251], v48 offset:8448\n"
    "ds_read_b64_tr_b16 v[252:253], v48 offset:12288\n"
    "ds_read_b64_tr_b16 v[254:255], v48 offset:12544\n"
    "s_waitcnt lgkmcnt(0)\n"
    "s_waitcnt vmcnt(24)\n"
    "s_barrier\n"
    "s_mov_b32 s28, s94\n"
    "s_mov_b32 s29, s95\n"
    "s_add_u32 m0, s62, 114688\n"
    "s_mov_b32 s38, 917504\n"
    "buffer_load_dwordx4 v17, s[28:31], s38 offen lds\n"
    "s_add_u32 m0, s62, 115712\n"
    "s_mov_b32 s38, 925696\n"
    "buffer_load_dwordx4 v17, s[28:31], s38 offen lds\n"
    "s_add_u32 m0, s62, 122880\n"
    "s_mov_b32 s38, 983040\n"
    "buffer_load_dwordx4 v17, s[28:31], s38 offen lds\n"
    "s_add_u32 m0, s62, 123904\n"
    "s_mov_b32 s38, 991232\n"
    "buffer_load_dwordx4 v17, s[28:31], s38 offen lds\n"
    "v_add_u32 v48, 0, v14\n"
    "ds_read_b64_tr_b16 a[0:1], v48 offset:0\n"
    "ds_read_b64_tr_b16 a[2:3], v48 offset:256\n"
    "ds_read_b64_tr_b16 a[4:5], v48 offset:4096\n"
    "ds_read_b64_tr_b16 a[6:7], v48 offset:4352\n"
    "ds_read_b64_tr_b16 a[8:9], v48 offset:8192\n"
    "ds_read_b64_tr_b16 a[10:11], v48 offset:8448\n"
    "ds_read_b64_tr_b16 a[12:13], v48 offset:12288\n"
    "ds_read_b64_tr_b16 a[14:15], v48 offset:12544\n"
    "s_waitcnt lgkmcnt(0)\n"
    "s_waitcnt vmcnt(24)\n"
    "s_barrier\n"
    "s_mov_b32 s28, s94\n"
    "s_mov_b32 s29, s95\n"
    "s_add_u32 m0, s62, 0\n"
    "s_mov_b32 s38, 1048576\n"
    "buffer_load_dwordx4 v17, s[28:31], s38 offen lds\n"
    "s_add_u32 m0, s62, 1024\n"
    "s_mov_b32 s38, 1056768\n"
    "buffer_load_dwordx4 v17, s[28:31], s38 offen lds\n"
    "s_add_u32 m0, s62, 8192\n"
    "s_mov_b32 s38, 1114112\n"
    "buffer_load_dwordx4 v17, s[28:31], s38 offen lds\n"
    "s_add_u32 m0, s62, 9216\n"
    "s_mov_b32 s38, 1122304\n"
    "buffer_load_dwordx4 v17, s[28:31], s38 offen lds\n"
    "v_add_u32 v48, 16384, v14\n"
    "ds_read_b64_tr_b16 a[16:17], v48 offset:0\n"
    "ds_read_b64_tr_b16 a[18:19], v48 offset:256\n"
    "ds_read_b64_tr_b16 a[20:21], v48 offset:4096\n"
    "ds_read_b64_tr_b16 a[22:23], v48 offset:4352\n"
    "ds_read_b64_tr_b16 a[24:25], v48 offset:8192\n"
    "ds_read_b64_tr_b16 a[26:27], v48 offset:8448\n"
    "ds_read_b64_tr_b16 a[28:29], v48 offset:12288\n"
    "ds_read_b64_tr_b16 a[30:31], v48 offset:12544\n"
    "s_waitcnt lgkmcnt(0)\n"
    "s_waitcnt vmcnt(24)\n"
    "s_barrier\n"
    "s_mov_b32 s28, s94\n"
    "s_mov_b32 s29, s95\n"
    "s_add_u32 m0, s62, 16384\n"
    "s_mov_b32 s38, 1179648\n"
    "buffer_load_dwordx4 v17, s[28:31], s38 offen lds\n"
    "s_add_u32 m0, s62, 17408\n"
    "s_mov_b32 s38, 1187840\n"
    "buffer_load_dwordx4 v17, s[28:31], s38 offen lds\n"
    "s_add_u32 m0, s62, 24576\n"
    "s_mov_b32 s38, 1245184\n"
    "buffer_load_dwordx4 v17, s[28:31], s38 offen lds\n"
    "s_add_u32 m0, s62, 25600\n"
    "s_mov_b32 s38, 1253376\n"
    "buffer_load_dwordx4 v17, s[28:31], s38 offen lds\n"
    "v_add_u32 v48, 32768, v14\n"
    "ds_read_b64_tr_b16 a[32:33], v48 offset:0\n"
    "ds_read_b64_tr_b16 a[34:35], v48 offset:256\n"
    "ds_read_b64_tr_b16 a[36:37], v48 offset:4096\n"
    "ds_read_b64_tr_b16 a[38:39], v48 offset:4352\n"
    "ds_read_b64_tr_b16 a[40:41], v48 offset:8192\n"
    "ds_read_b64_tr_b16 a[42:43], v48 offset:8448\n"
    "ds_read_b64_tr_b16 a[44:45], v48 offset:12288\n"
    "ds_read_b64_tr_b16 a[46:47], v48 offset:12544\n"
    "s_waitcnt lgkmcnt(0)\n"
    "s_waitcnt vmcnt(24)\n"
    "s_barrier\n"
    "s_mov_b32 s28, s94\n"
    "s_mov_b32 s29, s95\n"
    "s_add_u32 m0, s62, 32768\n"
    "s_mov_b32 s38, 1310720\n"
    "buffer_load_dwordx4 v17, s[28:31], s38 offen lds\n"
    "s_add_u32 m0, s62, 33792\n"
    "s_mov_b32 s38, 1318912\n"
    "buffer_load_dwordx4 v17, s[28:31], s38 offen lds\n"
    "s_add_u32 m0, s62, 40960\n"
    "s_mov_b32 s38, 1376256\n"
    "buffer_load_dwordx4 v17, s[28:31], s38 offen lds\n"
    "s_add_u32 m0, s62, 41984\n"
    "s_mov_b32 s38, 1384448\n"
    "buffer_load_dwordx4 v17, s[28:31], s38 offen lds\n"
    "v_add_u32 v48, 49152, v14\n"
    "ds_read_b64_tr_b16 a[48:49], v48 offset:0\n"
    "ds_read_b64_tr_b16 a[50:51], v48 offset:256\n"
    "ds_read_b64_tr_b16 a[52:53], v48 offset:4096\n"
    "ds_read_b64_tr_b16 a[54:55], v48 offset:4352\n"
    "ds_read_b64_tr_b16 a[56:57], v48 offset:8192\n"
    "ds_read_b64_tr_b16 a[58:59], v48 offset:8448\n"
    "ds_read_b64_tr_b16 a[60:61], v48 offset:12288\n"
    "ds_read_b64_tr_b16 a[62:63], v48 offset:12544\n"
    "s_waitcnt lgkmcnt(0)\n"
    "s_waitcnt vmcnt(24)\n"
    "s_barrier\n"
    "s_mov_b32 s28, s94\n"
    "s_mov_b32 s29, s95\n"
    "s_add_u32 m0, s62, 49152\n"
    "s_mov_b32 s38, 1441792\n"
    "buffer_load_dwordx4 v17, s[28:31], s38 offen lds\n"
    "s_add_u32 m0, s62, 50176\n"
    "s_mov_b32 s38, 1449984\n"
    "buffer_load_dwordx4 v17, s[28:31], s38 offen lds\n"
    "s_add_u32 m0, s62, 57344\n"
    "s_mov_b32 s38, 1507328\n"
    "buffer_load_dwordx4 v17, s[28:31], s38 offen lds\n"
    "s_add_u32 m0, s62, 58368\n"
    "s_mov_b32 s38, 1515520\n"
    "buffer_load_dwordx4 v17, s[28:31], s38 offen lds\n"
    "v_add_u32 v48, 65536, v14\n"
    "ds_read_b64_tr_b16 a[64:65], v48 offset:0\n"
    "ds_read_b64_tr_b16 a[66:67], v48 offset:256\n"
    "ds_read_b64_tr_b16 a[68:69], v48 offset:4096\n"
    "ds_read_b64_tr_b16 a[70:71], v48 offset:4352\n"
    "ds_read_b64_tr_b16 a[72:73], v48 offset:8192\n"
    "ds_read_b64_tr_b16 a[74:75], v48 offset:8448\n"
    "ds_read_b64_tr_b16 a[76:77], v48 offset:12288\n"
    "ds_read_b64_tr_b16 a[78:79], v48 offset:12544\n"
    "s_waitcnt lgkmcnt(0)\n"
    "s_waitcnt vmcnt(24)\n"
    "s_barrier\n"
    "s_mov_b32 s28, s94\n"
    "s_mov_b32 s29, s95\n"
    "s_add_u32 m0, s62, 65536\n"
    "s_mov_b32 s38, 1572864\n"
    "buffer_load_dwordx4 v17, s[28:31], s38 offen lds\n"
    "s_add_u32 m0, s62, 66560\n"
    "s_mov_b32 s38, 1581056\n"
    "buffer_load_dwordx4 v17, s[28:31], s38 offen lds\n"
    "s_add_u32 m0, s62, 73728\n"
    "s_mov_b32 s38, 1638400\n"
    "buffer_load_dwordx4 v17, s[28:31], s38 offen lds\n"
    "s_add_u32 m0, s62, 74752\n"
    "s_mov_b32 s38, 1646592\n"
    "buffer_load_dwordx4 v17, s[28:31], s38 offen lds\n"
    "v_add_u32 v48, 81920, v14\n"
    "ds_read_b64_tr_b16 a[80:81], v48 offset:0\n"
    "ds_read_b64_tr_b16 a[82:83], v48 offset:256\n"
    "ds_read_b64_tr_b16 a[84:85], v48 offset:4096\n"
    "ds_read_b64_tr_b16 a[86:87], v48 offset:4352\n"
    "ds_read_b64_tr_b16 a[88:89], v48 offset:8192\n"
    "ds_read_b64_tr_b16 a[90:91], v48 offset:8448\n"
    "ds_read_b64_tr_b16 a[92:93], v48 offset:12288\n"
    "ds_read_b64_tr_b16 a[94:95], v48 offset:12544\n"
    "s_waitcnt lgkmcnt(0)\n"
    "s_waitcnt vmcnt(24)\n"
    "s_barrier\n"
    "s_mov_b32 s28, s94\n"
    "s_mov_b32 s29, s95\n"
    "s_add_u32 m0, s62, 81920\n"
    "s_mov_b32 s38, 1703936\n"
    "buffer_load_dwordx4 v17, s[28:31], s38 offen lds\n"
    "s_add_u32 m0, s62, 82944\n"
    "s_mov_b32 s38, 1712128\n"
    "buffer_load_dwordx4 v17, s[28:31], s38 offen lds\n"
    "s_add_u32 m0, s62, 90112\n"
    "s_mov_b32 s38, 1769472\n"
    "buffer_load_dwordx4 v17, s[28:31], s38 offen lds\n"
    "s_add_u32 m0, s62, 91136\n"
    "s_mov_b32 s38, 1777664\n"
    "buffer_load_dwordx4 v17, s[28:31], s38 offen lds\n"
    "v_add_u32 v48, 98304, v14\n"
    "ds_read_b64_tr_b16 a[96:97], v48 offset:0\n"
    "ds_read_b64_tr_b16 a[98:99], v48 offset:256\n"
    "ds_read_b64_tr_b16 a[100:101], v48 offset:4096\n"
    "ds_read_b64_tr_b16 a[102:103], v48 offset:4352\n"
    "ds_read_b64_tr_b16 a[104:105], v48 offset:8192\n"
    "ds_read_b64_tr_b16 a[106:107], v48 offset:8448\n"
    "ds_read_b64_tr_b16 a[108:109], v48 offset:12288\n"
    "ds_read_b64_tr_b16 a[110:111], v48 offset:12544\n"
    "s_waitcnt lgkmcnt(0)\n"
    "s_waitcnt vmcnt(24)\n"
    "s_barrier\n"
    "v_add_u32 v48, 114688, v14\n"
    "ds_read_b64_tr_b16 a[112:113], v48 offset:0\n"
    "ds_read_b64_tr_b16 a[114:115], v48 offset:256\n"
    "ds_read_b64_tr_b16 a[116:117], v48 offset:4096\n"
    "ds_read_b64_tr_b16 a[118:119], v48 offset:4352\n"
    "ds_read_b64_tr_b16 a[120:121], v48 offset:8192\n"
    "ds_read_b64_tr_b16 a[122:123], v48 offset:8448\n"
    "ds_read_b64_tr_b16 a[124:125], v48 offset:12288\n"
    "ds_read_b64_tr_b16 a[126:127], v48 offset:12544\n"
    "s_waitcnt lgkmcnt(0)\n"
    "s_waitcnt vmcnt(20)\n"
    "s_barrier\n"
    "v_add_u32 v48, 0, v14\n"
    "ds_read_b64_tr_b16 a[128:129], v48 offset:0\n"
    "ds_read_b64_tr_b16 a[130:131], v48 offset:256\n"
    "ds_read_b64_tr_b16 a[132:133], v48 offset:4096\n"
    "ds_read_b64_tr_b16 a[134:135], v48 offset:4352\n"
    "ds_read_b64_tr_b16 a[136:137], v48 offset:8192\n"
    "ds_read_b64_tr_b16 a[138:139], v48 offset:8448\n"
    "ds_read_b64_tr_b16 a[140:141], v48 offset:12288\n"
    "ds_read_b64_tr_b16 a[142:143], v48 offset:12544\n"
    "s_waitcnt lgkmcnt(0)\n"
    "s_waitcnt vmcnt(16)\n"
    "s_barrier\n"
    "v_add_u32 v48, 16384, v14\n"
    "ds_read_b64_tr_b16 a[144:145], v48 offset:0\n"
    "ds_read_b64_tr_b16 a[146:147], v48 offset:256\n"
    "ds_read_b64_tr_b16 a[148:149], v48 offset:4096\n"
    "ds_read_b64_tr_b16 a[150:151], v48 offset:4352\n"
    "ds_read_b64_tr_b16 a[152:153], v48 offset:8192\n"
    "ds_read_b64_tr_b16 a[154:155], v48 offset:8448\n"
    "ds_read_b64_tr_b16 a[156:157], v48 offset:12288\n"
    "ds_read_b64_tr_b16 a[158:159], v48 offset:12544\n"
    "s_waitcnt lgkmcnt(0)\n"
    "s_waitcnt vmcnt(12)\n"
    "s_barrier\n"
    "v_add_u32 v48, 32768, v14\n"
    "ds_read_b64_tr_b16 a[160:161], v48 offset:0\n"
    "ds_read_b64_tr_b16 a[162:163], v48 offset:256\n"
    "ds_read_b64_tr_b16 a[164:165], v48 offset:4096\n"
    "ds_read_b64_tr_b16 a[166:167], v48 offset:4352\n"
    "ds_read_b64_tr_b16 a[168:169], v48 offset:8192\n"
    "ds_read_b64_tr_b16 a[170:171], v48 offset:8448\n"
    "ds_read_b64_tr_b16 a[172:173], v48 offset:12288\n"
    "ds_read_b64_tr_b16 a[174:175], v48 offset:12544\n"
    "s_waitcnt lgkmcnt(0)\n"
    "s_waitcnt vmcnt(8)\n"
    "s_barrier\n"
    "v_add_u32 v48, 49152, v14\n"
    "ds_read_b64_tr_b16 a[176:177], v48 offset:0\n"
    "ds_read_b64_tr_b16 a[178:179], v48 offset:256\n"
    "ds_read_b64_tr_b16 a[180:181], v48 offset:4096\n"
    "ds_read_b64_tr_b16 a[182:183], v48 offset:4352\n"
    "ds_read_b64_tr_b16 a[184:185], v48 offset:8192\n"
    "ds_read_b64_tr_b16 a[186:187], v48 offset:8448\n"
    "ds_read_b64_tr_b16 a[188:189], v48 offset:12288\n"
    "ds_read_b64_tr_b16 a[190:191], v48 offset:12544\n"
    "s_waitcnt lgkmcnt(0)\n"
    "s_waitcnt vmcnt(4)\n"
    "s_barrier\n"
    "v_add_u32 v48, 65536, v14\n"
    "ds_read_b64_tr_b16 a[192:193], v48 offset:0\n"
    "ds_read_b64_tr_b16 a[194:195], v48 offset:256\n"
    "ds_read_b64_tr_b16 a[196:197], v48 offset:4096\n"
    "ds_read_b64_tr_b16 a[198:199], v48 offset:4352\n"
    "ds_read_b64_tr_b16 a[200:201], v48 offset:8192\n"
    "ds_read_b64_tr_b16 a[202:203], v48 offset:8448\n"
    "ds_read_b64_tr_b16 a[204:205], v48 offset:12288\n"
    "ds_read_b64_tr_b16 a[206:207], v48 offset:12544\n"
    "s_waitcnt lgkmcnt(0)\n"
    "s_waitcnt vmcnt(0)\n"
    "s_barrier\n"
    "v_add_u32 v48, 81920, v14\n"
    "ds_read_b64_tr_b16 a[208:209], v48 offset:0\n"
    "ds_read_b64_tr_b16 a[210:211], v48 offset:256\n"
    "ds_read_b64_tr_b16 a[212:213], v48 offset:4096\n"
    "ds_read_b64_tr_b16 a[214:215], v48 offset:4352\n"
    "ds_read_b64_tr_b16 a[216:217], v48 offset:8192\n"
    "ds_read_b64_tr_b16 a[218:219], v48 offset:8448\n"
    "ds_read_b64_tr_b16 a[220:221], v48 offset:12288\n"
    "ds_read_b64_tr_b16 a[222:223], v48 offset:12544\n"
    "s_waitcnt lgkmcnt(0)\n"
    "s_barrier\n"
    "v_lshlrev_b32 v32, 16, v116\n"
    "v_and_b32 v33, 0xffff0000, v116\n"
    "v_lshlrev_b32 v34, 16, v117\n"
    "v_and_b32 v35, 0xffff0000, v117\n"
    "v_lshlrev_b32 v36, 16, v118\n"
    "v_and_b32 v37, 0xffff0000, v118\n"
    "v_lshlrev_b32 v38, 16, v119\n"
    "v_and_b32 v39, 0xffff0000, v119\n"
    "v_mov_b32 v24, v12\n"
    "v_mov_b32 v25, v13\n"
    "v_mov_b32 v30, v12\n"
    "v_mov_b32 v31, v13\n"
    "v_mov_b32 v26, 0\n"
    "v_mov_b32 v27, 0\n"
    "v_mov_b32 v28, 0\n"
    "v_mov_b32 v29, 0\n"
    "v_mov_b32 v42, 0\n"
    "v_mov_b32 v43, 0\n"
    "v_mov_b32 v48, 0\n"
    "v_mov_b32 v44, 0x3b000000\n"
    "s_mov_b32 s40, 0\n"
    "s_mov_b32 s41, 0\n"
    "s_mov_b32 s42, 0\n"
    "s_mov_b32 s43, 0\n"
    "s_mov_b32 s44, 0\n"
    "s_mov_b32 s45, 0\n"
    "s_mov_b32 s46, -1\n"
    "s_mov_b32 s47, -1\n"
    "s_mov_b32 s48, 0\n"
    "s_mov_b32 s51, 0\n"
    "s_mov_b32 s49, 0\n"
    "s_mov_b32 s37, 0\n"
    "s_mov_b64 s[64:65], 0\n"
    "s_mov_b32 s79, 0\n"
    "s_mov_b32 s74, 0xffff0000\n"
    "s_mov_b32 s75, 0\n"
    "s_mov_b32 s76, 0xaaaaaaaa\n"
    "s_mov_b32 s77, 0xaaaaaaaa\n"
    "LRY%=:\n"
    "global_load_dword v40, v48, s[96:97] sc1\n"
    "s_waitcnt vmcnt(0)\n"
    "v_readfirstlane_b32 s56, v40\n"
    "s_cmp_lg_u32 s56, 0\n"
    "s_cbranch_scc1 LRZ%=\n"
    "s_sleep 8\n"
    "s_add_u32 s37, s37, 1\n"
    "s_cmp_lt_u32 s37, 0x80000\n"
    "s_cbranch_scc1 LRY%=\n"
    "s_branch LFAIL%=\n"
    "LRZ%=:\n"
    "s_lshl_b32 s56, s56, 13\n"
    "s_and_b32 s56, s56, 0x2000\n"
    "s_xor_b32 s57, s56, 0x2000\n"
    "s_xor_b32 s58, s58, s56\n"
    "v_xor_b32 v2, s56, v2\n"
    "v_xor_b32 v11, s56, v11\n"
    "v_xor_b32 v12, s56, v21\n"
    "s_cmp_eq_u32 s55, 0\n"
    "s_cselect_b32 s38, 0, 0x80000000\n"
    "s_add_u32 s38, s38, 4096\n"
    "v_add_u32 v13, s38, v11\n"
    "LPR%=:\n"
    "buffer_load_dword v40, v12, s[16:19], 0 offen sc1\n"
    "s_waitcnt vmcnt(0)\n"
    "v_cmp_eq_u32 vcc, 0, v40\n"
    "s_cbranch_vccz LPOLL%=\n"
    "s_sleep 8\n"
    "s_add_u32 s37, s37, 1\n"
    "s_cmp_lt_u32 s37, 0x80000\n"
    "s_cbranch_scc1 LPR%=\n"
    "s_branch LFAIL%=\n"
    "LIDLE%=:\n"
    "s_min_u32 s38, s42, s41\n"
    "s_min_u32 s38, s38, s40\n"
    "s_sub_u32 s38, s38, s49\n"
    "s_cmp_eq_u32 s38, 64\n"
    "s_cbranch_scc1 LBEST%=\n"
    "s_add_u32 s37, s37, 1\n"
    "s_cmp_gt_u32 s37, 0x100000\n"
    "s_cbranch_scc1 LFAIL%=\n"
    "s_sleep 1\n"
    "LPOLL%=:\n"
    "v_mad_u32_u24 v41, v3, s41, v2\n"
    "v_mad_u32_u24 v41, v4, s42, v41\n"
    "v_mad_u32_u24 v41, v5, s40, v41\n"
    "buffer_load_dword v40, v41, s[16:19], 0 offen sc1\n"
    "s_waitcnt vmcnt(0)\n"
    "LTOP%=:\n"
    "v_readlane_b32 s52, v40, 32\n"
    "v_readlane_b32 s53, v40, 0\n"
    "v_readlane_b32 s54, v40, 16\n"
    "s_cmp_lg_u32 s52, 0\n"
    "s_cbranch_scc1 LDEC%=\n"
    "LPICK%=:\n"
    "s_cmp_ge_u32 s41, s43\n"
    "s_cselect_b32 s38, s40, 0\n"
    "s_cmp_eq_u32 s38, 64\n"
    "s_cselect_b32 s41, 64, s41\n"
    "s_cselect_b32 s53, 0, s53\n"
    "s_cmp_lt_u32 s42, s41\n"
    "s_cselect_b32 s51, s54, 0\n"
    "s_or_b32 s38, s51, s53\n"
    "s_mov_b32 s50, -1\n"
    "s_cbranch_scc0 LNONE%=\n"
    "s_cmp_lg_u32 s51, 0\n"
    "s_cselect_b32 s50, s42, s41\n"
    "s_addc_u32 s42, s42, 0\n"
    "s_add_u32 s41, s41, 1\n"
    "s_sub_u32 s41, s41, s51\n"
    "s_lshl_b32 s60, s50, 5\n"
    "s_add_u32 s60, s60, s55\n"
    "s_cmp_eq_u32 s60, s44\n"
    "s_cselect_b32 s38, s51, 0\n"
    "s_cmp_lg_u32 s38, 0\n"
    "s_cselect_b32 s60, s50, s60\n"
    "s_cselect_b32 s62, 22020096, 0\n"
    "s_add_u32 s62, s62, 241172480\n"
    "s_lshl_b32 s39, s60, 10\n"
    "s_andn2_b32 s63, s39, 0x1fff\n"
    "s_add_u32 s63, s63, s39\n"
    "s_add_u32 s63, s63, s62\n"
    "s_lshr_b32 s38, s63, 8\n"
    "s_xor_b32 s38, s38, s57\n"
    "s_and_b32 s38, s38, 0x2000\n"
    "s_or_b32 s63, s63, s38\n"
    "s_lshl_b32 s62, s51, 6\n"
    "s_add_u32 s62, s62, s50\n"
    "s_lshl_b32 s62, s62, 18\n"
    "s_add_u32 s62, s62, 197132288\n"
    "s_lshr_b32 s38, s62, 8\n"
    "s_xor_b32 s38, s38, s57\n"
    "s_and_b32 s38, s38, 0x2000\n"
    "s_or_b32 s62, s62, s38\n"
    "buffer_load_dwordx4 v[116:119], v0, s[16:19], s63 offen sc1\n"
    "buffer_load_dwordx4 v[100:103], v1, s[16:19], s62 offen sc1\n"
    "s_add_u32 s62, s62, 0x10000\n"
    "buffer_load_dwordx4 v[104:107], v1, s[16:19], s62 offen sc1\n"
    "s_add_u32 s62, s62, 0x10000\n"
    "buffer_load_dwordx4 v[108:111], v1, s[16:19], s62 offen sc1\n"
    "s_add_u32 s62, s62, 0x10000\n"
    "buffer_load_dwordx4 v[112:115], v1, s[16:19], s62 offen sc1\n"
    "s_cmp_ge_i32 s47, 0\n"
    "s_cbranch_scc1 LJOB%=\n"
    "s_lshl_b32 s60, s50, 5\n"
    "s_add_u32 s60, s60, s55\n"
    "s_cmp_lg_u32 s51, 0\n"
    "s_cselect_b32 s62, 8388608, 0\n"
    "s_cselect_b32 s63, s57, s56\n"
    "s_add_u32 s62, s62, 245366784\n"
    "s_lshl_b32 s39, s60, 11\n"
    "s_andn2_b32 s32, s39, 0x1fff\n"
    "s_add_u32 s32, s32, s39\n"
    "s_add_u32 s32, s32, s62\n"
    "s_lshr_b32 s38, s32, 8\n"
    "s_xor_b32 s38, s38, s63\n"
    "s_and_b32 s38, s38, 0x2000\n"
    "s_or_b32 s32, s32, s38\n"
    "s_mov_b32 s33, s60\n"
    "s_waitcnt vmcnt(4)\n"
    "v_lshlrev_b32 v64, 16, v116\n"
    "v_and_b32 v65, 0xffff0000, v116\n"
    "v_lshlrev_b32 v66, 16, v117\n"
    "v_and_b32 v67, 0xffff0000, v117\n"
    "v_lshlrev_b32 v68, 16, v118\n"
    "v_and_b32 v69, 0xffff0000, v118\n"
    "v_lshlrev_b32 v70, 16, v119\n"
    "v_and_b32 v71, 0xffff0000, v119\n"
    "s_waitcnt vmcnt(3)\n"
    "v_mfma_f32_16x16x32_bf16 v[64:67], v[24:27], v[100:103], v[64:67]\n"
    "v_mfma_f32_16x16x32_bf16 v[68:71], v[28:31], v[100:103], v[68:71]\n"
    "s_waitcnt vmcnt(2)\n"
    "v_mfma_f32_16x16x32_bf16 v[64:67], v[24:27], v[104:107], v[64:67]\n"
    "v_mfma_f32_16x16x32_bf16 v[68:71], v[28:31], v[104:107], v[68:71]\n"
    "s_waitcnt vmcnt(1)\n"
    "v_mfma_f32_16x16x32_bf16 v[64:67], v[24:27], v[108:111], v[64:67]\n"
    "v_mfma_f32_16x16x32_bf16 v[68:71], v[28:31], v[108:111], v[68:71]\n"
    "v_max3_u32 v48, v103, v107, v111\n"
    "s_waitcnt vmcnt(0)\n"
    "v_mfma_f32_16x16x32_bf16 v[64:67], v[24:27], v[112:115], v[64:67]\n"
    "v_mfma_f32_16x16x32_bf16 v[68:71], v[28:31], v[112:115], v[68:71]\n"
    "v_max_u32 v48, v48, v115\n"
    "v_cmp_eq_u32 vcc, -1, v48\n"
    "s_mov_b32 s47, s50\n"
    "s_mov_b32 s48, s51\n"
    "s_mov_b32 s50, -1\n"
    "s_cbranch_vccnz LEARLY%=\n"
    "LNXT%=:\n"
    "LJOB%=:\n"
    "v_mad_u32_u24 v41, v3, s41, v2\n"
    "v_mad_u32_u24 v41, v4, s42, v41\n"
    "v_mad_u32_u24 v41, v5, s40, v41\n"
    "buffer_load_dword v40, v41, s[16:19], 0 offen sc1\n"
    "v_cvt_pk_bf16_f32 v124, v64, v65\n"
    "v_lshlrev_b32 v72, 16, v124\n"
    "v_cvt_pk_bf16_f32 v125, v66, v67\n"
    "v_readfirstlane_b32 s68, v72\n"
    "v_cvt_pk_bf16_f32 v126, v68, v69\n"
    "v_cvt_pk_bf16_f32 v127, v70, v71\n"
    "v_and_b32 v73, 0xffff0000, v124\n"
    "v_lshlrev_b32 v74, 16, v125\n"
    "v_and_b32 v75, 0xffff0000, v125\n"
    "v_lshlrev_b32 v76, 16, v126\n"
    "v_and_b32 v77, 0xffff0000, v126\n"
    "v_lshlrev_b32 v78, 16, v127\n"
    "v_and_b32 v79, 0xffff0000, v127\n"
    "v_pk_add_f32 v[92:93], v[72:73], s[68:69] op_sel_hi:[1,0] neg_lo:[0,1] neg_hi:[0,1]\n"
    "v_pk_add_f32 v[94:95], v[74:75], s[68:69] op_sel_hi:[1,0] neg_lo:[0,1] neg_hi:[0,1]\n"
    "v_pk_add_f32 v[96:97], v[76:77], s[68:69] op_sel_hi:[1,0] neg_lo:[0,1] neg_hi:[0,1]\n"
    "v_pk_add_f32 v[98:99], v[78:79], s[68:69] op_sel_hi:[1,0] neg_lo:[0,1] neg_hi:[0,1]\n"
    "v_pk_add_f32 v[48:49], v[92:93], v[94:95]\n"
    "v_pk_mul_f32 v[50:51], v[92:93], v[92:93]\n"
    "v_pk_add_f32 v[52:53], v[96:97], v[98:99]\n"
    "v_pk_fma_f32 v[50:51], v[94:95], v[94:95], v[50:51]\n"
    "v_pk_add_f32 v[48:49], v[48:49], v[52:53]\n"
    "v_pk_fma_f32 v[50:51], v[96:97], v[96:97], v[50:51]\n"
    "v_add_f32 v48, v48, v49\n"
    "v_pk_fma_f32 v[50:51], v[98:99], v[98:99], v[50:51]\n"
    "v_mov_b32 v52, s68\n"
    "v_add_f32 v49, v50, v51\n"
    "v_add_f32_dpp v48, v48, v48 row_ror:8 row_mask:0xf bank_mask:0xf\n"
    "s_nop 0\n"
    "v_add_f32_dpp v49, v49, v49 row_ror:8 row_mask:0xf bank_mask:0xf\n"
    "buffer_store_dwordx4 v[124:127], v0, s[16:19], s32 offen offset:0 sc1\n"
    "v_add_f32_dpp v48, v48, v48 row_ror:4 row_mask:0xf bank_mask:0xf\n"
    "v_add_f32_dpp v49, v49, v49 row_ror:4 row_mask:0xf bank_mask:0xf\n"
    "s_nop 0\n"
    "v_add_f32_dpp v48, v48, v48 row_ror:2 row_mask:0xf bank_mask:0xf\n"
    "v_add_f32_dpp v49, v49, v49 row_ror:2 row_mask:0xf bank_mask:0xf\n"
    "s_nop 0\n"
    "v_add_f32_dpp v48, v48, v48 row_ror:1 row_mask:0xf bank_mask:0xf\n"
    "v_add_f32_dpp v49, v49, v49 row_ror:1 row_mask:0xf bank_mask:0xf\n"
    "s_nop 1\n"
    "v_permlane16_swap_b32 v48, v49\n"
    "v_add_f32 v48, v48, v49\n"
    "v_mov_b32 v49, v48\n"
    "s_nop 1\n"
    "v_permlane32_swap_b32 v48, v49\n"
    "v_add_f32 v48, v48, v49\n"
    "s_nop 0\n"
    "v_readlane_b32 s62, v48, 0\n"
    "v_readlane_b32 s63, v48, 16\n"
    "s_nop 0\n"
    "v_mul_f32 v48, s62, v44\n"
    "v_mul_f32 v49, s63, v44\n"
    "v_fma_f32 v49, -v48, v48, v49\n"
    "v_add_f32 v52, v52, v48\n"
    "v_max_f32 v49, 0, v49\n"
    "v_cvt_pk_bf16_f32 v48, v52, v49\n"
    "v_and_b32 v49, 0xffff0000, v48\n"
    "v_add_f32 v49, 0x3727c5ac, v49\n"
    "v_cvt_pk_bf16_f32 v49, v49, v49\n"
    "v_lshlrev_b32 v49, 16, v49\n"
    "v_sqrt_f32 v50, v49\n"
    "v_lshlrev_b32 v48, 16, v48\n"
    "v_cvt_pk_bf16_f32 v50, v50, v50\n"
    "v_lshlrev_b32 v50, 16, v50\n"
    "v_rcp_f32 v50, v50\n"
    "v_pk_add_f32 v[92:93], v[72:73], v[48:49] op_sel_hi:[1,0] neg_lo:[0,1] neg_hi:[0,1]\n"
    "v_pk_add_f32 v[94:95], v[74:75], v[48:49] op_sel_hi:[1,0] neg_lo:[0,1] neg_hi:[0,1]\n"
    "v_pk_add_f32 v[96:97], v[76:77], v[48:49] op_sel_hi:[1,0] neg_lo:[0,1] neg_hi:[0,1]\n"
    "v_pk_add_f32 v[98:99], v[78:79], v[48:49] op_sel_hi:[1,0] neg_lo:[0,1] neg_hi:[0,1]\n"
    "v_cvt_pk_bf16_f32 v52, v92, v93\n"
    "v_cvt_pk_bf16_f32 v53, v94, v95\n"
    "v_cvt_pk_bf16_f32 v54, v96, v97\n"
    "v_cvt_pk_bf16_f32 v55, v98, v99\n"
    "v_lshlrev_b32 v92, 16, v52\n"
    "v_and_b32 v93, 0xffff0000, v52\n"
    "v_lshlrev_b32 v94, 16, v53\n"
    "v_and_b32 v95, 0xffff0000, v53\n"
    "v_lshlrev_b32 v96, 16, v54\n"
    "v_and_b32 v97, 0xffff0000, v54\n"
    "v_lshlrev_b32 v98, 16, v55\n"
    "v_and_b32 v99, 0xffff0000, v55\n"
    "v_pk_mul_f32 v[80:81], v[92:93], v[50:51] op_sel_hi:[1,0]\n"
    "v_pk_mul_f32 v[82:83], v[94:95], v[50:51] op_sel_hi:[1,0]\n"
    "v_pk_mul_f32 v[84:85], v[96:97], v[50:51] op_sel_hi:[1,0]\n"
    "v_pk_mul_f32 v[86:87], v[98:99], v[50:51] op_sel_hi:[1,0]\n"
    "v_cvt_pk_bf16_f32 v88, v80, v81\n"
    "v_cvt_pk_bf16_f32 v89, v82, v83\n"
    "v_cvt_pk_bf16_f32 v90, v84, v85\n"
    "v_cvt_pk_bf16_f32 v91, v86, v87\n"
    "v_lshlrev_b32 v80, 16, v88\n"
    "v_and_b32 v81, 0xffff0000, v88\n"
    "v_lshlrev_b32 v82, 16, v89\n"
    "v_and_b32 v83, 0xffff0000, v89\n"
    "v_lshlrev_b32 v84, 16, v90\n"
    "v_and_b32 v85, 0xffff0000, v90\n"
    "v_lshlrev_b32 v86, 16, v91\n"
    "v_and_b32 v87, 0xffff0000, v91\n"
    "v_pk_add_f32 v[72:73], v[72:73], v[80:81]\n"
    "v_pk_add_f32 v[74:75], v[74:75], v[82:83]\n"
    "v_pk_add_f32 v[76:77], v[76:77], v[84:85]\n"
    "v_pk_add_f32 v[78:79], v[78:79], v[86:87]\n"
    "buffer_store_dwordx4 v[88:91], v0, s[16:19], s32 offen offset:1024 sc1\n"
    "v_pk_mul_f32 v[56:57], v[80:81], v[32:33]\n"
    "v_pk_fma_f32 v[56:57], v[82:83], v[34:35], v[56:57]\n"
    "v_pk_fma_f32 v[56:57], v[84:85], v[36:37], v[56:57]\n"
    "v_pk_fma_f32 v[56:57], v[86:87], v[38:39], v[56:57]\n"
    "v_cvt_pk_bf16_f32 v60, v72, v73\n"
    "v_cvt_pk_bf16_f32 v61, v74, v75\n"
    "v_cvt_pk_bf16_f32 v62, v76, v77\n"
    "v_cvt_pk_bf16_f32 v63, v78, v79\n"
    "s_cmp_ge_u32 s47, s43\n"
    "s_cselect_b32 s38, 1, 0\n"
    "s_cmp_eq_u32 s38, s48\n"
    "s_cbranch_scc0 LKEEP%=\n"
    "s_lshl_b32 s61, s33, 10\n"
    "buffer_store_dwordx4 v[60:63], v0, s[24:27], s61 offen offset:0 sc1\n"
    "s_branch LKEPT%=\n"
    "LKEEP%=:\n"
    "s_lshl_b32 s39, s33, 10\n"
    "s_andn2_b32 s61, s39, 0x1fff\n"
    "s_add_u32 s61, s61, s39\n"
    "s_add_u32 s61, s61, 241172480\n"
    "s_lshr_b32 s38, s61, 8\n"
    "s_xor_b32 s38, s38, s56\n"
    "s_and_b32 s38, s38, 0x2000\n"
    "s_or_b32 s61, s61, s38\n"
    "s_lshl_b32 s38, s48, 31\n"
    "s_or_b32 s61, s61, s38\n"
    "buffer_store_dwordx4 v[60:63], v0, s[16:19], s61 offen offset:0 sc1\n"
    "s_xor_b32 s66, s48, 1\n"
    "s_mov_b32 s67, 0\n"
    "s_lshl_b64 s[66:67], s[66:67], s47\n"
    "s_or_b64 s[64:65], s[64:65], s[66:67]\n"
    "LKEPT%=:\n"
    "v_add_f32_dpp v56, v56, v56 row_ror:8 row_mask:0xf bank_mask:0xf\n"
    "v_add_f32_dpp v57, v57, v57 row_ror:8 row_mask:0xf bank_mask:0xf\n"
    "s_nop 0\n"
    "v_add_f32_dpp v56, v56, v56 row_ror:4 row_mask:0xf bank_mask:0xf\n"
    "v_add_f32_dpp v57, v57, v57 row_ror:4 row_mask:0xf bank_mask:0xf\n"
    "s_nop 0\n"
    "v_add_f32_dpp v56, v56, v56 row_ror:2 row_mask:0xf bank_mask:0xf\n"
    "v_add_f32_dpp v57, v57, v57 row_ror:2 row_mask:0xf bank_mask:0xf\n"
    "s_nop 0\n"
    "v_add_f32_dpp v56, v56, v56 row_ror:1 row_mask:0xf bank_mask:0xf\n"
    "v_add_f32_dpp v57, v57, v57 row_ror:1 row_mask:0xf bank_mask:0xf\n"
    "s_nop 1\n"
    "v_permlane16_swap_b32 v56, v57\n"
    "v_add_f32 v56, v56, v57\n"
    "v_mov_b32 v57, v56\n"
    "s_nop 1\n"
    "v_permlane32_swap_b32 v56, v57\n"
    "v_add_f32 v56, v56, v57\n"
    "s_nop 0\n"
    "v_readlane_b32 s62, v56, 0\n"
    "v_readlane_b32 s63, v56, 16\n"
    "s_sub_u32 s39, 0xffff, s33\n"
    "v_mov_b32 v56, s62\n"
    "v_add_f32 v56, s63, v56\n"
    "v_cvt_pk_bf16_f32 v56, v56, v56\n"
    "v_lshlrev_b32 v56, 16, v56\n"
    "v_cmp_o_f32 vcc, v56, v56\n"
    "v_ashrrev_i32 v57, 31, v56\n"
    "v_or_b32 v57, 0x80000000, v57\n"
    "v_xor_b32 v56, v56, v57\n"
    "v_cndmask_b32 v56, 0, v56, vcc\n"
    "v_and_b32 v56, 0xffff0000, v56\n"
    "v_or_b32 v56, s39, v56\n"
    "s_lshl_b64 exec, 1, s47\n"
    "s_cmp_lg_u32 s48, 0\n"
    "s_cselect_b64 vcc, -1, 0\n"
    "v_cndmask_b32 v42, v42, v56, vcc\n"
    "v_cndmask_b32 v43, v56, v43, vcc\n"
    "s_mov_b64 exec, -1\n"
    "s_waitcnt vmcnt(3)\n"
    "s_mov_b32 s47, -1\n"
    "s_mov_b32 s37, 0\n"
    "s_cmp_lt_i32 s50, 0\n"
    "s_cbranch_scc1 LTOP%=\n"
    "s_lshl_b32 s60, s50, 5\n"
    "s_add_u32 s60, s60, s55\n"
    "s_cmp_lg_u32 s51, 0\n"
    "s_cselect_b32 s62, 8388608, 0\n"
    "s_cselect_b32 s63, s57, s56\n"
    "s_add_u32 s62, s62, 245366784\n"
    "s_lshl_b32 s39, s60, 11\n"
    "s_andn2_b32 s32, s39, 0x1fff\n"
    "s_add_u32 s32, s32, s39\n"
    "s_add_u32 s32, s32, s62\n"
    "s_lshr_b32 s38, s32, 8\n"
    "s_xor_b32 s38, s38, s63\n"
    "s_and_b32 s38, s38, 0x2000\n"
    "s_or_b32 s32, s32, s38\n"
    "s_mov_b32 s33, s60\n"
    "v_lshlrev_b32 v64, 16, v116\n"
    "v_and_b32 v65, 0xffff0000, v116\n"
    "v_lshlrev_b32 v66, 16, v117\n"
    "v_and_b32 v67, 0xffff0000, v117\n"
    "v_lshlrev_b32 v68, 16, v118\n"
    "v_and_b32 v69, 0xffff0000, v118\n"
    "v_lshlrev_b32 v70, 16, v119\n"
    "v_and_b32 v71, 0xffff0000, v119\n"
    "v_mfma_f32_16x16x32_bf16 v[64:67], v[24:27], v[100:103], v[64:67]\n"
    "s_nop 0\n"
    "v_mfma_f32_16x16x32_bf16 v[68:71], v[28:31], v[100:103], v[68:71]\n"
    "v_mfma_f32_16x16x32_bf16 v[64:67], v[24:27], v[104:107], v[64:67]\n"
    "v_mfma_f32_16x16x32_bf16 v[68:71], v[28:31], v[104:107], v[68:71]\n"
    "v_mfma_f32_16x16x32_bf16 v[64:67], v[24:27], v[108:111], v[64:67]\n"
    "v_mfma_f32_16x16x32_bf16 v[68:71], v[28:31], v[108:111], v[68:71]\n"
    "v_max3_u32 v48, v103, v107, v111\n"
    "v_mfma_f32_16x16x32_bf16 v[64:67], v[24:27], v[112:115], v[64:67]\n"
    "v_mfma_f32_16x16x32_bf16 v[68:71], v[28:31], v[112:115], v[68:71]\n"
    "v_max_u32 v48, v48, v115\n"
    "v_cmp_eq_u32 vcc, -1, v48\n"
    "s_mov_b32 s47, s50\n"
    "s_mov_b32 s48, s51\n"
    "s_mov_b32 s50, -1\n"
    "s_cbranch_vccnz LEARLY%=\n"
    "s_branch LTOP%=\n"
    "LNONE%=:\n"
    "s_cmp_ge_i32 s47, 0\n"
    "s_cbranch_scc1 LJOB%=\n"
    "s_branch LIDLE%=\n"
    "LEARLY%=:\n"
    "s_sub_u32 s42, s42, s48\n"
    "s_add_u32 s41, s41, s48\n"
    "s_sub_u32 s41, s41, 1\n"
    "s_mov_b32 s54, s48\n"
    "s_xor_b32 s53, s48, 1\n"
    "s_mov_b32 s47, -1\n"
    "s_and_b32 s38, s37, 3\n"
    "s_cmp_eq_u32 s38, 3\n"
    "s_cbranch_scc1 LIDLE%=\n"
    "s_sleep 2\n"
    "s_add_u32 s37, s37, 1\n"
    "s_cmp_lt_u32 s37, 0x80000\n"
    "s_cbranch_scc1 LPICK%=\n"
    "s_branch LFAIL%=\n"
    "LBEST%=:\n"
    "s_lshl_b64 vcc, -1, s43\n"
    "s_nop 0\n"
    "v_cndmask_b32 v48, v43, v42, vcc\n"
    "s_nop 1\n"
    "v_max_u32_dpp v48, v48, v48 row_ror:8 row_mask:0xf bank_mask:0xf\n"
    "s_nop 1\n"
    "v_max_u32_dpp v48, v48, v48 row_ror:4 row_mask:0xf bank_mask:0xf\n"
    "s_nop 1\n"
    "v_max_u32_dpp v48, v48, v48 row_ror:2 row_mask:0xf bank_mask:0xf\n"
    "s_nop 1\n"
    "v_max_u32_dpp v48, v48, v48 row_ror:1 row_mask:0xf bank_mask:0xf\n"
    "v_mov_b32 v49, v48\n"
    "s_nop 1\n"
    "v_permlane16_swap_b32 v48, v49\n"
    "v_max_u32 v48, v48, v49\n"
    "v_mov_b32 v49, v48\n"
    "s_nop 1\n"
    "v_permlane32_swap_b32 v48, v49\n"
    "v_max_u32 v48, v48, v49\n"
    "s_add_u32 s70, s58, 4096\n"
    "s_mov_b32 s49, 1\n"
    "s_mov_b64 exec, 1\n"
    "buffer_atomic_umax v48, off, s[16:19], s70 offset:4 sc1\n"
    "s_mov_b64 exec, -1\n"
    "v_mov_b64 v[120:121], 1\n"
    "v_mad_u32_u24 v41, v3, s41, v2\n"
    "v_mad_u32_u24 v41, v4, s42, v41\n"
    "v_mad_u32_u24 v41, v5, s40, v41\n"
    "s_mov_b64 exec, 1\n"
    "s_waitcnt vmcnt(0)\n"
    "buffer_atomic_add_x2 v[120:121], off, s[16:19], s70 sc0 sc1\n"
    "s_mov_b64 exec, -1\n"
    "buffer_load_dword v40, v41, s[16:19], 0 offen sc1\n"
    "s_lshl_b32 s39, s40, 2\n"
    "s_waitcnt vmcnt(1)\n"
    "v_readfirstlane_b32 s38, v120\n"
    "v_readfirstlane_b32 s62, v121\n"
    "s_cmp_lg_u32 s38, 31\n"
    "s_cbranch_scc1 LAS%=\n"
    "v_mov_b32 v120, s62\n"
    "s_mov_b64 exec, 0x38\n"
    "buffer_store_dword v120, v11, s[16:19], s39 offen sc1\n"
    "s_mov_b64 exec, -1\n"
    "s_waitcnt vmcnt(1)\n"
    "s_cmp_lg_u32 s40, s40\n"
    "s_cbranch_scc1 LASX%=\n"
    "v_writelane_b32 v40, s62, 32\n"
    "s_branch LASX%=\n"
    "LAS%=:\n"
    "s_waitcnt vmcnt(0)\n"
    "LASX%=:\n"
    "s_branch LTOP%=\n"
    "LDEC%=:\n"
    "s_mov_b32 s70, s40\n"
    "s_cmp_eq_u32 s40, 64\n"
    "s_cbranch_scc1 LMLP%=\n"
    "s_add_u32 s40, s40, 1\n"
    "s_mov_b32 s37, 0\n"
    "s_lshr_b32 s38, s52, 16\n"
    "s_cmp_gt_i32 s38, s46\n"
    "s_cselect_b32 s70, s70, -1\n"
    "s_cselect_b32 s46, s38, s46\n"
    "s_addc_u32 s45, s45, 0\n"
    "s_cmp_lt_i32 s70, 0\n"
    "s_cbranch_scc1 LPICK%=\n"
    "s_mov_b32 s43, s70\n"
    "s_andn2_b32 s44, 0x7ff, s52\n"
    "LMLP%=:\n"
    "s_andn2_b32 s71, 0x7ff, s52\n"
    "s_lshr_b32 s38, s71, 5\n"
    "s_cmp_ge_u32 s38, s43\n"
    "s_cselect_b32 s63, s57, s56\n"
    "s_cselect_b32 s38, s70, 0\n"
    "s_cmp_eq_u32 s38, 64\n"
    "s_cselect_b32 s62, 8388608, 0\n"
    "s_add_u32 s62, s62, 245366784\n"
    "s_lshl_b32 s39, s71, 11\n"
    "s_andn2_b32 s61, s39, 0x1fff\n"
    "s_add_u32 s61, s61, s39\n"
    "s_add_u32 s61, s61, s62\n"
    "s_lshr_b32 s38, s61, 8\n"
    "s_xor_b32 s38, s38, s63\n"
    "s_and_b32 s38, s38, 0x2000\n"
    "s_or_b32 s61, s61, s38\n"
    "buffer_load_dwordx4 v[116:119], v0, s[16:19], s61 offen offset:1024 sc1\n"
    "v_and_b32 v48, -4, v18\n"
    "buffer_load_dword v45, v48, s[16:19], s61 offen sc1\n"
    "buffer_load_dword v46, v48, s[16:19], s61 offen offset:1024 sc1\n"
    "s_lshl_b32 s39, s70, 12\n"
    "s_andn2_b32 s61, s39, 0x1fff\n"
    "s_add_u32 s61, s61, s39\n"
    "s_add_u32 s61, s61, 263323648\n"
    "s_lshr_b32 s38, s61, 8\n"
    "s_xor_b32 s38, s38, s57\n"
    "s_and_b32 s38, s38, 0x2000\n"
    "s_or_b32 s61, s61, s38\n"
    "v_mov_b64 v[104:105], 0\n"
    "v_mov_b64 v[106:107], 0\n"
    "v_mov_b64 v[108:109], 0\n"
    "v_mov_b64 v[110:111], 0\n"
    "s_waitcnt vmcnt(2)\n"
    "ds_write_b128 v8, v[116:119]\n"
    "ds_read_b128 v[72:75], v7 offset:0\n"
    "ds_read_b128 v[76:79], v7 offset:128\n"
    "ds_read_b128 v[80:83], v7 offset:256\n"
    "ds_read_b128 v[84:87], v7 offset:384\n"
    "ds_read_b128 v[88:91], v7 offset:512\n"
    "ds_read_b128 v[92:95], v7 offset:640\n"
    "ds_read_b128 v[96:99], v7 offset:768\n"
    "ds_read_b128 v[100:103], v7 offset:896\n"
    "s_waitcnt lgkmcnt(7)\n"
    "v_smfmac_f32_16x16x64_bf16 v[104:107], v[72:75], v[128:135], v6\n"
    "v_smfmac_f32_16x16x64_bf16 v[108:111], v[72:75], v[136:143], v6\n"
    "s_waitcnt lgkmcnt(6)\n"
    "v_smfmac_f32_16x16x64_bf16 v[104:107], v[76:79], v[144:151], v6\n"
    "v_smfmac_f32_16x16x64_bf16 v[108:111], v[76:79], v[152:159], v6\n"
    "s_waitcnt lgkmcnt(5)\n"
    "v_smfmac_f32_16x16x64_bf16 v[104:107], v[80:83], v[160:167], v6\n"
    "v_smfmac_f32_16x16x64_bf16 v[108:111], v[80:83], v[168:175], v6\n"
    "s_waitcnt lgkmcnt(4)\n"
    "v_smfmac_f32_16x16x64_bf16 v[104:107], v[84:87], v[176:183], v6\n"
    "v_smfmac_f32_16x16x64_bf16 v[108:111], v[84:87], v[184:191], v6\n"
    "s_waitcnt lgkmcnt(3)\n"
    "v_smfmac_f32_16x16x64_bf16 v[104:107], v[88:91], v[192:199], v6\n"
    "v_smfmac_f32_16x16x64_bf16 v[108:111], v[88:91], v[200:207], v6\n"
    "s_waitcnt lgkmcnt(2)\n"
    "v_smfmac_f32_16x16x64_bf16 v[104:107], v[92:95], v[208:215], v6\n"
    "v_smfmac_f32_16x16x64_bf16 v[108:111], v[92:95], v[216:223], v6\n"
    "s_waitcnt lgkmcnt(1)\n"
    "v_smfmac_f32_16x16x64_bf16 v[104:107], v[96:99], v[224:231], v6\n"
    "v_smfmac_f32_16x16x64_bf16 v[108:111], v[96:99], v[232:239], v6\n"
    "s_waitcnt lgkmcnt(0)\n"
    "v_smfmac_f32_16x16x64_bf16 v[104:107], v[100:103], v[240:247], v6\n"
    "v_smfmac_f32_16x16x64_bf16 v[108:111], v[100:103], v[248:255], v6\n"
    "s_nop 3\n"
    "v_add_f32 v104, v104, v106\n"
    "s_nop 2\n"
    "v_add_f32 v108, v108, v110\n"
    "v_cvt_pk_bf16_f32 v108, v104, v108\n"
    "v_lshlrev_b32 v104, 16, v108\n"
    "v_mul_f32 v105, 0xbfb8aa3b, v104\n"
    "v_exp_f32 v105, v105\n"
    "v_and_b32 v108, 0xffff0000, v108\n"
    "v_add_f32 v105, 1.0, v105\n"
    "v_rcp_f32 v105, v105\n"
    "s_nop 0\n"
    "v_mul_f32 v104, v104, v105\n"
    "v_cvt_pk_bf16_f32 v104, v104, v104\n"
    "v_lshlrev_b32 v104, 16, v104\n"
    "v_mul_f32 v104, v104, v108\n"
    "s_nop 1\n"
    "v_mov_b32_dpp v105, v104 quad_perm:[1,1,3,3] row_mask:0xf bank_mask:0xf\n"
    "v_cvt_pk_bf16_f32 v104, v104, v105\n"
    "v_min_u32 v104, -2, v104\n"
    "buffer_store_dword v104, v9, s[16:19], s61 offen sc1\n"
    "s_lshr_b32 s38, s52, 16\n"
    "s_bitcmp1_b32 s38, 15\n"
    "s_cselect_b32 s39, 0, 0x7fff\n"
    "s_or_b32 s39, s39, 0x8000\n"
    "s_xor_b32 s38, s38, s39\n"
    "s_lshl_b32 s38, s38, 16\n"
    "v_mov_b32 v52, 0xbfb8aa3b\n"
    "v_mul_f32 v52, s38, v52\n"
    "v_exp_f32 v52, v52\n"
    "s_nop 0\n"
    "v_add_f32 v52, 1.0, v52\n"
    "v_rcp_f32 v52, v52\n"
    "s_nop 0\n"
    "v_cvt_pk_bf16_f32 v52, v52, v52\n"
    "v_lshlrev_b32 v52, 16, v52\n"
    "s_waitcnt vmcnt(2)\n"
    "v_lshlrev_b32 v48, 16, v45\n"
    "v_and_b32 v45, 0xffff0000, v45\n"
    "v_cndmask_b32 v45, v48, v45, s[76:77]\n"
    "s_waitcnt vmcnt(1)\n"
    "v_lshlrev_b32 v48, 16, v46\n"
    "v_and_b32 v46, 0xffff0000, v46\n"
    "v_cndmask_b32 v46, v48, v46, s[76:77]\n"
    "v_mov_b32 v116, 0\n"
    "v_mov_b32 v117, 0\n"
    "v_mov_b32 v118, 0\n"
    "v_mov_b32 v119, 0\n"
    "v_mov_b64 v[56:57], 0\n"
    "v_mov_b64 v[58:59], 0\n"
    "v_mov_b64 v[60:61], 0\n"
    "v_mov_b64 v[62:63], 0\n"
    "s_sleep 6\n"
    "LH%=:\n"
    "buffer_load_dwordx4 v[104:107], v0, s[16:19], s61 offen offset:0 sc1\n"
    "buffer_load_dwordx4 v[108:111], v0, s[16:19], s61 offen offset:1024 sc1\n"
    "buffer_load_dwordx4 v[112:115], v0, s[16:19], s61 offen offset:2048 sc1\n"
    "s_mov_b32 exec_hi, 0\n"
    "buffer_load_dwordx4 v[116:119], v0, s[16:19], s61 offen offset:3072 sc1\n"
    "s_mov_b32 exec_hi, -1\n"
    "s_waitcnt vmcnt(0)\n"
    "v_max3_u32 v48, v104, v105, v106\n"
    "v_max3_u32 v48, v48, v107, v108\n"
    "v_max3_u32 v48, v48, v109, v110\n"
    "v_max3_u32 v48, v48, v111, v112\n"
    "v_max3_u32 v48, v48, v113, v114\n"
    "v_max3_u32 v48, v48, v115, v116\n"
    "v_max3_u32 v48, v48, v117, v118\n"
    "v_max_u32 v48, v48, v119\n"
    "v_cmp_eq_u32 vcc, -1, v48\n"
    "s_cbranch_vccz LHK%=\n"
    "s_sleep 2\n"
    "s_add_u32 s37, s37, 1\n"
    "s_cmp_lt_u32 s37, 0x80000\n"
    "s_cbranch_scc1 LH%=\n"
    "s_branch LFAIL%=\n"
    "LHK%=:\n"
    "ds_write_b128 v8, v[104:107] offset:0\n"
    "ds_write_b128 v8, v[108:111] offset:1024\n"
    "ds_write_b128 v8, v[112:115] offset:2048\n"
    "ds_write_b128 v8, v[116:119] offset:3072\n"
    "ds_read_b128 v[72:75], v7 offset:0\n"
    "ds_read_b128 v[76:79], v7 offset:128\n"
    "ds_read_b128 v[80:83], v7 offset:256\n"
    "ds_read_b128 v[84:87], v7 offset:384\n"
    "ds_read_b128 v[88:91], v7 offset:512\n"
    "ds_read_b128 v[92:95], v7 offset:640\n"
    "ds_read_b128 v[96:99], v7 offset:768\n"
    "s_waitcnt lgkmcnt(6)\n"
    "v_smfmac_f32_16x16x64_bf16 v[56:59], v[72:75], a[0:7], v6\n"
    "ds_read_b128 v[100:103], v7 offset:896\n"
    "s_waitcnt lgkmcnt(6)\n"
    "v_smfmac_f32_16x16x64_bf16 v[60:63], v[76:79], a[8:15], v6\n"
    "ds_read_b128 v[72:75], v7 offset:1024\n"
    "s_waitcnt lgkmcnt(6)\n"
    "v_smfmac_f32_16x16x64_bf16 v[56:59], v[80:83], a[16:23], v6\n"
    "ds_read_b128 v[76:79], v7 offset:1152\n"
    "s_waitcnt lgkmcnt(6)\n"
    "v_smfmac_f32_16x16x64_bf16 v[60:63], v[84:87], a[24:31], v6\n"
    "ds_read_b128 v[80:83], v7 offset:1280\n"
    "s_waitcnt lgkmcnt(6)\n"
    "v_smfmac_f32_16x16x64_bf16 v[56:59], v[88:91], a[32:39], v6\n"
    "ds_read_b128 v[84:87], v7 offset:1408\n"
    "s_waitcnt lgkmcnt(6)\n"
    "v_smfmac_f32_16x16x64_bf16 v[60:63], v[92:95], a[40:47], v6\n"
    "ds_read_b128 v[88:91], v7 offset:1536\n"
    "s_waitcnt lgkmcnt(6)\n"
    "v_smfmac_f32_16x16x64_bf16 v[56:59], v[96:99], a[48:55], v6\n"
    "ds_read_b128 v[92:95], v7 offset:1664\n"
    "s_waitcnt lgkmcnt(6)\n"
    "v_smfmac_f32_16x16x64_bf16 v[60:63], v[100:103], a[56:63], v6\n"
    "ds_read_b128 v[96:99], v7 offset:1792\n"
    "s_waitcnt lgkmcnt(6)\n"
    "v_smfmac_f32_16x16x64_bf16 v[56:59], v[72:75], a[64:71], v6\n"
    "ds_read_b128 v[100:103], v7 offset:1920\n"
    "s_waitcnt lgkmcnt(6)\n"
    "v_smfmac_f32_16x16x64_bf16 v[60:63], v[76:79], a[72:79], v6\n"
    "ds_read_b128 v[72:75], v7 offset:2048\n"
    "s_waitcnt lgkmcnt(6)\n"
    "v_smfmac_f32_16x16x64_bf16 v[56:59], v[80:83], a[80:87], v6\n"
    "ds_read_b128 v[76:79], v7 offset:2176\n"
    "s_waitcnt lgkmcnt(6)\n"
    "v_smfmac_f32_16x16x64_bf16 v[60:63], v[84:87], a[88:95], v6\n"
    "ds_read_b128 v[80:83], v7 offset:2304\n"
    "s_waitcnt lgkmcnt(6)\n"
    "v_smfmac_f32_16x16x64_bf16 v[56:59], v[88:91], a[96:103], v6\n"
    "ds_read_b128 v[84:87], v7 offset:2432\n"
    "s_waitcnt lgkmcnt(6)\n"
    "v_smfmac_f32_16x16x64_bf16 v[60:63], v[92:95], a[104:111], v6\n"
    "ds_read_b128 v[88:91], v7 offset:2560\n"
    "s_waitcnt lgkmcnt(6)\n"
    "v_smfmac_f32_16x16x64_bf16 v[56:59], v[96:99], a[112:119], v6\n"
    "ds_read_b128 v[92:95], v7 offset:2688\n"
    "s_waitcnt lgkmcnt(6)\n"
    "v_smfmac_f32_16x16x64_bf16 v[60:63], v[100:103], a[120:127], v6\n"
    "ds_read_b128 v[96:99], v7 offset:2816\n"
    "s_waitcnt lgkmcnt(6)\n"
    "v_smfmac_f32_16x16x64_bf16 v[56:59], v[72:75], a[128:135], v6\n"
    "ds_read_b128 v[100:103], v7 offset:2944\n"
    "s_waitcnt lgkmcnt(6)\n"
    "v_smfmac_f32_16x16x64_bf16 v[60:63], v[76:79], a[136:143], v6\n"
    "ds_read_b128 v[72:75], v7 offset:3072\n"
    "s_waitcnt lgkmcnt(6)\n"
    "v_smfmac_f32_16x16x64_bf16 v[56:59], v[80:83], a[144:151], v6\n"
    "ds_read_b128 v[76:79], v7 offset:3200\n"
    "s_waitcnt lgkmcnt(6)\n"
    "v_smfmac_f32_16x16x64_bf16 v[60:63], v[84:87], a[152:159], v6\n"
    "ds_read_b128 v[80:83], v7 offset:3328\n"
    "s_waitcnt lgkmcnt(6)\n"
    "v_smfmac_f32_16x16x64_bf16 v[56:59], v[88:91], a[160:167], v6\n"
    "ds_read_b128 v[84:87], v7 offset:3456\n"
    "s_waitcnt lgkmcnt(6)\n"
    "v_smfmac_f32_16x16x64_bf16 v[60:63], v[92:95], a[168:175], v6\n"
    "s_waitcnt lgkmcnt(5)\n"
    "v_smfmac_f32_16x16x64_bf16 v[56:59], v[96:99], a[176:183], v6\n"
    "s_waitcnt lgkmcnt(4)\n"
    "v_smfmac_f32_16x16x64_bf16 v[60:63], v[100:103], a[184:191], v6\n"
    "s_waitcnt lgkmcnt(3)\n"
    "v_smfmac_f32_16x16x64_bf16 v[56:59], v[72:75], a[192:199], v6\n"
    "s_waitcnt lgkmcnt(2)\n"
    "v_smfmac_f32_16x16x64_bf16 v[60:63], v[76:79], a[200:207], v6\n"
    "s_waitcnt lgkmcnt(1)\n"
    "v_smfmac_f32_16x16x64_bf16 v[56:59], v[80:83], a[208:215], v6\n"
    "s_waitcnt lgkmcnt(0)\n"
    "v_smfmac_f32_16x16x64_bf16 v[60:63], v[84:87], a[216:223], v6\n"
    "s_lshl_b32 s39, s70, 10\n"
    "s_andn2_b32 s61, s39, 0x1fff\n"
    "s_add_u32 s61, s61, s39\n"
    "s_add_u32 s61, s61, 263192576\n"
    "s_lshr_b32 s38, s61, 8\n"
    "s_xor_b32 s38, s38, s57\n"
    "s_and_b32 s38, s38, 0x2000\n"
    "s_or_b32 s61, s61, s38\n"
    "v_add_f32 v56, v56, v58\n"
    "v_add_f32 v60, v60, v62\n"
    "v_add_f32 v56, v56, v60\n"
    "v_cvt_pk_bf16_f32 v56, v56, v56\n"
    "v_lshlrev_b32 v56, 16, v56\n"
    "v_mul_f32 v56, v56, v52\n"
    "v_cvt_pk_bf16_f32 v56, v56, v56\n"
    "v_lshlrev_b32 v56, 16, v56\n"
    "v_add_f32 v56, v56, v46\n"
    "v_cvt_pk_bf16_f32 v56, v56, v56\n"
    "v_lshlrev_b32 v56, 16, v56\n"
    "v_add_f32 v56, v56, v45\n"
    "s_nop 1\n"
    "v_mov_b32_dpp v57, v56 quad_perm:[1,1,3,3] row_mask:0xf bank_mask:0xf\n"
    "v_cvt_pk_bf16_f32 v56, v56, v57\n"
    "s_cmp_eq_u32 s70, 64\n"
    "s_cbranch_scc0 LXP%=\n"
    "s_lshl_b32 s38, s71, 10\n"
    "buffer_store_dword v56, v10, s[24:27], s38 offen sc1\n"
    "s_branch LEND%=\n"
    "LXP%=:\n"
    "v_min_u32 v56, -2, v56\n"
    "buffer_store_dword v56, v10, s[16:19], s61 offen sc1\n"
    "LFIX%=:\n"
    "s_lshl_b64 vcc, -1, s43\n"
    "s_andn2_b64 s[66:67], s[64:65], vcc\n"
    "s_and_b64 s[64:65], s[64:65], vcc\n"
    "s_max_u32 s42, s42, s43\n"
    "LFL%=:\n"
    "s_cmp_eq_u64 s[66:67], 0\n"
    "s_cbranch_scc1 LPOLL%=\n"
    "s_waitcnt vmcnt(0)\n"
    "s_ff1_i32_b64 s62, s[66:67]\n"
    "s_bitset0_b64 s[66:67], s62\n"
    "s_lshl_b32 s90, s62, 5\n"
    "s_add_u32 s90, s90, s55\n"
    "s_lshl_b32 s39, s90, 10\n"
    "s_andn2_b32 s63, s39, 0x1fff\n"
    "s_add_u32 s63, s63, s39\n"
    "s_add_u32 s63, s63, 241172480\n"
    "s_lshr_b32 s38, s63, 8\n"
    "s_xor_b32 s38, s38, s56\n"
    "s_and_b32 s38, s38, 0x2000\n"
    "s_or_b32 s63, s63, s38\n"
    "s_lshl_b32 s90, s90, 10\n"
    "buffer_load_dwordx4 v[72:75], v0, s[16:19], s63 offen sc1\n"
    "s_ff1_i32_b64 s62, s[66:67]\n"
    "s_bitset0_b64 s[66:67], s62\n"
    "s_lshl_b32 s91, s62, 5\n"
    "s_add_u32 s91, s91, s55\n"
    "s_lshl_b32 s39, s91, 10\n"
    "s_andn2_b32 s63, s39, 0x1fff\n"
    "s_add_u32 s63, s63, s39\n"
    "s_add_u32 s63, s63, 241172480\n"
    "s_lshr_b32 s38, s63, 8\n"
    "s_xor_b32 s38, s38, s56\n"
    "s_and_b32 s38, s38, 0x2000\n"
    "s_or_b32 s63, s63, s38\n"
    "s_lshl_b32 s91, s91, 10\n"
    "buffer_load_dwordx4 v[76:79], v0, s[16:19], s63 offen sc1\n"
    "s_ff1_i32_b64 s62, s[66:67]\n"
    "s_bitset0_b64 s[66:67], s62\n"
    "s_lshl_b32 s92, s62, 5\n"
    "s_add_u32 s92, s92, s55\n"
    "s_lshl_b32 s39, s92, 10\n"
    "s_andn2_b32 s63, s39, 0x1fff\n"
    "s_add_u32 s63, s63, s39\n"
    "s_add_u32 s63, s63, 241172480\n"
    "s_lshr_b32 s38, s63, 8\n"
    "s_xor_b32 s38, s38, s56\n"
    "s_and_b32 s38, s38, 0x2000\n"
    "s_or_b32 s63, s63, s38\n"
    "s_lshl_b32 s92, s92, 10\n"
    "buffer_load_dwordx4 v[80:83], v0, s[16:19], s63 offen sc1\n"
    "s_ff1_i32_b64 s62, s[66:67]\n"
    "s_bitset0_b64 s[66:67], s62\n"
    "s_lshl_b32 s93, s62, 5\n"
    "s_add_u32 s93, s93, s55\n"
    "s_lshl_b32 s39, s93, 10\n"
    "s_andn2_b32 s63, s39, 0x1fff\n"
    "s_add_u32 s63, s63, s39\n"
    "s_add_u32 s63, s63, 241172480\n"
    "s_lshr_b32 s38, s63, 8\n"
    "s_xor_b32 s38, s38, s56\n"
    "s_and_b32 s38, s38, 0x2000\n"
    "s_or_b32 s63, s63, s38\n"
    "s_lshl_b32 s93, s93, 10\n"
    "buffer_load_dwordx4 v[84:87], v0, s[16:19], s63 offen sc1\n"
    "s_ff1_i32_b64 s62, s[66:67]\n"
    "s_bitset0_b64 s[66:67], s62\n"
    "s_lshl_b32 s94, s62, 5\n"
    "s_add_u32 s94, s94, s55\n"
    "s_lshl_b32 s39, s94, 10\n"
    "s_andn2_b32 s63, s39, 0x1fff\n"
    "s_add_u32 s63, s63, s39\n"
    "s_add_u32 s63, s63, 241172480\n"
    "s_lshr_b32 s38, s63, 8\n"
    "s_xor_b32 s38, s38, s56\n"
    "s_and_b32 s38, s38, 0x2000\n"
    "s_or_b32 s63, s63, s38\n"
    "s_lshl_b32 s94, s94, 10\n"
    "buffer_load_dwordx4 v[88:91], v0, s[16:19], s63 offen sc1\n"
    "s_ff1_i32_b64 s62, s[66:67]\n"
    "s_bitset0_b64 s[66:67], s62\n"
    "s_lshl_b32 s95, s62, 5\n"
    "s_add_u32 s95, s95, s55\n"
    "s_lshl_b32 s39, s95, 10\n"
    "s_andn2_b32 s63, s39, 0x1fff\n"
    "s_add_u32 s63, s63, s39\n"
    "s_add_u32 s63, s63, 241172480\n"
    "s_lshr_b32 s38, s63, 8\n"
    "s_xor_b32 s38, s38, s56\n"
    "s_and_b32 s38, s38, 0x2000\n"
    "s_or_b32 s63, s63, s38\n"
    "s_lshl_b32 s95, s95, 10\n"
    "buffer_load_dwordx4 v[92:95], v0, s[16:19], s63 offen sc1\n"
    "s_ff1_i32_b64 s62, s[66:67]\n"
    "s_bitset0_b64 s[66:67], s62\n"
    "s_lshl_b32 s96, s62, 5\n"
    "s_add_u32 s96, s96, s55\n"
    "s_lshl_b32 s39, s96, 10\n"
    "s_andn2_b32 s63, s39, 0x1fff\n"
    "s_add_u32 s63, s63, s39\n"
    "s_add_u32 s63, s63, 241172480\n"
    "s_lshr_b32 s38, s63, 8\n"
    "s_xor_b32 s38, s38, s56\n"
    "s_and_b32 s38, s38, 0x2000\n"
    "s_or_b32 s63, s63, s38\n"
    "s_lshl_b32 s96, s96, 10\n"
    "buffer_load_dwordx4 v[96:99], v0, s[16:19], s63 offen sc1\n"
    "s_ff1_i32_b64 s62, s[66:67]\n"
    "s_bitset0_b64 s[66:67], s62\n"
    "s_lshl_b32 s97, s62, 5\n"
    "s_add_u32 s97, s97, s55\n"
    "s_lshl_b32 s39, s97, 10\n"
    "s_andn2_b32 s63, s39, 0x1fff\n"
    "s_add_u32 s63, s63, s39\n"
    "s_add_u32 s63, s63, 241172480\n"
    "s_lshr_b32 s38, s63, 8\n"
    "s_xor_b32 s38, s38, s56\n"
    "s_and_b32 s38, s38, 0x2000\n"
    "s_or_b32 s63, s63, s38\n"
    "s_lshl_b32 s97, s97, 10\n"
    "buffer_load_dwordx4 v[100:103], v0, s[16:19], s63 offen sc1\n"
    "s_waitcnt vmcnt(7)\n"
    "buffer_store_dwordx4 v[72:75], v0, s[24:27], s90 offen offset:0 sc1\n"
    "s_waitcnt vmcnt(7)\n"
    "buffer_store_dwordx4 v[76:79], v0, s[24:27], s91 offen offset:0 sc1\n"
    "s_waitcnt vmcnt(7)\n"
    "buffer_store_dwordx4 v[80:83], v0, s[24:27], s92 offen offset:0 sc1\n"
    "s_waitcnt vmcnt(7)\n"
    "buffer_store_dwordx4 v[84:87], v0, s[24:27], s93 offen offset:0 sc1\n"
    "s_waitcnt vmcnt(7)\n"
    "buffer_store_dwordx4 v[88:91], v0, s[24:27], s94 offen offset:0 sc1\n"
    "s_waitcnt vmcnt(7)\n"
    "buffer_store_dwordx4 v[92:95], v0, s[24:27], s95 offen offset:0 sc1\n"
    "s_waitcnt vmcnt(7)\n"
    "buffer_store_dwordx4 v[96:99], v0, s[24:27], s96 offen offset:0 sc1\n"
    "s_waitcnt vmcnt(7)\n"
    "buffer_store_dwordx4 v[100:103], v0, s[24:27], s97 offen offset:0 sc1\n"
    "s_branch LFL%=\n"
    "LFAIL%=:\n"
    "buffer_store_dword v6, off, s[16:19], s59 sc1\n"
    "LEND%=:\n"
    "s_endpgm\n"
    :
    : "{v0}"(hv0), "{v1}"(hv1), "{v2}"(hv2), "{v3}"(hv3), "{v4}"(hv4), "{v5}"(hv5), "{v6}"(hv6), "{v7}"(hv7), "{v8}"(hv8), "{v9}"(hv9), "{v10}"(hv10), "{v11}"(hv11), "{v12}"(hv12), "{v13}"(hv13), "{v14}"(hv14), "{v15}"(hv15), "{v16}"(hv16), "{v17}"(hv17), "{v18}"(hv18), "{v19}"(hv19), "{v20}"(hv20), "{v21}"(hv21)
    : "v255", "a255", "s101", "vcc", "scc", "memory");
  }
  __builtin_unreachable();
}
#define NPROBE 5
#define PACE_ARG
#define PACE (Pace{4400,3060,0,0,3380,2000,60})
#define BIG_LDS 163840
#define SLPB 600u
#define SLPA 600u
#define WARM 0
#define XB 5
#define KSIZE 78000
#define BOOT_BYTES 4096
__device__ __forceinline__ v4u first_state(int f, const u16* mem0) {
  v4u m0 = (v4u){~0u, ~0u, ~0u, ~0u};
  if (f >= 0 && f < 512) {
    const int tk = (f >> 3) & 7, n7 = (f & 7) ^ (tk >= 4 ? 4 : 0);
    const u16* p = mem0 + tk * DM + 64 * (f >> 6) + 32 * (n7 >> 2) + (n7 & 3);
#pragma unroll
    for (int d = 0; d < 4; ++d) m0[d] = (unsigned)p[8 * d] | ((unsigned)p[8 * d + 4] << 16);
    m0[3] = min(m0[3], 0xfffffffeu);
  }
  return m0;
}
__device__ __forceinline__ unsigned choice(int w, unsigned* bt, char* lds) {
  if (w == 0) *(volatile unsigned*)(lds + 8192) = wait_word(bt + 640 + (blockIdx.x & 7) * 32);
  __syncthreads();
  return __builtin_amdgcn_readfirstlane(*(volatile unsigned*)(lds + 8192)) & 1u;
}
__device__ __forceinline__ void boot2(int n, int w, unsigned* bt, char* lds, char* ws, const u16* mem0, const u16* Wq, const u16* Wk, const u16* Wv) {
  const rsrc_t rs = __builtin_amdgcn_make_buffer_rsrc((void*)ws, 0, ZWS_BYTES, 0x00020000);
  const int lane = lane_id(), t = n * 256 + w * 64 + lane;
  const v4u EMPTY = (v4u){~0u, ~0u, ~0u, ~0u}, m0 = first_state(t, mem0);
#pragma unroll
  for (int m = 0; m < WARM; ++m) {
    const rsrc_t rw = __builtin_amdgcn_make_buffer_rsrc((void*)(m == 0 ? Wv : (m == 1 ? Wk : Wq)), 0, DM * HD * 2, 0x00020000);
#pragma unroll
    for (int k = 0; k < 16; ++k) dma16(rw, lds + w * 1024, t * 16, k * 262144);
  }
  const unsigned cb = choice(w, bt, lds), c2 = (cb << 13) ^ 0x2000u;
#pragma unroll
  for (unsigned k = 0; k < 2; ++k) { const unsigned o = (k * 16384 + t) * 16; st4c(rs, o < (Z_SEND - Z_X2P) / 2 ? blk(Z_X2P, o, c2) : OOR, EMPTY); }
#pragma unroll
  for (int k = 0; k < 8; ++k) st4c(rs, blk(Z_CH + (1 + (k >> 2)) * MBY(2), ((k & 3) * 16384 + t) * 16, c2), (k & 3) == 2 ? m0 : EMPTY);
#pragma unroll
  for (int k = 0; k < 8; ++k) st4c(rs, blk(Z_XN, (k * 16384 + t) * 16, c2), EMPTY);
  vm0();
  __syncthreads();
  if (w == 0) {
    unsigned o = 0u;
    if (lane == 0) o = __hip_atomic_fetch_add((gu32*)(bt + 96), 1u, RLX_AGENT);
    if (__builtin_amdgcn_readfirstlane(o) == 63u) {
      const unsigned v = wait_word(bt + 128);
      if (lane < 4) __hip_atomic_store((gu32*)(bt + 256 + lane * 32), v, RLX_AGENT);
    }
  }
}
__device__ __forceinline__ void boot(int n, int w, unsigned* bt, char* lds, char* ws, const u16* x, const u16* mem0) {
  const rsrc_t rs = __builtin_amdgcn_make_buffer_rsrc((void*)ws, 0, ZWS_BYTES, 0x00020000), rx = __builtin_amdgcn_make_buffer_rsrc((void*)x, 0, S_TOK * DM * 2, 0x00020000);
  const int lane = lane_id(), t = n * 256 + w * 64 + lane;
  if (w == 0 && n < 32) {
    unsigned tm[2] = {~0u, ~0u};
    for (int q = 0; q < NPROBE; ++q) {
      const unsigned long long t0 = __builtin_amdgcn_s_memtime();
      asm volatile("s_waitcnt lgkmcnt(0)" ::: "memory");
      st1c(rs, lane ? OOR : Z_PART + (n & 3) * MBY(2) + ((q ^ n) & 1) * 8192 + n * 128, 0u);
      vm0();
      tm[q & 1] = min(tm[q & 1], (unsigned)(__builtin_amdgcn_s_memtime() - t0));
    }
    const unsigned d = (unsigned)min(max((int)(tm[0] - tm[1]), -200), 200);
    unsigned long long o = 0ull;
    if (lane == 0) o = __hip_atomic_fetch_add((gu64*)bt, ((unsigned long long)d << 32) | 1ull, RLX_AGENT);
    if (__builtin_amdgcn_readfirstlane((unsigned)o) == 31u && lane < 8)
      __hip_atomic_store((gu32*)(bt + 640 + lane * 32), (int)(__builtin_amdgcn_readfirstlane((unsigned)(o >> 32)) + d) > 0 ? 3u : 2u, RLX_AGENT);
  }
  const int v = n < 32 ? (w ? n * 3 + w - 1 : 2048) : n * 4 + w - 32;
  v4u y[6];
#pragma unroll
  for (int k = 0; k < 6; ++k) y[k] = ld4(rx, (k * 352 + v) * 1024 + lane * 16);
  const v4u EMPTY = (v4u){~0u, ~0u, ~0u, ~0u};
  const v4u m0 = first_state(t - 8192, mem0);
#pragma unroll
  for (unsigned k = 0; k < 2; ++k) { const unsigned o = (k * 24576 + t) * 16; st4c(rs, o < Z_ZEND - Z_FLG ? Z_FLG + o : OOR, (v4u){0u, 0u, 0u, 0u}); }
#pragma unroll
  for (int k = 0; k < 6; ++k) y[k] = ln_row(y[k]);
  const unsigned cb = choice(w, bt, lds), c1 = cb << 13;
#pragma unroll
  for (int k = 0; k < 6; ++k) st4c(rs, k * 352 + v < S_TOK ? blk(Z_XN, (k * 352 + v) * 1024, c1) + lane * 16 : OOR, y[k]);
#pragma unroll
  for (int k = 0; k < 3; ++k) { const unsigned o = (k * 24576 + t) * 16; st4c(rs, o < MBY(1) ? blk(Z_CH, o, c1) : OOR, k == 1 ? m0 : EMPTY); }
  vm0();
  __syncthreads();
  if (w == 0) {
    unsigned o = 0u;
    if (lane == 0) o = __hip_atomic_fetch_add((gu32*)(bt + 32), 1u, RLX_AGENT);
    if (__builtin_amdgcn_readfirstlane(o) == 95u && lane < 4) __hip_atomic_store((gu32*)(bt + 128 + lane * 32), 2u | cb, RLX_AGENT);
  }
}

__global__ __launch_bounds__(256, 1) void main_kernel(unsigned* bt, char* ws, const u16* x, const u16* mem0, const u16* Wq, const u16* Wk, const u16* Wv, const u16* Wo, const u16* w_r,
                                                      const u16* wg, const u16* wu, const u16* wd, u16* out PACE_ARG) {
  extern __shared__ char lds[];
  const int b = blockIdx.x, xc = b & 7, i = b >> 3, w = __builtin_amdgcn_readfirstlane(threadIdx.x >> 6);
  unsigned* rdy = bt + 128 + xc * 32;
  {
    const unsigned o = min((unsigned)((i & 3) * 256 + threadIdx.x) * 128u, (unsigned)KSIZE);
    asm volatile("s_mov_b32 m0, 163584\n s_getpc_b64 s[20:21]\n global_load_lds_dword %0, s[20:21]" :: "v"(o) : "s20", "s21", "memory");
  }
  if (xc == 4 || xc == 5) {
    const unsigned t0 = (unsigned)__builtin_amdgcn_s_memrealtime();
    boot2(i * 2 + xc - 4, w, bt, lds, ws, mem0, Wq, Wk, Wv);
    while ((unsigned)__builtin_amdgcn_s_memrealtime() - t0 < (xc == 4 ? SLPB : SLPA)) __builtin_amdgcn_s_sleep(2);
  }
  if (xc == 0 || xc == XB) chain8<false>(w, i, xc >> 2, rdy, ws, lds, Wq, Wk, Wv, Wo, PACE);
  if (xc == 9 - XB) chain8<true>(w, i, 2, rdy, ws, lds, Wq, Wk, Wv, Wo, PACE);
  if (xc <= 3) {
    boot(i * 3 + xc - 1, w, bt, lds, ws, x, mem0);
    t_role8(w, xc - 1, i, bt, ws, xc == 1 ? Wk : (xc == 2 ? Wv : Wq));
  }
  if ((i >> 3) != 2) o_role8(w, i >= 16 ? 1 : (xc == 7 ? 0 : 2), i >= 16 ? (i & 7) + (xc & 1) * 8 : i, rdy, ws, Wo);
  if (xc == 7) l_role8<true>(w, i - 16, rdy, ws, x, w_r, wg, wu, wd, out);
  l_role8<false>(w, i - 16, rdy, ws, x, w_r, wg, wu, wd, out);
}
extern "C" void kernel_launch(void* const* d_in, const int* in_sizes, int n_in, void* d_out, int out_size,
                              void* d_ws, size_t ws_size, hipStream_t stream) {
  char* ws = (char*)(((size_t)d_ws + BOOT_BYTES + MBY(2) - 1) & ~(size_t)(MBY(2) - 1));
  (void)hipFuncSetAttribute((const void*)main_kernel, hipFuncAttributeMaxDynamicSharedMemorySize, BIG_LDS);
  (void)hipMemsetAsync(d_ws, 0, BOOT_BYTES, stream);
  main_kernel<<<256, 256, BIG_LDS, stream>>>((unsigned*)d_ws, ws, (const u16*)d_in[0], (const u16*)d_in[1], (const u16*)d_in[2], (const u16*)d_in[3], (const u16*)d_in[4], (const u16*)d_in[5],
                                             (const u16*)d_in[8], (const u16*)d_in[9], (const u16*)d_in[10], (const u16*)d_in[11], (u16*)d_out);
}
```

```cpp
#include <hip/hip_runtime.h>
#include <hip/hip_bf16.h>
#include <stdint.h>

typedef unsigned short u16;
using bf16x8 = __attribute__((ext_vector_type(8))) short;
using f32x4 = __attribute__((ext_vector_type(4))) float;

#define S_TOK 2048
#define DM 512
#define NH 32
#define DH 128
#define HD 4096
#define MEMT 8
#define SEG 32
#define NSEG 64
#define FF 1792

__device__ __forceinline__ float bf2f(u16 v) { return __uint_as_float(((unsigned)v) << 16); }
__device__ __forceinline__ float rbf(float f) { return (float)(__bf16)f; }

template <int N> __device__ __forceinline__ float ror_add(float v) {
  return v + __int_as_float(__builtin_amdgcn_update_dpp(0, __float_as_int(v), 0x120 + N, 0xf, 0xf, false));
}

typedef unsigned v4u __attribute__((ext_vector_type(4)));
typedef unsigned v2u __attribute__((ext_vector_type(2)));
typedef unsigned v8u __attribute__((ext_vector_type(8)));
typedef __bf16 bfx8 __attribute__((ext_vector_type(8)));
typedef __bf16 bfx16 __attribute__((ext_vector_type(16)));
typedef short s16x4 __attribute__((ext_vector_type(4)));
typedef __attribute__((address_space(3))) s16x4 lds_s16x4;
#define AUX_SC1 16
#define SPINCAP (1u << 19)

__device__ __forceinline__ unsigned pk_bf16(float a, float b) {
  typedef __bf16 bfx2 __attribute__((ext_vector_type(2)));
  bfx2 v; v[0] = (__bf16)a; v[1] = (__bf16)b;
  return __builtin_bit_cast(unsigned, v);
}
__device__ __forceinline__ f32x4 mf32(v4u a, v4u b, f32x4 c) {
  return __builtin_amdgcn_mfma_f32_16x16x32_bf16(__builtin_bit_cast(bfx8, a), __builtin_bit_cast(bfx8, b), c, 0, 0, 0);
}
#define MBY(x) ((unsigned)(x) << 20)
#define SEGST 2621440u
#define Z_PART MBY(172)
#define Z_CH MBY(220)
#define Z_XN1 MBY(160)
#define Z_XN MBY(226)
#define Z_X2 MBY(230)
#define Z_X1 MBY(234)
#define Z_X3A MBY(242)
#define Z_FLG MBY(250)
#define Z_FT (Z_FLG)
#define Z_FO (Z_FT + 32u * 16384u)
#define Z_RS (Z_FO + 3u * 16384u)
#define Z_FP (Z_RS + 3u * 16384u)
#define Z_R1 (Z_FP + 3u * 16384u)
#define Z_DEC (Z_R1 + 16384u)
#define Z_ERR (Z_DEC + 16384u)
#define Z_ZEND (Z_ERR + 128u)
#define Z_X2P (MBY(251))
#define Z_HB (Z_X2P + 131072u)
#define Z_SEND (Z_HB + 33u * 16384u)
#define Z_DBG MBY(252)
#define ZWS_BYTES MBY(252)
#define OOR 0x80000000u
__host__ __device__ __forceinline__ unsigned nearof(unsigned o, unsigned c13) { return o | (((o >> 8) ^ c13) & 0x2000u); }
__host__ __device__ __forceinline__ unsigned blk(unsigned base, unsigned o, unsigned c13) { return nearof(base + (o >> 13 << 14), c13) + (o & 8191u); }
typedef __amdgpu_buffer_rsrc_t rsrc_t;
__device__ __forceinline__ v4u ld4(rsrc_t rs, unsigned off) { return __builtin_amdgcn_raw_buffer_load_b128(rs, off, 0, 0); }
__device__ __forceinline__ v4u ld4c(rsrc_t rs, unsigned off) { return __builtin_amdgcn_raw_buffer_load_b128(rs, off, 0, AUX_SC1); }
__device__ __forceinline__ v2u ld2c(rsrc_t rs, unsigned off) { return __builtin_amdgcn_raw_buffer_load_b64(rs, off, 0, AUX_SC1); }
__device__ __forceinline__ unsigned ld1c(rsrc_t rs, unsigned off) { return __builtin_amdgcn_raw_buffer_load_b32(rs, off, 0, AUX_SC1); }
__device__ __forceinline__ void st4c(rsrc_t rs, unsigned off, v4u v) { __builtin_amdgcn_raw_buffer_store_b128(v, rs, off, 0, AUX_SC1); }
__device__ __forceinline__ void st1c(rsrc_t rs, unsigned off, unsigned v) { __builtin_amdgcn_raw_buffer_store_b32(v, rs, off, 0, AUX_SC1); }
__device__ __forceinline__ v4u ld4s(rsrc_t rs, unsigned off, unsigned so) { return __builtin_amdgcn_raw_buffer_load_b128(rs, off, so, AUX_SC1); }
__device__ __forceinline__ void st4s(rsrc_t rs, unsigned off, unsigned so, v4u v) {
  __builtin_amdgcn_raw_buffer_store_b128(v, rs, off, so, AUX_SC1);
  asm volatile("s_nop 1" :: "v"(v) : "memory");
}
__device__ __forceinline__ void vm0() { asm volatile("s_waitcnt vmcnt(0)" ::: "memory"); }
#define XSTR 1040
#define PSLEEP 1
#define ZACC ((f32x4){0.f, 0.f, 0.f, 0.f})
__device__ __forceinline__ void mfw(bool first, f32x4& acc, const v4u& wa, const v4u& xb) {
  if (first) asm volatile("s_nop 1\n v_mfma_f32_16x16x32_bf16 %0, %1, %2, 0" : "=&v"(acc) : "a"(wa), "v"(xb));
  else asm volatile("s_nop 1\n v_mfma_f32_16x16x32_bf16 %0, %1, %2, %0" : "+v"(acc) : "a"(wa), "v"(xb));
}
__device__ __forceinline__ void mfence4(f32x4& a, f32x4& b, f32x4& c, f32x4& d) { asm volatile("s_nop 7\n s_nop 7" : "+v"(a), "+v"(b), "+v"(c), "+v"(d)); }
__device__ __forceinline__ void unpack8(v4u v, float (&x)[8]) {
#pragma unroll
  for (int i = 0; i < 4; ++i) { x[2 * i] = __uint_as_float(v[i] << 16); x[2 * i + 1] = __uint_as_float(v[i] & 0xffff0000u); }
}

__device__ __forceinline__ void mfxv(bool first, f32x4& acc, const v4u& xa, const v4u& wb) {
  if (first) asm volatile("s_nop 1\n v_mfma_f32_16x16x32_bf16 %0, %1, %2, 0" : "=&v"(acc) : "v"(xa), "v"(wb));
  else asm volatile("s_nop 1\n v_mfma_f32_16x16x32_bf16 %0, %1, %2, %0" : "+v"(acc) : "v"(xa), "v"(wb));
}
__device__ __forceinline__ int lane_id() { int l; asm volatile("v_mbcnt_lo_u32_b32 %0, -1, 0\n v_mbcnt_hi_u32_b32 %0, -1, %0" : "=v"(l)); return l; }
typedef __attribute__((address_space(1))) unsigned gu32;
typedef __attribute__((address_space(1))) unsigned long long gu64;
#define RLX_AGENT __ATOMIC_RELAXED, __HIP_MEMORY_SCOPE_AGENT
__device__ __forceinline__ unsigned wait_word(unsigned* p) {
  unsigned v = 0u;
  for (unsigned it = 0; it < SPINCAP && v == 0u; ++it) { v = __builtin_amdgcn_readfirstlane(__hip_atomic_load((gu32*)p, RLX_AGENT)); if (v == 0u) __builtin_amdgcn_s_sleep(2); }
  return v;
}
#define CL_K 122880
#define CL_Q (CL_K + 16384)
#define CL_MEM (CL_K + 24576)
#define CL_QX (CL_K + 32768)
#define CL_SEL (CL_QX + 2560 + 256)
#define CL_VX (CL_MEM + 2304)
#define CL_PX (CL_MEM + 4352)
#define CL_OX CL_QX
__device__ __forceinline__ v4u mkrs(const void* p, unsigned bytes) {
  return (v4u){(unsigned)(size_t)p, (unsigned)((size_t)p >> 32) & 0xffffu, bytes, 0x00020000u};
}
__device__ __forceinline__ unsigned clock_offset() {
  unsigned first = 0u, sum = 0u;
  for (int e = 0; e < 8; ++e) {
    unsigned long long r0, r, m;
    asm volatile("s_memrealtime %0\n s_waitcnt lgkmcnt(0)\n 1:\n s_memrealtime %1\n s_memtime %2\n s_waitcnt lgkmcnt(0)\n s_cmp_eq_u64 %0, %1\n s_cbranch_scc1 1b" : "=&s"(r0), "=&s"(r), "=&s"(m) :: "scc", "memory");
    const unsigned o = (unsigned)m - (unsigned)(r * 381ull >> 4);
    if (e == 0) first = o;
    sum += o - first;
  }
  return first + (unsigned)((int)sum >> 3);
}
struct Pace { int per, gat, late, rsd, fck, slip, xl; };
template <bool RST>
__device__ __forceinline__ void chain8(int w, int h, int V, unsigned off, const unsigned* rdy, char* ws, char* lds, const u16* Wq, const u16* Wk, const u16* Wv, const u16* Wo, const Pace tm) {
  const int lane = lane_id(), c = lane & 15, g = lane >> 4, l16 = lane * 16;
  const unsigned c1 = 0u, cm = V ? c1 ^ 0x2000u : c1, ps = nearof(Z_CH + V * MBY(2), cm), mo = ps + MBY(1);
  const int gp = (h >> 1) & 3, sp = (h >> 3) * 2 + (h & 1);
  if (w == 0) {
    const unsigned one = (c & 1) ? 0x3f800000u : 0x3f80u;
    *(v4u*)(lds + CL_SEL + l16) = (v4u){(c >> 1) == 0 ? one : 0u, (c >> 1) == 1 ? one : 0u, (c >> 1) == 2 ? one : 0u, (c >> 1) == 3 ? one : 0u};
  }
  const int tk = 2 * (c >> 2) + (c & 1), hf = (c >> 1) & 1, j = 8 * (w >> 1) + (lane >> 3), q = (lane >> 2) & 3, p = lane & 3;
  const int pv[40] = {w, (int)ps, h * 16384, (int)mo, h * 16384 + w * 2048, (int)cm, (V == 1 ? 4 : 3) * 524288 + h * 16384 + (w & 1) * 4096 + (w >> 1) * 256, (int)cm,
                      (int)((nearof(Z_FO + V * 16384, cm) + h * 256) | OOR), (int)nearof(Z_FT + h * 16384, cm), (int)nearof(Z_RS + V * 16384, cm),
                      (int)(nearof(Z_CH + MBY(2), cm) + MBY(1)), -1, (int)nearof(Z_X2P, cm), 0, h * 4, (int)nearof(Z_RS + (V == 2 ? 1 : V) * 16384, cm), (int)off, (int)Z_ERR, (int)(Z_DBG + (V * 32 + h) * 4096),
                      (int)(size_t)Wq, (int)((size_t)Wq >> 32) & 0xffff, (int)(size_t)Wk, (int)((size_t)Wk >> 32) & 0xffff, (int)(size_t)Wv, (int)((size_t)Wv >> 32) & 0xffff, (int)(size_t)Wo, (int)((size_t)Wo >> 32) & 0xffff,
                      (int)(size_t)ws, (int)((size_t)ws >> 32) & 0xffff, (int)ZWS_BYTES, tm.per, h < 16 ? tm.rsd : tm.gat, -tm.late, 264 - min(max(tm.late - 230, 0) / 8, 63) * 4, tm.fck, (int)(size_t)rdy, (int)((size_t)rdy >> 32), tm.slip, tm.xl};
  int hv0 = l16, hv1 = hf ? 0xEEEE : 0x4444, hv2 = CL_MEM + tk * 128 + ((2 * g + hf) ^ (c >= 8 ? 4 : 0)) * 16;
  int hv3 = CL_QX + 2 * g * 288 + (32 * w + 2 * c) * 2, hv4 = hv3 - CL_QX + CL_MEM, hv5 = CL_QX + (c & 7) * 288 + g * 16, hv6 = CL_MEM + (2 * ((c >> 2) ^ (c & 2)) + (c & 1)) * 288 + g * 16;
  int hv7 = w * 30720 + l16, hv8 = CL_PX + w * 512 + g * 128 + (c & 7) * 16, hv9 = CL_K + w * 2048 + l16, hv10 = CL_VX + w * 512 + lane * 8, hv11 = CL_OX + (4 * w + g) * 128 + (c & 7) * 16, hv12 = CL_OX + (2 * g + hf) * 128 + tk * 16;
  int hv13 = j * 16384 + (w & 1) * 4096 + ((h + j) & 31) * 128 + (lane & 7) * 16;
  int hv14 = w ? CL_Q + (w & 1) * 4096 + l16 : ((lane & 0x28) ? OOR : sp * 1024 + (2 * gp + g) * 128 + ((lane & 7) ^ (gp >= 2 ? 4 : 0)) * 16), hv15 = CL_PX + (c >> 3) * 1024 + g * 128 + (c & 7) * 16, hv20 = 0;
  int hv16 = (w * 16 + g) * 8192 + h * 256 + (c ^ ((g & 1) * 2)) * 16, hv17 = (h * 128 + 16 * (w & 1) + 8 * (w >> 1)) * 1024 + l16;
  int hv18 = (g & 1) * 4480 + (q & 1) * 1088 + (2 * (g >> 1) + (q >> 1)) * 256 + 64 * w + 32 * (q >> 1) + 8 * p, hv19 = (lane >> 2) * 1040 + 256 * (w >> 1) + 32 * (w & 1) + 64 * p;
#pragma unroll
  for (int i = 0; i < 40; ++i) hv20 = lane == i ? pv[i] : hv20;
  if constexpr (RST) {
  asm volatile(
    "s_mov_b32 s33, 0\n"
    "s_nop 1\n"
    "v_mov_b32 v119, v20\n"
    "s_nop 0\n"
    "v_readlane_b32 s61, v119, 0\n"
    "v_readlane_b32 s46, v119, 1\n"
    "v_readlane_b32 s63, v119, 2\n"
    "v_readlane_b32 s48, v119, 3\n"
    "v_readlane_b32 s55, v119, 4\n"
    "v_readlane_b32 s56, v119, 5\n"
    "v_readlane_b32 s57, v119, 6\n"
    "v_readlane_b32 s74, v119, 7\n"
    "v_readlane_b32 s58, v119, 8\n"
    "v_readlane_b32 s59, v119, 9\n"
    "v_readlane_b32 s60, v119, 10\n"
    "v_readlane_b32 s67, v119, 11\n"
    "v_readlane_b32 s1, v119, 12\n"
    "v_readlane_b32 s69, v119, 13\n"
    "v_readlane_b32 s70, v119, 14\n"
    "v_readlane_b32 s71, v119, 15\n"
    "v_readlane_b32 s73, v119, 16\n"
    "v_readlane_b32 s45, v119, 17\n"
    "v_readlane_b32 s92, v119, 36\n"
    "v_readlane_b32 s93, v119, 37\n"
    "v_readlane_b32 s35, v119, 18\n"
    "v_readlane_b32 s75, v119, 19\n"
    "v_readlane_b32 s16, v119, 28\n"
    "v_readlane_b32 s17, v119, 29\n"
    "v_readlane_b32 s18, v119, 30\n"
    "v_readlane_b32 s8, v119, 31\n"
    "v_readlane_b32 s9, v119, 32\n"
    "v_readlane_b32 s10, v119, 33\n"
    "v_readlane_b32 s0, v119, 34\n"
    "v_readlane_b32 s12, v119, 35\n"
    "v_readlane_b32 s13, v119, 38\n"
    "v_readlane_b32 s21, v119, 39\n"
    "s_mov_b32 s19, 0x20000\n"
    "v_readlane_b32 s84, v119, 20\n"
    "v_readlane_b32 s85, v119, 21\n"
    "v_readlane_b32 s86, v119, 22\n"
    "v_readlane_b32 s87, v119, 23\n"
    "v_readlane_b32 s88, v119, 24\n"
    "v_readlane_b32 s89, v119, 25\n"
    "v_readlane_b32 s90, v119, 26\n"
    "v_readlane_b32 s91, v119, 27\n"
    "s_mov_b32 s76, 0xff00ff00\n"
    "s_mov_b32 s77, 0xff00ff00\n"
    "s_mov_b32 s78, 0x00ff00ff\n"
    "s_mov_b32 s79, 0x00ff00ff\n"
    "s_mov_b32 s65, 0x3e0293ee\n"
    "s_mov_b32 s66, 0xfffefffe\n"
    "s_lshl_b32 s62, s61, 11\n"
    "s_lshl_b32 s11, s61, 10\n"
    "s_mov_b32 s36, 0\n"
    "s_mov_b32 s64, 0\n"
    "s_mov_b32 s50, 0\n"
    "s_lshl_b32 s72, s61, 2\n"
    "s_add_u32 s72, s72, 158208\n"
    "s_add_u32 s47, s46, 0x40000\n"
    "s_and_b32 s20, s61, 1\n"
    "s_lshl_b32 s20, s20, 7\n"
    "s_mov_b32 s14, 0x3f803f80\n"
    "s_mov_b32 s15, 0x3f803f80\n"
    "s_mov_b32 s82, 0x400000\n"
    "s_mov_b32 s83, 0x20000\n"
    "v_mov_b32 v112, v16\n"
    "v_mov_b32 v113, v17\n"
    "v_mov_b32 v114, v18\n"
    "v_mov_b32 v115, v19\n"
    "v_mov_b32 v117, 0\n"
    "v_mov_b32 v118, 0\n"
    "s_cmp_eq_u32 s61, 1\n"
    "s_mov_b32 s80, s90\n"
    "s_mov_b32 s81, s91\n"
    "s_cbranch_scc1 LCG%=\n"
    "s_cmp_lg_u32 s61, 0\n"
    "s_cbranch_scc1 LCF0_%=\n"
    "s_mov_b32 s80, s90\n"
    "s_mov_b32 s81, s91\n"
    "s_mul_i32 s38, s61, 4160\n"
    "s_add_u32 m0, s38, 4160\n"
    "s_mov_b32 s39, 16384\n"
    "buffer_load_dwordx4 v113, s[80:83], s39 offen lds\n"
    "s_add_u32 m0, s38, 5200\n"
    "s_mov_b32 s39, 17408\n"
    "buffer_load_dwordx4 v113, s[80:83], s39 offen lds\n"
    "s_add_u32 m0, s38, 6240\n"
    "s_mov_b32 s39, 20480\n"
    "buffer_load_dwordx4 v113, s[80:83], s39 offen lds\n"
    "s_add_u32 m0, s38, 7280\n"
    "s_mov_b32 s39, 21504\n"
    "buffer_load_dwordx4 v113, s[80:83], s39 offen lds\n"
    "LCF0_%=:\n"
    "s_mov_b32 s80, s90\n"
    "s_mov_b32 s81, s91\n"
    "s_mul_i32 s38, s61, 4160\n"
    "s_add_u32 m0, s38, 0\n"
    "s_mov_b32 s39, 0\n"
    "buffer_load_dwordx4 v113, s[80:83], s39 offen lds\n"
    "s_add_u32 m0, s38, 1040\n"
    "s_mov_b32 s39, 1024\n"
    "buffer_load_dwordx4 v113, s[80:83], s39 offen lds\n"
    "s_add_u32 m0, s38, 2080\n"
    "s_mov_b32 s39, 4096\n"
    "buffer_load_dwordx4 v113, s[80:83], s39 offen lds\n"
    "s_add_u32 m0, s38, 3120\n"
    "s_mov_b32 s39, 5120\n"
    "buffer_load_dwordx4 v113, s[80:83], s39 offen lds\n"
    "s_cmp_lg_u32 s61, 0\n"
    "s_cbranch_scc1 LCF1_%=\n"
    "s_mul_i32 s38, s61, 4160\n"
    "s_add_u32 m0, s38, 22080\n"
    "s_mov_b32 s39, 18432\n"
    "buffer_load_dwordx4 v113, s[80:83], s39 offen lds\n"
    "s_add_u32 m0, s38, 23120\n"
    "s_mov_b32 s39, 19456\n"
    "buffer_load_dwordx4 v113, s[80:83], s39 offen lds\n"
    "s_add_u32 m0, s38, 24160\n"
    "s_mov_b32 s39, 22528\n"
    "buffer_load_dwordx4 v113, s[80:83], s39 offen lds\n"
    "s_add_u32 m0, s38, 25200\n"
    "s_mov_b32 s39, 23552\n"
    "buffer_load_dwordx4 v113, s[80:83], s39 offen lds\n"
    "LCF1_%=:\n"
    "s_mul_i32 s38, s61, 4160\n"
    "s_add_u32 m0, s38, 17920\n"
    "s_mov_b32 s39, 2048\n"
    "buffer_load_dwordx4 v113, s[80:83], s39 offen lds\n"
    "s_add_u32 m0, s38, 18960\n"
    "s_mov_b32 s39, 3072\n"
    "buffer_load_dwordx4 v113, s[80:83], s39 offen lds\n"
    "s_add_u32 m0, s38, 20000\n"
    "s_mov_b32 s39, 6144\n"
    "buffer_load_dwordx4 v113, s[80:83], s39 offen lds\n"
    "s_add_u32 m0, s38, 21040\n"
    "s_mov_b32 s39, 7168\n"
    "buffer_load_dwordx4 v113, s[80:83], s39 offen lds\n"
    "s_cmp_lg_u32 s61, 0\n"
    "s_cbranch_scc1 LCF2_%=\n"
    "s_mul_i32 s38, s61, 4160\n"
    "s_add_u32 m0, s38, 40000\n"
    "s_mov_b32 s39, 49152\n"
    "buffer_load_dwordx4 v113, s[80:83], s39 offen lds\n"
    "s_add_u32 m0, s38, 41040\n"
    "s_mov_b32 s39, 50176\n"
    "buffer_load_dwordx4 v113, s[80:83], s39 offen lds\n"
    "s_add_u32 m0, s38, 42080\n"
    "s_mov_b32 s39, 53248\n"
    "buffer_load_dwordx4 v113, s[80:83], s39 offen lds\n"
    "s_add_u32 m0, s38, 43120\n"
    "s_mov_b32 s39, 54272\n"
    "buffer_load_dwordx4 v113, s[80:83], s39 offen lds\n"
    "LCF2_%=:\n"
    "s_mul_i32 s38, s61, 4160\n"
    "s_add_u32 m0, s38, 35840\n"
    "s_mov_b32 s39, 32768\n"
    "buffer_load_dwordx4 v113, s[80:83], s39 offen lds\n"
    "s_add_u32 m0, s38, 36880\n"
    "s_mov_b32 s39, 33792\n"
    "buffer_load_dwordx4 v113, s[80:83], s39 offen lds\n"
    "s_add_u32 m0, s38, 37920\n"
    "s_mov_b32 s39, 36864\n"
    "buffer_load_dwordx4 v113, s[80:83], s39 offen lds\n"
    "s_add_u32 m0, s38, 38960\n"
    "s_mov_b32 s39, 37888\n"
    "buffer_load_dwordx4 v113, s[80:83], s39 offen lds\n"
    "s_cmp_lg_u32 s61, 0\n"
    "s_cbranch_scc1 LCF3_%=\n"
    "s_mul_i32 s38, s61, 4160\n"
    "s_add_u32 m0, s38, 57920\n"
    "s_mov_b32 s39, 51200\n"
    "buffer_load_dwordx4 v113, s[80:83], s39 offen lds\n"
    "s_add_u32 m0, s38, 58960\n"
    "s_mov_b32 s39, 52224\n"
    "buffer_load_dwordx4 v113, s[80:83], s39 offen lds\n"
    "s_add_u32 m0, s38, 60000\n"
    "s_mov_b32 s39, 55296\n"
    "buffer_load_dwordx4 v113, s[80:83], s39 offen lds\n"
    "s_add_u32 m0, s38, 61040\n"
    "s_mov_b32 s39, 56320\n"
    "buffer_load_dwordx4 v113, s[80:83], s39 offen lds\n"
    "LCF3_%=:\n"
    "s_mul_i32 s38, s61, 4160\n"
    "s_add_u32 m0, s38, 53760\n"
    "s_mov_b32 s39, 34816\n"
    "buffer_load_dwordx4 v113, s[80:83], s39 offen lds\n"
    "s_add_u32 m0, s38, 54800\n"
    "s_mov_b32 s39, 35840\n"
    "buffer_load_dwordx4 v113, s[80:83], s39 offen lds\n"
    "s_add_u32 m0, s38, 55840\n"
    "s_mov_b32 s39, 38912\n"
    "buffer_load_dwordx4 v113, s[80:83], s39 offen lds\n"
    "s_add_u32 m0, s38, 56880\n"
    "s_mov_b32 s39, 39936\n"
    "buffer_load_dwordx4 v113, s[80:83], s39 offen lds\n"
    "LCG%=:\n"
    "s_waitcnt vmcnt(12)\n"
    "s_barrier\n"
    "s_mul_i32 s38, s61, 4160\n"
    "s_add_u32 m0, s38, 71680\n"
    "s_mov_b32 s39, 65536\n"
    "buffer_load_dwordx4 v113, s[80:83], s39 offen lds\n"
    "s_add_u32 m0, s38, 72720\n"
    "s_mov_b32 s39, 66560\n"
    "buffer_load_dwordx4 v113, s[80:83], s39 offen lds\n"
    "s_add_u32 m0, s38, 73760\n"
    "s_mov_b32 s39, 69632\n"
    "buffer_load_dwordx4 v113, s[80:83], s39 offen lds\n"
    "s_add_u32 m0, s38, 74800\n"
    "s_mov_b32 s39, 70656\n"
    "buffer_load_dwordx4 v113, s[80:83], s39 offen lds\n"
    "v_add_u32 v116, 0, v115\n"
    "ds_read_b64_tr_b16 v[168:169], v116 offset:0\n"
    "ds_read_b64_tr_b16 v[184:185], v116 offset:8\n"
    "ds_read_b64_tr_b16 v[200:201], v116 offset:16\n"
    "ds_read_b64_tr_b16 v[216:217], v116 offset:24\n"
    "ds_read_b64_tr_b16 v[232:233], v116 offset:512\n"
    "ds_read_b64_tr_b16 v[248:249], v116 offset:520\n"
    "ds_read_b64_tr_b16 v[104:105], v116 offset:528\n"
    "ds_read_b64_tr_b16 v[32:33], v116 offset:536\n"
    "s_waitcnt lgkmcnt(0)\n"
    "s_waitcnt vmcnt(12)\n"
    "s_barrier\n"
    "s_mul_i32 s38, s61, 4160\n"
    "s_add_u32 m0, s38, 89600\n"
    "s_mov_b32 s39, 67584\n"
    "buffer_load_dwordx4 v113, s[80:83], s39 offen lds\n"
    "s_add_u32 m0, s38, 90640\n"
    "s_mov_b32 s39, 68608\n"
    "buffer_load_dwordx4 v113, s[80:83], s39 offen lds\n"
    "s_add_u32 m0, s38, 91680\n"
    "s_mov_b32 s39, 71680\n"
    "buffer_load_dwordx4 v113, s[80:83], s39 offen lds\n"
    "s_add_u32 m0, s38, 92720\n"
    "s_mov_b32 s39, 72704\n"
    "buffer_load_dwordx4 v113, s[80:83], s39 offen lds\n"
    "v_add_u32 v116, 17920, v115\n"
    "ds_read_b64_tr_b16 v[170:171], v116 offset:0\n"
    "ds_read_b64_tr_b16 v[186:187], v116 offset:8\n"
    "ds_read_b64_tr_b16 v[202:203], v116 offset:16\n"
    "ds_read_b64_tr_b16 v[218:219], v116 offset:24\n"
    "ds_read_b64_tr_b16 v[234:235], v116 offset:512\n"
    "ds_read_b64_tr_b16 v[250:251], v116 offset:520\n"
    "ds_read_b64_tr_b16 v[106:107], v116 offset:528\n"
    "ds_read_b64_tr_b16 v[34:35], v116 offset:536\n"
    "s_waitcnt lgkmcnt(0)\n"
    "s_waitcnt vmcnt(12)\n"
    "s_barrier\n"
    "s_mul_i32 s38, s61, 4160\n"
    "s_add_u32 m0, s38, 0\n"
    "s_mov_b32 s39, 98304\n"
    "buffer_load_dwordx4 v113, s[80:83], s39 offen lds\n"
    "s_add_u32 m0, s38, 1040\n"
    "s_mov_b32 s39, 99328\n"
    "buffer_load_dwordx4 v113, s[80:83], s39 offen lds\n"
    "s_add_u32 m0, s38, 2080\n"
    "s_mov_b32 s39, 102400\n"
    "buffer_load_dwordx4 v113, s[80:83], s39 offen lds\n"
    "s_add_u32 m0, s38, 3120\n"
    "s_mov_b32 s39, 103424\n"
    "buffer_load_dwordx4 v113, s[80:83], s39 offen lds\n"
    "v_add_u32 v116, 35840, v115\n"
    "ds_read_b64_tr_b16 v[172:173], v116 offset:0\n"
    "ds_read_b64_tr_b16 v[188:189], v116 offset:8\n"
    "ds_read_b64_tr_b16 v[204:205], v116 offset:16\n"
    "ds_read_b64_tr_b16 v[220:221], v116 offset:24\n"
    "ds_read_b64_tr_b16 v[236:237], v116 offset:512\n"
    "ds_read_b64_tr_b16 v[252:253], v116 offset:520\n"
    "ds_read_b64_tr_b16 v[108:109], v116 offset:528\n"
    "ds_read_b64_tr_b16 v[36:37], v116 offset:536\n"
    "s_waitcnt lgkmcnt(0)\n"
    "s_waitcnt vmcnt(12)\n"
    "s_barrier\n"
    "s_mul_i32 s38, s61, 4160\n"
    "s_add_u32 m0, s38, 17920\n"
    "s_mov_b32 s39, 100352\n"
    "buffer_load_dwordx4 v113, s[80:83], s39 offen lds\n"
    "s_add_u32 m0, s38, 18960\n"
    "s_mov_b32 s39, 101376\n"
    "buffer_load_dwordx4 v113, s[80:83], s39 offen lds\n"
    "s_add_u32 m0, s38, 20000\n"
    "s_mov_b32 s39, 104448\n"
    "buffer_load_dwordx4 v113, s[80:83], s39 offen lds\n"
    "s_add_u32 m0, s38, 21040\n"
    "s_mov_b32 s39, 105472\n"
    "buffer_load_dwordx4 v113, s[80:83], s39 offen lds\n"
    "v_add_u32 v116, 53760, v115\n"
    "ds_read_b64_tr_b16 v[174:175], v116 offset:0\n"
    "ds_read_b64_tr_b16 v[190:191], v116 offset:8\n"
    "ds_read_b64_tr_b16 v[206:207], v116 offset:16\n"
    "ds_read_b64_tr_b16 v[222:223], v116 offset:24\n"
    "ds_read_b64_tr_b16 v[238:239], v116 offset:512\n"
    "ds_read_b64_tr_b16 v[254:255], v116 offset:520\n"
    "ds_read_b64_tr_b16 v[110:111], v116 offset:528\n"
    "ds_read_b64_tr_b16 v[38:39], v116 offset:536\n"
    "s_waitcnt lgkmcnt(0)\n"
    "s_waitcnt vmcnt(12)\n"
    "s_barrier\n"
    "s_mov_b32 s80, s86\n"
    "s_mov_b32 s81, s87\n"
    "s_mul_i32 s38, s61, 4480\n"
    "s_add_u32 m0, s38, 35840\n"
    "s_mov_b32 s39, 0\n"
    "buffer_load_dwordx4 v112, s[80:83], s39 offen lds\n"
    "s_add_u32 m0, s38, 36928\n"
    "s_mov_b32 s39, 32768\n"
    "buffer_load_dwordx4 v112, s[80:83], s39 offen lds\n"
    "s_add_u32 m0, s38, 38016\n"
    "s_mov_b32 s39, 65536\n"
    "buffer_load_dwordx4 v112, s[80:83], s39 offen lds\n"
    "s_add_u32 m0, s38, 39104\n"
    "s_mov_b32 s39, 98304\n"
    "buffer_load_dwordx4 v112, s[80:83], s39 offen lds\n"
    "v_add_u32 v116, 71680, v115\n"
    "ds_read_b64_tr_b16 v[176:177], v116 offset:0\n"
    "ds_read_b64_tr_b16 v[192:193], v116 offset:8\n"
    "ds_read_b64_tr_b16 v[208:209], v116 offset:16\n"
    "ds_read_b64_tr_b16 v[224:225], v116 offset:24\n"
    "ds_read_b64_tr_b16 v[240:241], v116 offset:512\n"
    "ds_read_b64_tr_b16 v[96:97], v116 offset:520\n"
    "ds_read_b64_tr_b16 v[24:25], v116 offset:528\n"
    "ds_read_b64_tr_b16 v[40:41], v116 offset:536\n"
    "s_waitcnt lgkmcnt(0)\n"
    "s_waitcnt vmcnt(12)\n"
    "s_barrier\n"
    "s_mul_i32 s38, s61, 4480\n"
    "s_add_u32 m0, s38, 53760\n"
    "s_mov_b32 s39, 524288\n"
    "buffer_load_dwordx4 v112, s[80:83], s39 offen lds\n"
    "s_add_u32 m0, s38, 54848\n"
    "s_mov_b32 s39, 557056\n"
    "buffer_load_dwordx4 v112, s[80:83], s39 offen lds\n"
    "s_add_u32 m0, s38, 55936\n"
    "s_mov_b32 s39, 589824\n"
    "buffer_load_dwordx4 v112, s[80:83], s39 offen lds\n"
    "s_add_u32 m0, s38, 57024\n"
    "s_mov_b32 s39, 622592\n"
    "buffer_load_dwordx4 v112, s[80:83], s39 offen lds\n"
    "v_add_u32 v116, 89600, v115\n"
    "ds_read_b64_tr_b16 v[178:179], v116 offset:0\n"
    "ds_read_b64_tr_b16 v[194:195], v116 offset:8\n"
    "ds_read_b64_tr_b16 v[210:211], v116 offset:16\n"
    "ds_read_b64_tr_b16 v[226:227], v116 offset:24\n"
    "ds_read_b64_tr_b16 v[242:243], v116 offset:512\n"
    "ds_read_b64_tr_b16 v[98:99], v116 offset:520\n"
    "ds_read_b64_tr_b16 v[26:27], v116 offset:528\n"
    "ds_read_b64_tr_b16 v[42:43], v116 offset:536\n"
    "s_waitcnt lgkmcnt(0)\n"
    "s_waitcnt vmcnt(12)\n"
    "s_barrier\n"
    "s_mul_i32 s38, s61, 4480\n"
    "s_add_u32 m0, s38, 71680\n"
    "s_mov_b32 s39, 1048576\n"
    "buffer_load_dwordx4 v112, s[80:83], s39 offen lds\n"
    "s_add_u32 m0, s38, 72768\n"
    "s_mov_b32 s39, 1081344\n"
    "buffer_load_dwordx4 v112, s[80:83], s39 offen lds\n"
    "s_add_u32 m0, s38, 73856\n"
    "s_mov_b32 s39, 1114112\n"
    "buffer_load_dwordx4 v112, s[80:83], s39 offen lds\n"
    "s_add_u32 m0, s38, 74944\n"
    "s_mov_b32 s39, 1146880\n"
    "buffer_load_dwordx4 v112, s[80:83], s39 offen lds\n"
    "v_add_u32 v116, 0, v115\n"
    "ds_read_b64_tr_b16 v[180:181], v116 offset:0\n"
    "ds_read_b64_tr_b16 v[196:197], v116 offset:8\n"
    "ds_read_b64_tr_b16 v[212:213], v116 offset:16\n"
    "ds_read_b64_tr_b16 v[228:229], v116 offset:24\n"
    "ds_read_b64_tr_b16 v[244:245], v116 offset:512\n"
    "ds_read_b64_tr_b16 v[100:101], v116 offset:520\n"
    "ds_read_b64_tr_b16 v[28:29], v116 offset:528\n"
    "ds_read_b64_tr_b16 v[44:45], v116 offset:536\n"
    "s_waitcnt lgkmcnt(0)\n"
    "s_waitcnt vmcnt(12)\n"
    "s_barrier\n"
    "s_mul_i32 s38, s61, 4480\n"
    "s_add_u32 m0, s38, 89600\n"
    "s_mov_b32 s39, 1572864\n"
    "buffer_load_dwordx4 v112, s[80:83], s39 offen lds\n"
    "s_add_u32 m0, s38, 90688\n"
    "s_mov_b32 s39, 1605632\n"
    "buffer_load_dwordx4 v112, s[80:83], s39 offen lds\n"
    "s_add_u32 m0, s38, 91776\n"
    "s_mov_b32 s39, 1638400\n"
    "buffer_load_dwordx4 v112, s[80:83], s39 offen lds\n"
    "s_add_u32 m0, s38, 92864\n"
    "s_mov_b32 s39, 1671168\n"
    "buffer_load_dwordx4 v112, s[80:83], s39 offen lds\n"
    "v_add_u32 v116, 17920, v115\n"
    "ds_read_b64_tr_b16 v[182:183], v116 offset:0\n"
    "ds_read_b64_tr_b16 v[198:199], v116 offset:8\n"
    "ds_read_b64_tr_b16 v[214:215], v116 offset:16\n"
    "ds_read_b64_tr_b16 v[230:231], v116 offset:24\n"
    "ds_read_b64_tr_b16 v[246:247], v116 offset:512\n"
    "ds_read_b64_tr_b16 v[102:103], v116 offset:520\n"
    "ds_read_b64_tr_b16 v[30:31], v116 offset:528\n"
    "ds_read_b64_tr_b16 v[46:47], v116 offset:536\n"
    "ds_write_b128 v9, v[24:27] offset:0\n"
    "s_waitcnt lgkmcnt(2)\n"
    "ds_write_b128 v9, v[28:31] offset:1024\n"
    "ds_write_b128 v9, v[32:35] offset:8192\n"
    "ds_write_b128 v9, v[36:39] offset:9216\n"
    "ds_write_b128 v9, v[40:43] offset:16384\n"
    "s_waitcnt lgkmcnt(5)\n"
    "ds_write_b128 v9, v[44:47] offset:17408\n"
    "s_waitcnt lgkmcnt(0)\n"
    "s_waitcnt vmcnt(12)\n"
    "s_barrier\n"
    "s_mul_i32 s38, s61, 4480\n"
    "s_add_u32 m0, s38, 0\n"
    "s_mov_b32 s39, 2097152\n"
    "buffer_load_dwordx4 v112, s[80:83], s39 offen lds\n"
    "s_add_u32 m0, s38, 1088\n"
    "s_mov_b32 s39, 2129920\n"
    "buffer_load_dwordx4 v112, s[80:83], s39 offen lds\n"
    "s_add_u32 m0, s38, 2176\n"
    "s_mov_b32 s39, 2162688\n"
    "buffer_load_dwordx4 v112, s[80:83], s39 offen lds\n"
    "s_add_u32 m0, s38, 3264\n"
    "s_mov_b32 s39, 2195456\n"
    "buffer_load_dwordx4 v112, s[80:83], s39 offen lds\n"
    "v_add_u32 v116, 35840, v114\n"
    "ds_read_b64_tr_b16 v[64:65], v116 offset:0\n"
    "ds_read_b64_tr_b16 v[66:67], v116 offset:2176\n"
    "ds_read_b64_tr_b16 v[68:69], v116 offset:8960\n"
    "ds_read_b64_tr_b16 v[70:71], v116 offset:11136\n"
    "v_xor_b32 v116, 32, v116\n"
    "ds_read_b64_tr_b16 v[72:73], v116 offset:0\n"
    "ds_read_b64_tr_b16 v[74:75], v116 offset:2176\n"
    "ds_read_b64_tr_b16 v[76:77], v116 offset:8960\n"
    "ds_read_b64_tr_b16 v[78:79], v116 offset:11136\n"
    "s_waitcnt lgkmcnt(0)\n"
    "s_waitcnt vmcnt(12)\n"
    "s_barrier\n"
    "s_mul_i32 s38, s61, 4480\n"
    "s_add_u32 m0, s38, 17920\n"
    "s_mov_b32 s39, 2621440\n"
    "buffer_load_dwordx4 v112, s[80:83], s39 offen lds\n"
    "s_add_u32 m0, s38, 19008\n"
    "s_mov_b32 s39, 2654208\n"
    "buffer_load_dwordx4 v112, s[80:83], s39 offen lds\n"
    "s_add_u32 m0, s38, 20096\n"
    "s_mov_b32 s39, 2686976\n"
    "buffer_load_dwordx4 v112, s[80:83], s39 offen lds\n"
    "s_add_u32 m0, s38, 21184\n"
    "s_mov_b32 s39, 2719744\n"
    "buffer_load_dwordx4 v112, s[80:83], s39 offen lds\n"
    "v_add_u32 v116, 53760, v114\n"
    "ds_read_b64_tr_b16 v[80:81], v116 offset:0\n"
    "ds_read_b64_tr_b16 v[82:83], v116 offset:2176\n"
    "ds_read_b64_tr_b16 v[84:85], v116 offset:8960\n"
    "ds_read_b64_tr_b16 v[86:87], v116 offset:11136\n"
    "v_xor_b32 v116, 32, v116\n"
    "ds_read_b64_tr_b16 v[88:89], v116 offset:0\n"
    "ds_read_b64_tr_b16 v[90:91], v116 offset:2176\n"
    "ds_read_b64_tr_b16 v[92:93], v116 offset:8960\n"
    "ds_read_b64_tr_b16 v[94:95], v116 offset:11136\n"
    "s_waitcnt lgkmcnt(0)\n"
    "s_waitcnt vmcnt(12)\n"
    "s_barrier\n"
    "s_mul_i32 s38, s61, 4480\n"
    "s_add_u32 m0, s38, 35840\n"
    "s_mov_b32 s39, 3145728\n"
    "buffer_load_dwordx4 v112, s[80:83], s39 offen lds\n"
    "s_add_u32 m0, s38, 36928\n"
    "s_mov_b32 s39, 3178496\n"
    "buffer_load_dwordx4 v112, s[80:83], s39 offen lds\n"
    "s_add_u32 m0, s38, 38016\n"
    "s_mov_b32 s39, 3211264\n"
    "buffer_load_dwordx4 v112, s[80:83], s39 offen lds\n"
    "s_add_u32 m0, s38, 39104\n"
    "s_mov_b32 s39, 3244032\n"
    "buffer_load_dwordx4 v112, s[80:83], s39 offen lds\n"
    "v_add_u32 v116, 71680, v114\n"
    "ds_read_b64_tr_b16 a[80:81], v116 offset:0\n"
    "ds_read_b64_tr_b16 a[82:83], v116 offset:2176\n"
    "ds_read_b64_tr_b16 a[84:85], v116 offset:8960\n"
    "ds_read_b64_tr_b16 a[86:87], v116 offset:11136\n"
    "v_xor_b32 v116, 32, v116\n"
    "ds_read_b64_tr_b16 a[88:89], v116 offset:0\n"
    "ds_read_b64_tr_b16 a[90:91], v116 offset:2176\n"
    "ds_read_b64_tr_b16 a[92:93], v116 offset:8960\n"
    "ds_read_b64_tr_b16 a[94:95], v116 offset:11136\n"
    "s_waitcnt lgkmcnt(0)\n"
    "s_waitcnt vmcnt(12)\n"
    "s_barrier\n"
    "s_mul_i32 s38, s61, 4480\n"
    "s_add_u32 m0, s38, 53760\n"
    "s_mov_b32 s39, 3670016\n"
    "buffer_load_dwordx4 v112, s[80:83], s39 offen lds\n"
    "s_add_u32 m0, s38, 54848\n"
    "s_mov_b32 s39, 3702784\n"
    "buffer_load_dwordx4 v112, s[80:83], s39 offen lds\n"
    "s_add_u32 m0, s38, 55936\n"
    "s_mov_b32 s39, 3735552\n"
    "buffer_load_dwordx4 v112, s[80:83], s39 offen lds\n"
    "s_add_u32 m0, s38, 57024\n"
    "s_mov_b32 s39, 3768320\n"
    "buffer_load_dwordx4 v112, s[80:83], s39 offen lds\n"
    "v_add_u32 v116, 89600, v114\n"
    "ds_read_b64_tr_b16 a[96:97], v116 offset:0\n"
    "ds_read_b64_tr_b16 a[98:99], v116 offset:2176\n"
    "ds_read_b64_tr_b16 a[100:101], v116 offset:8960\n"
    "ds_read_b64_tr_b16 a[102:103], v116 offset:11136\n"
    "v_xor_b32 v116, 32, v116\n"
    "ds_read_b64_tr_b16 a[104:105], v116 offset:0\n"
    "ds_read_b64_tr_b16 a[106:107], v116 offset:2176\n"
    "ds_read_b64_tr_b16 a[108:109], v116 offset:8960\n"
    "ds_read_b64_tr_b16 a[110:111], v116 offset:11136\n"
    "s_waitcnt lgkmcnt(0)\n"
    "s_waitcnt vmcnt(12)\n"
    "s_barrier\n"
    "s_mov_b32 s80, s84\n"
    "s_mov_b32 s81, s85\n"
    "s_mul_i32 s38, s61, 4480\n"
    "s_add_u32 m0, s38, 71680\n"
    "s_mov_b32 s39, 0\n"
    "buffer_load_dwordx4 v112, s[80:83], s39 offen lds\n"
    "s_add_u32 m0, s38, 72768\n"
    "s_mov_b32 s39, 32768\n"
    "buffer_load_dwordx4 v112, s[80:83], s39 offen lds\n"
    "s_add_u32 m0, s38, 73856\n"
    "s_mov_b32 s39, 65536\n"
    "buffer_load_dwordx4 v112, s[80:83], s39 offen lds\n"
    "s_add_u32 m0, s38, 74944\n"
    "s_mov_b32 s39, 98304\n"
    "buffer_load_dwordx4 v112, s[80:83], s39 offen lds\n"
    "v_add_u32 v116, 0, v114\n"
    "ds_read_b64_tr_b16 a[112:113], v116 offset:0\n"
    "ds_read_b64_tr_b16 a[114:115], v116 offset:2176\n"
    "ds_read_b64_tr_b16 a[116:117], v116 offset:8960\n"
    "ds_read_b64_tr_b16 a[118:119], v116 offset:11136\n"
    "v_xor_b32 v116, 32, v116\n"
    "ds_read_b64_tr_b16 a[120:121], v116 offset:0\n"
    "ds_read_b64_tr_b16 a[122:123], v116 offset:2176\n"
    "ds_read_b64_tr_b16 a[124:125], v116 offset:8960\n"
    "ds_read_b64_tr_b16 a[126:127], v116 offset:11136\n"
    "s_waitcnt lgkmcnt(0)\n"
    "s_waitcnt vmcnt(12)\n"
    "s_barrier\n"
    "s_mul_i32 s38, s61, 4480\n"
    "s_add_u32 m0, s38, 89600\n"
    "s_mov_b32 s39, 524288\n"
    "buffer_load_dwordx4 v112, s[80:83], s39 offen lds\n"
    "s_add_u32 m0, s38, 90688\n"
    "s_mov_b32 s39, 557056\n"
    "buffer_load_dwordx4 v112, s[80:83], s39 offen lds\n"
    "s_add_u32 m0, s38, 91776\n"
    "s_mov_b32 s39, 589824\n"
    "buffer_load_dwordx4 v112, s[80:83], s39 offen lds\n"
    "s_add_u32 m0, s38, 92864\n"
    "s_mov_b32 s39, 622592\n"
    "buffer_load_dwordx4 v112, s[80:83], s39 offen lds\n"
    "v_add_u32 v116, 17920, v114\n"
    "ds_read_b64_tr_b16 a[128:129], v116 offset:0\n"
    "ds_read_b64_tr_b16 a[130:131], v116 offset:2176\n"
    "ds_read_b64_tr_b16 a[132:133], v116 offset:8960\n"
    "ds_read_b64_tr_b16 a[134:135], v116 offset:11136\n"
    "v_xor_b32 v116, 32, v116\n"
    "ds_read_b64_tr_b16 a[136:137], v116 offset:0\n"
    "ds_read_b64_tr_b16 a[138:139], v116 offset:2176\n"
    "ds_read_b64_tr_b16 a[140:141], v116 offset:8960\n"
    "ds_read_b64_tr_b16 a[142:143], v116 offset:11136\n"
    "s_waitcnt lgkmcnt(0)\n"
    "s_waitcnt vmcnt(12)\n"
    "s_barrier\n"
    "s_mul_i32 s38, s61, 4480\n"
    "s_add_u32 m0, s38, 0\n"
    "s_mov_b32 s39, 1048576\n"
    "buffer_load_dwordx4 v112, s[80:83], s39 offen lds\n"
    "s_add_u32 m0, s38, 1088\n"
    "s_mov_b32 s39, 1081344\n"
    "buffer_load_dwordx4 v112, s[80:83], s39 offen lds\n"
    "s_add_u32 m0, s38, 2176\n"
    "s_mov_b32 s39, 1114112\n"
    "buffer_load_dwordx4 v112, s[80:83], s39 offen lds\n"
    "s_add_u32 m0, s38, 3264\n"
    "s_mov_b32 s39, 1146880\n"
    "buffer_load_dwordx4 v112, s[80:83], s39 offen lds\n"
    "v_add_u32 v116, 35840, v114\n"
    "ds_read_b64_tr_b16 a[144:145], v116 offset:0\n"
    "ds_read_b64_tr_b16 a[146:147], v116 offset:2176\n"
    "ds_read_b64_tr_b16 a[148:149], v116 offset:8960\n"
    "ds_read_b64_tr_b16 a[150:151], v116 offset:11136\n"
    "v_xor_b32 v116, 32, v116\n"
    "ds_read_b64_tr_b16 a[152:153], v116 offset:0\n"
    "ds_read_b64_tr_b16 a[154:155], v116 offset:2176\n"
    "ds_read_b64_tr_b16 a[156:157], v116 offset:8960\n"
    "ds_read_b64_tr_b16 a[158:159], v116 offset:11136\n"
    "s_waitcnt lgkmcnt(0)\n"
    "s_waitcnt vmcnt(12)\n"
    "s_barrier\n"
    "s_mul_i32 s38, s61, 4480\n"
    "s_add_u32 m0, s38, 17920\n"
    "s_mov_b32 s39, 1572864\n"
    "buffer_load_dwordx4 v112, s[80:83], s39 offen lds\n"
    "s_add_u32 m0, s38, 19008\n"
    "s_mov_b32 s39, 1605632\n"
    "buffer_load_dwordx4 v112, s[80:83], s39 offen lds\n"
    "s_add_u32 m0, s38, 20096\n"
    "s_mov_b32 s39, 1638400\n"
    "buffer_load_dwordx4 v112, s[80:83], s39 offen lds\n"
    "s_add_u32 m0, s38, 21184\n"
    "s_mov_b32 s39, 1671168\n"
    "buffer_load_dwordx4 v112, s[80:83], s39 offen lds\n"
    "v_add_u32 v116, 53760, v114\n"
    "ds_read_b64_tr_b16 a[160:161], v116 offset:0\n"
    "ds_read_b64_tr_b16 a[162:163], v116 offset:2176\n"
    "ds_read_b64_tr_b16 a[164:165], v116 offset:8960\n"
    "ds_read_b64_tr_b16 a[166:167], v116 offset:11136\n"
    "v_xor_b32 v116, 32, v116\n"
    "ds_read_b64_tr_b16 a[168:169], v116 offset:0\n"
    "ds_read_b64_tr_b16 a[170:171], v116 offset:2176\n"
    "ds_read_b64_tr_b16 a[172:173], v116 offset:8960\n"
    "ds_read_b64_tr_b16 a[174:175], v116 offset:11136\n"
    "s_waitcnt lgkmcnt(0)\n"
    "s_waitcnt vmcnt(12)\n"
    "s_barrier\n"
    "s_mul_i32 s38, s61, 4480\n"
    "s_add_u32 m0, s38, 35840\n"
    "s_mov_b32 s39, 2097152\n"
    "buffer_load_dwordx4 v112, s[80:83], s39 offen lds\n"
    "s_add_u32 m0, s38, 36928\n"
    "s_mov_b32 s39, 2129920\n"
    "buffer_load_dwordx4 v112, s[80:83], s39 offen lds\n"
    "s_add_u32 m0, s38, 38016\n"
    "s_mov_b32 s39, 2162688\n"
    "buffer_load_dwordx4 v112, s[80:83], s39 offen lds\n"
    "s_add_u32 m0, s38, 39104\n"
    "s_mov_b32 s39, 2195456\n"
    "buffer_load_dwordx4 v112, s[80:83], s39 offen lds\n"
    "v_add_u32 v116, 71680, v114\n"
    "ds_read_b64_tr_b16 v[16:17], v116 offset:0\n"
    "ds_read_b64_tr_b16 v[18:19], v116 offset:2176\n"
    "ds_read_b64_tr_b16 v[20:21], v116 offset:8960\n"
    "ds_read_b64_tr_b16 v[22:23], v116 offset:11136\n"
    "v_xor_b32 v116, 32, v116\n"
    "ds_read_b64_tr_b16 v[24:25], v116 offset:0\n"
    "ds_read_b64_tr_b16 v[26:27], v116 offset:2176\n"
    "ds_read_b64_tr_b16 v[28:29], v116 offset:8960\n"
    "ds_read_b64_tr_b16 v[30:31], v116 offset:11136\n"
    "s_waitcnt lgkmcnt(0)\n"
    "s_waitcnt vmcnt(12)\n"
    "s_barrier\n"
    "s_mul_i32 s38, s61, 4480\n"
    "s_add_u32 m0, s38, 53760\n"
    "s_mov_b32 s39, 2621440\n"
    "buffer_load_dwordx4 v112, s[80:83], s39 offen lds\n"
    "s_add_u32 m0, s38, 54848\n"
    "s_mov_b32 s39, 2654208\n"
    "buffer_load_dwordx4 v112, s[80:83], s39 offen lds\n"
    "s_add_u32 m0, s38, 55936\n"
    "s_mov_b32 s39, 2686976\n"
    "buffer_load_dwordx4 v112, s[80:83], s39 offen lds\n"
    "s_add_u32 m0, s38, 57024\n"
    "s_mov_b32 s39, 2719744\n"
    "buffer_load_dwordx4 v112, s[80:83], s39 offen lds\n"
    "v_add_u32 v116, 89600, v114\n"
    "ds_read_b64_tr_b16 v[32:33], v116 offset:0\n"
    "ds_read_b64_tr_b16 v[34:35], v116 offset:2176\n"
    "ds_read_b64_tr_b16 v[36:37], v116 offset:8960\n"
    "ds_read_b64_tr_b16 v[38:39], v116 offset:11136\n"
    "v_xor_b32 v116, 32, v116\n"
    "ds_read_b64_tr_b16 v[40:41], v116 offset:0\n"
    "ds_read_b64_tr_b16 v[42:43], v116 offset:2176\n"
    "ds_read_b64_tr_b16 v[44:45], v116 offset:8960\n"
    "ds_read_b64_tr_b16 v[46:47], v116 offset:11136\n"
    "s_waitcnt lgkmcnt(0)\n"
    "s_waitcnt vmcnt(12)\n"
    "s_barrier\n"
    "s_mul_i32 s38, s61, 4480\n"
    "s_add_u32 m0, s38, 71680\n"
    "s_mov_b32 s39, 3145728\n"
    "buffer_load_dwordx4 v112, s[80:83], s39 offen lds\n"
    "s_add_u32 m0, s38, 72768\n"
    "s_mov_b32 s39, 3178496\n"
    "buffer_load_dwordx4 v112, s[80:83], s39 offen lds\n"
    "s_add_u32 m0, s38, 73856\n"
    "s_mov_b32 s39, 3211264\n"
    "buffer_load_dwordx4 v112, s[80:83], s39 offen lds\n"
    "s_add_u32 m0, s38, 74944\n"
    "s_mov_b32 s39, 3244032\n"
    "buffer_load_dwordx4 v112, s[80:83], s39 offen lds\n"
    "v_add_u32 v116, 0, v114\n"
    "ds_read_b64_tr_b16 v[48:49], v116 offset:0\n"
    "ds_read_b64_tr_b16 v[50:51], v116 offset:2176\n"
    "ds_read_b64_tr_b16 v[52:53], v116 offset:8960\n"
    "ds_read_b64_tr_b16 v[54:55], v116 offset:11136\n"
    "v_xor_b32 v116, 32, v116\n"
    "ds_read_b64_tr_b16 v[56:57], v116 offset:0\n"
    "ds_read_b64_tr_b16 v[58:59], v116 offset:2176\n"
    "ds_read_b64_tr_b16 v[60:61], v116 offset:8960\n"
    "ds_read_b64_tr_b16 v[62:63], v116 offset:11136\n"
    "s_waitcnt lgkmcnt(0)\n"
    "s_waitcnt vmcnt(12)\n"
    "s_barrier\n"
    "s_mul_i32 s38, s61, 4480\n"
    "s_add_u32 m0, s38, 89600\n"
    "s_mov_b32 s39, 3670016\n"
    "buffer_load_dwordx4 v112, s[80:83], s39 offen lds\n"
    "s_add_u32 m0, s38, 90688\n"
    "s_mov_b32 s39, 3702784\n"
    "buffer_load_dwordx4 v112, s[80:83], s39 offen lds\n"
    "s_add_u32 m0, s38, 91776\n"
    "s_mov_b32 s39, 3735552\n"
    "buffer_load_dwordx4 v112, s[80:83], s39 offen lds\n"
    "s_add_u32 m0, s38, 92864\n"
    "s_mov_b32 s39, 3768320\n"
    "buffer_load_dwordx4 v112, s[80:83], s39 offen lds\n"
    "v_add_u32 v116, 17920, v114\n"
    "ds_read_b64_tr_b16 a[0:1], v116 offset:0\n"
    "ds_read_b64_tr_b16 a[2:3], v116 offset:2176\n"
    "ds_read_b64_tr_b16 a[4:5], v116 offset:8960\n"
    "ds_read_b64_tr_b16 a[6:7], v116 offset:11136\n"
    "v_xor_b32 v116, 32, v116\n"
    "ds_read_b64_tr_b16 a[8:9], v116 offset:0\n"
    "ds_read_b64_tr_b16 a[10:11], v116 offset:2176\n"
    "ds_read_b64_tr_b16 a[12:13], v116 offset:8960\n"
    "ds_read_b64_tr_b16 a[14:15], v116 offset:11136\n"
    "s_waitcnt lgkmcnt(0)\n"
    "s_waitcnt vmcnt(12)\n"
    "s_barrier\n"
    "s_mov_b32 s80, s88\n"
    "s_mov_b32 s81, s89\n"
    "s_mul_i32 s38, s61, 4480\n"
    "s_add_u32 m0, s38, 0\n"
    "s_mov_b32 s39, 0\n"
    "buffer_load_dwordx4 v112, s[80:83], s39 offen lds\n"
    "s_add_u32 m0, s38, 1088\n"
    "s_mov_b32 s39, 32768\n"
    "buffer_load_dwordx4 v112, s[80:83], s39 offen lds\n"
    "s_add_u32 m0, s38, 2176\n"
    "s_mov_b32 s39, 65536\n"
    "buffer_load_dwordx4 v112, s[80:83], s39 offen lds\n"
    "s_add_u32 m0, s38, 3264\n"
    "s_mov_b32 s39, 98304\n"
    "buffer_load_dwordx4 v112, s[80:83], s39 offen lds\n"
    "v_add_u32 v116, 35840, v114\n"
    "ds_read_b64_tr_b16 a[16:17], v116 offset:0\n"
    "ds_read_b64_tr_b16 a[18:19], v116 offset:2176\n"
    "ds_read_b64_tr_b16 a[20:21], v116 offset:8960\n"
    "ds_read_b64_tr_b16 a[22:23], v116 offset:11136\n"
    "v_xor_b32 v116, 32, v116\n"
    "ds_read_b64_tr_b16 a[24:25], v116 offset:0\n"
    "ds_read_b64_tr_b16 a[26:27], v116 offset:2176\n"
    "ds_read_b64_tr_b16 a[28:29], v116 offset:8960\n"
    "ds_read_b64_tr_b16 a[30:31], v116 offset:11136\n"
    "s_waitcnt lgkmcnt(0)\n"
    "s_waitcnt vmcnt(12)\n"
    "s_barrier\n"
    "s_mul_i32 s38, s61, 4480\n"
    "s_add_u32 m0, s38, 17920\n"
    "s_mov_b32 s39, 524288\n"
    "buffer_load_dwordx4 v112, s[80:83], s39 offen lds\n"
    "s_add_u32 m0, s38, 19008\n"
    "s_mov_b32 s39, 557056\n"
    "buffer_load_dwordx4 v112, s[80:83], s39 offen lds\n"
    "s_add_u32 m0, s38, 20096\n"
    "s_mov_b32 s39, 589824\n"
    "buffer_load_dwordx4 v112, s[80:83], s39 offen lds\n"
    "s_add_u32 m0, s38, 21184\n"
    "s_mov_b32 s39, 622592\n"
    "buffer_load_dwordx4 v112, s[80:83], s39 offen lds\n"
    "v_add_u32 v116, 53760, v114\n"
    "ds_read_b64_tr_b16 a[32:33], v116 offset:0\n"
    "ds_read_b64_tr_b16 a[34:35], v116 offset:2176\n"
    "ds_read_b64_tr_b16 a[36:37], v116 offset:8960\n"
    "ds_read_b64_tr_b16 a[38:39], v116 offset:11136\n"
    "v_xor_b32 v116, 32, v116\n"
    "ds_read_b64_tr_b16 a[40:41], v116 offset:0\n"
    "ds_read_b64_tr_b16 a[42:43], v116 offset:2176\n"
    "ds_read_b64_tr_b16 a[44:45], v116 offset:8960\n"
    "ds_read_b64_tr_b16 a[46:47], v116 offset:11136\n"
    "s_waitcnt lgkmcnt(0)\n"
    "s_waitcnt vmcnt(12)\n"
    "s_barrier\n"
    "global_load_dword v117, v118, s[92:93] sc1\n"
    "s_mul_i32 s38, s61, 4480\n"
    "s_add_u32 m0, s38, 35840\n"
    "s_mov_b32 s39, 1048576\n"
    "buffer_load_dwordx4 v112, s[80:83], s39 offen lds\n"
    "s_add_u32 m0, s38, 36928\n"
    "s_mov_b32 s39, 1081344\n"
    "buffer_load_dwordx4 v112, s[80:83], s39 offen lds\n"
    "s_add_u32 m0, s38, 38016\n"
    "s_mov_b32 s39, 1114112\n"
    "buffer_load_dwordx4 v112, s[80:83], s39 offen lds\n"
    "s_add_u32 m0, s38, 39104\n"
    "s_mov_b32 s39, 1146880\n"
    "buffer_load_dwordx4 v112, s[80:83], s39 offen lds\n"
    "v_add_u32 v116, 71680, v114\n"
    "ds_read_b64_tr_b16 a[48:49], v116 offset:0\n"
    "ds_read_b64_tr_b16 a[50:51], v116 offset:2176\n"
    "ds_read_b64_tr_b16 a[52:53], v116 offset:8960\n"
    "ds_read_b64_tr_b16 a[54:55], v116 offset:11136\n"
    "v_xor_b32 v116, 32, v116\n"
    "ds_read_b64_tr_b16 a[56:57], v116 offset:0\n"
    "ds_read_b64_tr_b16 a[58:59], v116 offset:2176\n"
    "ds_read_b64_tr_b16 a[60:61], v116 offset:8960\n"
    "ds_read_b64_tr_b16 a[62:63], v116 offset:11136\n"
    "s_waitcnt lgkmcnt(0)\n"
    "s_waitcnt vmcnt(13)\n"
    "s_barrier\n"
    "s_mul_i32 s38, s61, 4480\n"
    "s_add_u32 m0, s38, 53760\n"
    "s_mov_b32 s39, 1572864\n"
    "buffer_load_dwordx4 v112, s[80:83], s39 offen lds\n"
    "s_add_u32 m0, s38, 54848\n"
    "s_mov_b32 s39, 1605632\n"
    "buffer_load_dwordx4 v112, s[80:83], s39 offen lds\n"
    "s_add_u32 m0, s38, 55936\n"
    "s_mov_b32 s39, 1638400\n"
    "buffer_load_dwordx4 v112, s[80:83], s39 offen lds\n"
    "s_add_u32 m0, s38, 57024\n"
    "s_mov_b32 s39, 1671168\n"
    "buffer_load_dwordx4 v112, s[80:83], s39 offen lds\n"
    "v_add_u32 v116, 89600, v114\n"
    "ds_read_b64_tr_b16 a[64:65], v116 offset:0\n"
    "ds_read_b64_tr_b16 a[66:67], v116 offset:2176\n"
    "ds_read_b64_tr_b16 a[68:69], v116 offset:8960\n"
    "ds_read_b64_tr_b16 a[70:71], v116 offset:11136\n"
    "v_xor_b32 v116, 32, v116\n"
    "ds_read_b64_tr_b16 a[72:73], v116 offset:0\n"
    "ds_read_b64_tr_b16 a[74:75], v116 offset:2176\n"
    "ds_read_b64_tr_b16 a[76:77], v116 offset:8960\n"
    "ds_read_b64_tr_b16 a[78:79], v116 offset:11136\n"
    "s_waitcnt lgkmcnt(0)\n"
    "s_waitcnt vmcnt(13)\n"
    "s_barrier\n"
    "s_mul_i32 s38, s61, 4480\n"
    "s_add_u32 m0, s38, 71680\n"
    "s_mov_b32 s39, 2097152\n"
    "buffer_load_dwordx4 v112, s[80:83], s39 offen lds\n"
    "s_add_u32 m0, s38, 72768\n"
    "s_mov_b32 s39, 2129920\n"
    "buffer_load_dwordx4 v112, s[80:83], s39 offen lds\n"
    "s_add_u32 m0, s38, 73856\n"
    "s_mov_b32 s39, 2162688\n"
    "buffer_load_dwordx4 v112, s[80:83], s39 offen lds\n"
    "s_add_u32 m0, s38, 74944\n"
    "s_mov_b32 s39, 2195456\n"
    "buffer_load_dwordx4 v112, s[80:83], s39 offen lds\n"
    "v_add_u32 v116, 0, v114\n"
    "ds_read_b64_tr_b16 a[176:177], v116 offset:0\n"
    "ds_read_b64_tr_b16 a[178:179], v116 offset:2176\n"
    "ds_read_b64_tr_b16 a[180:181], v116 offset:8960\n"
    "ds_read_b64_tr_b16 a[182:183], v116 offset:11136\n"
    "v_xor_b32 v116, 32, v116\n"
    "ds_read_b64_tr_b16 a[184:185], v116 offset:0\n"
    "ds_read_b64_tr_b16 a[186:187], v116 offset:2176\n"
    "ds_read_b64_tr_b16 a[188:189], v116 offset:8960\n"
    "ds_read_b64_tr_b16 a[190:191], v116 offset:11136\n"
    "s_waitcnt lgkmcnt(0)\n"
    "s_waitcnt vmcnt(13)\n"
    "s_barrier\n"
    "s_mul_i32 s38, s61, 4480\n"
    "s_add_u32 m0, s38, 89600\n"
    "s_mov_b32 s39, 2621440\n"
    "buffer_load_dwordx4 v112, s[80:83], s39 offen lds\n"
    "s_add_u32 m0, s38, 90688\n"
    "s_mov_b32 s39, 2654208\n"
    "buffer_load_dwordx4 v112, s[80:83], s39 offen lds\n"
    "s_add_u32 m0, s38, 91776\n"
    "s_mov_b32 s39, 2686976\n"
    "buffer_load_dwordx4 v112, s[80:83], s39 offen lds\n"
    "s_add_u32 m0, s38, 92864\n"
    "s_mov_b32 s39, 2719744\n"
    "buffer_load_dwordx4 v112, s[80:83], s39 offen lds\n"
    "v_add_u32 v116, 17920, v114\n"
    "ds_read_b64_tr_b16 a[192:193], v116 offset:0\n"
    "ds_read_b64_tr_b16 a[194:195], v116 offset:2176\n"
    "ds_read_b64_tr_b16 a[196:197], v116 offset:8960\n"
    "ds_read_b64_tr_b16 a[198:199], v116 offset:11136\n"
    "v_xor_b32 v116, 32, v116\n"
    "ds_read_b64_tr_b16 a[200:201], v116 offset:0\n"
    "ds_read_b64_tr_b16 a[202:203], v116 offset:2176\n"
    "ds_read_b64_tr_b16 a[204:205], v116 offset:8960\n"
    "ds_read_b64_tr_b16 a[206:207], v116 offset:11136\n"
    "s_waitcnt lgkmcnt(0)\n"
    "s_waitcnt vmcnt(12)\n"
    "s_barrier\n"
    "s_mul_i32 s38, s61, 4480\n"
    "s_add_u32 m0, s38, 0\n"
    "s_mov_b32 s39, 3145728\n"
    "buffer_load_dwordx4 v112, s[80:83], s39 offen lds\n"
    "s_add_u32 m0, s38, 1088\n"
    "s_mov_b32 s39, 3178496\n"
    "buffer_load_dwordx4 v112, s[80:83], s39 offen lds\n"
    "s_add_u32 m0, s38, 2176\n"
    "s_mov_b32 s39, 3211264\n"
    "buffer_load_dwordx4 v112, s[80:83], s39 offen lds\n"
    "s_add_u32 m0, s38, 3264\n"
    "s_mov_b32 s39, 3244032\n"
    "buffer_load_dwordx4 v112, s[80:83], s39 offen lds\n"
    "v_add_u32 v116, 35840, v114\n"
    "ds_read_b64_tr_b16 a[208:209], v116 offset:0\n"
    "ds_read_b64_tr_b16 a[210:211], v116 offset:2176\n"
    "ds_read_b64_tr_b16 a[212:213], v116 offset:8960\n"
    "ds_read_b64_tr_b16 a[214:215], v116 offset:11136\n"
    "v_xor_b32 v116, 32, v116\n"
    "ds_read_b64_tr_b16 a[216:217], v116 offset:0\n"
    "ds_read_b64_tr_b16 a[218:219], v116 offset:2176\n"
    "ds_read_b64_tr_b16 a[220:221], v116 offset:8960\n"
    "ds_read_b64_tr_b16 a[222:223], v116 offset:11136\n"
    "s_waitcnt lgkmcnt(0)\n"
    "s_waitcnt vmcnt(12)\n"
    "s_barrier\n"
    "s_mul_i32 s38, s61, 4480\n"
    "s_add_u32 m0, s38, 17920\n"
    "s_mov_b32 s39, 3670016\n"
    "buffer_load_dwordx4 v112, s[80:83], s39 offen lds\n"
    "s_add_u32 m0, s38, 19008\n"
    "s_mov_b32 s39, 3702784\n"
    "buffer_load_dwordx4 v112, s[80:83], s39 offen lds\n"
    "s_add_u32 m0, s38, 20096\n"
    "s_mov_b32 s39, 3735552\n"
    "buffer_load_dwordx4 v112, s[80:83], s39 offen lds\n"
    "s_add_u32 m0, s38, 21184\n"
    "s_mov_b32 s39, 3768320\n"
    "buffer_load_dwordx4 v112, s[80:83], s39 offen lds\n"
    "v_add_u32 v116, 53760, v114\n"
    "ds_read_b64_tr_b16 a[224:225], v116 offset:0\n"
    "ds_read_b64_tr_b16 a[226:227], v116 offset:2176\n"
    "ds_read_b64_tr_b16 a[228:229], v116 offset:8960\n"
    "ds_read_b64_tr_b16 a[230:231], v116 offset:11136\n"
    "v_xor_b32 v116, 32, v116\n"
    "ds_read_b64_tr_b16 a[232:233], v116 offset:0\n"
    "ds_read_b64_tr_b16 a[234:235], v116 offset:2176\n"
    "ds_read_b64_tr_b16 a[236:237], v116 offset:8960\n"
    "ds_read_b64_tr_b16 a[238:239], v116 offset:11136\n"
    "s_waitcnt lgkmcnt(0)\n"
    "s_waitcnt vmcnt(12)\n"
    "s_barrier\n"
    "v_add_u32 v116, 71680, v114\n"
    "ds_read_b64_tr_b16 a[240:241], v116 offset:0\n"
    "ds_read_b64_tr_b16 a[242:243], v116 offset:2176\n"
    "ds_read_b64_tr_b16 a[244:245], v116 offset:8960\n"
    "ds_read_b64_tr_b16 a[246:247], v116 offset:11136\n"
    "v_xor_b32 v116, 32, v116\n"
    "ds_read_b64_tr_b16 a[248:249], v116 offset:0\n"
    "ds_read_b64_tr_b16 a[250:251], v116 offset:2176\n"
    "ds_read_b64_tr_b16 a[252:253], v116 offset:8960\n"
    "ds_read_b64_tr_b16 a[254:255], v116 offset:11136\n"
    "s_waitcnt lgkmcnt(0)\n"
    "s_waitcnt vmcnt(8)\n"
    "s_barrier\n"
    "v_add_u32 v116, 89600, v114\n"
    "ds_read_b64_tr_b16 v[120:121], v116 offset:0\n"
    "ds_read_b64_tr_b16 v[122:123], v116 offset:2176\n"
    "ds_read_b64_tr_b16 v[124:125], v116 offset:8960\n"
    "ds_read_b64_tr_b16 v[126:127], v116 offset:11136\n"
    "v_xor_b32 v116, 32, v116\n"
    "ds_read_b64_tr_b16 v[128:129], v116 offset:0\n"
    "ds_read_b64_tr_b16 v[130:131], v116 offset:2176\n"
    "ds_read_b64_tr_b16 v[132:133], v116 offset:8960\n"
    "ds_read_b64_tr_b16 v[134:135], v116 offset:11136\n"
    "s_waitcnt lgkmcnt(0)\n"
    "s_mov_b32 s37, 0x80000\n"
    "LRY%=:\n"
    "v_readfirstlane_b32 s44, v117\n"
    "s_cmp_lg_u32 s44, 0\n"
    "s_cbranch_scc1 LRZ%=\n"
    "global_load_dword v117, v118, s[92:93] sc1\n"
    "s_waitcnt vmcnt(0)\n"
    "s_sleep 1\n"
    "s_sub_u32 s37, s37, 1\n"
    "s_cbranch_scc0 LRY%=\n"
    "s_branch LFAIL%=\n"
    "LRZ%=:\n"
    "s_lshl_b32 s38, s44, 13\n"
    "s_and_b32 s38, s38, 0x2000\n"
    "s_xor_b32 s46, s46, s38\n"
    "s_xor_b32 s47, s47, s38\n"
    "s_xor_b32 s48, s48, s38\n"
    "s_xor_b32 s56, s56, s38\n"
    "s_xor_b32 s74, s74, s38\n"
    "s_xor_b32 s58, s58, s38\n"
    "s_xor_b32 s59, s59, s38\n"
    "s_xor_b32 s67, s67, s38\n"
    "s_xor_b32 s69, s69, s38\n"
    "s_sub_u32 s58, s58, 4\n"
    "s_mov_b64 exec, 7\n"
    "buffer_load_dword v119, v0, s[16:19], s59 offen sc1\n"
    "s_mov_b64 exec, -1\n"
    "s_waitcnt vmcnt(5)\n"
    "s_barrier\n"
    "v_add_u32 v116, 0, v114\n"
    "ds_read_b64_tr_b16 v[136:137], v116 offset:0\n"
    "ds_read_b64_tr_b16 v[138:139], v116 offset:2176\n"
    "ds_read_b64_tr_b16 v[140:141], v116 offset:8960\n"
    "ds_read_b64_tr_b16 v[142:143], v116 offset:11136\n"
    "v_xor_b32 v116, 32, v116\n"
    "ds_read_b64_tr_b16 v[144:145], v116 offset:0\n"
    "ds_read_b64_tr_b16 v[146:147], v116 offset:2176\n"
    "ds_read_b64_tr_b16 v[148:149], v116 offset:8960\n"
    "ds_read_b64_tr_b16 v[150:151], v116 offset:11136\n"
    "s_waitcnt lgkmcnt(0)\n"
    "s_waitcnt vmcnt(1)\n"
    "s_barrier\n"
    "v_add_u32 v116, 17920, v114\n"
    "ds_read_b64_tr_b16 v[152:153], v116 offset:0\n"
    "ds_read_b64_tr_b16 v[154:155], v116 offset:2176\n"
    "ds_read_b64_tr_b16 v[156:157], v116 offset:8960\n"
    "ds_read_b64_tr_b16 v[158:159], v116 offset:11136\n"
    "v_xor_b32 v116, 32, v116\n"
    "ds_read_b64_tr_b16 v[160:161], v116 offset:0\n"
    "ds_read_b64_tr_b16 v[162:163], v116 offset:2176\n"
    "ds_read_b64_tr_b16 v[164:165], v116 offset:8960\n"
    "ds_read_b64_tr_b16 v[166:167], v116 offset:11136\n"
    "s_waitcnt lgkmcnt(0)\n"
    "s_barrier\n"
    "ds_write_b128 v7, v[16:19] offset:0\n"
    "ds_write_b128 v7, v[20:23] offset:1024\n"
    "ds_write_b128 v7, v[24:27] offset:2048\n"
    "ds_write_b128 v7, v[28:31] offset:3072\n"
    "ds_write_b128 v7, v[32:35] offset:4096\n"
    "ds_write_b128 v7, v[36:39] offset:5120\n"
    "ds_write_b128 v7, v[40:43] offset:6144\n"
    "ds_write_b128 v7, v[44:47] offset:7168\n"
    "ds_write_b128 v7, v[48:51] offset:8192\n"
    "ds_write_b128 v7, v[52:55] offset:9216\n"
    "ds_write_b128 v7, v[56:59] offset:10240\n"
    "ds_write_b128 v7, v[60:63] offset:11264\n"
    "ds_write_b128 v7, v[64:67] offset:12288\n"
    "ds_write_b128 v7, v[68:71] offset:13312\n"
    "ds_write_b128 v7, v[72:75] offset:14336\n"
    "s_waitcnt lgkmcnt(11)\n"
    "ds_write_b128 v7, v[76:79] offset:15360\n"
    "ds_write_b128 v7, v[80:83] offset:16384\n"
    "ds_write_b128 v7, v[84:87] offset:17408\n"
    "ds_write_b128 v7, v[88:91] offset:18432\n"
    "s_waitcnt lgkmcnt(11)\n"
    "ds_write_b128 v7, v[92:95] offset:19456\n"
    "ds_write_b128 v7, v[96:99] offset:20480\n"
    "ds_write_b128 v7, v[100:103] offset:21504\n"
    "ds_write_b128 v7, v[104:107] offset:22528\n"
    "s_waitcnt lgkmcnt(11)\n"
    "ds_write_b128 v7, v[108:111] offset:23552\n"
    "s_waitcnt lgkmcnt(0)\n"
    "ds_read_b128 v[16:19], v9 offset:0\n"
    "ds_read_b128 v[20:23], v9 offset:1024\n"
    "ds_read_b128 v[24:27], v9 offset:8192\n"
    "ds_read_b128 v[28:31], v9 offset:9216\n"
    "ds_read_b128 v[32:35], v9 offset:16384\n"
    "ds_read_b128 v[36:39], v9 offset:17408\n"
    "s_waitcnt lgkmcnt(5)\n"
    "ds_write_b128 v7, v[16:19] offset:24576\n"
    "s_waitcnt lgkmcnt(5)\n"
    "ds_write_b128 v7, v[20:23] offset:25600\n"
    "s_waitcnt lgkmcnt(5)\n"
    "ds_write_b128 v7, v[24:27] offset:26624\n"
    "s_waitcnt lgkmcnt(5)\n"
    "ds_write_b128 v7, v[28:31] offset:27648\n"
    "s_waitcnt lgkmcnt(5)\n"
    "ds_write_b128 v7, v[32:35] offset:28672\n"
    "s_waitcnt lgkmcnt(5)\n"
    "ds_write_b128 v7, v[36:39] offset:29696\n"
    "v_lshrrev_b32 v64, 4, v0\n"
    "v_lshrrev_b32 v65, 4, v64\n"
    "v_and_b32 v64, 15, v64\n"
    "v_lshlrev_b32 v65, 2, v65\n"
    "v_sub_u32 v64, v64, v65\n"
    "s_and_b32 s38, s61, 1\n"
    "s_lshl_b32 s38, s38, 4\n"
    "v_add_u32 v64, s38, v64\n"
    "v_cmp_le_i32 s[80:81], 0, v64\n"
    "v_cmp_le_i32 s[82:83], 1, v64\n"
    "v_cmp_le_i32 s[84:85], 2, v64\n"
    "v_cmp_le_i32 s[86:87], 3, v64\n"
    "v_cmp_le_i32 s[88:89], 16, v64\n"
    "v_cmp_le_i32 s[90:91], 17, v64\n"
    "v_cmp_le_i32 s[92:93], 18, v64\n"
    "v_cmp_le_i32 s[94:95], 19, v64\n"
    "v_mov_b32 v64, 158224\n"
    "v_mov_b64 v[66:67], 0\n"
    "ds_write_b64 v64, v[66:67] offset:16\n"
    "ds_write_b64 v64, v[66:67] offset:32\n"
    "ds_write_b64 v64, v[66:67] offset:40\n"
    "s_mov_b32 s54, 0x80000000\n"
    "s_xor_b32 s46, s46, 0x80000\n"
    "s_waitcnt lgkmcnt(0)\n"
    "s_memtime s[40:41]\n"
    "s_waitcnt lgkmcnt(0)\n"
    "s_sub_u32 s41, s40, s45\n"
    "s_sub_u32 s42, s13, 1\n"
    "s_and_b32 s41, s41, s42\n"
    "s_sub_u32 s44, s40, s41\n"
    "s_waitcnt vmcnt(0)\n"
    "v_mov_b32 v116, v119\n"
    "v_cmp_ne_u32 vcc, 0, v116\n"
    "v_readfirstlane_b32 s70, v116\n"
    "s_nop 0\n"
    "s_andn2_b32 s38, 7, vcc_lo\n"
    "s_cbranch_scc0 LS0K%=\n"
    "s_mov_b32 s37, 0x80000\n"
    "LS0W%=:\n"
    "s_mov_b64 exec, 7\n"
    "buffer_load_dword v116, v0, s[16:19], s59 offen sc1\n"
    "s_mov_b64 exec, -1\n"
    "s_waitcnt vmcnt(0)\n"
    "v_cmp_ne_u32 vcc, 0, v116\n"
    "v_readfirstlane_b32 s70, v116\n"
    "s_nop 0\n"
    "s_andn2_b32 s38, 7, vcc_lo\n"
    "s_cbranch_scc0 LS0K%=\n"
    "s_sleep 2\n"
    "s_sub_u32 s37, s37, 1\n"
    "s_cbranch_scc0 LS0W%=\n"
    "s_branch LFAIL%=\n"
    "LS0K%=:\n"
    "s_lshr_b32 s38, s70, 16\n"
    "s_cmp_gt_i32 s38, s1\n"
    "s_cbranch_scc1 LREC%=\n"
    "s_add_u32 s53, s50, s55\n"
    "s_mov_b32 s49, s48\n"
    "s_add_u32 s51, s53, 0\n"
    "s_lshr_b32 s38, s51, 8\n"
    "s_xor_b32 s38, s38, s56\n"
    "s_and_b32 s38, s38, 0x2000\n"
    "s_or_b32 s51, s51, s38\n"
    "s_add_u32 s52, s53, 524288\n"
    "s_lshr_b32 s38, s52, 8\n"
    "s_xor_b32 s38, s38, s56\n"
    "s_and_b32 s38, s38, 0x2000\n"
    "s_or_b32 s52, s52, s38\n"
    "s_add_u32 s53, s53, 1048576\n"
    "s_lshr_b32 s38, s53, 8\n"
    "s_xor_b32 s38, s38, s56\n"
    "s_and_b32 s38, s38, 0x2000\n"
    "s_or_b32 s53, s53, s38\n"
    "s_add_u32 s59, s59, 128\n"
    "s_barrier\n"
    "s_cmp_lg_u32 s61, 1\n"
    "s_cbranch_scc1 LPRE%=\n"
    "s_sub_u32 s51, s51, 2048\n"
    "s_mov_b32 m0, 122880\n"
    "s_nop 0\n"
    "buffer_load_dwordx4 v0, s[16:19], s51 offen offset:0 sc1 lds\n"
    "buffer_load_dwordx4 v0, s[16:19], s51 offen offset:1024 sc1 lds\n"
    "buffer_load_dwordx4 v0, s[16:19], s51 offen offset:2048 sc1 lds\n"
    "buffer_load_dwordx4 v0, s[16:19], s51 offen offset:3072 sc1 lds\n"
    "s_mov_b32 m0, 126976\n"
    "s_add_u32 s51, s51, 4096\n"
    "buffer_load_dwordx4 v0, s[16:19], s51 offen offset:0 sc1 lds\n"
    "buffer_load_dwordx4 v0, s[16:19], s51 offen offset:1024 sc1 lds\n"
    "buffer_load_dwordx4 v0, s[16:19], s51 offen offset:2048 sc1 lds\n"
    "buffer_load_dwordx4 v0, s[16:19], s51 offen offset:3072 sc1 lds\n"
    "s_memtime s[40:41]\n"
    "s_waitcnt lgkmcnt(0)\n"
    "s_sub_u32 s41, s40, s45\n"
    "s_sub_u32 s42, s13, 1\n"
    "s_and_b32 s41, s41, s42\n"
    "s_sub_u32 s44, s40, s41\n"
    "v_mov_b32 v112, 158220\n"
    "v_mov_b32 v113, s36\n"
    "v_mov_b32 v114, s44\n"
    "ds_write2_b32 v112, v113, v114 offset0:0 offset1:1\n"
    "s_waitcnt lgkmcnt(0)\n"
    "s_branch LPRE%=\n"
    "LBM%=:\n"
    "s_waitcnt lgkmcnt(0)\n"
    "s_barrier\n"
    "buffer_load_dwordx4 v0, s[16:19], s73 offen sc1 lds\n"
    "ds_read_b64 v[42:43], v41 offset:0\n"
    "ds_read_b128 v[32:35], v9 offset:16384\n"
    "ds_read_b128 v[36:39], v9 offset:17408\n"
    "s_waitcnt lgkmcnt(2)\n"
    "v_readfirstlane_b32 s39, v42\n"
    "v_readfirstlane_b32 s44, v43\n"
    "s_sub_u32 s38, s39, s44\n"
    "s_cmp_gt_i32 s38, 0\n"
    "s_cbranch_scc1 LSL%=\n"
    "s_add_u32 m0, s32, 131072\n"
    "s_nop 0\n"
    "buffer_load_dwordx4 v0, s[16:19], s52 offen offset:0 sc1 lds\n"
    "buffer_load_dwordx4 v0, s[16:19], s52 offen offset:1024 sc1 lds\n"
    "s_waitcnt lgkmcnt(0)\n"
    "s_cmp_lg_u32 s61, 1\n"
    "s_cbranch_scc1 LC1%=\n"
    "s_waitcnt vmcnt(3)\n"
    "LC1%=:\n"
    "s_barrier\n"
    "ds_read_b128 v[16:19], v2 offset:0\n"
    "ds_read_b128 v[20:23], v2 offset:1024\n"
    "ds_read_b128 v[24:27], v2 offset:2048\n"
    "s_add_u32 m0, s68, 139264\n"
    "s_nop 0\n"
    "buffer_load_dwordx4 v0, s[16:19], s53 offen offset:0 sc1 lds\n"
    "buffer_load_dwordx4 v0, s[16:19], s53 offen offset:1024 sc1 lds\n"
    "s_waitcnt lgkmcnt(2)\n"
    "v_smfmac_f32_16x16x64_bf16 v[48:51], v[16:19], v[64:71], v1\n"
    "ds_read_b128 v[28:31], v2 offset:3072\n"
    "v_smfmac_f32_16x16x64_bf16 v[52:55], v[16:19], v[72:79], v1\n"
    "s_waitcnt lgkmcnt(2)\n"
    "v_smfmac_f32_16x16x64_bf16 v[48:51], v[20:23], v[80:87], v1\n"
    "v_smfmac_f32_16x16x64_bf16 v[52:55], v[20:23], v[88:95], v1\n"
    "s_waitcnt lgkmcnt(1)\n"
    "v_smfmac_f32_16x16x64_bf16 v[48:51], v[24:27], v[96:103], v1\n"
    "ds_read_b128 v[64:67], v7 offset:12288\n"
    "v_smfmac_f32_16x16x64_bf16 v[52:55], v[24:27], v[104:111], v1\n"
    "ds_read_b128 v[68:71], v7 offset:13312\n"
    "s_waitcnt lgkmcnt(2)\n"
    "v_smfmac_f32_16x16x64_bf16 v[48:51], v[28:31], a[0:7], v1\n"
    "ds_read_b128 v[72:75], v7 offset:14336\n"
    "v_smfmac_f32_16x16x64_bf16 v[52:55], v[28:31], a[8:15], v1\n"
    "ds_read_b128 v[76:79], v7 offset:15360\n"
    "v_smfmac_f32_16x16x64_bf16 v[112:115], v[16:19], a[176:183], v1\n"
    "ds_read_b128 v[80:83], v7 offset:16384\n"
    "buffer_store_dwordx4 v[32:35], v40, s[16:19], s54 offen offset:0 sc1\n"
    "v_smfmac_f32_16x16x64_bf16 v[116:119], v[16:19], a[184:191], v1\n"
    "ds_read_b128 v[84:87], v7 offset:17408\n"
    "v_smfmac_f32_16x16x64_bf16 v[112:115], v[20:23], a[192:199], v1\n"
    "ds_read_b128 v[88:91], v7 offset:18432\n"
    "v_smfmac_f32_16x16x64_bf16 v[116:119], v[20:23], a[200:207], v1\n"
    "ds_read_b128 v[92:95], v7 offset:19456\n"
    "v_smfmac_f32_16x16x64_bf16 v[56:59], v[24:27], a[80:87], v1\n"
    "v_smfmac_f32_16x16x64_bf16 v[60:63], v[24:27], a[88:95], v1\n"
    "v_max3_u32 v111, v19, v23, v27\n"
    "buffer_store_dwordx4 v[36:39], v40, s[16:19], s54 offen offset:2048 sc1\n"
    "v_smfmac_f32_16x16x64_bf16 v[56:59], v[28:31], a[96:103], v1\n"
    "v_smfmac_f32_16x16x64_bf16 v[60:63], v[28:31], a[104:111], v1\n"
    "s_waitcnt vmcnt(6)\n"
    "s_barrier\n"
    "ds_read_b128 v[32:35], v2 offset:4096\n"
    "s_waitcnt lgkmcnt(7)\n"
    "v_smfmac_f32_16x16x64_bf16 v[56:59], v[16:19], v[64:71], v1\n"
    "ds_read_b128 v[36:39], v2 offset:5120\n"
    "s_waitcnt lgkmcnt(6)\n"
    "v_smfmac_f32_16x16x64_bf16 v[60:63], v[16:19], v[72:79], v1\n"
    "ds_read_b128 v[40:43], v2 offset:6144\n"
    "s_waitcnt lgkmcnt(5)\n"
    "v_smfmac_f32_16x16x64_bf16 v[56:59], v[20:23], v[80:87], v1\n"
    "ds_read_b128 v[44:47], v2 offset:7168\n"
    "s_waitcnt lgkmcnt(4)\n"
    "v_smfmac_f32_16x16x64_bf16 v[60:63], v[20:23], v[88:95], v1\n"
    "s_waitcnt lgkmcnt(3)\n"
    "v_smfmac_f32_16x16x64_bf16 v[48:51], v[32:35], a[16:23], v1\n"
    "v_max3_u32 v111, v111, v31, v35\n"
    "v_smfmac_f32_16x16x64_bf16 v[52:55], v[32:35], a[24:31], v1\n"
    "ds_read_b128 v[88:91], v9 offset:0\n"
    "s_waitcnt lgkmcnt(3)\n"
    "v_smfmac_f32_16x16x64_bf16 v[48:51], v[36:39], a[32:39], v1\n"
    "v_add_u32 v21, s20, v5\n"
    "v_smfmac_f32_16x16x64_bf16 v[52:55], v[36:39], a[40:47], v1\n"
    "ds_read_b128 v[92:95], v9 offset:1024\n"
    "s_waitcnt lgkmcnt(3)\n"
    "v_smfmac_f32_16x16x64_bf16 v[48:51], v[40:43], a[48:55], v1\n"
    "s_waitcnt lgkmcnt(2)\n"
    "v_max3_u32 v111, v111, v39, v43\n"
    "v_smfmac_f32_16x16x64_bf16 v[48:51], v[44:47], a[64:71], v1\n"
    "v_max_u32 v111, v111, v47\n"
    "v_smfmac_f32_16x16x64_bf16 v[52:55], v[40:43], a[56:63], v1\n"
    "v_cmp_eq_u32 vcc, -1, v111\n"
    "v_smfmac_f32_16x16x64_bf16 v[52:55], v[44:47], a[72:79], v1\n"
    "v_add_f32 v48, v48, v50\n"
    "v_smfmac_f32_16x16x64_bf16 v[56:59], v[32:35], a[112:119], v1\n"
    "v_add_f32 v49, v49, v51\n"
    "v_smfmac_f32_16x16x64_bf16 v[60:63], v[32:35], a[120:127], v1\n"
    "v_add_f32 v52, v52, v54\n"
    "v_add_f32 v53, v53, v55\n"
    "v_cvt_pk_bf16_f32 v48, v48, v52\n"
    "v_cvt_pk_bf16_f32 v49, v49, v53\n"
    "v_smfmac_f32_16x16x64_bf16 v[56:59], v[36:39], a[128:135], v1\n"
    "ds_write2_b32 v3, v48, v49 offset0:0 offset1:72\n"
    "v_smfmac_f32_16x16x64_bf16 v[60:63], v[36:39], a[136:143], v1\n"
    "s_cbranch_vccnz LMR%=\n"
    "v_smfmac_f32_16x16x64_bf16 v[56:59], v[40:43], a[144:151], v1\n"
    "v_mov_b32 v53, 0\n"
    "v_smfmac_f32_16x16x64_bf16 v[56:59], v[44:47], a[160:167], v1\n"
    "v_mov_b32 v54, 0\n"
    "v_smfmac_f32_16x16x64_bf16 v[60:63], v[40:43], a[152:159], v1\n"
    "s_waitcnt lgkmcnt(0)\n"
    "s_barrier\n"
    "ds_read_b128 v[80:83], v21 offset:0\n"
    "v_smfmac_f32_16x16x64_bf16 v[60:63], v[44:47], a[168:175], v1\n"
    "ds_read_b128 v[84:87], v21 offset:64\n"
    "v_add_f32 v56, v56, v58\n"
    "v_smfmac_f32_16x16x64_bf16 v[112:115], v[24:27], a[208:215], v1\n"
    "ds_read_b128 v[96:99], v5 offset:0\n"
    "v_add_f32 v57, v57, v59\n"
    "v_smfmac_f32_16x16x64_bf16 v[112:115], v[28:31], a[224:231], v1\n"
    "ds_read_b128 v[100:103], v5 offset:64\n"
    "s_waitcnt lgkmcnt(3)\n"
    "v_add_f32 v60, v60, v62\n"
    "v_mfma_f32_16x16x32_bf16 v[64:67], v[88:91], v[80:83], 0\n"
    "ds_read_b128 v[104:107], v5 offset:128\n"
    "s_waitcnt lgkmcnt(3)\n"
    "v_add_f32 v61, v61, v63\n"
    "v_cvt_pk_bf16_f32 v56, v56, v60\n"
    "v_mfma_f32_16x16x32_bf16 v[64:67], v[92:95], v[84:87], v[64:67]\n"
    "ds_read_b128 v[108:111], v5 offset:192\n"
    "v_cvt_pk_bf16_f32 v57, v57, v61\n"
    "v_mov_b64 v[50:51], 0\n"
    "v_smfmac_f32_16x16x64_bf16 v[112:115], v[32:35], a[240:247], v1\n"
    "ds_write2_b32 v4, v56, v57 offset0:0 offset1:72\n"
    "v_smfmac_f32_16x16x64_bf16 v[112:115], v[36:39], v[120:127], v1\n"
    "ds_write_b128 v8, v[64:67]\n"
    "v_smfmac_f32_16x16x64_bf16 v[112:115], v[40:43], v[136:143], v1\n"
    "v_mov_b64 v[60:61], s[14:15]\n"
    "v_smfmac_f32_16x16x64_bf16 v[112:115], v[44:47], v[152:159], v1\n"
    "v_mov_b64 v[62:63], s[14:15]\n"
    "v_smfmac_f32_16x16x64_bf16 v[116:119], v[24:27], a[216:223], v1\n"
    "v_smfmac_f32_16x16x64_bf16 v[116:119], v[28:31], a[232:239], v1\n"
    "s_waitcnt lgkmcnt(0)\n"
    "s_barrier\n"
    "ds_read_b128 v[64:67], v6 offset:0\n"
    "ds_read_b128 v[88:91], v15 offset:0\n"
    "v_smfmac_f32_16x16x64_bf16 v[116:119], v[32:35], a[248:255], v1\n"
    "ds_read_b128 v[92:95], v15 offset:512\n"
    "ds_read_b128 v[68:71], v6 offset:64\n"
    "v_smfmac_f32_16x16x64_bf16 v[116:119], v[36:39], v[128:135], v1\n"
    "ds_read_b128 v[72:75], v6 offset:128\n"
    "v_add_f32 v112, v112, v114\n"
    "v_smfmac_f32_16x16x64_bf16 v[116:119], v[40:43], v[144:151], v1\n"
    "ds_read_b128 v[76:79], v6 offset:192\n"
    "s_waitcnt lgkmcnt(5)\n"
    "v_add_f32 v113, v113, v115\n"
    "v_mfma_f32_16x16x32_bf16 v[16:19], v[64:67], v[96:99], 0\n"
    "s_waitcnt lgkmcnt(3)\n"
    "s_waitcnt lgkmcnt(2)\n"
    "v_add_f32 v88, v88, v92\n"
    "v_add_f32 v89, v89, v93\n"
    "v_mfma_f32_16x16x32_bf16 v[16:19], v[68:71], v[100:103], v[16:19]\n"
    "s_waitcnt lgkmcnt(1)\n"
    "v_add_f32 v90, v90, v94\n"
    "v_add_f32 v91, v91, v95\n"
    "v_mfma_f32_16x16x32_bf16 v[16:19], v[72:75], v[104:107], v[16:19]\n"
    "s_waitcnt lgkmcnt(0)\n"
    "v_max3_f32 v21, v88, v89, v90\n"
    "v_max_f32 v21, v21, v91\n"
    "v_mfma_f32_16x16x32_bf16 v[16:19], v[76:79], v[108:111], v[16:19]\n"
    "s_waitcnt vmcnt(4)\n"
    "ds_read_b128 v[80:83], v9 offset:8192\n"
    "v_max_f32_dpp v20, v21, v21 row_ror:8 row_mask:0xf bank_mask:0xf\n"
    "v_mov_b32 v21, v20\n"
    "v_smfmac_f32_16x16x64_bf16 v[116:119], v[44:47], v[160:167], v1\n"
    "ds_read_b128 v[84:87], v9 offset:9216\n"
    "v_permlane32_swap_b32 v20, v21\n"
    "v_max_f32 v20, v20, v21\n"
    "v_cvt_pk_bf16_f32 v112, v112, v113\n"
    "v_max3_f32 v21, v16, v17, v20\n"
    "v_max3_f32 v21, v18, v19, v21\n"
    "v_mov_b32 v22, v21\n"
    "s_nop 0\n"
    "v_add_f32 v116, v116, v118\n"
    "v_add_f32 v117, v117, v119\n"
    "v_permlane16_swap_b32 v21, v22\n"
    "v_max_f32 v21, v21, v22\n"
    "v_mul_f32 v21, 0xbe0293ee, v21\n"
    "v_fma_f32 v88, v88, s65, v21\n"
    "v_fma_f32 v89, v89, s65, v21\n"
    "v_fma_f32 v90, v90, s65, v21\n"
    "v_fma_f32 v91, v91, s65, v21\n"
    "v_fma_f32 v16, v16, s65, v21\n"
    "v_fma_f32 v17, v17, s65, v21\n"
    "v_exp_f32 v16, v16\n"
    "v_exp_f32 v17, v17\n"
    "v_exp_f32 v88, v88\n"
    "v_exp_f32 v89, v89\n"
    "v_cvt_pk_bf16_f32 v52, v16, v17\n"
    "v_cvt_pk_bf16_f32 v113, v116, v117\n"
    "v_cvt_pk_bf16_f32 v55, v16, v17\n"
    "v_cvt_pk_bf16_f32 v56, v88, v89\n"
    "v_mfma_f32_16x16x16_bf16 v[104:107], v[60:61], v[52:53], 0\n"
    "v_exp_f32 v90, v90\n"
    "v_mfma_f32_16x16x16_bf16 v[96:99], v[112:113], v[52:53], 0\n"
    "v_exp_f32 v91, v91\n"
    "v_mfma_f32_16x16x16_bf16 v[100:103], v[112:113], v[54:55], 0\n"
    "v_cvt_pk_bf16_f32 v57, v90, v91\n"
    "v_mov_b32_dpp v58, v56 row_shl:8 row_mask:0xf bank_mask:0xf bound_ctrl:1\n"
    "s_nop 0\n"
    "v_mov_b32_dpp v59, v57 row_shl:8 row_mask:0xf bank_mask:0xf bound_ctrl:1\n"
    "s_nop 1\n"
    "v_mfma_f32_16x16x32_bf16 v[104:107], v[60:63], v[56:59], v[104:107]\n"
    "ds_write_b64 v10, v[112:113]\n"
    "s_waitcnt lgkmcnt(2)\n"
    "v_mfma_f32_16x16x32_bf16 v[96:99], v[80:83], v[56:59], v[96:99]\n"
    "s_waitcnt lgkmcnt(1)\n"
    "v_mfma_f32_16x16x32_bf16 v[100:103], v[84:87], v[56:59], v[100:103]\n"
    "v_rcp_f32 v104, v104\n"
    "s_mov_b64 exec, s[78:79]\n"
    "s_nop 1\n"
    "v_pk_mul_f32 v[96:97], v[96:97], v[104:105] op_sel_hi:[1,0]\n"
    "v_pk_mul_f32 v[98:99], v[98:99], v[104:105] op_sel_hi:[1,0]\n"
    "v_cvt_pk_bf16_f32 v96, v96, v97\n"
    "v_cvt_pk_bf16_f32 v97, v98, v99\n"
    "ds_write_b64 v11, v[96:97] offset:0\n"
    "v_pk_mul_f32 v[100:101], v[100:101], v[104:105] op_sel_hi:[1,0]\n"
    "v_pk_mul_f32 v[102:103], v[102:103], v[104:105] op_sel_hi:[1,0]\n"
    "v_cvt_pk_bf16_f32 v98, v100, v101\n"
    "v_cvt_pk_bf16_f32 v99, v102, v103\n"
    "ds_write_b64 v11, v[98:99] offset:8\n"
    "s_mov_b64 exec, 7\n"
    "buffer_load_dword v116, v0, s[16:19], s59 offen sc1\n"
    "s_mov_b64 exec, -1\n"
    "v_mfma_f32_32x32x8_bf16 v[72:87], v[50:51], v[50:51], 0\n"
    "s_add_u32 s36, s36, 1\n"
    "s_cmp_eq_u32 s36, 64\n"
    "s_waitcnt lgkmcnt(0)\n"
    "s_waitcnt vmcnt(3)\n"
    "s_barrier\n"
    "ds_read_b128 v[104:107], v12 offset:0\n"
    "ds_read_b128 v[108:111], v12 offset:1024\n"
    "s_nop 4\n"
    "v_mfma_f32_32x32x8_bf16 v[48:63], v[72:73], v[72:73], 0\n"
    "s_cbranch_scc1 LEND%=\n"
    "s_waitcnt lgkmcnt(1)\n"
    "v_smfmac_f32_16x16x64_bf16 v[72:75], v[104:107], v[168:175], v1\n"
    "ds_read_b128 v[64:67], v7 offset:20480\n"
    "s_waitcnt lgkmcnt(1)\n"
    "v_smfmac_f32_16x16x64_bf16 v[72:75], v[108:111], v[176:183], v1\n"
    "ds_read_b128 v[68:71], v7 offset:21504\n"
    "v_smfmac_f32_16x16x64_bf16 v[76:79], v[104:107], v[184:191], v1\n"
    "ds_read_b128 v[16:19], v7 offset:22528\n"
    "v_smfmac_f32_16x16x64_bf16 v[76:79], v[108:111], v[192:199], v1\n"
    "ds_read_b128 v[20:23], v7 offset:23552\n"
    "v_add_f32 v72, v72, v74\n"
    "v_smfmac_f32_16x16x64_bf16 v[80:83], v[104:107], v[200:207], v1\n"
    "ds_read_b128 v[24:27], v7 offset:24576\n"
    "v_add_f32 v73, v73, v75\n"
    "v_smfmac_f32_16x16x64_bf16 v[80:83], v[108:111], v[208:215], v1\n"
    "ds_read_b128 v[28:31], v7 offset:25600\n"
    "v_add_f32 v76, v76, v78\n"
    "v_smfmac_f32_16x16x64_bf16 v[84:87], v[104:107], v[216:223], v1\n"
    "ds_read_b128 v[32:35], v7 offset:26624\n"
    "v_add_f32 v77, v77, v79\n"
    "v_smfmac_f32_16x16x64_bf16 v[84:87], v[108:111], v[224:231], v1\n"
    "ds_read_b128 v[36:39], v7 offset:27648\n"
    "v_cvt_pk_bf16_f32 v88, v72, v76\n"
    "v_cvt_pk_bf16_f32 v90, v73, v77\n"
    "v_smfmac_f32_16x16x64_bf16 v[48:51], v[104:107], v[232:239], v1\n"
    "ds_read_b128 v[40:43], v7 offset:28672\n"
    "v_add_f32 v80, v80, v82\n"
    "v_add_f32 v81, v81, v83\n"
    "v_smfmac_f32_16x16x64_bf16 v[48:51], v[108:111], v[240:247], v1\n"
    "ds_read_b128 v[44:47], v7 offset:29696\n"
    "v_add_f32 v85, v85, v87\n"
    "v_add_f32 v84, v84, v86\n"
    "v_cvt_pk_bf16_f32 v91, v81, v85\n"
    "v_cvt_pk_bf16_f32 v89, v80, v84\n"
    "v_and_or_b32 v91, v91, -2, s64\n"
    "buffer_store_dwordx4 v[88:91], v13, s[16:19], s46 offen sc1\n"
    "v_smfmac_f32_16x16x64_bf16 v[52:55], v[104:107], v[248:255], v1\n"
    "s_waitcnt lgkmcnt(8)\n"
    "v_smfmac_f32_16x16x64_bf16 v[52:55], v[108:111], v[64:71], v1\n"
    "s_waitcnt lgkmcnt(6)\n"
    "v_add_f32 v48, v48, v50\n"
    "v_smfmac_f32_16x16x64_bf16 v[56:59], v[104:107], v[16:23], v1\n"
    "s_waitcnt lgkmcnt(4)\n"
    "v_add_f32 v49, v49, v51\n"
    "v_smfmac_f32_16x16x64_bf16 v[56:59], v[108:111], v[24:31], v1\n"
    "s_waitcnt lgkmcnt(2)\n"
    "v_add_f32 v52, v52, v54\n"
    "v_add_f32 v53, v53, v55\n"
    "v_smfmac_f32_16x16x64_bf16 v[60:63], v[104:107], v[32:39], v1\n"
    "s_waitcnt lgkmcnt(0)\n"
    "v_cvt_pk_bf16_f32 v92, v48, v52\n"
    "v_cvt_pk_bf16_f32 v94, v49, v53\n"
    "v_smfmac_f32_16x16x64_bf16 v[60:63], v[108:111], v[40:47], v1\n"
    "v_add_f32 v56, v56, v58\n"
    "v_add_f32 v57, v57, v59\n"
    "s_nop 5\n"
    "v_add_f32 v61, v61, v63\n"
    "v_add_f32 v60, v60, v62\n"
    "v_cvt_pk_bf16_f32 v95, v57, v61\n"
    "v_cvt_pk_bf16_f32 v93, v56, v60\n"
    "v_and_or_b32 v95, v95, -2, s64\n"
    "buffer_store_dwordx4 v[92:95], v13, s[16:19], s47 offen sc1\n"
    "s_waitcnt vmcnt(3)\n"
    "v_mov_b32 v118, s72\n"
    "v_mov_b32 v119, s36\n"
    "ds_write_b32 v118, v119 offset:48\n"
    "s_add_u32 s54, s50, s57\n"
    "s_lshr_b32 s38, s54, 8\n"
    "s_xor_b32 s38, s38, s74\n"
    "s_and_b32 s38, s38, 0x2000\n"
    "s_or_b32 s54, s54, s38\n"
    "s_add_u32 s50, s50, 2621440\n"
    "s_add_u32 s48, s48, 16384\n"
    "s_cmp_eq_u32 s61, 1\n"
    "s_cbranch_scc1 LW1%=\n"
    "s_cmp_gt_u32 s61, 1\n"
    "s_cbranch_scc1 LW2%=\n"
    "s_waitcnt vmcnt(2)\n"
    "v_cmp_ne_u32 vcc, 0, v116\n"
    "v_readfirstlane_b32 s70, v116\n"
    "s_nop 0\n"
    "s_andn2_b32 s38, 7, vcc_lo\n"
    "s_cbranch_scc1 LE2%=\n"
    "s_lshr_b32 s38, s70, 16\n"
    "s_cmp_gt_i32 s38, s1\n"
    "s_cbranch_scc1 LREC%=\n"
    "LE2%=:\n"
    "s_add_u32 s73, s46, s63\n"
    "s_add_u32 s66, s73, 4096\n"
    "s_add_u32 s39, s44, s9\n"
    "s_waitcnt lgkmcnt(0)\n"
    "LUG%=:\n"
    "s_memtime s[40:41]\n"
    "s_waitcnt lgkmcnt(0)\n"
    "s_sub_u32 s40, s39, s40\n"
    "s_sub_u32 s41, s40, 253\n"
    "s_cmp_lt_u32 s41, 16384\n"
    "s_cbranch_scc1 LUG%=\n"
    "s_cmp_lt_u32 s40, 253\n"
    "s_cselect_b32 s40, s40, 0\n"
    "s_and_b32 s40, s40, 0xfc\n"
    "s_getpc_b64 s[42:43]\n"
    "s_sub_u32 s40, 272, s40\n"
    "s_add_u32 s42, s42, s40\n"
    "s_addc_u32 s43, s43, 0\n"
    "s_setpc_b64 s[42:43]\n"
    ".rept 63\n"
    "s_nop 0\n"
    ".endr\n"
    "buffer_load_dwordx4 v[16:19], v0, s[16:19], s73 offen offset:0 sc1\n"
    "buffer_load_dwordx4 v[20:23], v0, s[16:19], s73 offen offset:1024 sc1\n"
    "buffer_load_dwordx4 v[24:27], v0, s[16:19], s73 offen offset:2048 sc1\n"
    "buffer_load_dwordx4 v[28:31], v0, s[16:19], s73 offen offset:3072 sc1\n"
    "buffer_load_dwordx4 v[32:35], v0, s[16:19], s66 offen offset:0 sc1\n"
    "buffer_load_dwordx4 v[36:39], v0, s[16:19], s66 offen offset:1024 sc1\n"
    "buffer_load_dwordx4 v[40:43], v0, s[16:19], s66 offen offset:2048 sc1\n"
    "buffer_load_dwordx4 v[44:47], v0, s[16:19], s66 offen offset:3072 sc1\n"
    "ds_read_b128 v[48:51], v9 offset:35584\n"
    "ds_read_b128 v[64:67], v7 offset:0\n"
    "ds_read_b128 v[68:71], v7 offset:1024\n"
    "ds_read_b128 v[72:75], v7 offset:2048\n"
    "ds_read_b128 v[76:79], v7 offset:3072\n"
    "ds_read_b128 v[80:83], v7 offset:4096\n"
    "ds_read_b128 v[84:87], v7 offset:5120\n"
    "ds_read_b128 v[88:91], v7 offset:6144\n"
    "ds_read_b128 v[92:95], v7 offset:7168\n"
    "ds_read_b128 v[96:99], v7 offset:8192\n"
    "ds_read_b128 v[100:103], v7 offset:9216\n"
    "ds_read_b128 v[104:107], v7 offset:10240\n"
    "ds_read_b128 v[108:111], v7 offset:11264\n"
    "s_add_u32 s53, s50, s55\n"
    "s_mov_b32 s49, s48\n"
    "s_add_u32 s51, s53, 0\n"
    "s_lshr_b32 s38, s51, 8\n"
    "s_xor_b32 s38, s38, s56\n"
    "s_and_b32 s38, s38, 0x2000\n"
    "s_or_b32 s51, s51, s38\n"
    "s_add_u32 s52, s53, 524288\n"
    "s_lshr_b32 s38, s52, 8\n"
    "s_xor_b32 s38, s38, s56\n"
    "s_and_b32 s38, s38, 0x2000\n"
    "s_or_b32 s52, s52, s38\n"
    "s_add_u32 s53, s53, 1048576\n"
    "s_lshr_b32 s38, s53, 8\n"
    "s_xor_b32 s38, s38, s56\n"
    "s_and_b32 s38, s38, 0x2000\n"
    "s_or_b32 s53, s53, s38\n"
    "s_waitcnt lgkmcnt(12)\n"
    "s_waitcnt vmcnt(7)\n"
    "v_mfma_f32_16x16x32_bf16 v[52:55], v[48:51], v[16:19], 0\n"
    "s_waitcnt vmcnt(6)\n"
    "v_mfma_f32_16x16x32_bf16 v[52:55], v[48:51], v[20:23], v[52:55]\n"
    "s_waitcnt vmcnt(5)\n"
    "v_bitop3_b32 v60, v19, v23, s64 bitop3:0x7e\n"
    "v_mfma_f32_16x16x32_bf16 v[52:55], v[48:51], v[24:27], v[52:55]\n"
    "s_waitcnt vmcnt(4)\n"
    "v_mfma_f32_16x16x32_bf16 v[52:55], v[48:51], v[28:31], v[52:55]\n"
    "s_waitcnt vmcnt(3)\n"
    "v_bitop3_b32 v61, v27, v31, s64 bitop3:0x7e\n"
    "v_mfma_f32_16x16x32_bf16 v[56:59], v[48:51], v[32:35], 0\n"
    "s_waitcnt vmcnt(2)\n"
    "v_mfma_f32_16x16x32_bf16 v[56:59], v[48:51], v[36:39], v[56:59]\n"
    "v_bitop3_b32 v62, v35, v39, s64 bitop3:0x7e\n"
    "v_or3_b32 v60, v60, v61, v62\n"
    "v_add_f32_dpp v52, v52, v52 row_ror:8 row_mask:0xf bank_mask:0xf\n"
    "v_add_f32_dpp v53, v53, v53 row_ror:8 row_mask:0xf bank_mask:0xf\n"
    "v_add_f32_dpp v54, v54, v54 row_ror:8 row_mask:0xf bank_mask:0xf\n"
    "v_add_f32_dpp v55, v55, v55 row_ror:8 row_mask:0xf bank_mask:0xf\n"
    "v_cvt_pk_bf16_f32 v113, v54, v55\n"
    "v_cvt_pk_bf16_f32 v112, v52, v53\n"
    "s_waitcnt vmcnt(1)\n"
    "v_mfma_f32_16x16x32_bf16 v[56:59], v[48:51], v[40:43], v[56:59]\n"
    "s_waitcnt vmcnt(0)\n"
    "v_mfma_f32_16x16x32_bf16 v[56:59], v[48:51], v[44:47], v[56:59]\n"
    "v_bitop3_b32 v63, v43, v47, s64 bitop3:0x7e\n"
    "v_or_b32 v60, v60, v63\n"
    "v_and_b32 v61, 1, v60\n"
    "v_cmp_ne_u32 vcc, 0, v61\n"
    "s_cbranch_vccnz LGR%=\n"
    "s_nop 2\n"
    "v_add_f32_dpp v56, v56, v56 row_ror:8 row_mask:0xf bank_mask:0xf\n"
    "v_add_f32_dpp v57, v57, v57 row_ror:8 row_mask:0xf bank_mask:0xf\n"
    "v_add_f32_dpp v58, v58, v58 row_ror:8 row_mask:0xf bank_mask:0xf\n"
    "v_add_f32_dpp v59, v59, v59 row_ror:8 row_mask:0xf bank_mask:0xf\n"
    "v_cvt_pk_bf16_f32 v115, v58, v59\n"
    "v_cvt_pk_bf16_f32 v114, v56, v57\n"
    "v_min_u32 v115, -2, v115\n"
    "buffer_store_dwordx4 v[112:115], v14, s[16:19], s48 offen sc1\n"
    "ds_write_b32 v118, v119 offset:12\n"
    "v_cmp_ne_u32 vcc, 0, v116\n"
    "v_readfirstlane_b32 s70, v116\n"
    "s_nop 0\n"
    "s_andn2_b32 s38, 7, vcc_lo\n"
    "s_cbranch_scc0 LS1K%=\n"
    "s_mov_b32 s37, 0x80000\n"
    "LS1W%=:\n"
    "s_mov_b64 exec, 7\n"
    "buffer_load_dword v116, v0, s[16:19], s59 offen sc1\n"
    "s_mov_b64 exec, -1\n"
    "s_waitcnt vmcnt(0)\n"
    "v_cmp_ne_u32 vcc, 0, v116\n"
    "v_readfirstlane_b32 s70, v116\n"
    "s_nop 0\n"
    "s_andn2_b32 s38, 7, vcc_lo\n"
    "s_cbranch_scc0 LS1K%=\n"
    "s_sleep 2\n"
    "s_sub_u32 s37, s37, 1\n"
    "s_cbranch_scc0 LS1W%=\n"
    "s_branch LFAIL%=\n"
    "LS1K%=:\n"
    "s_lshr_b32 s38, s70, 16\n"
    "s_cmp_gt_i32 s38, s1\n"
    "s_cbranch_scc1 LREC%=\n"
    "s_mov_b32 s32, s62\n"
    "s_mov_b32 s68, s62\n"
    "s_xor_b32 s46, s46, 0x80000\n"
    "s_add_u32 s47, s46, 0x40000\n"
    "s_bfe_u32 s64, s36, 0x10001\n"
    "s_add_u32 s44, s44, s8\n"
    "s_add_u32 s59, s59, 128\n"
    "v_mov_b64 v[112:113], 0\n"
    "v_mov_b64 v[114:115], 0\n"
    "v_mov_b64 v[116:117], 0\n"
    "v_mov_b64 v[118:119], 0\n"
    "v_mfma_f32_32x32x8_bf16 v[48:63], v[112:113], v[112:113], 0\n"
    "v_lshrrev_b32 v40, 4, v0\n"
    "v_and_b32 v40, 0x30, v40\n"
    "v_lshl_add_u32 v40, v40, 4, v0\n"
    "v_mov_b32 v41, 158224\n"
    "s_add_u32 s73, s49, s11\n"
    "s_add_u32 s73, s73, 4096\n"
    "s_add_u32 m0, s11, 151552\n"
    "s_branch LBM%=\n"
    "LGR%=:\n"
    "s_add_u32 s33, s33, 1\n"
    "s_cmp_gt_u32 s33, 0x40000\n"
    "s_cbranch_scc1 LFAIL%=\n"
    "s_and_b32 s38, s33, 63\n"
    "s_cbranch_scc1 LE4%=\n"
    "s_mov_b64 exec, 7\n"
    "buffer_load_dword v116, v0, s[16:19], s59 offen sc1\n"
    "s_mov_b64 exec, -1\n"
    "s_waitcnt vmcnt(0)\n"
    "v_cmp_ne_u32 vcc, 0, v116\n"
    "v_readfirstlane_b32 s70, v116\n"
    "s_nop 0\n"
    "s_andn2_b32 s38, 7, vcc_lo\n"
    "s_cbranch_scc1 LE4%=\n"
    "s_lshr_b32 s38, s70, 16\n"
    "s_cmp_gt_i32 s38, s1\n"
    "s_cbranch_scc1 LREC%=\n"
    "LE4%=:\n"
    "s_add_u32 s44, s44, s13\n"
    "LGM%=:\n"
    "s_memtime s[40:41]\n"
    "s_add_u32 s42, s21, s9\n"
    "s_waitcnt lgkmcnt(0)\n"
    "s_sub_u32 s40, s40, s42\n"
    "s_sub_u32 s41, s44, s40\n"
    "s_cmp_ge_i32 s41, 0\n"
    "s_cbranch_scc1 LGMD%=\n"
    "LGMI%=:\n"
    "s_add_u32 s44, s44, s13\n"
    "s_add_u32 s41, s41, s13\n"
    "s_cbranch_scc0 LGMI%=\n"
    "s_branch LGM%=\n"
    "LGMD%=:\n"
    "s_add_u32 s39, s44, s9\n"
    "s_add_u32 s38, s44, s8\n"
    "v_mov_b32 v60, s72\n"
    "v_mov_b32 v61, s38\n"
    "ds_write_b32 v60, v61 offset:16\n"
    "s_branch LUG%=\n"
    "LMR%=:\n"
    "s_add_u32 s33, s33, 1\n"
    "s_cmp_gt_u32 s33, 0x40000\n"
    "s_cbranch_scc1 LFAIL%=\n"
    "s_add_u32 s39, s44, s13\n"
    "s_mov_b32 s54, 0x80000000\n"
    "LSL%=:\n"
    "s_mov_b32 s44, s39\n"
    "s_waitcnt lgkmcnt(0)\n"
    "s_barrier\n"
    "s_branch LPRE%=\n"
    "LW1%=:\n"
    "v_mov_b32 v64, 158240\n"
    "s_mov_b32 s37, 0x80000\n"
    "LOW%=:\n"
    "ds_read_b128 v[68:71], v64 offset:16\n"
    "s_waitcnt lgkmcnt(0)\n"
    "v_min3_u32 v68, v68, v70, v71\n"
    "v_cmp_eq_u32 vcc, s36, v68\n"
    "s_cbranch_vccnz LOK%=\n"
    "s_sleep 0\n"
    "s_sub_u32 s37, s37, 1\n"
    "s_cbranch_scc0 LOW%=\n"
    "s_branch LFAIL%=\n"
    "LOK%=:\n"
    "buffer_store_dword v1, off, s[16:19], s58 sc1\n"
    "s_add_u32 s58, s58, 4\n"
    "s_bitset0_b32 s58, 31\n"
    "s_waitcnt vmcnt(3)\n"
    "v_cmp_ne_u32 vcc, 0, v116\n"
    "v_readfirstlane_b32 s70, v116\n"
    "s_nop 0\n"
    "s_andn2_b32 s38, 7, vcc_lo\n"
    "s_cbranch_scc0 LS2K%=\n"
    "s_mov_b32 s37, 0x80000\n"
    "LS2W%=:\n"
    "s_mov_b64 exec, 7\n"
    "buffer_load_dword v116, v0, s[16:19], s59 offen sc1\n"
    "s_mov_b64 exec, -1\n"
    "s_waitcnt vmcnt(0)\n"
    "v_cmp_ne_u32 vcc, 0, v116\n"
    "v_readfirstlane_b32 s70, v116\n"
    "s_nop 0\n"
    "s_andn2_b32 s38, 7, vcc_lo\n"
    "s_cbranch_scc0 LS2K%=\n"
    "s_sleep 2\n"
    "s_sub_u32 s37, s37, 1\n"
    "s_cbranch_scc0 LS2W%=\n"
    "s_branch LFAIL%=\n"
    "LS2K%=:\n"
    "s_lshr_b32 s38, s70, 16\n"
    "s_cmp_gt_i32 s38, s1\n"
    "s_cbranch_scc1 LREC%=\n"
    "s_add_u32 s53, s50, s55\n"
    "s_mov_b32 s49, s48\n"
    "s_add_u32 s51, s53, 0\n"
    "s_lshr_b32 s38, s51, 8\n"
    "s_xor_b32 s38, s38, s56\n"
    "s_and_b32 s38, s38, 0x2000\n"
    "s_or_b32 s51, s51, s38\n"
    "s_add_u32 s52, s53, 524288\n"
    "s_lshr_b32 s38, s52, 8\n"
    "s_xor_b32 s38, s38, s56\n"
    "s_and_b32 s38, s38, 0x2000\n"
    "s_or_b32 s52, s52, s38\n"
    "s_add_u32 s53, s53, 1048576\n"
    "s_lshr_b32 s38, s53, 8\n"
    "s_xor_b32 s38, s38, s56\n"
    "s_and_b32 s38, s38, 0x2000\n"
    "s_or_b32 s53, s53, s38\n"
    "s_add_u32 s66, s44, s12\n"
    "s_mov_b32 s32, s62\n"
    "s_mov_b32 s68, s62\n"
    "s_xor_b32 s46, s46, 0x80000\n"
    "s_add_u32 s47, s46, 0x40000\n"
    "s_bfe_u32 s64, s36, 0x10001\n"
    "s_add_u32 s44, s44, s8\n"
    "s_add_u32 s59, s59, 128\n"
    "ds_read_b128 v[64:67], v7 offset:0\n"
    "ds_read_b128 v[68:71], v7 offset:1024\n"
    "ds_read_b128 v[72:75], v7 offset:2048\n"
    "ds_read_b128 v[76:79], v7 offset:3072\n"
    "ds_read_b128 v[80:83], v7 offset:4096\n"
    "ds_read_b128 v[84:87], v7 offset:5120\n"
    "ds_read_b128 v[88:91], v7 offset:6144\n"
    "ds_read_b128 v[92:95], v7 offset:7168\n"
    "ds_read_b128 v[96:99], v7 offset:8192\n"
    "ds_read_b128 v[100:103], v7 offset:9216\n"
    "ds_read_b128 v[104:107], v7 offset:10240\n"
    "ds_read_b128 v[108:111], v7 offset:11264\n"
    "v_mov_b64 v[112:113], 0\n"
    "v_mov_b64 v[114:115], 0\n"
    "v_mov_b64 v[116:117], 0\n"
    "v_mov_b64 v[118:119], 0\n"
    "v_mfma_f32_32x32x8_bf16 v[48:63], v[112:113], v[112:113], 0\n"
    "v_lshrrev_b32 v40, 4, v0\n"
    "v_and_b32 v40, 0x30, v40\n"
    "v_lshl_add_u32 v40, v40, 4, v0\n"
    "v_mov_b32 v41, 158224\n"
    "s_add_u32 s73, s49, s11\n"
    "s_add_u32 s73, s73, 4096\n"
    "s_add_u32 m0, s11, 151552\n"
    "s_mov_b32 s37, 0x80000\n"
    "LKW%=:\n"
    "ds_read_b64 v[16:17], v41 offset:16\n"
    "s_waitcnt lgkmcnt(0)\n"
    "v_min_u32 v16, v16, v17\n"
    "v_cmp_eq_u32 vcc, s36, v16\n"
    "s_cbranch_vccnz LKK%=\n"
    "s_sleep 1\n"
    "s_sub_u32 s37, s37, 1\n"
    "s_cbranch_scc0 LKW%=\n"
    "s_branch LFAIL%=\n"
    "LKK%=:\n"
    "LUF%=:\n"
    "s_memtime s[40:41]\n"
    "s_waitcnt lgkmcnt(0)\n"
    "s_sub_u32 s40, s66, s40\n"
    "s_sub_u32 s41, s40, 253\n"
    "s_cmp_lt_u32 s41, 16384\n"
    "s_cbranch_scc1 LUF%=\n"
    "s_cmp_lt_u32 s40, 253\n"
    "s_cselect_b32 s40, s40, 0\n"
    "s_and_b32 s40, s40, 0xfc\n"
    "s_getpc_b64 s[42:43]\n"
    "s_sub_u32 s40, 272, s40\n"
    "s_add_u32 s42, s42, s40\n"
    "s_addc_u32 s43, s43, 0\n"
    "s_setpc_b64 s[42:43]\n"
    ".rept 63\n"
    "s_nop 0\n"
    ".endr\n"
    "s_sub_u32 s51, s51, 2048\n"
    "s_mov_b32 m0, 122880\n"
    "s_nop 0\n"
    "buffer_load_dwordx4 v0, s[16:19], s51 offen offset:0 sc1 lds\n"
    "buffer_load_dwordx4 v0, s[16:19], s51 offen offset:1024 sc1 lds\n"
    "buffer_load_dwordx4 v0, s[16:19], s51 offen offset:2048 sc1 lds\n"
    "buffer_load_dwordx4 v0, s[16:19], s51 offen offset:3072 sc1 lds\n"
    "s_mov_b32 m0, 126976\n"
    "s_add_u32 s51, s51, 4096\n"
    "buffer_load_dwordx4 v0, s[16:19], s51 offen offset:0 sc1 lds\n"
    "buffer_load_dwordx4 v0, s[16:19], s51 offen offset:1024 sc1 lds\n"
    "buffer_load_dwordx4 v0, s[16:19], s51 offen offset:2048 sc1 lds\n"
    "buffer_load_dwordx4 v0, s[16:19], s51 offen offset:3072 sc1 lds\n"
    "LMN%=:\n"
    "LMM%=:\n"
    "s_memtime s[40:41]\n"
    "s_add_u32 s42, s21, s10\n"
    "s_waitcnt lgkmcnt(0)\n"
    "s_sub_u32 s40, s40, s42\n"
    "s_sub_u32 s41, s44, s40\n"
    "s_cmp_ge_i32 s41, 0\n"
    "s_cbranch_scc1 LMMD%=\n"
    "LMMI%=:\n"
    "s_add_u32 s44, s44, s13\n"
    "s_add_u32 s41, s41, s13\n"
    "s_cbranch_scc0 LMMI%=\n"
    "s_branch LMM%=\n"
    "LMMD%=:\n"
    "s_add_u32 s39, s44, s10\n"
    "v_mov_b32 v44, s72\n"
    "v_mov_b32 v45, s44\n"
    "ds_write_b32 v44, v45 offset:16\n"
    "s_mov_b32 m0, 147456\n"
    "s_mov_b32 s37, 64\n"
    "s_waitcnt lgkmcnt(0)\n"
    "LUL%=:\n"
    "s_memtime s[40:41]\n"
    "s_waitcnt lgkmcnt(0)\n"
    "s_sub_u32 s40, s39, s40\n"
    "s_sub_u32 s41, s40, 253\n"
    "s_cmp_lt_u32 s41, 16384\n"
    "s_cbranch_scc1 LUL%=\n"
    "s_cmp_lt_u32 s40, 253\n"
    "s_cselect_b32 s40, s40, 0\n"
    "s_and_b32 s40, s40, 0xfc\n"
    "s_getpc_b64 s[42:43]\n"
    "s_sub_u32 s40, 272, s40\n"
    "s_add_u32 s42, s42, s40\n"
    "s_addc_u32 s43, s43, 0\n"
    "s_setpc_b64 s[42:43]\n"
    ".rept 63\n"
    "s_nop 0\n"
    ".endr\n"
    "buffer_load_dwordx4 v0, s[16:19], s49 offen offset:0 sc1 lds\n"
    "buffer_load_dwordx4 v0, s[16:19], s49 offen offset:1024 sc1 lds\n"
    "buffer_load_dwordx4 v0, s[16:19], s49 offen offset:2048 sc1 lds\n"
    "buffer_load_dwordx4 v0, s[16:19], s49 offen offset:3072 sc1 lds\n"
    "s_mov_b32 m0, 152576\n"
    "s_getpc_b64 s[42:43]\n"
    "s_add_u32 s42, s42, s0\n"
    "s_addc_u32 s43, s43, 0\n"
    "s_setpc_b64 s[42:43]\n"
    ".rept 63\n"
    "s_nop 1\n"
    ".endr\n"
    "LDW%=:\n"
    "ds_read2_b32 v[46:47], v44 offset0:2 offset1:3\n"
    "s_waitcnt lgkmcnt(0)\n"
    "v_readfirstlane_b32 s39, v47\n"
    "v_readfirstlane_b32 s40, v46\n"
    "s_sub_u32 s38, s39, s44\n"
    "s_cmp_gt_i32 s38, 0\n"
    "s_cselect_b32 s44, s39, s44\n"
    "s_cbranch_scc1 LMN%=\n"
    "s_cmp_eq_u32 s40, s36\n"
    "s_cbranch_scc1 LBM%=\n"
    "s_sub_u32 s37, s37, 1\n"
    "s_cbranch_scc0 LDW%=\n"
    "s_branch LBM%=\n"
    "LW2%=:\n"
    "s_waitcnt vmcnt(2)\n"
    "v_cmp_ne_u32 vcc, 0, v116\n"
    "v_readfirstlane_b32 s70, v116\n"
    "s_nop 0\n"
    "s_andn2_b32 s38, 7, vcc_lo\n"
    "s_cbranch_scc1 LE3%=\n"
    "s_lshr_b32 s38, s70, 16\n"
    "s_cmp_gt_i32 s38, s1\n"
    "s_cbranch_scc1 LREC%=\n"
    "LE3%=:\n"
    "ds_read_b128 v[16:19], v14 offset:0\n"
    "v_add_u32 v118, 122880, v0\n"
    "ds_read_b128 v[32:35], v118 offset:0\n"
    "ds_read_b128 v[48:51], v118 offset:4096\n"
    "ds_read_b128 v[20:23], v14 offset:1024\n"
    "ds_read_b128 v[36:39], v118 offset:1024\n"
    "ds_read_b128 v[52:55], v118 offset:5120\n"
    "v_mov_b32 v111, 0xd0289000\n"
    "v_cndmask_b32 v80, v111, 0, s[80:81]\n"
    "v_cndmask_b32 v81, v111, 0, s[82:83]\n"
    "v_cndmask_b32 v82, v111, 0, s[84:85]\n"
    "v_cndmask_b32 v83, v111, 0, s[86:87]\n"
    "v_cndmask_b32 v84, v111, 0, s[88:89]\n"
    "v_cndmask_b32 v85, v111, 0, s[90:91]\n"
    "v_cndmask_b32 v86, v111, 0, s[92:93]\n"
    "v_cndmask_b32 v87, v111, 0, s[94:95]\n"
    "v_lshrrev_b32 v112, 1, v0\n"
    "v_add_u32 v112, 149760, v112\n"
    "ds_read_b128 v[24:27], v14 offset:2048\n"
    "ds_read_b128 v[40:43], v118 offset:2048\n"
    "ds_read_b128 v[56:59], v118 offset:6144\n"
    "s_waitcnt lgkmcnt(7)\n"
    "v_mfma_f32_16x16x32_bf16 v[80:83], v[32:35], v[16:19], v[80:83]\n"
    "ds_read_b128 v[28:31], v14 offset:3072\n"
    "ds_read_b128 v[44:47], v118 offset:3072\n"
    "s_waitcnt lgkmcnt(8)\n"
    "v_mfma_f32_16x16x32_bf16 v[84:87], v[48:51], v[16:19], v[84:87]\n"
    "ds_read_b128 v[60:63], v118 offset:7168\n"
    "s_waitcnt lgkmcnt(7)\n"
    "v_mov_b32 v114, s72\n"
    "v_mfma_f32_16x16x32_bf16 v[80:83], v[36:39], v[20:23], v[80:83]\n"
    "ds_read_b128 v[64:67], v6 offset:0\n"
    "ds_read_b128 v[68:71], v6 offset:64\n"
    "s_waitcnt lgkmcnt(8)\n"
    "v_mfma_f32_16x16x32_bf16 v[84:87], v[52:55], v[20:23], v[84:87]\n"
    "ds_read_b128 v[72:75], v6 offset:128\n"
    "s_waitcnt lgkmcnt(7)\n"
    "v_mov_b32 v105, 0\n"
    "v_mfma_f32_16x16x32_bf16 v[80:83], v[40:43], v[24:27], v[80:83]\n"
    "ds_read_b128 v[76:79], v6 offset:192\n"
    "ds_read_b64 v[92:93], v112 offset:0\n"
    "s_waitcnt lgkmcnt(8)\n"
    "v_mfma_f32_16x16x32_bf16 v[84:87], v[56:59], v[24:27], v[84:87]\n"
    "ds_read_b64 v[94:95], v112 offset:512\n"
    "s_waitcnt lgkmcnt(7)\n"
    "v_mov_b32 v106, 0\n"
    "v_mfma_f32_16x16x32_bf16 v[80:83], v[44:47], v[28:31], v[80:83]\n"
    "ds_read_b64 v[96:97], v112 offset:1024\n"
    "ds_read_b64 v[98:99], v112 offset:1536\n"
    "s_waitcnt lgkmcnt(8)\n"
    "v_mfma_f32_16x16x32_bf16 v[84:87], v[60:63], v[28:31], v[84:87]\n"
    "ds_write_b32 v114, v119 offset:24\n"
    "s_waitcnt lgkmcnt(8)\n"
    "v_mfma_f32_16x16x32_bf16 v[88:91], v[64:67], v[16:19], 0\n"
    "ds_read_b128 v[32:35], v118 offset:8192\n"
    "ds_read_b128 v[36:39], v118 offset:9216\n"
    "s_waitcnt lgkmcnt(9)\n"
    "v_mfma_f32_16x16x32_bf16 v[88:91], v[68:71], v[20:23], v[88:91]\n"
    "ds_read_b128 v[40:43], v118 offset:10240\n"
    "ds_read_b128 v[44:47], v118 offset:11264\n"
    "s_waitcnt lgkmcnt(10)\n"
    "v_mfma_f32_16x16x32_bf16 v[88:91], v[72:75], v[24:27], v[88:91]\n"
    "ds_read_b128 v[48:51], v118 offset:12288\n"
    "ds_read_b128 v[52:55], v118 offset:13312\n"
    "s_waitcnt lgkmcnt(11)\n"
    "v_max3_f32 v108, v80, v81, v82\n"
    "v_max3_f32 v109, v83, v84, v85\n"
    "v_mfma_f32_16x16x32_bf16 v[88:91], v[76:79], v[28:31], v[88:91]\n"
    "ds_read_b128 v[56:59], v118 offset:14336\n"
    "ds_read_b128 v[60:63], v118 offset:15360\n"
    "v_max3_f32 v108, v108, v86, v87\n"
    "v_max_f32 v108, v108, v109\n"
    "v_mov_b32 v109, v108\n"
    "s_nop 1\n"
    "v_permlane32_swap_b32 v108, v109\n"
    "v_max_f32 v108, v108, v109\n"
    "v_max3_f32 v108, v108, v88, v89\n"
    "v_max3_f32 v108, v108, v90, v91\n"
    "v_mov_b32 v109, v108\n"
    "s_nop 1\n"
    "v_permlane16_swap_b32 v108, v109\n"
    "v_max_f32 v108, v108, v109\n"
    "v_mul_f32 v108, 0xbe0293ee, v108\n"
    "v_pk_fma_f32 v[80:81], v[80:81], s[64:65], v[108:109] op_sel:[0,1,0] op_sel_hi:[1,1,0]\n"
    "v_pk_fma_f32 v[82:83], v[82:83], s[64:65], v[108:109] op_sel:[0,1,0] op_sel_hi:[1,1,0]\n"
    "v_pk_fma_f32 v[84:85], v[84:85], s[64:65], v[108:109] op_sel:[0,1,0] op_sel_hi:[1,1,0]\n"
    "v_pk_fma_f32 v[86:87], v[86:87], s[64:65], v[108:109] op_sel:[0,1,0] op_sel_hi:[1,1,0]\n"
    "v_pk_fma_f32 v[88:89], v[88:89], s[64:65], v[108:109] op_sel:[0,1,0] op_sel_hi:[1,1,0]\n"
    "v_exp_f32 v80, v80\n"
    "v_exp_f32 v81, v81\n"
    "v_exp_f32 v82, v82\n"
    "v_exp_f32 v83, v83\n"
    "v_exp_f32 v84, v84\n"
    "v_exp_f32 v85, v85\n"
    "v_exp_f32 v86, v86\n"
    "v_exp_f32 v87, v87\n"
    "v_exp_f32 v88, v88\n"
    "v_exp_f32 v89, v89\n"
    "v_pk_add_f32 v[108:109], v[80:81], v[82:83]\n"
    "v_pk_add_f32 v[110:111], v[84:85], v[86:87]\n"
    "v_pk_add_f32 v[108:109], v[108:109], v[88:89]\n"
    "v_pk_add_f32 v[108:109], v[108:109], v[110:111]\n"
    "v_add_f32 v108, v108, v109\n"
    "v_mov_b32 v109, v108\n"
    "s_nop 1\n"
    "v_permlane16_swap_b32 v108, v109\n"
    "v_add_f32 v108, v108, v109\n"
    "v_mov_b32 v109, v108\n"
    "s_nop 1\n"
    "v_permlane32_swap_b32 v108, v109\n"
    "v_add_f32 v108, v108, v109\n"
    "v_rcp_f32 v108, v108\n"
    "s_nop 0\n"
    "v_pk_mul_f32 v[80:81], v[80:81], v[108:109] op_sel_hi:[1,0]\n"
    "v_pk_mul_f32 v[82:83], v[82:83], v[108:109] op_sel_hi:[1,0]\n"
    "v_pk_mul_f32 v[84:85], v[84:85], v[108:109] op_sel_hi:[1,0]\n"
    "v_pk_mul_f32 v[86:87], v[86:87], v[108:109] op_sel_hi:[1,0]\n"
    "v_pk_mul_f32 v[88:89], v[88:89], v[108:109] op_sel_hi:[1,0]\n"
    "v_cvt_pk_bf16_f32 v100, v80, v81\n"
    "v_cvt_pk_bf16_f32 v101, v82, v83\n"
    "v_cvt_pk_bf16_f32 v102, v84, v85\n"
    "v_cvt_pk_bf16_f32 v103, v86, v87\n"
    "v_cvt_pk_bf16_f32 v104, v88, v89\n"
    "v_cvt_pk_bf16_f32 v107, v88, v89\n"
    "s_waitcnt lgkmcnt(7)\n"
    "v_mfma_f32_16x16x32_bf16 v[16:19], v[32:35], v[100:103], 0\n"
    "ds_read_b128 v[64:67], v7 offset:0\n"
    "s_waitcnt lgkmcnt(7)\n"
    "v_mfma_f32_16x16x32_bf16 v[20:23], v[36:39], v[100:103], 0\n"
    "ds_read_b128 v[68:71], v7 offset:1024\n"
    "v_mfma_f32_16x16x16_bf16 v[16:19], v[92:93], v[104:105], v[16:19]\n"
    "v_mfma_f32_16x16x16_bf16 v[20:23], v[92:93], v[106:107], v[20:23]\n"
    "s_waitcnt lgkmcnt(7)\n"
    "v_mfma_f32_16x16x32_bf16 v[24:27], v[40:43], v[100:103], 0\n"
    "ds_read_b128 v[72:75], v7 offset:2048\n"
    "s_waitcnt lgkmcnt(7)\n"
    "v_mfma_f32_16x16x32_bf16 v[28:31], v[44:47], v[100:103], 0\n"
    "ds_read_b128 v[76:79], v7 offset:3072\n"
    "v_cvt_pk_bf16_f32 v112, v16, v17\n"
    "v_cvt_pk_bf16_f32 v113, v18, v19\n"
    "v_mfma_f32_16x16x16_bf16 v[24:27], v[94:95], v[104:105], v[24:27]\n"
    "v_cvt_pk_bf16_f32 v114, v20, v21\n"
    "v_cvt_pk_bf16_f32 v115, v22, v23\n"
    "v_mfma_f32_16x16x16_bf16 v[28:31], v[94:95], v[106:107], v[28:31]\n"
    "ds_write_b128 v14, v[112:115] offset:0\n"
    "s_waitcnt lgkmcnt(8)\n"
    "v_mfma_f32_16x16x32_bf16 v[16:19], v[48:51], v[100:103], 0\n"
    "ds_read_b128 v[80:83], v7 offset:4096\n"
    "s_waitcnt lgkmcnt(8)\n"
    "v_mfma_f32_16x16x32_bf16 v[20:23], v[52:55], v[100:103], 0\n"
    "ds_read_b128 v[84:87], v7 offset:5120\n"
    "v_cvt_pk_bf16_f32 v108, v24, v25\n"
    "v_cvt_pk_bf16_f32 v109, v26, v27\n"
    "v_mfma_f32_16x16x16_bf16 v[16:19], v[96:97], v[104:105], v[16:19]\n"
    "v_cvt_pk_bf16_f32 v110, v28, v29\n"
    "v_cvt_pk_bf16_f32 v111, v30, v31\n"
    "v_mfma_f32_16x16x16_bf16 v[20:23], v[96:97], v[106:107], v[20:23]\n"
    "ds_write_b128 v14, v[108:111] offset:1024\n"
    "s_waitcnt lgkmcnt(9)\n"
    "v_mfma_f32_16x16x32_bf16 v[24:27], v[56:59], v[100:103], 0\n"
    "s_waitcnt lgkmcnt(8)\n"
    "v_mfma_f32_16x16x32_bf16 v[28:31], v[60:63], v[100:103], 0\n"
    "v_cvt_pk_bf16_f32 v112, v16, v17\n"
    "v_cvt_pk_bf16_f32 v113, v18, v19\n"
    "v_mfma_f32_16x16x16_bf16 v[24:27], v[98:99], v[104:105], v[24:27]\n"
    "v_cvt_pk_bf16_f32 v114, v20, v21\n"
    "v_cvt_pk_bf16_f32 v115, v22, v23\n"
    "v_mfma_f32_16x16x16_bf16 v[28:31], v[98:99], v[106:107], v[28:31]\n"
    "ds_write_b128 v14, v[112:115] offset:2048\n"
    "ds_read_b128 v[88:91], v7 offset:6144\n"
    "ds_read_b128 v[92:95], v7 offset:7168\n"
    "ds_read_b128 v[96:99], v7 offset:8192\n"
    "ds_read_b128 v[100:103], v7 offset:9216\n"
    "v_cvt_pk_bf16_f32 v108, v24, v25\n"
    "v_cvt_pk_bf16_f32 v109, v26, v27\n"
    "s_nop 0\n"
    "v_cvt_pk_bf16_f32 v110, v28, v29\n"
    "v_cvt_pk_bf16_f32 v111, v30, v31\n"
    "ds_write_b128 v14, v[108:111] offset:3072\n"
    "v_cmp_ne_u32 vcc, 0, v116\n"
    "v_readfirstlane_b32 s70, v116\n"
    "s_nop 0\n"
    "s_andn2_b32 s38, 7, vcc_lo\n"
    "s_cbranch_scc0 LS3K%=\n"
    "s_mov_b32 s37, 0x80000\n"
    "LS3W%=:\n"
    "s_mov_b64 exec, 7\n"
    "buffer_load_dword v116, v0, s[16:19], s59 offen sc1\n"
    "s_mov_b64 exec, -1\n"
    "s_waitcnt vmcnt(0)\n"
    "v_cmp_ne_u32 vcc, 0, v116\n"
    "v_readfirstlane_b32 s70, v116\n"
    "s_nop 0\n"
    "s_andn2_b32 s38, 7, vcc_lo\n"
    "s_cbranch_scc0 LS3K%=\n"
    "s_sleep 2\n"
    "s_sub_u32 s37, s37, 1\n"
    "s_cbranch_scc0 LS3W%=\n"
    "s_branch LFAIL%=\n"
    "LS3K%=:\n"
    "s_lshr_b32 s38, s70, 16\n"
    "s_cmp_gt_i32 s38, s1\n"
    "s_cbranch_scc1 LREC%=\n"
    "s_add_u32 s53, s50, s55\n"
    "s_mov_b32 s49, s48\n"
    "s_add_u32 s51, s53, 0\n"
    "s_lshr_b32 s38, s51, 8\n"
    "s_xor_b32 s38, s38, s56\n"
    "s_and_b32 s38, s38, 0x2000\n"
    "s_or_b32 s51, s51, s38\n"
    "s_add_u32 s52, s53, 524288\n"
    "s_lshr_b32 s38, s52, 8\n"
    "s_xor_b32 s38, s38, s56\n"
    "s_and_b32 s38, s38, 0x2000\n"
    "s_or_b32 s52, s52, s38\n"
    "s_add_u32 s53, s53, 1048576\n"
    "s_lshr_b32 s38, s53, 8\n"
    "s_xor_b32 s38, s38, s56\n"
    "s_and_b32 s38, s38, 0x2000\n"
    "s_or_b32 s53, s53, s38\n"
    "s_mov_b32 s32, s62\n"
    "s_mov_b32 s68, s62\n"
    "s_xor_b32 s46, s46, 0x80000\n"
    "s_add_u32 s47, s46, 0x40000\n"
    "s_bfe_u32 s64, s36, 0x10001\n"
    "s_add_u32 s44, s44, s8\n"
    "s_add_u32 s59, s59, 128\n"
    "s_waitcnt vmcnt(2)\n"
    "s_waitcnt lgkmcnt(0)\n"
    "ds_read_b128 v[104:107], v7 offset:10240\n"
    "ds_read_b128 v[108:111], v7 offset:11264\n"
    "v_mov_b64 v[112:113], 0\n"
    "v_mov_b64 v[114:115], 0\n"
    "v_mov_b64 v[116:117], 0\n"
    "v_mov_b64 v[118:119], 0\n"
    "v_mfma_f32_32x32x8_bf16 v[48:63], v[112:113], v[112:113], 0\n"
    "v_lshrrev_b32 v40, 4, v0\n"
    "v_and_b32 v40, 0x30, v40\n"
    "v_lshl_add_u32 v40, v40, 4, v0\n"
    "v_mov_b32 v41, 158224\n"
    "s_add_u32 s73, s49, s11\n"
    "s_add_u32 s73, s73, 4096\n"
    "s_add_u32 m0, s11, 151552\n"
    "s_branch LBM%=\n"
    "s_waitcnt lgkmcnt(0)\n"
    "LPRE%=:\n"
    "s_nop 7\n"
    "ds_read_b128 v[64:67], v7 offset:0\n"
    "ds_read_b128 v[68:71], v7 offset:1024\n"
    "ds_read_b128 v[72:75], v7 offset:2048\n"
    "ds_read_b128 v[76:79], v7 offset:3072\n"
    "ds_read_b128 v[80:83], v7 offset:4096\n"
    "ds_read_b128 v[84:87], v7 offset:5120\n"
    "ds_read_b128 v[88:91], v7 offset:6144\n"
    "ds_read_b128 v[92:95], v7 offset:7168\n"
    "ds_read_b128 v[96:99], v7 offset:8192\n"
    "ds_read_b128 v[100:103], v7 offset:9216\n"
    "ds_read_b128 v[104:107], v7 offset:10240\n"
    "ds_read_b128 v[108:111], v7 offset:11264\n"
    "v_mov_b64 v[112:113], 0\n"
    "v_mov_b64 v[114:115], 0\n"
    "v_mov_b64 v[116:117], 0\n"
    "v_mov_b64 v[118:119], 0\n"
    "v_mfma_f32_32x32x8_bf16 v[48:63], v[112:113], v[112:113], 0\n"
    "v_lshrrev_b32 v40, 4, v0\n"
    "v_and_b32 v40, 0x30, v40\n"
    "v_lshl_add_u32 v40, v40, 4, v0\n"
    "v_mov_b32 v41, 158224\n"
    "s_add_u32 s73, s49, s11\n"
    "s_add_u32 s73, s73, 4096\n"
    "s_add_u32 m0, s11, 151552\n"
    "s_cmp_lg_u32 s61, 1\n"
    "s_cbranch_scc1 LBM%=\n"
    "s_branch LMN%=\n"
    "LREC%=:\n"
    "s_mov_b32 s1, s38\n"
    "s_andn2_b32 s39, 31, s70\n"
    "s_add_u32 s53, s50, s55\n"
    "s_mov_b32 s49, s48\n"
    "s_add_u32 s51, s53, 0\n"
    "s_lshr_b32 s38, s51, 8\n"
    "s_xor_b32 s38, s38, s56\n"
    "s_and_b32 s38, s38, 0x2000\n"
    "s_or_b32 s51, s51, s38\n"
    "s_add_u32 s52, s53, 524288\n"
    "s_lshr_b32 s38, s52, 8\n"
    "s_xor_b32 s38, s38, s56\n"
    "s_and_b32 s38, s38, 0x2000\n"
    "s_or_b32 s52, s52, s38\n"
    "s_add_u32 s53, s53, 1048576\n"
    "s_lshr_b32 s38, s53, 8\n"
    "s_xor_b32 s38, s38, s56\n"
    "s_and_b32 s38, s38, 0x2000\n"
    "s_or_b32 s53, s53, s38\n"
    "s_mov_b32 s32, 30464\n"
    "s_mov_b32 s68, 22272\n"
    "s_xor_b32 s46, s46, 0x80000\n"
    "s_add_u32 s47, s46, 0x40000\n"
    "s_bfe_u32 s64, s36, 0x10001\n"
    "s_add_u32 s44, s44, s13\n"
    "s_add_u32 s59, s59, 128\n"
    "s_lshl_b32 s49, s36, 14\n"
    "s_add_u32 s49, s49, s67\n"
    "s_andn2_b32 s70, s36, 7\n"
    "s_add_u32 s70, s70, s36\n"
    "s_lshl_b32 s70, s70, 10\n"
    "s_add_u32 s70, s70, s69\n"
    "s_nop 7\n"
    "s_waitcnt lgkmcnt(0)\n"
    "buffer_load_dwordx4 v[16:19], v0, s[16:19], s70 offen sc1\n"
    "s_barrier\n"
    "s_add_u32 m0, s62, 122880\n"
    "s_nop 0\n"
    "buffer_load_dwordx4 v0, s[16:19], s51 offen offset:0 sc1 lds\n"
    "buffer_load_dwordx4 v0, s[16:19], s51 offen offset:1024 sc1 lds\n"
    "s_add_u32 m0, s62, 131072\n"
    "s_nop 0\n"
    "buffer_load_dwordx4 v0, s[16:19], s52 offen offset:0 sc1 lds\n"
    "buffer_load_dwordx4 v0, s[16:19], s52 offen offset:1024 sc1 lds\n"
    "s_add_u32 m0, s62, 139264\n"
    "s_nop 0\n"
    "buffer_load_dwordx4 v0, s[16:19], s53 offen offset:0 sc1 lds\n"
    "buffer_load_dwordx4 v0, s[16:19], s53 offen offset:1024 sc1 lds\n"
    "ds_read_b128 v[64:67], v7 offset:0\n"
    "ds_read_b128 v[68:71], v7 offset:1024\n"
    "ds_read_b128 v[72:75], v7 offset:2048\n"
    "ds_read_b128 v[76:79], v7 offset:3072\n"
    "ds_read_b128 v[80:83], v7 offset:4096\n"
    "ds_read_b128 v[84:87], v7 offset:5120\n"
    "ds_read_b128 v[88:91], v7 offset:6144\n"
    "ds_read_b128 v[92:95], v7 offset:7168\n"
    "ds_read_b128 v[96:99], v7 offset:8192\n"
    "ds_read_b128 v[100:103], v7 offset:9216\n"
    "ds_read_b128 v[104:107], v7 offset:10240\n"
    "ds_read_b128 v[108:111], v7 offset:11264\n"
    "v_mov_b64 v[112:113], 0\n"
    "v_mov_b64 v[114:115], 0\n"
    "v_mov_b64 v[116:117], 0\n"
    "v_mov_b64 v[118:119], 0\n"
    "v_mfma_f32_32x32x8_bf16 v[48:63], v[112:113], v[112:113], 0\n"
    "v_mov_b32 v1, 0x4444\n"
    "s_mov_b32 vcc_lo, 0xaaaaaaaa\n"
    "s_mov_b32 vcc_hi, 0xaaaaaaaa\n"
    "s_mov_b64 exec, vcc\n"
    "v_mov_b32 v1, 0xeeee\n"
    "s_mov_b64 exec, -1\n"
    "v_lshrrev_b32 v112, 3, v0\n"
    "v_and_b32 v112, 0x58, v112\n"
    "v_add_u32 v112, s62, v112\n"
    "v_add_u32 v112, 147456, v112\n"
    "s_mov_b32 s37, 0x80000\n"
    "s_waitcnt vmcnt(6)\n"
    "LXW%=:\n"
    "v_max3_u32 v36, v16, v17, v18\n"
    "v_max_u32 v36, v36, v19\n"
    "v_cmp_eq_u32 vcc, -1, v36\n"
    "s_cbranch_vccz LXK%=\n"
    "s_sleep 2\n"
    "buffer_load_dwordx4 v[16:19], v0, s[16:19], s70 offen sc1\n"
    "s_waitcnt vmcnt(0)\n"
    "s_sub_u32 s37, s37, 1\n"
    "s_cbranch_scc0 LXW%=\n"
    "s_branch LFAIL%=\n"
    "LXK%=:\n"
    "v_lshlrev_b32 v20, 16, v16\n"
    "v_and_b32 v21, 0xffff0000, v16\n"
    "v_readfirstlane_b32 s40, v20\n"
    "v_lshlrev_b32 v22, 16, v17\n"
    "v_and_b32 v23, 0xffff0000, v17\n"
    "v_lshlrev_b32 v24, 16, v18\n"
    "v_and_b32 v25, 0xffff0000, v18\n"
    "v_lshlrev_b32 v26, 16, v19\n"
    "v_and_b32 v27, 0xffff0000, v19\n"
    "v_pk_add_f32 v[28:29], v[20:21], s[40:41] op_sel_hi:[1,0] neg_lo:[0,1] neg_hi:[0,1]\n"
    "v_pk_add_f32 v[30:31], v[22:23], s[40:41] op_sel_hi:[1,0] neg_lo:[0,1] neg_hi:[0,1]\n"
    "v_pk_add_f32 v[32:33], v[24:25], s[40:41] op_sel_hi:[1,0] neg_lo:[0,1] neg_hi:[0,1]\n"
    "v_pk_add_f32 v[34:35], v[26:27], s[40:41] op_sel_hi:[1,0] neg_lo:[0,1] neg_hi:[0,1]\n"
    "v_pk_add_f32 v[36:37], v[28:29], v[30:31]\n"
    "v_pk_mul_f32 v[38:39], v[28:29], v[28:29]\n"
    "v_pk_add_f32 v[40:41], v[32:33], v[34:35]\n"
    "v_pk_fma_f32 v[38:39], v[30:31], v[30:31], v[38:39]\n"
    "v_pk_add_f32 v[36:37], v[36:37], v[40:41]\n"
    "v_pk_fma_f32 v[38:39], v[32:33], v[32:33], v[38:39]\n"
    "v_add_f32 v36, v36, v37\n"
    "v_pk_fma_f32 v[38:39], v[34:35], v[34:35], v[38:39]\n"
    "v_mov_b32 v41, 0x3b000000\n"
    "v_add_f32 v37, v38, v39\n"
    "v_add_f32_dpp v36, v36, v36 row_ror:8 row_mask:0xf bank_mask:0xf\n"
    "s_nop 0\n"
    "v_add_f32_dpp v37, v37, v37 row_ror:8 row_mask:0xf bank_mask:0xf\n"
    "v_add_f32_dpp v36, v36, v36 row_ror:4 row_mask:0xf bank_mask:0xf\n"
    "s_nop 0\n"
    "v_add_f32_dpp v37, v37, v37 row_ror:4 row_mask:0xf bank_mask:0xf\n"
    "v_add_f32_dpp v36, v36, v36 row_ror:2 row_mask:0xf bank_mask:0xf\n"
    "s_nop 0\n"
    "v_add_f32_dpp v37, v37, v37 row_ror:2 row_mask:0xf bank_mask:0xf\n"
    "v_add_f32_dpp v36, v36, v36 row_ror:1 row_mask:0xf bank_mask:0xf\n"
    "s_nop 0\n"
    "v_add_f32_dpp v37, v37, v37 row_ror:1 row_mask:0xf bank_mask:0xf\n"
    "s_nop 1\n"
    "v_permlane16_swap_b32 v36, v37\n"
    "v_add_f32 v36, v36, v37\n"
    "v_mov_b32 v37, v36\n"
    "s_nop 1\n"
    "v_permlane32_swap_b32 v36, v37\n"
    "v_add_f32 v36, v36, v37\n"
    "s_nop 0\n"
    "v_readlane_b32 s42, v36, 0\n"
    "v_readlane_b32 s43, v36, 16\n"
    "s_nop 0\n"
    "v_mul_f32 v36, s42, v41\n"
    "v_mul_f32 v37, s43, v41\n"
    "v_fma_f32 v37, -v36, v36, v37\n"
    "v_add_f32 v40, s40, v36\n"
    "v_max_f32 v37, 0, v37\n"
    "v_cvt_pk_bf16_f32 v36, v40, v37\n"
    "v_and_b32 v37, 0xffff0000, v36\n"
    "v_add_f32 v37, 0x3727c5ac, v37\n"
    "v_cvt_pk_bf16_f32 v37, v37, v37\n"
    "v_lshlrev_b32 v37, 16, v37\n"
    "v_sqrt_f32 v38, v37\n"
    "v_lshlrev_b32 v36, 16, v36\n"
    "v_cvt_pk_bf16_f32 v38, v38, v38\n"
    "v_lshlrev_b32 v38, 16, v38\n"
    "v_rcp_f32 v38, v38\n"
    "v_pk_add_f32 v[28:29], v[20:21], v[36:37] op_sel_hi:[1,0] neg_lo:[0,1] neg_hi:[0,1]\n"
    "v_pk_add_f32 v[30:31], v[22:23], v[36:37] op_sel_hi:[1,0] neg_lo:[0,1] neg_hi:[0,1]\n"
    "v_pk_add_f32 v[32:33], v[24:25], v[36:37] op_sel_hi:[1,0] neg_lo:[0,1] neg_hi:[0,1]\n"
    "v_pk_add_f32 v[34:35], v[26:27], v[36:37] op_sel_hi:[1,0] neg_lo:[0,1] neg_hi:[0,1]\n"
    "v_pk_mul_f32 v[28:29], v[28:29], v[38:39] op_sel_hi:[1,0]\n"
    "v_pk_mul_f32 v[30:31], v[30:31], v[38:39] op_sel_hi:[1,0]\n"
    "v_pk_mul_f32 v[32:33], v[32:33], v[38:39] op_sel_hi:[1,0]\n"
    "v_pk_mul_f32 v[34:35], v[34:35], v[38:39] op_sel_hi:[1,0]\n"
    "v_cvt_pk_bf16_f32 v116, v28, v29\n"
    "v_cvt_pk_bf16_f32 v117, v30, v31\n"
    "v_cvt_pk_bf16_f32 v118, v32, v33\n"
    "v_cvt_pk_bf16_f32 v119, v34, v35\n"
    "ds_write_b128 v9, v[116:119] offset:24576\n"
    "ds_read_b64_tr_b16 v[16:17], v112 offset:0\n"
    "ds_read_b64_tr_b16 v[18:19], v112 offset:32\n"
    "s_waitcnt lgkmcnt(11)\n"
    "ds_read_b64_tr_b16 v[20:21], v112 offset:128\n"
    "ds_read_b64_tr_b16 v[22:23], v112 offset:160\n"
    "ds_read_b64_tr_b16 v[24:25], v112 offset:256\n"
    "ds_read_b64_tr_b16 v[26:27], v112 offset:288\n"
    "s_waitcnt lgkmcnt(11)\n"
    "ds_read_b64_tr_b16 v[28:29], v112 offset:384\n"
    "ds_read_b64_tr_b16 v[30:31], v112 offset:416\n"
    "ds_read_b64_tr_b16 v[32:33], v112 offset:512\n"
    "ds_read_b64_tr_b16 v[34:35], v112 offset:544\n"
    "s_waitcnt lgkmcnt(11)\n"
    "ds_read_b64_tr_b16 v[36:37], v112 offset:640\n"
    "ds_read_b64_tr_b16 v[38:39], v112 offset:672\n"
    "ds_read_b64_tr_b16 v[40:41], v112 offset:768\n"
    "ds_read_b64_tr_b16 v[42:43], v112 offset:800\n"
    "s_waitcnt lgkmcnt(11)\n"
    "ds_read_b64_tr_b16 v[44:45], v112 offset:896\n"
    "ds_read_b64_tr_b16 v[46:47], v112 offset:928\n"
    "s_waitcnt lgkmcnt(0)\n"
    "s_mov_b32 exec_lo, 0x3333cccc\n"
    "s_mov_b32 exec_hi, 0x3333cccc\n"
    "v_mov_b64 v[16:17], 0\n"
    "v_mov_b64 v[18:19], 0\n"
    "v_mov_b64 v[20:21], 0\n"
    "v_mov_b64 v[22:23], 0\n"
    "v_mov_b64 v[24:25], 0\n"
    "v_mov_b64 v[26:27], 0\n"
    "v_mov_b64 v[28:29], 0\n"
    "v_mov_b64 v[30:31], 0\n"
    "v_mov_b64 v[32:33], 0\n"
    "v_mov_b64 v[34:35], 0\n"
    "v_mov_b64 v[36:37], 0\n"
    "v_mov_b64 v[38:39], 0\n"
    "v_mov_b64 v[40:41], 0\n"
    "v_mov_b64 v[42:43], 0\n"
    "v_mov_b64 v[44:45], 0\n"
    "v_mov_b64 v[46:47], 0\n"
    "s_mov_b64 exec, -1\n"
    "v_mov_b64 v[112:113], 0\n"
    "v_mov_b64 v[114:115], 0\n"
    "v_mov_b64 v[116:117], 0\n"
    "v_mov_b64 v[118:119], 0\n"
    "v_smfmac_f32_16x16x64_bf16 v[48:51], v[16:19], v[64:71], v1\n"
    "v_smfmac_f32_16x16x64_bf16 v[52:55], v[16:19], v[72:79], v1\n"
    "v_smfmac_f32_16x16x64_bf16 v[48:51], v[20:23], v[80:87], v1\n"
    "v_smfmac_f32_16x16x64_bf16 v[52:55], v[20:23], v[88:95], v1\n"
    "v_smfmac_f32_16x16x64_bf16 v[48:51], v[24:27], v[96:103], v1\n"
    "ds_read_b128 v[64:67], v7 offset:12288\n"
    "v_smfmac_f32_16x16x64_bf16 v[52:55], v[24:27], v[104:111], v1\n"
    "ds_read_b128 v[68:71], v7 offset:13312\n"
    "v_smfmac_f32_16x16x64_bf16 v[48:51], v[28:31], a[0:7], v1\n"
    "ds_read_b128 v[72:75], v7 offset:14336\n"
    "v_smfmac_f32_16x16x64_bf16 v[52:55], v[28:31], a[8:15], v1\n"
    "ds_read_b128 v[76:79], v7 offset:15360\n"
    "v_smfmac_f32_16x16x64_bf16 v[48:51], v[32:35], a[16:23], v1\n"
    "ds_read_b128 v[80:83], v7 offset:16384\n"
    "v_smfmac_f32_16x16x64_bf16 v[52:55], v[32:35], a[24:31], v1\n"
    "ds_read_b128 v[84:87], v7 offset:17408\n"
    "v_smfmac_f32_16x16x64_bf16 v[48:51], v[36:39], a[32:39], v1\n"
    "ds_read_b128 v[88:91], v7 offset:18432\n"
    "v_smfmac_f32_16x16x64_bf16 v[52:55], v[36:39], a[40:47], v1\n"
    "ds_read_b128 v[92:95], v7 offset:19456\n"
    "v_smfmac_f32_16x16x64_bf16 v[48:51], v[40:43], a[48:55], v1\n"
    "v_smfmac_f32_16x16x64_bf16 v[52:55], v[40:43], a[56:63], v1\n"
    "v_smfmac_f32_16x16x64_bf16 v[48:51], v[44:47], a[64:71], v1\n"
    "v_smfmac_f32_16x16x64_bf16 v[52:55], v[44:47], a[72:79], v1\n"
    "v_smfmac_f32_16x16x64_bf16 v[112:115], v[16:19], a[176:183], v1\n"
    "v_smfmac_f32_16x16x64_bf16 v[116:119], v[16:19], a[184:191], v1\n"
    "v_smfmac_f32_16x16x64_bf16 v[112:115], v[20:23], a[192:199], v1\n"
    "v_smfmac_f32_16x16x64_bf16 v[116:119], v[20:23], a[200:207], v1\n"
    "v_smfmac_f32_16x16x64_bf16 v[112:115], v[24:27], a[208:215], v1\n"
    "v_smfmac_f32_16x16x64_bf16 v[116:119], v[24:27], a[216:223], v1\n"
    "v_smfmac_f32_16x16x64_bf16 v[112:115], v[28:31], a[224:231], v1\n"
    "v_smfmac_f32_16x16x64_bf16 v[116:119], v[28:31], a[232:239], v1\n"
    "v_smfmac_f32_16x16x64_bf16 v[112:115], v[32:35], a[240:247], v1\n"
    "v_smfmac_f32_16x16x64_bf16 v[116:119], v[32:35], a[248:255], v1\n"
    "v_smfmac_f32_16x16x64_bf16 v[112:115], v[36:39], v[120:127], v1\n"
    "v_smfmac_f32_16x16x64_bf16 v[116:119], v[36:39], v[128:135], v1\n"
    "v_smfmac_f32_16x16x64_bf16 v[112:115], v[40:43], v[136:143], v1\n"
    "v_smfmac_f32_16x16x64_bf16 v[116:119], v[40:43], v[144:151], v1\n"
    "v_smfmac_f32_16x16x64_bf16 v[112:115], v[44:47], v[152:159], v1\n"
    "v_smfmac_f32_16x16x64_bf16 v[116:119], v[44:47], v[160:167], v1\n"
    "s_waitcnt lgkmcnt(6)\n"
    "v_smfmac_f32_16x16x64_bf16 v[56:59], v[16:19], v[64:71], v1\n"
    "s_waitcnt lgkmcnt(4)\n"
    "v_smfmac_f32_16x16x64_bf16 v[60:63], v[16:19], v[72:79], v1\n"
    "s_waitcnt lgkmcnt(2)\n"
    "v_smfmac_f32_16x16x64_bf16 v[56:59], v[20:23], v[80:87], v1\n"
    "s_waitcnt lgkmcnt(0)\n"
    "v_smfmac_f32_16x16x64_bf16 v[60:63], v[20:23], v[88:95], v1\n"
    "v_smfmac_f32_16x16x64_bf16 v[56:59], v[24:27], a[80:87], v1\n"
    "v_smfmac_f32_16x16x64_bf16 v[60:63], v[24:27], a[88:95], v1\n"
    "v_smfmac_f32_16x16x64_bf16 v[56:59], v[28:31], a[96:103], v1\n"
    "v_smfmac_f32_16x16x64_bf16 v[60:63], v[28:31], a[104:111], v1\n"
    "v_smfmac_f32_16x16x64_bf16 v[56:59], v[32:35], a[112:119], v1\n"
    "v_smfmac_f32_16x16x64_bf16 v[60:63], v[32:35], a[120:127], v1\n"
    "v_smfmac_f32_16x16x64_bf16 v[56:59], v[36:39], a[128:135], v1\n"
    "v_smfmac_f32_16x16x64_bf16 v[60:63], v[36:39], a[136:143], v1\n"
    "v_smfmac_f32_16x16x64_bf16 v[56:59], v[40:43], a[144:151], v1\n"
    "v_smfmac_f32_16x16x64_bf16 v[60:63], v[40:43], a[152:159], v1\n"
    "v_smfmac_f32_16x16x64_bf16 v[56:59], v[44:47], a[160:167], v1\n"
    "v_smfmac_f32_16x16x64_bf16 v[60:63], v[44:47], a[168:175], v1\n"
    "v_lshrrev_b32 v104, 2, v0\n"
    "v_and_b32 v104, 12, v104\n"
    "v_lshlrev_b32 v105, 2, v0\n"
    "v_and_b32 v105, 0x300, v105\n"
    "v_or_b32 v104, v104, v105\n"
    "s_lshr_b32 s40, s39, 4\n"
    "s_lshl_b32 s41, s40, 12\n"
    "s_and_b32 s42, s39, 15\n"
    "s_lshl_b32 s42, s42, 4\n"
    "s_add_u32 s41, s41, s42\n"
    "s_lshr_b32 s42, s62, 1\n"
    "s_add_u32 s41, s41, s42\n"
    "s_add_u32 s41, s41, 122880\n"
    "v_add_u32 v104, s41, v104\n"
    "s_lshl_b32 s40, s40, 3\n"
    "s_and_b32 s41, s39, 3\n"
    "s_lshl_b32 s41, s41, 1\n"
    "s_add_u32 s40, s40, s41\n"
    "s_bfe_u32 s41, s39, 0x20002\n"
    "s_lshl_b32 s41, s41, 8\n"
    "s_add_u32 s40, s40, s41\n"
    "s_add_u32 s40, s40, s62\n"
    "s_add_u32 s40, s40, 131072\n"
    "v_and_b32 v105, 0xf0, v0\n"
    "v_add_u32 v105, s40, v105\n"
    "v_add_f32 v48, v48, v49\n"
    "v_add_f32 v50, v50, v51\n"
    "v_add_f32 v48, v48, v50\n"
    "v_add_f32 v52, v52, v53\n"
    "v_add_f32 v54, v54, v55\n"
    "v_add_f32 v52, v52, v54\n"
    "v_cvt_pk_bf16_f32 v48, v48, v52\n"
    "v_add_f32 v112, v112, v113\n"
    "v_add_f32 v114, v114, v115\n"
    "v_add_f32 v112, v112, v114\n"
    "v_add_f32 v116, v116, v117\n"
    "v_add_f32 v118, v118, v119\n"
    "v_add_f32 v116, v116, v118\n"
    "v_cvt_pk_bf16_f32 v112, v112, v116\n"
    "v_add_f32 v56, v56, v57\n"
    "v_add_f32 v58, v58, v59\n"
    "v_add_f32 v56, v56, v58\n"
    "v_add_f32 v60, v60, v61\n"
    "v_add_f32 v62, v62, v63\n"
    "v_add_f32 v60, v60, v62\n"
    "v_cvt_pk_bf16_f32 v56, v56, v60\n"
    "v_mov_b32 v1, 0x4444\n"
    "s_mov_b32 vcc_lo, 0xcccccccc\n"
    "s_mov_b32 vcc_hi, 0xcccccccc\n"
    "s_mov_b64 exec, vcc\n"
    "v_mov_b32 v1, 0xeeee\n"
    "s_mov_b64 exec, -1\n"
    "s_waitcnt vmcnt(0)\n"
    "s_barrier\n"
    "ds_write_b32 v104, v56\n"
    "ds_write_b32 v104, v48 offset:16384\n"
    "ds_write_b16 v105, v112\n"
    "ds_write_b16_d16_hi v105, v112 offset:1024\n"
    "s_waitcnt lgkmcnt(0)\n"
    "s_cmp_lg_u32 s61, 1\n"
    "s_cbranch_scc1 LPRE%=\n"
    "v_mov_b32 v112, 158220\n"
    "v_mov_b32 v113, s36\n"
    "v_mov_b32 v114, s44\n"
    "ds_write2_b32 v112, v113, v114 offset0:0 offset1:1\n"
    "s_waitcnt lgkmcnt(0)\n"
    "s_branch LPRE%=\n"
    "LEND%=:\n"
    "s_waitcnt vmcnt(1) lgkmcnt(0)\n"
    "s_barrier\n"
    "s_cmp_lg_u32 s61, 1\n"
    "s_cbranch_scc1 LE0%=\n"
    "buffer_store_dword v1, off, s[16:19], s58 sc1\n"
    "s_add_u32 s58, s58, 4\n"
    "s_bitset0_b32 s58, 31\n"
    "LE0%=:\n"
    "s_add_u32 s54, s50, s57\n"
    "s_lshr_b32 s38, s54, 8\n"
    "s_xor_b32 s38, s38, s74\n"
    "s_and_b32 s38, s38, 0x2000\n"
    "s_or_b32 s54, s54, s38\n"
    "s_cmp_lt_u32 s61, 2\n"
    "s_cbranch_scc1 LE1%=\n"
    "ds_read_b128 v[16:19], v14 offset:0\n"
    "v_add_u32 v118, 122880, v0\n"
    "ds_read_b128 v[32:35], v118 offset:0\n"
    "ds_read_b128 v[48:51], v118 offset:4096\n"
    "ds_read_b128 v[20:23], v14 offset:1024\n"
    "ds_read_b128 v[36:39], v118 offset:1024\n"
    "ds_read_b128 v[52:55], v118 offset:5120\n"
    "v_mov_b32 v111, 0xd0289000\n"
    "v_cndmask_b32 v80, v111, 0, s[80:81]\n"
    "v_cndmask_b32 v81, v111, 0, s[82:83]\n"
    "v_cndmask_b32 v82, v111, 0, s[84:85]\n"
    "v_cndmask_b32 v83, v111, 0, s[86:87]\n"
    "v_cndmask_b32 v84, v111, 0, s[88:89]\n"
    "v_cndmask_b32 v85, v111, 0, s[90:91]\n"
    "v_cndmask_b32 v86, v111, 0, s[92:93]\n"
    "v_cndmask_b32 v87, v111, 0, s[94:95]\n"
    "v_lshrrev_b32 v112, 1, v0\n"
    "v_add_u32 v112, 149760, v112\n"
    "ds_read_b128 v[24:27], v14 offset:2048\n"
    "ds_read_b128 v[40:43], v118 offset:2048\n"
    "ds_read_b128 v[56:59], v118 offset:6144\n"
    "s_waitcnt lgkmcnt(7)\n"
    "v_mfma_f32_16x16x32_bf16 v[80:83], v[32:35], v[16:19], v[80:83]\n"
    "ds_read_b128 v[28:31], v14 offset:3072\n"
    "ds_read_b128 v[44:47], v118 offset:3072\n"
    "s_waitcnt lgkmcnt(8)\n"
    "v_mfma_f32_16x16x32_bf16 v[84:87], v[48:51], v[16:19], v[84:87]\n"
    "ds_read_b128 v[60:63], v118 offset:7168\n"
    "s_waitcnt lgkmcnt(7)\n"
    "v_mov_b32 v114, s72\n"
    "v_mfma_f32_16x16x32_bf16 v[80:83], v[36:39], v[20:23], v[80:83]\n"
    "ds_read_b128 v[64:67], v6 offset:0\n"
    "ds_read_b128 v[68:71], v6 offset:64\n"
    "s_waitcnt lgkmcnt(8)\n"
    "v_mfma_f32_16x16x32_bf16 v[84:87], v[52:55], v[20:23], v[84:87]\n"
    "ds_read_b128 v[72:75], v6 offset:128\n"
    "s_waitcnt lgkmcnt(7)\n"
    "v_mov_b32 v105, 0\n"
    "v_mfma_f32_16x16x32_bf16 v[80:83], v[40:43], v[24:27], v[80:83]\n"
    "ds_read_b128 v[76:79], v6 offset:192\n"
    "ds_read_b64 v[92:93], v112 offset:0\n"
    "s_waitcnt lgkmcnt(8)\n"
    "v_mfma_f32_16x16x32_bf16 v[84:87], v[56:59], v[24:27], v[84:87]\n"
    "ds_read_b64 v[94:95], v112 offset:512\n"
    "s_waitcnt lgkmcnt(7)\n"
    "v_mov_b32 v106, 0\n"
    "v_mfma_f32_16x16x32_bf16 v[80:83], v[44:47], v[28:31], v[80:83]\n"
    "ds_read_b64 v[96:97], v112 offset:1024\n"
    "ds_read_b64 v[98:99], v112 offset:1536\n"
    "s_waitcnt lgkmcnt(8)\n"
    "v_mfma_f32_16x16x32_bf16 v[84:87], v[60:63], v[28:31], v[84:87]\n"
    "ds_write_b32 v114, v119 offset:24\n"
    "s_waitcnt lgkmcnt(8)\n"
    "v_mfma_f32_16x16x32_bf16 v[88:91], v[64:67], v[16:19], 0\n"
    "ds_read_b128 v[32:35], v118 offset:8192\n"
    "ds_read_b128 v[36:39], v118 offset:9216\n"
    "s_waitcnt lgkmcnt(9)\n"
    "v_mfma_f32_16x16x32_bf16 v[88:91], v[68:71], v[20:23], v[88:91]\n"
    "ds_read_b128 v[40:43], v118 offset:10240\n"
    "ds_read_b128 v[44:47], v118 offset:11264\n"
    "s_waitcnt lgkmcnt(10)\n"
    "v_mfma_f32_16x16x32_bf16 v[88:91], v[72:75], v[24:27], v[88:91]\n"
    "ds_read_b128 v[48:51], v118 offset:12288\n"
    "ds_read_b128 v[52:55], v118 offset:13312\n"
    "s_waitcnt lgkmcnt(11)\n"
    "v_max3_f32 v108, v80, v81, v82\n"
    "v_max3_f32 v109, v83, v84, v85\n"
    "v_mfma_f32_16x16x32_bf16 v[88:91], v[76:79], v[28:31], v[88:91]\n"
    "ds_read_b128 v[56:59], v118 offset:14336\n"
    "ds_read_b128 v[60:63], v118 offset:15360\n"
    "v_max3_f32 v108, v108, v86, v87\n"
    "v_max_f32 v108, v108, v109\n"
    "v_mov_b32 v109, v108\n"
    "s_nop 1\n"
    "v_permlane32_swap_b32 v108, v109\n"
    "v_max_f32 v108, v108, v109\n"
    "v_max3_f32 v108, v108, v88, v89\n"
    "v_max3_f32 v108, v108, v90, v91\n"
    "v_mov_b32 v109, v108\n"
    "s_nop 1\n"
    "v_permlane16_swap_b32 v108, v109\n"
    "v_max_f32 v108, v108, v109\n"
    "v_mul_f32 v108, 0xbe0293ee, v108\n"
    "v_pk_fma_f32 v[80:81], v[80:81], s[64:65], v[108:109] op_sel:[0,1,0] op_sel_hi:[1,1,0]\n"
    "v_pk_fma_f32 v[82:83], v[82:83], s[64:65], v[108:109] op_sel:[0,1,0] op_sel_hi:[1,1,0]\n"
    "v_pk_fma_f32 v[84:85], v[84:85], s[64:65], v[108:109] op_sel:[0,1,0] op_sel_hi:[1,1,0]\n"
    "v_pk_fma_f32 v[86:87], v[86:87], s[64:65], v[108:109] op_sel:[0,1,0] op_sel_hi:[1,1,0]\n"
    "v_pk_fma_f32 v[88:89], v[88:89], s[64:65], v[108:109] op_sel:[0,1,0] op_sel_hi:[1,1,0]\n"
    "v_exp_f32 v80, v80\n"
    "v_exp_f32 v81, v81\n"
    "v_exp_f32 v82, v82\n"
    "v_exp_f32 v83, v83\n"
    "v_exp_f32 v84, v84\n"
    "v_exp_f32 v85, v85\n"
    "v_exp_f32 v86, v86\n"
    "v_exp_f32 v87, v87\n"
    "v_exp_f32 v88, v88\n"
    "v_exp_f32 v89, v89\n"
    "v_pk_add_f32 v[108:109], v[80:81], v[82:83]\n"
    "v_pk_add_f32 v[110:111], v[84:85], v[86:87]\n"
    "v_pk_add_f32 v[108:109], v[108:109], v[88:89]\n"
    "v_pk_add_f32 v[108:109], v[108:109], v[110:111]\n"
    "v_add_f32 v108, v108, v109\n"
    "v_mov_b32 v109, v108\n"
    "s_nop 1\n"
    "v_permlane16_swap_b32 v108, v109\n"
    "v_add_f32 v108, v108, v109\n"
    "v_mov_b32 v109, v108\n"
    "s_nop 1\n"
    "v_permlane32_swap_b32 v108, v109\n"
    "v_add_f32 v108, v108, v109\n"
    "v_rcp_f32 v108, v108\n"
    "s_nop 0\n"
    "v_pk_mul_f32 v[80:81], v[80:81], v[108:109] op_sel_hi:[1,0]\n"
    "v_pk_mul_f32 v[82:83], v[82:83], v[108:109] op_sel_hi:[1,0]\n"
    "v_pk_mul_f32 v[84:85], v[84:85], v[108:109] op_sel_hi:[1,0]\n"
    "v_pk_mul_f32 v[86:87], v[86:87], v[108:109] op_sel_hi:[1,0]\n"
    "v_pk_mul_f32 v[88:89], v[88:89], v[108:109] op_sel_hi:[1,0]\n"
    "v_cvt_pk_bf16_f32 v100, v80, v81\n"
    "v_cvt_pk_bf16_f32 v101, v82, v83\n"
    "v_cvt_pk_bf16_f32 v102, v84, v85\n"
    "v_cvt_pk_bf16_f32 v103, v86, v87\n"
    "v_cvt_pk_bf16_f32 v104, v88, v89\n"
    "v_cvt_pk_bf16_f32 v107, v88, v89\n"
    "s_waitcnt lgkmcnt(7)\n"
    "v_mfma_f32_16x16x32_bf16 v[16:19], v[32:35], v[100:103], 0\n"
    "s_waitcnt lgkmcnt(6)\n"
    "v_mfma_f32_16x16x32_bf16 v[20:23], v[36:39], v[100:103], 0\n"
    "v_mfma_f32_16x16x16_bf16 v[16:19], v[92:93], v[104:105], v[16:19]\n"
    "v_mfma_f32_16x16x16_bf16 v[20:23], v[92:93], v[106:107], v[20:23]\n"
    "s_waitcnt lgkmcnt(5)\n"
    "v_mfma_f32_16x16x32_bf16 v[24:27], v[40:43], v[100:103], 0\n"
    "s_waitcnt lgkmcnt(4)\n"
    "v_mfma_f32_16x16x32_bf16 v[28:31], v[44:47], v[100:103], 0\n"
    "v_cvt_pk_bf16_f32 v112, v16, v17\n"
    "v_cvt_pk_bf16_f32 v113, v18, v19\n"
    "v_mfma_f32_16x16x16_bf16 v[24:27], v[94:95], v[104:105], v[24:27]\n"
    "v_cvt_pk_bf16_f32 v114, v20, v21\n"
    "v_cvt_pk_bf16_f32 v115, v22, v23\n"
    "v_mfma_f32_16x16x16_bf16 v[28:31], v[94:95], v[106:107], v[28:31]\n"
    "ds_write_b128 v14, v[112:115] offset:0\n"
    "s_waitcnt lgkmcnt(4)\n"
    "v_mfma_f32_16x16x32_bf16 v[16:19], v[48:51], v[100:103], 0\n"
    "s_waitcnt lgkmcnt(3)\n"
    "v_mfma_f32_16x16x32_bf16 v[20:23], v[52:55], v[100:103], 0\n"
    "v_cvt_pk_bf16_f32 v108, v24, v25\n"
    "v_cvt_pk_bf16_f32 v109, v26, v27\n"
    "v_mfma_f32_16x16x16_bf16 v[16:19], v[96:97], v[104:105], v[16:19]\n"
    "v_cvt_pk_bf16_f32 v110, v28, v29\n"
    "v_cvt_pk_bf16_f32 v111, v30, v31\n"
    "v_mfma_f32_16x16x16_bf16 v[20:23], v[96:97], v[106:107], v[20:23]\n"
    "ds_write_b128 v14, v[108:111] offset:1024\n"
    "s_waitcnt lgkmcnt(3)\n"
    "v_mfma_f32_16x16x32_bf16 v[24:27], v[56:59], v[100:103], 0\n"
    "s_waitcnt lgkmcnt(2)\n"
    "v_mfma_f32_16x16x32_bf16 v[28:31], v[60:63], v[100:103], 0\n"
    "v_cvt_pk_bf16_f32 v112, v16, v17\n"
    "v_cvt_pk_bf16_f32 v113, v18, v19\n"
    "v_mfma_f32_16x16x16_bf16 v[24:27], v[98:99], v[104:105], v[24:27]\n"
    "v_cvt_pk_bf16_f32 v114, v20, v21\n"
    "v_cvt_pk_bf16_f32 v115, v22, v23\n"
    "v_mfma_f32_16x16x16_bf16 v[28:31], v[98:99], v[106:107], v[28:31]\n"
    "ds_write_b128 v14, v[112:115] offset:2048\n"
    "s_nop 2\n"
    "v_cvt_pk_bf16_f32 v108, v24, v25\n"
    "v_cvt_pk_bf16_f32 v109, v26, v27\n"
    "s_nop 1\n"
    "v_cvt_pk_bf16_f32 v110, v28, v29\n"
    "v_cvt_pk_bf16_f32 v111, v30, v31\n"
    "ds_write_b128 v14, v[108:111] offset:3072\n"
    "s_waitcnt lgkmcnt(0)\n"
    "LE1%=:\n"
    "s_barrier\n"
    "ds_read_b128 v[32:35], v9 offset:16384\n"
    "ds_read_b128 v[36:39], v9 offset:17408\n"
    "v_lshrrev_b32 v40, 4, v0\n"
    "v_and_b32 v40, 0x30, v40\n"
    "v_lshl_add_u32 v40, v40, 4, v0\n"
    "s_waitcnt lgkmcnt(1)\n"
    "buffer_store_dwordx4 v[32:35], v40, s[16:19], s54 offen offset:0 sc1\n"
    "s_waitcnt lgkmcnt(0)\n"
    "buffer_store_dwordx4 v[36:39], v40, s[16:19], s54 offen offset:2048 sc1\n"
    "s_waitcnt vmcnt(0)\n"
    "s_barrier\n"
    "buffer_store_dword v1, off, s[16:19], s58 sc1\n"
    "s_endpgm\n"
    "LFAIL%=:\n"
    "buffer_store_dword v1, off, s[16:19], s35 sc1\n"
    "s_endpgm\n"
    :
    : "{v0}"(hv0), "{v1}"(hv1), "{v2}"(hv2), "{v3}"(hv3), "{v4}"(hv4), "{v5}"(hv5), "{v6}"(hv6), "{v7}"(hv7), "{v8}"(hv8), "{v9}"(hv9), "{v10}"(hv10), "{v11}"(hv11), "{v12}"(hv12), "{v13}"(hv13), "{v14}"(hv14), "{v15}"(hv15), "{v16}"(hv16), "{v17}"(hv17), "{v18}"(hv18), "{v19}"(hv19), "{v20}"(hv20)
    : "v255", "a255", "s101", "vcc", "scc", "memory");
  } else {
  asm volatile(
    "s_mov_b32 s33, 0\n"
    "s_nop 1\n"
    "v_mov_b32 v119, v20\n"
    "s_nop 0\n"
    "v_readlane_b32 s61, v119, 0\n"
    "v_readlane_b32 s46, v119, 1\n"
    "v_readlane_b32 s63, v119, 2\n"
    "v_readlane_b32 s48, v119, 3\n"
    "v_readlane_b32 s55, v119, 4\n"
    "v_readlane_b32 s56, v119, 5\n"
    "v_readlane_b32 s57, v119, 6\n"
    "v_readlane_b32 s74, v119, 7\n"
    "v_readlane_b32 s58, v119, 8\n"
    "v_readlane_b32 s59, v119, 9\n"
    "v_readlane_b32 s60, v119, 10\n"
    "v_readlane_b32 s67, v119, 11\n"
    "v_readlane_b32 s1, v119, 12\n"
    "v_readlane_b32 s69, v119, 13\n"
    "v_readlane_b32 s70, v119, 14\n"
    "v_readlane_b32 s71, v119, 15\n"
    "v_readlane_b32 s73, v119, 16\n"
    "v_readlane_b32 s45, v119, 17\n"
    "v_readlane_b32 s92, v119, 36\n"
    "v_readlane_b32 s93, v119, 37\n"
    "v_readlane_b32 s35, v119, 18\n"
    "v_readlane_b32 s75, v119, 19\n"
    "v_readlane_b32 s16, v119, 28\n"
    "v_readlane_b32 s17, v119, 29\n"
    "v_readlane_b32 s18, v119, 30\n"
    "v_readlane_b32 s8, v119, 31\n"
    "v_readlane_b32 s9, v119, 32\n"
    "v_readlane_b32 s10, v119, 33\n"
    "v_readlane_b32 s0, v119, 34\n"
    "v_readlane_b32 s12, v119, 35\n"
    "v_readlane_b32 s13, v119, 38\n"
    "v_readlane_b32 s21, v119, 39\n"
    "s_mov_b32 s19, 0x20000\n"
    "v_readlane_b32 s84, v119, 20\n"
    "v_readlane_b32 s85, v119, 21\n"
    "v_readlane_b32 s86, v119, 22\n"
    "v_readlane_b32 s87, v119, 23\n"
    "v_readlane_b32 s88, v119, 24\n"
    "v_readlane_b32 s89, v119, 25\n"
    "v_readlane_b32 s90, v119, 26\n"
    "v_readlane_b32 s91, v119, 27\n"
    "s_mov_b32 s76, 0xff00ff00\n"
    "s_mov_b32 s77, 0xff00ff00\n"
    "s_mov_b32 s78, 0x00ff00ff\n"
    "s_mov_b32 s79, 0x00ff00ff\n"
    "s_mov_b32 s65, 0x3e0293ee\n"
    "s_mov_b32 s66, 0xfffefffe\n"
    "s_lshl_b32 s62, s61, 11\n"
    "s_lshl_b32 s11, s61, 10\n"
    "s_mov_b32 s36, 0\n"
    "s_mov_b32 s64, 0\n"
    "s_mov_b32 s50, 0\n"
    "s_lshl_b32 s72, s61, 2\n"
    "s_add_u32 s72, s72, 158208\n"
    "s_add_u32 s47, s46, 0x40000\n"
    "s_and_b32 s20, s61, 1\n"
    "s_lshl_b32 s20, s20, 7\n"
    "s_mov_b32 s14, 0x3f803f80\n"
    "s_mov_b32 s15, 0x3f803f80\n"
    "s_mov_b32 s82, 0x400000\n"
    "s_mov_b32 s83, 0x20000\n"
    "v_mov_b32 v112, v16\n"
    "v_mov_b32 v113, v17\n"
    "v_mov_b32 v114, v18\n"
    "v_mov_b32 v115, v19\n"
    "v_mov_b32 v117, 0\n"
    "v_mov_b32 v118, 0\n"
    "s_cmp_eq_u32 s61, 1\n"
    "s_mov_b32 s80, s90\n"
    "s_mov_b32 s81, s91\n"
    "s_cbranch_scc1 LCG%=\n"
    "s_cmp_lg_u32 s61, 0\n"
    "s_cbranch_scc1 LCF0_%=\n"
    "s_mov_b32 s80, s90\n"
    "s_mov_b32 s81, s91\n"
    "s_mul_i32 s38, s61, 4160\n"
    "s_add_u32 m0, s38, 4160\n"
    "s_mov_b32 s39, 16384\n"
    "buffer_load_dwordx4 v113, s[80:83], s39 offen lds\n"
    "s_add_u32 m0, s38, 5200\n"
    "s_mov_b32 s39, 17408\n"
    "buffer_load_dwordx4 v113, s[80:83], s39 offen lds\n"
    "s_add_u32 m0, s38, 6240\n"
    "s_mov_b32 s39, 20480\n"
    "buffer_load_dwordx4 v113, s[80:83], s39 offen lds\n"
    "s_add_u32 m0, s38, 7280\n"
    "s_mov_b32 s39, 21504\n"
    "buffer_load_dwordx4 v113, s[80:83], s39 offen lds\n"
    "LCF0_%=:\n"
    "s_mov_b32 s80, s90\n"
    "s_mov_b32 s81, s91\n"
    "s_mul_i32 s38, s61, 4160\n"
    "s_add_u32 m0, s38, 0\n"
    "s_mov_b32 s39, 0\n"
    "buffer_load_dwordx4 v113, s[80:83], s39 offen lds\n"
    "s_add_u32 m0, s38, 1040\n"
    "s_mov_b32 s39, 1024\n"
    "buffer_load_dwordx4 v113, s[80:83], s39 offen lds\n"
    "s_add_u32 m0, s38, 2080\n"
    "s_mov_b32 s39, 4096\n"
    "buffer_load_dwordx4 v113, s[80:83], s39 offen lds\n"
    "s_add_u32 m0, s38, 3120\n"
    "s_mov_b32 s39, 5120\n"
    "buffer_load_dwordx4 v113, s[80:83], s39 offen lds\n"
    "s_cmp_lg_u32 s61, 0\n"
    "s_cbranch_scc1 LCF1_%=\n"
    "s_mul_i32 s38, s61, 4160\n"
    "s_add_u32 m0, s38, 22080\n"
    "s_mov_b32 s39, 18432\n"
    "buffer_load_dwordx4 v113, s[80:83], s39 offen lds\n"
    "s_add_u32 m0, s38, 23120\n"
    "s_mov_b32 s39, 19456\n"
    "buffer_load_dwordx4 v113, s[80:83], s39 offen lds\n"
    "s_add_u32 m0, s38, 24160\n"
    "s_mov_b32 s39, 22528\n"
    "buffer_load_dwordx4 v113, s[80:83], s39 offen lds\n"
    "s_add_u32 m0, s38, 25200\n"
    "s_mov_b32 s39, 23552\n"
    "buffer_load_dwordx4 v113, s[80:83], s39 offen lds\n"
    "LCF1_%=:\n"
    "s_mul_i32 s38, s61, 4160\n"
    "s_add_u32 m0, s38, 17920\n"
    "s_mov_b32 s39, 2048\n"
    "buffer_load_dwordx4 v113, s[80:83], s39 offen lds\n"
    "s_add_u32 m0, s38, 18960\n"
    "s_mov_b32 s39, 3072\n"
    "buffer_load_dwordx4 v113, s[80:83], s39 offen lds\n"
    "s_add_u32 m0, s38, 20000\n"
    "s_mov_b32 s39, 6144\n"
    "buffer_load_dwordx4 v113, s[80:83], s39 offen lds\n"
    "s_add_u32 m0, s38, 21040\n"
    "s_mov_b32 s39, 7168\n"
    "buffer_load_dwordx4 v113, s[80:83], s39 offen lds\n"
    "s_cmp_lg_u32 s61, 0\n"
    "s_cbranch_scc1 LCF2_%=\n"
    "s_mul_i32 s38, s61, 4160\n"
    "s_add_u32 m0, s38, 40000\n"
    "s_mov_b32 s39, 49152\n"
    "buffer_load_dwordx4 v113, s[80:83], s39 offen lds\n"
    "s_add_u32 m0, s38, 41040\n"
    "s_mov_b32 s39, 50176\n"
    "buffer_load_dwordx4 v113, s[80:83], s39 offen lds\n"
    "s_add_u32 m0, s38, 42080\n"
    "s_mov_b32 s39, 53248\n"
    "buffer_load_dwordx4 v113, s[80:83], s39 offen lds\n"
    "s_add_u32 m0, s38, 43120\n"
    "s_mov_b32 s39, 54272\n"
    "buffer_load_dwordx4 v113, s[80:83], s39 offen lds\n"
    "LCF2_%=:\n"
    "s_mul_i32 s38, s61, 4160\n"
    "s_add_u32 m0, s38, 35840\n"
    "s_mov_b32 s39, 32768\n"
    "buffer_load_dwordx4 v113, s[80:83], s39 offen lds\n"
    "s_add_u32 m0, s38, 36880\n"
    "s_mov_b32 s39, 33792\n"
    "buffer_load_dwordx4 v113, s[80:83], s39 offen lds\n"
    "s_add_u32 m0, s38, 37920\n"
    "s_mov_b32 s39, 36864\n"
    "buffer_load_dwordx4 v113, s[80:83], s39 offen lds\n"
    "s_add_u32 m0, s38, 38960\n"
    "s_mov_b32 s39, 37888\n"
    "buffer_load_dwordx4 v113, s[80:83], s39 offen lds\n"
    "s_cmp_lg_u32 s61, 0\n"
    "s_cbranch_scc1 LCF3_%=\n"
    "s_mul_i32 s38, s61, 4160\n"
    "s_add_u32 m0, s38, 57920\n"
    "s_mov_b32 s39, 51200\n"
    "buffer_load_dwordx4 v113, s[80:83], s39 offen lds\n"
    "s_add_u32 m0, s38, 58960\n"
    "s_mov_b32 s39, 52224\n"
    "buffer_load_dwordx4 v113, s[80:83], s39 offen lds\n"
    "s_add_u32 m0, s38, 60000\n"
    "s_mov_b32 s39, 55296\n"
    "buffer_load_dwordx4 v113, s[80:83], s39 offen lds\n"
    "s_add_u32 m0, s38, 61040\n"
    "s_mov_b32 s39, 56320\n"
    "buffer_load_dwordx4 v113, s[80:83], s39 offen lds\n"
    "LCF3_%=:\n"
    "s_mul_i32 s38, s61, 4160\n"
    "s_add_u32 m0, s38, 53760\n"
    "s_mov_b32 s39, 34816\n"
    "buffer_load_dwordx4 v113, s[80:83], s39 offen lds\n"
    "s_add_u32 m0, s38, 54800\n"
    "s_mov_b32 s39, 35840\n"
    "buffer_load_dwordx4 v113, s[80:83], s39 offen lds\n"
    "s_add_u32 m0, s38, 55840\n"
    "s_mov_b32 s39, 38912\n"
    "buffer_load_dwordx4 v113, s[80:83], s39 offen lds\n"
    "s_add_u32 m0, s38, 56880\n"
    "s_mov_b32 s39, 39936\n"
    "buffer_load_dwordx4 v113, s[80:83], s39 offen lds\n"
    "LCG%=:\n"
    "s_waitcnt vmcnt(12)\n"
    "s_barrier\n"
    "s_mul_i32 s38, s61, 4160\n"
    "s_add_u32 m0, s38, 71680\n"
    "s_mov_b32 s39, 65536\n"
    "buffer_load_dwordx4 v113, s[80:83], s39 offen lds\n"
    "s_add_u32 m0, s38, 72720\n"
    "s_mov_b32 s39, 66560\n"
    "buffer_load_dwordx4 v113, s[80:83], s39 offen lds\n"
    "s_add_u32 m0, s38, 73760\n"
    "s_mov_b32 s39, 69632\n"
    "buffer_load_dwordx4 v113, s[80:83], s39 offen lds\n"
    "s_add_u32 m0, s38, 74800\n"
    "s_mov_b32 s39, 70656\n"
    "buffer_load_dwordx4 v113, s[80:83], s39 offen lds\n"
    "v_add_u32 v116, 0, v115\n"
    "ds_read_b64_tr_b16 v[168:169], v116 offset:0\n"
    "ds_read_b64_tr_b16 v[184:185], v116 offset:8\n"
    "ds_read_b64_tr_b16 v[200:201], v116 offset:16\n"
    "ds_read_b64_tr_b16 v[216:217], v116 offset:24\n"
    "ds_read_b64_tr_b16 v[232:233], v116 offset:512\n"
    "ds_read_b64_tr_b16 v[248:249], v116 offset:520\n"
    "ds_read_b64_tr_b16 v[104:105], v116 offset:528\n"
    "ds_read_b64_tr_b16 v[32:33], v116 offset:536\n"
    "s_waitcnt lgkmcnt(0)\n"
    "s_waitcnt vmcnt(12)\n"
    "s_barrier\n"
    "s_mul_i32 s38, s61, 4160\n"
    "s_add_u32 m0, s38, 89600\n"
    "s_mov_b32 s39, 67584\n"
    "buffer_load_dwordx4 v113, s[80:83], s39 offen lds\n"
    "s_add_u32 m0, s38, 90640\n"
    "s_mov_b32 s39, 68608\n"
    "buffer_load_dwordx4 v113, s[80:83], s39 offen lds\n"
    "s_add_u32 m0, s38, 91680\n"
    "s_mov_b32 s39, 71680\n"
    "buffer_load_dwordx4 v113, s[80:83], s39 offen lds\n"
    "s_add_u32 m0, s38, 92720\n"
    "s_mov_b32 s39, 72704\n"
    "buffer_load_dwordx4 v113, s[80:83], s39 offen lds\n"
    "v_add_u32 v116, 17920, v115\n"
    "ds_read_b64_tr_b16 v[170:171], v116 offset:0\n"
    "ds_read_b64_tr_b16 v[186:187], v116 offset:8\n"
    "ds_read_b64_tr_b16 v[202:203], v116 offset:16\n"
    "ds_read_b64_tr_b16 v[218:219], v116 offset:24\n"
    "ds_read_b64_tr_b16 v[234:235], v116 offset:512\n"
    "ds_read_b64_tr_b16 v[250:251], v116 offset:520\n"
    "ds_read_b64_tr_b16 v[106:107], v116 offset:528\n"
    "ds_read_b64_tr_b16 v[34:35], v116 offset:536\n"
    "s_waitcnt lgkmcnt(0)\n"
    "s_waitcnt vmcnt(12)\n"
    "s_barrier\n"
    "s_mul_i32 s38, s61, 4160\n"
    "s_add_u32 m0, s38, 0\n"
    "s_mov_b32 s39, 98304\n"
    "buffer_load_dwordx4 v113, s[80:83], s39 offen lds\n"
    "s_add_u32 m0, s38, 1040\n"
    "s_mov_b32 s39, 99328\n"
    "buffer_load_dwordx4 v113, s[80:83], s39 offen lds\n"
    "s_add_u32 m0, s38, 2080\n"
    "s_mov_b32 s39, 102400\n"
    "buffer_load_dwordx4 v113, s[80:83], s39 offen lds\n"
    "s_add_u32 m0, s38, 3120\n"
    "s_mov_b32 s39, 103424\n"
    "buffer_load_dwordx4 v113, s[80:83], s39 offen lds\n"
    "v_add_u32 v116, 35840, v115\n"
    "ds_read_b64_tr_b16 v[172:173], v116 offset:0\n"
    "ds_read_b64_tr_b16 v[188:189], v116 offset:8\n"
    "ds_read_b64_tr_b16 v[204:205], v116 offset:16\n"
    "ds_read_b64_tr_b16 v[220:221], v116 offset:24\n"
    "ds_read_b64_tr_b16 v[236:237], v116 offset:512\n"
    "ds_read_b64_tr_b16 v[252:253], v116 offset:520\n"
    "ds_read_b64_tr_b16 v[108:109], v116 offset:528\n"
    "ds_read_b64_tr_b16 v[36:37], v116 offset:536\n"
    "s_waitcnt lgkmcnt(0)\n"
    "s_waitcnt vmcnt(12)\n"
    "s_barrier\n"
    "s_mul_i32 s38, s61, 4160\n"
    "s_add_u32 m0, s38, 17920\n"
    "s_mov_b32 s39, 100352\n"
    "buffer_load_dwordx4 v113, s[80:83], s39 offen lds\n"
    "s_add_u32 m0, s38, 18960\n"
    "s_mov_b32 s39, 101376\n"
    "buffer_load_dwordx4 v113, s[80:83], s39 offen lds\n"
    "s_add_u32 m0, s38, 20000\n"
    "s_mov_b32 s39, 104448\n"
    "buffer_load_dwordx4 v113, s[80:83], s39 offen lds\n"
    "s_add_u32 m0, s38, 21040\n"
    "s_mov_b32 s39, 105472\n"
    "buffer_load_dwordx4 v113, s[80:83], s39 offen lds\n"
    "v_add_u32 v116, 53760, v115\n"
    "ds_read_b64_tr_b16 v[174:175], v116 offset:0\n"
    "ds_read_b64_tr_b16 v[190:191], v116 offset:8\n"
    "ds_read_b64_tr_b16 v[206:207], v116 offset:16\n"
    "ds_read_b64_tr_b16 v[222:223], v116 offset:24\n"
    "ds_read_b64_tr_b16 v[238:239], v116 offset:512\n"
    "ds_read_b64_tr_b16 v[254:255], v116 offset:520\n"
    "ds_read_b64_tr_b16 v[110:111], v116 offset:528\n"
    "ds_read_b64_tr_b16 v[38:39], v116 offset:536\n"
    "s_waitcnt lgkmcnt(0)\n"
    "s_waitcnt vmcnt(12)\n"
    "s_barrier\n"
    "s_mov_b32 s80, s88\n"
    "s_mov_b32 s81, s89\n"
    "s_mul_i32 s38, s61, 4480\n"
    "s_add_u32 m0, s38, 35840\n"
    "s_mov_b32 s39, 0\n"
    "buffer_load_dwordx4 v112, s[80:83], s39 offen lds\n"
    "s_add_u32 m0, s38, 36928\n"
    "s_mov_b32 s39, 32768\n"
    "buffer_load_dwordx4 v112, s[80:83], s39 offen lds\n"
    "s_add_u32 m0, s38, 38016\n"
    "s_mov_b32 s39, 65536\n"
    "buffer_load_dwordx4 v112, s[80:83], s39 offen lds\n"
    "s_add_u32 m0, s38, 39104\n"
    "s_mov_b32 s39, 98304\n"
    "buffer_load_dwordx4 v112, s[80:83], s39 offen lds\n"
    "v_add_u32 v116, 71680, v115\n"
    "ds_read_b64_tr_b16 v[176:177], v116 offset:0\n"
    "ds_read_b64_tr_b16 v[192:193], v116 offset:8\n"
    "ds_read_b64_tr_b16 v[208:209], v116 offset:16\n"
    "ds_read_b64_tr_b16 v[224:225], v116 offset:24\n"
    "ds_read_b64_tr_b16 v[240:241], v116 offset:512\n"
    "ds_read_b64_tr_b16 v[96:97], v116 offset:520\n"
    "ds_read_b64_tr_b16 v[24:25], v116 offset:528\n"
    "ds_read_b64_tr_b16 v[40:41], v116 offset:536\n"
    "s_waitcnt lgkmcnt(0)\n"
    "s_waitcnt vmcnt(12)\n"
    "s_barrier\n"
    "s_mul_i32 s38, s61, 4480\n"
    "s_add_u32 m0, s38, 53760\n"
    "s_mov_b32 s39, 524288\n"
    "buffer_load_dwordx4 v112, s[80:83], s39 offen lds\n"
    "s_add_u32 m0, s38, 54848\n"
    "s_mov_b32 s39, 557056\n"
    "buffer_load_dwordx4 v112, s[80:83], s39 offen lds\n"
    "s_add_u32 m0, s38, 55936\n"
    "s_mov_b32 s39, 589824\n"
    "buffer_load_dwordx4 v112, s[80:83], s39 offen lds\n"
    "s_add_u32 m0, s38, 57024\n"
    "s_mov_b32 s39, 622592\n"
    "buffer_load_dwordx4 v112, s[80:83], s39 offen lds\n"
    "v_add_u32 v116, 89600, v115\n"
    "ds_read_b64_tr_b16 v[178:179], v116 offset:0\n"
    "ds_read_b64_tr_b16 v[194:195], v116 offset:8\n"
    "ds_read_b64_tr_b16 v[210:211], v116 offset:16\n"
    "ds_read_b64_tr_b16 v[226:227], v116 offset:24\n"
    "ds_read_b64_tr_b16 v[242:243], v116 offset:512\n"
    "ds_read_b64_tr_b16 v[98:99], v116 offset:520\n"
    "ds_read_b64_tr_b16 v[26:27], v116 offset:528\n"
    "ds_read_b64_tr_b16 v[42:43], v116 offset:536\n"
    "s_waitcnt lgkmcnt(0)\n"
    "s_waitcnt vmcnt(12)\n"
    "s_barrier\n"
    "s_mul_i32 s38, s61, 4480\n"
    "s_add_u32 m0, s38, 71680\n"
    "s_mov_b32 s39, 1048576\n"
    "buffer_load_dwordx4 v112, s[80:83], s39 offen lds\n"
    "s_add_u32 m0, s38, 72768\n"
    "s_mov_b32 s39, 1081344\n"
    "buffer_load_dwordx4 v112, s[80:83], s39 offen lds\n"
    "s_add_u32 m0, s38, 73856\n"
    "s_mov_b32 s39, 1114112\n"
    "buffer_load_dwordx4 v112, s[80:83], s39 offen lds\n"
    "s_add_u32 m0, s38, 74944\n"
    "s_mov_b32 s39, 1146880\n"
    "buffer_load_dwordx4 v112, s[80:83], s39 offen lds\n"
    "v_add_u32 v116, 0, v115\n"
    "ds_read_b64_tr_b16 v[180:181], v116 offset:0\n"
    "ds_read_b64_tr_b16 v[196:197], v116 offset:8\n"
    "ds_read_b64_tr_b16 v[212:213], v116 offset:16\n"
    "ds_read_b64_tr_b16 v[228:229], v116 offset:24\n"
    "ds_read_b64_tr_b16 v[244:245], v116 offset:512\n"
    "ds_read_b64_tr_b16 v[100:101], v116 offset:520\n"
    "ds_read_b64_tr_b16 v[28:29], v116 offset:528\n"
    "ds_read_b64_tr_b16 v[44:45], v116 offset:536\n"
    "s_waitcnt lgkmcnt(0)\n"
    "s_waitcnt vmcnt(12)\n"
    "s_barrier\n"
    "s_mul_i32 s38, s61, 4480\n"
    "s_add_u32 m0, s38, 89600\n"
    "s_mov_b32 s39, 1572864\n"
    "buffer_load_dwordx4 v112, s[80:83], s39 offen lds\n"
    "s_add_u32 m0, s38, 90688\n"
    "s_mov_b32 s39, 1605632\n"
    "buffer_load_dwordx4 v112, s[80:83], s39 offen lds\n"
    "s_add_u32 m0, s38, 91776\n"
    "s_mov_b32 s39, 1638400\n"
    "buffer_load_dwordx4 v112, s[80:83], s39 offen lds\n"
    "s_add_u32 m0, s38, 92864\n"
    "s_mov_b32 s39, 1671168\n"
    "buffer_load_dwordx4 v112, s[80:83], s39 offen lds\n"
    "v_add_u32 v116, 17920, v115\n"
    "ds_read_b64_tr_b16 v[182:183], v116 offset:0\n"
    "ds_read_b64_tr_b16 v[198:199], v116 offset:8\n"
    "ds_read_b64_tr_b16 v[214:215], v116 offset:16\n"
    "ds_read_b64_tr_b16 v[230:231], v116 offset:24\n"
    "ds_read_b64_tr_b16 v[246:247], v116 offset:512\n"
    "ds_read_b64_tr_b16 v[102:103], v116 offset:520\n"
    "ds_read_b64_tr_b16 v[30:31], v116 offset:528\n"
    "ds_read_b64_tr_b16 v[46:47], v116 offset:536\n"
    "ds_write_b128 v9, v[24:27] offset:0\n"
    "s_waitcnt lgkmcnt(2)\n"
    "ds_write_b128 v9, v[28:31] offset:1024\n"
    "ds_write_b128 v9, v[32:35] offset:8192\n"
    "ds_write_b128 v9, v[36:39] offset:9216\n"
    "ds_write_b128 v9, v[40:43] offset:16384\n"
    "s_waitcnt lgkmcnt(5)\n"
    "ds_write_b128 v9, v[44:47] offset:17408\n"
    "s_waitcnt lgkmcnt(0)\n"
    "s_waitcnt vmcnt(12)\n"
    "s_barrier\n"
    "s_mul_i32 s38, s61, 4480\n"
    "s_add_u32 m0, s38, 0\n"
    "s_mov_b32 s39, 2097152\n"
    "buffer_load_dwordx4 v112, s[80:83], s39 offen lds\n"
    "s_add_u32 m0, s38, 1088\n"
    "s_mov_b32 s39, 2129920\n"
    "buffer_load_dwordx4 v112, s[80:83], s39 offen lds\n"
    "s_add_u32 m0, s38, 2176\n"
    "s_mov_b32 s39, 2162688\n"
    "buffer_load_dwordx4 v112, s[80:83], s39 offen lds\n"
    "s_add_u32 m0, s38, 3264\n"
    "s_mov_b32 s39, 2195456\n"
    "buffer_load_dwordx4 v112, s[80:83], s39 offen lds\n"
    "v_add_u32 v116, 35840, v114\n"
    "ds_read_b64_tr_b16 a[176:177], v116 offset:0\n"
    "ds_read_b64_tr_b16 a[178:179], v116 offset:2176\n"
    "ds_read_b64_tr_b16 a[180:181], v116 offset:8960\n"
    "ds_read_b64_tr_b16 a[182:183], v116 offset:11136\n"
    "v_xor_b32 v116, 32, v116\n"
    "ds_read_b64_tr_b16 a[184:185], v116 offset:0\n"
    "ds_read_b64_tr_b16 a[186:187], v116 offset:2176\n"
    "ds_read_b64_tr_b16 a[188:189], v116 offset:8960\n"
    "ds_read_b64_tr_b16 a[190:191], v116 offset:11136\n"
    "s_waitcnt lgkmcnt(0)\n"
    "s_waitcnt vmcnt(12)\n"
    "s_barrier\n"
    "s_mul_i32 s38, s61, 4480\n"
    "s_add_u32 m0, s38, 17920\n"
    "s_mov_b32 s39, 2621440\n"
    "buffer_load_dwordx4 v112, s[80:83], s39 offen lds\n"
    "s_add_u32 m0, s38, 19008\n"
    "s_mov_b32 s39, 2654208\n"
    "buffer_load_dwordx4 v112, s[80:83], s39 offen lds\n"
    "s_add_u32 m0, s38, 20096\n"
    "s_mov_b32 s39, 2686976\n"
    "buffer_load_dwordx4 v112, s[80:83], s39 offen lds\n"
    "s_add_u32 m0, s38, 21184\n"
    "s_mov_b32 s39, 2719744\n"
    "buffer_load_dwordx4 v112, s[80:83], s39 offen lds\n"
    "v_add_u32 v116, 53760, v114\n"
    "ds_read_b64_tr_b16 a[192:193], v116 offset:0\n"
    "ds_read_b64_tr_b16 a[194:195], v116 offset:2176\n"
    "ds_read_b64_tr_b16 a[196:197], v116 offset:8960\n"
    "ds_read_b64_tr_b16 a[198:199], v116 offset:11136\n"
    "v_xor_b32 v116, 32, v116\n"
    "ds_read_b64_tr_b16 a[200:201], v116 offset:0\n"
    "ds_read_b64_tr_b16 a[202:203], v116 offset:2176\n"
    "ds_read_b64_tr_b16 a[204:205], v116 offset:8960\n"
    "ds_read_b64_tr_b16 a[206:207], v116 offset:11136\n"
    "s_waitcnt lgkmcnt(0)\n"
    "s_waitcnt vmcnt(12)\n"
    "s_barrier\n"
    "s_mul_i32 s38, s61, 4480\n"
    "s_add_u32 m0, s38, 35840\n"
    "s_mov_b32 s39, 3145728\n"
    "buffer_load_dwordx4 v112, s[80:83], s39 offen lds\n"
    "s_add_u32 m0, s38, 36928\n"
    "s_mov_b32 s39, 3178496\n"
    "buffer_load_dwordx4 v112, s[80:83], s39 offen lds\n"
    "s_add_u32 m0, s38, 38016\n"
    "s_mov_b32 s39, 3211264\n"
    "buffer_load_dwordx4 v112, s[80:83], s39 offen lds\n"
    "s_add_u32 m0, s38, 39104\n"
    "s_mov_b32 s39, 3244032\n"
    "buffer_load_dwordx4 v112, s[80:83], s39 offen lds\n"
    "v_add_u32 v116, 71680, v114\n"
    "ds_read_b64_tr_b16 a[208:209], v116 offset:0\n"
    "ds_read_b64_tr_b16 a[210:211], v116 offset:2176\n"
    "ds_read_b64_tr_b16 a[212:213], v116 offset:8960\n"
    "ds_read_b64_tr_b16 a[214:215], v116 offset:11136\n"
    "v_xor_b32 v116, 32, v116\n"
    "ds_read_b64_tr_b16 a[216:217], v116 offset:0\n"
    "ds_read_b64_tr_b16 a[218:219], v116 offset:2176\n"
    "ds_read_b64_tr_b16 a[220:221], v116 offset:8960\n"
    "ds_read_b64_tr_b16 a[222:223], v116 offset:11136\n"
    "s_waitcnt lgkmcnt(0)\n"
    "s_waitcnt vmcnt(12)\n"
    "s_barrier\n"
    "s_mul_i32 s38, s61, 4480\n"
    "s_add_u32 m0, s38, 53760\n"
    "s_mov_b32 s39, 3670016\n"
    "buffer_load_dwordx4 v112, s[80:83], s39 offen lds\n"
    "s_add_u32 m0, s38, 54848\n"
    "s_mov_b32 s39, 3702784\n"
    "buffer_load_dwordx4 v112, s[80:83], s39 offen lds\n"
    "s_add_u32 m0, s38, 55936\n"
    "s_mov_b32 s39, 3735552\n"
    "buffer_load_dwordx4 v112, s[80:83], s39 offen lds\n"
    "s_add_u32 m0, s38, 57024\n"
    "s_mov_b32 s39, 3768320\n"
    "buffer_load_dwordx4 v112, s[80:83], s39 offen lds\n"
    "v_add_u32 v116, 89600, v114\n"
    "ds_read_b64_tr_b16 a[224:225], v116 offset:0\n"
    "ds_read_b64_tr_b16 a[226:227], v116 offset:2176\n"
    "ds_read_b64_tr_b16 a[228:229], v116 offset:8960\n"
    "ds_read_b64_tr_b16 a[230:231], v116 offset:11136\n"
    "v_xor_b32 v116, 32, v116\n"
    "ds_read_b64_tr_b16 a[232:233], v116 offset:0\n"
    "ds_read_b64_tr_b16 a[234:235], v116 offset:2176\n"
    "ds_read_b64_tr_b16 a[236:237], v116 offset:8960\n"
    "ds_read_b64_tr_b16 a[238:239], v116 offset:11136\n"
    "s_waitcnt lgkmcnt(0)\n"
    "s_waitcnt vmcnt(12)\n"
    "s_barrier\n"
    "s_mov_b32 s80, s86\n"
    "s_mov_b32 s81, s87\n"
    "s_mul_i32 s38, s61, 4480\n"
    "s_add_u32 m0, s38, 71680\n"
    "s_mov_b32 s39, 0\n"
    "buffer_load_dwordx4 v112, s[80:83], s39 offen lds\n"
    "s_add_u32 m0, s38, 72768\n"
    "s_mov_b32 s39, 32768\n"
    "buffer_load_dwordx4 v112, s[80:83], s39 offen lds\n"
    "s_add_u32 m0, s38, 73856\n"
    "s_mov_b32 s39, 65536\n"
    "buffer_load_dwordx4 v112, s[80:83], s39 offen lds\n"
    "s_add_u32 m0, s38, 74944\n"
    "s_mov_b32 s39, 98304\n"
    "buffer_load_dwordx4 v112, s[80:83], s39 offen lds\n"
    "v_add_u32 v116, 0, v114\n"
    "ds_read_b64_tr_b16 a[240:241], v116 offset:0\n"
    "ds_read_b64_tr_b16 a[242:243], v116 offset:2176\n"
    "ds_read_b64_tr_b16 a[244:245], v116 offset:8960\n"
    "ds_read_b64_tr_b16 a[246:247], v116 offset:11136\n"
    "v_xor_b32 v116, 32, v116\n"
    "ds_read_b64_tr_b16 a[248:249], v116 offset:0\n"
    "ds_read_b64_tr_b16 a[250:251], v116 offset:2176\n"
    "ds_read_b64_tr_b16 a[252:253], v116 offset:8960\n"
    "ds_read_b64_tr_b16 a[254:255], v116 offset:11136\n"
    "s_waitcnt lgkmcnt(0)\n"
    "s_waitcnt vmcnt(12)\n"
    "s_barrier\n"
    "s_mul_i32 s38, s61, 4480\n"
    "s_add_u32 m0, s38, 89600\n"
    "s_mov_b32 s39, 524288\n"
    "buffer_load_dwordx4 v112, s[80:83], s39 offen lds\n"
    "s_add_u32 m0, s38, 90688\n"
    "s_mov_b32 s39, 557056\n"
    "buffer_load_dwordx4 v112, s[80:83], s39 offen lds\n"
    "s_add_u32 m0, s38, 91776\n"
    "s_mov_b32 s39, 589824\n"
    "buffer_load_dwordx4 v112, s[80:83], s39 offen lds\n"
    "s_add_u32 m0, s38, 92864\n"
    "s_mov_b32 s39, 622592\n"
    "buffer_load_dwordx4 v112, s[80:83], s39 offen lds\n"
    "v_add_u32 v116, 17920, v114\n"
    "ds_read_b64_tr_b16 v[120:121], v116 offset:0\n"
    "ds_read_b64_tr_b16 v[122:123], v116 offset:2176\n"
    "ds_read_b64_tr_b16 v[124:125], v116 offset:8960\n"
    "ds_read_b64_tr_b16 v[126:127], v116 offset:11136\n"
    "v_xor_b32 v116, 32, v116\n"
    "ds_read_b64_tr_b16 v[128:129], v116 offset:0\n"
    "ds_read_b64_tr_b16 v[130:131], v116 offset:2176\n"
    "ds_read_b64_tr_b16 v[132:133], v116 offset:8960\n"
    "ds_read_b64_tr_b16 v[134:135], v116 offset:11136\n"
    "s_waitcnt lgkmcnt(0)\n"
    "s_waitcnt vmcnt(12)\n"
    "s_barrier\n"
    "s_mul_i32 s38, s61, 4480\n"
    "s_add_u32 m0, s38, 0\n"
    "s_mov_b32 s39, 1048576\n"
    "buffer_load_dwordx4 v112, s[80:83], s39 offen lds\n"
    "s_add_u32 m0, s38, 1088\n"
    "s_mov_b32 s39, 1081344\n"
    "buffer_load_dwordx4 v112, s[80:83], s39 offen lds\n"
    "s_add_u32 m0, s38, 2176\n"
    "s_mov_b32 s39, 1114112\n"
    "buffer_load_dwordx4 v112, s[80:83], s39 offen lds\n"
    "s_add_u32 m0, s38, 3264\n"
    "s_mov_b32 s39, 1146880\n"
    "buffer_load_dwordx4 v112, s[80:83], s39 offen lds\n"
    "v_add_u32 v116, 35840, v114\n"
    "ds_read_b64_tr_b16 v[136:137], v116 offset:0\n"
    "ds_read_b64_tr_b16 v[138:139], v116 offset:2176\n"
    "ds_read_b64_tr_b16 v[140:141], v116 offset:8960\n"
    "ds_read_b64_tr_b16 v[142:143], v116 offset:11136\n"
    "v_xor_b32 v116, 32, v116\n"
    "ds_read_b64_tr_b16 v[144:145], v116 offset:0\n"
    "ds_read_b64_tr_b16 v[146:147], v116 offset:2176\n"
    "ds_read_b64_tr_b16 v[148:149], v116 offset:8960\n"
    "ds_read_b64_tr_b16 v[150:151], v116 offset:11136\n"
    "s_waitcnt lgkmcnt(0)\n"
    "s_waitcnt vmcnt(12)\n"
    "s_barrier\n"
    "s_mul_i32 s38, s61, 4480\n"
    "s_add_u32 m0, s38, 17920\n"
    "s_mov_b32 s39, 1572864\n"
    "buffer_load_dwordx4 v112, s[80:83], s39 offen lds\n"
    "s_add_u32 m0, s38, 19008\n"
    "s_mov_b32 s39, 1605632\n"
    "buffer_load_dwordx4 v112, s[80:83], s39 offen lds\n"
    "s_add_u32 m0, s38, 20096\n"
    "s_mov_b32 s39, 1638400\n"
    "buffer_load_dwordx4 v112, s[80:83], s39 offen lds\n"
    "s_add_u32 m0, s38, 21184\n"
    "s_mov_b32 s39, 1671168\n"
    "buffer_load_dwordx4 v112, s[80:83], s39 offen lds\n"
    "v_add_u32 v116, 53760, v114\n"
    "ds_read_b64_tr_b16 v[152:153], v116 offset:0\n"
    "ds_read_b64_tr_b16 v[154:155], v116 offset:2176\n"
    "ds_read_b64_tr_b16 v[156:157], v116 offset:8960\n"
    "ds_read_b64_tr_b16 v[158:159], v116 offset:11136\n"
    "v_xor_b32 v116, 32, v116\n"
    "ds_read_b64_tr_b16 v[160:161], v116 offset:0\n"
    "ds_read_b64_tr_b16 v[162:163], v116 offset:2176\n"
    "ds_read_b64_tr_b16 v[164:165], v116 offset:8960\n"
    "ds_read_b64_tr_b16 v[166:167], v116 offset:11136\n"
    "s_waitcnt lgkmcnt(0)\n"
    "s_waitcnt vmcnt(12)\n"
    "s_barrier\n"
    "s_mul_i32 s38, s61, 4480\n"
    "s_add_u32 m0, s38, 35840\n"
    "s_mov_b32 s39, 2097152\n"
    "buffer_load_dwordx4 v112, s[80:83], s39 offen lds\n"
    "s_add_u32 m0, s38, 36928\n"
    "s_mov_b32 s39, 2129920\n"
    "buffer_load_dwordx4 v112, s[80:83], s39 offen lds\n"
    "s_add_u32 m0, s38, 38016\n"
    "s_mov_b32 s39, 2162688\n"
    "buffer_load_dwordx4 v112, s[80:83], s39 offen lds\n"
    "s_add_u32 m0, s38, 39104\n"
    "s_mov_b32 s39, 2195456\n"
    "buffer_load_dwordx4 v112, s[80:83], s39 offen lds\n"
    "v_add_u32 v116, 71680, v114\n"
    "ds_read_b64_tr_b16 v[64:65], v116 offset:0\n"
    "ds_read_b64_tr_b16 v[66:67], v116 offset:2176\n"
    "ds_read_b64_tr_b16 v[68:69], v116 offset:8960\n"
    "ds_read_b64_tr_b16 v[70:71], v116 offset:11136\n"
    "v_xor_b32 v116, 32, v116\n"
    "ds_read_b64_tr_b16 v[72:73], v116 offset:0\n"
    "ds_read_b64_tr_b16 v[74:75], v116 offset:2176\n"
    "ds_read_b64_tr_b16 v[76:77], v116 offset:8960\n"
    "ds_read_b64_tr_b16 v[78:79], v116 offset:11136\n"
    "s_waitcnt lgkmcnt(0)\n"
    "s_waitcnt vmcnt(12)\n"
    "s_barrier\n"
    "s_mul_i32 s38, s61, 4480\n"
    "s_add_u32 m0, s38, 53760\n"
    "s_mov_b32 s39, 2621440\n"
    "buffer_load_dwordx4 v112, s[80:83], s39 offen lds\n"
    "s_add_u32 m0, s38, 54848\n"
    "s_mov_b32 s39, 2654208\n"
    "buffer_load_dwordx4 v112, s[80:83], s39 offen lds\n"
    "s_add_u32 m0, s38, 55936\n"
    "s_mov_b32 s39, 2686976\n"
    "buffer_load_dwordx4 v112, s[80:83], s39 offen lds\n"
    "s_add_u32 m0, s38, 57024\n"
    "s_mov_b32 s39, 2719744\n"
    "buffer_load_dwordx4 v112, s[80:83], s39 offen lds\n"
    "v_add_u32 v116, 89600, v114\n"
    "ds_read_b64_tr_b16 v[80:81], v116 offset:0\n"
    "ds_read_b64_tr_b16 v[82:83], v116 offset:2176\n"
    "ds_read_b64_tr_b16 v[84:85], v116 offset:8960\n"
    "ds_read_b64_tr_b16 v[86:87], v116 offset:11136\n"
    "v_xor_b32 v116, 32, v116\n"
    "ds_read_b64_tr_b16 v[88:89], v116 offset:0\n"
    "ds_read_b64_tr_b16 v[90:91], v116 offset:2176\n"
    "ds_read_b64_tr_b16 v[92:93], v116 offset:8960\n"
    "ds_read_b64_tr_b16 v[94:95], v116 offset:11136\n"
    "s_waitcnt lgkmcnt(0)\n"
    "s_waitcnt vmcnt(12)\n"
    "s_barrier\n"
    "s_mul_i32 s38, s61, 4480\n"
    "s_add_u32 m0, s38, 71680\n"
    "s_mov_b32 s39, 3145728\n"
    "buffer_load_dwordx4 v112, s[80:83], s39 offen lds\n"
    "s_add_u32 m0, s38, 72768\n"
    "s_mov_b32 s39, 3178496\n"
    "buffer_load_dwordx4 v112, s[80:83], s39 offen lds\n"
    "s_add_u32 m0, s38, 73856\n"
    "s_mov_b32 s39, 3211264\n"
    "buffer_load_dwordx4 v112, s[80:83], s39 offen lds\n"
    "s_add_u32 m0, s38, 74944\n"
    "s_mov_b32 s39, 3244032\n"
    "buffer_load_dwordx4 v112, s[80:83], s39 offen lds\n"
    "v_add_u32 v116, 0, v114\n"
    "ds_read_b64_tr_b16 a[80:81], v116 offset:0\n"
    "ds_read_b64_tr_b16 a[82:83], v116 offset:2176\n"
    "ds_read_b64_tr_b16 a[84:85], v116 offset:8960\n"
    "ds_read_b64_tr_b16 a[86:87], v116 offset:11136\n"
    "v_xor_b32 v116, 32, v116\n"
    "ds_read_b64_tr_b16 a[88:89], v116 offset:0\n"
    "ds_read_b64_tr_b16 a[90:91], v116 offset:2176\n"
    "ds_read_b64_tr_b16 a[92:93], v116 offset:8960\n"
    "ds_read_b64_tr_b16 a[94:95], v116 offset:11136\n"
    "s_waitcnt lgkmcnt(0)\n"
    "s_waitcnt vmcnt(12)\n"
    "s_barrier\n"
    "s_mul_i32 s38, s61, 4480\n"
    "s_add_u32 m0, s38, 89600\n"
    "s_mov_b32 s39, 3670016\n"
    "buffer_load_dwordx4 v112, s[80:83], s39 offen lds\n"
    "s_add_u32 m0, s38, 90688\n"
    "s_mov_b32 s39, 3702784\n"
    "buffer_load_dwordx4 v112, s[80:83], s39 offen lds\n"
    "s_add_u32 m0, s38, 91776\n"
    "s_mov_b32 s39, 3735552\n"
    "buffer_load_dwordx4 v112, s[80:83], s39 offen lds\n"
    "s_add_u32 m0, s38, 92864\n"
    "s_mov_b32 s39, 3768320\n"
    "buffer_load_dwordx4 v112, s[80:83], s39 offen lds\n"
    "v_add_u32 v116, 17920, v114\n"
    "ds_read_b64_tr_b16 a[96:97], v116 offset:0\n"
    "ds_read_b64_tr_b16 a[98:99], v116 offset:2176\n"
    "ds_read_b64_tr_b16 a[100:101], v116 offset:8960\n"
    "ds_read_b64_tr_b16 a[102:103], v116 offset:11136\n"
    "v_xor_b32 v116, 32, v116\n"
    "ds_read_b64_tr_b16 a[104:105], v116 offset:0\n"
    "ds_read_b64_tr_b16 a[106:107], v116 offset:2176\n"
    "ds_read_b64_tr_b16 a[108:109], v116 offset:8960\n"
    "ds_read_b64_tr_b16 a[110:111], v116 offset:11136\n"
    "s_waitcnt lgkmcnt(0)\n"
    "s_waitcnt vmcnt(12)\n"
    "s_barrier\n"
    "s_mov_b32 s80, s84\n"
    "s_mov_b32 s81, s85\n"
    "s_mul_i32 s38, s61, 4480\n"
    "s_add_u32 m0, s38, 0\n"
    "s_mov_b32 s39, 0\n"
    "buffer_load_dwordx4 v112, s[80:83], s39 offen lds\n"
    "s_add_u32 m0, s38, 1088\n"
    "s_mov_b32 s39, 32768\n"
    "buffer_load_dwordx4 v112, s[80:83], s39 offen lds\n"
    "s_add_u32 m0, s38, 2176\n"
    "s_mov_b32 s39, 65536\n"
    "buffer_load_dwordx4 v112, s[80:83], s39 offen lds\n"
    "s_add_u32 m0, s38, 3264\n"
    "s_mov_b32 s39, 98304\n"
    "buffer_load_dwordx4 v112, s[80:83], s39 offen lds\n"
    "v_add_u32 v116, 35840, v114\n"
    "ds_read_b64_tr_b16 a[112:113], v116 offset:0\n"
    "ds_read_b64_tr_b16 a[114:115], v116 offset:2176\n"
    "ds_read_b64_tr_b16 a[116:117], v116 offset:8960\n"
    "ds_read_b64_tr_b16 a[118:119], v116 offset:11136\n"
    "v_xor_b32 v116, 32, v116\n"
    "ds_read_b64_tr_b16 a[120:121], v116 offset:0\n"
    "ds_read_b64_tr_b16 a[122:123], v116 offset:2176\n"
    "ds_read_b64_tr_b16 a[124:125], v116 offset:8960\n"
    "ds_read_b64_tr_b16 a[126:127], v116 offset:11136\n"
    "s_waitcnt lgkmcnt(0)\n"
    "s_waitcnt vmcnt(12)\n"
    "s_barrier\n"
    "s_mul_i32 s38, s61, 4480\n"
    "s_add_u32 m0, s38, 17920\n"
    "s_mov_b32 s39, 524288\n"
    "buffer_load_dwordx4 v112, s[80:83], s39 offen lds\n"
    "s_add_u32 m0, s38, 19008\n"
    "s_mov_b32 s39, 557056\n"
    "buffer_load_dwordx4 v112, s[80:83], s39 offen lds\n"
    "s_add_u32 m0, s38, 20096\n"
    "s_mov_b32 s39, 589824\n"
    "buffer_load_dwordx4 v112, s[80:83], s39 offen lds\n"
    "s_add_u32 m0, s38, 21184\n"
    "s_mov_b32 s39, 622592\n"
    "buffer_load_dwordx4 v112, s[80:83], s39 offen lds\n"
    "v_add_u32 v116, 53760, v114\n"
    "ds_read_b64_tr_b16 a[128:129], v116 offset:0\n"
    "ds_read_b64_tr_b16 a[130:131], v116 offset:2176\n"
    "ds_read_b64_tr_b16 a[132:133], v116 offset:8960\n"
    "ds_read_b64_tr_b16 a[134:135], v116 offset:11136\n"
    "v_xor_b32 v116, 32, v116\n"
    "ds_read_b64_tr_b16 a[136:137], v116 offset:0\n"
    "ds_read_b64_tr_b16 a[138:139], v116 offset:2176\n"
    "ds_read_b64_tr_b16 a[140:141], v116 offset:8960\n"
    "ds_read_b64_tr_b16 a[142:143], v116 offset:11136\n"
    "s_waitcnt lgkmcnt(0)\n"
    "s_waitcnt vmcnt(12)\n"
    "s_barrier\n"
    "global_load_dword v117, v118, s[92:93] sc1\n"
    "s_mul_i32 s38, s61, 4480\n"
    "s_add_u32 m0, s38, 35840\n"
    "s_mov_b32 s39, 1048576\n"
    "buffer_load_dwordx4 v112, s[80:83], s39 offen lds\n"
    "s_add_u32 m0, s38, 36928\n"
    "s_mov_b32 s39, 1081344\n"
    "buffer_load_dwordx4 v112, s[80:83], s39 offen lds\n"
    "s_add_u32 m0, s38, 38016\n"
    "s_mov_b32 s39, 1114112\n"
    "buffer_load_dwordx4 v112, s[80:83], s39 offen lds\n"
    "s_add_u32 m0, s38, 39104\n"
    "s_mov_b32 s39, 1146880\n"
    "buffer_load_dwordx4 v112, s[80:83], s39 offen lds\n"
    "v_add_u32 v116, 71680, v114\n"
    "ds_read_b64_tr_b16 a[144:145], v116 offset:0\n"
    "ds_read_b64_tr_b16 a[146:147], v116 offset:2176\n"
    "ds_read_b64_tr_b16 a[148:149], v116 offset:8960\n"
    "ds_read_b64_tr_b16 a[150:151], v116 offset:11136\n"
    "v_xor_b32 v116, 32, v116\n"
    "ds_read_b64_tr_b16 a[152:153], v116 offset:0\n"
    "ds_read_b64_tr_b16 a[154:155], v116 offset:2176\n"
    "ds_read_b64_tr_b16 a[156:157], v116 offset:8960\n"
    "ds_read_b64_tr_b16 a[158:159], v116 offset:11136\n"
    "s_waitcnt lgkmcnt(0)\n"
    "s_waitcnt vmcnt(13)\n"
    "s_barrier\n"
    "s_mul_i32 s38, s61, 4480\n"
    "s_add_u32 m0, s38, 53760\n"
    "s_mov_b32 s39, 1572864\n"
    "buffer_load_dwordx4 v112, s[80:83], s39 offen lds\n"
    "s_add_u32 m0, s38, 54848\n"
    "s_mov_b32 s39, 1605632\n"
    "buffer_load_dwordx4 v112, s[80:83], s39 offen lds\n"
    "s_add_u32 m0, s38, 55936\n"
    "s_mov_b32 s39, 1638400\n"
    "buffer_load_dwordx4 v112, s[80:83], s39 offen lds\n"
    "s_add_u32 m0, s38, 57024\n"
    "s_mov_b32 s39, 1671168\n"
    "buffer_load_dwordx4 v112, s[80:83], s39 offen lds\n"
    "v_add_u32 v116, 89600, v114\n"
    "ds_read_b64_tr_b16 a[160:161], v116 offset:0\n"
    "ds_read_b64_tr_b16 a[162:163], v116 offset:2176\n"
    "ds_read_b64_tr_b16 a[164:165], v116 offset:8960\n"
    "ds_read_b64_tr_b16 a[166:167], v116 offset:11136\n"
    "v_xor_b32 v116, 32, v116\n"
    "ds_read_b64_tr_b16 a[168:169], v116 offset:0\n"
    "ds_read_b64_tr_b16 a[170:171], v116 offset:2176\n"
    "ds_read_b64_tr_b16 a[172:173], v116 offset:8960\n"
    "ds_read_b64_tr_b16 a[174:175], v116 offset:11136\n"
    "s_waitcnt lgkmcnt(0)\n"
    "s_waitcnt vmcnt(13)\n"
    "s_barrier\n"
    "s_mul_i32 s38, s61, 4480\n"
    "s_add_u32 m0, s38, 71680\n"
    "s_mov_b32 s39, 2097152\n"
    "buffer_load_dwordx4 v112, s[80:83], s39 offen lds\n"
    "s_add_u32 m0, s38, 72768\n"
    "s_mov_b32 s39, 2129920\n"
    "buffer_load_dwordx4 v112, s[80:83], s39 offen lds\n"
    "s_add_u32 m0, s38, 73856\n"
    "s_mov_b32 s39, 2162688\n"
    "buffer_load_dwordx4 v112, s[80:83], s39 offen lds\n"
    "s_add_u32 m0, s38, 74944\n"
    "s_mov_b32 s39, 2195456\n"
    "buffer_load_dwordx4 v112, s[80:83], s39 offen lds\n"
    "v_add_u32 v116, 0, v114\n"
    "ds_read_b64_tr_b16 v[16:17], v116 offset:0\n"
    "ds_read_b64_tr_b16 v[18:19], v116 offset:2176\n"
    "ds_read_b64_tr_b16 v[20:21], v116 offset:8960\n"
    "ds_read_b64_tr_b16 v[22:23], v116 offset:11136\n"
    "v_xor_b32 v116, 32, v116\n"
    "ds_read_b64_tr_b16 v[24:25], v116 offset:0\n"
    "ds_read_b64_tr_b16 v[26:27], v116 offset:2176\n"
    "ds_read_b64_tr_b16 v[28:29], v116 offset:8960\n"
    "ds_read_b64_tr_b16 v[30:31], v116 offset:11136\n"
    "s_waitcnt lgkmcnt(0)\n"
    "s_waitcnt vmcnt(13)\n"
    "s_barrier\n"
    "s_mul_i32 s38, s61, 4480\n"
    "s_add_u32 m0, s38, 89600\n"
    "s_mov_b32 s39, 2621440\n"
    "buffer_load_dwordx4 v112, s[80:83], s39 offen lds\n"
    "s_add_u32 m0, s38, 90688\n"
    "s_mov_b32 s39, 2654208\n"
    "buffer_load_dwordx4 v112, s[80:83], s39 offen lds\n"
    "s_add_u32 m0, s38, 91776\n"
    "s_mov_b32 s39, 2686976\n"
    "buffer_load_dwordx4 v112, s[80:83], s39 offen lds\n"
    "s_add_u32 m0, s38, 92864\n"
    "s_mov_b32 s39, 2719744\n"
    "buffer_load_dwordx4 v112, s[80:83], s39 offen lds\n"
    "v_add_u32 v116, 17920, v114\n"
    "ds_read_b64_tr_b16 v[32:33], v116 offset:0\n"
    "ds_read_b64_tr_b16 v[34:35], v116 offset:2176\n"
    "ds_read_b64_tr_b16 v[36:37], v116 offset:8960\n"
    "ds_read_b64_tr_b16 v[38:39], v116 offset:11136\n"
    "v_xor_b32 v116, 32, v116\n"
    "ds_read_b64_tr_b16 v[40:41], v116 offset:0\n"
    "ds_read_b64_tr_b16 v[42:43], v116 offset:2176\n"
    "ds_read_b64_tr_b16 v[44:45], v116 offset:8960\n"
    "ds_read_b64_tr_b16 v[46:47], v116 offset:11136\n"
    "s_waitcnt lgkmcnt(0)\n"
    "s_waitcnt vmcnt(12)\n"
    "s_barrier\n"
    "s_mul_i32 s38, s61, 4480\n"
    "s_add_u32 m0, s38, 0\n"
    "s_mov_b32 s39, 3145728\n"
    "buffer_load_dwordx4 v112, s[80:83], s39 offen lds\n"
    "s_add_u32 m0, s38, 1088\n"
    "s_mov_b32 s39, 3178496\n"
    "buffer_load_dwordx4 v112, s[80:83], s39 offen lds\n"
    "s_add_u32 m0, s38, 2176\n"
    "s_mov_b32 s39, 3211264\n"
    "buffer_load_dwordx4 v112, s[80:83], s39 offen lds\n"
    "s_add_u32 m0, s38, 3264\n"
    "s_mov_b32 s39, 3244032\n"
    "buffer_load_dwordx4 v112, s[80:83], s39 offen lds\n"
    "v_add_u32 v116, 35840, v114\n"
    "ds_read_b64_tr_b16 v[48:49], v116 offset:0\n"
    "ds_read_b64_tr_b16 v[50:51], v116 offset:2176\n"
    "ds_read_b64_tr_b16 v[52:53], v116 offset:8960\n"
    "ds_read_b64_tr_b16 v[54:55], v116 offset:11136\n"
    "v_xor_b32 v116, 32, v116\n"
    "ds_read_b64_tr_b16 v[56:57], v116 offset:0\n"
    "ds_read_b64_tr_b16 v[58:59], v116 offset:2176\n"
    "ds_read_b64_tr_b16 v[60:61], v116 offset:8960\n"
    "ds_read_b64_tr_b16 v[62:63], v116 offset:11136\n"
    "s_waitcnt lgkmcnt(0)\n"
    "s_waitcnt vmcnt(12)\n"
    "s_barrier\n"
    "s_mul_i32 s38, s61, 4480\n"
    "s_add_u32 m0, s38, 17920\n"
    "s_mov_b32 s39, 3670016\n"
    "buffer_load_dwordx4 v112, s[80:83], s39 offen lds\n"
    "s_add_u32 m0, s38, 19008\n"
    "s_mov_b32 s39, 3702784\n"
    "buffer_load_dwordx4 v112, s[80:83], s39 offen lds\n"
    "s_add_u32 m0, s38, 20096\n"
    "s_mov_b32 s39, 3735552\n"
    "buffer_load_dwordx4 v112, s[80:83], s39 offen lds\n"
    "s_add_u32 m0, s38, 21184\n"
    "s_mov_b32 s39, 3768320\n"
    "buffer_load_dwordx4 v112, s[80:83], s39 offen lds\n"
    "v_add_u32 v116, 53760, v114\n"
    "ds_read_b64_tr_b16 a[0:1], v116 offset:0\n"
    "ds_read_b64_tr_b16 a[2:3], v116 offset:2176\n"
    "ds_read_b64_tr_b16 a[4:5], v116 offset:8960\n"
    "ds_read_b64_tr_b16 a[6:7], v116 offset:11136\n"
    "v_xor_b32 v116, 32, v116\n"
    "ds_read_b64_tr_b16 a[8:9], v116 offset:0\n"
    "ds_read_b64_tr_b16 a[10:11], v116 offset:2176\n"
    "ds_read_b64_tr_b16 a[12:13], v116 offset:8960\n"
    "ds_read_b64_tr_b16 a[14:15], v116 offset:11136\n"
    "s_waitcnt lgkmcnt(0)\n"
    "s_waitcnt vmcnt(12)\n"
    "s_barrier\n"
    "v_add_u32 v116, 71680, v114\n"
    "ds_read_b64_tr_b16 a[16:17], v116 offset:0\n"
    "ds_read_b64_tr_b16 a[18:19], v116 offset:2176\n"
    "ds_read_b64_tr_b16 a[20:21], v116 offset:8960\n"
    "ds_read_b64_tr_b16 a[22:23], v116 offset:11136\n"
    "v_xor_b32 v116, 32, v116\n"
    "ds_read_b64_tr_b16 a[24:25], v116 offset:0\n"
    "ds_read_b64_tr_b16 a[26:27], v116 offset:2176\n"
    "ds_read_b64_tr_b16 a[28:29], v116 offset:8960\n"
    "ds_read_b64_tr_b16 a[30:31], v116 offset:11136\n"
    "s_waitcnt lgkmcnt(0)\n"
    "s_waitcnt vmcnt(8)\n"
    "s_barrier\n"
    "v_add_u32 v116, 89600, v114\n"
    "ds_read_b64_tr_b16 a[32:33], v116 offset:0\n"
    "ds_read_b64_tr_b16 a[34:35], v116 offset:2176\n"
    "ds_read_b64_tr_b16 a[36:37], v116 offset:8960\n"
    "ds_read_b64_tr_b16 a[38:39], v116 offset:11136\n"
    "v_xor_b32 v116, 32, v116\n"
    "ds_read_b64_tr_b16 a[40:41], v116 offset:0\n"
    "ds_read_b64_tr_b16 a[42:43], v116 offset:2176\n"
    "ds_read_b64_tr_b16 a[44:45], v116 offset:8960\n"
    "ds_read_b64_tr_b16 a[46:47], v116 offset:11136\n"
    "s_waitcnt lgkmcnt(0)\n"
    "s_mov_b32 s37, 0x80000\n"
    "LRY%=:\n"
    "v_readfirstlane_b32 s44, v117\n"
    "s_cmp_lg_u32 s44, 0\n"
    "s_cbranch_scc1 LRZ%=\n"
    "global_load_dword v117, v118, s[92:93] sc1\n"
    "s_waitcnt vmcnt(0)\n"
    "s_sleep 1\n"
    "s_sub_u32 s37, s37, 1\n"
    "s_cbranch_scc0 LRY%=\n"
    "s_branch LFAIL%=\n"
    "LRZ%=:\n"
    "s_lshl_b32 s38, s44, 13\n"
    "s_and_b32 s38, s38, 0x2000\n"
    "s_xor_b32 s46, s46, s38\n"
    "s_xor_b32 s47, s47, s38\n"
    "s_xor_b32 s48, s48, s38\n"
    "s_xor_b32 s56, s56, s38\n"
    "s_xor_b32 s74, s74, s38\n"
    "s_xor_b32 s58, s58, s38\n"
    "s_xor_b32 s59, s59, s38\n"
    "s_xor_b32 s67, s67, s38\n"
    "s_xor_b32 s69, s69, s38\n"
    "s_sub_u32 s58, s58, 4\n"
    "s_mov_b64 exec, 7\n"
    "buffer_load_dword v119, v0, s[16:19], s59 offen sc1\n"
    "s_mov_b64 exec, -1\n"
    "s_waitcnt vmcnt(5)\n"
    "s_barrier\n"
    "v_add_u32 v116, 0, v114\n"
    "ds_read_b64_tr_b16 a[48:49], v116 offset:0\n"
    "ds_read_b64_tr_b16 a[50:51], v116 offset:2176\n"
    "ds_read_b64_tr_b16 a[52:53], v116 offset:8960\n"
    "ds_read_b64_tr_b16 a[54:55], v116 offset:11136\n"
    "v_xor_b32 v116, 32, v116\n"
    "ds_read_b64_tr_b16 a[56:57], v116 offset:0\n"
    "ds_read_b64_tr_b16 a[58:59], v116 offset:2176\n"
    "ds_read_b64_tr_b16 a[60:61], v116 offset:8960\n"
    "ds_read_b64_tr_b16 a[62:63], v116 offset:11136\n"
    "s_waitcnt lgkmcnt(0)\n"
    "s_waitcnt vmcnt(1)\n"
    "s_barrier\n"
    "v_add_u32 v116, 17920, v114\n"
    "ds_read_b64_tr_b16 a[64:65], v116 offset:0\n"
    "ds_read_b64_tr_b16 a[66:67], v116 offset:2176\n"
    "ds_read_b64_tr_b16 a[68:69], v116 offset:8960\n"
    "ds_read_b64_tr_b16 a[70:71], v116 offset:11136\n"
    "v_xor_b32 v116, 32, v116\n"
    "ds_read_b64_tr_b16 a[72:73], v116 offset:0\n"
    "ds_read_b64_tr_b16 a[74:75], v116 offset:2176\n"
    "ds_read_b64_tr_b16 a[76:77], v116 offset:8960\n"
    "ds_read_b64_tr_b16 a[78:79], v116 offset:11136\n"
    "s_waitcnt lgkmcnt(0)\n"
    "s_barrier\n"
    "ds_write_b128 v7, v[16:19] offset:0\n"
    "ds_write_b128 v7, v[20:23] offset:1024\n"
    "ds_write_b128 v7, v[24:27] offset:2048\n"
    "ds_write_b128 v7, v[28:31] offset:3072\n"
    "ds_write_b128 v7, v[32:35] offset:4096\n"
    "ds_write_b128 v7, v[36:39] offset:5120\n"
    "ds_write_b128 v7, v[40:43] offset:6144\n"
    "ds_write_b128 v7, v[44:47] offset:7168\n"
    "ds_write_b128 v7, v[48:51] offset:8192\n"
    "ds_write_b128 v7, v[52:55] offset:9216\n"
    "ds_write_b128 v7, v[56:59] offset:10240\n"
    "ds_write_b128 v7, v[60:63] offset:11264\n"
    "ds_write_b128 v7, v[64:67] offset:12288\n"
    "ds_write_b128 v7, v[68:71] offset:13312\n"
    "ds_write_b128 v7, v[72:75] offset:14336\n"
    "s_waitcnt lgkmcnt(11)\n"
    "ds_write_b128 v7, v[76:79] offset:15360\n"
    "ds_write_b128 v7, v[80:83] offset:16384\n"
    "ds_write_b128 v7, v[84:87] offset:17408\n"
    "ds_write_b128 v7, v[88:91] offset:18432\n"
    "s_waitcnt lgkmcnt(11)\n"
    "ds_write_b128 v7, v[92:95] offset:19456\n"
    "ds_write_b128 v7, v[96:99] offset:20480\n"
    "ds_write_b128 v7, v[100:103] offset:21504\n"
    "ds_write_b128 v7, v[104:107] offset:22528\n"
    "s_waitcnt lgkmcnt(11)\n"
    "ds_write_b128 v7, v[108:111] offset:23552\n"
    "s_waitcnt lgkmcnt(0)\n"
    "ds_read_b128 v[16:19], v9 offset:0\n"
    "ds_read_b128 v[20:23], v9 offset:1024\n"
    "ds_read_b128 v[24:27], v9 offset:8192\n"
    "ds_read_b128 v[28:31], v9 offset:9216\n"
    "ds_read_b128 v[32:35], v9 offset:16384\n"
    "ds_read_b128 v[36:39], v9 offset:17408\n"
    "s_waitcnt lgkmcnt(5)\n"
    "ds_write_b128 v7, v[16:19] offset:24576\n"
    "s_waitcnt lgkmcnt(5)\n"
    "ds_write_b128 v7, v[20:23] offset:25600\n"
    "s_waitcnt lgkmcnt(5)\n"
    "ds_write_b128 v7, v[24:27] offset:26624\n"
    "s_waitcnt lgkmcnt(5)\n"
    "ds_write_b128 v7, v[28:31] offset:27648\n"
    "s_waitcnt lgkmcnt(5)\n"
    "ds_write_b128 v7, v[32:35] offset:28672\n"
    "s_waitcnt lgkmcnt(5)\n"
    "ds_write_b128 v7, v[36:39] offset:29696\n"
    "v_lshrrev_b32 v64, 4, v0\n"
    "v_lshrrev_b32 v65, 4, v64\n"
    "v_and_b32 v64, 15, v64\n"
    "v_lshlrev_b32 v65, 2, v65\n"
    "v_sub_u32 v64, v64, v65\n"
    "s_and_b32 s38, s61, 1\n"
    "s_lshl_b32 s38, s38, 4\n"
    "v_add_u32 v64, s38, v64\n"
    "v_cmp_le_i32 s[80:81], 0, v64\n"
    "v_cmp_le_i32 s[82:83], 1, v64\n"
    "v_cmp_le_i32 s[84:85], 2, v64\n"
    "v_cmp_le_i32 s[86:87], 3, v64\n"
    "v_cmp_le_i32 s[88:89], 16, v64\n"
    "v_cmp_le_i32 s[90:91], 17, v64\n"
    "v_cmp_le_i32 s[92:93], 18, v64\n"
    "v_cmp_le_i32 s[94:95], 19, v64\n"
    "v_mov_b32 v64, 158224\n"
    "v_mov_b64 v[66:67], 0\n"
    "ds_write_b64 v64, v[66:67] offset:16\n"
    "ds_write_b64 v64, v[66:67] offset:32\n"
    "ds_write_b64 v64, v[66:67] offset:40\n"
    "s_mov_b32 s54, 0x80000000\n"
    "s_waitcnt vmcnt(0)\n"
    "v_mov_b32 v116, v119\n"
    "v_cmp_ne_u32 vcc, 0, v116\n"
    "v_readfirstlane_b32 s70, v116\n"
    "s_nop 0\n"
    "s_andn2_b32 s38, 7, vcc_lo\n"
    "s_cbranch_scc0 LS0K%=\n"
    "s_mov_b32 s37, 0x80000\n"
    "LS0W%=:\n"
    "s_mov_b64 exec, 7\n"
    "buffer_load_dword v116, v0, s[16:19], s59 offen sc1\n"
    "s_mov_b64 exec, -1\n"
    "s_waitcnt vmcnt(0)\n"
    "v_cmp_ne_u32 vcc, 0, v116\n"
    "v_readfirstlane_b32 s70, v116\n"
    "s_nop 0\n"
    "s_andn2_b32 s38, 7, vcc_lo\n"
    "s_cbranch_scc0 LS0K%=\n"
    "s_sleep 2\n"
    "s_sub_u32 s37, s37, 1\n"
    "s_cbranch_scc0 LS0W%=\n"
    "s_branch LFAIL%=\n"
    "LS0K%=:\n"
    "s_add_u32 s53, s50, s55\n"
    "s_mov_b32 s49, s48\n"
    "s_add_u32 s51, s53, 0\n"
    "s_lshr_b32 s38, s51, 8\n"
    "s_xor_b32 s38, s38, s56\n"
    "s_and_b32 s38, s38, 0x2000\n"
    "s_or_b32 s51, s51, s38\n"
    "s_add_u32 s52, s53, 524288\n"
    "s_lshr_b32 s38, s52, 8\n"
    "s_xor_b32 s38, s38, s56\n"
    "s_and_b32 s38, s38, 0x2000\n"
    "s_or_b32 s52, s52, s38\n"
    "s_add_u32 s53, s53, 1048576\n"
    "s_lshr_b32 s38, s53, 8\n"
    "s_xor_b32 s38, s38, s56\n"
    "s_and_b32 s38, s38, 0x2000\n"
    "s_or_b32 s53, s53, s38\n"
    "s_add_u32 s59, s59, 128\n"
    "s_waitcnt lgkmcnt(0)\n"
    "s_barrier\n"
    "s_cmp_lg_u32 s61, 1\n"
    "s_cbranch_scc1 LPRE%=\n"
    "s_sub_u32 s51, s51, 2048\n"
    "s_mov_b32 m0, 122880\n"
    "s_nop 0\n"
    "buffer_load_dwordx4 v0, s[16:19], s51 offen offset:0 sc1 lds\n"
    "buffer_load_dwordx4 v0, s[16:19], s51 offen offset:1024 sc1 lds\n"
    "buffer_load_dwordx4 v0, s[16:19], s51 offen offset:2048 sc1 lds\n"
    "buffer_load_dwordx4 v0, s[16:19], s51 offen offset:3072 sc1 lds\n"
    "s_mov_b32 m0, 126976\n"
    "s_add_u32 s51, s51, 4096\n"
    "buffer_load_dwordx4 v0, s[16:19], s51 offen offset:0 sc1 lds\n"
    "buffer_load_dwordx4 v0, s[16:19], s51 offen offset:1024 sc1 lds\n"
    "buffer_load_dwordx4 v0, s[16:19], s51 offen offset:2048 sc1 lds\n"
    "buffer_load_dwordx4 v0, s[16:19], s51 offen offset:3072 sc1 lds\n"
    "s_memtime s[40:41]\n"
    "s_waitcnt lgkmcnt(0)\n"
    "s_sub_u32 s41, s40, s45\n"
    "s_sub_u32 s42, s13, 1\n"
    "s_and_b32 s41, s41, s42\n"
    "s_sub_u32 s44, s40, s41\n"
    "v_mov_b32 v112, 158220\n"
    "v_mov_b32 v113, s36\n"
    "v_mov_b32 v114, s44\n"
    "ds_write2_b32 v112, v113, v114 offset0:0 offset1:1\n"
    "s_waitcnt lgkmcnt(0)\n"
    "s_branch LPRE%=\n"
    "LBM%=:\n"
    "s_waitcnt lgkmcnt(0)\n"
    "s_barrier\n"
    "buffer_load_dwordx4 v0, s[16:19], s73 offen sc1 lds\n"
    "ds_read_b64 v[42:43], v41 offset:0\n"
    "ds_read_b128 v[32:35], v9 offset:16384\n"
    "ds_read_b128 v[36:39], v9 offset:17408\n"
    "s_waitcnt lgkmcnt(2)\n"
    "v_readfirstlane_b32 s39, v42\n"
    "v_readfirstlane_b32 s44, v43\n"
    "s_sub_u32 s38, s39, s44\n"
    "s_cmp_gt_i32 s38, 0\n"
    "s_cbranch_scc1 LSL%=\n"
    "s_add_u32 m0, s62, 131072\n"
    "s_nop 0\n"
    "buffer_load_dwordx4 v0, s[16:19], s52 offen offset:0 sc1 lds\n"
    "buffer_load_dwordx4 v0, s[16:19], s52 offen offset:1024 sc1 lds\n"
    "s_waitcnt lgkmcnt(0)\n"
    "s_cmp_lg_u32 s61, 1\n"
    "s_cbranch_scc1 LC1%=\n"
    "s_waitcnt vmcnt(3)\n"
    "LC1%=:\n"
    "s_barrier\n"
    "ds_read_b128 v[16:19], v2 offset:0\n"
    "ds_read_b128 v[20:23], v2 offset:1024\n"
    "ds_read_b128 v[24:27], v2 offset:2048\n"
    "s_add_u32 m0, s62, 139264\n"
    "s_nop 0\n"
    "buffer_load_dwordx4 v0, s[16:19], s53 offen offset:0 sc1 lds\n"
    "buffer_load_dwordx4 v0, s[16:19], s53 offen offset:1024 sc1 lds\n"
    "s_waitcnt lgkmcnt(2)\n"
    "v_smfmac_f32_16x16x64_bf16 v[48:51], v[16:19], v[64:71], v1\n"
    "ds_read_b128 v[28:31], v2 offset:3072\n"
    "v_smfmac_f32_16x16x64_bf16 v[52:55], v[16:19], v[72:79], v1\n"
    "s_waitcnt lgkmcnt(2)\n"
    "v_smfmac_f32_16x16x64_bf16 v[48:51], v[20:23], v[80:87], v1\n"
    "v_smfmac_f32_16x16x64_bf16 v[52:55], v[20:23], v[88:95], v1\n"
    "s_waitcnt lgkmcnt(1)\n"
    "v_smfmac_f32_16x16x64_bf16 v[48:51], v[24:27], v[96:103], v1\n"
    "ds_read_b128 v[64:67], v7 offset:12288\n"
    "v_smfmac_f32_16x16x64_bf16 v[52:55], v[24:27], v[104:111], v1\n"
    "ds_read_b128 v[68:71], v7 offset:13312\n"
    "s_waitcnt lgkmcnt(2)\n"
    "v_smfmac_f32_16x16x64_bf16 v[48:51], v[28:31], a[0:7], v1\n"
    "ds_read_b128 v[72:75], v7 offset:14336\n"
    "v_smfmac_f32_16x16x64_bf16 v[52:55], v[28:31], a[8:15], v1\n"
    "ds_read_b128 v[76:79], v7 offset:15360\n"
    "v_smfmac_f32_16x16x64_bf16 v[112:115], v[16:19], a[176:183], v1\n"
    "ds_read_b128 v[80:83], v7 offset:16384\n"
    "buffer_store_dwordx4 v[32:35], v40, s[16:19], s54 offen offset:0 sc1\n"
    "v_smfmac_f32_16x16x64_bf16 v[116:119], v[16:19], a[184:191], v1\n"
    "ds_read_b128 v[84:87], v7 offset:17408\n"
    "v_smfmac_f32_16x16x64_bf16 v[112:115], v[20:23], a[192:199], v1\n"
    "ds_read_b128 v[88:91], v7 offset:18432\n"
    "v_smfmac_f32_16x16x64_bf16 v[116:119], v[20:23], a[200:207], v1\n"
    "ds_read_b128 v[92:95], v7 offset:19456\n"
    "v_smfmac_f32_16x16x64_bf16 v[56:59], v[24:27], a[80:87], v1\n"
    "v_smfmac_f32_16x16x64_bf16 v[60:63], v[24:27], a[88:95], v1\n"
    "v_max3_u32 v111, v19, v23, v27\n"
    "buffer_store_dwordx4 v[36:39], v40, s[16:19], s54 offen offset:2048 sc1\n"
    "v_smfmac_f32_16x16x64_bf16 v[56:59], v[28:31], a[96:103], v1\n"
    "v_smfmac_f32_16x16x64_bf16 v[60:63], v[28:31], a[104:111], v1\n"
    "s_waitcnt vmcnt(6)\n"
    "s_barrier\n"
    "ds_read_b128 v[32:35], v2 offset:4096\n"
    "s_waitcnt lgkmcnt(7)\n"
    "v_smfmac_f32_16x16x64_bf16 v[56:59], v[16:19], v[64:71], v1\n"
    "ds_read_b128 v[36:39], v2 offset:5120\n"
    "s_waitcnt lgkmcnt(6)\n"
    "v_smfmac_f32_16x16x64_bf16 v[60:63], v[16:19], v[72:79], v1\n"
    "ds_read_b128 v[40:43], v2 offset:6144\n"
    "s_waitcnt lgkmcnt(5)\n"
    "v_smfmac_f32_16x16x64_bf16 v[56:59], v[20:23], v[80:87], v1\n"
    "ds_read_b128 v[44:47], v2 offset:7168\n"
    "s_waitcnt lgkmcnt(4)\n"
    "v_smfmac_f32_16x16x64_bf16 v[60:63], v[20:23], v[88:95], v1\n"
    "s_waitcnt lgkmcnt(3)\n"
    "v_smfmac_f32_16x16x64_bf16 v[48:51], v[32:35], a[16:23], v1\n"
    "v_max3_u32 v111, v111, v31, v35\n"
    "v_smfmac_f32_16x16x64_bf16 v[52:55], v[32:35], a[24:31], v1\n"
    "ds_read_b128 v[88:91], v9 offset:0\n"
    "s_waitcnt lgkmcnt(3)\n"
    "v_smfmac_f32_16x16x64_bf16 v[48:51], v[36:39], a[32:39], v1\n"
    "v_add_u32 v21, s20, v5\n"
    "v_smfmac_f32_16x16x64_bf16 v[52:55], v[36:39], a[40:47], v1\n"
    "ds_read_b128 v[92:95], v9 offset:1024\n"
    "s_waitcnt lgkmcnt(3)\n"
    "v_smfmac_f32_16x16x64_bf16 v[48:51], v[40:43], a[48:55], v1\n"
    "s_waitcnt lgkmcnt(2)\n"
    "v_max3_u32 v111, v111, v39, v43\n"
    "v_smfmac_f32_16x16x64_bf16 v[48:51], v[44:47], a[64:71], v1\n"
    "v_max_u32 v111, v111, v47\n"
    "v_smfmac_f32_16x16x64_bf16 v[52:55], v[40:43], a[56:63], v1\n"
    "v_cmp_eq_u32 vcc, -1, v111\n"
    "v_smfmac_f32_16x16x64_bf16 v[52:55], v[44:47], a[72:79], v1\n"
    "v_add_f32 v48, v48, v50\n"
    "v_smfmac_f32_16x16x64_bf16 v[56:59], v[32:35], a[112:119], v1\n"
    "v_add_f32 v49, v49, v51\n"
    "v_smfmac_f32_16x16x64_bf16 v[60:63], v[32:35], a[120:127], v1\n"
    "v_add_f32 v52, v52, v54\n"
    "v_add_f32 v53, v53, v55\n"
    "v_cvt_pk_bf16_f32 v48, v48, v52\n"
    "v_cvt_pk_bf16_f32 v49, v49, v53\n"
    "v_smfmac_f32_16x16x64_bf16 v[56:59], v[36:39], a[128:135], v1\n"
    "ds_write2_b32 v3, v48, v49 offset0:0 offset1:72\n"
    "v_smfmac_f32_16x16x64_bf16 v[60:63], v[36:39], a[136:143], v1\n"
    "s_cbranch_vccnz LMR%=\n"
    "v_smfmac_f32_16x16x64_bf16 v[56:59], v[40:43], a[144:151], v1\n"
    "v_mov_b32 v53, 0\n"
    "v_smfmac_f32_16x16x64_bf16 v[56:59], v[44:47], a[160:167], v1\n"
    "v_mov_b32 v54, 0\n"
    "v_smfmac_f32_16x16x64_bf16 v[60:63], v[40:43], a[152:159], v1\n"
    "s_waitcnt lgkmcnt(0)\n"
    "s_barrier\n"
    "ds_read_b128 v[80:83], v21 offset:0\n"
    "v_smfmac_f32_16x16x64_bf16 v[60:63], v[44:47], a[168:175], v1\n"
    "ds_read_b128 v[84:87], v21 offset:64\n"
    "v_add_f32 v56, v56, v58\n"
    "v_smfmac_f32_16x16x64_bf16 v[112:115], v[24:27], a[208:215], v1\n"
    "ds_read_b128 v[96:99], v5 offset:0\n"
    "v_add_f32 v57, v57, v59\n"
    "v_smfmac_f32_16x16x64_bf16 v[112:115], v[28:31], a[224:231], v1\n"
    "ds_read_b128 v[100:103], v5 offset:64\n"
    "s_waitcnt lgkmcnt(3)\n"
    "v_add_f32 v60, v60, v62\n"
    "v_mfma_f32_16x16x32_bf16 v[64:67], v[88:91], v[80:83], 0\n"
    "ds_read_b128 v[104:107], v5 offset:128\n"
    "s_waitcnt lgkmcnt(3)\n"
    "v_add_f32 v61, v61, v63\n"
    "v_cvt_pk_bf16_f32 v56, v56, v60\n"
    "v_mfma_f32_16x16x32_bf16 v[64:67], v[92:95], v[84:87], v[64:67]\n"
    "ds_read_b128 v[108:111], v5 offset:192\n"
    "v_cvt_pk_bf16_f32 v57, v57, v61\n"
    "v_mov_b64 v[50:51], 0\n"
    "v_smfmac_f32_16x16x64_bf16 v[112:115], v[32:35], a[240:247], v1\n"
    "ds_write2_b32 v4, v56, v57 offset0:0 offset1:72\n"
    "v_smfmac_f32_16x16x64_bf16 v[112:115], v[36:39], v[120:127], v1\n"
    "ds_write_b128 v8, v[64:67]\n"
    "v_smfmac_f32_16x16x64_bf16 v[112:115], v[40:43], v[136:143], v1\n"
    "v_mov_b64 v[60:61], s[14:15]\n"
    "v_smfmac_f32_16x16x64_bf16 v[112:115], v[44:47], v[152:159], v1\n"
    "v_mov_b64 v[62:63], s[14:15]\n"
    "v_smfmac_f32_16x16x64_bf16 v[116:119], v[24:27], a[216:223], v1\n"
    "v_smfmac_f32_16x16x64_bf16 v[116:119], v[28:31], a[232:239], v1\n"
    "s_waitcnt lgkmcnt(0)\n"
    "s_barrier\n"
    "ds_read_b128 v[64:67], v6 offset:0\n"
    "ds_read_b128 v[88:91], v15 offset:0\n"
    "v_smfmac_f32_16x16x64_bf16 v[116:119], v[32:35], a[248:255], v1\n"
    "ds_read_b128 v[92:95], v15 offset:512\n"
    "ds_read_b128 v[68:71], v6 offset:64\n"
    "v_smfmac_f32_16x16x64_bf16 v[116:119], v[36:39], v[128:135], v1\n"
    "ds_read_b128 v[72:75], v6 offset:128\n"
    "v_add_f32 v112, v112, v114\n"
    "v_smfmac_f32_16x16x64_bf16 v[116:119], v[40:43], v[144:151], v1\n"
    "ds_read_b128 v[76:79], v6 offset:192\n"
    "s_waitcnt lgkmcnt(5)\n"
    "v_add_f32 v113, v113, v115\n"
    "v_mfma_f32_16x16x32_bf16 v[16:19], v[64:67], v[96:99], 0\n"
    "s_waitcnt lgkmcnt(3)\n"
    "s_waitcnt lgkmcnt(2)\n"
    "v_add_f32 v88, v88, v92\n"
    "v_add_f32 v89, v89, v93\n"
    "v_mfma_f32_16x16x32_bf16 v[16:19], v[68:71], v[100:103], v[16:19]\n"
    "s_waitcnt lgkmcnt(1)\n"
    "v_add_f32 v90, v90, v94\n"
    "v_add_f32 v91, v91, v95\n"
    "v_mfma_f32_16x16x32_bf16 v[16:19], v[72:75], v[104:107], v[16:19]\n"
    "s_waitcnt lgkmcnt(0)\n"
    "v_max3_f32 v21, v88, v89, v90\n"
    "v_max_f32 v21, v21, v91\n"
    "v_mfma_f32_16x16x32_bf16 v[16:19], v[76:79], v[108:111], v[16:19]\n"
    "s_waitcnt vmcnt(4)\n"
    "ds_read_b128 v[80:83], v9 offset:8192\n"
    "v_max_f32_dpp v20, v21, v21 row_ror:8 row_mask:0xf bank_mask:0xf\n"
    "v_mov_b32 v21, v20\n"
    "v_smfmac_f32_16x16x64_bf16 v[116:119], v[44:47], v[160:167], v1\n"
    "ds_read_b128 v[84:87], v9 offset:9216\n"
    "v_permlane32_swap_b32 v20, v21\n"
    "v_max_f32 v20, v20, v21\n"
    "v_cvt_pk_bf16_f32 v112, v112, v113\n"
    "v_max3_f32 v21, v16, v17, v20\n"
    "v_max3_f32 v21, v18, v19, v21\n"
    "v_mov_b32 v22, v21\n"
    "s_nop 0\n"
    "v_add_f32 v116, v116, v118\n"
    "v_add_f32 v117, v117, v119\n"
    "v_permlane16_swap_b32 v21, v22\n"
    "v_max_f32 v21, v21, v22\n"
    "v_mul_f32 v21, 0xbe0293ee, v21\n"
    "v_fma_f32 v88, v88, s65, v21\n"
    "v_fma_f32 v89, v89, s65, v21\n"
    "v_fma_f32 v90, v90, s65, v21\n"
    "v_fma_f32 v91, v91, s65, v21\n"
    "v_fma_f32 v16, v16, s65, v21\n"
    "v_fma_f32 v17, v17, s65, v21\n"
    "v_exp_f32 v16, v16\n"
    "v_exp_f32 v17, v17\n"
    "v_exp_f32 v88, v88\n"
    "v_exp_f32 v89, v89\n"
    "v_cvt_pk_bf16_f32 v52, v16, v17\n"
    "v_cvt_pk_bf16_f32 v113, v116, v117\n"
    "v_cvt_pk_bf16_f32 v55, v16, v17\n"
    "v_cvt_pk_bf16_f32 v56, v88, v89\n"
    "v_mfma_f32_16x16x16_bf16 v[104:107], v[60:61], v[52:53], 0\n"
    "v_exp_f32 v90, v90\n"
    "v_mfma_f32_16x16x16_bf16 v[96:99], v[112:113], v[52:53], 0\n"
    "v_exp_f32 v91, v91\n"
    "v_mfma_f32_16x16x16_bf16 v[100:103], v[112:113], v[54:55], 0\n"
    "v_cvt_pk_bf16_f32 v57, v90, v91\n"
    "v_mov_b32_dpp v58, v56 row_shl:8 row_mask:0xf bank_mask:0xf bound_ctrl:1\n"
    "s_nop 0\n"
    "v_mov_b32_dpp v59, v57 row_shl:8 row_mask:0xf bank_mask:0xf bound_ctrl:1\n"
    "s_nop 1\n"
    "v_mfma_f32_16x16x32_bf16 v[104:107], v[60:63], v[56:59], v[104:107]\n"
    "ds_write_b64 v10, v[112:113]\n"
    "s_waitcnt lgkmcnt(2)\n"
    "v_mfma_f32_16x16x32_bf16 v[96:99], v[80:83], v[56:59], v[96:99]\n"
    "s_waitcnt lgkmcnt(1)\n"
    "v_mfma_f32_16x16x32_bf16 v[100:103], v[84:87], v[56:59], v[100:103]\n"
    "v_rcp_f32 v104, v104\n"
    "s_mov_b64 exec, s[78:79]\n"
    "s_nop 1\n"
    "v_pk_mul_f32 v[96:97], v[96:97], v[104:105] op_sel_hi:[1,0]\n"
    "v_pk_mul_f32 v[98:99], v[98:99], v[104:105] op_sel_hi:[1,0]\n"
    "v_cvt_pk_bf16_f32 v96, v96, v97\n"
    "v_cvt_pk_bf16_f32 v97, v98, v99\n"
    "ds_write_b64 v11, v[96:97] offset:0\n"
    "v_pk_mul_f32 v[100:101], v[100:101], v[104:105] op_sel_hi:[1,0]\n"
    "v_pk_mul_f32 v[102:103], v[102:103], v[104:105] op_sel_hi:[1,0]\n"
    "v_cvt_pk_bf16_f32 v98, v100, v101\n"
    "v_cvt_pk_bf16_f32 v99, v102, v103\n"
    "ds_write_b64 v11, v[98:99] offset:8\n"
    "s_mov_b64 exec, 7\n"
    "buffer_load_dword v116, v0, s[16:19], s59 offen sc1\n"
    "s_mov_b64 exec, -1\n"
    "v_mfma_f32_32x32x8_bf16 v[72:87], v[50:51], v[50:51], 0\n"
    "s_add_u32 s36, s36, 1\n"
    "s_cmp_eq_u32 s36, 64\n"
    "s_waitcnt lgkmcnt(0)\n"
    "s_waitcnt vmcnt(3)\n"
    "s_barrier\n"
    "ds_read_b128 v[104:107], v12 offset:0\n"
    "ds_read_b128 v[108:111], v12 offset:1024\n"
    "s_nop 4\n"
    "v_mfma_f32_32x32x8_bf16 v[48:63], v[72:73], v[72:73], 0\n"
    "s_cbranch_scc1 LEND%=\n"
    "s_waitcnt lgkmcnt(1)\n"
    "v_smfmac_f32_16x16x64_bf16 v[72:75], v[104:107], v[168:175], v1\n"
    "ds_read_b128 v[64:67], v7 offset:20480\n"
    "s_waitcnt lgkmcnt(1)\n"
    "v_smfmac_f32_16x16x64_bf16 v[72:75], v[108:111], v[176:183], v1\n"
    "ds_read_b128 v[68:71], v7 offset:21504\n"
    "v_smfmac_f32_16x16x64_bf16 v[76:79], v[104:107], v[184:191], v1\n"
    "ds_read_b128 v[16:19], v7 offset:22528\n"
    "v_smfmac_f32_16x16x64_bf16 v[76:79], v[108:111], v[192:199], v1\n"
    "ds_read_b128 v[20:23], v7 offset:23552\n"
    "v_add_f32 v72, v72, v74\n"
    "v_smfmac_f32_16x16x64_bf16 v[80:83], v[104:107], v[200:207], v1\n"
    "ds_read_b128 v[24:27], v7 offset:24576\n"
    "v_add_f32 v73, v73, v75\n"
    "v_smfmac_f32_16x16x64_bf16 v[80:83], v[108:111], v[208:215], v1\n"
    "ds_read_b128 v[28:31], v7 offset:25600\n"
    "v_add_f32 v76, v76, v78\n"
    "v_smfmac_f32_16x16x64_bf16 v[84:87], v[104:107], v[216:223], v1\n"
    "ds_read_b128 v[32:35], v7 offset:26624\n"
    "v_add_f32 v77, v77, v79\n"
    "v_smfmac_f32_16x16x64_bf16 v[84:87], v[108:111], v[224:231], v1\n"
    "ds_read_b128 v[36:39], v7 offset:27648\n"
    "v_cvt_pk_bf16_f32 v88, v72, v76\n"
    "v_cvt_pk_bf16_f32 v90, v73, v77\n"
    "v_smfmac_f32_16x16x64_bf16 v[48:51], v[104:107], v[232:239], v1\n"
    "ds_read_b128 v[40:43], v7 offset:28672\n"
    "v_add_f32 v80, v80, v82\n"
    "v_add_f32 v81, v81, v83\n"
    "v_smfmac_f32_16x16x64_bf16 v[48:51], v[108:111], v[240:247], v1\n"
    "ds_read_b128 v[44:47], v7 offset:29696\n"
    "v_add_f32 v85, v85, v87\n"
    "v_add_f32 v84, v84, v86\n"
    "v_cvt_pk_bf16_f32 v91, v81, v85\n"
    "v_cvt_pk_bf16_f32 v89, v80, v84\n"
    "v_and_or_b32 v91, v91, -2, s64\n"
    "buffer_store_dwordx4 v[88:91], v13, s[16:19], s46 offen sc1\n"
    "v_smfmac_f32_16x16x64_bf16 v[52:55], v[104:107], v[248:255], v1\n"
    "s_waitcnt lgkmcnt(8)\n"
    "v_smfmac_f32_16x16x64_bf16 v[52:55], v[108:111], v[64:71], v1\n"
    "s_waitcnt lgkmcnt(6)\n"
    "v_add_f32 v48, v48, v50\n"
    "v_smfmac_f32_16x16x64_bf16 v[56:59], v[104:107], v[16:23], v1\n"
    "s_waitcnt lgkmcnt(4)\n"
    "v_add_f32 v49, v49, v51\n"
    "v_smfmac_f32_16x16x64_bf16 v[56:59], v[108:111], v[24:31], v1\n"
    "s_waitcnt lgkmcnt(2)\n"
    "v_add_f32 v52, v52, v54\n"
    "v_add_f32 v53, v53, v55\n"
    "v_smfmac_f32_16x16x64_bf16 v[60:63], v[104:107], v[32:39], v1\n"
    "s_waitcnt lgkmcnt(0)\n"
    "v_cvt_pk_bf16_f32 v92, v48, v52\n"
    "v_cvt_pk_bf16_f32 v94, v49, v53\n"
    "v_smfmac_f32_16x16x64_bf16 v[60:63], v[108:111], v[40:47], v1\n"
    "v_add_f32 v56, v56, v58\n"
    "v_add_f32 v57, v57, v59\n"
    "s_nop 5\n"
    "v_add_f32 v61, v61, v63\n"
    "v_add_f32 v60, v60, v62\n"
    "v_cvt_pk_bf16_f32 v95, v57, v61\n"
    "v_cvt_pk_bf16_f32 v93, v56, v60\n"
    "v_and_or_b32 v95, v95, -2, s64\n"
    "buffer_store_dwordx4 v[92:95], v13, s[16:19], s47 offen sc1\n"
    "s_waitcnt vmcnt(3)\n"
    "v_mov_b32 v118, s72\n"
    "v_mov_b32 v119, s36\n"
    "ds_write_b32 v118, v119 offset:48\n"
    "s_add_u32 s54, s50, s57\n"
    "s_lshr_b32 s38, s54, 8\n"
    "s_xor_b32 s38, s38, s74\n"
    "s_and_b32 s38, s38, 0x2000\n"
    "s_or_b32 s54, s54, s38\n"
    "s_add_u32 s50, s50, 2621440\n"
    "s_add_u32 s48, s48, 16384\n"
    "s_cmp_eq_u32 s61, 1\n"
    "s_cbranch_scc1 LW1%=\n"
    "s_cmp_gt_u32 s61, 1\n"
    "s_cbranch_scc1 LW2%=\n"
    "s_add_u32 s73, s46, s63\n"
    "s_add_u32 s66, s73, 4096\n"
    "s_add_u32 s39, s44, s9\n"
    "s_waitcnt lgkmcnt(0)\n"
    "LUG%=:\n"
    "s_memtime s[40:41]\n"
    "s_waitcnt lgkmcnt(0)\n"
    "s_sub_u32 s40, s39, s40\n"
    "s_sub_u32 s41, s40, 253\n"
    "s_cmp_lt_u32 s41, 16384\n"
    "s_cbranch_scc1 LUG%=\n"
    "s_cmp_lt_u32 s40, 253\n"
    "s_cselect_b32 s40, s40, 0\n"
    "s_and_b32 s40, s40, 0xfc\n"
    "s_getpc_b64 s[42:43]\n"
    "s_sub_u32 s40, 272, s40\n"
    "s_add_u32 s42, s42, s40\n"
    "s_addc_u32 s43, s43, 0\n"
    "s_setpc_b64 s[42:43]\n"
    ".rept 63\n"
    "s_nop 0\n"
    ".endr\n"
    "buffer_load_dwordx4 v[16:19], v0, s[16:19], s73 offen offset:0 sc1\n"
    "buffer_load_dwordx4 v[20:23], v0, s[16:19], s73 offen offset:1024 sc1\n"
    "buffer_load_dwordx4 v[24:27], v0, s[16:19], s73 offen offset:2048 sc1\n"
    "buffer_load_dwordx4 v[28:31], v0, s[16:19], s73 offen offset:3072 sc1\n"
    "buffer_load_dwordx4 v[32:35], v0, s[16:19], s66 offen offset:0 sc1\n"
    "buffer_load_dwordx4 v[36:39], v0, s[16:19], s66 offen offset:1024 sc1\n"
    "buffer_load_dwordx4 v[40:43], v0, s[16:19], s66 offen offset:2048 sc1\n"
    "buffer_load_dwordx4 v[44:47], v0, s[16:19], s66 offen offset:3072 sc1\n"
    "ds_read_b128 v[48:51], v9 offset:35584\n"
    "ds_read_b128 v[64:67], v7 offset:0\n"
    "ds_read_b128 v[68:71], v7 offset:1024\n"
    "ds_read_b128 v[72:75], v7 offset:2048\n"
    "ds_read_b128 v[76:79], v7 offset:3072\n"
    "ds_read_b128 v[80:83], v7 offset:4096\n"
    "ds_read_b128 v[84:87], v7 offset:5120\n"
    "ds_read_b128 v[88:91], v7 offset:6144\n"
    "ds_read_b128 v[92:95], v7 offset:7168\n"
    "ds_read_b128 v[96:99], v7 offset:8192\n"
    "ds_read_b128 v[100:103], v7 offset:9216\n"
    "ds_read_b128 v[104:107], v7 offset:10240\n"
    "ds_read_b128 v[108:111], v7 offset:11264\n"
    "s_add_u32 s53, s50, s55\n"
    "s_mov_b32 s49, s48\n"
    "s_add_u32 s51, s53, 0\n"
    "s_lshr_b32 s38, s51, 8\n"
    "s_xor_b32 s38, s38, s56\n"
    "s_and_b32 s38, s38, 0x2000\n"
    "s_or_b32 s51, s51, s38\n"
    "s_add_u32 s52, s53, 524288\n"
    "s_lshr_b32 s38, s52, 8\n"
    "s_xor_b32 s38, s38, s56\n"
    "s_and_b32 s38, s38, 0x2000\n"
    "s_or_b32 s52, s52, s38\n"
    "s_add_u32 s53, s53, 1048576\n"
    "s_lshr_b32 s38, s53, 8\n"
    "s_xor_b32 s38, s38, s56\n"
    "s_and_b32 s38, s38, 0x2000\n"
    "s_or_b32 s53, s53, s38\n"
    "s_waitcnt lgkmcnt(12)\n"
    "s_waitcnt vmcnt(7)\n"
    "v_mfma_f32_16x16x32_bf16 v[52:55], v[48:51], v[16:19], 0\n"
    "s_waitcnt vmcnt(6)\n"
    "v_mfma_f32_16x16x32_bf16 v[52:55], v[48:51], v[20:23], v[52:55]\n"
    "s_waitcnt vmcnt(5)\n"
    "v_bitop3_b32 v60, v19, v23, s64 bitop3:0x7e\n"
    "v_mfma_f32_16x16x32_bf16 v[52:55], v[48:51], v[24:27], v[52:55]\n"
    "s_waitcnt vmcnt(4)\n"
    "v_mfma_f32_16x16x32_bf16 v[52:55], v[48:51], v[28:31], v[52:55]\n"
    "s_waitcnt vmcnt(3)\n"
    "v_bitop3_b32 v61, v27, v31, s64 bitop3:0x7e\n"
    "v_mfma_f32_16x16x32_bf16 v[56:59], v[48:51], v[32:35], 0\n"
    "s_waitcnt vmcnt(2)\n"
    "v_mfma_f32_16x16x32_bf16 v[56:59], v[48:51], v[36:39], v[56:59]\n"
    "v_bitop3_b32 v62, v35, v39, s64 bitop3:0x7e\n"
    "v_or3_b32 v60, v60, v61, v62\n"
    "v_add_f32_dpp v52, v52, v52 row_ror:8 row_mask:0xf bank_mask:0xf\n"
    "v_add_f32_dpp v53, v53, v53 row_ror:8 row_mask:0xf bank_mask:0xf\n"
    "v_add_f32_dpp v54, v54, v54 row_ror:8 row_mask:0xf bank_mask:0xf\n"
    "v_add_f32_dpp v55, v55, v55 row_ror:8 row_mask:0xf bank_mask:0xf\n"
    "v_cvt_pk_bf16_f32 v113, v54, v55\n"
    "v_cvt_pk_bf16_f32 v112, v52, v53\n"
    "s_waitcnt vmcnt(1)\n"
    "v_mfma_f32_16x16x32_bf16 v[56:59], v[48:51], v[40:43], v[56:59]\n"
    "s_waitcnt vmcnt(0)\n"
    "v_mfma_f32_16x16x32_bf16 v[56:59], v[48:51], v[44:47], v[56:59]\n"
    "v_bitop3_b32 v63, v43, v47, s64 bitop3:0x7e\n"
    "v_or_b32 v60, v60, v63\n"
    "v_and_b32 v61, 1, v60\n"
    "v_cmp_ne_u32 vcc, 0, v61\n"
    "s_cbranch_vccnz LGR%=\n"
    "s_nop 2\n"
    "v_add_f32_dpp v56, v56, v56 row_ror:8 row_mask:0xf bank_mask:0xf\n"
    "v_add_f32_dpp v57, v57, v57 row_ror:8 row_mask:0xf bank_mask:0xf\n"
    "v_add_f32_dpp v58, v58, v58 row_ror:8 row_mask:0xf bank_mask:0xf\n"
    "v_add_f32_dpp v59, v59, v59 row_ror:8 row_mask:0xf bank_mask:0xf\n"
    "v_cvt_pk_bf16_f32 v115, v58, v59\n"
    "v_cvt_pk_bf16_f32 v114, v56, v57\n"
    "v_min_u32 v115, -2, v115\n"
    "buffer_store_dwordx4 v[112:115], v14, s[16:19], s48 offen sc1\n"
    "ds_write_b32 v118, v119 offset:12\n"
    "s_xor_b32 s46, s46, 0x80000\n"
    "s_add_u32 s47, s46, 0x40000\n"
    "s_bfe_u32 s64, s36, 0x10001\n"
    "s_add_u32 s44, s44, s8\n"
    "s_add_u32 s59, s59, 128\n"
    "v_mov_b64 v[112:113], 0\n"
    "v_mov_b64 v[114:115], 0\n"
    "v_mov_b64 v[116:117], 0\n"
    "v_mov_b64 v[118:119], 0\n"
    "v_mfma_f32_32x32x8_bf16 v[48:63], v[112:113], v[112:113], 0\n"
    "v_lshrrev_b32 v40, 4, v0\n"
    "v_and_b32 v40, 0x30, v40\n"
    "v_lshl_add_u32 v40, v40, 4, v0\n"
    "v_mov_b32 v41, 158224\n"
    "s_add_u32 s73, s49, s11\n"
    "s_add_u32 s73, s73, 4096\n"
    "s_add_u32 m0, s11, 151552\n"
    "s_branch LBM%=\n"
    "LGR%=:\n"
    "s_add_u32 s33, s33, 1\n"
    "s_cmp_gt_u32 s33, 0x40000\n"
    "s_cbranch_scc1 LFAIL%=\n"
    "s_add_u32 s44, s44, s13\n"
    "LGM%=:\n"
    "s_memtime s[40:41]\n"
    "s_add_u32 s42, s21, s9\n"
    "s_waitcnt lgkmcnt(0)\n"
    "s_sub_u32 s40, s40, s42\n"
    "s_sub_u32 s41, s44, s40\n"
    "s_cmp_ge_i32 s41, 0\n"
    "s_cbranch_scc1 LGMD%=\n"
    "LGMI%=:\n"
    "s_add_u32 s44, s44, s13\n"
    "s_add_u32 s41, s41, s13\n"
    "s_cbranch_scc0 LGMI%=\n"
    "s_branch LGM%=\n"
    "LGMD%=:\n"
    "s_add_u32 s39, s44, s9\n"
    "s_add_u32 s38, s44, s8\n"
    "v_mov_b32 v60, s72\n"
    "v_mov_b32 v61, s38\n"
    "ds_write_b32 v60, v61 offset:16\n"
    "s_branch LUG%=\n"
    "LMR%=:\n"
    "s_add_u32 s33, s33, 1\n"
    "s_cmp_gt_u32 s33, 0x40000\n"
    "s_cbranch_scc1 LFAIL%=\n"
    "s_add_u32 s39, s44, s13\n"
    "s_mov_b32 s54, 0x80000000\n"
    "LSL%=:\n"
    "s_mov_b32 s44, s39\n"
    "s_waitcnt lgkmcnt(0)\n"
    "s_barrier\n"
    "s_branch LPRE%=\n"
    "LW1%=:\n"
    "v_mov_b32 v64, 158240\n"
    "s_mov_b32 s37, 0x80000\n"
    "LOW%=:\n"
    "ds_read_b128 v[68:71], v64 offset:16\n"
    "s_waitcnt lgkmcnt(0)\n"
    "v_min3_u32 v68, v68, v70, v71\n"
    "v_cmp_eq_u32 vcc, s36, v68\n"
    "s_cbranch_vccnz LOK%=\n"
    "s_sleep 0\n"
    "s_sub_u32 s37, s37, 1\n"
    "s_cbranch_scc0 LOW%=\n"
    "s_branch LFAIL%=\n"
    "LOK%=:\n"
    "buffer_store_dword v1, off, s[16:19], s58 sc1\n"
    "s_add_u32 s58, s58, 4\n"
    "s_bitset0_b32 s58, 31\n"
    "s_waitcnt vmcnt(3)\n"
    "v_cmp_ne_u32 vcc, 0, v116\n"
    "v_readfirstlane_b32 s70, v116\n"
    "s_nop 0\n"
    "s_andn2_b32 s38, 7, vcc_lo\n"
    "s_cbranch_scc0 LS2K%=\n"
    "s_mov_b32 s37, 0x80000\n"
    "LS2W%=:\n"
    "s_mov_b64 exec, 7\n"
    "buffer_load_dword v116, v0, s[16:19], s59 offen sc1\n"
    "s_mov_b64 exec, -1\n"
    "s_waitcnt vmcnt(0)\n"
    "v_cmp_ne_u32 vcc, 0, v116\n"
    "v_readfirstlane_b32 s70, v116\n"
    "s_nop 0\n"
    "s_andn2_b32 s38, 7, vcc_lo\n"
    "s_cbranch_scc0 LS2K%=\n"
    "s_sleep 2\n"
    "s_sub_u32 s37, s37, 1\n"
    "s_cbranch_scc0 LS2W%=\n"
    "s_branch LFAIL%=\n"
    "LS2K%=:\n"
    "s_add_u32 s53, s50, s55\n"
    "s_mov_b32 s49, s48\n"
    "s_add_u32 s51, s53, 0\n"
    "s_lshr_b32 s38, s51, 8\n"
    "s_xor_b32 s38, s38, s56\n"
    "s_and_b32 s38, s38, 0x2000\n"
    "s_or_b32 s51, s51, s38\n"
    "s_add_u32 s52, s53, 524288\n"
    "s_lshr_b32 s38, s52, 8\n"
    "s_xor_b32 s38, s38, s56\n"
    "s_and_b32 s38, s38, 0x2000\n"
    "s_or_b32 s52, s52, s38\n"
    "s_add_u32 s53, s53, 1048576\n"
    "s_lshr_b32 s38, s53, 8\n"
    "s_xor_b32 s38, s38, s56\n"
    "s_and_b32 s38, s38, 0x2000\n"
    "s_or_b32 s53, s53, s38\n"
    "s_add_u32 s66, s44, s12\n"
    "s_xor_b32 s46, s46, 0x80000\n"
    "s_add_u32 s47, s46, 0x40000\n"
    "s_bfe_u32 s64, s36, 0x10001\n"
    "s_add_u32 s44, s44, s8\n"
    "s_add_u32 s59, s59, 128\n"
    "ds_read_b128 v[64:67], v7 offset:0\n"
    "ds_read_b128 v[68:71], v7 offset:1024\n"
    "ds_read_b128 v[72:75], v7 offset:2048\n"
    "ds_read_b128 v[76:79], v7 offset:3072\n"
    "ds_read_b128 v[80:83], v7 offset:4096\n"
    "ds_read_b128 v[84:87], v7 offset:5120\n"
    "ds_read_b128 v[88:91], v7 offset:6144\n"
    "ds_read_b128 v[92:95], v7 offset:7168\n"
    "ds_read_b128 v[96:99], v7 offset:8192\n"
    "ds_read_b128 v[100:103], v7 offset:9216\n"
    "ds_read_b128 v[104:107], v7 offset:10240\n"
    "ds_read_b128 v[108:111], v7 offset:11264\n"
    "v_mov_b64 v[112:113], 0\n"
    "v_mov_b64 v[114:115], 0\n"
    "v_mov_b64 v[116:117], 0\n"
    "v_mov_b64 v[118:119], 0\n"
    "v_mfma_f32_32x32x8_bf16 v[48:63], v[112:113], v[112:113], 0\n"
    "v_lshrrev_b32 v40, 4, v0\n"
    "v_and_b32 v40, 0x30, v40\n"
    "v_lshl_add_u32 v40, v40, 4, v0\n"
    "v_mov_b32 v41, 158224\n"
    "s_add_u32 s73, s49, s11\n"
    "s_add_u32 s73, s73, 4096\n"
    "s_add_u32 m0, s11, 151552\n"
    "s_mov_b32 s37, 0x80000\n"
    "LKW%=:\n"
    "ds_read_b64 v[16:17], v41 offset:16\n"
    "s_waitcnt lgkmcnt(0)\n"
    "v_min_u32 v16, v16, v17\n"
    "v_cmp_eq_u32 vcc, s36, v16\n"
    "s_cbranch_vccnz LKK%=\n"
    "s_sleep 1\n"
    "s_sub_u32 s37, s37, 1\n"
    "s_cbranch_scc0 LKW%=\n"
    "s_branch LFAIL%=\n"
    "LKK%=:\n"
    "LUF%=:\n"
    "s_memtime s[40:41]\n"
    "s_waitcnt lgkmcnt(0)\n"
    "s_sub_u32 s40, s66, s40\n"
    "s_sub_u32 s41, s40, 253\n"
    "s_cmp_lt_u32 s41, 16384\n"
    "s_cbranch_scc1 LUF%=\n"
    "s_cmp_lt_u32 s40, 253\n"
    "s_cselect_b32 s40, s40, 0\n"
    "s_and_b32 s40, s40, 0xfc\n"
    "s_getpc_b64 s[42:43]\n"
    "s_sub_u32 s40, 272, s40\n"
    "s_add_u32 s42, s42, s40\n"
    "s_addc_u32 s43, s43, 0\n"
    "s_setpc_b64 s[42:43]\n"
    ".rept 63\n"
    "s_nop 0\n"
    ".endr\n"
    "s_sub_u32 s51, s51, 2048\n"
    "s_mov_b32 m0, 122880\n"
    "s_nop 0\n"
    "buffer_load_dwordx4 v0, s[16:19], s51 offen offset:0 sc1 lds\n"
    "buffer_load_dwordx4 v0, s[16:19], s51 offen offset:1024 sc1 lds\n"
    "buffer_load_dwordx4 v0, s[16:19], s51 offen offset:2048 sc1 lds\n"
    "buffer_load_dwordx4 v0, s[16:19], s51 offen offset:3072 sc1 lds\n"
    "s_mov_b32 m0, 126976\n"
    "s_add_u32 s51, s51, 4096\n"
    "buffer_load_dwordx4 v0, s[16:19], s51 offen offset:0 sc1 lds\n"
    "buffer_load_dwordx4 v0, s[16:19], s51 offen offset:1024 sc1 lds\n"
    "buffer_load_dwordx4 v0, s[16:19], s51 offen offset:2048 sc1 lds\n"
    "buffer_load_dwordx4 v0, s[16:19], s51 offen offset:3072 sc1 lds\n"
    "LMN%=:\n"
    "LMM%=:\n"
    "s_memtime s[40:41]\n"
    "s_add_u32 s42, s21, s10\n"
    "s_waitcnt lgkmcnt(0)\n"
    "s_sub_u32 s40, s40, s42\n"
    "s_sub_u32 s41, s44, s40\n"
    "s_cmp_ge_i32 s41, 0\n"
    "s_cbranch_scc1 LMMD%=\n"
    "LMMI%=:\n"
    "s_add_u32 s44, s44, s13\n"
    "s_add_u32 s41, s41, s13\n"
    "s_cbranch_scc0 LMMI%=\n"
    "s_branch LMM%=\n"
    "LMMD%=:\n"
    "s_add_u32 s39, s44, s10\n"
    "v_mov_b32 v44, s72\n"
    "v_mov_b32 v45, s44\n"
    "ds_write_b32 v44, v45 offset:16\n"
    "s_mov_b32 m0, 147456\n"
    "s_mov_b32 s37, 64\n"
    "s_waitcnt lgkmcnt(0)\n"
    "LUL%=:\n"
    "s_memtime s[40:41]\n"
    "s_waitcnt lgkmcnt(0)\n"
    "s_sub_u32 s40, s39, s40\n"
    "s_sub_u32 s41, s40, 253\n"
    "s_cmp_lt_u32 s41, 16384\n"
    "s_cbranch_scc1 LUL%=\n"
    "s_cmp_lt_u32 s40, 253\n"
    "s_cselect_b32 s40, s40, 0\n"
    "s_and_b32 s40, s40, 0xfc\n"
    "s_getpc_b64 s[42:43]\n"
    "s_sub_u32 s40, 272, s40\n"
    "s_add_u32 s42, s42, s40\n"
    "s_addc_u32 s43, s43, 0\n"
    "s_setpc_b64 s[42:43]\n"
    ".rept 63\n"
    "s_nop 0\n"
    ".endr\n"
    "buffer_load_dwordx4 v0, s[16:19], s49 offen offset:0 sc1 lds\n"
    "buffer_load_dwordx4 v0, s[16:19], s49 offen offset:1024 sc1 lds\n"
    "buffer_load_dwordx4 v0, s[16:19], s49 offen offset:2048 sc1 lds\n"
    "buffer_load_dwordx4 v0, s[16:19], s49 offen offset:3072 sc1 lds\n"
    "s_mov_b32 m0, 152576\n"
    "s_getpc_b64 s[42:43]\n"
    "s_add_u32 s42, s42, s0\n"
    "s_addc_u32 s43, s43, 0\n"
    "s_setpc_b64 s[42:43]\n"
    ".rept 63\n"
    "s_nop 1\n"
    ".endr\n"
    "LDW%=:\n"
    "ds_read2_b32 v[46:47], v44 offset0:2 offset1:3\n"
    "s_waitcnt lgkmcnt(0)\n"
    "v_readfirstlane_b32 s39, v47\n"
    "v_readfirstlane_b32 s40, v46\n"
    "s_sub_u32 s38, s39, s44\n"
    "s_cmp_gt_i32 s38, 0\n"
    "s_cselect_b32 s44, s39, s44\n"
    "s_cbranch_scc1 LMN%=\n"
    "s_cmp_eq_u32 s40, s36\n"
    "s_cbranch_scc1 LBM%=\n"
    "s_sub_u32 s37, s37, 1\n"
    "s_cbranch_scc0 LDW%=\n"
    "s_branch LBM%=\n"
    "LW2%=:\n"
    "ds_read_b128 v[16:19], v14 offset:0\n"
    "v_add_u32 v118, 122880, v0\n"
    "ds_read_b128 v[32:35], v118 offset:0\n"
    "ds_read_b128 v[48:51], v118 offset:4096\n"
    "ds_read_b128 v[20:23], v14 offset:1024\n"
    "ds_read_b128 v[36:39], v118 offset:1024\n"
    "ds_read_b128 v[52:55], v118 offset:5120\n"
    "v_mov_b32 v111, 0xd0289000\n"
    "v_cndmask_b32 v80, v111, 0, s[80:81]\n"
    "v_cndmask_b32 v81, v111, 0, s[82:83]\n"
    "v_cndmask_b32 v82, v111, 0, s[84:85]\n"
    "v_cndmask_b32 v83, v111, 0, s[86:87]\n"
    "v_cndmask_b32 v84, v111, 0, s[88:89]\n"
    "v_cndmask_b32 v85, v111, 0, s[90:91]\n"
    "v_cndmask_b32 v86, v111, 0, s[92:93]\n"
    "v_cndmask_b32 v87, v111, 0, s[94:95]\n"
    "v_lshrrev_b32 v112, 1, v0\n"
    "v_add_u32 v112, 149760, v112\n"
    "ds_read_b128 v[24:27], v14 offset:2048\n"
    "ds_read_b128 v[40:43], v118 offset:2048\n"
    "ds_read_b128 v[56:59], v118 offset:6144\n"
    "s_waitcnt lgkmcnt(7)\n"
    "v_mfma_f32_16x16x32_bf16 v[80:83], v[32:35], v[16:19], v[80:83]\n"
    "ds_read_b128 v[28:31], v14 offset:3072\n"
    "ds_read_b128 v[44:47], v118 offset:3072\n"
    "s_waitcnt lgkmcnt(8)\n"
    "v_mfma_f32_16x16x32_bf16 v[84:87], v[48:51], v[16:19], v[84:87]\n"
    "ds_read_b128 v[60:63], v118 offset:7168\n"
    "s_waitcnt lgkmcnt(7)\n"
    "v_mov_b32 v114, s72\n"
    "v_mfma_f32_16x16x32_bf16 v[80:83], v[36:39], v[20:23], v[80:83]\n"
    "ds_read_b128 v[64:67], v6 offset:0\n"
    "ds_read_b128 v[68:71], v6 offset:64\n"
    "s_waitcnt lgkmcnt(8)\n"
    "v_mfma_f32_16x16x32_bf16 v[84:87], v[52:55], v[20:23], v[84:87]\n"
    "ds_read_b128 v[72:75], v6 offset:128\n"
    "s_waitcnt lgkmcnt(7)\n"
    "v_mov_b32 v105, 0\n"
    "v_mfma_f32_16x16x32_bf16 v[80:83], v[40:43], v[24:27], v[80:83]\n"
    "ds_read_b128 v[76:79], v6 offset:192\n"
    "ds_read_b64 v[92:93], v112 offset:0\n"
    "s_waitcnt lgkmcnt(8)\n"
    "v_mfma_f32_16x16x32_bf16 v[84:87], v[56:59], v[24:27], v[84:87]\n"
    "ds_read_b64 v[94:95], v112 offset:512\n"
    "s_waitcnt lgkmcnt(7)\n"
    "v_mov_b32 v106, 0\n"
    "v_mfma_f32_16x16x32_bf16 v[80:83], v[44:47], v[28:31], v[80:83]\n"
    "ds_read_b64 v[96:97], v112 offset:1024\n"
    "ds_read_b64 v[98:99], v112 offset:1536\n"
    "s_waitcnt lgkmcnt(8)\n"
    "v_mfma_f32_16x16x32_bf16 v[84:87], v[60:63], v[28:31], v[84:87]\n"
    "ds_write_b32 v114, v119 offset:24\n"
    "s_waitcnt lgkmcnt(8)\n"
    "v_mfma_f32_16x16x32_bf16 v[88:91], v[64:67], v[16:19], 0\n"
    "ds_read_b128 v[32:35], v118 offset:8192\n"
    "ds_read_b128 v[36:39], v118 offset:9216\n"
    "s_waitcnt lgkmcnt(9)\n"
    "v_mfma_f32_16x16x32_bf16 v[88:91], v[68:71], v[20:23], v[88:91]\n"
    "ds_read_b128 v[40:43], v118 offset:10240\n"
    "ds_read_b128 v[44:47], v118 offset:11264\n"
    "s_waitcnt lgkmcnt(10)\n"
    "v_mfma_f32_16x16x32_bf16 v[88:91], v[72:75], v[24:27], v[88:91]\n"
    "ds_read_b128 v[48:51], v118 offset:12288\n"
    "ds_read_b128 v[52:55], v118 offset:13312\n"
    "s_waitcnt lgkmcnt(11)\n"
    "v_max3_f32 v108, v80, v81, v82\n"
    "v_max3_f32 v109, v83, v84, v85\n"
    "v_mfma_f32_16x16x32_bf16 v[88:91], v[76:79], v[28:31], v[88:91]\n"
    "ds_read_b128 v[56:59], v118 offset:14336\n"
    "ds_read_b128 v[60:63], v118 offset:15360\n"
    "v_max3_f32 v108, v108, v86, v87\n"
    "v_max_f32 v108, v108, v109\n"
    "v_mov_b32 v109, v108\n"
    "s_nop 1\n"
    "v_permlane32_swap_b32 v108, v109\n"
    "v_max_f32 v108, v108, v109\n"
    "v_max3_f32 v108, v108, v88, v89\n"
    "v_max3_f32 v108, v108, v90, v91\n"
    "v_mov_b32 v109, v108\n"
    "s_nop 1\n"
    "v_permlane16_swap_b32 v108, v109\n"
    "v_max_f32 v108, v108, v109\n"
    "v_mul_f32 v108, 0xbe0293ee, v108\n"
    "v_pk_fma_f32 v[80:81], v[80:81], s[64:65], v[108:109] op_sel:[0,1,0] op_sel_hi:[1,1,0]\n"
    "v_pk_fma_f32 v[82:83], v[82:83], s[64:65], v[108:109] op_sel:[0,1,0] op_sel_hi:[1,1,0]\n"
    "v_pk_fma_f32 v[84:85], v[84:85], s[64:65], v[108:109] op_sel:[0,1,0] op_sel_hi:[1,1,0]\n"
    "v_pk_fma_f32 v[86:87], v[86:87], s[64:65], v[108:109] op_sel:[0,1,0] op_sel_hi:[1,1,0]\n"
    "v_pk_fma_f32 v[88:89], v[88:89], s[64:65], v[108:109] op_sel:[0,1,0] op_sel_hi:[1,1,0]\n"
    "v_exp_f32 v80, v80\n"
    "v_exp_f32 v81, v81\n"
    "v_exp_f32 v82, v82\n"
    "v_exp_f32 v83, v83\n"
    "v_exp_f32 v84, v84\n"
    "v_exp_f32 v85, v85\n"
    "v_exp_f32 v86, v86\n"
    "v_exp_f32 v87, v87\n"
    "v_exp_f32 v88, v88\n"
    "v_exp_f32 v89, v89\n"
    "v_pk_add_f32 v[108:109], v[80:81], v[82:83]\n"
    "v_pk_add_f32 v[110:111], v[84:85], v[86:87]\n"
    "v_pk_add_f32 v[108:109], v[108:109], v[88:89]\n"
    "v_pk_add_f32 v[108:109], v[108:109], v[110:111]\n"
    "v_add_f32 v108, v108, v109\n"
    "v_mov_b32 v109, v108\n"
    "s_nop 1\n"
    "v_permlane16_swap_b32 v108, v109\n"
    "v_add_f32 v108, v108, v109\n"
    "v_mov_b32 v109, v108\n"
    "s_nop 1\n"
    "v_permlane32_swap_b32 v108, v109\n"
    "v_add_f32 v108, v108, v109\n"
    "v_rcp_f32 v108, v108\n"
    "s_nop 0\n"
    "v_pk_mul_f32 v[80:81], v[80:81], v[108:109] op_sel_hi:[1,0]\n"
    "v_pk_mul_f32 v[82:83], v[82:83], v[108:109] op_sel_hi:[1,0]\n"
    "v_pk_mul_f32 v[84:85], v[84:85], v[108:109] op_sel_hi:[1,0]\n"
    "v_pk_mul_f32 v[86:87], v[86:87], v[108:109] op_sel_hi:[1,0]\n"
    "v_pk_mul_f32 v[88:89], v[88:89], v[108:109] op_sel_hi:[1,0]\n"
    "v_cvt_pk_bf16_f32 v100, v80, v81\n"
    "v_cvt_pk_bf16_f32 v101, v82, v83\n"
    "v_cvt_pk_bf16_f32 v102, v84, v85\n"
    "v_cvt_pk_bf16_f32 v103, v86, v87\n"
    "v_cvt_pk_bf16_f32 v104, v88, v89\n"
    "v_cvt_pk_bf16_f32 v107, v88, v89\n"
    "s_waitcnt lgkmcnt(7)\n"
    "v_mfma_f32_16x16x32_bf16 v[16:19], v[32:35], v[100:103], 0\n"
    "ds_read_b128 v[64:67], v7 offset:0\n"
    "s_waitcnt lgkmcnt(7)\n"
    "v_mfma_f32_16x16x32_bf16 v[20:23], v[36:39], v[100:103], 0\n"
    "ds_read_b128 v[68:71], v7 offset:1024\n"
    "v_mfma_f32_16x16x16_bf16 v[16:19], v[92:93], v[104:105], v[16:19]\n"
    "v_mfma_f32_16x16x16_bf16 v[20:23], v[92:93], v[106:107], v[20:23]\n"
    "s_waitcnt lgkmcnt(7)\n"
    "v_mfma_f32_16x16x32_bf16 v[24:27], v[40:43], v[100:103], 0\n"
    "ds_read_b128 v[72:75], v7 offset:2048\n"
    "s_waitcnt lgkmcnt(7)\n"
    "v_mfma_f32_16x16x32_bf16 v[28:31], v[44:47], v[100:103], 0\n"
    "ds_read_b128 v[76:79], v7 offset:3072\n"
    "v_cvt_pk_bf16_f32 v112, v16, v17\n"
    "v_cvt_pk_bf16_f32 v113, v18, v19\n"
    "v_mfma_f32_16x16x16_bf16 v[24:27], v[94:95], v[104:105], v[24:27]\n"
    "v_cvt_pk_bf16_f32 v114, v20, v21\n"
    "v_cvt_pk_bf16_f32 v115, v22, v23\n"
    "v_mfma_f32_16x16x16_bf16 v[28:31], v[94:95], v[106:107], v[28:31]\n"
    "ds_write_b128 v14, v[112:115] offset:0\n"
    "s_waitcnt lgkmcnt(8)\n"
    "v_mfma_f32_16x16x32_bf16 v[16:19], v[48:51], v[100:103], 0\n"
    "ds_read_b128 v[80:83], v7 offset:4096\n"
    "s_waitcnt lgkmcnt(8)\n"
    "v_mfma_f32_16x16x32_bf16 v[20:23], v[52:55], v[100:103], 0\n"
    "ds_read_b128 v[84:87], v7 offset:5120\n"
    "v_cvt_pk_bf16_f32 v108, v24, v25\n"
    "v_cvt_pk_bf16_f32 v109, v26, v27\n"
    "v_mfma_f32_16x16x16_bf16 v[16:19], v[96:97], v[104:105], v[16:19]\n"
    "v_cvt_pk_bf16_f32 v110, v28, v29\n"
    "v_cvt_pk_bf16_f32 v111, v30, v31\n"
    "v_mfma_f32_16x16x16_bf16 v[20:23], v[96:97], v[106:107], v[20:23]\n"
    "ds_write_b128 v14, v[108:111] offset:1024\n"
    "s_waitcnt lgkmcnt(9)\n"
    "v_mfma_f32_16x16x32_bf16 v[24:27], v[56:59], v[100:103], 0\n"
    "s_waitcnt lgkmcnt(8)\n"
    "v_mfma_f32_16x16x32_bf16 v[28:31], v[60:63], v[100:103], 0\n"
    "v_cvt_pk_bf16_f32 v112, v16, v17\n"
    "v_cvt_pk_bf16_f32 v113, v18, v19\n"
    "v_mfma_f32_16x16x16_bf16 v[24:27], v[98:99], v[104:105], v[24:27]\n"
    "v_cvt_pk_bf16_f32 v114, v20, v21\n"
    "v_cvt_pk_bf16_f32 v115, v22, v23\n"
    "v_mfma_f32_16x16x16_bf16 v[28:31], v[98:99], v[106:107], v[28:31]\n"
    "ds_write_b128 v14, v[112:115] offset:2048\n"
    "ds_read_b128 v[88:91], v7 offset:6144\n"
    "ds_read_b128 v[92:95], v7 offset:7168\n"
    "ds_read_b128 v[96:99], v7 offset:8192\n"
    "ds_read_b128 v[100:103], v7 offset:9216\n"
    "v_cvt_pk_bf16_f32 v108, v24, v25\n"
    "v_cvt_pk_bf16_f32 v109, v26, v27\n"
    "s_nop 0\n"
    "v_cvt_pk_bf16_f32 v110, v28, v29\n"
    "v_cvt_pk_bf16_f32 v111, v30, v31\n"
    "ds_write_b128 v14, v[108:111] offset:3072\n"
    "s_add_u32 s53, s50, s55\n"
    "s_mov_b32 s49, s48\n"
    "s_add_u32 s51, s53, 0\n"
    "s_lshr_b32 s38, s51, 8\n"
    "s_xor_b32 s38, s38, s56\n"
    "s_and_b32 s38, s38, 0x2000\n"
    "s_or_b32 s51, s51, s38\n"
    "s_add_u32 s52, s53, 524288\n"
    "s_lshr_b32 s38, s52, 8\n"
    "s_xor_b32 s38, s38, s56\n"
    "s_and_b32 s38, s38, 0x2000\n"
    "s_or_b32 s52, s52, s38\n"
    "s_add_u32 s53, s53, 1048576\n"
    "s_lshr_b32 s38, s53, 8\n"
    "s_xor_b32 s38, s38, s56\n"
    "s_and_b32 s38, s38, 0x2000\n"
    "s_or_b32 s53, s53, s38\n"
    "s_xor_b32 s46, s46, 0x80000\n"
    "s_add_u32 s47, s46, 0x40000\n"
    "s_bfe_u32 s64, s36, 0x10001\n"
    "s_add_u32 s44, s44, s8\n"
    "s_add_u32 s59, s59, 128\n"
    "s_waitcnt vmcnt(2)\n"
    "s_waitcnt lgkmcnt(0)\n"
    "ds_read_b128 v[104:107], v7 offset:10240\n"
    "ds_read_b128 v[108:111], v7 offset:11264\n"
    "v_mov_b64 v[112:113], 0\n"
    "v_mov_b64 v[114:115], 0\n"
    "s_waitcnt vmcnt(2)\n"
    "v_mov_b64 v[116:117], 0\n"
    "v_mov_b64 v[118:119], 0\n"
    "v_mfma_f32_32x32x8_bf16 v[48:63], v[112:113], v[112:113], 0\n"
    "v_lshrrev_b32 v40, 4, v0\n"
    "v_and_b32 v40, 0x30, v40\n"
    "v_lshl_add_u32 v40, v40, 4, v0\n"
    "v_mov_b32 v41, 158224\n"
    "s_add_u32 s73, s49, s11\n"
    "s_add_u32 s73, s73, 4096\n"
    "s_add_u32 m0, s11, 151552\n"
    "s_branch LBM%=\n"
    "s_waitcnt lgkmcnt(0)\n"
    "LPRE%=:\n"
    "s_nop 7\n"
    "ds_read_b128 v[64:67], v7 offset:0\n"
    "ds_read_b128 v[68:71], v7 offset:1024\n"
    "ds_read_b128 v[72:75], v7 offset:2048\n"
    "ds_read_b128 v[76:79], v7 offset:3072\n"
    "ds_read_b128 v[80:83], v7 offset:4096\n"
    "ds_read_b128 v[84:87], v7 offset:5120\n"
    "ds_read_b128 v[88:91], v7 offset:6144\n"
    "ds_read_b128 v[92:95], v7 offset:7168\n"
    "ds_read_b128 v[96:99], v7 offset:8192\n"
    "ds_read_b128 v[100:103], v7 offset:9216\n"
    "ds_read_b128 v[104:107], v7 offset:10240\n"
    "ds_read_b128 v[108:111], v7 offset:11264\n"
    "v_mov_b64 v[112:113], 0\n"
    "v_mov_b64 v[114:115], 0\n"
    "v_mov_b64 v[116:117], 0\n"
    "v_mov_b64 v[118:119], 0\n"
    "v_mfma_f32_32x32x8_bf16 v[48:63], v[112:113], v[112:113], 0\n"
    "v_lshrrev_b32 v40, 4, v0\n"
    "v_and_b32 v40, 0x30, v40\n"
    "v_lshl_add_u32 v40, v40, 4, v0\n"
    "v_mov_b32 v41, 158224\n"
    "s_add_u32 s73, s49, s11\n"
    "s_add_u32 s73, s73, 4096\n"
    "s_add_u32 m0, s11, 151552\n"
    "s_cmp_lg_u32 s61, 1\n"
    "s_cbranch_scc1 LBM%=\n"
    "s_branch LMN%=\n"
    "LEND%=:\n"
    "s_waitcnt vmcnt(1) lgkmcnt(0)\n"
    "s_barrier\n"
    "s_cmp_lg_u32 s61, 1\n"
    "s_cbranch_scc1 LE0%=\n"
    "buffer_store_dword v1, off, s[16:19], s58 sc1\n"
    "s_add_u32 s58, s58, 4\n"
    "s_bitset0_b32 s58, 31\n"
    "LE0%=:\n"
    "s_add_u32 s54, s50, s57\n"
    "s_lshr_b32 s38, s54, 8\n"
    "s_xor_b32 s38, s38, s74\n"
    "s_and_b32 s38, s38, 0x2000\n"
    "s_or_b32 s54, s54, s38\n"
    "s_cmp_lt_u32 s61, 2\n"
    "s_cbranch_scc1 LE1%=\n"
    "ds_read_b128 v[16:19], v14 offset:0\n"
    "v_add_u32 v118, 122880, v0\n"
    "ds_read_b128 v[32:35], v118 offset:0\n"
    "ds_read_b128 v[48:51], v118 offset:4096\n"
    "ds_read_b128 v[20:23], v14 offset:1024\n"
    "ds_read_b128 v[36:39], v118 offset:1024\n"
    "ds_read_b128 v[52:55], v118 offset:5120\n"
    "v_mov_b32 v111, 0xd0289000\n"
    "v_cndmask_b32 v80, v111, 0, s[80:81]\n"
    "v_cndmask_b32 v81, v111, 0, s[82:83]\n"
    "v_cndmask_b32 v82, v111, 0, s[84:85]\n"
    "v_cndmask_b32 v83, v111, 0, s[86:87]\n"
    "v_cndmask_b32 v84, v111, 0, s[88:89]\n"
    "v_cndmask_b32 v85, v111, 0, s[90:91]\n"
    "v_cndmask_b32 v86, v111, 0, s[92:93]\n"
    "v_cndmask_b32 v87, v111, 0, s[94:95]\n"
    "v_lshrrev_b32 v112, 1, v0\n"
    "v_add_u32 v112, 149760, v112\n"
    "ds_read_b128 v[24:27], v14 offset:2048\n"
    "ds_read_b128 v[40:43], v118 offset:2048\n"
    "ds_read_b128 v[56:59], v118 offset:6144\n"
    "s_waitcnt lgkmcnt(7)\n"
    "v_mfma_f32_16x16x32_bf16 v[80:83], v[32:35], v[16:19], v[80:83]\n"
    "ds_read_b128 v[28:31], v14 offset:3072\n"
    "ds_read_b128 v[44:47], v118 offset:3072\n"
    "s_waitcnt lgkmcnt(8)\n"
    "v_mfma_f32_16x16x32_bf16 v[84:87], v[48:51], v[16:19], v[84:87]\n"
    "ds_read_b128 v[60:63], v118 offset:7168\n"
    "s_waitcnt lgkmcnt(7)\n"
    "v_mov_b32 v114, s72\n"
    "v_mfma_f32_16x16x32_bf16 v[80:83], v[36:39], v[20:23], v[80:83]\n"
    "ds_read_b128 v[64:67], v6 offset:0\n"
    "ds_read_b128 v[68:71], v6 offset:64\n"
    "s_waitcnt lgkmcnt(8)\n"
    "v_mfma_f32_16x16x32_bf16 v[84:87], v[52:55], v[20:23], v[84:87]\n"
    "ds_read_b128 v[72:75], v6 offset:128\n"
    "s_waitcnt lgkmcnt(7)\n"
    "v_mov_b32 v105, 0\n"
    "v_mfma_f32_16x16x32_bf16 v[80:83], v[40:43], v[24:27], v[80:83]\n"
    "ds_read_b128 v[76:79], v6 offset:192\n"
    "ds_read_b64 v[92:93], v112 offset:0\n"
    "s_waitcnt lgkmcnt(8)\n"
    "v_mfma_f32_16x16x32_bf16 v[84:87], v[56:59], v[24:27], v[84:87]\n"
    "ds_read_b64 v[94:95], v112 offset:512\n"
    "s_waitcnt lgkmcnt(7)\n"
    "v_mov_b32 v106, 0\n"
    "v_mfma_f32_16x16x32_bf16 v[80:83], v[44:47], v[28:31], v[80:83]\n"
    "ds_read_b64 v[96:97], v112 offset:1024\n"
    "ds_read_b64 v[98:99], v112 offset:1536\n"
    "s_waitcnt lgkmcnt(8)\n"
    "v_mfma_f32_16x16x32_bf16 v[84:87], v[60:63], v[28:31], v[84:87]\n"
    "ds_write_b32 v114, v119 offset:24\n"
    "s_waitcnt lgkmcnt(8)\n"
    "v_mfma_f32_16x16x32_bf16 v[88:91], v[64:67], v[16:19], 0\n"
    "ds_read_b128 v[32:35], v118 offset:8192\n"
    "ds_read_b128 v[36:39], v118 offset:9216\n"
    "s_waitcnt lgkmcnt(9)\n"
    "v_mfma_f32_16x16x32_bf16 v[88:91], v[68:71], v[20:23], v[88:91]\n"
    "ds_read_b128 v[40:43], v118 offset:10240\n"
    "ds_read_b128 v[44:47], v118 offset:11264\n"
    "s_waitcnt lgkmcnt(10)\n"
    "v_mfma_f32_16x16x32_bf16 v[88:91], v[72:75], v[24:27], v[88:91]\n"
    "ds_read_b128 v[48:51], v118 offset:12288\n"
    "ds_read_b128 v[52:55], v118 offset:13312\n"
    "s_waitcnt lgkmcnt(11)\n"
    "v_max3_f32 v108, v80, v81, v82\n"
    "v_max3_f32 v109, v83, v84, v85\n"
    "v_mfma_f32_16x16x32_bf16 v[88:91], v[76:79], v[28:31], v[88:91]\n"
    "ds_read_b128 v[56:59], v118 offset:14336\n"
    "ds_read_b128 v[60:63], v118 offset:15360\n"
    "v_max3_f32 v108, v108, v86, v87\n"
    "v_max_f32 v108, v108, v109\n"
    "v_mov_b32 v109, v108\n"
    "s_nop 1\n"
    "v_permlane32_swap_b32 v108, v109\n"
    "v_max_f32 v108, v108, v109\n"
    "v_max3_f32 v108, v108, v88, v89\n"
    "v_max3_f32 v108, v108, v90, v91\n"
    "v_mov_b32 v109, v108\n"
    "s_nop 1\n"
    "v_permlane16_swap_b32 v108, v109\n"
    "v_max_f32 v108, v108, v109\n"
    "v_mul_f32 v108, 0xbe0293ee, v108\n"
    "v_pk_fma_f32 v[80:81], v[80:81], s[64:65], v[108:109] op_sel:[0,1,0] op_sel_hi:[1,1,0]\n"
    "v_pk_fma_f32 v[82:83], v[82:83], s[64:65], v[108:109] op_sel:[0,1,0] op_sel_hi:[1,1,0]\n"
    "v_pk_fma_f32 v[84:85], v[84:85], s[64:65], v[108:109] op_sel:[0,1,0] op_sel_hi:[1,1,0]\n"
    "v_pk_fma_f32 v[86:87], v[86:87], s[64:65], v[108:109] op_sel:[0,1,0] op_sel_hi:[1,1,0]\n"
    "v_pk_fma_f32 v[88:89], v[88:89], s[64:65], v[108:109] op_sel:[0,1,0] op_sel_hi:[1,1,0]\n"
    "v_exp_f32 v80, v80\n"
    "v_exp_f32 v81, v81\n"
    "v_exp_f32 v82, v82\n"
    "v_exp_f32 v83, v83\n"
    "v_exp_f32 v84, v84\n"
    "v_exp_f32 v85, v85\n"
    "v_exp_f32 v86, v86\n"
    "v_exp_f32 v87, v87\n"
    "v_exp_f32 v88, v88\n"
    "v_exp_f32 v89, v89\n"
    "v_pk_add_f32 v[108:109], v[80:81], v[82:83]\n"
    "v_pk_add_f32 v[110:111], v[84:85], v[86:87]\n"
    "v_pk_add_f32 v[108:109], v[108:109], v[88:89]\n"
    "v_pk_add_f32 v[108:109], v[108:109], v[110:111]\n"
    "v_add_f32 v108, v108, v109\n"
    "v_mov_b32 v109, v108\n"
    "s_nop 1\n"
    "v_permlane16_swap_b32 v108, v109\n"
    "v_add_f32 v108, v108, v109\n"
    "v_mov_b32 v109, v108\n"
    "s_nop 1\n"
    "v_permlane32_swap_b32 v108, v109\n"
    "v_add_f32 v108, v108, v109\n"
    "v_rcp_f32 v108, v108\n"
    "s_nop 0\n"
    "v_pk_mul_f32 v[80:81], v[80:81], v[108:109] op_sel_hi:[1,0]\n"
    "v_pk_mul_f32 v[82:83], v[82:83], v[108:109] op_sel_hi:[1,0]\n"
    "v_pk_mul_f32 v[84:85], v[84:85], v[108:109] op_sel_hi:[1,0]\n"
    "v_pk_mul_f32 v[86:87], v[86:87], v[108:109] op_sel_hi:[1,0]\n"
    "v_pk_mul_f32 v[88:89], v[88:89], v[108:109] op_sel_hi:[1,0]\n"
    "v_cvt_pk_bf16_f32 v100, v80, v81\n"
    "v_cvt_pk_bf16_f32 v101, v82, v83\n"
    "v_cvt_pk_bf16_f32 v102, v84, v85\n"
    "v_cvt_pk_bf16_f32 v103, v86, v87\n"
    "v_cvt_pk_bf16_f32 v104, v88, v89\n"
    "v_cvt_pk_bf16_f32 v107, v88, v89\n"
    "s_waitcnt lgkmcnt(7)\n"
    "v_mfma_f32_16x16x32_bf16 v[16:19], v[32:35], v[100:103], 0\n"
    "s_waitcnt lgkmcnt(6)\n"
    "v_mfma_f32_16x16x32_bf16 v[20:23], v[36:39], v[100:103], 0\n"
    "v_mfma_f32_16x16x16_bf16 v[16:19], v[92:93], v[104:105], v[16:19]\n"
    "v_mfma_f32_16x16x16_bf16 v[20:23], v[92:93], v[106:107], v[20:23]\n"
    "s_waitcnt lgkmcnt(5)\n"
    "v_mfma_f32_16x16x32_bf16 v[24:27], v[40:43], v[100:103], 0\n"
    "s_waitcnt lgkmcnt(4)\n"
    "v_mfma_f32_16x16x32_bf16 v[28:31], v[44:47], v[100:103], 0\n"
    "v_cvt_pk_bf16_f32 v112, v16, v17\n"
    "v_cvt_pk_bf16_f32 v113, v18, v19\n"
    "v_mfma_f32_16x16x16_bf16 v[24:27], v[94:95], v[104:105], v[24:27]\n"
    "v_cvt_pk_bf16_f32 v114, v20, v21\n"
    "v_cvt_pk_bf16_f32 v115, v22, v23\n"
    "v_mfma_f32_16x16x16_bf16 v[28:31], v[94:95], v[106:107], v[28:31]\n"
    "ds_write_b128 v14, v[112:115] offset:0\n"
    "s_waitcnt lgkmcnt(4)\n"
    "v_mfma_f32_16x16x32_bf16 v[16:19], v[48:51], v[100:103], 0\n"
    "s_waitcnt lgkmcnt(3)\n"
    "v_mfma_f32_16x16x32_bf16 v[20:23], v[52:55], v[100:103], 0\n"
    "v_cvt_pk_bf16_f32 v108, v24, v25\n"
    "v_cvt_pk_bf16_f32 v109, v26, v27\n"
    "v_mfma_f32_16x16x16_bf16 v[16:19], v[96:97], v[104:105], v[16:19]\n"
    "v_cvt_pk_bf16_f32 v110, v28, v29\n"
    "v_cvt_pk_bf16_f32 v111, v30, v31\n"
    "v_mfma_f32_16x16x16_bf16 v[20:23], v[96:97], v[106:107], v[20:23]\n"
    "ds_write_b128 v14, v[108:111] offset:1024\n"
    "s_waitcnt lgkmcnt(3)\n"
    "v_mfma_f32_16x16x32_bf16 v[24:27], v[56:59], v[100:103], 0\n"
    "s_waitcnt lgkmcnt(2)\n"
    "v_mfma_f32_16x16x32_bf16 v[28:31], v[60:63], v[100:103], 0\n"
    "v_cvt_pk_bf16_f32 v112, v16, v17\n"
    "v_cvt_pk_bf16_f32 v113, v18, v19\n"
    "v_mfma_f32_16x16x16_bf16 v[24:27], v[98:99], v[104:105], v[24:27]\n"
    "v_cvt_pk_bf16_f32 v114, v20, v21\n"
    "v_cvt_pk_bf16_f32 v115, v22, v23\n"
    "v_mfma_f32_16x16x16_bf16 v[28:31], v[98:99], v[106:107], v[28:31]\n"
    "ds_write_b128 v14, v[112:115] offset:2048\n"
    "s_nop 2\n"
    "v_cvt_pk_bf16_f32 v108, v24, v25\n"
    "v_cvt_pk_bf16_f32 v109, v26, v27\n"
    "s_nop 1\n"
    "v_cvt_pk_bf16_f32 v110, v28, v29\n"
    "v_cvt_pk_bf16_f32 v111, v30, v31\n"
    "ds_write_b128 v14, v[108:111] offset:3072\n"
    "s_waitcnt lgkmcnt(0)\n"
    "LE1%=:\n"
    "s_barrier\n"
    "ds_read_b128 v[32:35], v9 offset:16384\n"
    "ds_read_b128 v[36:39], v9 offset:17408\n"
    "v_lshrrev_b32 v40, 4, v0\n"
    "v_and_b32 v40, 0x30, v40\n"
    "v_lshl_add_u32 v40, v40, 4, v0\n"
    "s_waitcnt lgkmcnt(1)\n"
    "buffer_store_dwordx4 v[32:35], v40, s[16:19], s54 offen offset:0 sc1\n"
    "s_waitcnt lgkmcnt(0)\n"
    "buffer_store_dwordx4 v[36:39], v40, s[16:19], s54 offen offset:2048 sc1\n"
    "s_waitcnt vmcnt(0)\n"
    "s_barrier\n"
    "buffer_store_dword v1, off, s[16:19], s58 sc1\n"
    "s_endpgm\n"
    "LFAIL%=:\n"
    "buffer_store_dword v1, off, s[16:19], s35 sc1\n"
    "s_endpgm\n"
    :
    : "{v0}"(hv0), "{v1}"(hv1), "{v2}"(hv2), "{v3}"(hv3), "{v4}"(hv4), "{v5}"(hv5), "{v6}"(hv6), "{v7}"(hv7), "{v8}"(hv8), "{v9}"(hv9), "{v10}"(hv10), "{v11}"(hv11), "{v12}"(hv12), "{v13}"(hv13), "{v14}"(hv14), "{v15}"(hv15), "{v16}"(hv16), "{v17}"(hv17), "{v18}"(hv18), "{v19}"(hv19), "{v20}"(hv20)
    : "v255", "a255", "s101", "vcc", "scc", "memory");
  }
  __builtin_unreachable();
}
#define SW 1
#define ODL 9
#define ROLL 2
#define MEMFENCE() asm volatile("" ::: "memory")
__device__ __forceinline__ void t_role8(int w, int m, int i, const unsigned* rdy, char* ws, const u16* W) {
  const int lane = lane_id(), g = lane >> 4, q = (lane >> 2) & 3, p = lane & 3;
  const unsigned me = w ? OOR : 0u, c1 = 0u, c2 = 0x2000u;
  const int h = i & 30, par = i & 1, ob = m * 524288 + h * 16384 + (m == 1 ? w * 2048 : w * 1024);
  const int pv[24] = {w, (int)Z_XN1 + par * 65536 + w * 16384, (int)Z_XN + w * 16384, ob, (int)c1, (int)nearof(Z_FT + h * 16384, c1) + m * 16, (int)nearof(Z_FT + h * 16384, c2) + m * 16,
                      (int)((nearof(Z_DEC, c1) + 4096 + m * 512 + par * 4) | me), (int)((nearof(Z_DEC, c1) + 2048 + m * 512) | me), 0, 0, (int)(nearof(Z_FO + 32768, c2) + h * 256), (int)Z_ERR, m, par,
                      (int)(size_t)W, (int)((size_t)W >> 32) & 0xffff, (int)(size_t)ws, (int)((size_t)ws >> 32) & 0xffff, (int)ZWS_BYTES, (int)((Z_DBG + MBY(1) + (m * 32 + i) * 4096) | (w == 2 ? 0u : OOR)),
                      (int)(size_t)rdy, (int)((size_t)rdy >> 32), (int)((SW ? nearof(Z_DEC, c1) + 1536 : nearof(Z_DEC, c2) + 1024) | (w == 1 ? 0u : OOR))};
  int hv0 = lane * 16, hv1 = (lane & 15) * 1056 + g * 16, hv2 = 0;
  int hv3 = (w * 8 + (lane >> 5)) * 8192 + h * 256 + ((lane & 31) ^ ((lane >> 5) * 2)) * 16, hv4 = g * 4480 + (q >> 1) * 1088 + (q & 1) * 544 + 64 * w + 8 * p, hv5 = 0;
#pragma unroll
  for (int j = 0; j < 24; ++j) hv5 = lane == j ? pv[j] : hv5;
  asm volatile(
    "s_nop 1\n"
    "v_readlane_b32 s36, v5, 0\n"
    "v_readlane_b32 s45, v5, 1\n"
    "v_readlane_b32 s46, v5, 2\n"
    "v_readlane_b32 s47, v5, 3\n"
    "v_readlane_b32 s48, v5, 4\n"
    "v_readlane_b32 s50, v5, 5\n"
    "v_readlane_b32 s51, v5, 6\n"
    "v_readlane_b32 s53, v5, 7\n"
    "v_readlane_b32 s54, v5, 8\n"
    "v_readlane_b32 s55, v5, 9\n"
    "v_readlane_b32 s56, v5, 10\n"
    "v_readlane_b32 s57, v5, 11\n"
    "v_readlane_b32 s58, v5, 12\n"
    "v_readlane_b32 s61, v5, 13\n"
    "v_readlane_b32 s40, v5, 14\n"
    "v_readlane_b32 s20, v5, 15\n"
    "v_readlane_b32 s21, v5, 16\n"
    "v_readlane_b32 s16, v5, 17\n"
    "v_readlane_b32 s17, v5, 18\n"
    "v_readlane_b32 s18, v5, 19\n"
    "v_readlane_b32 s65, v5, 20\n"
    "v_readlane_b32 s24, v5, 21\n"
    "v_readlane_b32 s25, v5, 22\n"
    "v_readlane_b32 s26, v5, 23\n"
    "v_mov_b32 v5, 1\n"
    "s_mov_b32 s19, 0x20000\n"
    "s_mov_b32 s22, 0x400000\n"
    "s_mov_b32 s23, 0x20000\n"
    "s_xor_b32 s49, s48, 0x2000\n"
    "s_mul_i32 s44, s36, 8448\n"
    "s_mul_i32 s39, s36, 4480\n"
    "s_mov_b32 s43, 0\n"
    "s_mov_b32 s27, 0\n"
    "s_mov_b32 s41, s40\n"
    "s_mov_b32 s42, s40\n"
    "s_mov_b32 s52, 0x80000000\n"
    "s_cmp_eq_u32 s36, 1\n"
    "s_cselect_b32 s68, 0, 0x80000000\n"
    "s_add_u32 m0, s39, 0\n"
    "s_mov_b32 s38, 0\n"
    "buffer_load_dwordx4 v3, s[20:23], s38 offen lds\n"
    "s_add_u32 m0, s39, 1088\n"
    "s_mov_b32 s38, 16384\n"
    "buffer_load_dwordx4 v3, s[20:23], s38 offen lds\n"
    "s_add_u32 m0, s39, 2176\n"
    "s_mov_b32 s38, 32768\n"
    "buffer_load_dwordx4 v3, s[20:23], s38 offen lds\n"
    "s_add_u32 m0, s39, 3264\n"
    "s_mov_b32 s38, 49152\n"
    "buffer_load_dwordx4 v3, s[20:23], s38 offen lds\n"
    "s_add_u32 m0, s39, 17920\n"
    "s_mov_b32 s38, 262144\n"
    "buffer_load_dwordx4 v3, s[20:23], s38 offen lds\n"
    "s_add_u32 m0, s39, 19008\n"
    "s_mov_b32 s38, 278528\n"
    "buffer_load_dwordx4 v3, s[20:23], s38 offen lds\n"
    "s_add_u32 m0, s39, 20096\n"
    "s_mov_b32 s38, 294912\n"
    "buffer_load_dwordx4 v3, s[20:23], s38 offen lds\n"
    "s_add_u32 m0, s39, 21184\n"
    "s_mov_b32 s38, 311296\n"
    "buffer_load_dwordx4 v3, s[20:23], s38 offen lds\n"
    "s_add_u32 m0, s39, 35840\n"
    "s_mov_b32 s38, 524288\n"
    "buffer_load_dwordx4 v3, s[20:23], s38 offen lds\n"
    "s_add_u32 m0, s39, 36928\n"
    "s_mov_b32 s38, 540672\n"
    "buffer_load_dwordx4 v3, s[20:23], s38 offen lds\n"
    "s_add_u32 m0, s39, 38016\n"
    "s_mov_b32 s38, 557056\n"
    "buffer_load_dwordx4 v3, s[20:23], s38 offen lds\n"
    "s_add_u32 m0, s39, 39104\n"
    "s_mov_b32 s38, 573440\n"
    "buffer_load_dwordx4 v3, s[20:23], s38 offen lds\n"
    "s_add_u32 m0, s39, 53760\n"
    "s_mov_b32 s38, 786432\n"
    "buffer_load_dwordx4 v3, s[20:23], s38 offen lds\n"
    "s_add_u32 m0, s39, 54848\n"
    "s_mov_b32 s38, 802816\n"
    "buffer_load_dwordx4 v3, s[20:23], s38 offen lds\n"
    "s_add_u32 m0, s39, 55936\n"
    "s_mov_b32 s38, 819200\n"
    "buffer_load_dwordx4 v3, s[20:23], s38 offen lds\n"
    "s_add_u32 m0, s39, 57024\n"
    "s_mov_b32 s38, 835584\n"
    "buffer_load_dwordx4 v3, s[20:23], s38 offen lds\n"
    "s_add_u32 m0, s39, 71680\n"
    "s_mov_b32 s38, 1048576\n"
    "buffer_load_dwordx4 v3, s[20:23], s38 offen lds\n"
    "s_add_u32 m0, s39, 72768\n"
    "s_mov_b32 s38, 1064960\n"
    "buffer_load_dwordx4 v3, s[20:23], s38 offen lds\n"
    "s_add_u32 m0, s39, 73856\n"
    "s_mov_b32 s38, 1081344\n"
    "buffer_load_dwordx4 v3, s[20:23], s38 offen lds\n"
    "s_add_u32 m0, s39, 74944\n"
    "s_mov_b32 s38, 1097728\n"
    "buffer_load_dwordx4 v3, s[20:23], s38 offen lds\n"
    "s_add_u32 m0, s39, 89600\n"
    "s_mov_b32 s38, 1310720\n"
    "buffer_load_dwordx4 v3, s[20:23], s38 offen lds\n"
    "s_add_u32 m0, s39, 90688\n"
    "s_mov_b32 s38, 1327104\n"
    "buffer_load_dwordx4 v3, s[20:23], s38 offen lds\n"
    "s_add_u32 m0, s39, 91776\n"
    "s_mov_b32 s38, 1343488\n"
    "buffer_load_dwordx4 v3, s[20:23], s38 offen lds\n"
    "s_add_u32 m0, s39, 92864\n"
    "s_mov_b32 s38, 1359872\n"
    "buffer_load_dwordx4 v3, s[20:23], s38 offen lds\n"
    "s_add_u32 m0, s39, 107520\n"
    "s_mov_b32 s38, 1572864\n"
    "buffer_load_dwordx4 v3, s[20:23], s38 offen lds\n"
    "s_add_u32 m0, s39, 108608\n"
    "s_mov_b32 s38, 1589248\n"
    "buffer_load_dwordx4 v3, s[20:23], s38 offen lds\n"
    "s_add_u32 m0, s39, 109696\n"
    "s_mov_b32 s38, 1605632\n"
    "buffer_load_dwordx4 v3, s[20:23], s38 offen lds\n"
    "s_add_u32 m0, s39, 110784\n"
    "s_mov_b32 s38, 1622016\n"
    "buffer_load_dwordx4 v3, s[20:23], s38 offen lds\n"
    "s_waitcnt vmcnt(24)\n"
    "s_barrier\n"
    "s_add_u32 m0, s39, 125440\n"
    "s_mov_b32 s38, 1835008\n"
    "buffer_load_dwordx4 v3, s[20:23], s38 offen lds\n"
    "s_add_u32 m0, s39, 126528\n"
    "s_mov_b32 s38, 1851392\n"
    "buffer_load_dwordx4 v3, s[20:23], s38 offen lds\n"
    "s_add_u32 m0, s39, 127616\n"
    "s_mov_b32 s38, 1867776\n"
    "buffer_load_dwordx4 v3, s[20:23], s38 offen lds\n"
    "s_add_u32 m0, s39, 128704\n"
    "s_mov_b32 s38, 1884160\n"
    "buffer_load_dwordx4 v3, s[20:23], s38 offen lds\n"
    "v_add_u32 v8, 0, v4\n"
    "v_xor_b32 v9, 32, v8\n"
    "ds_read_b64_tr_b16 a[0:1], v8 offset:0\n"
    "ds_read_b64_tr_b16 a[2:3], v8 offset:2176\n"
    "ds_read_b64_tr_b16 a[4:5], v9 offset:0\n"
    "ds_read_b64_tr_b16 a[6:7], v9 offset:2176\n"
    "ds_read_b64_tr_b16 a[128:129], v8 offset:256\n"
    "ds_read_b64_tr_b16 a[130:131], v8 offset:2432\n"
    "ds_read_b64_tr_b16 a[132:133], v9 offset:256\n"
    "ds_read_b64_tr_b16 a[134:135], v9 offset:2432\n"
    "s_waitcnt lgkmcnt(0)\n"
    "s_waitcnt vmcnt(24)\n"
    "s_barrier\n"
    "s_add_u32 m0, s39, 0\n"
    "s_mov_b32 s38, 2097152\n"
    "buffer_load_dwordx4 v3, s[20:23], s38 offen lds\n"
    "s_add_u32 m0, s39, 1088\n"
    "s_mov_b32 s38, 2113536\n"
    "buffer_load_dwordx4 v3, s[20:23], s38 offen lds\n"
    "s_add_u32 m0, s39, 2176\n"
    "s_mov_b32 s38, 2129920\n"
    "buffer_load_dwordx4 v3, s[20:23], s38 offen lds\n"
    "s_add_u32 m0, s39, 3264\n"
    "s_mov_b32 s38, 2146304\n"
    "buffer_load_dwordx4 v3, s[20:23], s38 offen lds\n"
    "v_add_u32 v8, 17920, v4\n"
    "v_xor_b32 v9, 32, v8\n"
    "ds_read_b64_tr_b16 a[8:9], v8 offset:0\n"
    "ds_read_b64_tr_b16 a[10:11], v8 offset:2176\n"
    "ds_read_b64_tr_b16 a[12:13], v9 offset:0\n"
    "ds_read_b64_tr_b16 a[14:15], v9 offset:2176\n"
    "ds_read_b64_tr_b16 a[136:137], v8 offset:256\n"
    "ds_read_b64_tr_b16 a[138:139], v8 offset:2432\n"
    "ds_read_b64_tr_b16 a[140:141], v9 offset:256\n"
    "ds_read_b64_tr_b16 a[142:143], v9 offset:2432\n"
    "s_waitcnt lgkmcnt(0)\n"
    "s_waitcnt vmcnt(24)\n"
    "s_barrier\n"
    "s_add_u32 m0, s39, 17920\n"
    "s_mov_b32 s38, 2359296\n"
    "buffer_load_dwordx4 v3, s[20:23], s38 offen lds\n"
    "s_add_u32 m0, s39, 19008\n"
    "s_mov_b32 s38, 2375680\n"
    "buffer_load_dwordx4 v3, s[20:23], s38 offen lds\n"
    "s_add_u32 m0, s39, 20096\n"
    "s_mov_b32 s38, 2392064\n"
    "buffer_load_dwordx4 v3, s[20:23], s38 offen lds\n"
    "s_add_u32 m0, s39, 21184\n"
    "s_mov_b32 s38, 2408448\n"
    "buffer_load_dwordx4 v3, s[20:23], s38 offen lds\n"
    "v_add_u32 v8, 35840, v4\n"
    "v_xor_b32 v9, 32, v8\n"
    "ds_read_b64_tr_b16 a[16:17], v8 offset:0\n"
    "ds_read_b64_tr_b16 a[18:19], v8 offset:2176\n"
    "ds_read_b64_tr_b16 a[20:21], v9 offset:0\n"
    "ds_read_b64_tr_b16 a[22:23], v9 offset:2176\n"
    "ds_read_b64_tr_b16 a[144:145], v8 offset:256\n"
    "ds_read_b64_tr_b16 a[146:147], v8 offset:2432\n"
    "ds_read_b64_tr_b16 a[148:149], v9 offset:256\n"
    "ds_read_b64_tr_b16 a[150:151], v9 offset:2432\n"
    "s_waitcnt lgkmcnt(0)\n"
    "s_waitcnt vmcnt(24)\n"
    "s_barrier\n"
    "s_add_u32 m0, s39, 35840\n"
    "s_mov_b32 s38, 2621440\n"
    "buffer_load_dwordx4 v3, s[20:23], s38 offen lds\n"
    "s_add_u32 m0, s39, 36928\n"
    "s_mov_b32 s38, 2637824\n"
    "buffer_load_dwordx4 v3, s[20:23], s38 offen lds\n"
    "s_add_u32 m0, s39, 38016\n"
    "s_mov_b32 s38, 2654208\n"
    "buffer_load_dwordx4 v3, s[20:23], s38 offen lds\n"
    "s_add_u32 m0, s39, 39104\n"
    "s_mov_b32 s38, 2670592\n"
    "buffer_load_dwordx4 v3, s[20:23], s38 offen lds\n"
    "v_add_u32 v8, 53760, v4\n"
    "v_xor_b32 v9, 32, v8\n"
    "ds_read_b64_tr_b16 a[24:25], v8 offset:0\n"
    "ds_read_b64_tr_b16 a[26:27], v8 offset:2176\n"
    "ds_read_b64_tr_b16 a[28:29], v9 offset:0\n"
    "ds_read_b64_tr_b16 a[30:31], v9 offset:2176\n"
    "ds_read_b64_tr_b16 a[152:153], v8 offset:256\n"
    "ds_read_b64_tr_b16 a[154:155], v8 offset:2432\n"
    "ds_read_b64_tr_b16 a[156:157], v9 offset:256\n"
    "ds_read_b64_tr_b16 a[158:159], v9 offset:2432\n"
    "s_waitcnt lgkmcnt(0)\n"
    "s_waitcnt vmcnt(24)\n"
    "s_barrier\n"
    "s_add_u32 m0, s39, 53760\n"
    "s_mov_b32 s38, 2883584\n"
    "buffer_load_dwordx4 v3, s[20:23], s38 offen lds\n"
    "s_add_u32 m0, s39, 54848\n"
    "s_mov_b32 s38, 2899968\n"
    "buffer_load_dwordx4 v3, s[20:23], s38 offen lds\n"
    "s_add_u32 m0, s39, 55936\n"
    "s_mov_b32 s38, 2916352\n"
    "buffer_load_dwordx4 v3, s[20:23], s38 offen lds\n"
    "s_add_u32 m0, s39, 57024\n"
    "s_mov_b32 s38, 2932736\n"
    "buffer_load_dwordx4 v3, s[20:23], s38 offen lds\n"
    "v_add_u32 v8, 71680, v4\n"
    "v_xor_b32 v9, 32, v8\n"
    "ds_read_b64_tr_b16 a[32:33], v8 offset:0\n"
    "ds_read_b64_tr_b16 a[34:35], v8 offset:2176\n"
    "ds_read_b64_tr_b16 a[36:37], v9 offset:0\n"
    "ds_read_b64_tr_b16 a[38:39], v9 offset:2176\n"
    "ds_read_b64_tr_b16 a[160:161], v8 offset:256\n"
    "ds_read_b64_tr_b16 a[162:163], v8 offset:2432\n"
    "ds_read_b64_tr_b16 a[164:165], v9 offset:256\n"
    "ds_read_b64_tr_b16 a[166:167], v9 offset:2432\n"
    "s_waitcnt lgkmcnt(0)\n"
    "s_waitcnt vmcnt(24)\n"
    "s_barrier\n"
    "s_add_u32 m0, s39, 71680\n"
    "s_mov_b32 s38, 3145728\n"
    "buffer_load_dwordx4 v3, s[20:23], s38 offen lds\n"
    "s_add_u32 m0, s39, 72768\n"
    "s_mov_b32 s38, 3162112\n"
    "buffer_load_dwordx4 v3, s[20:23], s38 offen lds\n"
    "s_add_u32 m0, s39, 73856\n"
    "s_mov_b32 s38, 3178496\n"
    "buffer_load_dwordx4 v3, s[20:23], s38 offen lds\n"
    "s_add_u32 m0, s39, 74944\n"
    "s_mov_b32 s38, 3194880\n"
    "buffer_load_dwordx4 v3, s[20:23], s38 offen lds\n"
    "v_add_u32 v8, 89600, v4\n"
    "v_xor_b32 v9, 32, v8\n"
    "ds_read_b64_tr_b16 a[40:41], v8 offset:0\n"
    "ds_read_b64_tr_b16 a[42:43], v8 offset:2176\n"
    "ds_read_b64_tr_b16 a[44:45], v9 offset:0\n"
    "ds_read_b64_tr_b16 a[46:47], v9 offset:2176\n"
    "ds_read_b64_tr_b16 a[168:169], v8 offset:256\n"
    "ds_read_b64_tr_b16 a[170:171], v8 offset:2432\n"
    "ds_read_b64_tr_b16 a[172:173], v9 offset:256\n"
    "ds_read_b64_tr_b16 a[174:175], v9 offset:2432\n"
    "s_waitcnt lgkmcnt(0)\n"
    "s_waitcnt vmcnt(24)\n"
    "s_barrier\n"
    "s_add_u32 m0, s39, 89600\n"
    "s_mov_b32 s38, 3407872\n"
    "buffer_load_dwordx4 v3, s[20:23], s38 offen lds\n"
    "s_add_u32 m0, s39, 90688\n"
    "s_mov_b32 s38, 3424256\n"
    "buffer_load_dwordx4 v3, s[20:23], s38 offen lds\n"
    "s_add_u32 m0, s39, 91776\n"
    "s_mov_b32 s38, 3440640\n"
    "buffer_load_dwordx4 v3, s[20:23], s38 offen lds\n"
    "s_add_u32 m0, s39, 92864\n"
    "s_mov_b32 s38, 3457024\n"
    "buffer_load_dwordx4 v3, s[20:23], s38 offen lds\n"
    "v_add_u32 v8, 107520, v4\n"
    "v_xor_b32 v9, 32, v8\n"
    "ds_read_b64_tr_b16 a[48:49], v8 offset:0\n"
    "ds_read_b64_tr_b16 a[50:51], v8 offset:2176\n"
    "ds_read_b64_tr_b16 a[52:53], v9 offset:0\n"
    "ds_read_b64_tr_b16 a[54:55], v9 offset:2176\n"
    "ds_read_b64_tr_b16 a[176:177], v8 offset:256\n"
    "ds_read_b64_tr_b16 a[178:179], v8 offset:2432\n"
    "ds_read_b64_tr_b16 a[180:181], v9 offset:256\n"
    "ds_read_b64_tr_b16 a[182:183], v9 offset:2432\n"
    "s_waitcnt lgkmcnt(0)\n"
    "s_waitcnt vmcnt(24)\n"
    "s_barrier\n"
    "s_add_u32 m0, s39, 107520\n"
    "s_mov_b32 s38, 3670016\n"
    "buffer_load_dwordx4 v3, s[20:23], s38 offen lds\n"
    "s_add_u32 m0, s39, 108608\n"
    "s_mov_b32 s38, 3686400\n"
    "buffer_load_dwordx4 v3, s[20:23], s38 offen lds\n"
    "s_add_u32 m0, s39, 109696\n"
    "s_mov_b32 s38, 3702784\n"
    "buffer_load_dwordx4 v3, s[20:23], s38 offen lds\n"
    "s_add_u32 m0, s39, 110784\n"
    "s_mov_b32 s38, 3719168\n"
    "buffer_load_dwordx4 v3, s[20:23], s38 offen lds\n"
    "v_add_u32 v8, 125440, v4\n"
    "v_xor_b32 v9, 32, v8\n"
    "ds_read_b64_tr_b16 a[56:57], v8 offset:0\n"
    "ds_read_b64_tr_b16 a[58:59], v8 offset:2176\n"
    "ds_read_b64_tr_b16 a[60:61], v9 offset:0\n"
    "ds_read_b64_tr_b16 a[62:63], v9 offset:2176\n"
    "ds_read_b64_tr_b16 a[184:185], v8 offset:256\n"
    "ds_read_b64_tr_b16 a[186:187], v8 offset:2432\n"
    "ds_read_b64_tr_b16 a[188:189], v9 offset:256\n"
    "ds_read_b64_tr_b16 a[190:191], v9 offset:2432\n"
    "s_waitcnt lgkmcnt(0)\n"
    "s_waitcnt vmcnt(24)\n"
    "s_barrier\n"
    "s_add_u32 m0, s39, 125440\n"
    "s_mov_b32 s38, 3932160\n"
    "buffer_load_dwordx4 v3, s[20:23], s38 offen lds\n"
    "s_add_u32 m0, s39, 126528\n"
    "s_mov_b32 s38, 3948544\n"
    "buffer_load_dwordx4 v3, s[20:23], s38 offen lds\n"
    "s_add_u32 m0, s39, 127616\n"
    "s_mov_b32 s38, 3964928\n"
    "buffer_load_dwordx4 v3, s[20:23], s38 offen lds\n"
    "s_add_u32 m0, s39, 128704\n"
    "s_mov_b32 s38, 3981312\n"
    "buffer_load_dwordx4 v3, s[20:23], s38 offen lds\n"
    "v_add_u32 v8, 0, v4\n"
    "v_xor_b32 v9, 32, v8\n"
    "ds_read_b64_tr_b16 a[64:65], v8 offset:0\n"
    "ds_read_b64_tr_b16 a[66:67], v8 offset:2176\n"
    "ds_read_b64_tr_b16 a[68:69], v9 offset:0\n"
    "ds_read_b64_tr_b16 a[70:71], v9 offset:2176\n"
    "ds_read_b64_tr_b16 a[192:193], v8 offset:256\n"
    "ds_read_b64_tr_b16 a[194:195], v8 offset:2432\n"
    "ds_read_b64_tr_b16 a[196:197], v9 offset:256\n"
    "ds_read_b64_tr_b16 a[198:199], v9 offset:2432\n"
    "s_waitcnt lgkmcnt(0)\n"
    "s_waitcnt vmcnt(24)\n"
    "s_barrier\n"
    "v_add_u32 v8, 17920, v4\n"
    "v_xor_b32 v9, 32, v8\n"
    "ds_read_b64_tr_b16 a[72:73], v8 offset:0\n"
    "ds_read_b64_tr_b16 a[74:75], v8 offset:2176\n"
    "ds_read_b64_tr_b16 a[76:77], v9 offset:0\n"
    "ds_read_b64_tr_b16 a[78:79], v9 offset:2176\n"
    "ds_read_b64_tr_b16 a[200:201], v8 offset:256\n"
    "ds_read_b64_tr_b16 a[202:203], v8 offset:2432\n"
    "ds_read_b64_tr_b16 a[204:205], v9 offset:256\n"
    "ds_read_b64_tr_b16 a[206:207], v9 offset:2432\n"
    "s_waitcnt lgkmcnt(0)\n"
    "s_waitcnt vmcnt(20)\n"
    "s_barrier\n"
    "v_add_u32 v8, 35840, v4\n"
    "v_xor_b32 v9, 32, v8\n"
    "ds_read_b64_tr_b16 a[80:81], v8 offset:0\n"
    "ds_read_b64_tr_b16 a[82:83], v8 offset:2176\n"
    "ds_read_b64_tr_b16 a[84:85], v9 offset:0\n"
    "ds_read_b64_tr_b16 a[86:87], v9 offset:2176\n"
    "ds_read_b64_tr_b16 a[208:209], v8 offset:256\n"
    "ds_read_b64_tr_b16 a[210:211], v8 offset:2432\n"
    "ds_read_b64_tr_b16 a[212:213], v9 offset:256\n"
    "ds_read_b64_tr_b16 a[214:215], v9 offset:2432\n"
    "s_waitcnt lgkmcnt(0)\n"
    "s_waitcnt vmcnt(16)\n"
    "s_barrier\n"
    "v_add_u32 v8, 53760, v4\n"
    "v_xor_b32 v9, 32, v8\n"
    "ds_read_b64_tr_b16 a[88:89], v8 offset:0\n"
    "ds_read_b64_tr_b16 a[90:91], v8 offset:2176\n"
    "ds_read_b64_tr_b16 a[92:93], v9 offset:0\n"
    "ds_read_b64_tr_b16 a[94:95], v9 offset:2176\n"
    "ds_read_b64_tr_b16 a[216:217], v8 offset:256\n"
    "ds_read_b64_tr_b16 a[218:219], v8 offset:2432\n"
    "ds_read_b64_tr_b16 a[220:221], v9 offset:256\n"
    "ds_read_b64_tr_b16 a[222:223], v9 offset:2432\n"
    "s_waitcnt lgkmcnt(0)\n"
    "s_waitcnt vmcnt(12)\n"
    "s_barrier\n"
    "v_add_u32 v8, 71680, v4\n"
    "v_xor_b32 v9, 32, v8\n"
    "ds_read_b64_tr_b16 a[96:97], v8 offset:0\n"
    "ds_read_b64_tr_b16 a[98:99], v8 offset:2176\n"
    "ds_read_b64_tr_b16 a[100:101], v9 offset:0\n"
    "ds_read_b64_tr_b16 a[102:103], v9 offset:2176\n"
    "ds_read_b64_tr_b16 a[224:225], v8 offset:256\n"
    "ds_read_b64_tr_b16 a[226:227], v8 offset:2432\n"
    "ds_read_b64_tr_b16 a[228:229], v9 offset:256\n"
    "ds_read_b64_tr_b16 a[230:231], v9 offset:2432\n"
    "s_waitcnt lgkmcnt(0)\n"
    "s_waitcnt vmcnt(8)\n"
    "s_barrier\n"
    "v_add_u32 v8, 89600, v4\n"
    "v_xor_b32 v9, 32, v8\n"
    "ds_read_b64_tr_b16 a[104:105], v8 offset:0\n"
    "ds_read_b64_tr_b16 a[106:107], v8 offset:2176\n"
    "ds_read_b64_tr_b16 a[108:109], v9 offset:0\n"
    "ds_read_b64_tr_b16 a[110:111], v9 offset:2176\n"
    "ds_read_b64_tr_b16 a[232:233], v8 offset:256\n"
    "ds_read_b64_tr_b16 a[234:235], v8 offset:2432\n"
    "ds_read_b64_tr_b16 a[236:237], v9 offset:256\n"
    "ds_read_b64_tr_b16 a[238:239], v9 offset:2432\n"
    "s_waitcnt lgkmcnt(0)\n"
    "s_waitcnt vmcnt(4)\n"
    "s_barrier\n"
    "v_add_u32 v8, 107520, v4\n"
    "v_xor_b32 v9, 32, v8\n"
    "ds_read_b64_tr_b16 a[112:113], v8 offset:0\n"
    "ds_read_b64_tr_b16 a[114:115], v8 offset:2176\n"
    "ds_read_b64_tr_b16 a[116:117], v9 offset:0\n"
    "ds_read_b64_tr_b16 a[118:119], v9 offset:2176\n"
    "ds_read_b64_tr_b16 a[240:241], v8 offset:256\n"
    "ds_read_b64_tr_b16 a[242:243], v8 offset:2432\n"
    "ds_read_b64_tr_b16 a[244:245], v9 offset:256\n"
    "ds_read_b64_tr_b16 a[246:247], v9 offset:2432\n"
    "s_waitcnt lgkmcnt(0)\n"
    "s_waitcnt vmcnt(0)\n"
    "s_barrier\n"
    "v_add_u32 v8, 125440, v4\n"
    "v_xor_b32 v9, 32, v8\n"
    "ds_read_b64_tr_b16 a[120:121], v8 offset:0\n"
    "ds_read_b64_tr_b16 a[122:123], v8 offset:2176\n"
    "ds_read_b64_tr_b16 a[124:125], v9 offset:0\n"
    "ds_read_b64_tr_b16 a[126:127], v9 offset:2176\n"
    "ds_read_b64_tr_b16 a[248:249], v8 offset:256\n"
    "ds_read_b64_tr_b16 a[250:251], v8 offset:2432\n"
    "ds_read_b64_tr_b16 a[252:253], v9 offset:256\n"
    "ds_read_b64_tr_b16 a[254:255], v9 offset:2432\n"
    "s_waitcnt lgkmcnt(0)\n"
    "s_lshl_b32 s38, s61, 7\n"
    "v_mov_b32 v8, s38\n"
    "s_mov_b32 s37, 0x80000\n"
    "LRY%=:\n"
    "global_load_dword v6, v8, s[24:25] offset:640 sc1\n"
    "s_waitcnt vmcnt(0)\n"
    "v_readfirstlane_b32 s60, v6\n"
    "s_cmp_lg_u32 s60, 0\n"
    "s_cbranch_scc1 LRZ%=\n"
    "s_sleep 2\n"
    "s_sub_u32 s37, s37, 1\n"
    "s_cbranch_scc0 LRY%=\n"
    "s_endpgm\n"
    "LRZ%=:\n"
    "s_lshl_b32 s38, s60, 13\n"
    "s_and_b32 s38, s38, 0x2000\n"
    "s_xor_b32 s48, s48, s38\n"
    "s_xor_b32 s49, s49, s38\n"
    "s_xor_b32 s50, s50, s38\n"
    "s_xor_b32 s51, s51, s38\n"
    "s_xor_b32 s53, s53, s38\n"
    "s_xor_b32 s26, s26, s38\n"
    "s_barrier\n"
    "v_bfe_u32 v9, v0, 4, 1\n"
    "v_lshlrev_b32 v4, 8, v9\n"
    "v_lshlrev_b32 v9, 14, v9\n"
    "s_mov_b32 s63, s45\n"
    "s_lshr_b32 s38, s63, 8\n"
    "s_xor_b32 s38, s38, s48\n"
    "s_and_b32 s38, s38, 0x2000\n"
    "s_or_b32 s63, s63, s38\n"
    "s_add_u32 s39, s44, s43\n"
    "s_add_u32 s69, s63, 4096\n"
    "s_add_u32 m0, s39, 0\n"
    "s_nop 0\n"
    "buffer_load_dwordx4 v0, s[16:19], s63 offen offset:0 sc1 lds\n"
    "s_add_u32 m0, s39, 32\n"
    "s_nop 0\n"
    "buffer_load_dwordx4 v0, s[16:19], s63 offen offset:1024 sc1 lds\n"
    "s_add_u32 m0, s39, 64\n"
    "s_nop 0\n"
    "buffer_load_dwordx4 v0, s[16:19], s63 offen offset:2048 sc1 lds\n"
    "s_add_u32 m0, s39, 96\n"
    "s_nop 0\n"
    "buffer_load_dwordx4 v0, s[16:19], s63 offen offset:3072 sc1 lds\n"
    "s_add_u32 m0, s39, 4224\n"
    "s_nop 0\n"
    "buffer_load_dwordx4 v0, s[16:19], s69 offen offset:0 sc1 lds\n"
    "s_add_u32 m0, s39, 4256\n"
    "s_nop 0\n"
    "buffer_load_dwordx4 v0, s[16:19], s69 offen offset:1024 sc1 lds\n"
    "s_add_u32 m0, s39, 4288\n"
    "s_nop 0\n"
    "buffer_load_dwordx4 v0, s[16:19], s69 offen offset:2048 sc1 lds\n"
    "s_add_u32 m0, s39, 4320\n"
    "s_nop 0\n"
    "buffer_load_dwordx4 v0, s[16:19], s69 offen offset:3072 sc1 lds\n"
    "s_add_u32 s45, s45, 131072\n"
    "v_mov_b32 v10, 101376\n"
    "v_lshl_add_u32 v11, s36, 3, v10\n"
    "LT%=:\n"
    "s_cmp_lg_u32 s36, 0\n"
    "s_cbranch_scc1 LB%=\n"
    "s_mov_b32 s37, 0x80000\n"
    "LP%=:\n"
    "buffer_load_dword v6, off, s[16:19], s53 sc1\n"
    "s_waitcnt vmcnt(0)\n"
    "v_readfirstlane_b32 s38, v6\n"
    "s_mov_b32 s60, 3\n"
    "s_cmp_ge_u32 s41, 64\n"
    "s_cbranch_scc1 LD%=\n"
    "s_mov_b32 s60, 2\n"
    "s_cmp_lg_u32 s38, 0\n"
    "s_cbranch_scc1 LD%=\n"
    "s_add_u32 s38, s41, 8\n"
    "s_cmp_lt_u32 s41, 2\n"
    "s_cselect_b32 s38, 12, s38\n"
    "s_min_u32 s38, s38, 64\n"
    "s_mov_b32 s60, 1\n"
    "s_cmp_lt_u32 s40, s38\n"
    "s_cbranch_scc1 LD%=\n"
    "s_mov_b32 s60, 0\n"
    "s_cmp_lt_u32 s52, 0x80000000\n"
    "s_cbranch_scc1 LD%=\n"
    "s_sleep 1\n"
    "s_sub_u32 s37, s37, 1\n"
    "s_cbranch_scc0 LP%=\n"
    "buffer_store_dword v10, off, s[16:19], s58 sc1\n"
    "s_mov_b32 s60, 3\n"
    "LD%=:\n"
    "v_mov_b32 v6, s60\n"
    "ds_write_b32 v10, v6\n"
    "LB%=:\n"
    "s_waitcnt vmcnt(0) lgkmcnt(0)\n"
    "s_barrier\n"
    "ds_read_b32 v6, v10\n"
    "v_readfirstlane_b32 s38, v5\n"
    "s_or_b32 s52, s52, s68\n"
    "s_or_b32 s38, s38, s68\n"
    "s_cbranch_scc0 LK0_%=\n"
    "LJ0_%=:\n"
    "buffer_store_dword v5, v9, s[16:19], s52 offen sc1\n"
    "s_mov_b32 s52, 0x80000000\n"
    "s_lshl_b32 s63, s41, 16\n"
    "s_add_u32 s63, s63, s46\n"
    "s_lshr_b32 s38, s63, 8\n"
    "s_xor_b32 s38, s38, s48\n"
    "s_and_b32 s38, s38, 0x2000\n"
    "s_or_b32 s63, s63, s38\n"
    "s_waitcnt lgkmcnt(0)\n"
    "v_readfirstlane_b32 s60, v6\n"
    "s_cmp_eq_u32 s60, 1\n"
    "s_cbranch_scc1 L1%=\n"
    "s_cmp_eq_u32 s60, 2\n"
    "s_cbranch_scc0 LX%=\n"
    "s_add_u32 s39, s44, 67584\n"
    "s_add_u32 s69, s63, 4096\n"
    "s_add_u32 m0, s39, 0\n"
    "s_nop 0\n"
    "buffer_load_dwordx4 v0, s[16:19], s63 offen offset:0 sc1 lds\n"
    "s_add_u32 m0, s39, 32\n"
    "s_nop 0\n"
    "buffer_load_dwordx4 v0, s[16:19], s63 offen offset:1024 sc1 lds\n"
    "s_add_u32 m0, s39, 64\n"
    "s_nop 0\n"
    "buffer_load_dwordx4 v0, s[16:19], s63 offen offset:2048 sc1 lds\n"
    "s_add_u32 m0, s39, 96\n"
    "s_nop 0\n"
    "buffer_load_dwordx4 v0, s[16:19], s63 offen offset:3072 sc1 lds\n"
    "s_add_u32 m0, s39, 4224\n"
    "s_nop 0\n"
    "buffer_load_dwordx4 v0, s[16:19], s69 offen offset:0 sc1 lds\n"
    "s_add_u32 m0, s39, 4256\n"
    "s_nop 0\n"
    "buffer_load_dwordx4 v0, s[16:19], s69 offen offset:1024 sc1 lds\n"
    "s_add_u32 m0, s39, 4288\n"
    "s_nop 0\n"
    "buffer_load_dwordx4 v0, s[16:19], s69 offen offset:2048 sc1 lds\n"
    "s_add_u32 m0, s39, 4320\n"
    "s_nop 0\n"
    "buffer_load_dwordx4 v0, s[16:19], s69 offen offset:3072 sc1 lds\n"
    "s_waitcnt vmcnt(0)\n"
    "L2%=:\n"
    "s_mul_i32 s59, s41, 2621440\n"
    "s_add_u32 s59, s59, s47\n"
    "s_lshr_b32 s38, s59, 8\n"
    "s_xor_b32 s38, s38, s49\n"
    "s_and_b32 s38, s38, 0x2000\n"
    "s_or_b32 s59, s59, s38\n"
    "s_lshl_b32 s64, s41, 7\n"
    "s_add_u32 s64, s64, s51\n"
    "s_lshl_b32 s67, s41, 2\n"
    "s_add_u32 s67, s67, s26\n"
    "s_add_u32 s41, s41, 2\n"
    "s_add_u32 s53, s53, 8\n"
    "s_mov_b32 s60, 2\n"
    "LG%=:\n"
    "v_add_u32 v8, 67584, v1\n"
    "s_waitcnt vmcnt(2)\n"
    "s_barrier\n"
    "s_cmp_eq_u32 s61, 1\n"
    "s_cbranch_scc1 LHV%=\n"
    "s_add_u32 s62, s59, 4096\n"
    "ds_read_b128 v[20:23], v8 offset:0\n"
    "ds_read_b128 v[24:27], v8 offset:16896\n"
    "ds_read_b128 v[28:31], v8 offset:64\n"
    "ds_read_b128 v[32:35], v8 offset:16960\n"
    "ds_read_b128 v[36:39], v8 offset:128\n"
    "ds_read_b128 v[40:43], v8 offset:17024\n"
    "ds_read_b128 v[44:47], v8 offset:192\n"
    "ds_read_b128 v[48:51], v8 offset:17088\n"
    "s_waitcnt lgkmcnt(7)\n"
    "v_mfma_f32_16x16x32_bf16 v[64:67], a[0:3], v[20:23], 0\n"
    "ds_read_b128 v[52:55], v8 offset:256\n"
    "s_waitcnt lgkmcnt(7)\n"
    "v_mfma_f32_16x16x32_bf16 v[68:71], a[0:3], v[24:27], 0\n"
    "ds_read_b128 v[56:59], v8 offset:17152\n"
    "v_max3_u32 v6, v23, v27, v23\n"
    "v_mfma_f32_16x16x32_bf16 v[76:79], a[4:7], v[24:27], 0\n"
    "v_mfma_f32_16x16x32_bf16 v[72:75], a[4:7], v[20:23], 0\n"
    "s_waitcnt lgkmcnt(7)\n"
    "v_mfma_f32_16x16x32_bf16 v[64:67], a[8:11], v[28:31], v[64:67]\n"
    "ds_read_b128 v[20:23], v8 offset:320\n"
    "s_waitcnt lgkmcnt(7)\n"
    "v_mfma_f32_16x16x32_bf16 v[68:71], a[8:11], v[32:35], v[68:71]\n"
    "ds_read_b128 v[24:27], v8 offset:17216\n"
    "v_max3_u32 v6, v31, v35, v6\n"
    "v_mfma_f32_16x16x32_bf16 v[76:79], a[12:15], v[32:35], v[76:79]\n"
    "v_mfma_f32_16x16x32_bf16 v[72:75], a[12:15], v[28:31], v[72:75]\n"
    "s_waitcnt lgkmcnt(7)\n"
    "v_mfma_f32_16x16x32_bf16 v[64:67], a[16:19], v[36:39], v[64:67]\n"
    "ds_read_b128 v[28:31], v8 offset:384\n"
    "s_waitcnt lgkmcnt(7)\n"
    "v_mfma_f32_16x16x32_bf16 v[68:71], a[16:19], v[40:43], v[68:71]\n"
    "ds_read_b128 v[32:35], v8 offset:17280\n"
    "v_max3_u32 v6, v39, v43, v6\n"
    "v_mfma_f32_16x16x32_bf16 v[76:79], a[20:23], v[40:43], v[76:79]\n"
    "v_mfma_f32_16x16x32_bf16 v[72:75], a[20:23], v[36:39], v[72:75]\n"
    "s_waitcnt lgkmcnt(7)\n"
    "v_mfma_f32_16x16x32_bf16 v[64:67], a[24:27], v[44:47], v[64:67]\n"
    "ds_read_b128 v[36:39], v8 offset:448\n"
    "s_waitcnt lgkmcnt(7)\n"
    "v_mfma_f32_16x16x32_bf16 v[68:71], a[24:27], v[48:51], v[68:71]\n"
    "ds_read_b128 v[40:43], v8 offset:17344\n"
    "v_max3_u32 v6, v47, v51, v6\n"
    "v_mfma_f32_16x16x32_bf16 v[76:79], a[28:31], v[48:51], v[76:79]\n"
    "v_mfma_f32_16x16x32_bf16 v[72:75], a[28:31], v[44:47], v[72:75]\n"
    "s_waitcnt lgkmcnt(7)\n"
    "v_mfma_f32_16x16x32_bf16 v[64:67], a[32:35], v[52:55], v[64:67]\n"
    "ds_read_b128 v[44:47], v8 offset:512\n"
    "s_waitcnt lgkmcnt(7)\n"
    "v_mfma_f32_16x16x32_bf16 v[68:71], a[32:35], v[56:59], v[68:71]\n"
    "ds_read_b128 v[48:51], v8 offset:17408\n"
    "v_max3_u32 v6, v55, v59, v6\n"
    "v_mfma_f32_16x16x32_bf16 v[76:79], a[36:39], v[56:59], v[76:79]\n"
    "v_mfma_f32_16x16x32_bf16 v[72:75], a[36:39], v[52:55], v[72:75]\n"
    "s_waitcnt lgkmcnt(7)\n"
    "v_mfma_f32_16x16x32_bf16 v[64:67], a[40:43], v[20:23], v[64:67]\n"
    "ds_read_b128 v[52:55], v8 offset:576\n"
    "s_waitcnt lgkmcnt(7)\n"
    "v_mfma_f32_16x16x32_bf16 v[68:71], a[40:43], v[24:27], v[68:71]\n"
    "ds_read_b128 v[56:59], v8 offset:17472\n"
    "v_max3_u32 v6, v23, v27, v6\n"
    "v_mfma_f32_16x16x32_bf16 v[76:79], a[44:47], v[24:27], v[76:79]\n"
    "v_mfma_f32_16x16x32_bf16 v[72:75], a[44:47], v[20:23], v[72:75]\n"
    "s_waitcnt lgkmcnt(7)\n"
    "v_mfma_f32_16x16x32_bf16 v[64:67], a[48:51], v[28:31], v[64:67]\n"
    "ds_read_b128 v[20:23], v8 offset:640\n"
    "s_waitcnt lgkmcnt(7)\n"
    "v_mfma_f32_16x16x32_bf16 v[68:71], a[48:51], v[32:35], v[68:71]\n"
    "ds_read_b128 v[24:27], v8 offset:17536\n"
    "v_max3_u32 v6, v31, v35, v6\n"
    "v_mfma_f32_16x16x32_bf16 v[76:79], a[52:55], v[32:35], v[76:79]\n"
    "v_mfma_f32_16x16x32_bf16 v[72:75], a[52:55], v[28:31], v[72:75]\n"
    "s_waitcnt lgkmcnt(7)\n"
    "v_mfma_f32_16x16x32_bf16 v[64:67], a[56:59], v[36:39], v[64:67]\n"
    "ds_read_b128 v[28:31], v8 offset:704\n"
    "s_waitcnt lgkmcnt(7)\n"
    "v_mfma_f32_16x16x32_bf16 v[68:71], a[56:59], v[40:43], v[68:71]\n"
    "ds_read_b128 v[32:35], v8 offset:17600\n"
    "v_max3_u32 v6, v39, v43, v6\n"
    "v_mfma_f32_16x16x32_bf16 v[76:79], a[60:63], v[40:43], v[76:79]\n"
    "v_mfma_f32_16x16x32_bf16 v[72:75], a[60:63], v[36:39], v[72:75]\n"
    "s_waitcnt lgkmcnt(7)\n"
    "v_mfma_f32_16x16x32_bf16 v[64:67], a[64:67], v[44:47], v[64:67]\n"
    "ds_read_b128 v[36:39], v8 offset:768\n"
    "s_waitcnt lgkmcnt(7)\n"
    "v_mfma_f32_16x16x32_bf16 v[68:71], a[64:67], v[48:51], v[68:71]\n"
    "ds_read_b128 v[40:43], v8 offset:17664\n"
    "v_max3_u32 v6, v47, v51, v6\n"
    "v_mfma_f32_16x16x32_bf16 v[76:79], a[68:71], v[48:51], v[76:79]\n"
    "v_mfma_f32_16x16x32_bf16 v[72:75], a[68:71], v[44:47], v[72:75]\n"
    "s_waitcnt lgkmcnt(7)\n"
    "v_mfma_f32_16x16x32_bf16 v[64:67], a[72:75], v[52:55], v[64:67]\n"
    "ds_read_b128 v[44:47], v8 offset:832\n"
    "s_waitcnt lgkmcnt(7)\n"
    "v_mfma_f32_16x16x32_bf16 v[68:71], a[72:75], v[56:59], v[68:71]\n"
    "ds_read_b128 v[48:51], v8 offset:17728\n"
    "v_max3_u32 v6, v55, v59, v6\n"
    "v_mfma_f32_16x16x32_bf16 v[76:79], a[76:79], v[56:59], v[76:79]\n"
    "v_mfma_f32_16x16x32_bf16 v[72:75], a[76:79], v[52:55], v[72:75]\n"
    "s_waitcnt lgkmcnt(7)\n"
    "v_mfma_f32_16x16x32_bf16 v[64:67], a[80:83], v[20:23], v[64:67]\n"
    "ds_read_b128 v[52:55], v8 offset:896\n"
    "s_waitcnt lgkmcnt(7)\n"
    "v_mfma_f32_16x16x32_bf16 v[68:71], a[80:83], v[24:27], v[68:71]\n"
    "ds_read_b128 v[56:59], v8 offset:17792\n"
    "v_max3_u32 v6, v23, v27, v6\n"
    "v_mfma_f32_16x16x32_bf16 v[76:79], a[84:87], v[24:27], v[76:79]\n"
    "v_mfma_f32_16x16x32_bf16 v[72:75], a[84:87], v[20:23], v[72:75]\n"
    "s_waitcnt lgkmcnt(7)\n"
    "v_mfma_f32_16x16x32_bf16 v[64:67], a[88:91], v[28:31], v[64:67]\n"
    "ds_read_b128 v[20:23], v8 offset:960\n"
    "s_waitcnt lgkmcnt(7)\n"
    "v_mfma_f32_16x16x32_bf16 v[68:71], a[88:91], v[32:35], v[68:71]\n"
    "ds_read_b128 v[24:27], v8 offset:17856\n"
    "v_max3_u32 v6, v31, v35, v6\n"
    "v_mfma_f32_16x16x32_bf16 v[76:79], a[92:95], v[32:35], v[76:79]\n"
    "v_mfma_f32_16x16x32_bf16 v[72:75], a[92:95], v[28:31], v[72:75]\n"
    "s_waitcnt lgkmcnt(7)\n"
    "v_mfma_f32_16x16x32_bf16 v[64:67], a[96:99], v[36:39], v[64:67]\n"
    "ds_read_b128 v[28:31], v8 offset:0\n"
    "s_waitcnt lgkmcnt(7)\n"
    "v_mfma_f32_16x16x32_bf16 v[68:71], a[96:99], v[40:43], v[68:71]\n"
    "ds_read_b128 v[32:35], v8 offset:16896\n"
    "s_waitcnt vmcnt(0)\n"
    "s_barrier\n"
    "v_readfirstlane_b32 s38, v5\n"
    "s_or_b32 s52, s52, s68\n"
    "s_or_b32 s38, s38, s68\n"
    "s_cbranch_scc0 LK1_%=\n"
    "LJ1_%=:\n"
    "buffer_store_dword v5, v9, s[16:19], s52 offen sc1\n"
    "s_mov_b32 s52, 0x80000000\n"
    "v_mfma_f32_16x16x32_bf16 v[76:79], a[100:103], v[40:43], v[76:79]\n"
    "v_max3_u32 v6, v39, v43, v6\n"
    "v_mfma_f32_16x16x32_bf16 v[72:75], a[100:103], v[36:39], v[72:75]\n"
    "s_waitcnt lgkmcnt(7)\n"
    "v_mfma_f32_16x16x32_bf16 v[64:67], a[104:107], v[44:47], v[64:67]\n"
    "ds_read_b128 v[36:39], v8 offset:64\n"
    "s_waitcnt lgkmcnt(7)\n"
    "v_mfma_f32_16x16x32_bf16 v[68:71], a[104:107], v[48:51], v[68:71]\n"
    "ds_read_b128 v[40:43], v8 offset:16960\n"
    "v_max3_u32 v6, v47, v51, v6\n"
    "v_mfma_f32_16x16x32_bf16 v[76:79], a[108:111], v[48:51], v[76:79]\n"
    "v_mfma_f32_16x16x32_bf16 v[72:75], a[108:111], v[44:47], v[72:75]\n"
    "s_waitcnt lgkmcnt(7)\n"
    "v_mfma_f32_16x16x32_bf16 v[64:67], a[112:115], v[52:55], v[64:67]\n"
    "ds_read_b128 v[44:47], v8 offset:128\n"
    "s_waitcnt lgkmcnt(7)\n"
    "v_mfma_f32_16x16x32_bf16 v[68:71], a[112:115], v[56:59], v[68:71]\n"
    "ds_read_b128 v[48:51], v8 offset:17024\n"
    "v_max3_u32 v6, v55, v59, v6\n"
    "v_mfma_f32_16x16x32_bf16 v[76:79], a[116:119], v[56:59], v[76:79]\n"
    "v_mfma_f32_16x16x32_bf16 v[72:75], a[116:119], v[52:55], v[72:75]\n"
    "s_waitcnt lgkmcnt(7)\n"
    "v_mfma_f32_16x16x32_bf16 v[64:67], a[120:123], v[20:23], v[64:67]\n"
    "ds_read_b128 v[52:55], v8 offset:192\n"
    "s_waitcnt lgkmcnt(7)\n"
    "v_mfma_f32_16x16x32_bf16 v[68:71], a[120:123], v[24:27], v[68:71]\n"
    "ds_read_b128 v[56:59], v8 offset:17088\n"
    "v_max3_u32 v6, v23, v27, v6\n"
    "v_mfma_f32_16x16x32_bf16 v[76:79], a[124:127], v[24:27], v[76:79]\n"
    "v_mfma_f32_16x16x32_bf16 v[72:75], a[124:127], v[20:23], v[72:75]\n"
    "s_waitcnt lgkmcnt(7)\n"
    "v_mfma_f32_16x16x32_bf16 v[80:83], a[128:131], v[28:31], 0\n"
    "ds_read_b128 v[20:23], v8 offset:256\n"
    "s_waitcnt lgkmcnt(7)\n"
    "v_mfma_f32_16x16x32_bf16 v[84:87], a[128:131], v[32:35], 0\n"
    "ds_read_b128 v[24:27], v8 offset:17152\n"
    "v_cmp_eq_u32 vcc, -1, v6\n"
    "v_mfma_f32_16x16x32_bf16 v[92:95], a[132:135], v[32:35], 0\n"
    "s_cbranch_vccnz LRD%=\n"
    "v_mfma_f32_16x16x32_bf16 v[88:91], a[132:135], v[28:31], 0\n"
    "s_waitcnt lgkmcnt(7)\n"
    "v_mfma_f32_16x16x32_bf16 v[80:83], a[136:139], v[36:39], v[80:83]\n"
    "ds_read_b128 v[28:31], v8 offset:320\n"
    "s_waitcnt lgkmcnt(7)\n"
    "v_mfma_f32_16x16x32_bf16 v[84:87], a[136:139], v[40:43], v[84:87]\n"
    "ds_read_b128 v[32:35], v8 offset:17216\n"
    "v_cvt_pk_bf16_f32 v12, v64, v72\n"
    "v_mfma_f32_16x16x32_bf16 v[92:95], a[140:143], v[40:43], v[92:95]\n"
    "v_cvt_pk_bf16_f32 v13, v65, v73\n"
    "v_mfma_f32_16x16x32_bf16 v[88:91], a[140:143], v[36:39], v[88:91]\n"
    "s_waitcnt lgkmcnt(7)\n"
    "v_cvt_pk_bf16_f32 v14, v66, v74\n"
    "v_mfma_f32_16x16x32_bf16 v[80:83], a[144:147], v[44:47], v[80:83]\n"
    "ds_read_b128 v[36:39], v8 offset:384\n"
    "s_waitcnt lgkmcnt(7)\n"
    "v_mfma_f32_16x16x32_bf16 v[84:87], a[144:147], v[48:51], v[84:87]\n"
    "ds_read_b128 v[40:43], v8 offset:17280\n"
    "v_cvt_pk_bf16_f32 v15, v67, v75\n"
    "v_mfma_f32_16x16x32_bf16 v[92:95], a[148:151], v[48:51], v[92:95]\n"
    "buffer_store_dwordx4 v[12:15], v0, s[16:19], s59 offen sc1\n"
    "v_mfma_f32_16x16x32_bf16 v[88:91], a[148:151], v[44:47], v[88:91]\n"
    "s_waitcnt lgkmcnt(7)\n"
    "v_cvt_pk_bf16_f32 v16, v68, v76\n"
    "v_mfma_f32_16x16x32_bf16 v[80:83], a[152:155], v[52:55], v[80:83]\n"
    "ds_read_b128 v[44:47], v8 offset:448\n"
    "s_waitcnt lgkmcnt(7)\n"
    "v_mfma_f32_16x16x32_bf16 v[84:87], a[152:155], v[56:59], v[84:87]\n"
    "ds_read_b128 v[48:51], v8 offset:17344\n"
    "v_cvt_pk_bf16_f32 v17, v69, v77\n"
    "v_mfma_f32_16x16x32_bf16 v[92:95], a[156:159], v[56:59], v[92:95]\n"
    "v_cvt_pk_bf16_f32 v18, v70, v78\n"
    "v_mfma_f32_16x16x32_bf16 v[88:91], a[156:159], v[52:55], v[88:91]\n"
    "s_waitcnt lgkmcnt(7)\n"
    "v_cvt_pk_bf16_f32 v19, v71, v79\n"
    "v_mfma_f32_16x16x32_bf16 v[80:83], a[160:163], v[20:23], v[80:83]\n"
    "ds_read_b128 v[52:55], v8 offset:512\n"
    "s_waitcnt lgkmcnt(7)\n"
    "v_mfma_f32_16x16x32_bf16 v[84:87], a[160:163], v[24:27], v[84:87]\n"
    "ds_read_b128 v[56:59], v8 offset:17408\n"
    "buffer_store_dwordx4 v[16:19], v0, s[16:19], s62 offen sc1\n"
    "v_mfma_f32_16x16x32_bf16 v[92:95], a[164:167], v[24:27], v[92:95]\n"
    "s_add_u32 s59, s59, 16384\n"
    "s_add_u32 s62, s62, 16384\n"
    "v_mfma_f32_16x16x32_bf16 v[88:91], a[164:167], v[20:23], v[88:91]\n"
    "s_waitcnt lgkmcnt(7)\n"
    "v_mfma_f32_16x16x32_bf16 v[80:83], a[168:171], v[28:31], v[80:83]\n"
    "ds_read_b128 v[20:23], v8 offset:576\n"
    "s_waitcnt lgkmcnt(7)\n"
    "v_mfma_f32_16x16x32_bf16 v[84:87], a[168:171], v[32:35], v[84:87]\n"
    "ds_read_b128 v[24:27], v8 offset:17472\n"
    "v_mfma_f32_16x16x32_bf16 v[92:95], a[172:175], v[32:35], v[92:95]\n"
    "v_mfma_f32_16x16x32_bf16 v[88:91], a[172:175], v[28:31], v[88:91]\n"
    "s_waitcnt lgkmcnt(7)\n"
    "v_mfma_f32_16x16x32_bf16 v[80:83], a[176:179], v[36:39], v[80:83]\n"
    "ds_read_b128 v[28:31], v8 offset:640\n"
    "s_waitcnt lgkmcnt(7)\n"
    "v_mfma_f32_16x16x32_bf16 v[84:87], a[176:179], v[40:43], v[84:87]\n"
    "ds_read_b128 v[32:35], v8 offset:17536\n"
    "v_mfma_f32_16x16x32_bf16 v[92:95], a[180:183], v[40:43], v[92:95]\n"
    "v_mfma_f32_16x16x32_bf16 v[88:91], a[180:183], v[36:39], v[88:91]\n"
    "s_waitcnt lgkmcnt(7)\n"
    "v_mfma_f32_16x16x32_bf16 v[80:83], a[184:187], v[44:47], v[80:83]\n"
    "ds_read_b128 v[36:39], v8 offset:704\n"
    "s_waitcnt lgkmcnt(7)\n"
    "v_mfma_f32_16x16x32_bf16 v[84:87], a[184:187], v[48:51], v[84:87]\n"
    "ds_read_b128 v[40:43], v8 offset:17600\n"
    "v_mfma_f32_16x16x32_bf16 v[92:95], a[188:191], v[48:51], v[92:95]\n"
    "v_mfma_f32_16x16x32_bf16 v[88:91], a[188:191], v[44:47], v[88:91]\n"
    "s_waitcnt lgkmcnt(7)\n"
    "v_mfma_f32_16x16x32_bf16 v[80:83], a[192:195], v[52:55], v[80:83]\n"
    "ds_read_b128 v[44:47], v8 offset:768\n"
    "s_waitcnt lgkmcnt(7)\n"
    "v_mfma_f32_16x16x32_bf16 v[84:87], a[192:195], v[56:59], v[84:87]\n"
    "ds_read_b128 v[48:51], v8 offset:17664\n"
    "buffer_load_dword v5, off, s[16:19], s67 sc1\n"
    "v_mfma_f32_16x16x32_bf16 v[92:95], a[196:199], v[56:59], v[92:95]\n"
    "v_mfma_f32_16x16x32_bf16 v[88:91], a[196:199], v[52:55], v[88:91]\n"
    "s_waitcnt lgkmcnt(7)\n"
    "v_mfma_f32_16x16x32_bf16 v[80:83], a[200:203], v[20:23], v[80:83]\n"
    "ds_read_b128 v[52:55], v8 offset:832\n"
    "s_waitcnt lgkmcnt(7)\n"
    "v_mfma_f32_16x16x32_bf16 v[84:87], a[200:203], v[24:27], v[84:87]\n"
    "ds_read_b128 v[56:59], v8 offset:17728\n"
    "v_mfma_f32_16x16x32_bf16 v[92:95], a[204:207], v[24:27], v[92:95]\n"
    "v_mfma_f32_16x16x32_bf16 v[88:91], a[204:207], v[20:23], v[88:91]\n"
    "s_waitcnt lgkmcnt(7)\n"
    "v_mfma_f32_16x16x32_bf16 v[80:83], a[208:211], v[28:31], v[80:83]\n"
    "ds_read_b128 v[20:23], v8 offset:896\n"
    "s_waitcnt lgkmcnt(7)\n"
    "v_mfma_f32_16x16x32_bf16 v[84:87], a[208:211], v[32:35], v[84:87]\n"
    "ds_read_b128 v[24:27], v8 offset:17792\n"
    "v_mfma_f32_16x16x32_bf16 v[92:95], a[212:215], v[32:35], v[92:95]\n"
    "v_mfma_f32_16x16x32_bf16 v[88:91], a[212:215], v[28:31], v[88:91]\n"
    "s_waitcnt lgkmcnt(7)\n"
    "v_mfma_f32_16x16x32_bf16 v[80:83], a[216:219], v[36:39], v[80:83]\n"
    "ds_read_b128 v[28:31], v8 offset:960\n"
    "s_waitcnt lgkmcnt(7)\n"
    "v_mfma_f32_16x16x32_bf16 v[84:87], a[216:219], v[40:43], v[84:87]\n"
    "ds_read_b128 v[32:35], v8 offset:17856\n"
    "v_mfma_f32_16x16x32_bf16 v[92:95], a[220:223], v[40:43], v[92:95]\n"
    "v_mfma_f32_16x16x32_bf16 v[88:91], a[220:223], v[36:39], v[88:91]\n"
    "s_waitcnt lgkmcnt(7)\n"
    "v_mfma_f32_16x16x32_bf16 v[80:83], a[224:227], v[44:47], v[80:83]\n"
    "s_waitcnt lgkmcnt(6)\n"
    "v_mfma_f32_16x16x32_bf16 v[84:87], a[224:227], v[48:51], v[84:87]\n"
    "v_mfma_f32_16x16x32_bf16 v[92:95], a[228:231], v[48:51], v[92:95]\n"
    "v_mfma_f32_16x16x32_bf16 v[88:91], a[228:231], v[44:47], v[88:91]\n"
    "s_waitcnt lgkmcnt(5)\n"
    "v_mfma_f32_16x16x32_bf16 v[80:83], a[232:235], v[52:55], v[80:83]\n"
    "s_waitcnt lgkmcnt(4)\n"
    "v_mfma_f32_16x16x32_bf16 v[84:87], a[232:235], v[56:59], v[84:87]\n"
    "v_mfma_f32_16x16x32_bf16 v[92:95], a[236:239], v[56:59], v[92:95]\n"
    "v_mfma_f32_16x16x32_bf16 v[88:91], a[236:239], v[52:55], v[88:91]\n"
    "s_waitcnt lgkmcnt(3)\n"
    "v_mfma_f32_16x16x32_bf16 v[80:83], a[240:243], v[20:23], v[80:83]\n"
    "s_waitcnt lgkmcnt(2)\n"
    "v_mfma_f32_16x16x32_bf16 v[84:87], a[240:243], v[24:27], v[84:87]\n"
    "v_mfma_f32_16x16x32_bf16 v[92:95], a[244:247], v[24:27], v[92:95]\n"
    "v_mfma_f32_16x16x32_bf16 v[88:91], a[244:247], v[20:23], v[88:91]\n"
    "s_waitcnt lgkmcnt(1)\n"
    "v_mfma_f32_16x16x32_bf16 v[80:83], a[248:251], v[28:31], v[80:83]\n"
    "s_waitcnt lgkmcnt(0)\n"
    "v_mfma_f32_16x16x32_bf16 v[84:87], a[248:251], v[32:35], v[84:87]\n"
    "v_mfma_f32_16x16x32_bf16 v[92:95], a[252:255], v[32:35], v[92:95]\n"
    "v_mfma_f32_16x16x32_bf16 v[88:91], a[252:255], v[28:31], v[88:91]\n"
    "s_nop 7\n"
    "v_cvt_pk_bf16_f32 v12, v80, v88\n"
    "v_cvt_pk_bf16_f32 v13, v81, v89\n"
    "v_cvt_pk_bf16_f32 v14, v82, v90\n"
    "v_cvt_pk_bf16_f32 v15, v83, v91\n"
    "buffer_store_dwordx4 v[12:15], v0, s[16:19], s59 offen sc1\n"
    "v_cvt_pk_bf16_f32 v16, v84, v92\n"
    "v_cvt_pk_bf16_f32 v17, v85, v93\n"
    "v_cvt_pk_bf16_f32 v18, v86, v94\n"
    "v_cvt_pk_bf16_f32 v19, v87, v95\n"
    "buffer_store_dwordx4 v[16:19], v0, s[16:19], s62 offen sc1\n"
    "s_mov_b32 s52, s64\n"
    "s_branch LHE%=\n"
    "LHV%=:\n"
    "s_add_u32 s62, s59, 1024\n"
    "ds_read_b128 v[20:23], v8 offset:0\n"
    "ds_read_b128 v[24:27], v8 offset:16896\n"
    "ds_read_b128 v[28:31], v8 offset:64\n"
    "ds_read_b128 v[32:35], v8 offset:16960\n"
    "ds_read_b128 v[36:39], v8 offset:128\n"
    "ds_read_b128 v[40:43], v8 offset:17024\n"
    "ds_read_b128 v[44:47], v8 offset:192\n"
    "ds_read_b128 v[48:51], v8 offset:17088\n"
    "s_waitcnt lgkmcnt(7)\n"
    "v_mfma_f32_16x16x32_bf16 v[64:67], v[20:23], a[0:3], 0\n"
    "ds_read_b128 v[52:55], v8 offset:256\n"
    "s_waitcnt lgkmcnt(7)\n"
    "v_mfma_f32_16x16x32_bf16 v[68:71], v[24:27], a[0:3], 0\n"
    "ds_read_b128 v[56:59], v8 offset:17152\n"
    "v_max3_u32 v6, v23, v27, v23\n"
    "v_mfma_f32_16x16x32_bf16 v[76:79], v[24:27], a[4:7], 0\n"
    "v_mfma_f32_16x16x32_bf16 v[72:75], v[20:23], a[4:7], 0\n"
    "s_waitcnt lgkmcnt(7)\n"
    "v_mfma_f32_16x16x32_bf16 v[64:67], v[28:31], a[8:11], v[64:67]\n"
    "ds_read_b128 v[20:23], v8 offset:320\n"
    "s_waitcnt lgkmcnt(7)\n"
    "v_mfma_f32_16x16x32_bf16 v[68:71], v[32:35], a[8:11], v[68:71]\n"
    "ds_read_b128 v[24:27], v8 offset:17216\n"
    "v_max3_u32 v6, v31, v35, v6\n"
    "v_mfma_f32_16x16x32_bf16 v[76:79], v[32:35], a[12:15], v[76:79]\n"
    "v_mfma_f32_16x16x32_bf16 v[72:75], v[28:31], a[12:15], v[72:75]\n"
    "s_waitcnt lgkmcnt(7)\n"
    "v_mfma_f32_16x16x32_bf16 v[64:67], v[36:39], a[16:19], v[64:67]\n"
    "ds_read_b128 v[28:31], v8 offset:384\n"
    "s_waitcnt lgkmcnt(7)\n"
    "v_mfma_f32_16x16x32_bf16 v[68:71], v[40:43], a[16:19], v[68:71]\n"
    "ds_read_b128 v[32:35], v8 offset:17280\n"
    "v_max3_u32 v6, v39, v43, v6\n"
    "v_mfma_f32_16x16x32_bf16 v[76:79], v[40:43], a[20:23], v[76:79]\n"
    "v_mfma_f32_16x16x32_bf16 v[72:75], v[36:39], a[20:23], v[72:75]\n"
    "s_waitcnt lgkmcnt(7)\n"
    "v_mfma_f32_16x16x32_bf16 v[64:67], v[44:47], a[24:27], v[64:67]\n"
    "ds_read_b128 v[36:39], v8 offset:448\n"
    "s_waitcnt lgkmcnt(7)\n"
    "v_mfma_f32_16x16x32_bf16 v[68:71], v[48:51], a[24:27], v[68:71]\n"
    "ds_read_b128 v[40:43], v8 offset:17344\n"
    "v_max3_u32 v6, v47, v51, v6\n"
    "v_mfma_f32_16x16x32_bf16 v[76:79], v[48:51], a[28:31], v[76:79]\n"
    "v_mfma_f32_16x16x32_bf16 v[72:75], v[44:47], a[28:31], v[72:75]\n"
    "s_waitcnt lgkmcnt(7)\n"
    "v_mfma_f32_16x16x32_bf16 v[64:67], v[52:55], a[32:35], v[64:67]\n"
    "ds_read_b128 v[44:47], v8 offset:512\n"
    "s_waitcnt lgkmcnt(7)\n"
    "v_mfma_f32_16x16x32_bf16 v[68:71], v[56:59], a[32:35], v[68:71]\n"
    "ds_read_b128 v[48:51], v8 offset:17408\n"
    "v_max3_u32 v6, v55, v59, v6\n"
    "v_mfma_f32_16x16x32_bf16 v[76:79], v[56:59], a[36:39], v[76:79]\n"
    "v_mfma_f32_16x16x32_bf16 v[72:75], v[52:55], a[36:39], v[72:75]\n"
    "s_waitcnt lgkmcnt(7)\n"
    "v_mfma_f32_16x16x32_bf16 v[64:67], v[20:23], a[40:43], v[64:67]\n"
    "ds_read_b128 v[52:55], v8 offset:576\n"
    "s_waitcnt lgkmcnt(7)\n"
    "v_mfma_f32_16x16x32_bf16 v[68:71], v[24:27], a[40:43], v[68:71]\n"
    "ds_read_b128 v[56:59], v8 offset:17472\n"
    "v_max3_u32 v6, v23, v27, v6\n"
    "v_mfma_f32_16x16x32_bf16 v[76:79], v[24:27], a[44:47], v[76:79]\n"
    "v_mfma_f32_16x16x32_bf16 v[72:75], v[20:23], a[44:47], v[72:75]\n"
    "s_waitcnt lgkmcnt(7)\n"
    "v_mfma_f32_16x16x32_bf16 v[64:67], v[28:31], a[48:51], v[64:67]\n"
    "ds_read_b128 v[20:23], v8 offset:640\n"
    "s_waitcnt lgkmcnt(7)\n"
    "v_mfma_f32_16x16x32_bf16 v[68:71], v[32:35], a[48:51], v[68:71]\n"
    "ds_read_b128 v[24:27], v8 offset:17536\n"
    "v_max3_u32 v6, v31, v35, v6\n"
    "v_mfma_f32_16x16x32_bf16 v[76:79], v[32:35], a[52:55], v[76:79]\n"
    "v_mfma_f32_16x16x32_bf16 v[72:75], v[28:31], a[52:55], v[72:75]\n"
    "s_waitcnt lgkmcnt(7)\n"
    "v_mfma_f32_16x16x32_bf16 v[64:67], v[36:39], a[56:59], v[64:67]\n"
    "ds_read_b128 v[28:31], v8 offset:704\n"
    "s_waitcnt lgkmcnt(7)\n"
    "v_mfma_f32_16x16x32_bf16 v[68:71], v[40:43], a[56:59], v[68:71]\n"
    "ds_read_b128 v[32:35], v8 offset:17600\n"
    "v_max3_u32 v6, v39, v43, v6\n"
    "v_mfma_f32_16x16x32_bf16 v[76:79], v[40:43], a[60:63], v[76:79]\n"
    "v_mfma_f32_16x16x32_bf16 v[72:75], v[36:39], a[60:63], v[72:75]\n"
    "s_waitcnt lgkmcnt(7)\n"
    "v_mfma_f32_16x16x32_bf16 v[64:67], v[44:47], a[64:67], v[64:67]\n"
    "ds_read_b128 v[36:39], v8 offset:768\n"
    "s_waitcnt lgkmcnt(7)\n"
    "v_mfma_f32_16x16x32_bf16 v[68:71], v[48:51], a[64:67], v[68:71]\n"
    "ds_read_b128 v[40:43], v8 offset:17664\n"
    "v_max3_u32 v6, v47, v51, v6\n"
    "v_mfma_f32_16x16x32_bf16 v[76:79], v[48:51], a[68:71], v[76:79]\n"
    "v_mfma_f32_16x16x32_bf16 v[72:75], v[44:47], a[68:71], v[72:75]\n"
    "s_waitcnt lgkmcnt(7)\n"
    "v_mfma_f32_16x16x32_bf16 v[64:67], v[52:55], a[72:75], v[64:67]\n"
    "ds_read_b128 v[44:47], v8 offset:832\n"
    "s_waitcnt lgkmcnt(7)\n"
    "v_mfma_f32_16x16x32_bf16 v[68:71], v[56:59], a[72:75], v[68:71]\n"
    "ds_read_b128 v[48:51], v8 offset:17728\n"
    "v_max3_u32 v6, v55, v59, v6\n"
    "v_mfma_f32_16x16x32_bf16 v[76:79], v[56:59], a[76:79], v[76:79]\n"
    "v_mfma_f32_16x16x32_bf16 v[72:75], v[52:55], a[76:79], v[72:75]\n"
    "s_waitcnt lgkmcnt(7)\n"
    "v_mfma_f32_16x16x32_bf16 v[64:67], v[20:23], a[80:83], v[64:67]\n"
    "ds_read_b128 v[52:55], v8 offset:896\n"
    "s_waitcnt lgkmcnt(7)\n"
    "v_mfma_f32_16x16x32_bf16 v[68:71], v[24:27], a[80:83], v[68:71]\n"
    "ds_read_b128 v[56:59], v8 offset:17792\n"
    "v_max3_u32 v6, v23, v27, v6\n"
    "v_mfma_f32_16x16x32_bf16 v[76:79], v[24:27], a[84:87], v[76:79]\n"
    "v_mfma_f32_16x16x32_bf16 v[72:75], v[20:23], a[84:87], v[72:75]\n"
    "s_waitcnt lgkmcnt(7)\n"
    "v_mfma_f32_16x16x32_bf16 v[64:67], v[28:31], a[88:91], v[64:67]\n"
    "ds_read_b128 v[20:23], v8 offset:960\n"
    "s_waitcnt lgkmcnt(7)\n"
    "v_mfma_f32_16x16x32_bf16 v[68:71], v[32:35], a[88:91], v[68:71]\n"
    "ds_read_b128 v[24:27], v8 offset:17856\n"
    "v_max3_u32 v6, v31, v35, v6\n"
    "v_mfma_f32_16x16x32_bf16 v[76:79], v[32:35], a[92:95], v[76:79]\n"
    "v_mfma_f32_16x16x32_bf16 v[72:75], v[28:31], a[92:95], v[72:75]\n"
    "s_waitcnt lgkmcnt(7)\n"
    "v_mfma_f32_16x16x32_bf16 v[64:67], v[36:39], a[96:99], v[64:67]\n"
    "ds_read_b128 v[28:31], v8 offset:0\n"
    "s_waitcnt lgkmcnt(7)\n"
    "v_mfma_f32_16x16x32_bf16 v[68:71], v[40:43], a[96:99], v[68:71]\n"
    "ds_read_b128 v[32:35], v8 offset:16896\n"
    "s_waitcnt vmcnt(0)\n"
    "s_barrier\n"
    "v_readfirstlane_b32 s38, v5\n"
    "s_or_b32 s52, s52, s68\n"
    "s_or_b32 s38, s38, s68\n"
    "s_cbranch_scc0 LK2_%=\n"
    "LJ2_%=:\n"
    "buffer_store_dword v5, v9, s[16:19], s52 offen sc1\n"
    "s_mov_b32 s52, 0x80000000\n"
    "v_mfma_f32_16x16x32_bf16 v[76:79], v[40:43], a[100:103], v[76:79]\n"
    "v_max3_u32 v6, v39, v43, v6\n"
    "v_mfma_f32_16x16x32_bf16 v[72:75], v[36:39], a[100:103], v[72:75]\n"
    "s_waitcnt lgkmcnt(7)\n"
    "v_mfma_f32_16x16x32_bf16 v[64:67], v[44:47], a[104:107], v[64:67]\n"
    "ds_read_b128 v[36:39], v8 offset:64\n"
    "s_waitcnt lgkmcnt(7)\n"
    "v_mfma_f32_16x16x32_bf16 v[68:71], v[48:51], a[104:107], v[68:71]\n"
    "ds_read_b128 v[40:43], v8 offset:16960\n"
    "v_max3_u32 v6, v47, v51, v6\n"
    "v_mfma_f32_16x16x32_bf16 v[76:79], v[48:51], a[108:111], v[76:79]\n"
    "v_mfma_f32_16x16x32_bf16 v[72:75], v[44:47], a[108:111], v[72:75]\n"
    "s_waitcnt lgkmcnt(7)\n"
    "v_mfma_f32_16x16x32_bf16 v[64:67], v[52:55], a[112:115], v[64:67]\n"
    "ds_read_b128 v[44:47], v8 offset:128\n"
    "s_waitcnt lgkmcnt(7)\n"
    "v_mfma_f32_16x16x32_bf16 v[68:71], v[56:59], a[112:115], v[68:71]\n"
    "ds_read_b128 v[48:51], v8 offset:17024\n"
    "v_max3_u32 v6, v55, v59, v6\n"
    "v_mfma_f32_16x16x32_bf16 v[76:79], v[56:59], a[116:119], v[76:79]\n"
    "v_mfma_f32_16x16x32_bf16 v[72:75], v[52:55], a[116:119], v[72:75]\n"
    "s_waitcnt lgkmcnt(7)\n"
    "v_mfma_f32_16x16x32_bf16 v[64:67], v[20:23], a[120:123], v[64:67]\n"
    "ds_read_b128 v[52:55], v8 offset:192\n"
    "s_waitcnt lgkmcnt(7)\n"
    "v_mfma_f32_16x16x32_bf16 v[68:71], v[24:27], a[120:123], v[68:71]\n"
    "ds_read_b128 v[56:59], v8 offset:17088\n"
    "v_max3_u32 v6, v23, v27, v6\n"
    "v_mfma_f32_16x16x32_bf16 v[76:79], v[24:27], a[124:127], v[76:79]\n"
    "v_mfma_f32_16x16x32_bf16 v[72:75], v[20:23], a[124:127], v[72:75]\n"
    "s_waitcnt lgkmcnt(7)\n"
    "v_mfma_f32_16x16x32_bf16 v[80:83], v[28:31], a[128:131], 0\n"
    "ds_read_b128 v[20:23], v8 offset:256\n"
    "s_waitcnt lgkmcnt(7)\n"
    "v_mfma_f32_16x16x32_bf16 v[84:87], v[32:35], a[128:131], 0\n"
    "ds_read_b128 v[24:27], v8 offset:17152\n"
    "v_cmp_eq_u32 vcc, -1, v6\n"
    "v_mfma_f32_16x16x32_bf16 v[92:95], v[32:35], a[132:135], 0\n"
    "s_cbranch_vccnz LRD%=\n"
    "v_mfma_f32_16x16x32_bf16 v[88:91], v[28:31], a[132:135], 0\n"
    "s_waitcnt lgkmcnt(7)\n"
    "v_mfma_f32_16x16x32_bf16 v[80:83], v[36:39], a[136:139], v[80:83]\n"
    "ds_read_b128 v[28:31], v8 offset:320\n"
    "s_waitcnt lgkmcnt(7)\n"
    "v_mfma_f32_16x16x32_bf16 v[84:87], v[40:43], a[136:139], v[84:87]\n"
    "ds_read_b128 v[32:35], v8 offset:17216\n"
    "v_cvt_pk_bf16_f32 v12, v64, v65\n"
    "v_mfma_f32_16x16x32_bf16 v[92:95], v[40:43], a[140:143], v[92:95]\n"
    "v_cvt_pk_bf16_f32 v13, v66, v67\n"
    "v_mfma_f32_16x16x32_bf16 v[88:91], v[36:39], a[140:143], v[88:91]\n"
    "s_waitcnt lgkmcnt(7)\n"
    "v_cvt_pk_bf16_f32 v14, v68, v69\n"
    "v_mfma_f32_16x16x32_bf16 v[80:83], v[44:47], a[144:147], v[80:83]\n"
    "ds_read_b128 v[36:39], v8 offset:384\n"
    "s_waitcnt lgkmcnt(7)\n"
    "v_mfma_f32_16x16x32_bf16 v[84:87], v[48:51], a[144:147], v[84:87]\n"
    "ds_read_b128 v[40:43], v8 offset:17280\n"
    "v_cvt_pk_bf16_f32 v15, v70, v71\n"
    "v_mfma_f32_16x16x32_bf16 v[92:95], v[48:51], a[148:151], v[92:95]\n"
    "buffer_store_dwordx4 v[12:15], v0, s[16:19], s59 offen sc1\n"
    "v_mfma_f32_16x16x32_bf16 v[88:91], v[44:47], a[148:151], v[88:91]\n"
    "s_waitcnt lgkmcnt(7)\n"
    "v_cvt_pk_bf16_f32 v16, v72, v73\n"
    "v_mfma_f32_16x16x32_bf16 v[80:83], v[52:55], a[152:155], v[80:83]\n"
    "ds_read_b128 v[44:47], v8 offset:448\n"
    "s_waitcnt lgkmcnt(7)\n"
    "v_mfma_f32_16x16x32_bf16 v[84:87], v[56:59], a[152:155], v[84:87]\n"
    "ds_read_b128 v[48:51], v8 offset:17344\n"
    "v_cvt_pk_bf16_f32 v17, v74, v75\n"
    "v_mfma_f32_16x16x32_bf16 v[92:95], v[56:59], a[156:159], v[92:95]\n"
    "v_cvt_pk_bf16_f32 v18, v76, v77\n"
    "v_mfma_f32_16x16x32_bf16 v[88:91], v[52:55], a[156:159], v[88:91]\n"
    "s_waitcnt lgkmcnt(7)\n"
    "v_cvt_pk_bf16_f32 v19, v78, v79\n"
    "v_mfma_f32_16x16x32_bf16 v[80:83], v[20:23], a[160:163], v[80:83]\n"
    "ds_read_b128 v[52:55], v8 offset:512\n"
    "s_waitcnt lgkmcnt(7)\n"
    "v_mfma_f32_16x16x32_bf16 v[84:87], v[24:27], a[160:163], v[84:87]\n"
    "ds_read_b128 v[56:59], v8 offset:17408\n"
    "buffer_store_dwordx4 v[16:19], v0, s[16:19], s62 offen sc1\n"
    "v_mfma_f32_16x16x32_bf16 v[92:95], v[24:27], a[164:167], v[92:95]\n"
    "s_add_u32 s59, s59, 16384\n"
    "s_add_u32 s62, s62, 16384\n"
    "v_mfma_f32_16x16x32_bf16 v[88:91], v[20:23], a[164:167], v[88:91]\n"
    "s_waitcnt lgkmcnt(7)\n"
    "v_mfma_f32_16x16x32_bf16 v[80:83], v[28:31], a[168:171], v[80:83]\n"
    "ds_read_b128 v[20:23], v8 offset:576\n"
    "s_waitcnt lgkmcnt(7)\n"
    "v_mfma_f32_16x16x32_bf16 v[84:87], v[32:35], a[168:171], v[84:87]\n"
    "ds_read_b128 v[24:27], v8 offset:17472\n"
    "v_mfma_f32_16x16x32_bf16 v[92:95], v[32:35], a[172:175], v[92:95]\n"
    "v_mfma_f32_16x16x32_bf16 v[88:91], v[28:31], a[172:175], v[88:91]\n"
    "s_waitcnt lgkmcnt(7)\n"
    "v_mfma_f32_16x16x32_bf16 v[80:83], v[36:39], a[176:179], v[80:83]\n"
    "ds_read_b128 v[28:31], v8 offset:640\n"
    "s_waitcnt lgkmcnt(7)\n"
    "v_mfma_f32_16x16x32_bf16 v[84:87], v[40:43], a[176:179], v[84:87]\n"
    "ds_read_b128 v[32:35], v8 offset:17536\n"
    "v_mfma_f32_16x16x32_bf16 v[92:95], v[40:43], a[180:183], v[92:95]\n"
    "v_mfma_f32_16x16x32_bf16 v[88:91], v[36:39], a[180:183], v[88:91]\n"
    "s_waitcnt lgkmcnt(7)\n"
    "v_mfma_f32_16x16x32_bf16 v[80:83], v[44:47], a[184:187], v[80:83]\n"
    "ds_read_b128 v[36:39], v8 offset:704\n"
    "s_waitcnt lgkmcnt(7)\n"
    "v_mfma_f32_16x16x32_bf16 v[84:87], v[48:51], a[184:187], v[84:87]\n"
    "ds_read_b128 v[40:43], v8 offset:17600\n"
    "v_mfma_f32_16x16x32_bf16 v[92:95], v[48:51], a[188:191], v[92:95]\n"
    "v_mfma_f32_16x16x32_bf16 v[88:91], v[44:47], a[188:191], v[88:91]\n"
    "s_waitcnt lgkmcnt(7)\n"
    "v_mfma_f32_16x16x32_bf16 v[80:83], v[52:55], a[192:195], v[80:83]\n"
    "ds_read_b128 v[44:47], v8 offset:768\n"
    "s_waitcnt lgkmcnt(7)\n"
    "v_mfma_f32_16x16x32_bf16 v[84:87], v[56:59], a[192:195], v[84:87]\n"
    "ds_read_b128 v[48:51], v8 offset:17664\n"
    "buffer_load_dword v5, off, s[16:19], s67 sc1\n"
    "v_mfma_f32_16x16x32_bf16 v[92:95], v[56:59], a[196:199], v[92:95]\n"
    "v_mfma_f32_16x16x32_bf16 v[88:91], v[52:55], a[196:199], v[88:91]\n"
    "s_waitcnt lgkmcnt(7)\n"
    "v_mfma_f32_16x16x32_bf16 v[80:83], v[20:23], a[200:203], v[80:83]\n"
    "ds_read_b128 v[52:55], v8 offset:832\n"
    "s_waitcnt lgkmcnt(7)\n"
    "v_mfma_f32_16x16x32_bf16 v[84:87], v[24:27], a[200:203], v[84:87]\n"
    "ds_read_b128 v[56:59], v8 offset:17728\n"
    "v_mfma_f32_16x16x32_bf16 v[92:95], v[24:27], a[204:207], v[92:95]\n"
    "v_mfma_f32_16x16x32_bf16 v[88:91], v[20:23], a[204:207], v[88:91]\n"
    "s_waitcnt lgkmcnt(7)\n"
    "v_mfma_f32_16x16x32_bf16 v[80:83], v[28:31], a[208:211], v[80:83]\n"
    "ds_read_b128 v[20:23], v8 offset:896\n"
    "s_waitcnt lgkmcnt(7)\n"
    "v_mfma_f32_16x16x32_bf16 v[84:87], v[32:35], a[208:211], v[84:87]\n"
    "ds_read_b128 v[24:27], v8 offset:17792\n"
    "v_mfma_f32_16x16x32_bf16 v[92:95], v[32:35], a[212:215], v[92:95]\n"
    "v_mfma_f32_16x16x32_bf16 v[88:91], v[28:31], a[212:215], v[88:91]\n"
    "s_waitcnt lgkmcnt(7)\n"
    "v_mfma_f32_16x16x32_bf16 v[80:83], v[36:39], a[216:219], v[80:83]\n"
    "ds_read_b128 v[28:31], v8 offset:960\n"
    "s_waitcnt lgkmcnt(7)\n"
    "v_mfma_f32_16x16x32_bf16 v[84:87], v[40:43], a[216:219], v[84:87]\n"
    "ds_read_b128 v[32:35], v8 offset:17856\n"
    "v_mfma_f32_16x16x32_bf16 v[92:95], v[40:43], a[220:223], v[92:95]\n"
    "v_mfma_f32_16x16x32_bf16 v[88:91], v[36:39], a[220:223], v[88:91]\n"
    "s_waitcnt lgkmcnt(7)\n"
    "v_mfma_f32_16x16x32_bf16 v[80:83], v[44:47], a[224:227], v[80:83]\n"
    "s_waitcnt lgkmcnt(6)\n"
    "v_mfma_f32_16x16x32_bf16 v[84:87], v[48:51], a[224:227], v[84:87]\n"
    "v_mfma_f32_16x16x32_bf16 v[92:95], v[48:51], a[228:231], v[92:95]\n"
    "v_mfma_f32_16x16x32_bf16 v[88:91], v[44:47], a[228:231], v[88:91]\n"
    "s_waitcnt lgkmcnt(5)\n"
    "v_mfma_f32_16x16x32_bf16 v[80:83], v[52:55], a[232:235], v[80:83]\n"
    "s_waitcnt lgkmcnt(4)\n"
    "v_mfma_f32_16x16x32_bf16 v[84:87], v[56:59], a[232:235], v[84:87]\n"
    "v_mfma_f32_16x16x32_bf16 v[92:95], v[56:59], a[236:239], v[92:95]\n"
    "v_mfma_f32_16x16x32_bf16 v[88:91], v[52:55], a[236:239], v[88:91]\n"
    "s_waitcnt lgkmcnt(3)\n"
    "v_mfma_f32_16x16x32_bf16 v[80:83], v[20:23], a[240:243], v[80:83]\n"
    "s_waitcnt lgkmcnt(2)\n"
    "v_mfma_f32_16x16x32_bf16 v[84:87], v[24:27], a[240:243], v[84:87]\n"
    "v_mfma_f32_16x16x32_bf16 v[92:95], v[24:27], a[244:247], v[92:95]\n"
    "v_mfma_f32_16x16x32_bf16 v[88:91], v[20:23], a[244:247], v[88:91]\n"
    "s_waitcnt lgkmcnt(1)\n"
    "v_mfma_f32_16x16x32_bf16 v[80:83], v[28:31], a[248:251], v[80:83]\n"
    "s_waitcnt lgkmcnt(0)\n"
    "v_mfma_f32_16x16x32_bf16 v[84:87], v[32:35], a[248:251], v[84:87]\n"
    "v_mfma_f32_16x16x32_bf16 v[92:95], v[32:35], a[252:255], v[92:95]\n"
    "v_mfma_f32_16x16x32_bf16 v[88:91], v[28:31], a[252:255], v[88:91]\n"
    "v_cvt_pk_bf16_f32 v12, v80, v81\n"
    "v_cvt_pk_bf16_f32 v13, v82, v83\n"
    "v_cvt_pk_bf16_f32 v14, v84, v85\n"
    "v_cvt_pk_bf16_f32 v15, v86, v87\n"
    "buffer_store_dwordx4 v[12:15], v0, s[16:19], s59 offen sc1\n"
    "s_nop 2\n"
    "v_cvt_pk_bf16_f32 v16, v88, v89\n"
    "v_cvt_pk_bf16_f32 v17, v90, v91\n"
    "v_cvt_pk_bf16_f32 v18, v92, v93\n"
    "v_cvt_pk_bf16_f32 v19, v94, v95\n"
    "buffer_store_dwordx4 v[16:19], v0, s[16:19], s62 offen sc1\n"
    "s_mov_b32 s52, s64\n"
    "LHE%=:\n"
    "s_cmp_ge_u32 s40, 64\n"
    "s_cbranch_scc1 LT%=\n"
    "L1%=:\n"
    "v_add_u32 v8, s43, v1\n"
    "s_xor_b32 s43, s43, 33792\n"
    "s_mov_b32 s63, s45\n"
    "s_lshr_b32 s38, s63, 8\n"
    "s_xor_b32 s38, s38, s48\n"
    "s_and_b32 s38, s38, 0x2000\n"
    "s_or_b32 s63, s63, s38\n"
    "s_add_u32 s39, s44, s43\n"
    "s_add_u32 s69, s63, 4096\n"
    "s_add_u32 m0, s39, 0\n"
    "s_nop 0\n"
    "buffer_load_dwordx4 v0, s[16:19], s63 offen offset:0 sc1 lds\n"
    "s_add_u32 m0, s39, 32\n"
    "s_nop 0\n"
    "buffer_load_dwordx4 v0, s[16:19], s63 offen offset:1024 sc1 lds\n"
    "s_add_u32 m0, s39, 64\n"
    "s_nop 0\n"
    "buffer_load_dwordx4 v0, s[16:19], s63 offen offset:2048 sc1 lds\n"
    "s_add_u32 m0, s39, 96\n"
    "s_nop 0\n"
    "buffer_load_dwordx4 v0, s[16:19], s63 offen offset:3072 sc1 lds\n"
    "s_add_u32 m0, s39, 4224\n"
    "s_nop 0\n"
    "buffer_load_dwordx4 v0, s[16:19], s69 offen offset:0 sc1 lds\n"
    "s_add_u32 m0, s39, 4256\n"
    "s_nop 0\n"
    "buffer_load_dwordx4 v0, s[16:19], s69 offen offset:1024 sc1 lds\n"
    "s_add_u32 m0, s39, 4288\n"
    "s_nop 0\n"
    "buffer_load_dwordx4 v0, s[16:19], s69 offen offset:2048 sc1 lds\n"
    "s_add_u32 m0, s39, 4320\n"
    "s_nop 0\n"
    "buffer_load_dwordx4 v0, s[16:19], s69 offen offset:3072 sc1 lds\n"
    "s_add_u32 s45, s45, 131072\n"
    "s_mul_i32 s59, s40, 2621440\n"
    "s_add_u32 s59, s59, s47\n"
    "s_lshr_b32 s38, s59, 8\n"
    "s_xor_b32 s38, s38, s48\n"
    "s_and_b32 s38, s38, 0x2000\n"
    "s_or_b32 s59, s59, s38\n"
    "s_lshl_b32 s64, s40, 7\n"
    "s_add_u32 s64, s64, s50\n"
    "s_add_u32 s40, s40, 2\n"
    "s_cmp_eq_u32 s61, 1\n"
    "s_cbranch_scc1 LIV%=\n"
    "s_add_u32 s62, s59, 4096\n"
    "ds_read_b128 v[20:23], v8 offset:0\n"
    "ds_read_b128 v[24:27], v8 offset:16896\n"
    "ds_read_b128 v[28:31], v8 offset:64\n"
    "ds_read_b128 v[32:35], v8 offset:16960\n"
    "ds_read_b128 v[36:39], v8 offset:128\n"
    "ds_read_b128 v[40:43], v8 offset:17024\n"
    "ds_read_b128 v[44:47], v8 offset:192\n"
    "ds_read_b128 v[48:51], v8 offset:17088\n"
    "s_waitcnt lgkmcnt(7)\n"
    "v_mfma_f32_16x16x32_bf16 v[64:67], a[0:3], v[20:23], 0\n"
    "ds_read_b128 v[52:55], v8 offset:256\n"
    "s_waitcnt lgkmcnt(7)\n"
    "v_mfma_f32_16x16x32_bf16 v[68:71], a[0:3], v[24:27], 0\n"
    "ds_read_b128 v[56:59], v8 offset:17152\n"
    "v_mfma_f32_16x16x32_bf16 v[76:79], a[4:7], v[24:27], 0\n"
    "v_mfma_f32_16x16x32_bf16 v[72:75], a[4:7], v[20:23], 0\n"
    "s_waitcnt lgkmcnt(7)\n"
    "v_mfma_f32_16x16x32_bf16 v[64:67], a[8:11], v[28:31], v[64:67]\n"
    "ds_read_b128 v[20:23], v8 offset:320\n"
    "s_waitcnt lgkmcnt(7)\n"
    "v_mfma_f32_16x16x32_bf16 v[68:71], a[8:11], v[32:35], v[68:71]\n"
    "ds_read_b128 v[24:27], v8 offset:17216\n"
    "v_mfma_f32_16x16x32_bf16 v[76:79], a[12:15], v[32:35], v[76:79]\n"
    "v_mfma_f32_16x16x32_bf16 v[72:75], a[12:15], v[28:31], v[72:75]\n"
    "s_waitcnt lgkmcnt(7)\n"
    "v_mfma_f32_16x16x32_bf16 v[64:67], a[16:19], v[36:39], v[64:67]\n"
    "ds_read_b128 v[28:31], v8 offset:384\n"
    "s_waitcnt lgkmcnt(7)\n"
    "v_mfma_f32_16x16x32_bf16 v[68:71], a[16:19], v[40:43], v[68:71]\n"
    "ds_read_b128 v[32:35], v8 offset:17280\n"
    "v_mfma_f32_16x16x32_bf16 v[76:79], a[20:23], v[40:43], v[76:79]\n"
    "v_mfma_f32_16x16x32_bf16 v[72:75], a[20:23], v[36:39], v[72:75]\n"
    "s_waitcnt lgkmcnt(7)\n"
    "v_mfma_f32_16x16x32_bf16 v[64:67], a[24:27], v[44:47], v[64:67]\n"
    "ds_read_b128 v[36:39], v8 offset:448\n"
    "s_waitcnt lgkmcnt(7)\n"
    "v_mfma_f32_16x16x32_bf16 v[68:71], a[24:27], v[48:51], v[68:71]\n"
    "ds_read_b128 v[40:43], v8 offset:17344\n"
    "v_mfma_f32_16x16x32_bf16 v[76:79], a[28:31], v[48:51], v[76:79]\n"
    "v_mfma_f32_16x16x32_bf16 v[72:75], a[28:31], v[44:47], v[72:75]\n"
    "s_waitcnt lgkmcnt(7)\n"
    "v_mfma_f32_16x16x32_bf16 v[64:67], a[32:35], v[52:55], v[64:67]\n"
    "ds_read_b128 v[44:47], v8 offset:512\n"
    "s_waitcnt lgkmcnt(7)\n"
    "v_mfma_f32_16x16x32_bf16 v[68:71], a[32:35], v[56:59], v[68:71]\n"
    "ds_read_b128 v[48:51], v8 offset:17408\n"
    "v_mfma_f32_16x16x32_bf16 v[76:79], a[36:39], v[56:59], v[76:79]\n"
    "v_mfma_f32_16x16x32_bf16 v[72:75], a[36:39], v[52:55], v[72:75]\n"
    "s_waitcnt lgkmcnt(7)\n"
    "v_mfma_f32_16x16x32_bf16 v[64:67], a[40:43], v[20:23], v[64:67]\n"
    "ds_read_b128 v[52:55], v8 offset:576\n"
    "s_waitcnt lgkmcnt(7)\n"
    "v_mfma_f32_16x16x32_bf16 v[68:71], a[40:43], v[24:27], v[68:71]\n"
    "ds_read_b128 v[56:59], v8 offset:17472\n"
    "v_mfma_f32_16x16x32_bf16 v[76:79], a[44:47], v[24:27], v[76:79]\n"
    "v_mfma_f32_16x16x32_bf16 v[72:75], a[44:47], v[20:23], v[72:75]\n"
    "s_waitcnt lgkmcnt(7)\n"
    "v_mfma_f32_16x16x32_bf16 v[64:67], a[48:51], v[28:31], v[64:67]\n"
    "ds_read_b128 v[20:23], v8 offset:640\n"
    "s_waitcnt lgkmcnt(7)\n"
    "v_mfma_f32_16x16x32_bf16 v[68:71], a[48:51], v[32:35], v[68:71]\n"
    "ds_read_b128 v[24:27], v8 offset:17536\n"
    "v_mfma_f32_16x16x32_bf16 v[76:79], a[52:55], v[32:35], v[76:79]\n"
    "v_mfma_f32_16x16x32_bf16 v[72:75], a[52:55], v[28:31], v[72:75]\n"
    "s_waitcnt lgkmcnt(7)\n"
    "v_mfma_f32_16x16x32_bf16 v[64:67], a[56:59], v[36:39], v[64:67]\n"
    "ds_read_b128 v[28:31], v8 offset:704\n"
    "s_waitcnt lgkmcnt(7)\n"
    "v_mfma_f32_16x16x32_bf16 v[68:71], a[56:59], v[40:43], v[68:71]\n"
    "ds_read_b128 v[32:35], v8 offset:17600\n"
    "v_mfma_f32_16x16x32_bf16 v[76:79], a[60:63], v[40:43], v[76:79]\n"
    "v_mfma_f32_16x16x32_bf16 v[72:75], a[60:63], v[36:39], v[72:75]\n"
    "s_waitcnt lgkmcnt(7)\n"
    "v_mfma_f32_16x16x32_bf16 v[64:67], a[64:67], v[44:47], v[64:67]\n"
    "ds_read_b128 v[36:39], v8 offset:768\n"
    "s_waitcnt lgkmcnt(7)\n"
    "v_mfma_f32_16x16x32_bf16 v[68:71], a[64:67], v[48:51], v[68:71]\n"
    "ds_read_b128 v[40:43], v8 offset:17664\n"
    "v_mfma_f32_16x16x32_bf16 v[76:79], a[68:71], v[48:51], v[76:79]\n"
    "v_mfma_f32_16x16x32_bf16 v[72:75], a[68:71], v[44:47], v[72:75]\n"
    "s_waitcnt lgkmcnt(7)\n"
    "v_mfma_f32_16x16x32_bf16 v[64:67], a[72:75], v[52:55], v[64:67]\n"
    "ds_read_b128 v[44:47], v8 offset:832\n"
    "s_waitcnt lgkmcnt(7)\n"
    "v_mfma_f32_16x16x32_bf16 v[68:71], a[72:75], v[56:59], v[68:71]\n"
    "ds_read_b128 v[48:51], v8 offset:17728\n"
    "v_mfma_f32_16x16x32_bf16 v[76:79], a[76:79], v[56:59], v[76:79]\n"
    "v_mfma_f32_16x16x32_bf16 v[72:75], a[76:79], v[52:55], v[72:75]\n"
    "s_waitcnt lgkmcnt(7)\n"
    "v_mfma_f32_16x16x32_bf16 v[64:67], a[80:83], v[20:23], v[64:67]\n"
    "ds_read_b128 v[52:55], v8 offset:896\n"
    "s_waitcnt lgkmcnt(7)\n"
    "v_mfma_f32_16x16x32_bf16 v[68:71], a[80:83], v[24:27], v[68:71]\n"
    "ds_read_b128 v[56:59], v8 offset:17792\n"
    "v_mfma_f32_16x16x32_bf16 v[76:79], a[84:87], v[24:27], v[76:79]\n"
    "v_mfma_f32_16x16x32_bf16 v[72:75], a[84:87], v[20:23], v[72:75]\n"
    "s_waitcnt lgkmcnt(7)\n"
    "v_mfma_f32_16x16x32_bf16 v[64:67], a[88:91], v[28:31], v[64:67]\n"
    "ds_read_b128 v[20:23], v8 offset:960\n"
    "s_waitcnt lgkmcnt(7)\n"
    "v_mfma_f32_16x16x32_bf16 v[68:71], a[88:91], v[32:35], v[68:71]\n"
    "ds_read_b128 v[24:27], v8 offset:17856\n"
    "v_mfma_f32_16x16x32_bf16 v[76:79], a[92:95], v[32:35], v[76:79]\n"
    "v_mfma_f32_16x16x32_bf16 v[72:75], a[92:95], v[28:31], v[72:75]\n"
    "s_waitcnt lgkmcnt(7)\n"
    "v_mfma_f32_16x16x32_bf16 v[64:67], a[96:99], v[36:39], v[64:67]\n"
    "ds_read_b128 v[28:31], v8 offset:0\n"
    "s_waitcnt lgkmcnt(7)\n"
    "v_mfma_f32_16x16x32_bf16 v[68:71], a[96:99], v[40:43], v[68:71]\n"
    "ds_read_b128 v[32:35], v8 offset:16896\n"
    "s_waitcnt vmcnt(8)\n"
    "s_barrier\n"
    "v_readfirstlane_b32 s38, v5\n"
    "s_or_b32 s52, s52, s68\n"
    "s_or_b32 s38, s38, s68\n"
    "s_cbranch_scc0 LK3_%=\n"
    "LJ3_%=:\n"
    "buffer_store_dword v5, v9, s[16:19], s52 offen sc1\n"
    "s_mov_b32 s52, 0x80000000\n"
    "v_mfma_f32_16x16x32_bf16 v[76:79], a[100:103], v[40:43], v[76:79]\n"
    "v_mfma_f32_16x16x32_bf16 v[72:75], a[100:103], v[36:39], v[72:75]\n"
    "s_waitcnt lgkmcnt(7)\n"
    "v_mfma_f32_16x16x32_bf16 v[64:67], a[104:107], v[44:47], v[64:67]\n"
    "ds_read_b128 v[36:39], v8 offset:64\n"
    "s_waitcnt lgkmcnt(7)\n"
    "v_mfma_f32_16x16x32_bf16 v[68:71], a[104:107], v[48:51], v[68:71]\n"
    "ds_read_b128 v[40:43], v8 offset:16960\n"
    "v_mfma_f32_16x16x32_bf16 v[76:79], a[108:111], v[48:51], v[76:79]\n"
    "v_mfma_f32_16x16x32_bf16 v[72:75], a[108:111], v[44:47], v[72:75]\n"
    "s_waitcnt lgkmcnt(7)\n"
    "v_mfma_f32_16x16x32_bf16 v[64:67], a[112:115], v[52:55], v[64:67]\n"
    "ds_read_b128 v[44:47], v8 offset:128\n"
    "s_waitcnt lgkmcnt(7)\n"
    "v_mfma_f32_16x16x32_bf16 v[68:71], a[112:115], v[56:59], v[68:71]\n"
    "ds_read_b128 v[48:51], v8 offset:17024\n"
    "v_mfma_f32_16x16x32_bf16 v[76:79], a[116:119], v[56:59], v[76:79]\n"
    "v_mfma_f32_16x16x32_bf16 v[72:75], a[116:119], v[52:55], v[72:75]\n"
    "s_waitcnt lgkmcnt(7)\n"
    "v_mfma_f32_16x16x32_bf16 v[64:67], a[120:123], v[20:23], v[64:67]\n"
    "ds_read_b128 v[52:55], v8 offset:192\n"
    "s_waitcnt lgkmcnt(7)\n"
    "v_mfma_f32_16x16x32_bf16 v[68:71], a[120:123], v[24:27], v[68:71]\n"
    "ds_read_b128 v[56:59], v8 offset:17088\n"
    "v_mfma_f32_16x16x32_bf16 v[76:79], a[124:127], v[24:27], v[76:79]\n"
    "v_mfma_f32_16x16x32_bf16 v[72:75], a[124:127], v[20:23], v[72:75]\n"
    "s_waitcnt lgkmcnt(7)\n"
    "v_mfma_f32_16x16x32_bf16 v[80:83], a[128:131], v[28:31], 0\n"
    "ds_read_b128 v[20:23], v8 offset:256\n"
    "s_waitcnt lgkmcnt(7)\n"
    "v_mfma_f32_16x16x32_bf16 v[84:87], a[128:131], v[32:35], 0\n"
    "ds_read_b128 v[24:27], v8 offset:17152\n"
    "buffer_load_dword v6, off, s[16:19], s53 sc1\n"
    "v_mfma_f32_16x16x32_bf16 v[92:95], a[132:135], v[32:35], 0\n"
    "v_mfma_f32_16x16x32_bf16 v[88:91], a[132:135], v[28:31], 0\n"
    "s_waitcnt lgkmcnt(7)\n"
    "v_mfma_f32_16x16x32_bf16 v[80:83], a[136:139], v[36:39], v[80:83]\n"
    "ds_read_b128 v[28:31], v8 offset:320\n"
    "s_waitcnt lgkmcnt(7)\n"
    "v_mfma_f32_16x16x32_bf16 v[84:87], a[136:139], v[40:43], v[84:87]\n"
    "ds_read_b128 v[32:35], v8 offset:17216\n"
    "v_cvt_pk_bf16_f32 v12, v64, v72\n"
    "v_mfma_f32_16x16x32_bf16 v[92:95], a[140:143], v[40:43], v[92:95]\n"
    "v_cvt_pk_bf16_f32 v13, v65, v73\n"
    "v_mfma_f32_16x16x32_bf16 v[88:91], a[140:143], v[36:39], v[88:91]\n"
    "s_waitcnt lgkmcnt(7)\n"
    "v_cvt_pk_bf16_f32 v14, v66, v74\n"
    "v_mfma_f32_16x16x32_bf16 v[80:83], a[144:147], v[44:47], v[80:83]\n"
    "ds_read_b128 v[36:39], v8 offset:384\n"
    "s_waitcnt lgkmcnt(7)\n"
    "v_mfma_f32_16x16x32_bf16 v[84:87], a[144:147], v[48:51], v[84:87]\n"
    "ds_read_b128 v[40:43], v8 offset:17280\n"
    "v_cvt_pk_bf16_f32 v15, v67, v75\n"
    "v_mfma_f32_16x16x32_bf16 v[92:95], a[148:151], v[48:51], v[92:95]\n"
    "buffer_store_dwordx4 v[12:15], v0, s[16:19], s59 offen sc1\n"
    "v_mfma_f32_16x16x32_bf16 v[88:91], a[148:151], v[44:47], v[88:91]\n"
    "s_waitcnt lgkmcnt(7)\n"
    "v_cvt_pk_bf16_f32 v16, v68, v76\n"
    "v_mfma_f32_16x16x32_bf16 v[80:83], a[152:155], v[52:55], v[80:83]\n"
    "ds_read_b128 v[44:47], v8 offset:448\n"
    "s_waitcnt lgkmcnt(7)\n"
    "v_mfma_f32_16x16x32_bf16 v[84:87], a[152:155], v[56:59], v[84:87]\n"
    "ds_read_b128 v[48:51], v8 offset:17344\n"
    "v_cvt_pk_bf16_f32 v17, v69, v77\n"
    "v_mfma_f32_16x16x32_bf16 v[92:95], a[156:159], v[56:59], v[92:95]\n"
    "v_cvt_pk_bf16_f32 v18, v70, v78\n"
    "v_mfma_f32_16x16x32_bf16 v[88:91], a[156:159], v[52:55], v[88:91]\n"
    "s_waitcnt lgkmcnt(7)\n"
    "v_cvt_pk_bf16_f32 v19, v71, v79\n"
    "v_mfma_f32_16x16x32_bf16 v[80:83], a[160:163], v[20:23], v[80:83]\n"
    "ds_read_b128 v[52:55], v8 offset:512\n"
    "s_waitcnt lgkmcnt(7)\n"
    "v_mfma_f32_16x16x32_bf16 v[84:87], a[160:163], v[24:27], v[84:87]\n"
    "ds_read_b128 v[56:59], v8 offset:17408\n"
    "buffer_store_dwordx4 v[16:19], v0, s[16:19], s62 offen sc1\n"
    "v_mfma_f32_16x16x32_bf16 v[92:95], a[164:167], v[24:27], v[92:95]\n"
    "s_add_u32 s59, s59, 16384\n"
    "s_add_u32 s62, s62, 16384\n"
    "v_mfma_f32_16x16x32_bf16 v[88:91], a[164:167], v[20:23], v[88:91]\n"
    "s_waitcnt lgkmcnt(7)\n"
    "v_mfma_f32_16x16x32_bf16 v[80:83], a[168:171], v[28:31], v[80:83]\n"
    "ds_read_b128 v[20:23], v8 offset:576\n"
    "s_waitcnt lgkmcnt(7)\n"
    "v_mfma_f32_16x16x32_bf16 v[84:87], a[168:171], v[32:35], v[84:87]\n"
    "ds_read_b128 v[24:27], v8 offset:17472\n"
    "v_mfma_f32_16x16x32_bf16 v[92:95], a[172:175], v[32:35], v[92:95]\n"
    "v_mfma_f32_16x16x32_bf16 v[88:91], a[172:175], v[28:31], v[88:91]\n"
    "s_waitcnt lgkmcnt(7)\n"
    "v_mfma_f32_16x16x32_bf16 v[80:83], a[176:179], v[36:39], v[80:83]\n"
    "ds_read_b128 v[28:31], v8 offset:640\n"
    "s_waitcnt lgkmcnt(7)\n"
    "v_mfma_f32_16x16x32_bf16 v[84:87], a[176:179], v[40:43], v[84:87]\n"
    "ds_read_b128 v[32:35], v8 offset:17536\n"
    "v_mfma_f32_16x16x32_bf16 v[92:95], a[180:183], v[40:43], v[92:95]\n"
    "v_mfma_f32_16x16x32_bf16 v[88:91], a[180:183], v[36:39], v[88:91]\n"
    "s_waitcnt lgkmcnt(7)\n"
    "v_mfma_f32_16x16x32_bf16 v[80:83], a[184:187], v[44:47], v[80:83]\n"
    "ds_read_b128 v[36:39], v8 offset:704\n"
    "s_waitcnt lgkmcnt(7)\n"
    "v_mfma_f32_16x16x32_bf16 v[84:87], a[184:187], v[48:51], v[84:87]\n"
    "ds_read_b128 v[40:43], v8 offset:17600\n"
    "s_waitcnt vmcnt(2)\n"
    "ds_write_b32 v11, v6 offset:64\n"
    "v_mfma_f32_16x16x32_bf16 v[92:95], a[188:191], v[48:51], v[92:95]\n"
    "v_mfma_f32_16x16x32_bf16 v[88:91], a[188:191], v[44:47], v[88:91]\n"
    "s_waitcnt lgkmcnt(8)\n"
    "v_mfma_f32_16x16x32_bf16 v[80:83], a[192:195], v[52:55], v[80:83]\n"
    "ds_read_b128 v[44:47], v8 offset:768\n"
    "s_waitcnt lgkmcnt(8)\n"
    "v_mfma_f32_16x16x32_bf16 v[84:87], a[192:195], v[56:59], v[84:87]\n"
    "ds_read_b128 v[48:51], v8 offset:17664\n"
    "v_mfma_f32_16x16x32_bf16 v[92:95], a[196:199], v[56:59], v[92:95]\n"
    "v_mfma_f32_16x16x32_bf16 v[88:91], a[196:199], v[52:55], v[88:91]\n"
    "s_waitcnt lgkmcnt(8)\n"
    "v_mfma_f32_16x16x32_bf16 v[80:83], a[200:203], v[20:23], v[80:83]\n"
    "ds_read_b128 v[52:55], v8 offset:832\n"
    "s_waitcnt lgkmcnt(8)\n"
    "v_mfma_f32_16x16x32_bf16 v[84:87], a[200:203], v[24:27], v[84:87]\n"
    "ds_read_b128 v[56:59], v8 offset:17728\n"
    "s_waitcnt lgkmcnt(2)\n"
    "s_barrier\n"
    "ds_read_b32 v6, v10 offset:64\n"
    "v_mfma_f32_16x16x32_bf16 v[92:95], a[204:207], v[24:27], v[92:95]\n"
    "v_mfma_f32_16x16x32_bf16 v[88:91], a[204:207], v[20:23], v[88:91]\n"
    "v_mfma_f32_16x16x32_bf16 v[80:83], a[208:211], v[28:31], v[80:83]\n"
    "ds_read_b128 v[20:23], v8 offset:896\n"
    "v_mfma_f32_16x16x32_bf16 v[84:87], a[208:211], v[32:35], v[84:87]\n"
    "ds_read_b128 v[24:27], v8 offset:17792\n"
    "s_lshl_b32 s63, s41, 16\n"
    "s_add_u32 s63, s63, s46\n"
    "s_lshr_b32 s38, s63, 8\n"
    "s_xor_b32 s38, s38, s48\n"
    "s_and_b32 s38, s38, 0x2000\n"
    "s_or_b32 s63, s63, s38\n"
    "v_mfma_f32_16x16x32_bf16 v[92:95], a[212:215], v[32:35], v[92:95]\n"
    "v_mfma_f32_16x16x32_bf16 v[88:91], a[212:215], v[28:31], v[88:91]\n"
    "s_waitcnt lgkmcnt(2)\n"
    "v_readfirstlane_b32 s60, v6\n"
    "s_cmp_lg_u32 s60, 0\n"
    "s_cbranch_scc1 LIKA%=\n"
    "v_mfma_f32_16x16x32_bf16 v[80:83], a[216:219], v[36:39], v[80:83]\n"
    "ds_read_b128 v[28:31], v8 offset:960\n"
    "v_mfma_f32_16x16x32_bf16 v[84:87], a[216:219], v[40:43], v[84:87]\n"
    "ds_read_b128 v[32:35], v8 offset:17856\n"
    "v_mfma_f32_16x16x32_bf16 v[92:95], a[220:223], v[40:43], v[92:95]\n"
    "v_mfma_f32_16x16x32_bf16 v[88:91], a[220:223], v[36:39], v[88:91]\n"
    "v_mfma_f32_16x16x32_bf16 v[80:83], a[224:227], v[44:47], v[80:83]\n"
    "v_mfma_f32_16x16x32_bf16 v[84:87], a[224:227], v[48:51], v[84:87]\n"
    "v_mfma_f32_16x16x32_bf16 v[92:95], a[228:231], v[48:51], v[92:95]\n"
    "v_mfma_f32_16x16x32_bf16 v[88:91], a[228:231], v[44:47], v[88:91]\n"
    "v_mfma_f32_16x16x32_bf16 v[80:83], a[232:235], v[52:55], v[80:83]\n"
    "v_mfma_f32_16x16x32_bf16 v[84:87], a[232:235], v[56:59], v[84:87]\n"
    "v_mfma_f32_16x16x32_bf16 v[92:95], a[236:239], v[56:59], v[92:95]\n"
    "v_mfma_f32_16x16x32_bf16 v[88:91], a[236:239], v[52:55], v[88:91]\n"
    "s_waitcnt lgkmcnt(3)\n"
    "v_mfma_f32_16x16x32_bf16 v[80:83], a[240:243], v[20:23], v[80:83]\n"
    "s_waitcnt lgkmcnt(2)\n"
    "v_mfma_f32_16x16x32_bf16 v[84:87], a[240:243], v[24:27], v[84:87]\n"
    "v_mfma_f32_16x16x32_bf16 v[92:95], a[244:247], v[24:27], v[92:95]\n"
    "v_mfma_f32_16x16x32_bf16 v[88:91], a[244:247], v[20:23], v[88:91]\n"
    "s_waitcnt lgkmcnt(1)\n"
    "v_mfma_f32_16x16x32_bf16 v[80:83], a[248:251], v[28:31], v[80:83]\n"
    "s_waitcnt lgkmcnt(0)\n"
    "v_mfma_f32_16x16x32_bf16 v[84:87], a[248:251], v[32:35], v[84:87]\n"
    "v_mfma_f32_16x16x32_bf16 v[92:95], a[252:255], v[32:35], v[92:95]\n"
    "v_mfma_f32_16x16x32_bf16 v[88:91], a[252:255], v[28:31], v[88:91]\n"
    "s_branch LIKB%=\n"
    "LIKA%=:\n"
    "v_mfma_f32_16x16x32_bf16 v[80:83], a[216:219], v[36:39], v[80:83]\n"
    "ds_read_b128 v[28:31], v8 offset:960\n"
    "v_mfma_f32_16x16x32_bf16 v[84:87], a[216:219], v[40:43], v[84:87]\n"
    "ds_read_b128 v[32:35], v8 offset:17856\n"
    "s_add_u32 s39, s44, 67584\n"
    "s_add_u32 s69, s63, 4096\n"
    "v_mfma_f32_16x16x32_bf16 v[92:95], a[220:223], v[40:43], v[92:95]\n"
    "s_add_u32 m0, s39, 0\n"
    "s_nop 0\n"
    "buffer_load_dwordx4 v0, s[16:19], s63 offen offset:0 sc1 lds\n"
    "v_mfma_f32_16x16x32_bf16 v[88:91], a[220:223], v[36:39], v[88:91]\n"
    "v_mfma_f32_16x16x32_bf16 v[80:83], a[224:227], v[44:47], v[80:83]\n"
    "v_mfma_f32_16x16x32_bf16 v[84:87], a[224:227], v[48:51], v[84:87]\n"
    "s_add_u32 m0, s39, 32\n"
    "s_nop 0\n"
    "buffer_load_dwordx4 v0, s[16:19], s63 offen offset:1024 sc1 lds\n"
    "v_mfma_f32_16x16x32_bf16 v[92:95], a[228:231], v[48:51], v[92:95]\n"
    "s_add_u32 m0, s39, 64\n"
    "s_nop 0\n"
    "buffer_load_dwordx4 v0, s[16:19], s63 offen offset:2048 sc1 lds\n"
    "v_mfma_f32_16x16x32_bf16 v[88:91], a[228:231], v[44:47], v[88:91]\n"
    "v_mfma_f32_16x16x32_bf16 v[80:83], a[232:235], v[52:55], v[80:83]\n"
    "v_mfma_f32_16x16x32_bf16 v[84:87], a[232:235], v[56:59], v[84:87]\n"
    "s_add_u32 m0, s39, 96\n"
    "s_nop 0\n"
    "buffer_load_dwordx4 v0, s[16:19], s63 offen offset:3072 sc1 lds\n"
    "v_mfma_f32_16x16x32_bf16 v[92:95], a[236:239], v[56:59], v[92:95]\n"
    "s_add_u32 m0, s39, 4224\n"
    "s_nop 0\n"
    "buffer_load_dwordx4 v0, s[16:19], s69 offen offset:0 sc1 lds\n"
    "v_mfma_f32_16x16x32_bf16 v[88:91], a[236:239], v[52:55], v[88:91]\n"
    "s_waitcnt lgkmcnt(3)\n"
    "v_mfma_f32_16x16x32_bf16 v[80:83], a[240:243], v[20:23], v[80:83]\n"
    "s_waitcnt lgkmcnt(2)\n"
    "v_mfma_f32_16x16x32_bf16 v[84:87], a[240:243], v[24:27], v[84:87]\n"
    "s_add_u32 m0, s39, 4256\n"
    "s_nop 0\n"
    "buffer_load_dwordx4 v0, s[16:19], s69 offen offset:1024 sc1 lds\n"
    "v_mfma_f32_16x16x32_bf16 v[92:95], a[244:247], v[24:27], v[92:95]\n"
    "s_add_u32 m0, s39, 4288\n"
    "s_nop 0\n"
    "buffer_load_dwordx4 v0, s[16:19], s69 offen offset:2048 sc1 lds\n"
    "v_mfma_f32_16x16x32_bf16 v[88:91], a[244:247], v[20:23], v[88:91]\n"
    "s_waitcnt lgkmcnt(1)\n"
    "v_mfma_f32_16x16x32_bf16 v[80:83], a[248:251], v[28:31], v[80:83]\n"
    "s_waitcnt lgkmcnt(0)\n"
    "v_mfma_f32_16x16x32_bf16 v[84:87], a[248:251], v[32:35], v[84:87]\n"
    "s_add_u32 m0, s39, 4320\n"
    "s_nop 0\n"
    "buffer_load_dwordx4 v0, s[16:19], s69 offen offset:3072 sc1 lds\n"
    "v_mfma_f32_16x16x32_bf16 v[92:95], a[252:255], v[32:35], v[92:95]\n"
    "v_mfma_f32_16x16x32_bf16 v[88:91], a[252:255], v[28:31], v[88:91]\n"
    "LIKB%=:\n"
    "s_nop 7\n"
    "v_cvt_pk_bf16_f32 v12, v80, v88\n"
    "v_cvt_pk_bf16_f32 v13, v81, v89\n"
    "v_cvt_pk_bf16_f32 v14, v82, v90\n"
    "v_cvt_pk_bf16_f32 v15, v83, v91\n"
    "buffer_store_dwordx4 v[12:15], v0, s[16:19], s59 offen sc1\n"
    "v_cvt_pk_bf16_f32 v16, v84, v92\n"
    "v_cvt_pk_bf16_f32 v17, v85, v93\n"
    "v_cvt_pk_bf16_f32 v18, v86, v94\n"
    "v_cvt_pk_bf16_f32 v19, v87, v95\n"
    "buffer_store_dwordx4 v[16:19], v0, s[16:19], s62 offen sc1\n"
    "s_mov_b32 s52, s64\n"
    "s_branch LIE%=\n"
    "LIV%=:\n"
    "s_add_u32 s62, s59, 1024\n"
    "ds_read_b128 v[20:23], v8 offset:0\n"
    "ds_read_b128 v[24:27], v8 offset:16896\n"
    "ds_read_b128 v[28:31], v8 offset:64\n"
    "ds_read_b128 v[32:35], v8 offset:16960\n"
    "ds_read_b128 v[36:39], v8 offset:128\n"
    "ds_read_b128 v[40:43], v8 offset:17024\n"
    "ds_read_b128 v[44:47], v8 offset:192\n"
    "ds_read_b128 v[48:51], v8 offset:17088\n"
    "s_waitcnt lgkmcnt(7)\n"
    "v_mfma_f32_16x16x32_bf16 v[64:67], v[20:23], a[0:3], 0\n"
    "ds_read_b128 v[52:55], v8 offset:256\n"
    "s_waitcnt lgkmcnt(7)\n"
    "v_mfma_f32_16x16x32_bf16 v[68:71], v[24:27], a[0:3], 0\n"
    "ds_read_b128 v[56:59], v8 offset:17152\n"
    "v_mfma_f32_16x16x32_bf16 v[76:79], v[24:27], a[4:7], 0\n"
    "v_mfma_f32_16x16x32_bf16 v[72:75], v[20:23], a[4:7], 0\n"
    "s_waitcnt lgkmcnt(7)\n"
    "v_mfma_f32_16x16x32_bf16 v[64:67], v[28:31], a[8:11], v[64:67]\n"
    "ds_read_b128 v[20:23], v8 offset:320\n"
    "s_waitcnt lgkmcnt(7)\n"
    "v_mfma_f32_16x16x32_bf16 v[68:71], v[32:35], a[8:11], v[68:71]\n"
    "ds_read_b128 v[24:27], v8 offset:17216\n"
    "v_mfma_f32_16x16x32_bf16 v[76:79], v[32:35], a[12:15], v[76:79]\n"
    "v_mfma_f32_16x16x32_bf16 v[72:75], v[28:31], a[12:15], v[72:75]\n"
    "s_waitcnt lgkmcnt(7)\n"
    "v_mfma_f32_16x16x32_bf16 v[64:67], v[36:39], a[16:19], v[64:67]\n"
    "ds_read_b128 v[28:31], v8 offset:384\n"
    "s_waitcnt lgkmcnt(7)\n"
    "v_mfma_f32_16x16x32_bf16 v[68:71], v[40:43], a[16:19], v[68:71]\n"
    "ds_read_b128 v[32:35], v8 offset:17280\n"
    "v_mfma_f32_16x16x32_bf16 v[76:79], v[40:43], a[20:23], v[76:79]\n"
    "v_mfma_f32_16x16x32_bf16 v[72:75], v[36:39], a[20:23], v[72:75]\n"
    "s_waitcnt lgkmcnt(7)\n"
    "v_mfma_f32_16x16x32_bf16 v[64:67], v[44:47], a[24:27], v[64:67]\n"
    "ds_read_b128 v[36:39], v8 offset:448\n"
    "s_waitcnt lgkmcnt(7)\n"
    "v_mfma_f32_16x16x32_bf16 v[68:71], v[48:51], a[24:27], v[68:71]\n"
    "ds_read_b128 v[40:43], v8 offset:17344\n"
    "v_mfma_f32_16x16x32_bf16 v[76:79], v[48:51], a[28:31], v[76:79]\n"
    "v_mfma_f32_16x16x32_bf16 v[72:75], v[44:47], a[28:31], v[72:75]\n"
    "s_waitcnt lgkmcnt(7)\n"
    "v_mfma_f32_16x16x32_bf16 v[64:67], v[52:55], a[32:35], v[64:67]\n"
    "ds_read_b128 v[44:47], v8 offset:512\n"
    "s_waitcnt lgkmcnt(7)\n"
    "v_mfma_f32_16x16x32_bf16 v[68:71], v[56:59], a[32:35], v[68:71]\n"
    "ds_read_b128 v[48:51], v8 offset:17408\n"
    "v_mfma_f32_16x16x32_bf16 v[76:79], v[56:59], a[36:39], v[76:79]\n"
    "v_mfma_f32_16x16x32_bf16 v[72:75], v[52:55], a[36:39], v[72:75]\n"
    "s_waitcnt lgkmcnt(7)\n"
    "v_mfma_f32_16x16x32_bf16 v[64:67], v[20:23], a[40:43], v[64:67]\n"
    "ds_read_b128 v[52:55], v8 offset:576\n"
    "s_waitcnt lgkmcnt(7)\n"
    "v_mfma_f32_16x16x32_bf16 v[68:71], v[24:27], a[40:43], v[68:71]\n"
    "ds_read_b128 v[56:59], v8 offset:17472\n"
    "v_mfma_f32_16x16x32_bf16 v[76:79], v[24:27], a[44:47], v[76:79]\n"
    "v_mfma_f32_16x16x32_bf16 v[72:75], v[20:23], a[44:47], v[72:75]\n"
    "s_waitcnt lgkmcnt(7)\n"
    "v_mfma_f32_16x16x32_bf16 v[64:67], v[28:31], a[48:51], v[64:67]\n"
    "ds_read_b128 v[20:23], v8 offset:640\n"
    "s_waitcnt lgkmcnt(7)\n"
    "v_mfma_f32_16x16x32_bf16 v[68:71], v[32:35], a[48:51], v[68:71]\n"
    "ds_read_b128 v[24:27], v8 offset:17536\n"
    "v_mfma_f32_16x16x32_bf16 v[76:79], v[32:35], a[52:55], v[76:79]\n"
    "v_mfma_f32_16x16x32_bf16 v[72:75], v[28:31], a[52:55], v[72:75]\n"
    "s_waitcnt lgkmcnt(7)\n"
    "v_mfma_f32_16x16x32_bf16 v[64:67], v[36:39], a[56:59], v[64:67]\n"
    "ds_read_b128 v[28:31], v8 offset:704\n"
    "s_waitcnt lgkmcnt(7)\n"
    "v_mfma_f32_16x16x32_bf16 v[68:71], v[40:43], a[56:59], v[68:71]\n"
    "ds_read_b128 v[32:35], v8 offset:17600\n"
    "v_mfma_f32_16x16x32_bf16 v[76:79], v[40:43], a[60:63], v[76:79]\n"
    "v_mfma_f32_16x16x32_bf16 v[72:75], v[36:39], a[60:63], v[72:75]\n"
    "s_waitcnt lgkmcnt(7)\n"
    "v_mfma_f32_16x16x32_bf16 v[64:67], v[44:47], a[64:67], v[64:67]\n"
    "ds_read_b128 v[36:39], v8 offset:768\n"
    "s_waitcnt lgkmcnt(7)\n"
    "v_mfma_f32_16x16x32_bf16 v[68:71], v[48:51], a[64:67], v[68:71]\n"
    "ds_read_b128 v[40:43], v8 offset:17664\n"
    "v_mfma_f32_16x16x32_bf16 v[76:79], v[48:51], a[68:71], v[76:79]\n"
    "v_mfma_f32_16x16x32_bf16 v[72:75], v[44:47], a[68:71], v[72:75]\n"
    "s_waitcnt lgkmcnt(7)\n"
    "v_mfma_f32_16x16x32_bf16 v[64:67], v[52:55], a[72:75], v[64:67]\n"
    "ds_read_b128 v[44:47], v8 offset:832\n"
    "s_waitcnt lgkmcnt(7)\n"
    "v_mfma_f32_16x16x32_bf16 v[68:71], v[56:59], a[72:75], v[68:71]\n"
    "ds_read_b128 v[48:51], v8 offset:17728\n"
    "v_mfma_f32_16x16x32_bf16 v[76:79], v[56:59], a[76:79], v[76:79]\n"
    "v_mfma_f32_16x16x32_bf16 v[72:75], v[52:55], a[76:79], v[72:75]\n"
    "s_waitcnt lgkmcnt(7)\n"
    "v_mfma_f32_16x16x32_bf16 v[64:67], v[20:23], a[80:83], v[64:67]\n"
    "ds_read_b128 v[52:55], v8 offset:896\n"
    "s_waitcnt lgkmcnt(7)\n"
    "v_mfma_f32_16x16x32_bf16 v[68:71], v[24:27], a[80:83], v[68:71]\n"
    "ds_read_b128 v[56:59], v8 offset:17792\n"
    "v_mfma_f32_16x16x32_bf16 v[76:79], v[24:27], a[84:87], v[76:79]\n"
    "v_mfma_f32_16x16x32_bf16 v[72:75], v[20:23], a[84:87], v[72:75]\n"
    "s_waitcnt lgkmcnt(7)\n"
    "v_mfma_f32_16x16x32_bf16 v[64:67], v[28:31], a[88:91], v[64:67]\n"
    "ds_read_b128 v[20:23], v8 offset:960\n"
    "s_waitcnt lgkmcnt(7)\n"
    "v_mfma_f32_16x16x32_bf16 v[68:71], v[32:35], a[88:91], v[68:71]\n"
    "ds_read_b128 v[24:27], v8 offset:17856\n"
    "v_mfma_f32_16x16x32_bf16 v[76:79], v[32:35], a[92:95], v[76:79]\n"
    "v_mfma_f32_16x16x32_bf16 v[72:75], v[28:31], a[92:95], v[72:75]\n"
    "s_waitcnt lgkmcnt(7)\n"
    "v_mfma_f32_16x16x32_bf16 v[64:67], v[36:39], a[96:99], v[64:67]\n"
    "ds_read_b128 v[28:31], v8 offset:0\n"
    "s_waitcnt lgkmcnt(7)\n"
    "v_mfma_f32_16x16x32_bf16 v[68:71], v[40:43], a[96:99], v[68:71]\n"
    "ds_read_b128 v[32:35], v8 offset:16896\n"
    "s_waitcnt vmcnt(8)\n"
    "s_barrier\n"
    "v_readfirstlane_b32 s38, v5\n"
    "s_or_b32 s52, s52, s68\n"
    "s_or_b32 s38, s38, s68\n"
    "s_cbranch_scc0 LK4_%=\n"
    "LJ4_%=:\n"
    "buffer_store_dword v5, v9, s[16:19], s52 offen sc1\n"
    "s_mov_b32 s52, 0x80000000\n"
    "v_mfma_f32_16x16x32_bf16 v[76:79], v[40:43], a[100:103], v[76:79]\n"
    "v_mfma_f32_16x16x32_bf16 v[72:75], v[36:39], a[100:103], v[72:75]\n"
    "s_waitcnt lgkmcnt(7)\n"
    "v_mfma_f32_16x16x32_bf16 v[64:67], v[44:47], a[104:107], v[64:67]\n"
    "ds_read_b128 v[36:39], v8 offset:64\n"
    "s_waitcnt lgkmcnt(7)\n"
    "v_mfma_f32_16x16x32_bf16 v[68:71], v[48:51], a[104:107], v[68:71]\n"
    "ds_read_b128 v[40:43], v8 offset:16960\n"
    "v_mfma_f32_16x16x32_bf16 v[76:79], v[48:51], a[108:111], v[76:79]\n"
    "v_mfma_f32_16x16x32_bf16 v[72:75], v[44:47], a[108:111], v[72:75]\n"
    "s_waitcnt lgkmcnt(7)\n"
    "v_mfma_f32_16x16x32_bf16 v[64:67], v[52:55], a[112:115], v[64:67]\n"
    "ds_read_b128 v[44:47], v8 offset:128\n"
    "s_waitcnt lgkmcnt(7)\n"
    "v_mfma_f32_16x16x32_bf16 v[68:71], v[56:59], a[112:115], v[68:71]\n"
    "ds_read_b128 v[48:51], v8 offset:17024\n"
    "v_mfma_f32_16x16x32_bf16 v[76:79], v[56:59], a[116:119], v[76:79]\n"
    "v_mfma_f32_16x16x32_bf16 v[72:75], v[52:55], a[116:119], v[72:75]\n"
    "s_waitcnt lgkmcnt(7)\n"
    "v_mfma_f32_16x16x32_bf16 v[64:67], v[20:23], a[120:123], v[64:67]\n"
    "ds_read_b128 v[52:55], v8 offset:192\n"
    "s_waitcnt lgkmcnt(7)\n"
    "v_mfma_f32_16x16x32_bf16 v[68:71], v[24:27], a[120:123], v[68:71]\n"
    "ds_read_b128 v[56:59], v8 offset:17088\n"
    "v_mfma_f32_16x16x32_bf16 v[76:79], v[24:27], a[124:127], v[76:79]\n"
    "v_mfma_f32_16x16x32_bf16 v[72:75], v[20:23], a[124:127], v[72:75]\n"
    "s_waitcnt lgkmcnt(7)\n"
    "v_mfma_f32_16x16x32_bf16 v[80:83], v[28:31], a[128:131], 0\n"
    "ds_read_b128 v[20:23], v8 offset:256\n"
    "s_waitcnt lgkmcnt(7)\n"
    "v_mfma_f32_16x16x32_bf16 v[84:87], v[32:35], a[128:131], 0\n"
    "ds_read_b128 v[24:27], v8 offset:17152\n"
    "buffer_load_dword v6, off, s[16:19], s53 sc1\n"
    "v_mfma_f32_16x16x32_bf16 v[92:95], v[32:35], a[132:135], 0\n"
    "v_mfma_f32_16x16x32_bf16 v[88:91], v[28:31], a[132:135], 0\n"
    "s_waitcnt lgkmcnt(7)\n"
    "v_mfma_f32_16x16x32_bf16 v[80:83], v[36:39], a[136:139], v[80:83]\n"
    "ds_read_b128 v[28:31], v8 offset:320\n"
    "s_waitcnt lgkmcnt(7)\n"
    "v_mfma_f32_16x16x32_bf16 v[84:87], v[40:43], a[136:139], v[84:87]\n"
    "ds_read_b128 v[32:35], v8 offset:17216\n"
    "v_cvt_pk_bf16_f32 v12, v64, v65\n"
    "v_mfma_f32_16x16x32_bf16 v[92:95], v[40:43], a[140:143], v[92:95]\n"
    "v_cvt_pk_bf16_f32 v13, v66, v67\n"
    "v_mfma_f32_16x16x32_bf16 v[88:91], v[36:39], a[140:143], v[88:91]\n"
    "s_waitcnt lgkmcnt(7)\n"
    "v_cvt_pk_bf16_f32 v14, v68, v69\n"
    "v_mfma_f32_16x16x32_bf16 v[80:83], v[44:47], a[144:147], v[80:83]\n"
    "ds_read_b128 v[36:39], v8 offset:384\n"
    "s_waitcnt lgkmcnt(7)\n"
    "v_mfma_f32_16x16x32_bf16 v[84:87], v[48:51], a[144:147], v[84:87]\n"
    "ds_read_b128 v[40:43], v8 offset:17280\n"
    "v_cvt_pk_bf16_f32 v15, v70, v71\n"
    "v_mfma_f32_16x16x32_bf16 v[92:95], v[48:51], a[148:151], v[92:95]\n"
    "buffer_store_dwordx4 v[12:15], v0, s[16:19], s59 offen sc1\n"
    "v_mfma_f32_16x16x32_bf16 v[88:91], v[44:47], a[148:151], v[88:91]\n"
    "s_waitcnt lgkmcnt(7)\n"
    "v_cvt_pk_bf16_f32 v16, v72, v73\n"
    "v_mfma_f32_16x16x32_bf16 v[80:83], v[52:55], a[152:155], v[80:83]\n"
    "ds_read_b128 v[44:47], v8 offset:448\n"
    "s_waitcnt lgkmcnt(7)\n"
    "v_mfma_f32_16x16x32_bf16 v[84:87], v[56:59], a[152:155], v[84:87]\n"
    "ds_read_b128 v[48:51], v8 offset:17344\n"
    "v_cvt_pk_bf16_f32 v17, v74, v75\n"
    "v_mfma_f32_16x16x32_bf16 v[92:95], v[56:59], a[156:159], v[92:95]\n"
    "v_cvt_pk_bf16_f32 v18, v76, v77\n"
    "v_mfma_f32_16x16x32_bf16 v[88:91], v[52:55], a[156:159], v[88:91]\n"
    "s_waitcnt lgkmcnt(7)\n"
    "v_cvt_pk_bf16_f32 v19, v78, v79\n"
    "v_mfma_f32_16x16x32_bf16 v[80:83], v[20:23], a[160:163], v[80:83]\n"
    "ds_read_b128 v[52:55], v8 offset:512\n"
    "s_waitcnt lgkmcnt(7)\n"
    "v_mfma_f32_16x16x32_bf16 v[84:87], v[24:27], a[160:163], v[84:87]\n"
    "ds_read_b128 v[56:59], v8 offset:17408\n"
    "buffer_store_dwordx4 v[16:19], v0, s[16:19], s62 offen sc1\n"
    "v_mfma_f32_16x16x32_bf16 v[92:95], v[24:27], a[164:167], v[92:95]\n"
    "s_add_u32 s59, s59, 16384\n"
    "s_add_u32 s62, s62, 16384\n"
    "v_mfma_f32_16x16x32_bf16 v[88:91], v[20:23], a[164:167], v[88:91]\n"
    "s_waitcnt lgkmcnt(7)\n"
    "v_mfma_f32_16x16x32_bf16 v[80:83], v[28:31], a[168:171], v[80:83]\n"
    "ds_read_b128 v[20:23], v8 offset:576\n"
    "s_waitcnt lgkmcnt(7)\n"
    "v_mfma_f32_16x16x32_bf16 v[84:87], v[32:35], a[168:171], v[84:87]\n"
    "ds_read_b128 v[24:27], v8 offset:17472\n"
    "v_mfma_f32_16x16x32_bf16 v[92:95], v[32:35], a[172:175], v[92:95]\n"
    "v_mfma_f32_16x16x32_bf16 v[88:91], v[28:31], a[172:175], v[88:91]\n"
    "s_waitcnt lgkmcnt(7)\n"
    "v_mfma_f32_16x16x32_bf16 v[80:83], v[36:39], a[176:179], v[80:83]\n"
    "ds_read_b128 v[28:31], v8 offset:640\n"
    "s_waitcnt lgkmcnt(7)\n"
    "v_mfma_f32_16x16x32_bf16 v[84:87], v[40:43], a[176:179], v[84:87]\n"
    "ds_read_b128 v[32:35], v8 offset:17536\n"
    "v_mfma_f32_16x16x32_bf16 v[92:95], v[40:43], a[180:183], v[92:95]\n"
    "v_mfma_f32_16x16x32_bf16 v[88:91], v[36:39], a[180:183], v[88:91]\n"
    "s_waitcnt lgkmcnt(7)\n"
    "v_mfma_f32_16x16x32_bf16 v[80:83], v[44:47], a[184:187], v[80:83]\n"
    "ds_read_b128 v[36:39], v8 offset:704\n"
    "s_waitcnt lgkmcnt(7)\n"
    "v_mfma_f32_16x16x32_bf16 v[84:87], v[48:51], a[184:187], v[84:87]\n"
    "ds_read_b128 v[40:43], v8 offset:17600\n"
    "s_waitcnt vmcnt(2)\n"
    "ds_write_b32 v11, v6 offset:64\n"
    "v_mfma_f32_16x16x32_bf16 v[92:95], v[48:51], a[188:191], v[92:95]\n"
    "v_mfma_f32_16x16x32_bf16 v[88:91], v[44:47], a[188:191], v[88:91]\n"
    "s_waitcnt lgkmcnt(8)\n"
    "v_mfma_f32_16x16x32_bf16 v[80:83], v[52:55], a[192:195], v[80:83]\n"
    "ds_read_b128 v[44:47], v8 offset:768\n"
    "s_waitcnt lgkmcnt(8)\n"
    "v_mfma_f32_16x16x32_bf16 v[84:87], v[56:59], a[192:195], v[84:87]\n"
    "ds_read_b128 v[48:51], v8 offset:17664\n"
    "v_mfma_f32_16x16x32_bf16 v[92:95], v[56:59], a[196:199], v[92:95]\n"
    "v_mfma_f32_16x16x32_bf16 v[88:91], v[52:55], a[196:199], v[88:91]\n"
    "s_waitcnt lgkmcnt(8)\n"
    "v_mfma_f32_16x16x32_bf16 v[80:83], v[20:23], a[200:203], v[80:83]\n"
    "ds_read_b128 v[52:55], v8 offset:832\n"
    "s_waitcnt lgkmcnt(8)\n"
    "v_mfma_f32_16x16x32_bf16 v[84:87], v[24:27], a[200:203], v[84:87]\n"
    "ds_read_b128 v[56:59], v8 offset:17728\n"
    "s_waitcnt lgkmcnt(2)\n"
    "s_barrier\n"
    "ds_read_b32 v6, v10 offset:64\n"
    "v_mfma_f32_16x16x32_bf16 v[92:95], v[24:27], a[204:207], v[92:95]\n"
    "v_mfma_f32_16x16x32_bf16 v[88:91], v[20:23], a[204:207], v[88:91]\n"
    "v_mfma_f32_16x16x32_bf16 v[80:83], v[28:31], a[208:211], v[80:83]\n"
    "ds_read_b128 v[20:23], v8 offset:896\n"
    "v_mfma_f32_16x16x32_bf16 v[84:87], v[32:35], a[208:211], v[84:87]\n"
    "ds_read_b128 v[24:27], v8 offset:17792\n"
    "s_lshl_b32 s63, s41, 16\n"
    "s_add_u32 s63, s63, s46\n"
    "s_lshr_b32 s38, s63, 8\n"
    "s_xor_b32 s38, s38, s48\n"
    "s_and_b32 s38, s38, 0x2000\n"
    "s_or_b32 s63, s63, s38\n"
    "v_mfma_f32_16x16x32_bf16 v[92:95], v[32:35], a[212:215], v[92:95]\n"
    "v_mfma_f32_16x16x32_bf16 v[88:91], v[28:31], a[212:215], v[88:91]\n"
    "s_waitcnt lgkmcnt(2)\n"
    "v_readfirstlane_b32 s60, v6\n"
    "s_cmp_lg_u32 s60, 0\n"
    "s_cbranch_scc1 LITA%=\n"
    "v_mfma_f32_16x16x32_bf16 v[80:83], v[36:39], a[216:219], v[80:83]\n"
    "ds_read_b128 v[28:31], v8 offset:960\n"
    "v_mfma_f32_16x16x32_bf16 v[84:87], v[40:43], a[216:219], v[84:87]\n"
    "ds_read_b128 v[32:35], v8 offset:17856\n"
    "v_mfma_f32_16x16x32_bf16 v[92:95], v[40:43], a[220:223], v[92:95]\n"
    "v_mfma_f32_16x16x32_bf16 v[88:91], v[36:39], a[220:223], v[88:91]\n"
    "v_mfma_f32_16x16x32_bf16 v[80:83], v[44:47], a[224:227], v[80:83]\n"
    "v_mfma_f32_16x16x32_bf16 v[84:87], v[48:51], a[224:227], v[84:87]\n"
    "v_mfma_f32_16x16x32_bf16 v[92:95], v[48:51], a[228:231], v[92:95]\n"
    "v_mfma_f32_16x16x32_bf16 v[88:91], v[44:47], a[228:231], v[88:91]\n"
    "v_mfma_f32_16x16x32_bf16 v[80:83], v[52:55], a[232:235], v[80:83]\n"
    "v_mfma_f32_16x16x32_bf16 v[84:87], v[56:59], a[232:235], v[84:87]\n"
    "v_mfma_f32_16x16x32_bf16 v[92:95], v[56:59], a[236:239], v[92:95]\n"
    "v_mfma_f32_16x16x32_bf16 v[88:91], v[52:55], a[236:239], v[88:91]\n"
    "s_waitcnt lgkmcnt(3)\n"
    "v_mfma_f32_16x16x32_bf16 v[80:83], v[20:23], a[240:243], v[80:83]\n"
    "s_waitcnt lgkmcnt(2)\n"
    "v_mfma_f32_16x16x32_bf16 v[84:87], v[24:27], a[240:243], v[84:87]\n"
    "v_mfma_f32_16x16x32_bf16 v[92:95], v[24:27], a[244:247], v[92:95]\n"
    "v_mfma_f32_16x16x32_bf16 v[88:91], v[20:23], a[244:247], v[88:91]\n"
    "s_waitcnt lgkmcnt(1)\n"
    "v_mfma_f32_16x16x32_bf16 v[80:83], v[28:31], a[248:251], v[80:83]\n"
    "s_waitcnt lgkmcnt(0)\n"
    "v_mfma_f32_16x16x32_bf16 v[84:87], v[32:35], a[248:251], v[84:87]\n"
    "v_mfma_f32_16x16x32_bf16 v[92:95], v[32:35], a[252:255], v[92:95]\n"
    "v_mfma_f32_16x16x32_bf16 v[88:91], v[28:31], a[252:255], v[88:91]\n"
    "s_branch LITB%=\n"
    "LITA%=:\n"
    "v_mfma_f32_16x16x32_bf16 v[80:83], v[36:39], a[216:219], v[80:83]\n"
    "ds_read_b128 v[28:31], v8 offset:960\n"
    "v_mfma_f32_16x16x32_bf16 v[84:87], v[40:43], a[216:219], v[84:87]\n"
    "ds_read_b128 v[32:35], v8 offset:17856\n"
    "s_add_u32 s39, s44, 67584\n"
    "s_add_u32 s69, s63, 4096\n"
    "v_mfma_f32_16x16x32_bf16 v[92:95], v[40:43], a[220:223], v[92:95]\n"
    "s_add_u32 m0, s39, 0\n"
    "s_nop 0\n"
    "buffer_load_dwordx4 v0, s[16:19], s63 offen offset:0 sc1 lds\n"
    "v_mfma_f32_16x16x32_bf16 v[88:91], v[36:39], a[220:223], v[88:91]\n"
    "v_mfma_f32_16x16x32_bf16 v[80:83], v[44:47], a[224:227], v[80:83]\n"
    "v_mfma_f32_16x16x32_bf16 v[84:87], v[48:51], a[224:227], v[84:87]\n"
    "s_add_u32 m0, s39, 32\n"
    "s_nop 0\n"
    "buffer_load_dwordx4 v0, s[16:19], s63 offen offset:1024 sc1 lds\n"
    "v_mfma_f32_16x16x32_bf16 v[92:95], v[48:51], a[228:231], v[92:95]\n"
    "s_add_u32 m0, s39, 64\n"
    "s_nop 0\n"
    "buffer_load_dwordx4 v0, s[16:19], s63 offen offset:2048 sc1 lds\n"
    "v_mfma_f32_16x16x32_bf16 v[88:91], v[44:47], a[228:231], v[88:91]\n"
    "v_mfma_f32_16x16x32_bf16 v[80:83], v[52:55], a[232:235], v[80:83]\n"
    "v_mfma_f32_16x16x32_bf16 v[84:87], v[56:59], a[232:235], v[84:87]\n"
    "s_add_u32 m0, s39, 96\n"
    "s_nop 0\n"
    "buffer_load_dwordx4 v0, s[16:19], s63 offen offset:3072 sc1 lds\n"
    "v_mfma_f32_16x16x32_bf16 v[92:95], v[56:59], a[236:239], v[92:95]\n"
    "s_add_u32 m0, s39, 4224\n"
    "s_nop 0\n"
    "buffer_load_dwordx4 v0, s[16:19], s69 offen offset:0 sc1 lds\n"
    "v_mfma_f32_16x16x32_bf16 v[88:91], v[52:55], a[236:239], v[88:91]\n"
    "s_waitcnt lgkmcnt(3)\n"
    "v_mfma_f32_16x16x32_bf16 v[80:83], v[20:23], a[240:243], v[80:83]\n"
    "s_waitcnt lgkmcnt(2)\n"
    "v_mfma_f32_16x16x32_bf16 v[84:87], v[24:27], a[240:243], v[84:87]\n"
    "s_add_u32 m0, s39, 4256\n"
    "s_nop 0\n"
    "buffer_load_dwordx4 v0, s[16:19], s69 offen offset:1024 sc1 lds\n"
    "v_mfma_f32_16x16x32_bf16 v[92:95], v[24:27], a[244:247], v[92:95]\n"
    "s_add_u32 m0, s39, 4288\n"
    "s_nop 0\n"
    "buffer_load_dwordx4 v0, s[16:19], s69 offen offset:2048 sc1 lds\n"
    "v_mfma_f32_16x16x32_bf16 v[88:91], v[20:23], a[244:247], v[88:91]\n"
    "s_waitcnt lgkmcnt(1)\n"
    "v_mfma_f32_16x16x32_bf16 v[80:83], v[28:31], a[248:251], v[80:83]\n"
    "s_waitcnt lgkmcnt(0)\n"
    "v_mfma_f32_16x16x32_bf16 v[84:87], v[32:35], a[248:251], v[84:87]\n"
    "s_add_u32 m0, s39, 4320\n"
    "s_nop 0\n"
    "buffer_load_dwordx4 v0, s[16:19], s69 offen offset:3072 sc1 lds\n"
    "v_mfma_f32_16x16x32_bf16 v[92:95], v[32:35], a[252:255], v[92:95]\n"
    "v_mfma_f32_16x16x32_bf16 v[88:91], v[28:31], a[252:255], v[88:91]\n"
    "LITB%=:\n"
    "s_nop 7\n"
    "v_cvt_pk_bf16_f32 v12, v80, v81\n"
    "v_cvt_pk_bf16_f32 v13, v82, v83\n"
    "v_cvt_pk_bf16_f32 v14, v84, v85\n"
    "v_cvt_pk_bf16_f32 v15, v86, v87\n"
    "buffer_store_dwordx4 v[12:15], v0, s[16:19], s59 offen sc1\n"
    "v_cvt_pk_bf16_f32 v16, v88, v89\n"
    "v_cvt_pk_bf16_f32 v17, v90, v91\n"
    "v_cvt_pk_bf16_f32 v18, v92, v93\n"
    "v_cvt_pk_bf16_f32 v19, v94, v95\n"
    "buffer_store_dwordx4 v[16:19], v0, s[16:19], s62 offen sc1\n"
    "s_mov_b32 s52, s64\n"
    "LIE%=:\n"
    "s_cmp_lg_u32 s60, 0\n"
    "s_cbranch_scc1 L2%=\n"
    "s_branch LT%=\n"
    "LX%=:\n"
    "s_barrier\n"
    "s_cmp_eq_u32 s60, 0\n"
    "s_cbranch_scc1 LT%=\n"
    "s_branch LE%=\n"
    "LRD%=:\n"
    "s_waitcnt lgkmcnt(0)\n"
    "s_barrier\n"
    "s_add_u32 s27, s27, 1\n"
    "s_cmp_gt_u32 s27, 0x40000\n"
    "s_cbranch_scc1 LE%=\n"
    "s_add_u32 s39, s44, 67584\n"
    "s_add_u32 s69, s63, 4096\n"
    "s_add_u32 m0, s39, 0\n"
    "s_nop 0\n"
    "buffer_load_dwordx4 v0, s[16:19], s63 offen offset:0 sc1 lds\n"
    "s_add_u32 m0, s39, 32\n"
    "s_nop 0\n"
    "buffer_load_dwordx4 v0, s[16:19], s63 offen offset:1024 sc1 lds\n"
    "s_add_u32 m0, s39, 64\n"
    "s_nop 0\n"
    "buffer_load_dwordx4 v0, s[16:19], s63 offen offset:2048 sc1 lds\n"
    "s_add_u32 m0, s39, 96\n"
    "s_nop 0\n"
    "buffer_load_dwordx4 v0, s[16:19], s63 offen offset:3072 sc1 lds\n"
    "s_add_u32 m0, s39, 4224\n"
    "s_nop 0\n"
    "buffer_load_dwordx4 v0, s[16:19], s69 offen offset:0 sc1 lds\n"
    "s_add_u32 m0, s39, 4256\n"
    "s_nop 0\n"
    "buffer_load_dwordx4 v0, s[16:19], s69 offen offset:1024 sc1 lds\n"
    "s_add_u32 m0, s39, 4288\n"
    "s_nop 0\n"
    "buffer_load_dwordx4 v0, s[16:19], s69 offen offset:2048 sc1 lds\n"
    "s_add_u32 m0, s39, 4320\n"
    "s_nop 0\n"
    "buffer_load_dwordx4 v0, s[16:19], s69 offen offset:3072 sc1 lds\n"
    "s_waitcnt vmcnt(0)\n"
    "s_branch LG%=\n"
    "LK0_%=:\n"
    "s_mov_b32 s37, 0x80000\n"
    "LL0_%=:\n"
    "buffer_load_dword v5, off, s[16:19], s67 sc1\n"
    "s_waitcnt vmcnt(0)\n"
    "v_readfirstlane_b32 s38, v5\n"
    "s_cmp_lg_u32 s38, 0\n"
    "s_cbranch_scc1 LJ0_%=\n"
    "s_sleep 1\n"
    "s_sub_u32 s37, s37, 1\n"
    "s_cbranch_scc0 LL0_%=\n"
    "s_endpgm\n"
    "LK1_%=:\n"
    "s_mov_b32 s37, 0x80000\n"
    "LL1_%=:\n"
    "buffer_load_dword v5, off, s[16:19], s67 sc1\n"
    "s_waitcnt vmcnt(0)\n"
    "v_readfirstlane_b32 s38, v5\n"
    "s_cmp_lg_u32 s38, 0\n"
    "s_cbranch_scc1 LJ1_%=\n"
    "s_sleep 1\n"
    "s_sub_u32 s37, s37, 1\n"
    "s_cbranch_scc0 LL1_%=\n"
    "s_endpgm\n"
    "LK2_%=:\n"
    "s_mov_b32 s37, 0x80000\n"
    "LL2_%=:\n"
    "buffer_load_dword v5, off, s[16:19], s67 sc1\n"
    "s_waitcnt vmcnt(0)\n"
    "v_readfirstlane_b32 s38, v5\n"
    "s_cmp_lg_u32 s38, 0\n"
    "s_cbranch_scc1 LJ2_%=\n"
    "s_sleep 1\n"
    "s_sub_u32 s37, s37, 1\n"
    "s_cbranch_scc0 LL2_%=\n"
    "s_endpgm\n"
    "LK3_%=:\n"
    "s_mov_b32 s37, 0x80000\n"
    "LL3_%=:\n"
    "buffer_load_dword v5, off, s[16:19], s67 sc1\n"
    "s_waitcnt vmcnt(0)\n"
    "v_readfirstlane_b32 s38, v5\n"
    "s_cmp_lg_u32 s38, 0\n"
    "s_cbranch_scc1 LJ3_%=\n"
    "s_sleep 1\n"
    "s_sub_u32 s37, s37, 1\n"
    "s_cbranch_scc0 LL3_%=\n"
    "s_endpgm\n"
    "LK4_%=:\n"
    "s_mov_b32 s37, 0x80000\n"
    "LL4_%=:\n"
    "buffer_load_dword v5, off, s[16:19], s67 sc1\n"
    "s_waitcnt vmcnt(0)\n"
    "v_readfirstlane_b32 s38, v5\n"
    "s_cmp_lg_u32 s38, 0\n"
    "s_cbranch_scc1 LJ4_%=\n"
    "s_sleep 1\n"
    "s_sub_u32 s37, s37, 1\n"
    "s_cbranch_scc0 LL4_%=\n"
    "s_endpgm\n"
    "LE%=:\n"
    "s_endpgm\n"
    :
    : "{v0}"(hv0), "{v1}"(hv1), "{v2}"(hv2), "{v3}"(hv3), "{v4}"(hv4), "{v5}"(hv5)
    : "v255", "a255", "s101", "vcc", "scc", "memory");
  __builtin_unreachable();
}
__device__ __forceinline__ void o_role8(int w, int V, int u, const unsigned* rdy, char* ws, const u16* Wo) {
  const int lane = lane_id(), g = lane >> 4, q = (lane >> 2) & 3, p = lane & 3, hg = u >> 2, cs = u & 3;
  const unsigned me = w == 1 ? 0u : OOR, c1 = 0u, co = V || !SW ? 0x2000u : 0u;
  const int pv[19] = {w, (V == 1 ? 4 : 3) * 524288 + hg * 131072 + w * 2048, (int)(V ? c1 ^ 0x2000u : c1), (int)co, (int)((nearof(Z_FO + V * 16384, V ? c1 ^ 0x2000u : c1) + hg * 2048) | (w ? OOR : 0u)), (int)((nearof(Z_FP + V * 16384, co) + u * 256) | me),
                      (int)(Z_PART + V * MBY(16)) + u * 16384 + w * 2048, (int)Z_ERR, (int)(size_t)Wo, (int)((size_t)Wo >> 32) & 0xffff, (int)(size_t)ws, (int)((size_t)ws >> 32) & 0xffff, (int)ZWS_BYTES,
                      (int)((Z_DBG + MBY(2) + (V * 16 + u) * 1024) | me), (int)(size_t)rdy, (int)((size_t)rdy >> 32), V ? ODL : 0, (int)(u ? OOR & ~me : 4096u), V < ROLL ? 3 : 63};
  int hv0 = lane * 16, hv1 = 131072 + (lane & 31) * 272 + w * 64 + (lane >> 5) * 16, hv2 = 131072 + (8 * w + g) * 272 + (lane & 15) * 16;
  int hv3 = (hg * 1024 + w * 128 + g) * 1024 + cs * 256 + ((lane & 15) ^ (g * 4)) * 16, hv4 = (4 * (g >> 1) + q) * 256 + 64 * (w ^ q) + 32 * (g & 1) + 16 * (p & 1) + 8 * (p >> 1), hv5 = 0;
#pragma unroll
  for (int i = 0; i < 19; ++i) hv5 = lane == i ? pv[i] : hv5;
  asm volatile(
    "s_nop 1\n"
    "v_readlane_b32 s36, v5, 0\n"
    "v_readlane_b32 s41, v5, 1\n"
    "v_readlane_b32 s42, v5, 2\n"
    "v_readlane_b32 s43, v5, 3\n"
    "v_readlane_b32 s44, v5, 4\n"
    "v_readlane_b32 s45, v5, 5\n"
    "v_readlane_b32 s46, v5, 6\n"
    "v_readlane_b32 s47, v5, 7\n"
    "v_readlane_b32 s20, v5, 8\n"
    "v_readlane_b32 s21, v5, 9\n"
    "v_readlane_b32 s16, v5, 10\n"
    "v_readlane_b32 s17, v5, 11\n"
    "v_readlane_b32 s18, v5, 12\n"
    "v_readlane_b32 s51, v5, 13\n"
    "v_readlane_b32 s24, v5, 14\n"
    "v_readlane_b32 s25, v5, 15\n"
    "v_readlane_b32 s53, v5, 16\n"
    "v_readlane_b32 s52, v5, 17\n"
    "v_readlane_b32 s55, v5, 18\n"
    "v_mov_b32 v5, 1\n"
    "s_mov_b32 s19, 0x20000\n"
    "s_mov_b32 s22, 0x400000\n"
    "s_mov_b32 s23, 0x20000\n"
    "s_lshl_b32 s39, s36, 15\n"
    "s_lshl_b32 s49, s36, 11\n"
    "s_mov_b32 s40, 0\n"
    "s_add_u32 m0, s39, 0\n"
    "s_mov_b32 s38, 0\n"
    "buffer_load_dwordx4 v3, s[20:23], s38 offen lds\n"
    "s_add_u32 m0, s39, 1024\n"
    "s_mov_b32 s38, 4096\n"
    "buffer_load_dwordx4 v3, s[20:23], s38 offen lds\n"
    "s_add_u32 m0, s39, 2048\n"
    "s_mov_b32 s38, 8192\n"
    "buffer_load_dwordx4 v3, s[20:23], s38 offen lds\n"
    "s_add_u32 m0, s39, 3072\n"
    "s_mov_b32 s38, 12288\n"
    "buffer_load_dwordx4 v3, s[20:23], s38 offen lds\n"
    "s_add_u32 m0, s39, 4096\n"
    "s_mov_b32 s38, 16384\n"
    "buffer_load_dwordx4 v3, s[20:23], s38 offen lds\n"
    "s_add_u32 m0, s39, 5120\n"
    "s_mov_b32 s38, 20480\n"
    "buffer_load_dwordx4 v3, s[20:23], s38 offen lds\n"
    "s_add_u32 m0, s39, 6144\n"
    "s_mov_b32 s38, 24576\n"
    "buffer_load_dwordx4 v3, s[20:23], s38 offen lds\n"
    "s_add_u32 m0, s39, 7168\n"
    "s_mov_b32 s38, 28672\n"
    "buffer_load_dwordx4 v3, s[20:23], s38 offen lds\n"
    "s_add_u32 m0, s39, 8192\n"
    "s_mov_b32 s38, 32768\n"
    "buffer_load_dwordx4 v3, s[20:23], s38 offen lds\n"
    "s_add_u32 m0, s39, 9216\n"
    "s_mov_b32 s38, 36864\n"
    "buffer_load_dwordx4 v3, s[20:23], s38 offen lds\n"
    "s_add_u32 m0, s39, 10240\n"
    "s_mov_b32 s38, 40960\n"
    "buffer_load_dwordx4 v3, s[20:23], s38 offen lds\n"
    "s_add_u32 m0, s39, 11264\n"
    "s_mov_b32 s38, 45056\n"
    "buffer_load_dwordx4 v3, s[20:23], s38 offen lds\n"
    "s_add_u32 m0, s39, 12288\n"
    "s_mov_b32 s38, 49152\n"
    "buffer_load_dwordx4 v3, s[20:23], s38 offen lds\n"
    "s_add_u32 m0, s39, 13312\n"
    "s_mov_b32 s38, 53248\n"
    "buffer_load_dwordx4 v3, s[20:23], s38 offen lds\n"
    "s_add_u32 m0, s39, 14336\n"
    "s_mov_b32 s38, 57344\n"
    "buffer_load_dwordx4 v3, s[20:23], s38 offen lds\n"
    "s_add_u32 m0, s39, 15360\n"
    "s_mov_b32 s38, 61440\n"
    "buffer_load_dwordx4 v3, s[20:23], s38 offen lds\n"
    "s_add_u32 m0, s39, 16384\n"
    "s_mov_b32 s38, 65536\n"
    "buffer_load_dwordx4 v3, s[20:23], s38 offen lds\n"
    "s_add_u32 m0, s39, 17408\n"
    "s_mov_b32 s38, 69632\n"
    "buffer_load_dwordx4 v3, s[20:23], s38 offen lds\n"
    "s_add_u32 m0, s39, 18432\n"
    "s_mov_b32 s38, 73728\n"
    "buffer_load_dwordx4 v3, s[20:23], s38 offen lds\n"
    "s_add_u32 m0, s39, 19456\n"
    "s_mov_b32 s38, 77824\n"
    "buffer_load_dwordx4 v3, s[20:23], s38 offen lds\n"
    "s_add_u32 m0, s39, 20480\n"
    "s_mov_b32 s38, 81920\n"
    "buffer_load_dwordx4 v3, s[20:23], s38 offen lds\n"
    "s_add_u32 m0, s39, 21504\n"
    "s_mov_b32 s38, 86016\n"
    "buffer_load_dwordx4 v3, s[20:23], s38 offen lds\n"
    "s_add_u32 m0, s39, 22528\n"
    "s_mov_b32 s38, 90112\n"
    "buffer_load_dwordx4 v3, s[20:23], s38 offen lds\n"
    "s_add_u32 m0, s39, 23552\n"
    "s_mov_b32 s38, 94208\n"
    "buffer_load_dwordx4 v3, s[20:23], s38 offen lds\n"
    "s_add_u32 m0, s39, 24576\n"
    "s_mov_b32 s38, 98304\n"
    "buffer_load_dwordx4 v3, s[20:23], s38 offen lds\n"
    "s_add_u32 m0, s39, 25600\n"
    "s_mov_b32 s38, 102400\n"
    "buffer_load_dwordx4 v3, s[20:23], s38 offen lds\n"
    "s_add_u32 m0, s39, 26624\n"
    "s_mov_b32 s38, 106496\n"
    "buffer_load_dwordx4 v3, s[20:23], s38 offen lds\n"
    "s_add_u32 m0, s39, 27648\n"
    "s_mov_b32 s38, 110592\n"
    "buffer_load_dwordx4 v3, s[20:23], s38 offen lds\n"
    "s_add_u32 m0, s39, 28672\n"
    "s_mov_b32 s38, 114688\n"
    "buffer_load_dwordx4 v3, s[20:23], s38 offen lds\n"
    "s_add_u32 m0, s39, 29696\n"
    "s_mov_b32 s38, 118784\n"
    "buffer_load_dwordx4 v3, s[20:23], s38 offen lds\n"
    "s_add_u32 m0, s39, 30720\n"
    "s_mov_b32 s38, 122880\n"
    "buffer_load_dwordx4 v3, s[20:23], s38 offen lds\n"
    "s_add_u32 m0, s39, 31744\n"
    "s_mov_b32 s38, 126976\n"
    "buffer_load_dwordx4 v3, s[20:23], s38 offen lds\n"
    "s_waitcnt vmcnt(0)\n"
    "s_barrier\n"
    "v_add_u32 v7, 0, v4\n"
    "ds_read_b64_tr_b16 a[0:1], v7 offset:0\n"
    "ds_read_b64_tr_b16 a[2:3], v7 offset:4096\n"
    "ds_read_b64_tr_b16 a[4:5], v7 offset:2048\n"
    "ds_read_b64_tr_b16 a[6:7], v7 offset:6144\n"
    "ds_read_b64_tr_b16 a[8:9], v7 offset:8192\n"
    "ds_read_b64_tr_b16 a[10:11], v7 offset:12288\n"
    "ds_read_b64_tr_b16 a[12:13], v7 offset:10240\n"
    "ds_read_b64_tr_b16 a[14:15], v7 offset:14336\n"
    "ds_read_b64_tr_b16 a[16:17], v7 offset:16384\n"
    "ds_read_b64_tr_b16 a[18:19], v7 offset:20480\n"
    "ds_read_b64_tr_b16 a[20:21], v7 offset:18432\n"
    "ds_read_b64_tr_b16 a[22:23], v7 offset:22528\n"
    "ds_read_b64_tr_b16 a[24:25], v7 offset:24576\n"
    "ds_read_b64_tr_b16 a[26:27], v7 offset:28672\n"
    "ds_read_b64_tr_b16 a[28:29], v7 offset:26624\n"
    "s_waitcnt lgkmcnt(11)\n"
    "ds_read_b64_tr_b16 a[30:31], v7 offset:30720\n"
    "ds_read_b64_tr_b16 a[32:33], v7 offset:32768\n"
    "ds_read_b64_tr_b16 a[34:35], v7 offset:36864\n"
    "ds_read_b64_tr_b16 a[36:37], v7 offset:34816\n"
    "s_waitcnt lgkmcnt(11)\n"
    "ds_read_b64_tr_b16 a[38:39], v7 offset:38912\n"
    "ds_read_b64_tr_b16 a[40:41], v7 offset:40960\n"
    "ds_read_b64_tr_b16 a[42:43], v7 offset:45056\n"
    "ds_read_b64_tr_b16 a[44:45], v7 offset:43008\n"
    "s_waitcnt lgkmcnt(11)\n"
    "ds_read_b64_tr_b16 a[46:47], v7 offset:47104\n"
    "ds_read_b64_tr_b16 a[48:49], v7 offset:49152\n"
    "ds_read_b64_tr_b16 a[50:51], v7 offset:53248\n"
    "ds_read_b64_tr_b16 a[52:53], v7 offset:51200\n"
    "s_waitcnt lgkmcnt(11)\n"
    "ds_read_b64_tr_b16 a[54:55], v7 offset:55296\n"
    "ds_read_b64_tr_b16 a[56:57], v7 offset:57344\n"
    "ds_read_b64_tr_b16 a[58:59], v7 offset:61440\n"
    "ds_read_b64_tr_b16 a[60:61], v7 offset:59392\n"
    "s_waitcnt lgkmcnt(11)\n"
    "ds_read_b64_tr_b16 a[62:63], v7 offset:63488\n"
    "s_waitcnt lgkmcnt(0)\n"
    "v_add_u32 v7, 65536, v4\n"
    "ds_read_b64_tr_b16 a[64:65], v7 offset:0\n"
    "ds_read_b64_tr_b16 a[66:67], v7 offset:4096\n"
    "ds_read_b64_tr_b16 a[68:69], v7 offset:2048\n"
    "ds_read_b64_tr_b16 a[70:71], v7 offset:6144\n"
    "ds_read_b64_tr_b16 a[72:73], v7 offset:8192\n"
    "ds_read_b64_tr_b16 a[74:75], v7 offset:12288\n"
    "ds_read_b64_tr_b16 a[76:77], v7 offset:10240\n"
    "ds_read_b64_tr_b16 a[78:79], v7 offset:14336\n"
    "ds_read_b64_tr_b16 a[80:81], v7 offset:16384\n"
    "ds_read_b64_tr_b16 a[82:83], v7 offset:20480\n"
    "ds_read_b64_tr_b16 a[84:85], v7 offset:18432\n"
    "ds_read_b64_tr_b16 a[86:87], v7 offset:22528\n"
    "ds_read_b64_tr_b16 a[88:89], v7 offset:24576\n"
    "ds_read_b64_tr_b16 a[90:91], v7 offset:28672\n"
    "ds_read_b64_tr_b16 a[92:93], v7 offset:26624\n"
    "s_waitcnt lgkmcnt(11)\n"
    "ds_read_b64_tr_b16 a[94:95], v7 offset:30720\n"
    "ds_read_b64_tr_b16 a[96:97], v7 offset:32768\n"
    "ds_read_b64_tr_b16 a[98:99], v7 offset:36864\n"
    "ds_read_b64_tr_b16 a[100:101], v7 offset:34816\n"
    "s_waitcnt lgkmcnt(11)\n"
    "ds_read_b64_tr_b16 a[102:103], v7 offset:38912\n"
    "ds_read_b64_tr_b16 a[104:105], v7 offset:40960\n"
    "ds_read_b64_tr_b16 a[106:107], v7 offset:45056\n"
    "ds_read_b64_tr_b16 a[108:109], v7 offset:43008\n"
    "s_waitcnt lgkmcnt(11)\n"
    "ds_read_b64_tr_b16 a[110:111], v7 offset:47104\n"
    "ds_read_b64_tr_b16 a[112:113], v7 offset:49152\n"
    "ds_read_b64_tr_b16 a[114:115], v7 offset:53248\n"
    "ds_read_b64_tr_b16 a[116:117], v7 offset:51200\n"
    "s_waitcnt lgkmcnt(11)\n"
    "ds_read_b64_tr_b16 a[118:119], v7 offset:55296\n"
    "ds_read_b64_tr_b16 a[120:121], v7 offset:57344\n"
    "ds_read_b64_tr_b16 a[122:123], v7 offset:61440\n"
    "ds_read_b64_tr_b16 a[124:125], v7 offset:59392\n"
    "s_waitcnt lgkmcnt(11)\n"
    "ds_read_b64_tr_b16 a[126:127], v7 offset:63488\n"
    "s_waitcnt lgkmcnt(0)\n"
    "s_barrier\n"
    "s_add_u32 m0, s39, 0\n"
    "s_mov_b32 s38, 524288\n"
    "buffer_load_dwordx4 v3, s[20:23], s38 offen lds\n"
    "s_add_u32 m0, s39, 1024\n"
    "s_mov_b32 s38, 528384\n"
    "buffer_load_dwordx4 v3, s[20:23], s38 offen lds\n"
    "s_add_u32 m0, s39, 2048\n"
    "s_mov_b32 s38, 532480\n"
    "buffer_load_dwordx4 v3, s[20:23], s38 offen lds\n"
    "s_add_u32 m0, s39, 3072\n"
    "s_mov_b32 s38, 536576\n"
    "buffer_load_dwordx4 v3, s[20:23], s38 offen lds\n"
    "s_add_u32 m0, s39, 4096\n"
    "s_mov_b32 s38, 540672\n"
    "buffer_load_dwordx4 v3, s[20:23], s38 offen lds\n"
    "s_add_u32 m0, s39, 5120\n"
    "s_mov_b32 s38, 544768\n"
    "buffer_load_dwordx4 v3, s[20:23], s38 offen lds\n"
    "s_add_u32 m0, s39, 6144\n"
    "s_mov_b32 s38, 548864\n"
    "buffer_load_dwordx4 v3, s[20:23], s38 offen lds\n"
    "s_add_u32 m0, s39, 7168\n"
    "s_mov_b32 s38, 552960\n"
    "buffer_load_dwordx4 v3, s[20:23], s38 offen lds\n"
    "s_add_u32 m0, s39, 8192\n"
    "s_mov_b32 s38, 557056\n"
    "buffer_load_dwordx4 v3, s[20:23], s38 offen lds\n"
    "s_add_u32 m0, s39, 9216\n"
    "s_mov_b32 s38, 561152\n"
    "buffer_load_dwordx4 v3, s[20:23], s38 offen lds\n"
    "s_add_u32 m0, s39, 10240\n"
    "s_mov_b32 s38, 565248\n"
    "buffer_load_dwordx4 v3, s[20:23], s38 offen lds\n"
    "s_add_u32 m0, s39, 11264\n"
    "s_mov_b32 s38, 569344\n"
    "buffer_load_dwordx4 v3, s[20:23], s38 offen lds\n"
    "s_add_u32 m0, s39, 12288\n"
    "s_mov_b32 s38, 573440\n"
    "buffer_load_dwordx4 v3, s[20:23], s38 offen lds\n"
    "s_add_u32 m0, s39, 13312\n"
    "s_mov_b32 s38, 577536\n"
    "buffer_load_dwordx4 v3, s[20:23], s38 offen lds\n"
    "s_add_u32 m0, s39, 14336\n"
    "s_mov_b32 s38, 581632\n"
    "buffer_load_dwordx4 v3, s[20:23], s38 offen lds\n"
    "s_add_u32 m0, s39, 15360\n"
    "s_mov_b32 s38, 585728\n"
    "buffer_load_dwordx4 v3, s[20:23], s38 offen lds\n"
    "s_add_u32 m0, s39, 16384\n"
    "s_mov_b32 s38, 589824\n"
    "buffer_load_dwordx4 v3, s[20:23], s38 offen lds\n"
    "s_add_u32 m0, s39, 17408\n"
    "s_mov_b32 s38, 593920\n"
    "buffer_load_dwordx4 v3, s[20:23], s38 offen lds\n"
    "s_add_u32 m0, s39, 18432\n"
    "s_mov_b32 s38, 598016\n"
    "buffer_load_dwordx4 v3, s[20:23], s38 offen lds\n"
    "s_add_u32 m0, s39, 19456\n"
    "s_mov_b32 s38, 602112\n"
    "buffer_load_dwordx4 v3, s[20:23], s38 offen lds\n"
    "s_add_u32 m0, s39, 20480\n"
    "s_mov_b32 s38, 606208\n"
    "buffer_load_dwordx4 v3, s[20:23], s38 offen lds\n"
    "s_add_u32 m0, s39, 21504\n"
    "s_mov_b32 s38, 610304\n"
    "buffer_load_dwordx4 v3, s[20:23], s38 offen lds\n"
    "s_add_u32 m0, s39, 22528\n"
    "s_mov_b32 s38, 614400\n"
    "buffer_load_dwordx4 v3, s[20:23], s38 offen lds\n"
    "s_add_u32 m0, s39, 23552\n"
    "s_mov_b32 s38, 618496\n"
    "buffer_load_dwordx4 v3, s[20:23], s38 offen lds\n"
    "s_add_u32 m0, s39, 24576\n"
    "s_mov_b32 s38, 622592\n"
    "buffer_load_dwordx4 v3, s[20:23], s38 offen lds\n"
    "s_add_u32 m0, s39, 25600\n"
    "s_mov_b32 s38, 626688\n"
    "buffer_load_dwordx4 v3, s[20:23], s38 offen lds\n"
    "s_add_u32 m0, s39, 26624\n"
    "s_mov_b32 s38, 630784\n"
    "buffer_load_dwordx4 v3, s[20:23], s38 offen lds\n"
    "s_add_u32 m0, s39, 27648\n"
    "s_mov_b32 s38, 634880\n"
    "buffer_load_dwordx4 v3, s[20:23], s38 offen lds\n"
    "s_add_u32 m0, s39, 28672\n"
    "s_mov_b32 s38, 638976\n"
    "buffer_load_dwordx4 v3, s[20:23], s38 offen lds\n"
    "s_add_u32 m0, s39, 29696\n"
    "s_mov_b32 s38, 643072\n"
    "buffer_load_dwordx4 v3, s[20:23], s38 offen lds\n"
    "s_add_u32 m0, s39, 30720\n"
    "s_mov_b32 s38, 647168\n"
    "buffer_load_dwordx4 v3, s[20:23], s38 offen lds\n"
    "s_add_u32 m0, s39, 31744\n"
    "s_mov_b32 s38, 651264\n"
    "buffer_load_dwordx4 v3, s[20:23], s38 offen lds\n"
    "s_waitcnt vmcnt(0)\n"
    "s_barrier\n"
    "v_add_u32 v7, 0, v4\n"
    "ds_read_b64_tr_b16 a[128:129], v7 offset:0\n"
    "ds_read_b64_tr_b16 a[130:131], v7 offset:4096\n"
    "ds_read_b64_tr_b16 a[132:133], v7 offset:2048\n"
    "ds_read_b64_tr_b16 a[134:135], v7 offset:6144\n"
    "ds_read_b64_tr_b16 a[136:137], v7 offset:8192\n"
    "ds_read_b64_tr_b16 a[138:139], v7 offset:12288\n"
    "ds_read_b64_tr_b16 a[140:141], v7 offset:10240\n"
    "ds_read_b64_tr_b16 a[142:143], v7 offset:14336\n"
    "ds_read_b64_tr_b16 a[144:145], v7 offset:16384\n"
    "ds_read_b64_tr_b16 a[146:147], v7 offset:20480\n"
    "ds_read_b64_tr_b16 a[148:149], v7 offset:18432\n"
    "ds_read_b64_tr_b16 a[150:151], v7 offset:22528\n"
    "ds_read_b64_tr_b16 a[152:153], v7 offset:24576\n"
    "ds_read_b64_tr_b16 a[154:155], v7 offset:28672\n"
    "ds_read_b64_tr_b16 a[156:157], v7 offset:26624\n"
    "s_waitcnt lgkmcnt(11)\n"
    "ds_read_b64_tr_b16 a[158:159], v7 offset:30720\n"
    "ds_read_b64_tr_b16 a[160:161], v7 offset:32768\n"
    "ds_read_b64_tr_b16 a[162:163], v7 offset:36864\n"
    "ds_read_b64_tr_b16 a[164:165], v7 offset:34816\n"
    "s_waitcnt lgkmcnt(11)\n"
    "ds_read_b64_tr_b16 a[166:167], v7 offset:38912\n"
    "ds_read_b64_tr_b16 a[168:169], v7 offset:40960\n"
    "ds_read_b64_tr_b16 a[170:171], v7 offset:45056\n"
    "ds_read_b64_tr_b16 a[172:173], v7 offset:43008\n"
    "s_waitcnt lgkmcnt(11)\n"
    "ds_read_b64_tr_b16 a[174:175], v7 offset:47104\n"
    "ds_read_b64_tr_b16 a[176:177], v7 offset:49152\n"
    "ds_read_b64_tr_b16 a[178:179], v7 offset:53248\n"
    "ds_read_b64_tr_b16 a[180:181], v7 offset:51200\n"
    "s_waitcnt lgkmcnt(11)\n"
    "ds_read_b64_tr_b16 a[182:183], v7 offset:55296\n"
    "ds_read_b64_tr_b16 a[184:185], v7 offset:57344\n"
    "ds_read_b64_tr_b16 a[186:187], v7 offset:61440\n"
    "ds_read_b64_tr_b16 a[188:189], v7 offset:59392\n"
    "s_waitcnt lgkmcnt(11)\n"
    "ds_read_b64_tr_b16 a[190:191], v7 offset:63488\n"
    "s_waitcnt lgkmcnt(0)\n"
    "v_add_u32 v7, 65536, v4\n"
    "ds_read_b64_tr_b16 a[192:193], v7 offset:0\n"
    "ds_read_b64_tr_b16 a[194:195], v7 offset:4096\n"
    "ds_read_b64_tr_b16 a[196:197], v7 offset:2048\n"
    "ds_read_b64_tr_b16 a[198:199], v7 offset:6144\n"
    "ds_read_b64_tr_b16 a[200:201], v7 offset:8192\n"
    "ds_read_b64_tr_b16 a[202:203], v7 offset:12288\n"
    "ds_read_b64_tr_b16 a[204:205], v7 offset:10240\n"
    "ds_read_b64_tr_b16 a[206:207], v7 offset:14336\n"
    "ds_read_b64_tr_b16 a[208:209], v7 offset:16384\n"
    "ds_read_b64_tr_b16 a[210:211], v7 offset:20480\n"
    "ds_read_b64_tr_b16 a[212:213], v7 offset:18432\n"
    "ds_read_b64_tr_b16 a[214:215], v7 offset:22528\n"
    "ds_read_b64_tr_b16 a[216:217], v7 offset:24576\n"
    "ds_read_b64_tr_b16 a[218:219], v7 offset:28672\n"
    "ds_read_b64_tr_b16 a[220:221], v7 offset:26624\n"
    "s_waitcnt lgkmcnt(11)\n"
    "ds_read_b64_tr_b16 a[222:223], v7 offset:30720\n"
    "ds_read_b64_tr_b16 a[224:225], v7 offset:32768\n"
    "ds_read_b64_tr_b16 a[226:227], v7 offset:36864\n"
    "ds_read_b64_tr_b16 a[228:229], v7 offset:34816\n"
    "s_waitcnt lgkmcnt(11)\n"
    "ds_read_b64_tr_b16 a[230:231], v7 offset:38912\n"
    "ds_read_b64_tr_b16 a[232:233], v7 offset:40960\n"
    "ds_read_b64_tr_b16 a[234:235], v7 offset:45056\n"
    "ds_read_b64_tr_b16 a[236:237], v7 offset:43008\n"
    "s_waitcnt lgkmcnt(11)\n"
    "ds_read_b64_tr_b16 a[238:239], v7 offset:47104\n"
    "ds_read_b64_tr_b16 a[240:241], v7 offset:49152\n"
    "ds_read_b64_tr_b16 a[242:243], v7 offset:53248\n"
    "ds_read_b64_tr_b16 a[244:245], v7 offset:51200\n"
    "s_waitcnt lgkmcnt(11)\n"
    "ds_read_b64_tr_b16 a[246:247], v7 offset:55296\n"
    "ds_read_b64_tr_b16 a[248:249], v7 offset:57344\n"
    "ds_read_b64_tr_b16 a[250:251], v7 offset:61440\n"
    "ds_read_b64_tr_b16 a[252:253], v7 offset:59392\n"
    "s_waitcnt lgkmcnt(11)\n"
    "ds_read_b64_tr_b16 a[254:255], v7 offset:63488\n"
    "s_waitcnt lgkmcnt(0)\n"
    "s_barrier\n"
    "v_mov_b32 v7, 0\n"
    "s_mov_b32 s37, 0x80000\n"
    "LRY%=:\n"
    "global_load_dword v6, v7, s[24:25] sc1\n"
    "s_waitcnt vmcnt(0)\n"
    "v_readfirstlane_b32 s38, v6\n"
    "s_cmp_lg_u32 s38, 0\n"
    "s_cbranch_scc1 LRZ%=\n"
    "s_sleep 8\n"
    "s_sub_u32 s37, s37, 1\n"
    "s_cbranch_scc0 LRY%=\n"
    "s_endpgm\n"
    "LRZ%=:\n"
    "s_lshl_b32 s38, s38, 13\n"
    "s_and_b32 s38, s38, 0x2000\n"
    "s_xor_b32 s42, s42, s38\n"
    "s_xor_b32 s43, s43, s38\n"
    "s_xor_b32 s44, s44, s38\n"
    "s_xor_b32 s45, s45, s38\n"
    "LDL%=:\n"
    "s_sub_u32 s53, s53, 1\n"
    "s_cbranch_scc1 LDM%=\n"
    "s_sleep 37\n"
    "s_branch LDL%=\n"
    "LDM%=:\n"
    "v_mov_b32 v80, -1\n"
    "v_mov_b32 v81, -1\n"
    "v_mov_b32 v82, -1\n"
    "v_mov_b32 v83, -1\n"
    "s_mov_b32 s54, s46\n"
    "s_mov_b32 s37, s55\n"
    "LF%=:\n"
    "s_lshr_b32 s38, s54, 8\n"
    "s_xor_b32 s38, s38, s43\n"
    "s_and_b32 s38, s38, 0x2000\n"
    "s_or_b32 s50, s54, s38\n"
    "buffer_store_dwordx4 v[80:83], v0, s[16:19], s50 offen offset:0 sc1\n"
    "buffer_store_dwordx4 v[80:83], v0, s[16:19], s50 offen offset:1024 sc1\n"
    "s_waitcnt vmcnt(24)\n"
    "s_add_u32 s54, s54, 262144\n"
    "s_sub_u32 s37, s37, 1\n"
    "s_cbranch_scc0 LF%=\n"
    "s_waitcnt vmcnt(0)\n"
    "s_barrier\n"
    "buffer_store_dword v5, off, s[16:19], s45 sc1\n"
    "s_add_u32 s45, s45, s52\n"
    "v_lshlrev_b32 v7, 4, v0\n"
    "v_and_b32 v7, 0x700, v7\n"
    "v_mov_b32 v9, 139776\n"
    "v_lshl_add_u32 v10, s36, 2, v9\n"
    "s_lshr_b32 s38, s41, 8\n"
    "s_xor_b32 s38, s38, s42\n"
    "s_and_b32 s38, s38, 0x2000\n"
    "s_or_b32 s48, s41, s38\n"
    "LT%=:\n"
    "s_cmp_lg_u32 s36, 0\n"
    "s_cbranch_scc1 LB%=\n"
    "s_mov_b32 s37, 0x80000\n"
    "LP%=:\n"
    "v_mov_b32 v6, 1\n"
    "s_mov_b64 exec, 0xff\n"
    "v_mov_b32 v6, 0\n"
    "buffer_atomic_or v6, v7, s[16:19], s44 offen sc0 sc1\n"
    "s_mov_b64 exec, -1\n"
    "s_waitcnt vmcnt(0)\n"
    "v_cmp_eq_u32 vcc, 0, v6\n"
    "s_cbranch_vccz LB%=\n"
    "s_sleep 1\n"
    "s_sub_u32 s37, s37, 1\n"
    "s_cbranch_scc0 LP%=\n"
    "buffer_store_dword v5, off, s[16:19], s47 sc1\n"
    "s_endpgm\n"
    "LB%=:\n"
    "s_barrier\n"
    "LC%=:\n"
    "s_add_u32 m0, s49, 0\n"
    "s_nop 0\n"
    "buffer_load_dwordx4 v0, s[16:19], s48 offen offset:0 sc1 lds\n"
    "buffer_load_dwordx4 v0, s[16:19], s48 offen offset:1024 sc1 lds\n"
    "s_add_u32 s48, s48, 16384\n"
    "s_add_u32 m0, s49, 8192\n"
    "s_nop 0\n"
    "buffer_load_dwordx4 v0, s[16:19], s48 offen offset:0 sc1 lds\n"
    "buffer_load_dwordx4 v0, s[16:19], s48 offen offset:1024 sc1 lds\n"
    "s_add_u32 s48, s48, 16384\n"
    "s_add_u32 m0, s49, 16384\n"
    "s_nop 0\n"
    "buffer_load_dwordx4 v0, s[16:19], s48 offen offset:0 sc1 lds\n"
    "buffer_load_dwordx4 v0, s[16:19], s48 offen offset:1024 sc1 lds\n"
    "s_add_u32 s48, s48, 16384\n"
    "s_add_u32 m0, s49, 24576\n"
    "s_nop 0\n"
    "buffer_load_dwordx4 v0, s[16:19], s48 offen offset:0 sc1 lds\n"
    "buffer_load_dwordx4 v0, s[16:19], s48 offen offset:1024 sc1 lds\n"
    "s_add_u32 s48, s48, 16384\n"
    "LG%=:\n"
    "s_lshr_b32 s38, s46, 8\n"
    "s_xor_b32 s38, s38, s43\n"
    "s_and_b32 s38, s38, 0x2000\n"
    "s_or_b32 s50, s46, s38\n"
    "s_add_u32 s41, s41, 2621440\n"
    "s_add_u32 s46, s46, 262144\n"
    "s_add_u32 s44, s44, 4\n"
    "s_add_u32 s40, s40, 1\n"
    "s_waitcnt vmcnt(6)\n"
    "s_barrier\n"
    "ds_read_b128 v[20:23], v0 offset:0\n"
    "ds_read_b128 v[24:27], v0 offset:1024\n"
    "ds_read_b128 v[28:31], v0 offset:2048\n"
    "ds_read_b128 v[32:35], v0 offset:3072\n"
    "ds_read_b128 v[36:39], v0 offset:4096\n"
    "ds_read_b128 v[40:43], v0 offset:5120\n"
    "s_waitcnt lgkmcnt(5)\n"
    "v_mfma_f32_32x32x16_bf16 v[64:79], a[0:3], v[20:23], 0\n"
    "s_add_u32 m0, s49, 32768\n"
    "s_nop 0\n"
    "buffer_load_dwordx4 v0, s[16:19], s48 offen offset:0 sc1 lds\n"
    "ds_read_b128 v[44:47], v0 offset:6144\n"
    "s_waitcnt lgkmcnt(5)\n"
    "v_mfma_f32_32x32x16_bf16 v[64:79], a[4:7], v[24:27], v[64:79]\n"
    "ds_read_b128 v[48:51], v0 offset:7168\n"
    "s_waitcnt lgkmcnt(5)\n"
    "v_mfma_f32_32x32x16_bf16 v[64:79], a[8:11], v[28:31], v[64:79]\n"
    "s_waitcnt vmcnt(5)\n"
    "s_barrier\n"
    "ds_read_b128 v[20:23], v0 offset:8192\n"
    "s_waitcnt lgkmcnt(5)\n"
    "v_mfma_f32_32x32x16_bf16 v[64:79], a[12:15], v[32:35], v[64:79]\n"
    "buffer_load_dwordx4 v0, s[16:19], s48 offen offset:1024 sc1 lds\n"
    "s_add_u32 s48, s48, 16384\n"
    "ds_read_b128 v[24:27], v0 offset:9216\n"
    "s_waitcnt lgkmcnt(5)\n"
    "v_mfma_f32_32x32x16_bf16 v[64:79], a[16:19], v[36:39], v[64:79]\n"
    "ds_read_b128 v[28:31], v0 offset:10240\n"
    "s_waitcnt lgkmcnt(5)\n"
    "v_mfma_f32_32x32x16_bf16 v[64:79], a[20:23], v[40:43], v[64:79]\n"
    "ds_read_b128 v[32:35], v0 offset:11264\n"
    "s_waitcnt lgkmcnt(5)\n"
    "v_mfma_f32_32x32x16_bf16 v[64:79], a[24:27], v[44:47], v[64:79]\n"
    "s_add_u32 m0, s49, 40960\n"
    "s_nop 0\n"
    "buffer_load_dwordx4 v0, s[16:19], s48 offen offset:0 sc1 lds\n"
    "ds_read_b128 v[36:39], v0 offset:12288\n"
    "s_waitcnt lgkmcnt(5)\n"
    "v_mfma_f32_32x32x16_bf16 v[64:79], a[28:31], v[48:51], v[64:79]\n"
    "ds_read_b128 v[40:43], v0 offset:13312\n"
    "s_waitcnt lgkmcnt(5)\n"
    "v_mfma_f32_32x32x16_bf16 v[64:79], a[32:35], v[20:23], v[64:79]\n"
    "ds_read_b128 v[44:47], v0 offset:14336\n"
    "s_waitcnt lgkmcnt(5)\n"
    "v_mfma_f32_32x32x16_bf16 v[64:79], a[36:39], v[24:27], v[64:79]\n"
    "buffer_load_dwordx4 v0, s[16:19], s48 offen offset:1024 sc1 lds\n"
    "s_add_u32 s48, s48, 16384\n"
    "ds_read_b128 v[48:51], v0 offset:15360\n"
    "s_waitcnt lgkmcnt(5)\n"
    "v_mfma_f32_32x32x16_bf16 v[64:79], a[40:43], v[28:31], v[64:79]\n"
    "s_waitcnt vmcnt(6)\n"
    "s_barrier\n"
    "ds_read_b128 v[20:23], v0 offset:16384\n"
    "s_waitcnt lgkmcnt(5)\n"
    "v_mfma_f32_32x32x16_bf16 v[64:79], a[44:47], v[32:35], v[64:79]\n"
    "ds_read_b128 v[24:27], v0 offset:17408\n"
    "s_waitcnt lgkmcnt(5)\n"
    "v_mfma_f32_32x32x16_bf16 v[64:79], a[48:51], v[36:39], v[64:79]\n"
    "s_add_u32 m0, s49, 49152\n"
    "s_nop 0\n"
    "buffer_load_dwordx4 v0, s[16:19], s48 offen offset:0 sc1 lds\n"
    "ds_read_b128 v[28:31], v0 offset:18432\n"
    "s_waitcnt lgkmcnt(5)\n"
    "v_mfma_f32_32x32x16_bf16 v[64:79], a[52:55], v[40:43], v[64:79]\n"
    "ds_read_b128 v[32:35], v0 offset:19456\n"
    "s_waitcnt lgkmcnt(5)\n"
    "v_mfma_f32_32x32x16_bf16 v[64:79], a[56:59], v[44:47], v[64:79]\n"
    "ds_read_b128 v[36:39], v0 offset:20480\n"
    "s_waitcnt lgkmcnt(5)\n"
    "v_mfma_f32_32x32x16_bf16 v[64:79], a[60:63], v[48:51], v[64:79]\n"
    "buffer_load_dwordx4 v0, s[16:19], s48 offen offset:1024 sc1 lds\n"
    "s_add_u32 s48, s48, 16384\n"
    "ds_read_b128 v[40:43], v0 offset:21504\n"
    "s_waitcnt lgkmcnt(5)\n"
    "v_mfma_f32_32x32x16_bf16 v[64:79], a[64:67], v[20:23], v[64:79]\n"
    "ds_read_b128 v[44:47], v0 offset:22528\n"
    "s_waitcnt lgkmcnt(5)\n"
    "v_mfma_f32_32x32x16_bf16 v[64:79], a[68:71], v[24:27], v[64:79]\n"
    "ds_read_b128 v[48:51], v0 offset:23552\n"
    "s_waitcnt lgkmcnt(5)\n"
    "v_mfma_f32_32x32x16_bf16 v[64:79], a[72:75], v[28:31], v[64:79]\n"
    "s_add_u32 m0, s49, 57344\n"
    "s_nop 0\n"
    "buffer_load_dwordx4 v0, s[16:19], s48 offen offset:0 sc1 lds\n"
    "s_waitcnt vmcnt(7)\n"
    "s_barrier\n"
    "ds_read_b128 v[20:23], v0 offset:24576\n"
    "s_waitcnt lgkmcnt(5)\n"
    "v_mfma_f32_32x32x16_bf16 v[64:79], a[76:79], v[32:35], v[64:79]\n"
    "ds_read_b128 v[24:27], v0 offset:25600\n"
    "s_waitcnt lgkmcnt(5)\n"
    "v_mfma_f32_32x32x16_bf16 v[64:79], a[80:83], v[36:39], v[64:79]\n"
    "ds_read_b128 v[28:31], v0 offset:26624\n"
    "s_waitcnt lgkmcnt(5)\n"
    "v_mfma_f32_32x32x16_bf16 v[64:79], a[84:87], v[40:43], v[64:79]\n"
    "buffer_load_dwordx4 v0, s[16:19], s48 offen offset:1024 sc1 lds\n"
    "s_add_u32 s48, s48, 16384\n"
    "s_lshr_b32 s38, s41, 8\n"
    "s_xor_b32 s38, s38, s42\n"
    "s_and_b32 s38, s38, 0x2000\n"
    "s_or_b32 s48, s41, s38\n"
    "ds_read_b128 v[32:35], v0 offset:27648\n"
    "s_waitcnt lgkmcnt(5)\n"
    "v_mfma_f32_32x32x16_bf16 v[64:79], a[88:91], v[44:47], v[64:79]\n"
    "ds_read_b128 v[36:39], v0 offset:28672\n"
    "s_waitcnt lgkmcnt(5)\n"
    "v_mfma_f32_32x32x16_bf16 v[64:79], a[92:95], v[48:51], v[64:79]\n"
    "ds_read_b128 v[40:43], v0 offset:29696\n"
    "s_waitcnt lgkmcnt(5)\n"
    "v_mfma_f32_32x32x16_bf16 v[64:79], a[96:99], v[20:23], v[64:79]\n"
    "ds_read_b128 v[44:47], v0 offset:30720\n"
    "s_waitcnt lgkmcnt(5)\n"
    "v_mfma_f32_32x32x16_bf16 v[64:79], a[100:103], v[24:27], v[64:79]\n"
    "ds_read_b128 v[48:51], v0 offset:31744\n"
    "s_waitcnt lgkmcnt(5)\n"
    "v_mfma_f32_32x32x16_bf16 v[64:79], a[104:107], v[28:31], v[64:79]\n"
    "v_mov_b32 v6, 1\n"
    "s_mov_b64 exec, 0xff\n"
    "v_mov_b32 v6, 0\n"
    "buffer_atomic_or v6, v7, s[16:19], s44 offen sc0 sc1\n"
    "s_mov_b64 exec, -1\n"
    "s_waitcnt vmcnt(7)\n"
    "s_barrier\n"
    "ds_read_b128 v[20:23], v0 offset:32768\n"
    "s_waitcnt lgkmcnt(5)\n"
    "v_mfma_f32_32x32x16_bf16 v[64:79], a[108:111], v[32:35], v[64:79]\n"
    "ds_read_b128 v[24:27], v0 offset:33792\n"
    "s_waitcnt lgkmcnt(5)\n"
    "v_mfma_f32_32x32x16_bf16 v[64:79], a[112:115], v[36:39], v[64:79]\n"
    "ds_read_b128 v[28:31], v0 offset:34816\n"
    "s_waitcnt lgkmcnt(5)\n"
    "v_mfma_f32_32x32x16_bf16 v[64:79], a[116:119], v[40:43], v[64:79]\n"
    "ds_read_b128 v[32:35], v0 offset:35840\n"
    "s_waitcnt lgkmcnt(5)\n"
    "v_mfma_f32_32x32x16_bf16 v[64:79], a[120:123], v[44:47], v[64:79]\n"
    "ds_read_b128 v[36:39], v0 offset:36864\n"
    "s_waitcnt lgkmcnt(5)\n"
    "v_mfma_f32_32x32x16_bf16 v[64:79], a[124:127], v[48:51], v[64:79]\n"
    "ds_read_b128 v[40:43], v0 offset:37888\n"
    "s_waitcnt lgkmcnt(5)\n"
    "v_mfma_f32_32x32x16_bf16 v[64:79], a[128:131], v[20:23], v[64:79]\n"
    "ds_read_b128 v[44:47], v0 offset:38912\n"
    "s_waitcnt lgkmcnt(5)\n"
    "v_mfma_f32_32x32x16_bf16 v[64:79], a[132:135], v[24:27], v[64:79]\n"
    "ds_read_b128 v[48:51], v0 offset:39936\n"
    "s_waitcnt lgkmcnt(5)\n"
    "v_mfma_f32_32x32x16_bf16 v[64:79], a[136:139], v[28:31], v[64:79]\n"
    "s_waitcnt vmcnt(5)\n"
    "s_barrier\n"
    "ds_read_b128 v[20:23], v0 offset:40960\n"
    "s_waitcnt lgkmcnt(5)\n"
    "v_mfma_f32_32x32x16_bf16 v[64:79], a[140:143], v[32:35], v[64:79]\n"
    "ds_read_b128 v[24:27], v0 offset:41984\n"
    "s_waitcnt lgkmcnt(5)\n"
    "v_mfma_f32_32x32x16_bf16 v[64:79], a[144:147], v[36:39], v[64:79]\n"
    "ds_read_b128 v[28:31], v0 offset:43008\n"
    "s_waitcnt lgkmcnt(5)\n"
    "v_mfma_f32_32x32x16_bf16 v[64:79], a[148:151], v[40:43], v[64:79]\n"
    "ds_read_b128 v[32:35], v0 offset:44032\n"
    "s_waitcnt lgkmcnt(5)\n"
    "v_mfma_f32_32x32x16_bf16 v[64:79], a[152:155], v[44:47], v[64:79]\n"
    "ds_read_b128 v[36:39], v0 offset:45056\n"
    "s_waitcnt lgkmcnt(5)\n"
    "v_mfma_f32_32x32x16_bf16 v[64:79], a[156:159], v[48:51], v[64:79]\n"
    "ds_read_b128 v[40:43], v0 offset:46080\n"
    "s_waitcnt lgkmcnt(5)\n"
    "v_mfma_f32_32x32x16_bf16 v[64:79], a[160:163], v[20:23], v[64:79]\n"
    "ds_read_b128 v[44:47], v0 offset:47104\n"
    "s_waitcnt lgkmcnt(5)\n"
    "v_mfma_f32_32x32x16_bf16 v[64:79], a[164:167], v[24:27], v[64:79]\n"
    "ds_read_b128 v[48:51], v0 offset:48128\n"
    "s_waitcnt lgkmcnt(5)\n"
    "v_mfma_f32_32x32x16_bf16 v[64:79], a[168:171], v[28:31], v[64:79]\n"
    "s_waitcnt vmcnt(3)\n"
    "s_barrier\n"
    "ds_read_b128 v[20:23], v0 offset:49152\n"
    "s_waitcnt lgkmcnt(5)\n"
    "v_mfma_f32_32x32x16_bf16 v[64:79], a[172:175], v[32:35], v[64:79]\n"
    "ds_read_b128 v[24:27], v0 offset:50176\n"
    "s_waitcnt lgkmcnt(5)\n"
    "v_mfma_f32_32x32x16_bf16 v[64:79], a[176:179], v[36:39], v[64:79]\n"
    "ds_read_b128 v[28:31], v0 offset:51200\n"
    "s_waitcnt lgkmcnt(5)\n"
    "v_mfma_f32_32x32x16_bf16 v[64:79], a[180:183], v[40:43], v[64:79]\n"
    "s_waitcnt vmcnt(0)\n"
    "v_cmp_eq_u32 vcc, 0, v6\n"
    "s_cmp_eq_u32 s40, 64\n"
    "s_cselect_b32 s38, 1, 0\n"
    "s_nop 1\n"
    "s_or_b32 s38, s38, vcc_lo\n"
    "v_mov_b32 v6, s38\n"
    "ds_write_b32 v10, v6\n"
    "ds_read_b128 v[32:35], v0 offset:52224\n"
    "s_waitcnt lgkmcnt(6)\n"
    "v_mfma_f32_32x32x16_bf16 v[64:79], a[184:187], v[44:47], v[64:79]\n"
    "ds_read_b128 v[36:39], v0 offset:53248\n"
    "s_waitcnt lgkmcnt(6)\n"
    "v_mfma_f32_32x32x16_bf16 v[64:79], a[188:191], v[48:51], v[64:79]\n"
    "ds_read_b128 v[40:43], v0 offset:54272\n"
    "s_waitcnt lgkmcnt(6)\n"
    "v_mfma_f32_32x32x16_bf16 v[64:79], a[192:195], v[20:23], v[64:79]\n"
    "ds_read_b128 v[44:47], v0 offset:55296\n"
    "s_waitcnt lgkmcnt(6)\n"
    "v_mfma_f32_32x32x16_bf16 v[64:79], a[196:199], v[24:27], v[64:79]\n"
    "ds_read_b128 v[48:51], v0 offset:56320\n"
    "s_waitcnt lgkmcnt(6)\n"
    "v_mfma_f32_32x32x16_bf16 v[64:79], a[200:203], v[28:31], v[64:79]\n"
    "s_add_u32 s55, s55, 1\n"
    "s_lshr_b32 s38, s54, 8\n"
    "s_xor_b32 s38, s38, s43\n"
    "s_and_b32 s38, s38, 0x2000\n"
    "s_or_b32 s39, s54, s38\n"
    "s_cmp_lt_u32 s55, 64\n"
    "s_cselect_b32 s39, s39, 0x80000000\n"
    "s_add_u32 s54, s54, 262144\n"
    "s_barrier\n"
    "ds_read_b128 v[20:23], v0 offset:57344\n"
    "ds_read_b32 v6, v9\n"
    "s_waitcnt lgkmcnt(6)\n"
    "v_mfma_f32_32x32x16_bf16 v[64:79], a[204:207], v[32:35], v[64:79]\n"
    "buffer_store_dwordx4 v[80:83], v0, s[16:19], s39 offen offset:0 sc1\n"
    "ds_read_b128 v[24:27], v0 offset:58368\n"
    "s_waitcnt lgkmcnt(6)\n"
    "v_mfma_f32_32x32x16_bf16 v[64:79], a[208:211], v[36:39], v[64:79]\n"
    "ds_read_b128 v[28:31], v0 offset:59392\n"
    "s_waitcnt lgkmcnt(6)\n"
    "v_mfma_f32_32x32x16_bf16 v[64:79], a[212:215], v[40:43], v[64:79]\n"
    "buffer_store_dwordx4 v[80:83], v0, s[16:19], s39 offen offset:1024 sc1\n"
    "ds_read_b128 v[32:35], v0 offset:60416\n"
    "s_waitcnt lgkmcnt(6)\n"
    "v_mfma_f32_32x32x16_bf16 v[64:79], a[216:219], v[44:47], v[64:79]\n"
    "ds_read_b128 v[36:39], v0 offset:61440\n"
    "s_waitcnt lgkmcnt(6)\n"
    "v_mfma_f32_32x32x16_bf16 v[64:79], a[220:223], v[48:51], v[64:79]\n"
    "ds_read_b128 v[40:43], v0 offset:62464\n"
    "s_waitcnt lgkmcnt(5)\n"
    "v_readfirstlane_b32 s52, v6\n"
    "s_cmp_eq_u32 s52, 0\n"
    "s_cbranch_scc1 LH%=\n"
    "v_mfma_f32_32x32x16_bf16 v[64:79], a[224:227], v[20:23], v[64:79]\n"
    "ds_read_b128 v[44:47], v0 offset:63488\n"
    "s_waitcnt lgkmcnt(5)\n"
    "v_mfma_f32_32x32x16_bf16 v[64:79], a[228:231], v[24:27], v[64:79]\n"
    "ds_read_b128 v[48:51], v0 offset:64512\n"
    "s_waitcnt lgkmcnt(5)\n"
    "v_mfma_f32_32x32x16_bf16 v[64:79], a[232:235], v[28:31], v[64:79]\n"
    "s_waitcnt lgkmcnt(4)\n"
    "v_mfma_f32_32x32x16_bf16 v[64:79], a[236:239], v[32:35], v[64:79]\n"
    "s_waitcnt lgkmcnt(3)\n"
    "v_mfma_f32_32x32x16_bf16 v[64:79], a[240:243], v[36:39], v[64:79]\n"
    "s_waitcnt lgkmcnt(2)\n"
    "v_mfma_f32_32x32x16_bf16 v[64:79], a[244:247], v[40:43], v[64:79]\n"
    "s_waitcnt lgkmcnt(1)\n"
    "v_mfma_f32_32x32x16_bf16 v[64:79], a[248:251], v[44:47], v[64:79]\n"
    "s_waitcnt lgkmcnt(0)\n"
    "v_mfma_f32_32x32x16_bf16 v[64:79], a[252:255], v[48:51], v[64:79]\n"
    "s_branch LI%=\n"
    "LH%=:\n"
    "v_mfma_f32_32x32x16_bf16 v[64:79], a[224:227], v[20:23], v[64:79]\n"
    "ds_read_b128 v[44:47], v0 offset:63488\n"
    "s_add_u32 m0, s49, 0\n"
    "s_nop 0\n"
    "buffer_load_dwordx4 v0, s[16:19], s48 offen offset:0 sc1 lds\n"
    "s_waitcnt lgkmcnt(5)\n"
    "v_mfma_f32_32x32x16_bf16 v[64:79], a[228:231], v[24:27], v[64:79]\n"
    "ds_read_b128 v[48:51], v0 offset:64512\n"
    "buffer_load_dwordx4 v0, s[16:19], s48 offen offset:1024 sc1 lds\n"
    "s_add_u32 s48, s48, 16384\n"
    "s_waitcnt lgkmcnt(5)\n"
    "v_mfma_f32_32x32x16_bf16 v[64:79], a[232:235], v[28:31], v[64:79]\n"
    "s_add_u32 m0, s49, 8192\n"
    "s_nop 0\n"
    "buffer_load_dwordx4 v0, s[16:19], s48 offen offset:0 sc1 lds\n"
    "s_waitcnt lgkmcnt(4)\n"
    "v_mfma_f32_32x32x16_bf16 v[64:79], a[236:239], v[32:35], v[64:79]\n"
    "buffer_load_dwordx4 v0, s[16:19], s48 offen offset:1024 sc1 lds\n"
    "s_add_u32 s48, s48, 16384\n"
    "s_waitcnt lgkmcnt(3)\n"
    "v_mfma_f32_32x32x16_bf16 v[64:79], a[240:243], v[36:39], v[64:79]\n"
    "s_add_u32 m0, s49, 16384\n"
    "s_nop 0\n"
    "buffer_load_dwordx4 v0, s[16:19], s48 offen offset:0 sc1 lds\n"
    "s_waitcnt lgkmcnt(2)\n"
    "v_mfma_f32_32x32x16_bf16 v[64:79], a[244:247], v[40:43], v[64:79]\n"
    "buffer_load_dwordx4 v0, s[16:19], s48 offen offset:1024 sc1 lds\n"
    "s_add_u32 s48, s48, 16384\n"
    "s_waitcnt lgkmcnt(1)\n"
    "v_mfma_f32_32x32x16_bf16 v[64:79], a[248:251], v[44:47], v[64:79]\n"
    "s_add_u32 m0, s49, 24576\n"
    "s_nop 0\n"
    "buffer_load_dwordx4 v0, s[16:19], s48 offen offset:0 sc1 lds\n"
    "s_waitcnt lgkmcnt(0)\n"
    "v_mfma_f32_32x32x16_bf16 v[64:79], a[252:255], v[48:51], v[64:79]\n"
    "buffer_load_dwordx4 v0, s[16:19], s48 offen offset:1024 sc1 lds\n"
    "s_add_u32 s48, s48, 16384\n"
    "LI%=:\n"
    "s_nop 7\n"
    "s_nop 3\n"
    "v_cvt_pk_bf16_f32 v12, v64, v65\n"
    "v_cvt_pk_bf16_f32 v13, v66, v67\n"
    "v_cvt_pk_bf16_f32 v14, v68, v69\n"
    "v_cvt_pk_bf16_f32 v15, v70, v71\n"
    "v_min_u32 v15, -2, v15\n"
    "ds_write_b128 v1, v[12:15] offset:0\n"
    "v_cvt_pk_bf16_f32 v16, v72, v73\n"
    "v_cvt_pk_bf16_f32 v17, v74, v75\n"
    "v_cvt_pk_bf16_f32 v18, v76, v77\n"
    "v_cvt_pk_bf16_f32 v19, v78, v79\n"
    "v_min_u32 v19, -2, v19\n"
    "ds_write_b128 v1, v[16:19] offset:32\n"
    "v_mov_b32 v6, 1\n"
    "s_mov_b64 exec, 0xff\n"
    "v_mov_b32 v6, 0\n"
    "buffer_atomic_or v6, v7, s[16:19], s44 offen sc0 sc1\n"
    "s_mov_b64 exec, -1\n"
    "s_waitcnt lgkmcnt(0)\n"
    "s_barrier\n"
    "ds_read_b128 v[12:15], v2 offset:0\n"
    "ds_read_b128 v[16:19], v2 offset:1088\n"
    "s_waitcnt lgkmcnt(1)\n"
    "buffer_store_dwordx4 v[12:15], v0, s[16:19], s50 offen offset:0 sc1\n"
    "s_waitcnt lgkmcnt(0)\n"
    "buffer_store_dwordx4 v[16:19], v0, s[16:19], s50 offen offset:1024 sc1\n"
    "buffer_store_dword v5, off, s[16:19], s45 sc1\n"
    "s_add_u32 s45, s45, 4\n"
    "s_waitcnt vmcnt(3)\n"
    "s_cmp_eq_u32 s52, 0\n"
    "s_cbranch_scc1 LG%=\n"
    "s_cmp_eq_u32 s40, 64\n"
    "s_cbranch_scc1 LE%=\n"
    "s_cmp_lg_u32 s36, 0\n"
    "s_cbranch_scc1 LB%=\n"
    "v_cmp_eq_u32 vcc, 0, v6\n"
    "s_cbranch_vccz LB%=\n"
    "s_branch LT%=\n"
    "LE%=:\n"
    "s_endpgm\n"
    :
    : "{v0}"(hv0), "{v1}"(hv1), "{v2}"(hv2), "{v3}"(hv3), "{v4}"(hv4), "{v5}"(hv5)
    : "v255", "a255", "s101", "vcc", "scc", "memory");
  __builtin_unreachable();
}

typedef float f2 __attribute__((ext_vector_type(2)));
__device__ __forceinline__ unsigned cvt2(f2 v) { unsigned p; asm("v_cvt_pk_bf16_f32 %0, %1, %2" : "=v"(p) : "v"(v[0]), "v"(v[1])); return p; }
__device__ __forceinline__ f2 up2(unsigned p) { return (f2){__uint_as_float(p << 16), __uint_as_float(p & 0xffff0000u)}; }
__device__ __forceinline__ void row_sum2(float& x, float& y) {
  x = ror_add<8>(x); y = ror_add<8>(y); x = ror_add<4>(x); y = ror_add<4>(y); x = ror_add<2>(x); y = ror_add<2>(y); x = ror_add<1>(x); y = ror_add<1>(y);
  auto q = __builtin_amdgcn_permlane16_swap(__float_as_uint(x), __float_as_uint(y), false, false);
  float t = __uint_as_float(q[0]) + __uint_as_float(q[1]);
  auto s = __builtin_amdgcn_permlane32_swap(__float_as_uint(t), __float_as_uint(t), false, false);
  t = __uint_as_float(s[0]) + __uint_as_float(s[1]);
  x = __int_as_float(__builtin_amdgcn_readlane(__float_as_int(t), 0)); y = __int_as_float(__builtin_amdgcn_readlane(__float_as_int(t), 16));
}
__device__ __forceinline__ void ln8(const f2 (&x)[4], f2 (&y)[4]) {
  const float c0 = __int_as_float(__builtin_amdgcn_readfirstlane(__float_as_int(x[0][0])));
  const f2 d0 = x[0] - c0, d1 = x[1] - c0, d2 = x[2] - c0, d3 = x[3] - c0;
  const f2 s2 = (d0 + d1) + (d2 + d3), v2 = d3 * d3 + (d2 * d2 + (d1 * d1 + d0 * d0));
  float s = s2[0] + s2[1], vs = v2[0] + v2[1];
  row_sum2(s, vs);
  s *= 1.f / DM;
  const f2 mv = up2(cvt2((f2){c0 + s, fmaxf(__builtin_fmaf(-s, s, vs * (1.f / DM)), 0.f)}));
  const float rc = __builtin_amdgcn_rcpf(rbf(__builtin_amdgcn_sqrtf(rbf(mv[1] + 1e-5f))));
#pragma unroll
  for (int i = 0; i < 4; ++i) y[i] = (x[i] - mv[0]) * rc;
}
__device__ __forceinline__ v4u ln_row(v4u q) {
  f2 x[4], y[4];
#pragma unroll
  for (int i = 0; i < 4; ++i) x[i] = up2(q[i]);
  ln8(x, y);
  return (v4u){cvt2(y[0]), cvt2(y[1]), cvt2(y[2]), cvt2(y[3])};
}
__device__ __forceinline__ void dma16(rsrc_t r, char* lp, unsigned vo, unsigned so) { __builtin_amdgcn_raw_ptr_buffer_load_lds(r, (__attribute__((address_space(3))) void*)lp, 16, vo, so, 0, 0); }
#define PTR(p) (int)(size_t)(p), (int)((size_t)(p) >> 32) & 0xffff
template <bool L1>
__device__ __forceinline__ void l_role8(int w, int u, const unsigned* rdy, char* ws, const u16* x, const u16* w_r, const u16* wg, const u16* wu, const u16* wd, u16* out) {
  const int lane = lane_id(), c = lane & 15, g = lane >> 4, q = (lane >> 2) & 3, r = u * 4 + w, t0 = L1 ? (w < 2 ? 3 * w : 2 * w + 2) : w;
  const unsigned c1 = 0u, c2 = 0x2000u;
  const unsigned zd1 = nearof(Z_DEC, c1), zd2 = nearof(Z_DEC, c2), cl = L1 && SW ? c1 : c2, fp = nearof(Z_FP + (L1 ? 0 : 16384 + (g & 1) * 16384), cl), zdl = SW ? zd1 + 1024 : zd2 + 512;
  const int pv[22] = {w, r, (int)nearof(Z_R1, cl), (int)Z_ERR, PTR(ws), (int)ZWS_BYTES, PTR(x), PTR(out), PTR(wg), PTR(wu), PTR(wd), PTR(rdy), PTR(w_r), (int)(Z_DBG + MBY(3)) + ((L1 ? 0 : 32) + r) * 16};
  const int one = (c >> 2) != g ? 0 : ((c & 1) ? 0x3f800000 : 0x3f80), trow = 16 * (g >> 1) + 8 * (q >> 1) + 4 * (g & 1) + (q & 1);
  int hv0 = lane * 16, hv1 = g * 16384 + r * 256 + c * 16, hv2 = lane < 32 ? fp + 4096 + (L1 ? g * 4 : 0) : (L1 ? zdl : zd2), hv3 = g < (L1 ? 2 : 1) ? 4 : 0, hv4 = g == 1 ? 4 : 0, hv5 = g >= 2 ? 4 : 0;
  int hv6 = (c & 2) ? 0xEEEE : 0x4444, hv7 = w * 4096 + g * 32 + (c & 2) * 8, hv8 = w * 4096 + lane * 16;
  int hv9 = !(c & 1) && g < (L1 ? (w < 2 ? 3 : 2) : 1) ? (L1 ? u * 320 : 2560 + u * 128) + (t0 + g) * 32 + c * 2 : OOR, hv10 = (lane & 0x31) == 0 ? r * 32 + c * 2 : OOR;
  int hv11 = lane < 6 ? (lane < 3 ? zd1 + lane * 512 : (lane == 3 ? zd2 : zdl + (lane - 4) * 512)) : OOR, hv12 = (c & 2) ? 0 : one, hv13 = (c & 2) ? one : 0;
  int hv14 = trow * (L1 ? 320 : 128) + t0 * 32 + (lane & 3) * 8, hv15 = 0, hv21 = fp + c * 256;
#define VO(i) ((w * 16 + (i * 64 + lane) / 20) * (FF * 2) + u * 320 + ((i * 64 + lane) % 20) * 16)
  int hv16 = L1 ? VO(0) : (w * 16 + (lane >> 3)) * (FF * 2) + 2560 + u * 128 + (lane & 7) * 16, hv17 = L1 ? VO(1) : (w * 16 + (lane >> 3)) * (DM * 2) + u * 128 + (lane & 7) * 16;
  int hv18 = L1 ? VO(2) : r * 32 + c * 2, hv19 = VO(3), hv20 = VO(4);
#pragma unroll
  for (int i = 0; i < 22; ++i) hv15 = lane == i ? pv[i] : hv15;
  if constexpr (L1) {
  asm volatile(
    "s_nop 1\n"
    "v_readlane_b32 s36, v15, 0\n"
    "v_readlane_b32 s55, v15, 1\n"
    "v_readlane_b32 s58, v15, 2\n"
    "v_readlane_b32 s59, v15, 3\n"
    "v_readlane_b32 s16, v15, 4\n"
    "v_readlane_b32 s17, v15, 5\n"
    "v_readlane_b32 s18, v15, 6\n"
    "v_readlane_b32 s20, v15, 7\n"
    "v_readlane_b32 s21, v15, 8\n"
    "v_readlane_b32 s24, v15, 9\n"
    "v_readlane_b32 s25, v15, 10\n"
    "v_readlane_b32 s90, v15, 11\n"
    "v_readlane_b32 s91, v15, 12\n"
    "v_readlane_b32 s92, v15, 13\n"
    "v_readlane_b32 s93, v15, 14\n"
    "v_readlane_b32 s94, v15, 15\n"
    "v_readlane_b32 s95, v15, 16\n"
    "v_readlane_b32 s96, v15, 17\n"
    "v_readlane_b32 s97, v15, 18\n"
    "v_readlane_b32 s98, v15, 19\n"
    "v_readlane_b32 s99, v15, 20\n"
    "v_readlane_b32 s72, v15, 21\n"
    "s_mov_b32 s19, 0x20000\n"
    "s_mov_b32 s23, 0x20000\n"
    "s_mov_b32 s27, 0x20000\n"
    "s_mov_b32 s31, 0x20000\n"
    "s_mov_b32 s22, 0x200000\n"
    "s_mov_b32 s26, 0x200000\n"
    "s_mov_b32 s30, 1835008\n"
    "s_nop 3\n"
    "global_load_dwordx4 v[116:119], v0, s[98:99]\n"
    "s_mul_i32 s62, s36, 5120\n"
    "s_mov_b32 s28, s90\n"
    "s_mov_b32 s29, s91\n"
    "s_add_u32 m0, s62, 0\n"
    "s_mov_b32 s38, 0\n"
    "buffer_load_dwordx4 v16, s[28:31], s38 offen lds\n"
    "s_add_u32 m0, s62, 1024\n"
    "s_mov_b32 s38, 0\n"
    "buffer_load_dwordx4 v17, s[28:31], s38 offen lds\n"
    "s_add_u32 m0, s62, 2048\n"
    "s_mov_b32 s38, 0\n"
    "buffer_load_dwordx4 v18, s[28:31], s38 offen lds\n"
    "s_add_u32 m0, s62, 3072\n"
    "s_mov_b32 s38, 0\n"
    "buffer_load_dwordx4 v19, s[28:31], s38 offen lds\n"
    "s_add_u32 m0, s62, 4096\n"
    "s_mov_b32 s38, 0\n"
    "buffer_load_dwordx4 v20, s[28:31], s38 offen lds\n"
    "s_mov_b32 s28, s92\n"
    "s_mov_b32 s29, s93\n"
    "s_add_u32 m0, s62, 20480\n"
    "s_mov_b32 s38, 0\n"
    "buffer_load_dwordx4 v16, s[28:31], s38 offen lds\n"
    "s_add_u32 m0, s62, 21504\n"
    "s_mov_b32 s38, 0\n"
    "buffer_load_dwordx4 v17, s[28:31], s38 offen lds\n"
    "s_add_u32 m0, s62, 22528\n"
    "s_mov_b32 s38, 0\n"
    "buffer_load_dwordx4 v18, s[28:31], s38 offen lds\n"
    "s_add_u32 m0, s62, 23552\n"
    "s_mov_b32 s38, 0\n"
    "buffer_load_dwordx4 v19, s[28:31], s38 offen lds\n"
    "s_add_u32 m0, s62, 24576\n"
    "s_mov_b32 s38, 0\n"
    "buffer_load_dwordx4 v20, s[28:31], s38 offen lds\n"
    "s_mov_b32 s28, s90\n"
    "s_mov_b32 s29, s91\n"
    "s_add_u32 m0, s62, 40960\n"
    "s_mov_b32 s38, 229376\n"
    "buffer_load_dwordx4 v16, s[28:31], s38 offen lds\n"
    "s_add_u32 m0, s62, 41984\n"
    "s_mov_b32 s38, 229376\n"
    "buffer_load_dwordx4 v17, s[28:31], s38 offen lds\n"
    "s_add_u32 m0, s62, 43008\n"
    "s_mov_b32 s38, 229376\n"
    "buffer_load_dwordx4 v18, s[28:31], s38 offen lds\n"
    "s_add_u32 m0, s62, 44032\n"
    "s_mov_b32 s38, 229376\n"
    "buffer_load_dwordx4 v19, s[28:31], s38 offen lds\n"
    "s_add_u32 m0, s62, 45056\n"
    "s_mov_b32 s38, 229376\n"
    "buffer_load_dwordx4 v20, s[28:31], s38 offen lds\n"
    "s_mov_b32 s28, s92\n"
    "s_mov_b32 s29, s93\n"
    "s_add_u32 m0, s62, 61440\n"
    "s_mov_b32 s38, 229376\n"
    "buffer_load_dwordx4 v16, s[28:31], s38 offen lds\n"
    "s_add_u32 m0, s62, 62464\n"
    "s_mov_b32 s38, 229376\n"
    "buffer_load_dwordx4 v17, s[28:31], s38 offen lds\n"
    "s_add_u32 m0, s62, 63488\n"
    "s_mov_b32 s38, 229376\n"
    "buffer_load_dwordx4 v18, s[28:31], s38 offen lds\n"
    "s_add_u32 m0, s62, 64512\n"
    "s_mov_b32 s38, 229376\n"
    "buffer_load_dwordx4 v19, s[28:31], s38 offen lds\n"
    "s_add_u32 m0, s62, 65536\n"
    "s_mov_b32 s38, 229376\n"
    "buffer_load_dwordx4 v20, s[28:31], s38 offen lds\n"
    "s_mov_b32 s28, s90\n"
    "s_mov_b32 s29, s91\n"
    "s_add_u32 m0, s62, 81920\n"
    "s_mov_b32 s38, 458752\n"
    "buffer_load_dwordx4 v16, s[28:31], s38 offen lds\n"
    "s_add_u32 m0, s62, 82944\n"
    "s_mov_b32 s38, 458752\n"
    "buffer_load_dwordx4 v17, s[28:31], s38 offen lds\n"
    "s_add_u32 m0, s62, 83968\n"
    "s_mov_b32 s38, 458752\n"
    "buffer_load_dwordx4 v18, s[28:31], s38 offen lds\n"
    "s_add_u32 m0, s62, 84992\n"
    "s_mov_b32 s38, 458752\n"
    "buffer_load_dwordx4 v19, s[28:31], s38 offen lds\n"
    "s_add_u32 m0, s62, 86016\n"
    "s_mov_b32 s38, 458752\n"
    "buffer_load_dwordx4 v20, s[28:31], s38 offen lds\n"
    "s_mov_b32 s28, s92\n"
    "s_mov_b32 s29, s93\n"
    "s_add_u32 m0, s62, 102400\n"
    "s_mov_b32 s38, 458752\n"
    "buffer_load_dwordx4 v16, s[28:31], s38 offen lds\n"
    "s_add_u32 m0, s62, 103424\n"
    "s_mov_b32 s38, 458752\n"
    "buffer_load_dwordx4 v17, s[28:31], s38 offen lds\n"
    "s_add_u32 m0, s62, 104448\n"
    "s_mov_b32 s38, 458752\n"
    "buffer_load_dwordx4 v18, s[28:31], s38 offen lds\n"
    "s_add_u32 m0, s62, 105472\n"
    "s_mov_b32 s38, 458752\n"
    "buffer_load_dwordx4 v19, s[28:31], s38 offen lds\n"
    "s_add_u32 m0, s62, 106496\n"
    "s_mov_b32 s38, 458752\n"
    "buffer_load_dwordx4 v20, s[28:31], s38 offen lds\n"
    "s_waitcnt vmcnt(25)\n"
    "s_barrier\n"
    "s_mov_b32 s28, s90\n"
    "s_mov_b32 s29, s91\n"
    "s_add_u32 m0, s62, 122880\n"
    "s_mov_b32 s38, 688128\n"
    "buffer_load_dwordx4 v16, s[28:31], s38 offen lds\n"
    "s_add_u32 m0, s62, 123904\n"
    "s_mov_b32 s38, 688128\n"
    "buffer_load_dwordx4 v17, s[28:31], s38 offen lds\n"
    "s_add_u32 m0, s62, 124928\n"
    "s_mov_b32 s38, 688128\n"
    "buffer_load_dwordx4 v18, s[28:31], s38 offen lds\n"
    "s_add_u32 m0, s62, 125952\n"
    "s_mov_b32 s38, 688128\n"
    "buffer_load_dwordx4 v19, s[28:31], s38 offen lds\n"
    "s_add_u32 m0, s62, 126976\n"
    "s_mov_b32 s38, 688128\n"
    "buffer_load_dwordx4 v20, s[28:31], s38 offen lds\n"
    "v_add_u32 v48, 0, v14\n"
    "ds_read_b64_tr_b16 a[0:1], v48 offset:0\n"
    "ds_read_b64_tr_b16 a[2:3], v48 offset:640\n"
    "ds_read_b64_tr_b16 a[4:5], v48 offset:10240\n"
    "ds_read_b64_tr_b16 a[6:7], v48 offset:10880\n"
    "ds_read_b64_tr_b16 a[16:17], v48 offset:32\n"
    "ds_read_b64_tr_b16 a[18:19], v48 offset:672\n"
    "ds_read_b64_tr_b16 a[20:21], v48 offset:10272\n"
    "ds_read_b64_tr_b16 a[22:23], v48 offset:10912\n"
    "ds_read_b64_tr_b16 v[128:129], v48 offset:64\n"
    "ds_read_b64_tr_b16 v[130:131], v48 offset:704\n"
    "ds_read_b64_tr_b16 v[132:133], v48 offset:10304\n"
    "ds_read_b64_tr_b16 v[134:135], v48 offset:10944\n"
    "s_waitcnt lgkmcnt(0)\n"
    "s_waitcnt vmcnt(25)\n"
    "s_barrier\n"
    "s_mov_b32 s28, s92\n"
    "s_mov_b32 s29, s93\n"
    "s_add_u32 m0, s62, 0\n"
    "s_mov_b32 s38, 688128\n"
    "buffer_load_dwordx4 v16, s[28:31], s38 offen lds\n"
    "s_add_u32 m0, s62, 1024\n"
    "s_mov_b32 s38, 688128\n"
    "buffer_load_dwordx4 v17, s[28:31], s38 offen lds\n"
    "s_add_u32 m0, s62, 2048\n"
    "s_mov_b32 s38, 688128\n"
    "buffer_load_dwordx4 v18, s[28:31], s38 offen lds\n"
    "s_add_u32 m0, s62, 3072\n"
    "s_mov_b32 s38, 688128\n"
    "buffer_load_dwordx4 v19, s[28:31], s38 offen lds\n"
    "s_add_u32 m0, s62, 4096\n"
    "s_mov_b32 s38, 688128\n"
    "buffer_load_dwordx4 v20, s[28:31], s38 offen lds\n"
    "v_add_u32 v48, 20480, v14\n"
    "ds_read_b64_tr_b16 a[8:9], v48 offset:0\n"
    "ds_read_b64_tr_b16 a[10:11], v48 offset:640\n"
    "ds_read_b64_tr_b16 a[12:13], v48 offset:10240\n"
    "ds_read_b64_tr_b16 a[14:15], v48 offset:10880\n"
    "ds_read_b64_tr_b16 a[24:25], v48 offset:32\n"
    "ds_read_b64_tr_b16 a[26:27], v48 offset:672\n"
    "ds_read_b64_tr_b16 a[28:29], v48 offset:10272\n"
    "ds_read_b64_tr_b16 a[30:31], v48 offset:10912\n"
    "ds_read_b64_tr_b16 v[136:137], v48 offset:64\n"
    "ds_read_b64_tr_b16 v[138:139], v48 offset:704\n"
    "ds_read_b64_tr_b16 v[140:141], v48 offset:10304\n"
    "ds_read_b64_tr_b16 v[142:143], v48 offset:10944\n"
    "s_waitcnt lgkmcnt(0)\n"
    "s_waitcnt vmcnt(25)\n"
    "s_barrier\n"
    "s_mov_b32 s28, s90\n"
    "s_mov_b32 s29, s91\n"
    "s_add_u32 m0, s62, 20480\n"
    "s_mov_b32 s38, 917504\n"
    "buffer_load_dwordx4 v16, s[28:31], s38 offen lds\n"
    "s_add_u32 m0, s62, 21504\n"
    "s_mov_b32 s38, 917504\n"
    "buffer_load_dwordx4 v17, s[28:31], s38 offen lds\n"
    "s_add_u32 m0, s62, 22528\n"
    "s_mov_b32 s38, 917504\n"
    "buffer_load_dwordx4 v18, s[28:31], s38 offen lds\n"
    "s_add_u32 m0, s62, 23552\n"
    "s_mov_b32 s38, 917504\n"
    "buffer_load_dwordx4 v19, s[28:31], s38 offen lds\n"
    "s_add_u32 m0, s62, 24576\n"
    "s_mov_b32 s38, 917504\n"
    "buffer_load_dwordx4 v20, s[28:31], s38 offen lds\n"
    "v_add_u32 v48, 40960, v14\n"
    "ds_read_b64_tr_b16 a[32:33], v48 offset:0\n"
    "ds_read_b64_tr_b16 a[34:35], v48 offset:640\n"
    "ds_read_b64_tr_b16 a[36:37], v48 offset:10240\n"
    "ds_read_b64_tr_b16 a[38:39], v48 offset:10880\n"
    "ds_read_b64_tr_b16 a[48:49], v48 offset:32\n"
    "ds_read_b64_tr_b16 a[50:51], v48 offset:672\n"
    "ds_read_b64_tr_b16 a[52:53], v48 offset:10272\n"
    "ds_read_b64_tr_b16 a[54:55], v48 offset:10912\n"
    "ds_read_b64_tr_b16 v[144:145], v48 offset:64\n"
    "ds_read_b64_tr_b16 v[146:147], v48 offset:704\n"
    "ds_read_b64_tr_b16 v[148:149], v48 offset:10304\n"
    "ds_read_b64_tr_b16 v[150:151], v48 offset:10944\n"
    "s_waitcnt lgkmcnt(0)\n"
    "s_waitcnt vmcnt(25)\n"
    "s_barrier\n"
    "s_mov_b32 s28, s92\n"
    "s_mov_b32 s29, s93\n"
    "s_add_u32 m0, s62, 40960\n"
    "s_mov_b32 s38, 917504\n"
    "buffer_load_dwordx4 v16, s[28:31], s38 offen lds\n"
    "s_add_u32 m0, s62, 41984\n"
    "s_mov_b32 s38, 917504\n"
    "buffer_load_dwordx4 v17, s[28:31], s38 offen lds\n"
    "s_add_u32 m0, s62, 43008\n"
    "s_mov_b32 s38, 917504\n"
    "buffer_load_dwordx4 v18, s[28:31], s38 offen lds\n"
    "s_add_u32 m0, s62, 44032\n"
    "s_mov_b32 s38, 917504\n"
    "buffer_load_dwordx4 v19, s[28:31], s38 offen lds\n"
    "s_add_u32 m0, s62, 45056\n"
    "s_mov_b32 s38, 917504\n"
    "buffer_load_dwordx4 v20, s[28:31], s38 offen lds\n"
    "v_add_u32 v48, 61440, v14\n"
    "ds_read_b64_tr_b16 a[40:41], v48 offset:0\n"
    "ds_read_b64_tr_b16 a[42:43], v48 offset:640\n"
    "ds_read_b64_tr_b16 a[44:45], v48 offset:10240\n"
    "ds_read_b64_tr_b16 a[46:47], v48 offset:10880\n"
    "ds_read_b64_tr_b16 a[56:57], v48 offset:32\n"
    "ds_read_b64_tr_b16 a[58:59], v48 offset:672\n"
    "ds_read_b64_tr_b16 a[60:61], v48 offset:10272\n"
    "ds_read_b64_tr_b16 a[62:63], v48 offset:10912\n"
    "ds_read_b64_tr_b16 v[152:153], v48 offset:64\n"
    "ds_read_b64_tr_b16 v[154:155], v48 offset:704\n"
    "ds_read_b64_tr_b16 v[156:157], v48 offset:10304\n"
    "ds_read_b64_tr_b16 v[158:159], v48 offset:10944\n"
    "s_waitcnt lgkmcnt(0)\n"
    "s_waitcnt vmcnt(25)\n"
    "s_barrier\n"
    "s_mov_b32 s28, s90\n"
    "s_mov_b32 s29, s91\n"
    "s_add_u32 m0, s62, 61440\n"
    "s_mov_b32 s38, 1146880\n"
    "buffer_load_dwordx4 v16, s[28:31], s38 offen lds\n"
    "s_add_u32 m0, s62, 62464\n"
    "s_mov_b32 s38, 1146880\n"
    "buffer_load_dwordx4 v17, s[28:31], s38 offen lds\n"
    "s_add_u32 m0, s62, 63488\n"
    "s_mov_b32 s38, 1146880\n"
    "buffer_load_dwordx4 v18, s[28:31], s38 offen lds\n"
    "s_add_u32 m0, s62, 64512\n"
    "s_mov_b32 s38, 1146880\n"
    "buffer_load_dwordx4 v19, s[28:31], s38 offen lds\n"
    "s_add_u32 m0, s62, 65536\n"
    "s_mov_b32 s38, 1146880\n"
    "buffer_load_dwordx4 v20, s[28:31], s38 offen lds\n"
    "v_add_u32 v48, 81920, v14\n"
    "ds_read_b64_tr_b16 a[64:65], v48 offset:0\n"
    "ds_read_b64_tr_b16 a[66:67], v48 offset:640\n"
    "ds_read_b64_tr_b16 a[68:69], v48 offset:10240\n"
    "ds_read_b64_tr_b16 a[70:71], v48 offset:10880\n"
    "ds_read_b64_tr_b16 a[80:81], v48 offset:32\n"
    "ds_read_b64_tr_b16 a[82:83], v48 offset:672\n"
    "ds_read_b64_tr_b16 a[84:85], v48 offset:10272\n"
    "ds_read_b64_tr_b16 a[86:87], v48 offset:10912\n"
    "ds_read_b64_tr_b16 v[160:161], v48 offset:64\n"
    "ds_read_b64_tr_b16 v[162:163], v48 offset:704\n"
    "ds_read_b64_tr_b16 v[164:165], v48 offset:10304\n"
    "ds_read_b64_tr_b16 v[166:167], v48 offset:10944\n"
    "s_waitcnt lgkmcnt(0)\n"
    "s_waitcnt vmcnt(25)\n"
    "s_barrier\n"
    "s_mov_b32 s28, s92\n"
    "s_mov_b32 s29, s93\n"
    "s_add_u32 m0, s62, 81920\n"
    "s_mov_b32 s38, 1146880\n"
    "buffer_load_dwordx4 v16, s[28:31], s38 offen lds\n"
    "s_add_u32 m0, s62, 82944\n"
    "s_mov_b32 s38, 1146880\n"
    "buffer_load_dwordx4 v17, s[28:31], s38 offen lds\n"
    "s_add_u32 m0, s62, 83968\n"
    "s_mov_b32 s38, 1146880\n"
    "buffer_load_dwordx4 v18, s[28:31], s38 offen lds\n"
    "s_add_u32 m0, s62, 84992\n"
    "s_mov_b32 s38, 1146880\n"
    "buffer_load_dwordx4 v19, s[28:31], s38 offen lds\n"
    "s_add_u32 m0, s62, 86016\n"
    "s_mov_b32 s38, 1146880\n"
    "buffer_load_dwordx4 v20, s[28:31], s38 offen lds\n"
    "v_add_u32 v48, 102400, v14\n"
    "ds_read_b64_tr_b16 a[72:73], v48 offset:0\n"
    "ds_read_b64_tr_b16 a[74:75], v48 offset:640\n"
    "ds_read_b64_tr_b16 a[76:77], v48 offset:10240\n"
    "ds_read_b64_tr_b16 a[78:79], v48 offset:10880\n"
    "ds_read_b64_tr_b16 a[88:89], v48 offset:32\n"
    "ds_read_b64_tr_b16 a[90:91], v48 offset:672\n"
    "ds_read_b64_tr_b16 a[92:93], v48 offset:10272\n"
    "ds_read_b64_tr_b16 a[94:95], v48 offset:10912\n"
    "ds_read_b64_tr_b16 v[168:169], v48 offset:64\n"
    "ds_read_b64_tr_b16 v[170:171], v48 offset:704\n"
    "ds_read_b64_tr_b16 v[172:173], v48 offset:10304\n"
    "ds_read_b64_tr_b16 v[174:175], v48 offset:10944\n"
    "s_waitcnt lgkmcnt(0)\n"
    "s_waitcnt vmcnt(25)\n"
    "s_barrier\n"
    "s_mov_b32 s28, s90\n"
    "s_mov_b32 s29, s91\n"
    "s_add_u32 m0, s62, 102400\n"
    "s_mov_b32 s38, 1376256\n"
    "buffer_load_dwordx4 v16, s[28:31], s38 offen lds\n"
    "s_add_u32 m0, s62, 103424\n"
    "s_mov_b32 s38, 1376256\n"
    "buffer_load_dwordx4 v17, s[28:31], s38 offen lds\n"
    "s_add_u32 m0, s62, 104448\n"
    "s_mov_b32 s38, 1376256\n"
    "buffer_load_dwordx4 v18, s[28:31], s38 offen lds\n"
    "s_add_u32 m0, s62, 105472\n"
    "s_mov_b32 s38, 1376256\n"
    "buffer_load_dwordx4 v19, s[28:31], s38 offen lds\n"
    "s_add_u32 m0, s62, 106496\n"
    "s_mov_b32 s38, 1376256\n"
    "buffer_load_dwordx4 v20, s[28:31], s38 offen lds\n"
    "v_add_u32 v48, 122880, v14\n"
    "ds_read_b64_tr_b16 a[96:97], v48 offset:0\n"
    "ds_read_b64_tr_b16 a[98:99], v48 offset:640\n"
    "ds_read_b64_tr_b16 a[100:101], v48 offset:10240\n"
    "ds_read_b64_tr_b16 a[102:103], v48 offset:10880\n"
    "ds_read_b64_tr_b16 a[112:113], v48 offset:32\n"
    "ds_read_b64_tr_b16 a[114:115], v48 offset:672\n"
    "ds_read_b64_tr_b16 a[116:117], v48 offset:10272\n"
    "ds_read_b64_tr_b16 a[118:119], v48 offset:10912\n"
    "ds_read_b64_tr_b16 v[176:177], v48 offset:64\n"
    "ds_read_b64_tr_b16 v[178:179], v48 offset:704\n"
    "ds_read_b64_tr_b16 v[180:181], v48 offset:10304\n"
    "ds_read_b64_tr_b16 v[182:183], v48 offset:10944\n"
    "s_waitcnt lgkmcnt(0)\n"
    "s_waitcnt vmcnt(25)\n"
    "s_barrier\n"
    "s_mov_b32 s28, s92\n"
    "s_mov_b32 s29, s93\n"
    "s_add_u32 m0, s62, 122880\n"
    "s_mov_b32 s38, 1376256\n"
    "buffer_load_dwordx4 v16, s[28:31], s38 offen lds\n"
    "s_add_u32 m0, s62, 123904\n"
    "s_mov_b32 s38, 1376256\n"
    "buffer_load_dwordx4 v17, s[28:31], s38 offen lds\n"
    "s_add_u32 m0, s62, 124928\n"
    "s_mov_b32 s38, 1376256\n"
    "buffer_load_dwordx4 v18, s[28:31], s38 offen lds\n"
    "s_add_u32 m0, s62, 125952\n"
    "s_mov_b32 s38, 1376256\n"
    "buffer_load_dwordx4 v19, s[28:31], s38 offen lds\n"
    "s_add_u32 m0, s62, 126976\n"
    "s_mov_b32 s38, 1376256\n"
    "buffer_load_dwordx4 v20, s[28:31], s38 offen lds\n"
    "v_add_u32 v48, 0, v14\n"
    "ds_read_b64_tr_b16 a[104:105], v48 offset:0\n"
    "ds_read_b64_tr_b16 a[106:107], v48 offset:640\n"
    "ds_read_b64_tr_b16 a[108:109], v48 offset:10240\n"
    "ds_read_b64_tr_b16 a[110:111], v48 offset:10880\n"
    "ds_read_b64_tr_b16 a[120:121], v48 offset:32\n"
    "ds_read_b64_tr_b16 a[122:123], v48 offset:672\n"
    "ds_read_b64_tr_b16 a[124:125], v48 offset:10272\n"
    "ds_read_b64_tr_b16 a[126:127], v48 offset:10912\n"
    "ds_read_b64_tr_b16 v[184:185], v48 offset:64\n"
    "ds_read_b64_tr_b16 v[186:187], v48 offset:704\n"
    "ds_read_b64_tr_b16 v[188:189], v48 offset:10304\n"
    "ds_read_b64_tr_b16 v[190:191], v48 offset:10944\n"
    "s_waitcnt lgkmcnt(0)\n"
    "s_waitcnt vmcnt(25)\n"
    "s_barrier\n"
    "s_mov_b32 s28, s90\n"
    "s_mov_b32 s29, s91\n"
    "s_add_u32 m0, s62, 0\n"
    "s_mov_b32 s38, 1605632\n"
    "buffer_load_dwordx4 v16, s[28:31], s38 offen lds\n"
    "s_add_u32 m0, s62, 1024\n"
    "s_mov_b32 s38, 1605632\n"
    "buffer_load_dwordx4 v17, s[28:31], s38 offen lds\n"
    "s_add_u32 m0, s62, 2048\n"
    "s_mov_b32 s38, 1605632\n"
    "buffer_load_dwordx4 v18, s[28:31], s38 offen lds\n"
    "s_add_u32 m0, s62, 3072\n"
    "s_mov_b32 s38, 1605632\n"
    "buffer_load_dwordx4 v19, s[28:31], s38 offen lds\n"
    "s_add_u32 m0, s62, 4096\n"
    "s_mov_b32 s38, 1605632\n"
    "buffer_load_dwordx4 v20, s[28:31], s38 offen lds\n"
    "v_add_u32 v48, 20480, v14\n"
    "ds_read_b64_tr_b16 a[128:129], v48 offset:0\n"
    "ds_read_b64_tr_b16 a[130:131], v48 offset:640\n"
    "ds_read_b64_tr_b16 a[132:133], v48 offset:10240\n"
    "ds_read_b64_tr_b16 a[134:135], v48 offset:10880\n"
    "ds_read_b64_tr_b16 a[144:145], v48 offset:32\n"
    "ds_read_b64_tr_b16 a[146:147], v48 offset:672\n"
    "ds_read_b64_tr_b16 a[148:149], v48 offset:10272\n"
    "ds_read_b64_tr_b16 a[150:151], v48 offset:10912\n"
    "ds_read_b64_tr_b16 v[192:193], v48 offset:64\n"
    "ds_read_b64_tr_b16 v[194:195], v48 offset:704\n"
    "ds_read_b64_tr_b16 v[196:197], v48 offset:10304\n"
    "ds_read_b64_tr_b16 v[198:199], v48 offset:10944\n"
    "s_waitcnt lgkmcnt(0)\n"
    "s_waitcnt vmcnt(25)\n"
    "s_barrier\n"
    "s_mov_b32 s28, s92\n"
    "s_mov_b32 s29, s93\n"
    "s_add_u32 m0, s62, 20480\n"
    "s_mov_b32 s38, 1605632\n"
    "buffer_load_dwordx4 v16, s[28:31], s38 offen lds\n"
    "s_add_u32 m0, s62, 21504\n"
    "s_mov_b32 s38, 1605632\n"
    "buffer_load_dwordx4 v17, s[28:31], s38 offen lds\n"
    "s_add_u32 m0, s62, 22528\n"
    "s_mov_b32 s38, 1605632\n"
    "buffer_load_dwordx4 v18, s[28:31], s38 offen lds\n"
    "s_add_u32 m0, s62, 23552\n"
    "s_mov_b32 s38, 1605632\n"
    "buffer_load_dwordx4 v19, s[28:31], s38 offen lds\n"
    "s_add_u32 m0, s62, 24576\n"
    "s_mov_b32 s38, 1605632\n"
    "buffer_load_dwordx4 v20, s[28:31], s38 offen lds\n"
    "v_add_u32 v48, 40960, v14\n"
    "ds_read_b64_tr_b16 a[136:137], v48 offset:0\n"
    "ds_read_b64_tr_b16 a[138:139], v48 offset:640\n"
    "ds_read_b64_tr_b16 a[140:141], v48 offset:10240\n"
    "ds_read_b64_tr_b16 a[142:143], v48 offset:10880\n"
    "ds_read_b64_tr_b16 a[152:153], v48 offset:32\n"
    "ds_read_b64_tr_b16 a[154:155], v48 offset:672\n"
    "ds_read_b64_tr_b16 a[156:157], v48 offset:10272\n"
    "ds_read_b64_tr_b16 a[158:159], v48 offset:10912\n"
    "ds_read_b64_tr_b16 v[200:201], v48 offset:64\n"
    "ds_read_b64_tr_b16 v[202:203], v48 offset:704\n"
    "ds_read_b64_tr_b16 v[204:205], v48 offset:10304\n"
    "ds_read_b64_tr_b16 v[206:207], v48 offset:10944\n"
    "s_waitcnt lgkmcnt(0)\n"
    "s_waitcnt vmcnt(25)\n"
    "s_barrier\n"
    "v_add_u32 v48, 61440, v14\n"
    "ds_read_b64_tr_b16 a[160:161], v48 offset:0\n"
    "ds_read_b64_tr_b16 a[162:163], v48 offset:640\n"
    "ds_read_b64_tr_b16 a[164:165], v48 offset:10240\n"
    "ds_read_b64_tr_b16 a[166:167], v48 offset:10880\n"
    "ds_read_b64_tr_b16 a[176:177], v48 offset:32\n"
    "ds_read_b64_tr_b16 a[178:179], v48 offset:672\n"
    "ds_read_b64_tr_b16 a[180:181], v48 offset:10272\n"
    "ds_read_b64_tr_b16 a[182:183], v48 offset:10912\n"
    "ds_read_b64_tr_b16 v[208:209], v48 offset:64\n"
    "ds_read_b64_tr_b16 v[210:211], v48 offset:704\n"
    "ds_read_b64_tr_b16 v[212:213], v48 offset:10304\n"
    "ds_read_b64_tr_b16 v[214:215], v48 offset:10944\n"
    "s_waitcnt lgkmcnt(0)\n"
    "s_waitcnt vmcnt(20)\n"
    "s_barrier\n"
    "v_add_u32 v48, 81920, v14\n"
    "ds_read_b64_tr_b16 a[168:169], v48 offset:0\n"
    "ds_read_b64_tr_b16 a[170:171], v48 offset:640\n"
    "ds_read_b64_tr_b16 a[172:173], v48 offset:10240\n"
    "ds_read_b64_tr_b16 a[174:175], v48 offset:10880\n"
    "ds_read_b64_tr_b16 a[184:185], v48 offset:32\n"
    "ds_read_b64_tr_b16 a[186:187], v48 offset:672\n"
    "ds_read_b64_tr_b16 a[188:189], v48 offset:10272\n"
    "ds_read_b64_tr_b16 a[190:191], v48 offset:10912\n"
    "ds_read_b64_tr_b16 v[216:217], v48 offset:64\n"
    "ds_read_b64_tr_b16 v[218:219], v48 offset:704\n"
    "ds_read_b64_tr_b16 v[220:221], v48 offset:10304\n"
    "ds_read_b64_tr_b16 v[222:223], v48 offset:10944\n"
    "s_waitcnt lgkmcnt(0)\n"
    "s_waitcnt vmcnt(15)\n"
    "s_barrier\n"
    "v_add_u32 v48, 102400, v14\n"
    "ds_read_b64_tr_b16 a[192:193], v48 offset:0\n"
    "ds_read_b64_tr_b16 a[194:195], v48 offset:640\n"
    "ds_read_b64_tr_b16 a[196:197], v48 offset:10240\n"
    "ds_read_b64_tr_b16 a[198:199], v48 offset:10880\n"
    "ds_read_b64_tr_b16 a[208:209], v48 offset:32\n"
    "ds_read_b64_tr_b16 a[210:211], v48 offset:672\n"
    "ds_read_b64_tr_b16 a[212:213], v48 offset:10272\n"
    "ds_read_b64_tr_b16 a[214:215], v48 offset:10912\n"
    "ds_read_b64_tr_b16 v[224:225], v48 offset:64\n"
    "ds_read_b64_tr_b16 v[226:227], v48 offset:704\n"
    "ds_read_b64_tr_b16 v[228:229], v48 offset:10304\n"
    "ds_read_b64_tr_b16 v[230:231], v48 offset:10944\n"
    "s_waitcnt lgkmcnt(0)\n"
    "s_waitcnt vmcnt(10)\n"
    "s_barrier\n"
    "v_add_u32 v48, 122880, v14\n"
    "ds_read_b64_tr_b16 a[200:201], v48 offset:0\n"
    "ds_read_b64_tr_b16 a[202:203], v48 offset:640\n"
    "ds_read_b64_tr_b16 a[204:205], v48 offset:10240\n"
    "ds_read_b64_tr_b16 a[206:207], v48 offset:10880\n"
    "ds_read_b64_tr_b16 a[216:217], v48 offset:32\n"
    "ds_read_b64_tr_b16 a[218:219], v48 offset:672\n"
    "ds_read_b64_tr_b16 a[220:221], v48 offset:10272\n"
    "ds_read_b64_tr_b16 a[222:223], v48 offset:10912\n"
    "ds_read_b64_tr_b16 v[232:233], v48 offset:64\n"
    "ds_read_b64_tr_b16 v[234:235], v48 offset:704\n"
    "ds_read_b64_tr_b16 v[236:237], v48 offset:10304\n"
    "ds_read_b64_tr_b16 v[238:239], v48 offset:10944\n"
    "s_waitcnt lgkmcnt(0)\n"
    "s_waitcnt vmcnt(5)\n"
    "s_barrier\n"
    "v_add_u32 v48, 0, v14\n"
    "ds_read_b64_tr_b16 a[224:225], v48 offset:0\n"
    "ds_read_b64_tr_b16 a[226:227], v48 offset:640\n"
    "ds_read_b64_tr_b16 a[228:229], v48 offset:10240\n"
    "ds_read_b64_tr_b16 a[230:231], v48 offset:10880\n"
    "ds_read_b64_tr_b16 a[240:241], v48 offset:32\n"
    "ds_read_b64_tr_b16 a[242:243], v48 offset:672\n"
    "ds_read_b64_tr_b16 a[244:245], v48 offset:10272\n"
    "ds_read_b64_tr_b16 a[246:247], v48 offset:10912\n"
    "ds_read_b64_tr_b16 v[240:241], v48 offset:64\n"
    "ds_read_b64_tr_b16 v[242:243], v48 offset:704\n"
    "ds_read_b64_tr_b16 v[244:245], v48 offset:10304\n"
    "ds_read_b64_tr_b16 v[246:247], v48 offset:10944\n"
    "s_waitcnt lgkmcnt(0)\n"
    "s_waitcnt vmcnt(0)\n"
    "s_barrier\n"
    "v_add_u32 v48, 20480, v14\n"
    "ds_read_b64_tr_b16 a[232:233], v48 offset:0\n"
    "ds_read_b64_tr_b16 a[234:235], v48 offset:640\n"
    "ds_read_b64_tr_b16 a[236:237], v48 offset:10240\n"
    "ds_read_b64_tr_b16 a[238:239], v48 offset:10880\n"
    "ds_read_b64_tr_b16 a[248:249], v48 offset:32\n"
    "ds_read_b64_tr_b16 a[250:251], v48 offset:672\n"
    "ds_read_b64_tr_b16 a[252:253], v48 offset:10272\n"
    "ds_read_b64_tr_b16 a[254:255], v48 offset:10912\n"
    "ds_read_b64_tr_b16 v[248:249], v48 offset:64\n"
    "ds_read_b64_tr_b16 v[250:251], v48 offset:704\n"
    "ds_read_b64_tr_b16 v[252:253], v48 offset:10304\n"
    "ds_read_b64_tr_b16 v[254:255], v48 offset:10944\n"
    "s_waitcnt lgkmcnt(0)\n"
    "s_barrier\n"
    "v_lshlrev_b32 v32, 16, v116\n"
    "v_and_b32 v33, 0xffff0000, v116\n"
    "v_lshlrev_b32 v34, 16, v117\n"
    "v_and_b32 v35, 0xffff0000, v117\n"
    "v_lshlrev_b32 v36, 16, v118\n"
    "v_and_b32 v37, 0xffff0000, v118\n"
    "v_lshlrev_b32 v38, 16, v119\n"
    "v_and_b32 v39, 0xffff0000, v119\n"
    "v_mov_b32 v24, v12\n"
    "v_mov_b32 v25, v13\n"
    "v_mov_b32 v30, v12\n"
    "v_mov_b32 v31, v13\n"
    "v_mov_b32 v26, 0\n"
    "v_mov_b32 v27, 0\n"
    "v_mov_b32 v28, 0\n"
    "v_mov_b32 v29, 0\n"
    "v_mov_b32 v42, 0\n"
    "v_mov_b32 v43, 0\n"
    "v_mov_b32 v48, 0\n"
    "v_mov_b32 v44, 0x3b000000\n"
    "s_mov_b32 s40, 0\n"
    "s_mov_b32 s41, 0\n"
    "s_mov_b32 s42, 64\n"
    "s_mov_b32 s43, 0\n"
    "s_mov_b32 s44, 0\n"
    "s_mov_b32 s45, 0\n"
    "s_mov_b32 s46, -1\n"
    "s_mov_b32 s47, -1\n"
    "s_mov_b32 s48, 0\n"
    "s_mov_b32 s51, 0\n"
    "s_mov_b32 s49, 0\n"
    "s_mov_b32 s37, 0\n"
    "s_mov_b64 s[64:65], 0\n"
    "s_mov_b32 s79, 0\n"
    "s_mov_b32 s74, 0xffff0000\n"
    "s_mov_b32 s75, 0\n"
    "s_mov_b32 s76, 0xaaaaaaaa\n"
    "s_mov_b32 s77, 0xaaaaaaaa\n"
    "LRY%=:\n"
    "global_load_dword v40, v48, s[96:97] sc1\n"
    "s_waitcnt vmcnt(0)\n"
    "v_readfirstlane_b32 s56, v40\n"
    "s_cmp_lg_u32 s56, 0\n"
    "s_cbranch_scc1 LRZ%=\n"
    "s_sleep 8\n"
    "s_add_u32 s37, s37, 1\n"
    "s_cmp_lt_u32 s37, 0x80000\n"
    "s_cbranch_scc1 LRY%=\n"
    "s_branch LFAIL%=\n"
    "LRZ%=:\n"
    "s_lshl_b32 s56, s56, 13\n"
    "s_and_b32 s56, s56, 0x2000\n"
    "s_xor_b32 s57, s56, 0x2000\n"
    "s_xor_b32 s58, s58, s56\n"
    "v_xor_b32 v2, s56, v2\n"
    "v_xor_b32 v11, s56, v11\n"
    "v_xor_b32 v12, s56, v21\n"
    "s_cmp_eq_u32 s55, 0\n"
    "s_cselect_b32 s38, 0, 0x80000000\n"
    "s_add_u32 s38, s38, 4096\n"
    "v_add_u32 v13, s38, v11\n"
    "v_mov_b32 v16, -1\n"
    "v_mov_b32 v17, -1\n"
    "v_mov_b32 v18, -1\n"
    "v_mov_b32 v19, -1\n"
    "s_mov_b32 s60, s55\n"
    "LEM%=:\n"
    "s_lshl_b32 s39, s60, 10\n"
    "s_andn2_b32 s61, s39, 0x1fff\n"
    "s_add_u32 s61, s61, s39\n"
    "s_add_u32 s61, s61, 236978176\n"
    "s_lshr_b32 s38, s61, 8\n"
    "s_xor_b32 s38, s38, s56\n"
    "s_and_b32 s38, s38, 0x2000\n"
    "s_or_b32 s61, s61, s38\n"
    "buffer_store_dwordx4 v[16:19], v0, s[16:19], s61 offen sc1\n"
    "s_add_u32 s60, s60, 32\n"
    "s_cmp_lt_u32 s60, 128\n"
    "s_cbranch_scc1 LEM%=\n"
    "s_add_u32 s70, s58, 4160\n"
    "v_mov_b32 v72, 1\n"
    "s_waitcnt vmcnt(0)\n"
    "s_mov_b64 exec, 1\n"
    "buffer_atomic_add v72, off, s[16:19], s70 sc1\n"
    "s_mov_b64 exec, -1\n"
    "s_cmp_lg_u32 s55, 0\n"
    "s_cbranch_scc1 LPR%=\n"
    "LEW%=:\n"
    "buffer_load_dword v40, off, s[16:19], s70 sc1\n"
    "s_waitcnt vmcnt(0)\n"
    "v_cmp_eq_u32 vcc, 32, v40\n"
    "s_cbranch_vccnz LPR%=\n"
    "s_sleep 8\n"
    "s_add_u32 s37, s37, 1\n"
    "s_cmp_lt_u32 s37, 0x80000\n"
    "s_cbranch_scc1 LEW%=\n"
    "s_branch LFAIL%=\n"
    "LPR%=:\n"
    "buffer_load_dword v40, v12, s[16:19], 0 offen sc1\n"
    "s_waitcnt vmcnt(0)\n"
    "v_cmp_eq_u32 vcc, 0, v40\n"
    "s_cbranch_vccz LPOLL%=\n"
    "s_sleep 8\n"
    "s_add_u32 s37, s37, 1\n"
    "s_cmp_lt_u32 s37, 0x80000\n"
    "s_cbranch_scc1 LPR%=\n"
    "s_branch LFAIL%=\n"
    "LIDLE%=:\n"
    "s_add_u32 s37, s37, 1\n"
    "s_cmp_gt_u32 s37, 0x100000\n"
    "s_cbranch_scc1 LFAIL%=\n"
    "s_sleep 1\n"
    "LPOLL%=:\n"
    "v_mad_u32_u24 v41, v3, s41, v2\n"
    "v_mad_u32_u24 v41, v5, s40, v41\n"
    "buffer_load_dword v40, v41, s[16:19], 0 offen sc1\n"
    "s_waitcnt vmcnt(0)\n"
    "LTOP%=:\n"
    "v_readlane_b32 s52, v40, 32\n"
    "v_readlane_b32 s53, v40, 0\n"
    "v_readlane_b32 s54, v40, 16\n"
    "s_cmp_lg_u32 s52, 0\n"
    "s_cbranch_scc1 LDEC%=\n"
    "LPICK%=:\n"
    "s_cmp_eq_u32 s53, 0\n"
    "s_mov_b32 s50, -1\n"
    "s_cbranch_scc1 LNONE%=\n"
    "s_mov_b32 s50, s41\n"
    "s_add_u32 s41, s41, 1\n"
    "s_lshl_b32 s62, s50, 18\n"
    "s_add_u32 s62, s62, 180355072\n"
    "s_lshl_b32 s63, s50, 5\n"
    "s_add_u32 s63, s63, s55\n"
    "s_lshl_b32 s63, s63, 10\n"
    "s_lshr_b32 s38, s62, 8\n"
    "s_xor_b32 s38, s38, s56\n"
    "s_and_b32 s38, s38, 0x2000\n"
    "s_or_b32 s62, s62, s38\n"
    "buffer_load_dwordx4 v[116:119], v0, s[20:23], s63 offen\n"
    "buffer_load_dwordx4 v[100:103], v1, s[16:19], s62 offen sc1\n"
    "s_add_u32 s62, s62, 0x10000\n"
    "buffer_load_dwordx4 v[104:107], v1, s[16:19], s62 offen sc1\n"
    "s_add_u32 s62, s62, 0x10000\n"
    "buffer_load_dwordx4 v[108:111], v1, s[16:19], s62 offen sc1\n"
    "s_add_u32 s62, s62, 0x10000\n"
    "buffer_load_dwordx4 v[112:115], v1, s[16:19], s62 offen sc1\n"
    "s_cmp_ge_i32 s47, 0\n"
    "s_cbranch_scc1 LJOB%=\n"
    "s_lshl_b32 s60, s50, 5\n"
    "s_add_u32 s60, s60, s55\n"
    "s_lshl_b32 s39, s60, 11\n"
    "s_andn2_b32 s32, s39, 0x1fff\n"
    "s_add_u32 s32, s32, s39\n"
    "s_add_u32 s32, s32, 245366784\n"
    "s_lshr_b32 s38, s32, 8\n"
    "s_xor_b32 s38, s38, s56\n"
    "s_and_b32 s38, s38, 0x2000\n"
    "s_or_b32 s32, s32, s38\n"
    "s_lshl_b32 s39, s60, 10\n"
    "s_andn2_b32 s78, s39, 0x1fff\n"
    "s_add_u32 s78, s78, s39\n"
    "s_add_u32 s78, s78, 236978176\n"
    "s_lshr_b32 s38, s78, 8\n"
    "s_xor_b32 s38, s38, s56\n"
    "s_and_b32 s38, s38, 0x2000\n"
    "s_or_b32 s78, s78, s38\n"
    "s_lshl_b32 s39, s60, 10\n"
    "s_andn2_b32 s34, s39, 0x1fff\n"
    "s_add_u32 s34, s34, s39\n"
    "s_add_u32 s34, s34, 237240320\n"
    "s_lshr_b32 s38, s34, 8\n"
    "s_xor_b32 s38, s38, s56\n"
    "s_and_b32 s38, s38, 0x2000\n"
    "s_or_b32 s34, s34, s38\n"
    "s_cmp_lt_u32 s50, 60\n"
    "s_cselect_b32 s34, s34, 0x80000000\n"
    "s_mov_b32 s33, s60\n"
    "s_waitcnt vmcnt(4)\n"
    "v_lshlrev_b32 v64, 16, v116\n"
    "v_and_b32 v65, 0xffff0000, v116\n"
    "v_lshlrev_b32 v66, 16, v117\n"
    "v_and_b32 v67, 0xffff0000, v117\n"
    "v_lshlrev_b32 v68, 16, v118\n"
    "v_and_b32 v69, 0xffff0000, v118\n"
    "v_lshlrev_b32 v70, 16, v119\n"
    "v_and_b32 v71, 0xffff0000, v119\n"
    "s_waitcnt vmcnt(3)\n"
    "v_mfma_f32_16x16x32_bf16 v[64:67], v[24:27], v[100:103], v[64:67]\n"
    "v_mfma_f32_16x16x32_bf16 v[68:71], v[28:31], v[100:103], v[68:71]\n"
    "s_waitcnt vmcnt(2)\n"
    "v_mfma_f32_16x16x32_bf16 v[64:67], v[24:27], v[104:107], v[64:67]\n"
    "v_mfma_f32_16x16x32_bf16 v[68:71], v[28:31], v[104:107], v[68:71]\n"
    "s_waitcnt vmcnt(1)\n"
    "v_mfma_f32_16x16x32_bf16 v[64:67], v[24:27], v[108:111], v[64:67]\n"
    "v_mfma_f32_16x16x32_bf16 v[68:71], v[28:31], v[108:111], v[68:71]\n"
    "v_max3_u32 v48, v103, v107, v111\n"
    "s_waitcnt vmcnt(0)\n"
    "v_mfma_f32_16x16x32_bf16 v[64:67], v[24:27], v[112:115], v[64:67]\n"
    "v_mfma_f32_16x16x32_bf16 v[68:71], v[28:31], v[112:115], v[68:71]\n"
    "v_max_u32 v48, v48, v115\n"
    "v_cmp_eq_u32 vcc, -1, v48\n"
    "s_mov_b32 s47, s50\n"
    "s_mov_b32 s48, s51\n"
    "s_mov_b32 s50, -1\n"
    "s_cbranch_vccnz LEARLY%=\n"
    "LNXT%=:\n"
    "s_mov_b32 s53, s54\n"
    "s_cmp_lg_u32 s54, 0\n"
    "s_cbranch_scc1 LPICK%=\n"
    "LJOB%=:\n"
    "v_cvt_pk_bf16_f32 v124, v64, v65\n"
    "v_lshlrev_b32 v72, 16, v124\n"
    "v_cvt_pk_bf16_f32 v125, v66, v67\n"
    "v_readfirstlane_b32 s68, v72\n"
    "v_cvt_pk_bf16_f32 v126, v68, v69\n"
    "v_cvt_pk_bf16_f32 v127, v70, v71\n"
    "v_and_b32 v73, 0xffff0000, v124\n"
    "v_lshlrev_b32 v74, 16, v125\n"
    "v_and_b32 v75, 0xffff0000, v125\n"
    "v_lshlrev_b32 v76, 16, v126\n"
    "v_and_b32 v77, 0xffff0000, v126\n"
    "v_lshlrev_b32 v78, 16, v127\n"
    "v_and_b32 v79, 0xffff0000, v127\n"
    "v_pk_add_f32 v[92:93], v[72:73], s[68:69] op_sel_hi:[1,0] neg_lo:[0,1] neg_hi:[0,1]\n"
    "v_pk_add_f32 v[94:95], v[74:75], s[68:69] op_sel_hi:[1,0] neg_lo:[0,1] neg_hi:[0,1]\n"
    "v_pk_add_f32 v[96:97], v[76:77], s[68:69] op_sel_hi:[1,0] neg_lo:[0,1] neg_hi:[0,1]\n"
    "v_pk_add_f32 v[98:99], v[78:79], s[68:69] op_sel_hi:[1,0] neg_lo:[0,1] neg_hi:[0,1]\n"
    "v_pk_add_f32 v[48:49], v[92:93], v[94:95]\n"
    "v_pk_mul_f32 v[50:51], v[92:93], v[92:93]\n"
    "v_pk_add_f32 v[52:53], v[96:97], v[98:99]\n"
    "v_pk_fma_f32 v[50:51], v[94:95], v[94:95], v[50:51]\n"
    "v_pk_add_f32 v[48:49], v[48:49], v[52:53]\n"
    "v_pk_fma_f32 v[50:51], v[96:97], v[96:97], v[50:51]\n"
    "v_add_f32 v48, v48, v49\n"
    "v_pk_fma_f32 v[50:51], v[98:99], v[98:99], v[50:51]\n"
    "v_mov_b32 v52, s68\n"
    "v_add_f32 v49, v50, v51\n"
    "v_add_f32_dpp v48, v48, v48 row_ror:8 row_mask:0xf bank_mask:0xf\n"
    "s_nop 0\n"
    "v_add_f32_dpp v49, v49, v49 row_ror:8 row_mask:0xf bank_mask:0xf\n"
    "v_add_f32_dpp v48, v48, v48 row_ror:4 row_mask:0xf bank_mask:0xf\n"
    "s_nop 0\n"
    "v_add_f32_dpp v49, v49, v49 row_ror:4 row_mask:0xf bank_mask:0xf\n"
    "v_add_f32_dpp v48, v48, v48 row_ror:2 row_mask:0xf bank_mask:0xf\n"
    "s_nop 0\n"
    "v_add_f32_dpp v49, v49, v49 row_ror:2 row_mask:0xf bank_mask:0xf\n"
    "v_add_f32_dpp v48, v48, v48 row_ror:1 row_mask:0xf bank_mask:0xf\n"
    "s_nop 0\n"
    "v_add_f32_dpp v49, v49, v49 row_ror:1 row_mask:0xf bank_mask:0xf\n"
    "s_nop 1\n"
    "v_permlane16_swap_b32 v48, v49\n"
    "v_add_f32 v48, v48, v49\n"
    "v_mov_b32 v49, v48\n"
    "s_nop 1\n"
    "v_permlane32_swap_b32 v48, v49\n"
    "v_add_f32 v48, v48, v49\n"
    "s_nop 0\n"
    "v_readlane_b32 s62, v48, 0\n"
    "v_readlane_b32 s63, v48, 16\n"
    "s_nop 0\n"
    "v_mul_f32 v48, s62, v44\n"
    "v_mul_f32 v49, s63, v44\n"
    "v_fma_f32 v49, -v48, v48, v49\n"
    "v_add_f32 v52, v52, v48\n"
    "v_max_f32 v49, 0, v49\n"
    "v_cvt_pk_bf16_f32 v48, v52, v49\n"
    "v_and_b32 v49, 0xffff0000, v48\n"
    "v_add_f32 v49, 0x3727c5ac, v49\n"
    "v_cvt_pk_bf16_f32 v49, v49, v49\n"
    "v_lshlrev_b32 v49, 16, v49\n"
    "v_sqrt_f32 v50, v49\n"
    "v_lshlrev_b32 v48, 16, v48\n"
    "v_cvt_pk_bf16_f32 v50, v50, v50\n"
    "v_lshlrev_b32 v50, 16, v50\n"
    "v_rcp_f32 v50, v50\n"
    "v_pk_add_f32 v[92:93], v[72:73], v[48:49] op_sel_hi:[1,0] neg_lo:[0,1] neg_hi:[0,1]\n"
    "v_pk_add_f32 v[94:95], v[74:75], v[48:49] op_sel_hi:[1,0] neg_lo:[0,1] neg_hi:[0,1]\n"
    "v_pk_add_f32 v[96:97], v[76:77], v[48:49] op_sel_hi:[1,0] neg_lo:[0,1] neg_hi:[0,1]\n"
    "v_pk_add_f32 v[98:99], v[78:79], v[48:49] op_sel_hi:[1,0] neg_lo:[0,1] neg_hi:[0,1]\n"
    "v_cvt_pk_bf16_f32 v52, v92, v93\n"
    "v_cvt_pk_bf16_f32 v53, v94, v95\n"
    "v_cvt_pk_bf16_f32 v54, v96, v97\n"
    "v_cvt_pk_bf16_f32 v55, v98, v99\n"
    "v_lshlrev_b32 v92, 16, v52\n"
    "v_and_b32 v93, 0xffff0000, v52\n"
    "v_lshlrev_b32 v94, 16, v53\n"
    "v_and_b32 v95, 0xffff0000, v53\n"
    "v_lshlrev_b32 v96, 16, v54\n"
    "v_and_b32 v97, 0xffff0000, v54\n"
    "v_lshlrev_b32 v98, 16, v55\n"
    "v_and_b32 v99, 0xffff0000, v55\n"
    "v_pk_mul_f32 v[80:81], v[92:93], v[50:51] op_sel_hi:[1,0]\n"
    "v_pk_mul_f32 v[82:83], v[94:95], v[50:51] op_sel_hi:[1,0]\n"
    "v_pk_mul_f32 v[84:85], v[96:97], v[50:51] op_sel_hi:[1,0]\n"
    "v_pk_mul_f32 v[86:87], v[98:99], v[50:51] op_sel_hi:[1,0]\n"
    "v_cvt_pk_bf16_f32 v88, v80, v81\n"
    "v_cvt_pk_bf16_f32 v89, v82, v83\n"
    "v_cvt_pk_bf16_f32 v90, v84, v85\n"
    "v_cvt_pk_bf16_f32 v91, v86, v87\n"
    "v_lshlrev_b32 v80, 16, v88\n"
    "v_and_b32 v81, 0xffff0000, v88\n"
    "v_lshlrev_b32 v82, 16, v89\n"
    "v_and_b32 v83, 0xffff0000, v89\n"
    "v_lshlrev_b32 v84, 16, v90\n"
    "v_and_b32 v85, 0xffff0000, v90\n"
    "v_lshlrev_b32 v86, 16, v91\n"
    "v_and_b32 v87, 0xffff0000, v91\n"
    "v_pk_add_f32 v[72:73], v[72:73], v[80:81]\n"
    "v_pk_add_f32 v[74:75], v[74:75], v[82:83]\n"
    "v_pk_add_f32 v[76:77], v[76:77], v[84:85]\n"
    "v_pk_add_f32 v[78:79], v[78:79], v[86:87]\n"
    "v_cvt_pk_bf16_f32 v60, v72, v73\n"
    "v_cvt_pk_bf16_f32 v61, v74, v75\n"
    "v_cvt_pk_bf16_f32 v62, v76, v77\n"
    "v_cvt_pk_bf16_f32 v63, v78, v79\n"
    "v_lshlrev_b32 v72, 16, v60\n"
    "v_and_b32 v73, 0xffff0000, v60\n"
    "v_lshlrev_b32 v74, 16, v61\n"
    "v_and_b32 v75, 0xffff0000, v61\n"
    "v_lshlrev_b32 v76, 16, v62\n"
    "v_and_b32 v77, 0xffff0000, v62\n"
    "v_lshlrev_b32 v78, 16, v63\n"
    "v_and_b32 v79, 0xffff0000, v63\n"
    "s_lshl_b32 s70, s47, 6\n"
    "s_add_u32 s70, s70, s58\n"
    "v_pk_add_f32 v[92:93], v[72:73], s[68:69] op_sel_hi:[1,0] neg_lo:[0,1] neg_hi:[0,1]\n"
    "v_pk_add_f32 v[94:95], v[74:75], s[68:69] op_sel_hi:[1,0] neg_lo:[0,1] neg_hi:[0,1]\n"
    "v_pk_add_f32 v[96:97], v[76:77], s[68:69] op_sel_hi:[1,0] neg_lo:[0,1] neg_hi:[0,1]\n"
    "v_pk_add_f32 v[98:99], v[78:79], s[68:69] op_sel_hi:[1,0] neg_lo:[0,1] neg_hi:[0,1]\n"
    "v_pk_add_f32 v[48:49], v[92:93], v[94:95]\n"
    "v_pk_mul_f32 v[50:51], v[92:93], v[92:93]\n"
    "v_pk_add_f32 v[52:53], v[96:97], v[98:99]\n"
    "v_pk_fma_f32 v[50:51], v[94:95], v[94:95], v[50:51]\n"
    "v_pk_add_f32 v[48:49], v[48:49], v[52:53]\n"
    "v_pk_fma_f32 v[50:51], v[96:97], v[96:97], v[50:51]\n"
    "v_add_f32 v48, v48, v49\n"
    "v_pk_fma_f32 v[50:51], v[98:99], v[98:99], v[50:51]\n"
    "v_mov_b32 v52, s68\n"
    "v_add_f32 v49, v50, v51\n"
    "v_add_f32_dpp v48, v48, v48 row_ror:8 row_mask:0xf bank_mask:0xf\n"
    "s_nop 0\n"
    "v_add_f32_dpp v49, v49, v49 row_ror:8 row_mask:0xf bank_mask:0xf\n"
    "v_pk_mul_f32 v[56:57], v[80:81], v[32:33]\n"
    "v_add_f32_dpp v48, v48, v48 row_ror:4 row_mask:0xf bank_mask:0xf\n"
    "v_add_f32_dpp v49, v49, v49 row_ror:4 row_mask:0xf bank_mask:0xf\n"
    "v_pk_fma_f32 v[56:57], v[82:83], v[34:35], v[56:57]\n"
    "v_add_f32_dpp v48, v48, v48 row_ror:2 row_mask:0xf bank_mask:0xf\n"
    "v_add_f32_dpp v49, v49, v49 row_ror:2 row_mask:0xf bank_mask:0xf\n"
    "v_pk_fma_f32 v[56:57], v[84:85], v[36:37], v[56:57]\n"
    "v_add_f32_dpp v48, v48, v48 row_ror:1 row_mask:0xf bank_mask:0xf\n"
    "v_add_f32_dpp v49, v49, v49 row_ror:1 row_mask:0xf bank_mask:0xf\n"
    "v_pk_fma_f32 v[56:57], v[86:87], v[38:39], v[56:57]\n"
    "s_nop 0\n"
    "v_permlane16_swap_b32 v48, v49\n"
    "v_add_f32 v48, v48, v49\n"
    "v_mov_b32 v49, v48\n"
    "s_nop 1\n"
    "v_permlane32_swap_b32 v48, v49\n"
    "v_add_f32 v48, v48, v49\n"
    "s_nop 0\n"
    "v_readlane_b32 s62, v48, 0\n"
    "v_readlane_b32 s63, v48, 16\n"
    "s_nop 0\n"
    "v_mul_f32 v48, s62, v44\n"
    "v_mul_f32 v49, s63, v44\n"
    "v_fma_f32 v49, -v48, v48, v49\n"
    "v_add_f32 v52, v52, v48\n"
    "v_max_f32 v49, 0, v49\n"
    "v_cvt_pk_bf16_f32 v48, v52, v49\n"
    "v_and_b32 v49, 0xffff0000, v48\n"
    "v_add_f32 v49, 0x3727c5ac, v49\n"
    "v_cvt_pk_bf16_f32 v49, v49, v49\n"
    "v_lshlrev_b32 v49, 16, v49\n"
    "v_sqrt_f32 v50, v49\n"
    "v_lshlrev_b32 v48, 16, v48\n"
    "v_cvt_pk_bf16_f32 v50, v50, v50\n"
    "v_lshlrev_b32 v50, 16, v50\n"
    "v_rcp_f32 v50, v50\n"
    "v_pk_add_f32 v[92:93], v[72:73], v[48:49] op_sel_hi:[1,0] neg_lo:[0,1] neg_hi:[0,1]\n"
    "v_pk_add_f32 v[94:95], v[74:75], v[48:49] op_sel_hi:[1,0] neg_lo:[0,1] neg_hi:[0,1]\n"
    "v_pk_add_f32 v[96:97], v[76:77], v[48:49] op_sel_hi:[1,0] neg_lo:[0,1] neg_hi:[0,1]\n"
    "v_pk_add_f32 v[98:99], v[78:79], v[48:49] op_sel_hi:[1,0] neg_lo:[0,1] neg_hi:[0,1]\n"
    "v_pk_mul_f32 v[64:65], v[92:93], v[50:51] op_sel_hi:[1,0]\n"
    "v_pk_mul_f32 v[66:67], v[94:95], v[50:51] op_sel_hi:[1,0]\n"
    "v_pk_mul_f32 v[68:69], v[96:97], v[50:51] op_sel_hi:[1,0]\n"
    "v_pk_mul_f32 v[70:71], v[98:99], v[50:51] op_sel_hi:[1,0]\n"
    "v_cvt_pk_bf16_f32 v95, v70, v71\n"
    "v_cvt_pk_bf16_f32 v92, v64, v65\n"
    "v_cvt_pk_bf16_f32 v93, v66, v67\n"
    "v_cvt_pk_bf16_f32 v94, v68, v69\n"
    "v_min_u32 v95, -2, v95\n"
    "buffer_store_dwordx4 v[92:95], v0, s[16:19], s78 offen offset:0 sc1\n"
    "s_lshl_b32 s39, s47, 2\n"
    "s_mov_b64 exec, 7\n"
    "buffer_store_dword v6, v13, s[16:19], s39 offen sc1\n"
    "s_mov_b64 exec, -1\n"
    "v_add_f32_dpp v56, v56, v56 row_ror:8 row_mask:0xf bank_mask:0xf\n"
    "v_add_f32_dpp v57, v57, v57 row_ror:8 row_mask:0xf bank_mask:0xf\n"
    "s_nop 0\n"
    "v_add_f32_dpp v56, v56, v56 row_ror:4 row_mask:0xf bank_mask:0xf\n"
    "v_add_f32_dpp v57, v57, v57 row_ror:4 row_mask:0xf bank_mask:0xf\n"
    "s_nop 0\n"
    "v_add_f32_dpp v56, v56, v56 row_ror:2 row_mask:0xf bank_mask:0xf\n"
    "v_add_f32_dpp v57, v57, v57 row_ror:2 row_mask:0xf bank_mask:0xf\n"
    "s_nop 0\n"
    "v_add_f32_dpp v56, v56, v56 row_ror:1 row_mask:0xf bank_mask:0xf\n"
    "v_add_f32_dpp v57, v57, v57 row_ror:1 row_mask:0xf bank_mask:0xf\n"
    "s_nop 1\n"
    "v_permlane16_swap_b32 v56, v57\n"
    "v_add_f32 v56, v56, v57\n"
    "v_mov_b32 v57, v56\n"
    "s_nop 1\n"
    "v_permlane32_swap_b32 v56, v57\n"
    "v_add_f32 v56, v56, v57\n"
    "s_nop 0\n"
    "v_readlane_b32 s62, v56, 0\n"
    "v_readlane_b32 s63, v56, 16\n"
    "s_sub_u32 s39, 0xffff, s33\n"
    "v_mov_b32 v56, s62\n"
    "v_add_f32 v56, s63, v56\n"
    "v_cvt_pk_bf16_f32 v56, v56, v56\n"
    "v_lshlrev_b32 v56, 16, v56\n"
    "v_cmp_o_f32 vcc, v56, v56\n"
    "v_ashrrev_i32 v57, 31, v56\n"
    "v_or_b32 v57, 0x80000000, v57\n"
    "v_xor_b32 v56, v56, v57\n"
    "v_cndmask_b32 v56, 0, v56, vcc\n"
    "v_and_b32 v56, 0xffff0000, v56\n"
    "v_or_b32 v56, s39, v56\n"
    "s_mov_b64 exec, 1\n"
    "buffer_atomic_umax v56, off, s[16:19], s70 offset:4 sc1\n"
    "s_mov_b64 exec, -1\n"
    "buffer_store_dwordx4 v[88:91], v0, s[16:19], s32 offen offset:1024 sc1\n"
    "buffer_store_dwordx4 v[124:127], v0, s[16:19], s32 offen offset:0 sc1\n"
    "s_add_u32 s61, s78, 4194304\n"
    "buffer_store_dwordx4 v[60:63], v0, s[16:19], s61 offen offset:0 sc1\n"
    "buffer_store_dwordx4 v[16:19], v0, s[16:19], s34 offen offset:0 sc1\n"
    "v_mov_b64 v[120:121], 1\n"
    "v_mad_u32_u24 v41, v3, s41, v2\n"
    "v_mad_u32_u24 v41, v5, s40, v41\n"
    "s_nop 7\n"
    "s_nop 7\n"
    "s_nop 7\n"
    "buffer_load_dword v40, v41, s[16:19], 0 offen sc1\n"
    "s_mov_b32 s73, s47\n"
    "s_mov_b64 exec, 1\n"
    "s_waitcnt vmcnt(1)\n"
    "buffer_atomic_add_x2 v[120:121], off, s[16:19], s70 sc0 sc1\n"
    "s_mov_b64 exec, -1\n"
    "s_mov_b32 s47, -1\n"
    "s_mov_b32 s37, 0\n"
    "s_cmp_ge_i32 s50, 0\n"
    "s_cbranch_scc1 LAB%=\n"
    "s_waitcnt vmcnt(1)\n"
    "v_readlane_b32 s52, v40, 32\n"
    "v_readlane_b32 s53, v40, 0\n"
    "v_readlane_b32 s54, v40, 16\n"
    "s_cmp_eq_u32 s52, 0\n"
    "s_cselect_b32 s38, s53, 0\n"
    "s_cmp_lg_u32 s38, 0\n"
    "s_cbranch_scc1 LAC%=\n"
    "buffer_load_dword v40, v41, s[16:19], 0 offen sc1\n"
    "s_waitcnt vmcnt(1)\n"
    "v_readfirstlane_b32 s38, v120\n"
    "v_readfirstlane_b32 s52, v121\n"
    "s_lshl_b32 s39, s73, 2\n"
    "s_cmp_eq_u32 s38, 31\n"
    "v_mov_b32 v120, s52\n"
    "s_cselect_b32 s39, s39, 0x80000000\n"
    "s_cselect_b32 s73, s73, -1\n"
    "s_mov_b64 exec, 0x38\n"
    "buffer_store_dword v120, v11, s[16:19], s39 offen sc1\n"
    "s_mov_b64 exec, -1\n"
    "s_waitcnt vmcnt(1)\n"
    "LAD%=:\n"
    "s_cmp_lg_u32 s40, s73\n"
    "s_cbranch_scc1 LTOP%=\n"
    "v_writelane_b32 v40, s52, 32\n"
    "s_nop 0\n"
    "s_branch LTOP%=\n"
    "LAC%=:\n"
    "s_mov_b32 s50, s41\n"
    "s_add_u32 s41, s41, 1\n"
    "s_lshl_b32 s62, s50, 18\n"
    "s_add_u32 s62, s62, 180355072\n"
    "s_lshl_b32 s63, s50, 5\n"
    "s_add_u32 s63, s63, s55\n"
    "s_lshl_b32 s63, s63, 10\n"
    "s_lshr_b32 s38, s62, 8\n"
    "s_xor_b32 s38, s38, s56\n"
    "s_and_b32 s38, s38, 0x2000\n"
    "s_or_b32 s62, s62, s38\n"
    "buffer_load_dwordx4 v[116:119], v0, s[20:23], s63 offen\n"
    "buffer_load_dwordx4 v[100:103], v1, s[16:19], s62 offen sc1\n"
    "s_add_u32 s62, s62, 0x10000\n"
    "buffer_load_dwordx4 v[104:107], v1, s[16:19], s62 offen sc1\n"
    "s_add_u32 s62, s62, 0x10000\n"
    "buffer_load_dwordx4 v[108:111], v1, s[16:19], s62 offen sc1\n"
    "s_add_u32 s62, s62, 0x10000\n"
    "buffer_load_dwordx4 v[112:115], v1, s[16:19], s62 offen sc1\n"
    "s_waitcnt vmcnt(5)\n"
    "v_readfirstlane_b32 s38, v120\n"
    "v_readfirstlane_b32 s52, v121\n"
    "s_lshl_b32 s39, s73, 2\n"
    "s_cmp_eq_u32 s38, 31\n"
    "v_mov_b32 v120, s52\n"
    "s_cselect_b32 s39, s39, 0x80000000\n"
    "s_cselect_b32 s73, s73, -1\n"
    "s_mov_b64 exec, 0x38\n"
    "buffer_store_dword v120, v11, s[16:19], s39 offen sc1\n"
    "s_mov_b64 exec, -1\n"
    "s_cmp_lg_u32 s40, s73\n"
    "s_cbranch_scc1 LAE%=\n"
    "s_lshr_b32 s38, s52, 16\n"
    "s_cmp_gt_i32 s38, s46\n"
    "s_cbranch_scc0 LAE%=\n"
    "s_sub_u32 s41, s41, 1\n"
    "s_waitcnt vmcnt(1)\n"
    "s_branch LDEC%=\n"
    "LAE%=:\n"
    "s_lshl_b32 s60, s50, 5\n"
    "s_add_u32 s60, s60, s55\n"
    "s_lshl_b32 s39, s60, 11\n"
    "s_andn2_b32 s32, s39, 0x1fff\n"
    "s_add_u32 s32, s32, s39\n"
    "s_add_u32 s32, s32, 245366784\n"
    "s_lshr_b32 s38, s32, 8\n"
    "s_xor_b32 s38, s38, s56\n"
    "s_and_b32 s38, s38, 0x2000\n"
    "s_or_b32 s32, s32, s38\n"
    "s_lshl_b32 s39, s60, 10\n"
    "s_andn2_b32 s78, s39, 0x1fff\n"
    "s_add_u32 s78, s78, s39\n"
    "s_add_u32 s78, s78, 236978176\n"
    "s_lshr_b32 s38, s78, 8\n"
    "s_xor_b32 s38, s38, s56\n"
    "s_and_b32 s38, s38, 0x2000\n"
    "s_or_b32 s78, s78, s38\n"
    "s_lshl_b32 s39, s60, 10\n"
    "s_andn2_b32 s34, s39, 0x1fff\n"
    "s_add_u32 s34, s34, s39\n"
    "s_add_u32 s34, s34, 237240320\n"
    "s_lshr_b32 s38, s34, 8\n"
    "s_xor_b32 s38, s38, s56\n"
    "s_and_b32 s38, s38, 0x2000\n"
    "s_or_b32 s34, s34, s38\n"
    "s_cmp_lt_u32 s50, 60\n"
    "s_cselect_b32 s34, s34, 0x80000000\n"
    "s_mov_b32 s33, s60\n"
    "s_waitcnt vmcnt(5)\n"
    "v_lshlrev_b32 v64, 16, v116\n"
    "v_and_b32 v65, 0xffff0000, v116\n"
    "v_lshlrev_b32 v66, 16, v117\n"
    "v_and_b32 v67, 0xffff0000, v117\n"
    "v_lshlrev_b32 v68, 16, v118\n"
    "v_and_b32 v69, 0xffff0000, v118\n"
    "v_lshlrev_b32 v70, 16, v119\n"
    "v_and_b32 v71, 0xffff0000, v119\n"
    "s_waitcnt vmcnt(4)\n"
    "v_mfma_f32_16x16x32_bf16 v[64:67], v[24:27], v[100:103], v[64:67]\n"
    "v_mfma_f32_16x16x32_bf16 v[68:71], v[28:31], v[100:103], v[68:71]\n"
    "s_waitcnt vmcnt(3)\n"
    "v_mfma_f32_16x16x32_bf16 v[64:67], v[24:27], v[104:107], v[64:67]\n"
    "v_mfma_f32_16x16x32_bf16 v[68:71], v[28:31], v[104:107], v[68:71]\n"
    "s_waitcnt vmcnt(2)\n"
    "v_mfma_f32_16x16x32_bf16 v[64:67], v[24:27], v[108:111], v[64:67]\n"
    "v_mfma_f32_16x16x32_bf16 v[68:71], v[28:31], v[108:111], v[68:71]\n"
    "v_max3_u32 v48, v103, v107, v111\n"
    "s_waitcnt vmcnt(1)\n"
    "v_mfma_f32_16x16x32_bf16 v[64:67], v[24:27], v[112:115], v[64:67]\n"
    "v_mfma_f32_16x16x32_bf16 v[68:71], v[28:31], v[112:115], v[68:71]\n"
    "v_max_u32 v48, v48, v115\n"
    "v_cmp_eq_u32 vcc, -1, v48\n"
    "s_mov_b32 s47, s50\n"
    "s_mov_b32 s48, s51\n"
    "s_mov_b32 s50, -1\n"
    "s_cbranch_vccnz LEARLY%=\n"
    "s_branch LNXT%=\n"
    "LAB%=:\n"
    "s_waitcnt vmcnt(0)\n"
    "v_readfirstlane_b32 s38, v120\n"
    "v_readfirstlane_b32 s52, v121\n"
    "s_lshl_b32 s39, s73, 2\n"
    "s_cmp_eq_u32 s38, 31\n"
    "v_mov_b32 v120, s52\n"
    "s_cselect_b32 s39, s39, 0x80000000\n"
    "s_cselect_b32 s73, s73, -1\n"
    "s_mov_b64 exec, 0x38\n"
    "buffer_store_dword v120, v11, s[16:19], s39 offen sc1\n"
    "s_mov_b64 exec, -1\n"
    "s_lshl_b32 s60, s50, 5\n"
    "s_add_u32 s60, s60, s55\n"
    "s_lshl_b32 s39, s60, 11\n"
    "s_andn2_b32 s32, s39, 0x1fff\n"
    "s_add_u32 s32, s32, s39\n"
    "s_add_u32 s32, s32, 245366784\n"
    "s_lshr_b32 s38, s32, 8\n"
    "s_xor_b32 s38, s38, s56\n"
    "s_and_b32 s38, s38, 0x2000\n"
    "s_or_b32 s32, s32, s38\n"
    "s_lshl_b32 s39, s60, 10\n"
    "s_andn2_b32 s78, s39, 0x1fff\n"
    "s_add_u32 s78, s78, s39\n"
    "s_add_u32 s78, s78, 236978176\n"
    "s_lshr_b32 s38, s78, 8\n"
    "s_xor_b32 s38, s38, s56\n"
    "s_and_b32 s38, s38, 0x2000\n"
    "s_or_b32 s78, s78, s38\n"
    "s_lshl_b32 s39, s60, 10\n"
    "s_andn2_b32 s34, s39, 0x1fff\n"
    "s_add_u32 s34, s34, s39\n"
    "s_add_u32 s34, s34, 237240320\n"
    "s_lshr_b32 s38, s34, 8\n"
    "s_xor_b32 s38, s38, s56\n"
    "s_and_b32 s38, s38, 0x2000\n"
    "s_or_b32 s34, s34, s38\n"
    "s_cmp_lt_u32 s50, 60\n"
    "s_cselect_b32 s34, s34, 0x80000000\n"
    "s_mov_b32 s33, s60\n"
    "v_lshlrev_b32 v64, 16, v116\n"
    "v_and_b32 v65, 0xffff0000, v116\n"
    "v_lshlrev_b32 v66, 16, v117\n"
    "v_and_b32 v67, 0xffff0000, v117\n"
    "v_lshlrev_b32 v68, 16, v118\n"
    "v_and_b32 v69, 0xffff0000, v118\n"
    "v_lshlrev_b32 v70, 16, v119\n"
    "v_and_b32 v71, 0xffff0000, v119\n"
    "v_mfma_f32_16x16x32_bf16 v[64:67], v[24:27], v[100:103], v[64:67]\n"
    "s_nop 0\n"
    "v_mfma_f32_16x16x32_bf16 v[68:71], v[28:31], v[100:103], v[68:71]\n"
    "v_mfma_f32_16x16x32_bf16 v[64:67], v[24:27], v[104:107], v[64:67]\n"
    "v_mfma_f32_16x16x32_bf16 v[68:71], v[28:31], v[104:107], v[68:71]\n"
    "v_mfma_f32_16x16x32_bf16 v[64:67], v[24:27], v[108:111], v[64:67]\n"
    "v_mfma_f32_16x16x32_bf16 v[68:71], v[28:31], v[108:111], v[68:71]\n"
    "v_max3_u32 v48, v103, v107, v111\n"
    "v_mfma_f32_16x16x32_bf16 v[64:67], v[24:27], v[112:115], v[64:67]\n"
    "v_mfma_f32_16x16x32_bf16 v[68:71], v[28:31], v[112:115], v[68:71]\n"
    "v_max_u32 v48, v48, v115\n"
    "v_cmp_eq_u32 vcc, -1, v48\n"
    "s_mov_b32 s47, s50\n"
    "s_mov_b32 s48, s51\n"
    "s_mov_b32 s50, -1\n"
    "s_cbranch_vccnz LEARLY%=\n"
    "s_branch LAD%=\n"
    "LNONE%=:\n"
    "s_cmp_ge_i32 s47, 0\n"
    "s_cbranch_scc1 LJOB%=\n"
    "s_branch LIDLE%=\n"
    "LEARLY%=:\n"
    "s_mov_b32 s41, s47\n"
    "s_mov_b32 s53, 1\n"
    "s_mov_b32 s54, 0\n"
    "s_mov_b32 s47, -1\n"
    "s_and_b32 s38, s37, 3\n"
    "s_cmp_eq_u32 s38, 3\n"
    "s_cbranch_scc1 LIDLE%=\n"
    "s_sleep 2\n"
    "s_add_u32 s37, s37, 1\n"
    "s_cmp_lt_u32 s37, 0x80000\n"
    "s_cbranch_scc1 LPICK%=\n"
    "s_branch LFAIL%=\n"
    "LDEC%=:\n"
    "s_mov_b32 s70, s40\n"
    "s_cmp_eq_u32 s40, 64\n"
    "s_cbranch_scc1 LMLP%=\n"
    "s_add_u32 s40, s40, 1\n"
    "s_mov_b32 s37, 0\n"
    "s_lshr_b32 s38, s52, 16\n"
    "s_cmp_gt_i32 s38, s46\n"
    "s_cselect_b32 s70, s70, -1\n"
    "s_cselect_b32 s46, s38, s46\n"
    "s_addc_u32 s45, s45, 0\n"
    "s_cmp_lt_i32 s70, 0\n"
    "s_cbranch_scc1 LPICK%=\n"
    "s_mov_b32 s43, s70\n"
    "s_andn2_b32 s44, 0x7ff, s52\n"
    "LMLP%=:\n"
    "s_andn2_b32 s71, 0x7ff, s52\n"
    "s_lshr_b32 s38, s71, 5\n"
    "s_cmp_ge_u32 s38, s43\n"
    "s_cselect_b32 s63, s56, s57\n"
    "s_cselect_b32 s38, s70, 0\n"
    "s_cmp_eq_u32 s38, 64\n"
    "s_cselect_b32 s63, s57, s63\n"
    "s_cselect_b32 s62, 8388608, 0\n"
    "s_add_u32 s62, s62, 245366784\n"
    "s_lshl_b32 s39, s71, 11\n"
    "s_andn2_b32 s61, s39, 0x1fff\n"
    "s_add_u32 s61, s61, s39\n"
    "s_add_u32 s61, s61, s62\n"
    "s_lshr_b32 s38, s61, 8\n"
    "s_xor_b32 s38, s38, s63\n"
    "s_and_b32 s38, s38, 0x2000\n"
    "s_or_b32 s61, s61, s38\n"
    "buffer_load_dwordx4 v[116:119], v0, s[16:19], s61 offen offset:1024 sc1\n"
    "s_lshl_b32 s39, s70, 12\n"
    "s_andn2_b32 s61, s39, 0x1fff\n"
    "s_add_u32 s61, s61, s39\n"
    "s_add_u32 s61, s61, 263323648\n"
    "s_lshr_b32 s38, s61, 8\n"
    "s_xor_b32 s38, s38, s57\n"
    "s_and_b32 s38, s38, 0x2000\n"
    "s_or_b32 s61, s61, s38\n"
    "v_mov_b64 v[48:49], 0\n"
    "v_mov_b64 v[50:51], 0\n"
    "v_mov_b64 v[52:53], 0\n"
    "v_mov_b64 v[54:55], 0\n"
    "v_mov_b64 v[56:57], 0\n"
    "v_mov_b64 v[58:59], 0\n"
    "v_mov_b64 v[60:61], 0\n"
    "v_mov_b64 v[62:63], 0\n"
    "v_mov_b64 v[104:105], 0\n"
    "v_mov_b64 v[106:107], 0\n"
    "v_mov_b64 v[108:109], 0\n"
    "v_mov_b64 v[110:111], 0\n"
    "s_waitcnt vmcnt(0)\n"
    "ds_write_b128 v8, v[116:119]\n"
    "ds_read_b128 v[72:75], v7 offset:0\n"
    "ds_read_b128 v[76:79], v7 offset:128\n"
    "ds_read_b128 v[80:83], v7 offset:256\n"
    "ds_read_b128 v[84:87], v7 offset:384\n"
    "ds_read_b128 v[88:91], v7 offset:512\n"
    "ds_read_b128 v[92:95], v7 offset:640\n"
    "ds_read_b128 v[96:99], v7 offset:768\n"
    "ds_read_b128 v[100:103], v7 offset:896\n"
    "s_waitcnt lgkmcnt(7)\n"
    "v_smfmac_f32_16x16x64_bf16 v[48:51], v[72:75], a[0:7], v6\n"
    "v_smfmac_f32_16x16x64_bf16 v[52:55], v[72:75], a[8:15], v6\n"
    "v_smfmac_f32_16x16x64_bf16 v[56:59], v[72:75], a[16:23], v6\n"
    "v_smfmac_f32_16x16x64_bf16 v[60:63], v[72:75], a[24:31], v6\n"
    "v_smfmac_f32_16x16x64_bf16 v[104:107], v[72:75], v[128:135], v6\n"
    "v_smfmac_f32_16x16x64_bf16 v[108:111], v[72:75], v[136:143], v6\n"
    "s_waitcnt lgkmcnt(6)\n"
    "v_smfmac_f32_16x16x64_bf16 v[48:51], v[76:79], a[32:39], v6\n"
    "v_smfmac_f32_16x16x64_bf16 v[52:55], v[76:79], a[40:47], v6\n"
    "v_smfmac_f32_16x16x64_bf16 v[56:59], v[76:79], a[48:55], v6\n"
    "v_smfmac_f32_16x16x64_bf16 v[60:63], v[76:79], a[56:63], v6\n"
    "v_smfmac_f32_16x16x64_bf16 v[104:107], v[76:79], v[144:151], v6\n"
    "v_smfmac_f32_16x16x64_bf16 v[108:111], v[76:79], v[152:159], v6\n"
    "s_waitcnt lgkmcnt(5)\n"
    "v_smfmac_f32_16x16x64_bf16 v[48:51], v[80:83], a[64:71], v6\n"
    "v_smfmac_f32_16x16x64_bf16 v[52:55], v[80:83], a[72:79], v6\n"
    "v_smfmac_f32_16x16x64_bf16 v[56:59], v[80:83], a[80:87], v6\n"
    "v_smfmac_f32_16x16x64_bf16 v[60:63], v[80:83], a[88:95], v6\n"
    "v_smfmac_f32_16x16x64_bf16 v[104:107], v[80:83], v[160:167], v6\n"
    "v_smfmac_f32_16x16x64_bf16 v[108:111], v[80:83], v[168:175], v6\n"
    "s_waitcnt lgkmcnt(4)\n"
    "v_smfmac_f32_16x16x64_bf16 v[48:51], v[84:87], a[96:103], v6\n"
    "v_smfmac_f32_16x16x64_bf16 v[52:55], v[84:87], a[104:111], v6\n"
    "v_smfmac_f32_16x16x64_bf16 v[56:59], v[84:87], a[112:119], v6\n"
    "v_smfmac_f32_16x16x64_bf16 v[60:63], v[84:87], a[120:127], v6\n"
    "v_smfmac_f32_16x16x64_bf16 v[104:107], v[84:87], v[176:183], v6\n"
    "v_smfmac_f32_16x16x64_bf16 v[108:111], v[84:87], v[184:191], v6\n"
    "s_waitcnt lgkmcnt(3)\n"
    "v_smfmac_f32_16x16x64_bf16 v[48:51], v[88:91], a[128:135], v6\n"
    "v_smfmac_f32_16x16x64_bf16 v[52:55], v[88:91], a[136:143], v6\n"
    "v_smfmac_f32_16x16x64_bf16 v[56:59], v[88:91], a[144:151], v6\n"
    "v_smfmac_f32_16x16x64_bf16 v[60:63], v[88:91], a[152:159], v6\n"
    "v_smfmac_f32_16x16x64_bf16 v[104:107], v[88:91], v[192:199], v6\n"
    "v_smfmac_f32_16x16x64_bf16 v[108:111], v[88:91], v[200:207], v6\n"
    "s_waitcnt lgkmcnt(2)\n"
    "v_smfmac_f32_16x16x64_bf16 v[48:51], v[92:95], a[160:167], v6\n"
    "v_smfmac_f32_16x16x64_bf16 v[52:55], v[92:95], a[168:175], v6\n"
    "v_smfmac_f32_16x16x64_bf16 v[56:59], v[92:95], a[176:183], v6\n"
    "v_smfmac_f32_16x16x64_bf16 v[60:63], v[92:95], a[184:191], v6\n"
    "v_smfmac_f32_16x16x64_bf16 v[104:107], v[92:95], v[208:215], v6\n"
    "v_smfmac_f32_16x16x64_bf16 v[108:111], v[92:95], v[216:223], v6\n"
    "s_waitcnt lgkmcnt(1)\n"
    "v_smfmac_f32_16x16x64_bf16 v[48:51], v[96:99], a[192:199], v6\n"
    "v_smfmac_f32_16x16x64_bf16 v[52:55], v[96:99], a[200:207], v6\n"
    "v_smfmac_f32_16x16x64_bf16 v[56:59], v[96:99], a[208:215], v6\n"
    "v_smfmac_f32_16x16x64_bf16 v[60:63], v[96:99], a[216:223], v6\n"
    "v_smfmac_f32_16x16x64_bf16 v[104:107], v[96:99], v[224:231], v6\n"
    "v_smfmac_f32_16x16x64_bf16 v[108:111], v[96:99], v[232:239], v6\n"
    "s_waitcnt lgkmcnt(0)\n"
    "v_smfmac_f32_16x16x64_bf16 v[48:51], v[100:103], a[224:231], v6\n"
    "v_smfmac_f32_16x16x64_bf16 v[52:55], v[100:103], a[232:239], v6\n"
    "v_smfmac_f32_16x16x64_bf16 v[56:59], v[100:103], a[240:247], v6\n"
    "v_smfmac_f32_16x16x64_bf16 v[60:63], v[100:103], a[248:255], v6\n"
    "v_smfmac_f32_16x16x64_bf16 v[104:107], v[100:103], v[240:247], v6\n"
    "v_smfmac_f32_16x16x64_bf16 v[108:111], v[100:103], v[248:255], v6\n"
    "v_add_f32 v48, v48, v50\n"
    "v_add_f32 v52, v52, v54\n"
    "v_add_f32 v56, v56, v58\n"
    "v_add_f32 v60, v60, v62\n"
    "v_cndmask_b32 v48, v48, v56, s[74:75]\n"
    "v_cndmask_b32 v52, v52, v60, s[74:75]\n"
    "s_mov_b32 vcc_lo, -1\n"
    "s_mov_b32 vcc_hi, 0\n"
    "v_add_f32 v104, v104, v106\n"
    "v_add_f32 v108, v108, v110\n"
    "v_cndmask_b32 v104, v104, v48, vcc\n"
    "v_cndmask_b32 v108, v108, v52, vcc\n"
    "v_cvt_pk_bf16_f32 v108, v104, v108\n"
    "v_lshlrev_b32 v104, 16, v108\n"
    "v_mul_f32 v105, 0xbfb8aa3b, v104\n"
    "v_exp_f32 v105, v105\n"
    "v_and_b32 v108, 0xffff0000, v108\n"
    "v_add_f32 v105, 1.0, v105\n"
    "v_rcp_f32 v105, v105\n"
    "s_nop 0\n"
    "v_mul_f32 v104, v104, v105\n"
    "v_cvt_pk_bf16_f32 v104, v104, v104\n"
    "v_lshlrev_b32 v104, 16, v104\n"
    "v_mul_f32 v104, v104, v108\n"
    "s_nop 1\n"
    "v_mov_b32_dpp v105, v104 quad_perm:[1,1,3,3] row_mask:0xf bank_mask:0xf\n"
    "v_cvt_pk_bf16_f32 v104, v104, v105\n"
    "v_min_u32 v104, -2, v104\n"
    "buffer_store_dword v104, v9, s[16:19], s61 offen sc1\n"
    "s_cmp_lg_u32 s70, 64\n"
    "s_cbranch_scc1 LPOLL%=\n"
    "s_branch LEND%=\n"
    "LFAIL%=:\n"
    "buffer_store_dword v6, off, s[16:19], s59 sc1\n"
    "LEND%=:\n"
    "s_endpgm\n"
    :
    : "{v0}"(hv0), "{v1}"(hv1), "{v2}"(hv2), "{v3}"(hv3), "{v4}"(hv4), "{v5}"(hv5), "{v6}"(hv6), "{v7}"(hv7), "{v8}"(hv8), "{v9}"(hv9), "{v10}"(hv10), "{v11}"(hv11), "{v12}"(hv12), "{v13}"(hv13), "{v14}"(hv14), "{v15}"(hv15), "{v16}"(hv16), "{v17}"(hv17), "{v18}"(hv18), "{v19}"(hv19), "{v20}"(hv20), "{v21}"(hv21)
    : "v255", "a255", "s101", "vcc", "scc", "memory");
  } else {
  asm volatile(
    "s_nop 1\n"
    "v_readlane_b32 s36, v15, 0\n"
    "v_readlane_b32 s55, v15, 1\n"
    "v_readlane_b32 s58, v15, 2\n"
    "v_readlane_b32 s59, v15, 3\n"
    "v_readlane_b32 s16, v15, 4\n"
    "v_readlane_b32 s17, v15, 5\n"
    "v_readlane_b32 s18, v15, 6\n"
    "v_readlane_b32 s20, v15, 7\n"
    "v_readlane_b32 s21, v15, 8\n"
    "v_readlane_b32 s24, v15, 9\n"
    "v_readlane_b32 s25, v15, 10\n"
    "v_readlane_b32 s90, v15, 11\n"
    "v_readlane_b32 s91, v15, 12\n"
    "v_readlane_b32 s92, v15, 13\n"
    "v_readlane_b32 s93, v15, 14\n"
    "v_readlane_b32 s94, v15, 15\n"
    "v_readlane_b32 s95, v15, 16\n"
    "v_readlane_b32 s96, v15, 17\n"
    "v_readlane_b32 s97, v15, 18\n"
    "v_readlane_b32 s98, v15, 19\n"
    "v_readlane_b32 s99, v15, 20\n"
    "v_readlane_b32 s72, v15, 21\n"
    "s_mov_b32 s19, 0x20000\n"
    "s_mov_b32 s23, 0x20000\n"
    "s_mov_b32 s27, 0x20000\n"
    "s_mov_b32 s31, 0x20000\n"
    "s_mov_b32 s22, 0x200000\n"
    "s_mov_b32 s26, 0x200000\n"
    "s_mov_b32 s30, 1835008\n"
    "s_nop 3\n"
    "global_load_dwordx4 v[116:119], v0, s[98:99]\n"
    "s_lshl_b32 s62, s36, 11\n"
    "s_mov_b32 s28, s90\n"
    "s_mov_b32 s29, s91\n"
    "s_add_u32 m0, s62, 0\n"
    "s_mov_b32 s38, 0\n"
    "buffer_load_dwordx4 v16, s[28:31], s38 offen lds\n"
    "s_add_u32 m0, s62, 1024\n"
    "s_mov_b32 s38, 28672\n"
    "buffer_load_dwordx4 v16, s[28:31], s38 offen lds\n"
    "s_add_u32 m0, s62, 8192\n"
    "s_mov_b32 s38, 229376\n"
    "buffer_load_dwordx4 v16, s[28:31], s38 offen lds\n"
    "s_add_u32 m0, s62, 9216\n"
    "s_mov_b32 s38, 258048\n"
    "buffer_load_dwordx4 v16, s[28:31], s38 offen lds\n"
    "s_mov_b32 s28, s90\n"
    "s_mov_b32 s29, s91\n"
    "s_add_u32 m0, s62, 16384\n"
    "s_mov_b32 s38, 458752\n"
    "buffer_load_dwordx4 v16, s[28:31], s38 offen lds\n"
    "s_add_u32 m0, s62, 17408\n"
    "s_mov_b32 s38, 487424\n"
    "buffer_load_dwordx4 v16, s[28:31], s38 offen lds\n"
    "s_add_u32 m0, s62, 24576\n"
    "s_mov_b32 s38, 688128\n"
    "buffer_load_dwordx4 v16, s[28:31], s38 offen lds\n"
    "s_add_u32 m0, s62, 25600\n"
    "s_mov_b32 s38, 716800\n"
    "buffer_load_dwordx4 v16, s[28:31], s38 offen lds\n"
    "s_mov_b32 s28, s90\n"
    "s_mov_b32 s29, s91\n"
    "s_add_u32 m0, s62, 32768\n"
    "s_mov_b32 s38, 917504\n"
    "buffer_load_dwordx4 v16, s[28:31], s38 offen lds\n"
    "s_add_u32 m0, s62, 33792\n"
    "s_mov_b32 s38, 946176\n"
    "buffer_load_dwordx4 v16, s[28:31], s38 offen lds\n"
    "s_add_u32 m0, s62, 40960\n"
    "s_mov_b32 s38, 1146880\n"
    "buffer_load_dwordx4 v16, s[28:31], s38 offen lds\n"
    "s_add_u32 m0, s62, 41984\n"
    "s_mov_b32 s38, 1175552\n"
    "buffer_load_dwordx4 v16, s[28:31], s38 offen lds\n"
    "s_mov_b32 s28, s90\n"
    "s_mov_b32 s29, s91\n"
    "s_add_u32 m0, s62, 49152\n"
    "s_mov_b32 s38, 1376256\n"
    "buffer_load_dwordx4 v16, s[28:31], s38 offen lds\n"
    "s_add_u32 m0, s62, 50176\n"
    "s_mov_b32 s38, 1404928\n"
    "buffer_load_dwordx4 v16, s[28:31], s38 offen lds\n"
    "s_add_u32 m0, s62, 57344\n"
    "s_mov_b32 s38, 1605632\n"
    "buffer_load_dwordx4 v16, s[28:31], s38 offen lds\n"
    "s_add_u32 m0, s62, 58368\n"
    "s_mov_b32 s38, 1634304\n"
    "buffer_load_dwordx4 v16, s[28:31], s38 offen lds\n"
    "s_mov_b32 s28, s92\n"
    "s_mov_b32 s29, s93\n"
    "s_add_u32 m0, s62, 65536\n"
    "s_mov_b32 s38, 0\n"
    "buffer_load_dwordx4 v16, s[28:31], s38 offen lds\n"
    "s_add_u32 m0, s62, 66560\n"
    "s_mov_b32 s38, 28672\n"
    "buffer_load_dwordx4 v16, s[28:31], s38 offen lds\n"
    "s_add_u32 m0, s62, 73728\n"
    "s_mov_b32 s38, 229376\n"
    "buffer_load_dwordx4 v16, s[28:31], s38 offen lds\n"
    "s_add_u32 m0, s62, 74752\n"
    "s_mov_b32 s38, 258048\n"
    "buffer_load_dwordx4 v16, s[28:31], s38 offen lds\n"
    "s_mov_b32 s28, s92\n"
    "s_mov_b32 s29, s93\n"
    "s_add_u32 m0, s62, 81920\n"
    "s_mov_b32 s38, 458752\n"
    "buffer_load_dwordx4 v16, s[28:31], s38 offen lds\n"
    "s_add_u32 m0, s62, 82944\n"
    "s_mov_b32 s38, 487424\n"
    "buffer_load_dwordx4 v16, s[28:31], s38 offen lds\n"
    "s_add_u32 m0, s62, 90112\n"
    "s_mov_b32 s38, 688128\n"
    "buffer_load_dwordx4 v16, s[28:31], s38 offen lds\n"
    "s_add_u32 m0, s62, 91136\n"
    "s_mov_b32 s38, 716800\n"
    "buffer_load_dwordx4 v16, s[28:31], s38 offen lds\n"
    "s_mov_b32 s28, s92\n"
    "s_mov_b32 s29, s93\n"
    "s_add_u32 m0, s62, 98304\n"
    "s_mov_b32 s38, 917504\n"
    "buffer_load_dwordx4 v16, s[28:31], s38 offen lds\n"
    "s_add_u32 m0, s62, 99328\n"
    "s_mov_b32 s38, 946176\n"
    "buffer_load_dwordx4 v16, s[28:31], s38 offen lds\n"
    "s_add_u32 m0, s62, 106496\n"
    "s_mov_b32 s38, 1146880\n"
    "buffer_load_dwordx4 v16, s[28:31], s38 offen lds\n"
    "s_add_u32 m0, s62, 107520\n"
    "s_mov_b32 s38, 1175552\n"
    "buffer_load_dwordx4 v16, s[28:31], s38 offen lds\n"
    "s_waitcnt vmcnt(24)\n"
    "s_barrier\n"
    "s_mov_b32 s28, s92\n"
    "s_mov_b32 s29, s93\n"
    "s_add_u32 m0, s62, 114688\n"
    "s_mov_b32 s38, 1376256\n"
    "buffer_load_dwordx4 v16, s[28:31], s38 offen lds\n"
    "s_add_u32 m0, s62, 115712\n"
    "s_mov_b32 s38, 1404928\n"
    "buffer_load_dwordx4 v16, s[28:31], s38 offen lds\n"
    "s_add_u32 m0, s62, 122880\n"
    "s_mov_b32 s38, 1605632\n"
    "buffer_load_dwordx4 v16, s[28:31], s38 offen lds\n"
    "s_add_u32 m0, s62, 123904\n"
    "s_mov_b32 s38, 1634304\n"
    "buffer_load_dwordx4 v16, s[28:31], s38 offen lds\n"
    "v_add_u32 v48, 0, v14\n"
    "ds_read_b64_tr_b16 v[128:129], v48 offset:0\n"
    "ds_read_b64_tr_b16 v[130:131], v48 offset:256\n"
    "ds_read_b64_tr_b16 v[132:133], v48 offset:4096\n"
    "ds_read_b64_tr_b16 v[134:135], v48 offset:4352\n"
    "ds_read_b64_tr_b16 v[144:145], v48 offset:8192\n"
    "ds_read_b64_tr_b16 v[146:147], v48 offset:8448\n"
    "ds_read_b64_tr_b16 v[148:149], v48 offset:12288\n"
    "ds_read_b64_tr_b16 v[150:151], v48 offset:12544\n"
    "s_waitcnt lgkmcnt(0)\n"
    "s_waitcnt vmcnt(24)\n"
    "s_barrier\n"
    "s_mov_b32 s28, s94\n"
    "s_mov_b32 s29, s95\n"
    "s_add_u32 m0, s62, 0\n"
    "s_mov_b32 s38, 0\n"
    "buffer_load_dwordx4 v17, s[28:31], s38 offen lds\n"
    "s_add_u32 m0, s62, 1024\n"
    "s_mov_b32 s38, 8192\n"
    "buffer_load_dwordx4 v17, s[28:31], s38 offen lds\n"
    "s_add_u32 m0, s62, 8192\n"
    "s_mov_b32 s38, 65536\n"
    "buffer_load_dwordx4 v17, s[28:31], s38 offen lds\n"
    "s_add_u32 m0, s62, 9216\n"
    "s_mov_b32 s38, 73728\n"
    "buffer_load_dwordx4 v17, s[28:31], s38 offen lds\n"
    "v_add_u32 v48, 16384, v14\n"
    "ds_read_b64_tr_b16 v[160:161], v48 offset:0\n"
    "ds_read_b64_tr_b16 v[162:163], v48 offset:256\n"
    "ds_read_b64_tr_b16 v[164:165], v48 offset:4096\n"
    "ds_read_b64_tr_b16 v[166:167], v48 offset:4352\n"
    "ds_read_b64_tr_b16 v[176:177], v48 offset:8192\n"
    "ds_read_b64_tr_b16 v[178:179], v48 offset:8448\n"
    "ds_read_b64_tr_b16 v[180:181], v48 offset:12288\n"
    "ds_read_b64_tr_b16 v[182:183], v48 offset:12544\n"
    "s_waitcnt lgkmcnt(0)\n"
    "s_waitcnt vmcnt(24)\n"
    "s_barrier\n"
    "s_mov_b32 s28, s94\n"
    "s_mov_b32 s29, s95\n"
    "s_add_u32 m0, s62, 16384\n"
    "s_mov_b32 s38, 131072\n"
    "buffer_load_dwordx4 v17, s[28:31], s38 offen lds\n"
    "s_add_u32 m0, s62, 17408\n"
    "s_mov_b32 s38, 139264\n"
    "buffer_load_dwordx4 v17, s[28:31], s38 offen lds\n"
    "s_add_u32 m0, s62, 24576\n"
    "s_mov_b32 s38, 196608\n"
    "buffer_load_dwordx4 v17, s[28:31], s38 offen lds\n"
    "s_add_u32 m0, s62, 25600\n"
    "s_mov_b32 s38, 204800\n"
    "buffer_load_dwordx4 v17, s[28:31], s38 offen lds\n"
    "v_add_u32 v48, 32768, v14\n"
    "ds_read_b64_tr_b16 v[192:193], v48 offset:0\n"
    "ds_read_b64_tr_b16 v[194:195], v48 offset:256\n"
    "ds_read_b64_tr_b16 v[196:197], v48 offset:4096\n"
    "ds_read_b64_tr_b16 v[198:199], v48 offset:4352\n"
    "ds_read_b64_tr_b16 v[208:209], v48 offset:8192\n"
    "ds_read_b64_tr_b16 v[210:211], v48 offset:8448\n"
    "ds_read_b64_tr_b16 v[212:213], v48 offset:12288\n"
    "ds_read_b64_tr_b16 v[214:215], v48 offset:12544\n"
    "s_waitcnt lgkmcnt(0)\n"
    "s_waitcnt vmcnt(24)\n"
    "s_barrier\n"
    "s_mov_b32 s28, s94\n"
    "s_mov_b32 s29, s95\n"
    "s_add_u32 m0, s62, 32768\n"
    "s_mov_b32 s38, 262144\n"
    "buffer_load_dwordx4 v17, s[28:31], s38 offen lds\n"
    "s_add_u32 m0, s62, 33792\n"
    "s_mov_b32 s38, 270336\n"
    "buffer_load_dwordx4 v17, s[28:31], s38 offen lds\n"
    "s_add_u32 m0, s62, 40960\n"
    "s_mov_b32 s38, 327680\n"
    "buffer_load_dwordx4 v17, s[28:31], s38 offen lds\n"
    "s_add_u32 m0, s62, 41984\n"
    "s_mov_b32 s38, 335872\n"
    "buffer_load_dwordx4 v17, s[28:31], s38 offen lds\n"
    "v_add_u32 v48, 49152, v14\n"
    "ds_read_b64_tr_b16 v[224:225], v48 offset:0\n"
    "ds_read_b64_tr_b16 v[226:227], v48 offset:256\n"
    "ds_read_b64_tr_b16 v[228:229], v48 offset:4096\n"
    "ds_read_b64_tr_b16 v[230:231], v48 offset:4352\n"
    "ds_read_b64_tr_b16 v[240:241], v48 offset:8192\n"
    "ds_read_b64_tr_b16 v[242:243], v48 offset:8448\n"
    "ds_read_b64_tr_b16 v[244:245], v48 offset:12288\n"
    "ds_read_b64_tr_b16 v[246:247], v48 offset:12544\n"
    "s_waitcnt lgkmcnt(0)\n"
    "s_waitcnt vmcnt(24)\n"
    "s_barrier\n"
    "s_mov_b32 s28, s94\n"
    "s_mov_b32 s29, s95\n"
    "s_add_u32 m0, s62, 49152\n"
    "s_mov_b32 s38, 393216\n"
    "buffer_load_dwordx4 v17, s[28:31], s38 offen lds\n"
    "s_add_u32 m0, s62, 50176\n"
    "s_mov_b32 s38, 401408\n"
    "buffer_load_dwordx4 v17, s[28:31], s38 offen lds\n"
    "s_add_u32 m0, s62, 57344\n"
    "s_mov_b32 s38, 458752\n"
    "buffer_load_dwordx4 v17, s[28:31], s38 offen lds\n"
    "s_add_u32 m0, s62, 58368\n"
    "s_mov_b32 s38, 466944\n"
    "buffer_load_dwordx4 v17, s[28:31], s38 offen lds\n"
    "v_add_u32 v48, 65536, v14\n"
    "ds_read_b64_tr_b16 v[136:137], v48 offset:0\n"
    "ds_read_b64_tr_b16 v[138:139], v48 offset:256\n"
    "ds_read_b64_tr_b16 v[140:141], v48 offset:4096\n"
    "ds_read_b64_tr_b16 v[142:143], v48 offset:4352\n"
    "ds_read_b64_tr_b16 v[152:153], v48 offset:8192\n"
    "ds_read_b64_tr_b16 v[154:155], v48 offset:8448\n"
    "ds_read_b64_tr_b16 v[156:157], v48 offset:12288\n"
    "ds_read_b64_tr_b16 v[158:159], v48 offset:12544\n"
    "s_waitcnt lgkmcnt(0)\n"
    "s_waitcnt vmcnt(24)\n"
    "s_barrier\n"
    "s_mov_b32 s28, s94\n"
    "s_mov_b32 s29, s95\n"
    "s_add_u32 m0, s62, 65536\n"
    "s_mov_b32 s38, 524288\n"
    "buffer_load_dwordx4 v17, s[28:31], s38 offen lds\n"
    "s_add_u32 m0, s62, 66560\n"
    "s_mov_b32 s38, 532480\n"
    "buffer_load_dwordx4 v17, s[28:31], s38 offen lds\n"
    "s_add_u32 m0, s62, 73728\n"
    "s_mov_b32 s38, 589824\n"
    "buffer_load_dwordx4 v17, s[28:31], s38 offen lds\n"
    "s_add_u32 m0, s62, 74752\n"
    "s_mov_b32 s38, 598016\n"
    "buffer_load_dwordx4 v17, s[28:31], s38 offen lds\n"
    "v_add_u32 v48, 81920, v14\n"
    "ds_read_b64_tr_b16 v[168:169], v48 offset:0\n"
    "ds_read_b64_tr_b16 v[170:171], v48 offset:256\n"
    "ds_read_b64_tr_b16 v[172:173], v48 offset:4096\n"
    "ds_read_b64_tr_b16 v[174:175], v48 offset:4352\n"
    "ds_read_b64_tr_b16 v[184:185], v48 offset:8192\n"
    "ds_read_b64_tr_b16 v[186:187], v48 offset:8448\n"
    "ds_read_b64_tr_b16 v[188:189], v48 offset:12288\n"
    "ds_read_b64_tr_b16 v[190:191], v48 offset:12544\n"
    "s_waitcnt lgkmcnt(0)\n"
    "s_waitcnt vmcnt(24)\n"
    "s_barrier\n"
    "s_mov_b32 s28, s94\n"
    "s_mov_b32 s29, s95\n"
    "s_add_u32 m0, s62, 81920\n"
    "s_mov_b32 s38, 655360\n"
    "buffer_load_dwordx4 v17, s[28:31], s38 offen lds\n"
    "s_add_u32 m0, s62, 82944\n"
    "s_mov_b32 s38, 663552\n"
    "buffer_load_dwordx4 v17, s[28:31], s38 offen lds\n"
    "s_add_u32 m0, s62, 90112\n"
    "s_mov_b32 s38, 720896\n"
    "buffer_load_dwordx4 v17, s[28:31], s38 offen lds\n"
    "s_add_u32 m0, s62, 91136\n"
    "s_mov_b32 s38, 729088\n"
    "buffer_load_dwordx4 v17, s[28:31], s38 offen lds\n"
    "v_add_u32 v48, 98304, v14\n"
    "ds_read_b64_tr_b16 v[200:201], v48 offset:0\n"
    "ds_read_b64_tr_b16 v[202:203], v48 offset:256\n"
    "ds_read_b64_tr_b16 v[204:205], v48 offset:4096\n"
    "ds_read_b64_tr_b16 v[206:207], v48 offset:4352\n"
    "ds_read_b64_tr_b16 v[216:217], v48 offset:8192\n"
    "ds_read_b64_tr_b16 v[218:219], v48 offset:8448\n"
    "ds_read_b64_tr_b16 v[220:221], v48 offset:12288\n"
    "ds_read_b64_tr_b16 v[222:223], v48 offset:12544\n"
    "s_waitcnt lgkmcnt(0)\n"
    "s_waitcnt vmcnt(24)\n"
    "s_barrier\n"
    "s_mov_b32 s28, s94\n"
    "s_mov_b32 s29, s95\n"
    "s_add_u32 m0, s62, 98304\n"
    "s_mov_b32 s38, 786432\n"
    "buffer_load_dwordx4 v17, s[28:31], s38 offen lds\n"
    "s_add_u32 m0, s62, 99328\n"
    "s_mov_b32 s38, 794624\n"
    "buffer_load_dwordx4 v17, s[28:31], s38 offen lds\n"
    "s_add_u32 m0, s62, 106496\n"
    "s_mov_b32 s38, 851968\n"
    "buffer_load_dwordx4 v17, s[28:31], s38 offen lds\n"
    "s_add_u32 m0, s62, 107520\n"
    "s_mov_b32 s38, 860160\n"
    "buffer_load_dwordx4 v17, s[28:31], s38 offen lds\n"
    "v_add_u32 v48, 114688, v14\n"
    "ds_read_b64_tr_b16 v[232:233], v48 offset:0\n"
    "ds_read_b64_tr_b16 v[234:235], v48 offset:256\n"
    "ds_read_b64_tr_b16 v[236:237], v48 offset:4096\n"
    "ds_read_b64_tr_b16 v[238:239], v48 offset:4352\n"
    "ds_read_b64_tr_b16 v[248:249], v48 offset:8192\n"
    "ds_read_b64_tr_b16 v[250:251], v48 offset:8448\n"
    "ds_read_b64_tr_b16 v[252:253], v48 offset:12288\n"
    "ds_read_b64_tr_b16 v[254:255], v48 offset:12544\n"
    "s_waitcnt lgkmcnt(0)\n"
    "s_waitcnt vmcnt(24)\n"
    "s_barrier\n"
    "s_mov_b32 s28, s94\n"
    "s_mov_b32 s29, s95\n"
    "s_add_u32 m0, s62, 114688\n"
    "s_mov_b32 s38, 917504\n"
    "buffer_load_dwordx4 v17, s[28:31], s38 offen lds\n"
    "s_add_u32 m0, s62, 115712\n"
    "s_mov_b32 s38, 925696\n"
    "buffer_load_dwordx4 v17, s[28:31], s38 offen lds\n"
    "s_add_u32 m0, s62, 122880\n"
    "s_mov_b32 s38, 983040\n"
    "buffer_load_dwordx4 v17, s[28:31], s38 offen lds\n"
    "s_add_u32 m0, s62, 123904\n"
    "s_mov_b32 s38, 991232\n"
    "buffer_load_dwordx4 v17, s[28:31], s38 offen lds\n"
    "v_add_u32 v48, 0, v14\n"
    "ds_read_b64_tr_b16 a[0:1], v48 offset:0\n"
    "ds_read_b64_tr_b16 a[2:3], v48 offset:256\n"
    "ds_read_b64_tr_b16 a[4:5], v48 offset:4096\n"
    "ds_read_b64_tr_b16 a[6:7], v48 offset:4352\n"
    "ds_read_b64_tr_b16 a[8:9], v48 offset:8192\n"
    "ds_read_b64_tr_b16 a[10:11], v48 offset:8448\n"
    "ds_read_b64_tr_b16 a[12:13], v48 offset:12288\n"
    "ds_read_b64_tr_b16 a[14:15], v48 offset:12544\n"
    "s_waitcnt lgkmcnt(0)\n"
    "s_waitcnt vmcnt(24)\n"
    "s_barrier\n"
    "s_mov_b32 s28, s94\n"
    "s_mov_b32 s29, s95\n"
    "s_add_u32 m0, s62, 0\n"
    "s_mov_b32 s38, 1048576\n"
    "buffer_load_dwordx4 v17, s[28:31], s38 offen lds\n"
    "s_add_u32 m0, s62, 1024\n"
    "s_mov_b32 s38, 1056768\n"
    "buffer_load_dwordx4 v17, s[28:31], s38 offen lds\n"
    "s_add_u32 m0, s62, 8192\n"
    "s_mov_b32 s38, 1114112\n"
    "buffer_load_dwordx4 v17, s[28:31], s38 offen lds\n"
    "s_add_u32 m0, s62, 9216\n"
    "s_mov_b32 s38, 1122304\n"
    "buffer_load_dwordx4 v17, s[28:31], s38 offen lds\n"
    "v_add_u32 v48, 16384, v14\n"
    "ds_read_b64_tr_b16 a[16:17], v48 offset:0\n"
    "ds_read_b64_tr_b16 a[18:19], v48 offset:256\n"
    "ds_read_b64_tr_b16 a[20:21], v48 offset:4096\n"
    "ds_read_b64_tr_b16 a[22:23], v48 offset:4352\n"
    "ds_read_b64_tr_b16 a[24:25], v48 offset:8192\n"
    "ds_read_b64_tr_b16 a[26:27], v48 offset:8448\n"
    "ds_read_b64_tr_b16 a[28:29], v48 offset:12288\n"
    "ds_read_b64_tr_b16 a[30:31], v48 offset:12544\n"
    "s_waitcnt lgkmcnt(0)\n"
    "s_waitcnt vmcnt(24)\n"
    "s_barrier\n"
    "s_mov_b32 s28, s94\n"
    "s_mov_b32 s29, s95\n"
    "s_add_u32 m0, s62, 16384\n"
    "s_mov_b32 s38, 1179648\n"
    "buffer_load_dwordx4 v17, s[28:31], s38 offen lds\n"
    "s_add_u32 m0, s62, 17408\n"
    "s_mov_b32 s38, 1187840\n"
    "buffer_load_dwordx4 v17, s[28:31], s38 offen lds\n"
    "s_add_u32 m0, s62, 24576\n"
    "s_mov_b32 s38, 1245184\n"
    "buffer_load_dwordx4 v17, s[28:31], s38 offen lds\n"
    "s_add_u32 m0, s62, 25600\n"
    "s_mov_b32 s38, 1253376\n"
    "buffer_load_dwordx4 v17, s[28:31], s38 offen lds\n"
    "v_add_u32 v48, 32768, v14\n"
    "ds_read_b64_tr_b16 a[32:33], v48 offset:0\n"
    "ds_read_b64_tr_b16 a[34:35], v48 offset:256\n"
    "ds_read_b64_tr_b16 a[36:37], v48 offset:4096\n"
    "ds_read_b64_tr_b16 a[38:39], v48 offset:4352\n"
    "ds_read_b64_tr_b16 a[40:41], v48 offset:8192\n"
    "ds_read_b64_tr_b16 a[42:43], v48 offset:8448\n"
    "ds_read_b64_tr_b16 a[44:45], v48 offset:12288\n"
    "ds_read_b64_tr_b16 a[46:47], v48 offset:12544\n"
    "s_waitcnt lgkmcnt(0)\n"
    "s_waitcnt vmcnt(24)\n"
    "s_barrier\n"
    "s_mov_b32 s28, s94\n"
    "s_mov_b32 s29, s95\n"
    "s_add_u32 m0, s62, 32768\n"
    "s_mov_b32 s38, 1310720\n"
    "buffer_load_dwordx4 v17, s[28:31], s38 offen lds\n"
    "s_add_u32 m0, s62, 33792\n"
    "s_mov_b32 s38, 1318912\n"
    "buffer_load_dwordx4 v17, s[28:31], s38 offen lds\n"
    "s_add_u32 m0, s62, 40960\n"
    "s_mov_b32 s38, 1376256\n"
    "buffer_load_dwordx4 v17, s[28:31], s38 offen lds\n"
    "s_add_u32 m0, s62, 41984\n"
    "s_mov_b32 s38, 1384448\n"
    "buffer_load_dwordx4 v17, s[28:31], s38 offen lds\n"
    "v_add_u32 v48, 49152, v14\n"
    "ds_read_b64_tr_b16 a[48:49], v48 offset:0\n"
    "ds_read_b64_tr_b16 a[50:51], v48 offset:256\n"
    "ds_read_b64_tr_b16 a[52:53], v48 offset:4096\n"
    "ds_read_b64_tr_b16 a[54:55], v48 offset:4352\n"
    "ds_read_b64_tr_b16 a[56:57], v48 offset:8192\n"
    "ds_read_b64_tr_b16 a[58:59], v48 offset:8448\n"
    "ds_read_b64_tr_b16 a[60:61], v48 offset:12288\n"
    "ds_read_b64_tr_b16 a[62:63], v48 offset:12544\n"
    "s_waitcnt lgkmcnt(0)\n"
    "s_waitcnt vmcnt(24)\n"
    "s_barrier\n"
    "s_mov_b32 s28, s94\n"
    "s_mov_b32 s29, s95\n"
    "s_add_u32 m0, s62, 49152\n"
    "s_mov_b32 s38, 1441792\n"
    "buffer_load_dwordx4 v17, s[28:31], s38 offen lds\n"
    "s_add_u32 m0, s62, 50176\n"
    "s_mov_b32 s38, 1449984\n"
    "buffer_load_dwordx4 v17, s[28:31], s38 offen lds\n"
    "s_add_u32 m0, s62, 57344\n"
    "s_mov_b32 s38, 1507328\n"
    "buffer_load_dwordx4 v17, s[28:31], s38 offen lds\n"
    "s_add_u32 m0, s62, 58368\n"
    "s_mov_b32 s38, 1515520\n"
    "buffer_load_dwordx4 v17, s[28:31], s38 offen lds\n"
    "v_add_u32 v48, 65536, v14\n"
    "ds_read_b64_tr_b16 a[64:65], v48 offset:0\n"
    "ds_read_b64_tr_b16 a[66:67], v48 offset:256\n"
    "ds_read_b64_tr_b16 a[68:69], v48 offset:4096\n"
    "ds_read_b64_tr_b16 a[70:71], v48 offset:4352\n"
    "ds_read_b64_tr_b16 a[72:73], v48 offset:8192\n"
    "ds_read_b64_tr_b16 a[74:75], v48 offset:8448\n"
    "ds_read_b64_tr_b16 a[76:77], v48 offset:12288\n"
    "ds_read_b64_tr_b16 a[78:79], v48 offset:12544\n"
    "s_waitcnt lgkmcnt(0)\n"
    "s_waitcnt vmcnt(24)\n"
    "s_barrier\n"
    "s_mov_b32 s28, s94\n"
    "s_mov_b32 s29, s95\n"
    "s_add_u32 m0, s62, 65536\n"
    "s_mov_b32 s38, 1572864\n"
    "buffer_load_dwordx4 v17, s[28:31], s38 offen lds\n"
    "s_add_u32 m0, s62, 66560\n"
    "s_mov_b32 s38, 1581056\n"
    "buffer_load_dwordx4 v17, s[28:31], s38 offen lds\n"
    "s_add_u32 m0, s62, 73728\n"
    "s_mov_b32 s38, 1638400\n"
    "buffer_load_dwordx4 v17, s[28:31], s38 offen lds\n"
    "s_add_u32 m0, s62, 74752\n"
    "s_mov_b32 s38, 1646592\n"
    "buffer_load_dwordx4 v17, s[28:31], s38 offen lds\n"
    "v_add_u32 v48, 81920, v14\n"
    "ds_read_b64_tr_b16 a[80:81], v48 offset:0\n"
    "ds_read_b64_tr_b16 a[82:83], v48 offset:256\n"
    "ds_read_b64_tr_b16 a[84:85], v48 offset:4096\n"
    "ds_read_b64_tr_b16 a[86:87], v48 offset:4352\n"
    "ds_read_b64_tr_b16 a[88:89], v48 offset:8192\n"
    "ds_read_b64_tr_b16 a[90:91], v48 offset:8448\n"
    "ds_read_b64_tr_b16 a[92:93], v48 offset:12288\n"
    "ds_read_b64_tr_b16 a[94:95], v48 offset:12544\n"
    "s_waitcnt lgkmcnt(0)\n"
    "s_waitcnt vmcnt(24)\n"
    "s_barrier\n"
    "s_mov_b32 s28, s94\n"
    "s_mov_b32 s29, s95\n"
    "s_add_u32 m0, s62, 81920\n"
    "s_mov_b32 s38, 1703936\n"
    "buffer_load_dwordx4 v17, s[28:31], s38 offen lds\n"
    "s_add_u32 m0, s62, 82944\n"
    "s_mov_b32 s38, 1712128\n"
    "buffer_load_dwordx4 v17, s[28:31], s38 offen lds\n"
    "s_add_u32 m0, s62, 90112\n"
    "s_mov_b32 s38, 1769472\n"
    "buffer_load_dwordx4 v17, s[28:31], s38 offen lds\n"
    "s_add_u32 m0, s62, 91136\n"
    "s_mov_b32 s38, 1777664\n"
    "buffer_load_dwordx4 v17, s[28:31], s38 offen lds\n"
    "v_add_u32 v48, 98304, v14\n"
    "ds_read_b64_tr_b16 a[96:97], v48 offset:0\n"
    "ds_read_b64_tr_b16 a[98:99], v48 offset:256\n"
    "ds_read_b64_tr_b16 a[100:101], v48 offset:4096\n"
    "ds_read_b64_tr_b16 a[102:103], v48 offset:4352\n"
    "ds_read_b64_tr_b16 a[104:105], v48 offset:8192\n"
    "ds_read_b64_tr_b16 a[106:107], v48 offset:8448\n"
    "ds_read_b64_tr_b16 a[108:109], v48 offset:12288\n"
    "ds_read_b64_tr_b16 a[110:111], v48 offset:12544\n"
    "s_waitcnt lgkmcnt(0)\n"
    "s_waitcnt vmcnt(24)\n"
    "s_barrier\n"
    "v_add_u32 v48, 114688, v14\n"
    "ds_read_b64_tr_b16 a[112:113], v48 offset:0\n"
    "ds_read_b64_tr_b16 a[114:115], v48 offset:256\n"
    "ds_read_b64_tr_b16 a[116:117], v48 offset:4096\n"
    "ds_read_b64_tr_b16 a[118:119], v48 offset:4352\n"
    "ds_read_b64_tr_b16 a[120:121], v48 offset:8192\n"
    "ds_read_b64_tr_b16 a[122:123], v48 offset:8448\n"
    "ds_read_b64_tr_b16 a[124:125], v48 offset:12288\n"
    "ds_read_b64_tr_b16 a[126:127], v48 offset:12544\n"
    "s_waitcnt lgkmcnt(0)\n"
    "s_waitcnt vmcnt(20)\n"
    "s_barrier\n"
    "v_add_u32 v48, 0, v14\n"
    "ds_read_b64_tr_b16 a[128:129], v48 offset:0\n"
    "ds_read_b64_tr_b16 a[130:131], v48 offset:256\n"
    "ds_read_b64_tr_b16 a[132:133], v48 offset:4096\n"
    "ds_read_b64_tr_b16 a[134:135], v48 offset:4352\n"
    "ds_read_b64_tr_b16 a[136:137], v48 offset:8192\n"
    "ds_read_b64_tr_b16 a[138:139], v48 offset:8448\n"
    "ds_read_b64_tr_b16 a[140:141], v48 offset:12288\n"
    "ds_read_b64_tr_b16 a[142:143], v48 offset:12544\n"
    "s_waitcnt lgkmcnt(0)\n"
    "s_waitcnt vmcnt(16)\n"
    "s_barrier\n"
    "v_add_u32 v48, 16384, v14\n"
    "ds_read_b64_tr_b16 a[144:145], v48 offset:0\n"
    "ds_read_b64_tr_b16 a[146:147], v48 offset:256\n"
    "ds_read_b64_tr_b16 a[148:149], v48 offset:4096\n"
    "ds_read_b64_tr_b16 a[150:151], v48 offset:4352\n"
    "ds_read_b64_tr_b16 a[152:153], v48 offset:8192\n"
    "ds_read_b64_tr_b16 a[154:155], v48 offset:8448\n"
    "ds_read_b64_tr_b16 a[156:157], v48 offset:12288\n"
    "ds_read_b64_tr_b16 a[158:159], v48 offset:12544\n"
    "s_waitcnt lgkmcnt(0)\n"
    "s_waitcnt vmcnt(12)\n"
    "s_barrier\n"
    "v_add_u32 v48, 32768, v14\n"
    "ds_read_b64_tr_b16 a[160:161], v48 offset:0\n"
    "ds_read_b64_tr_b16 a[162:163], v48 offset:256\n"
    "ds_read_b64_tr_b16 a[164:165], v48 offset:4096\n"
    "ds_read_b64_tr_b16 a[166:167], v48 offset:4352\n"
    "ds_read_b64_tr_b16 a[168:169], v48 offset:8192\n"
    "ds_read_b64_tr_b16 a[170:171], v48 offset:8448\n"
    "ds_read_b64_tr_b16 a[172:173], v48 offset:12288\n"
    "ds_read_b64_tr_b16 a[174:175], v48 offset:12544\n"
    "s_waitcnt lgkmcnt(0)\n"
    "s_waitcnt vmcnt(8)\n"
    "s_barrier\n"
    "v_add_u32 v48, 49152, v14\n"
    "ds_read_b64_tr_b16 a[176:177], v48 offset:0\n"
    "ds_read_b64_tr_b16 a[178:179], v48 offset:256\n"
    "ds_read_b64_tr_b16 a[180:181], v48 offset:4096\n"
    "ds_read_b64_tr_b16 a[182:183], v48 offset:4352\n"
    "ds_read_b64_tr_b16 a[184:185], v48 offset:8192\n"
    "ds_read_b64_tr_b16 a[186:187], v48 offset:8448\n"
    "ds_read_b64_tr_b16 a[188:189], v48 offset:12288\n"
    "ds_read_b64_tr_b16 a[190:191], v48 offset:12544\n"
    "s_waitcnt lgkmcnt(0)\n"
    "s_waitcnt vmcnt(4)\n"
    "s_barrier\n"
    "v_add_u32 v48, 65536, v14\n"
    "ds_read_b64_tr_b16 a[192:193], v48 offset:0\n"
    "ds_read_b64_tr_b16 a[194:195], v48 offset:256\n"
    "ds_read_b64_tr_b16 a[196:197], v48 offset:4096\n"
    "ds_read_b64_tr_b16 a[198:199], v48 offset:4352\n"
    "ds_read_b64_tr_b16 a[200:201], v48 offset:8192\n"
    "ds_read_b64_tr_b16 a[202:203], v48 offset:8448\n"
    "ds_read_b64_tr_b16 a[204:205], v48 offset:12288\n"
    "ds_read_b64_tr_b16 a[206:207], v48 offset:12544\n"
    "s_waitcnt lgkmcnt(0)\n"
    "s_waitcnt vmcnt(0)\n"
    "s_barrier\n"
    "v_add_u32 v48, 81920, v14\n"
    "ds_read_b64_tr_b16 a[208:209], v48 offset:0\n"
    "ds_read_b64_tr_b16 a[210:211], v48 offset:256\n"
    "ds_read_b64_tr_b16 a[212:213], v48 offset:4096\n"
    "ds_read_b64_tr_b16 a[214:215], v48 offset:4352\n"
    "ds_read_b64_tr_b16 a[216:217], v48 offset:8192\n"
    "ds_read_b64_tr_b16 a[218:219], v48 offset:8448\n"
    "ds_read_b64_tr_b16 a[220:221], v48 offset:12288\n"
    "ds_read_b64_tr_b16 a[222:223], v48 offset:12544\n"
    "s_waitcnt lgkmcnt(0)\n"
    "s_barrier\n"
    "v_lshlrev_b32 v32, 16, v116\n"
    "v_and_b32 v33, 0xffff0000, v116\n"
    "v_lshlrev_b32 v34, 16, v117\n"
    "v_and_b32 v35, 0xffff0000, v117\n"
    "v_lshlrev_b32 v36, 16, v118\n"
    "v_and_b32 v37, 0xffff0000, v118\n"
    "v_lshlrev_b32 v38, 16, v119\n"
    "v_and_b32 v39, 0xffff0000, v119\n"
    "v_mov_b32 v24, v12\n"
    "v_mov_b32 v25, v13\n"
    "v_mov_b32 v30, v12\n"
    "v_mov_b32 v31, v13\n"
    "v_mov_b32 v26, 0\n"
    "v_mov_b32 v27, 0\n"
    "v_mov_b32 v28, 0\n"
    "v_mov_b32 v29, 0\n"
    "v_mov_b32 v42, 0\n"
    "v_mov_b32 v43, 0\n"
    "v_mov_b32 v48, 0\n"
    "v_mov_b32 v44, 0x3b000000\n"
    "s_mov_b32 s40, 0\n"
    "s_mov_b32 s41, 0\n"
    "s_mov_b32 s42, 0\n"
    "s_mov_b32 s43, 0\n"
    "s_mov_b32 s44, 0\n"
    "s_mov_b32 s45, 0\n"
    "s_mov_b32 s46, -1\n"
    "s_mov_b32 s47, -1\n"
    "s_mov_b32 s48, 0\n"
    "s_mov_b32 s51, 0\n"
    "s_mov_b32 s49, 0\n"
    "s_mov_b32 s37, 0\n"
    "s_mov_b64 s[64:65], 0\n"
    "s_mov_b32 s79, 0\n"
    "s_mov_b32 s74, 0xffff0000\n"
    "s_mov_b32 s75, 0\n"
    "s_mov_b32 s76, 0xaaaaaaaa\n"
    "s_mov_b32 s77, 0xaaaaaaaa\n"
    "LRY%=:\n"
    "global_load_dword v40, v48, s[96:97] sc1\n"
    "s_waitcnt vmcnt(0)\n"
    "v_readfirstlane_b32 s56, v40\n"
    "s_cmp_lg_u32 s56, 0\n"
    "s_cbranch_scc1 LRZ%=\n"
    "s_sleep 8\n"
    "s_add_u32 s37, s37, 1\n"
    "s_cmp_lt_u32 s37, 0x80000\n"
    "s_cbranch_scc1 LRY%=\n"
    "s_branch LFAIL%=\n"
    "LRZ%=:\n"
    "s_lshl_b32 s56, s56, 13\n"
    "s_and_b32 s56, s56, 0x2000\n"
    "s_xor_b32 s57, s56, 0x2000\n"
    "s_xor_b32 s58, s58, s56\n"
    "v_xor_b32 v2, s56, v2\n"
    "v_xor_b32 v11, s56, v11\n"
    "v_xor_b32 v12, s56, v21\n"
    "s_cmp_eq_u32 s55, 0\n"
    "s_cselect_b32 s38, 0, 0x80000000\n"
    "s_add_u32 s38, s38, 4096\n"
    "v_add_u32 v13, s38, v11\n"
    "LPR%=:\n"
    "buffer_load_dword v40, v12, s[16:19], 0 offen sc1\n"
    "s_waitcnt vmcnt(0)\n"
    "v_cmp_eq_u32 vcc, 0, v40\n"
    "s_cbranch_vccz LPOLL%=\n"
    "s_sleep 8\n"
    "s_add_u32 s37, s37, 1\n"
    "s_cmp_lt_u32 s37, 0x80000\n"
    "s_cbranch_scc1 LPR%=\n"
    "s_branch LFAIL%=\n"
    "LIDLE%=:\n"
    "s_min_u32 s38, s42, s41\n"
    "s_min_u32 s38, s38, s40\n"
    "s_sub_u32 s38, s38, s49\n"
    "s_cmp_eq_u32 s38, 64\n"
    "s_cbranch_scc1 LBEST%=\n"
    "s_add_u32 s37, s37, 1\n"
    "s_cmp_gt_u32 s37, 0x100000\n"
    "s_cbranch_scc1 LFAIL%=\n"
    "s_sleep 1\n"
    "LPOLL%=:\n"
    "v_mad_u32_u24 v41, v3, s41, v2\n"
    "v_mad_u32_u24 v41, v4, s42, v41\n"
    "v_mad_u32_u24 v41, v5, s40, v41\n"
    "buffer_load_dword v40, v41, s[16:19], 0 offen sc1\n"
    "s_waitcnt vmcnt(0)\n"
    "LTOP%=:\n"
    "v_readlane_b32 s52, v40, 32\n"
    "v_readlane_b32 s53, v40, 0\n"
    "v_readlane_b32 s54, v40, 16\n"
    "s_cmp_lg_u32 s52, 0\n"
    "s_cbranch_scc1 LDEC%=\n"
    "LPICK%=:\n"
    "s_cmp_ge_u32 s41, s43\n"
    "s_cselect_b32 s38, s40, 0\n"
    "s_cmp_eq_u32 s38, 64\n"
    "s_cselect_b32 s41, 64, s41\n"
    "s_cselect_b32 s53, 0, s53\n"
    "s_cmp_lt_u32 s42, s41\n"
    "s_cselect_b32 s51, s54, 0\n"
    "s_or_b32 s38, s51, s53\n"
    "s_mov_b32 s50, -1\n"
    "s_cbranch_scc0 LNONE%=\n"
    "s_cmp_lg_u32 s51, 0\n"
    "s_cselect_b32 s50, s42, s41\n"
    "s_addc_u32 s42, s42, 0\n"
    "s_add_u32 s41, s41, 1\n"
    "s_sub_u32 s41, s41, s51\n"
    "s_lshl_b32 s60, s50, 5\n"
    "s_add_u32 s60, s60, s55\n"
    "s_cmp_eq_u32 s60, s44\n"
    "s_cselect_b32 s38, s51, 0\n"
    "s_cmp_lg_u32 s38, 0\n"
    "s_cselect_b32 s60, s50, s60\n"
    "s_cselect_b32 s62, 22020096, 0\n"
    "s_cselect_b32 s61, s57, s56\n"
    "s_add_u32 s62, s62, 241172480\n"
    "s_lshl_b32 s39, s60, 10\n"
    "s_andn2_b32 s63, s39, 0x1fff\n"
    "s_add_u32 s63, s63, s39\n"
    "s_add_u32 s63, s63, s62\n"
    "s_lshr_b32 s38, s63, 8\n"
    "s_xor_b32 s38, s38, s61\n"
    "s_and_b32 s38, s38, 0x2000\n"
    "s_or_b32 s63, s63, s38\n"
    "s_lshl_b32 s62, s51, 6\n"
    "s_add_u32 s62, s62, s50\n"
    "s_lshl_b32 s62, s62, 18\n"
    "s_add_u32 s62, s62, 197132288\n"
    "s_lshr_b32 s38, s62, 8\n"
    "s_xor_b32 s38, s38, s57\n"
    "s_and_b32 s38, s38, 0x2000\n"
    "s_or_b32 s62, s62, s38\n"
    "buffer_load_dwordx4 v[116:119], v0, s[16:19], s63 offen sc1\n"
    "buffer_load_dwordx4 v[100:103], v1, s[16:19], s62 offen sc1\n"
    "s_add_u32 s62, s62, 0x10000\n"
    "buffer_load_dwordx4 v[104:107], v1, s[16:19], s62 offen sc1\n"
    "s_add_u32 s62, s62, 0x10000\n"
    "buffer_load_dwordx4 v[108:111], v1, s[16:19], s62 offen sc1\n"
    "s_add_u32 s62, s62, 0x10000\n"
    "buffer_load_dwordx4 v[112:115], v1, s[16:19], s62 offen sc1\n"
    "s_cmp_ge_i32 s47, 0\n"
    "s_cbranch_scc1 LJOB%=\n"
    "s_lshl_b32 s60, s50, 5\n"
    "s_add_u32 s60, s60, s55\n"
    "s_cmp_lg_u32 s51, 0\n"
    "s_cselect_b32 s62, 8388608, 0\n"
    "s_cselect_b32 s63, s57, s57\n"
    "s_add_u32 s62, s62, 245366784\n"
    "s_lshl_b32 s39, s60, 11\n"
    "s_andn2_b32 s32, s39, 0x1fff\n"
    "s_add_u32 s32, s32, s39\n"
    "s_add_u32 s32, s32, s62\n"
    "s_lshr_b32 s38, s32, 8\n"
    "s_xor_b32 s38, s38, s63\n"
    "s_and_b32 s38, s38, 0x2000\n"
    "s_or_b32 s32, s32, s38\n"
    "s_mov_b32 s33, s60\n"
    "s_waitcnt vmcnt(4)\n"
    "v_lshlrev_b32 v64, 16, v116\n"
    "v_and_b32 v65, 0xffff0000, v116\n"
    "v_lshlrev_b32 v66, 16, v117\n"
    "v_and_b32 v67, 0xffff0000, v117\n"
    "v_lshlrev_b32 v68, 16, v118\n"
    "v_and_b32 v69, 0xffff0000, v118\n"
    "v_lshlrev_b32 v70, 16, v119\n"
    "v_and_b32 v71, 0xffff0000, v119\n"
    "s_waitcnt vmcnt(3)\n"
    "v_mfma_f32_16x16x32_bf16 v[64:67], v[24:27], v[100:103], v[64:67]\n"
    "v_mfma_f32_16x16x32_bf16 v[68:71], v[28:31], v[100:103], v[68:71]\n"
    "s_waitcnt vmcnt(2)\n"
    "v_mfma_f32_16x16x32_bf16 v[64:67], v[24:27], v[104:107], v[64:67]\n"
    "v_mfma_f32_16x16x32_bf16 v[68:71], v[28:31], v[104:107], v[68:71]\n"
    "s_waitcnt vmcnt(1)\n"
    "v_mfma_f32_16x16x32_bf16 v[64:67], v[24:27], v[108:111], v[64:67]\n"
    "v_mfma_f32_16x16x32_bf16 v[68:71], v[28:31], v[108:111], v[68:71]\n"
    "v_max3_u32 v48, v103, v107, v111\n"
    "s_waitcnt vmcnt(0)\n"
    "v_mfma_f32_16x16x32_bf16 v[64:67], v[24:27], v[112:115], v[64:67]\n"
    "v_mfma_f32_16x16x32_bf16 v[68:71], v[28:31], v[112:115], v[68:71]\n"
    "v_max_u32 v48, v48, v115\n"
    "v_cmp_eq_u32 vcc, -1, v48\n"
    "s_mov_b32 s47, s50\n"
    "s_mov_b32 s48, s51\n"
    "s_mov_b32 s50, -1\n"
    "s_cbranch_vccnz LEARLY%=\n"
    "LNXT%=:\n"
    "LJOB%=:\n"
    "v_mad_u32_u24 v41, v3, s41, v2\n"
    "v_mad_u32_u24 v41, v4, s42, v41\n"
    "v_mad_u32_u24 v41, v5, s40, v41\n"
    "buffer_load_dword v40, v41, s[16:19], 0 offen sc1\n"
    "v_cvt_pk_bf16_f32 v124, v64, v65\n"
    "v_lshlrev_b32 v72, 16, v124\n"
    "v_cvt_pk_bf16_f32 v125, v66, v67\n"
    "v_readfirstlane_b32 s68, v72\n"
    "v_cvt_pk_bf16_f32 v126, v68, v69\n"
    "v_cvt_pk_bf16_f32 v127, v70, v71\n"
    "v_and_b32 v73, 0xffff0000, v124\n"
    "v_lshlrev_b32 v74, 16, v125\n"
    "v_and_b32 v75, 0xffff0000, v125\n"
    "v_lshlrev_b32 v76, 16, v126\n"
    "v_and_b32 v77, 0xffff0000, v126\n"
    "v_lshlrev_b32 v78, 16, v127\n"
    "v_and_b32 v79, 0xffff0000, v127\n"
    "v_pk_add_f32 v[92:93], v[72:73], s[68:69] op_sel_hi:[1,0] neg_lo:[0,1] neg_hi:[0,1]\n"
    "v_pk_add_f32 v[94:95], v[74:75], s[68:69] op_sel_hi:[1,0] neg_lo:[0,1] neg_hi:[0,1]\n"
    "v_pk_add_f32 v[96:97], v[76:77], s[68:69] op_sel_hi:[1,0] neg_lo:[0,1] neg_hi:[0,1]\n"
    "v_pk_add_f32 v[98:99], v[78:79], s[68:69] op_sel_hi:[1,0] neg_lo:[0,1] neg_hi:[0,1]\n"
    "v_pk_add_f32 v[48:49], v[92:93], v[94:95]\n"
    "v_pk_mul_f32 v[50:51], v[92:93], v[92:93]\n"
    "v_pk_add_f32 v[52:53], v[96:97], v[98:99]\n"
    "v_pk_fma_f32 v[50:51], v[94:95], v[94:95], v[50:51]\n"
    "v_pk_add_f32 v[48:49], v[48:49], v[52:53]\n"
    "v_pk_fma_f32 v[50:51], v[96:97], v[96:97], v[50:51]\n"
    "v_add_f32 v48, v48, v49\n"
    "v_pk_fma_f32 v[50:51], v[98:99], v[98:99], v[50:51]\n"
    "v_mov_b32 v52, s68\n"
    "v_add_f32 v49, v50, v51\n"
    "v_add_f32_dpp v48, v48, v48 row_ror:8 row_mask:0xf bank_mask:0xf\n"
    "s_nop 0\n"
    "v_add_f32_dpp v49, v49, v49 row_ror:8 row_mask:0xf bank_mask:0xf\n"
    "buffer_store_dwordx4 v[124:127], v0, s[16:19], s32 offen offset:0 sc1\n"
    "v_add_f32_dpp v48, v48, v48 row_ror:4 row_mask:0xf bank_mask:0xf\n"
    "v_add_f32_dpp v49, v49, v49 row_ror:4 row_mask:0xf bank_mask:0xf\n"
    "s_nop 0\n"
    "v_add_f32_dpp v48, v48, v48 row_ror:2 row_mask:0xf bank_mask:0xf\n"
    "v_add_f32_dpp v49, v49, v49 row_ror:2 row_mask:0xf bank_mask:0xf\n"
    "s_nop 0\n"
    "v_add_f32_dpp v48, v48, v48 row_ror:1 row_mask:0xf bank_mask:0xf\n"
    "v_add_f32_dpp v49, v49, v49 row_ror:1 row_mask:0xf bank_mask:0xf\n"
    "s_nop 1\n"
    "v_permlane16_swap_b32 v48, v49\n"
    "v_add_f32 v48, v48, v49\n"
    "v_mov_b32 v49, v48\n"
    "s_nop 1\n"
    "v_permlane32_swap_b32 v48, v49\n"
    "v_add_f32 v48, v48, v49\n"
    "s_nop 0\n"
    "v_readlane_b32 s62, v48, 0\n"
    "v_readlane_b32 s63, v48, 16\n"
    "s_nop 0\n"
    "v_mul_f32 v48, s62, v44\n"
    "v_mul_f32 v49, s63, v44\n"
    "v_fma_f32 v49, -v48, v48, v49\n"
    "v_add_f32 v52, v52, v48\n"
    "v_max_f32 v49, 0, v49\n"
    "v_cvt_pk_bf16_f32 v48, v52, v49\n"
    "v_and_b32 v49, 0xffff0000, v48\n"
    "v_add_f32 v49, 0x3727c5ac, v49\n"
    "v_cvt_pk_bf16_f32 v49, v49, v49\n"
    "v_lshlrev_b32 v49, 16, v49\n"
    "v_sqrt_f32 v50, v49\n"
    "v_lshlrev_b32 v48, 16, v48\n"
    "v_cvt_pk_bf16_f32 v50, v50, v50\n"
    "v_lshlrev_b32 v50, 16, v50\n"
    "v_rcp_f32 v50, v50\n"
    "v_pk_add_f32 v[92:93], v[72:73], v[48:49] op_sel_hi:[1,0] neg_lo:[0,1] neg_hi:[0,1]\n"
    "v_pk_add_f32 v[94:95], v[74:75], v[48:49] op_sel_hi:[1,0] neg_lo:[0,1] neg_hi:[0,1]\n"
    "v_pk_add_f32 v[96:97], v[76:77], v[48:49] op_sel_hi:[1,0] neg_lo:[0,1] neg_hi:[0,1]\n"
    "v_pk_add_f32 v[98:99], v[78:79], v[48:49] op_sel_hi:[1,0] neg_lo:[0,1] neg_hi:[0,1]\n"
    "v_cvt_pk_bf16_f32 v52, v92, v93\n"
    "v_cvt_pk_bf16_f32 v53, v94, v95\n"
    "v_cvt_pk_bf16_f32 v54, v96, v97\n"
    "v_cvt_pk_bf16_f32 v55, v98, v99\n"
    "v_lshlrev_b32 v92, 16, v52\n"
    "v_and_b32 v93, 0xffff0000, v52\n"
    "v_lshlrev_b32 v94, 16, v53\n"
    "v_and_b32 v95, 0xffff0000, v53\n"
    "v_lshlrev_b32 v96, 16, v54\n"
    "v_and_b32 v97, 0xffff0000, v54\n"
    "v_lshlrev_b32 v98, 16, v55\n"
    "v_and_b32 v99, 0xffff0000, v55\n"
    "v_pk_mul_f32 v[80:81], v[92:93], v[50:51] op_sel_hi:[1,0]\n"
    "v_pk_mul_f32 v[82:83], v[94:95], v[50:51] op_sel_hi:[1,0]\n"
    "v_pk_mul_f32 v[84:85], v[96:97], v[50:51] op_sel_hi:[1,0]\n"
    "v_pk_mul_f32 v[86:87], v[98:99], v[50:51] op_sel_hi:[1,0]\n"
    "v_cvt_pk_bf16_f32 v88, v80, v81\n"
    "v_cvt_pk_bf16_f32 v89, v82, v83\n"
    "v_cvt_pk_bf16_f32 v90, v84, v85\n"
    "v_cvt_pk_bf16_f32 v91, v86, v87\n"
    "v_lshlrev_b32 v80, 16, v88\n"
    "v_and_b32 v81, 0xffff0000, v88\n"
    "v_lshlrev_b32 v82, 16, v89\n"
    "v_and_b32 v83, 0xffff0000, v89\n"
    "v_lshlrev_b32 v84, 16, v90\n"
    "v_and_b32 v85, 0xffff0000, v90\n"
    "v_lshlrev_b32 v86, 16, v91\n"
    "v_and_b32 v87, 0xffff0000, v91\n"
    "v_pk_add_f32 v[72:73], v[72:73], v[80:81]\n"
    "v_pk_add_f32 v[74:75], v[74:75], v[82:83]\n"
    "v_pk_add_f32 v[76:77], v[76:77], v[84:85]\n"
    "v_pk_add_f32 v[78:79], v[78:79], v[86:87]\n"
    "buffer_store_dwordx4 v[88:91], v0, s[16:19], s32 offen offset:1024 sc1\n"
    "v_pk_mul_f32 v[56:57], v[80:81], v[32:33]\n"
    "v_pk_fma_f32 v[56:57], v[82:83], v[34:35], v[56:57]\n"
    "v_pk_fma_f32 v[56:57], v[84:85], v[36:37], v[56:57]\n"
    "v_pk_fma_f32 v[56:57], v[86:87], v[38:39], v[56:57]\n"
    "v_cvt_pk_bf16_f32 v60, v72, v73\n"
    "v_cvt_pk_bf16_f32 v61, v74, v75\n"
    "v_cvt_pk_bf16_f32 v62, v76, v77\n"
    "v_cvt_pk_bf16_f32 v63, v78, v79\n"
    "s_cmp_ge_u32 s47, s43\n"
    "s_cselect_b32 s38, 1, 0\n"
    "s_cmp_eq_u32 s38, s48\n"
    "s_cbranch_scc0 LKEEP%=\n"
    "s_lshl_b32 s61, s33, 10\n"
    "buffer_store_dwordx4 v[60:63], v0, s[24:27], s61 offen offset:0 sc1\n"
    "s_branch LKEPT%=\n"
    "LKEEP%=:\n"
    "s_lshl_b32 s39, s33, 10\n"
    "s_andn2_b32 s61, s39, 0x1fff\n"
    "s_add_u32 s61, s61, s39\n"
    "s_add_u32 s61, s61, 241172480\n"
    "s_lshr_b32 s38, s61, 8\n"
    "s_xor_b32 s38, s38, s57\n"
    "s_and_b32 s38, s38, 0x2000\n"
    "s_or_b32 s61, s61, s38\n"
    "s_lshl_b32 s38, s48, 31\n"
    "s_or_b32 s61, s61, s38\n"
    "buffer_store_dwordx4 v[60:63], v0, s[16:19], s61 offen offset:0 sc1\n"
    "s_xor_b32 s66, s48, 1\n"
    "s_mov_b32 s67, 0\n"
    "s_lshl_b64 s[66:67], s[66:67], s47\n"
    "s_or_b64 s[64:65], s[64:65], s[66:67]\n"
    "LKEPT%=:\n"
    "v_add_f32_dpp v56, v56, v56 row_ror:8 row_mask:0xf bank_mask:0xf\n"
    "v_add_f32_dpp v57, v57, v57 row_ror:8 row_mask:0xf bank_mask:0xf\n"
    "s_nop 0\n"
    "v_add_f32_dpp v56, v56, v56 row_ror:4 row_mask:0xf bank_mask:0xf\n"
    "v_add_f32_dpp v57, v57, v57 row_ror:4 row_mask:0xf bank_mask:0xf\n"
    "s_nop 0\n"
    "v_add_f32_dpp v56, v56, v56 row_ror:2 row_mask:0xf bank_mask:0xf\n"
    "v_add_f32_dpp v57, v57, v57 row_ror:2 row_mask:0xf bank_mask:0xf\n"
    "s_nop 0\n"
    "v_add_f32_dpp v56, v56, v56 row_ror:1 row_mask:0xf bank_mask:0xf\n"
    "v_add_f32_dpp v57, v57, v57 row_ror:1 row_mask:0xf bank_mask:0xf\n"
    "s_nop 1\n"
    "v_permlane16_swap_b32 v56, v57\n"
    "v_add_f32 v56, v56, v57\n"
    "v_mov_b32 v57, v56\n"
    "s_nop 1\n"
    "v_permlane32_swap_b32 v56, v57\n"
    "v_add_f32 v56, v56, v57\n"
    "s_nop 0\n"
    "v_readlane_b32 s62, v56, 0\n"
    "v_readlane_b32 s63, v56, 16\n"
    "s_sub_u32 s39, 0xffff, s33\n"
    "v_mov_b32 v56, s62\n"
    "v_add_f32 v56, s63, v56\n"
    "v_cvt_pk_bf16_f32 v56, v56, v56\n"
    "v_lshlrev_b32 v56, 16, v56\n"
    "v_cmp_o_f32 vcc, v56, v56\n"
    "v_ashrrev_i32 v57, 31, v56\n"
    "v_or_b32 v57, 0x80000000, v57\n"
    "v_xor_b32 v56, v56, v57\n"
    "v_cndmask_b32 v56, 0, v56, vcc\n"
    "v_and_b32 v56, 0xffff0000, v56\n"
    "v_or_b32 v56, s39, v56\n"
    "s_lshl_b64 exec, 1, s47\n"
    "s_cmp_lg_u32 s48, 0\n"
    "s_cselect_b64 vcc, -1, 0\n"
    "v_cndmask_b32 v42, v42, v56, vcc\n"
    "v_cndmask_b32 v43, v56, v43, vcc\n"
    "s_mov_b64 exec, -1\n"
    "s_waitcnt vmcnt(3)\n"
    "s_mov_b32 s47, -1\n"
    "s_mov_b32 s37, 0\n"
    "s_cmp_lt_i32 s50, 0\n"
    "s_cbranch_scc1 LTOP%=\n"
    "s_lshl_b32 s60, s50, 5\n"
    "s_add_u32 s60, s60, s55\n"
    "s_cmp_lg_u32 s51, 0\n"
    "s_cselect_b32 s62, 8388608, 0\n"
    "s_cselect_b32 s63, s57, s57\n"
    "s_add_u32 s62, s62, 245366784\n"
    "s_lshl_b32 s39, s60, 11\n"
    "s_andn2_b32 s32, s39, 0x1fff\n"
    "s_add_u32 s32, s32, s39\n"
    "s_add_u32 s32, s32, s62\n"
    "s_lshr_b32 s38, s32, 8\n"
    "s_xor_b32 s38, s38, s63\n"
    "s_and_b32 s38, s38, 0x2000\n"
    "s_or_b32 s32, s32, s38\n"
    "s_mov_b32 s33, s60\n"
    "v_lshlrev_b32 v64, 16, v116\n"
    "v_and_b32 v65, 0xffff0000, v116\n"
    "v_lshlrev_b32 v66, 16, v117\n"
    "v_and_b32 v67, 0xffff0000, v117\n"
    "v_lshlrev_b32 v68, 16, v118\n"
    "v_and_b32 v69, 0xffff0000, v118\n"
    "v_lshlrev_b32 v70, 16, v119\n"
    "v_and_b32 v71, 0xffff0000, v119\n"
    "v_mfma_f32_16x16x32_bf16 v[64:67], v[24:27], v[100:103], v[64:67]\n"
    "s_nop 0\n"
    "v_mfma_f32_16x16x32_bf16 v[68:71], v[28:31], v[100:103], v[68:71]\n"
    "v_mfma_f32_16x16x32_bf16 v[64:67], v[24:27], v[104:107], v[64:67]\n"
    "v_mfma_f32_16x16x32_bf16 v[68:71], v[28:31], v[104:107], v[68:71]\n"
    "v_mfma_f32_16x16x32_bf16 v[64:67], v[24:27], v[108:111], v[64:67]\n"
    "v_mfma_f32_16x16x32_bf16 v[68:71], v[28:31], v[108:111], v[68:71]\n"
    "v_max3_u32 v48, v103, v107, v111\n"
    "v_mfma_f32_16x16x32_bf16 v[64:67], v[24:27], v[112:115], v[64:67]\n"
    "v_mfma_f32_16x16x32_bf16 v[68:71], v[28:31], v[112:115], v[68:71]\n"
    "v_max_u32 v48, v48, v115\n"
    "v_cmp_eq_u32 vcc, -1, v48\n"
    "s_mov_b32 s47, s50\n"
    "s_mov_b32 s48, s51\n"
    "s_mov_b32 s50, -1\n"
    "s_cbranch_vccnz LEARLY%=\n"
    "s_branch LTOP%=\n"
    "LNONE%=:\n"
    "s_cmp_ge_i32 s47, 0\n"
    "s_cbranch_scc1 LJOB%=\n"
    "s_branch LIDLE%=\n"
    "LEARLY%=:\n"
    "s_sub_u32 s42, s42, s48\n"
    "s_add_u32 s41, s41, s48\n"
    "s_sub_u32 s41, s41, 1\n"
    "s_mov_b32 s54, s48\n"
    "s_xor_b32 s53, s48, 1\n"
    "s_mov_b32 s47, -1\n"
    "s_and_b32 s38, s37, 3\n"
    "s_cmp_eq_u32 s38, 3\n"
    "s_cbranch_scc1 LIDLE%=\n"
    "s_sleep 2\n"
    "s_add_u32 s37, s37, 1\n"
    "s_cmp_lt_u32 s37, 0x80000\n"
    "s_cbranch_scc1 LPICK%=\n"
    "s_branch LFAIL%=\n"
    "LBEST%=:\n"
    "s_lshl_b64 vcc, -1, s43\n"
    "s_nop 0\n"
    "v_cndmask_b32 v48, v43, v42, vcc\n"
    "s_nop 1\n"
    "v_max_u32_dpp v48, v48, v48 row_ror:8 row_mask:0xf bank_mask:0xf\n"
    "s_nop 1\n"
    "v_max_u32_dpp v48, v48, v48 row_ror:4 row_mask:0xf bank_mask:0xf\n"
    "s_nop 1\n"
    "v_max_u32_dpp v48, v48, v48 row_ror:2 row_mask:0xf bank_mask:0xf\n"
    "s_nop 1\n"
    "v_max_u32_dpp v48, v48, v48 row_ror:1 row_mask:0xf bank_mask:0xf\n"
    "v_mov_b32 v49, v48\n"
    "s_nop 1\n"
    "v_permlane16_swap_b32 v48, v49\n"
    "v_max_u32 v48, v48, v49\n"
    "v_mov_b32 v49, v48\n"
    "s_nop 1\n"
    "v_permlane32_swap_b32 v48, v49\n"
    "v_max_u32 v48, v48, v49\n"
    "s_add_u32 s70, s58, 4096\n"
    "s_mov_b32 s49, 1\n"
    "s_mov_b64 exec, 1\n"
    "buffer_atomic_umax v48, off, s[16:19], s70 offset:4 sc1\n"
    "s_mov_b64 exec, -1\n"
    "v_mov_b64 v[120:121], 1\n"
    "v_mad_u32_u24 v41, v3, s41, v2\n"
    "v_mad_u32_u24 v41, v4, s42, v41\n"
    "v_mad_u32_u24 v41, v5, s40, v41\n"
    "s_mov_b64 exec, 1\n"
    "s_waitcnt vmcnt(0)\n"
    "buffer_atomic_add_x2 v[120:121], off, s[16:19], s70 sc0 sc1\n"
    "s_mov_b64 exec, -1\n"
    "buffer_load_dword v40, v41, s[16:19], 0 offen sc1\n"
    "s_lshl_b32 s39, s40, 2\n"
    "s_waitcnt vmcnt(1)\n"
    "v_readfirstlane_b32 s38, v120\n"
    "v_readfirstlane_b32 s62, v121\n"
    "s_cmp_lg_u32 s38, 31\n"
    "s_cbranch_scc1 LAS%=\n"
    "v_mov_b32 v120, s62\n"
    "s_mov_b64 exec, 0x38\n"
    "buffer_store_dword v120, v11, s[16:19], s39 offen sc1\n"
    "s_mov_b64 exec, -1\n"
    "s_waitcnt vmcnt(1)\n"
    "s_cmp_lg_u32 s40, s40\n"
    "s_cbranch_scc1 LASX%=\n"
    "v_writelane_b32 v40, s62, 32\n"
    "s_branch LASX%=\n"
    "LAS%=:\n"
    "s_waitcnt vmcnt(0)\n"
    "LASX%=:\n"
    "s_branch LTOP%=\n"
    "LDEC%=:\n"
    "s_mov_b32 s70, s40\n"
    "s_cmp_eq_u32 s40, 64\n"
    "s_cbranch_scc1 LMLP%=\n"
    "s_add_u32 s40, s40, 1\n"
    "s_mov_b32 s37, 0\n"
    "s_lshr_b32 s38, s52, 16\n"
    "s_cmp_gt_i32 s38, s46\n"
    "s_cselect_b32 s70, s70, -1\n"
    "s_cselect_b32 s46, s38, s46\n"
    "s_addc_u32 s45, s45, 0\n"
    "s_cmp_lt_i32 s70, 0\n"
    "s_cbranch_scc1 LPICK%=\n"
    "s_mov_b32 s43, s70\n"
    "s_andn2_b32 s44, 0x7ff, s52\n"
    "LMLP%=:\n"
    "s_andn2_b32 s71, 0x7ff, s52\n"
    "s_lshr_b32 s38, s71, 5\n"
    "s_cmp_ge_u32 s38, s43\n"
    "s_cselect_b32 s63, s56, s57\n"
    "s_cselect_b32 s38, s70, 0\n"
    "s_cmp_eq_u32 s38, 64\n"
    "s_cselect_b32 s63, s57, s63\n"
    "s_cselect_b32 s62, 8388608, 0\n"
    "s_add_u32 s62, s62, 245366784\n"
    "s_lshl_b32 s39, s71, 11\n"
    "s_andn2_b32 s61, s39, 0x1fff\n"
    "s_add_u32 s61, s61, s39\n"
    "s_add_u32 s61, s61, s62\n"
    "s_lshr_b32 s38, s61, 8\n"
    "s_xor_b32 s38, s38, s63\n"
    "s_and_b32 s38, s38, 0x2000\n"
    "s_or_b32 s61, s61, s38\n"
    "buffer_load_dwordx4 v[116:119], v0, s[16:19], s61 offen offset:1024 sc1\n"
    "v_and_b32 v48, -4, v18\n"
    "buffer_load_dword v45, v48, s[16:19], s61 offen sc1\n"
    "buffer_load_dword v46, v48, s[16:19], s61 offen offset:1024 sc1\n"
    "s_lshl_b32 s39, s70, 12\n"
    "s_andn2_b32 s61, s39, 0x1fff\n"
    "s_add_u32 s61, s61, s39\n"
    "s_add_u32 s61, s61, 263323648\n"
    "s_lshr_b32 s38, s61, 8\n"
    "s_xor_b32 s38, s38, s57\n"
    "s_and_b32 s38, s38, 0x2000\n"
    "s_or_b32 s61, s61, s38\n"
    "v_mov_b64 v[104:105], 0\n"
    "v_mov_b64 v[106:107], 0\n"
    "v_mov_b64 v[108:109], 0\n"
    "v_mov_b64 v[110:111], 0\n"
    "s_waitcnt vmcnt(2)\n"
    "ds_write_b128 v8, v[116:119]\n"
    "ds_read_b128 v[72:75], v7 offset:0\n"
    "ds_read_b128 v[76:79], v7 offset:128\n"
    "ds_read_b128 v[80:83], v7 offset:256\n"
    "ds_read_b128 v[84:87], v7 offset:384\n"
    "ds_read_b128 v[88:91], v7 offset:512\n"
    "ds_read_b128 v[92:95], v7 offset:640\n"
    "ds_read_b128 v[96:99], v7 offset:768\n"
    "ds_read_b128 v[100:103], v7 offset:896\n"
    "s_waitcnt lgkmcnt(7)\n"
    "v_smfmac_f32_16x16x64_bf16 v[104:107], v[72:75], v[128:135], v6\n"
    "v_smfmac_f32_16x16x64_bf16 v[108:111], v[72:75], v[136:143], v6\n"
    "s_waitcnt lgkmcnt(6)\n"
    "v_smfmac_f32_16x16x64_bf16 v[104:107], v[76:79], v[144:151], v6\n"
    "v_smfmac_f32_16x16x64_bf16 v[108:111], v[76:79], v[152:159], v6\n"
    "s_waitcnt lgkmcnt(5)\n"
    "v_smfmac_f32_16x16x64_bf16 v[104:107], v[80:83], v[160:167], v6\n"
    "v_smfmac_f32_16x16x64_bf16 v[108:111], v[80:83], v[168:175], v6\n"
    "s_waitcnt lgkmcnt(4)\n"
    "v_smfmac_f32_16x16x64_bf16 v[104:107], v[84:87], v[176:183], v6\n"
    "v_smfmac_f32_16x16x64_bf16 v[108:111], v[84:87], v[184:191], v6\n"
    "s_waitcnt lgkmcnt(3)\n"
    "v_smfmac_f32_16x16x64_bf16 v[104:107], v[88:91], v[192:199], v6\n"
    "v_smfmac_f32_16x16x64_bf16 v[108:111], v[88:91], v[200:207], v6\n"
    "s_waitcnt lgkmcnt(2)\n"
    "v_smfmac_f32_16x16x64_bf16 v[104:107], v[92:95], v[208:215], v6\n"
    "v_smfmac_f32_16x16x64_bf16 v[108:111], v[92:95], v[216:223], v6\n"
    "s_waitcnt lgkmcnt(1)\n"
    "v_smfmac_f32_16x16x64_bf16 v[104:107], v[96:99], v[224:231], v6\n"
    "v_smfmac_f32_16x16x64_bf16 v[108:111], v[96:99], v[232:239], v6\n"
    "s_waitcnt lgkmcnt(0)\n"
    "v_smfmac_f32_16x16x64_bf16 v[104:107], v[100:103], v[240:247], v6\n"
    "v_smfmac_f32_16x16x64_bf16 v[108:111], v[100:103], v[248:255], v6\n"
    "s_nop 3\n"
    "v_add_f32 v104, v104, v106\n"
    "s_nop 2\n"
    "v_add_f32 v108, v108, v110\n"
    "v_cvt_pk_bf16_f32 v108, v104, v108\n"
    "v_lshlrev_b32 v104, 16, v108\n"
    "v_mul_f32 v105, 0xbfb8aa3b, v104\n"
    "v_exp_f32 v105, v105\n"
    "v_and_b32 v108, 0xffff0000, v108\n"
    "v_add_f32 v105, 1.0, v105\n"
    "v_rcp_f32 v105, v105\n"
    "s_nop 0\n"
    "v_mul_f32 v104, v104, v105\n"
    "v_cvt_pk_bf16_f32 v104, v104, v104\n"
    "v_lshlrev_b32 v104, 16, v104\n"
    "v_mul_f32 v104, v104, v108\n"
    "s_nop 1\n"
    "v_mov_b32_dpp v105, v104 quad_perm:[1,1,3,3] row_mask:0xf bank_mask:0xf\n"
    "v_cvt_pk_bf16_f32 v104, v104, v105\n"
    "v_min_u32 v104, -2, v104\n"
    "buffer_store_dword v104, v9, s[16:19], s61 offen sc1\n"
    "s_lshr_b32 s38, s52, 16\n"
    "s_bitcmp1_b32 s38, 15\n"
    "s_cselect_b32 s39, 0, 0x7fff\n"
    "s_or_b32 s39, s39, 0x8000\n"
    "s_xor_b32 s38, s38, s39\n"
    "s_lshl_b32 s38, s38, 16\n"
    "v_mov_b32 v52, 0xbfb8aa3b\n"
    "v_mul_f32 v52, s38, v52\n"
    "v_exp_f32 v52, v52\n"
    "s_nop 0\n"
    "v_add_f32 v52, 1.0, v52\n"
    "v_rcp_f32 v52, v52\n"
    "s_nop 0\n"
    "v_cvt_pk_bf16_f32 v52, v52, v52\n"
    "v_lshlrev_b32 v52, 16, v52\n"
    "s_waitcnt vmcnt(2)\n"
    "v_lshlrev_b32 v48, 16, v45\n"
    "v_and_b32 v45, 0xffff0000, v45\n"
    "v_cndmask_b32 v45, v48, v45, s[76:77]\n"
    "s_waitcnt vmcnt(1)\n"
    "v_lshlrev_b32 v48, 16, v46\n"
    "v_and_b32 v46, 0xffff0000, v46\n"
    "v_cndmask_b32 v46, v48, v46, s[76:77]\n"
    "v_mov_b32 v116, 0\n"
    "v_mov_b32 v117, 0\n"
    "v_mov_b32 v118, 0\n"
    "v_mov_b32 v119, 0\n"
    "v_mov_b64 v[56:57], 0\n"
    "v_mov_b64 v[58:59], 0\n"
    "v_mov_b64 v[60:61], 0\n"
    "v_mov_b64 v[62:63], 0\n"
    "s_sleep 6\n"
    "LH%=:\n"
    "buffer_load_dwordx4 v[104:107], v0, s[16:19], s61 offen offset:0 sc1\n"
    "buffer_load_dwordx4 v[108:111], v0, s[16:19], s61 offen offset:1024 sc1\n"
    "buffer_load_dwordx4 v[112:115], v0, s[16:19], s61 offen offset:2048 sc1\n"
    "s_mov_b32 exec_hi, 0\n"
    "buffer_load_dwordx4 v[116:119], v0, s[16:19], s61 offen offset:3072 sc1\n"
    "s_mov_b32 exec_hi, -1\n"
    "s_waitcnt vmcnt(0)\n"
    "v_max3_u32 v48, v104, v105, v106\n"
    "v_max3_u32 v48, v48, v107, v108\n"
    "v_max3_u32 v48, v48, v109, v110\n"
    "v_max3_u32 v48, v48, v111, v112\n"
    "v_max3_u32 v48, v48, v113, v114\n"
    "v_max3_u32 v48, v48, v115, v116\n"
    "v_max3_u32 v48, v48, v117, v118\n"
    "v_max_u32 v48, v48, v119\n"
    "v_cmp_eq_u32 vcc, -1, v48\n"
    "s_cbranch_vccz LHK%=\n"
    "s_sleep 2\n"
    "s_add_u32 s37, s37, 1\n"
    "s_cmp_lt_u32 s37, 0x80000\n"
    "s_cbranch_scc1 LH%=\n"
    "s_branch LFAIL%=\n"
    "LHK%=:\n"
    "ds_write_b128 v8, v[104:107] offset:0\n"
    "ds_write_b128 v8, v[108:111] offset:1024\n"
    "ds_write_b128 v8, v[112:115] offset:2048\n"
    "ds_write_b128 v8, v[116:119] offset:3072\n"
    "ds_read_b128 v[72:75], v7 offset:0\n"
    "ds_read_b128 v[76:79], v7 offset:128\n"
    "ds_read_b128 v[80:83], v7 offset:256\n"
    "ds_read_b128 v[84:87], v7 offset:384\n"
    "ds_read_b128 v[88:91], v7 offset:512\n"
    "ds_read_b128 v[92:95], v7 offset:640\n"
    "ds_read_b128 v[96:99], v7 offset:768\n"
    "s_waitcnt lgkmcnt(6)\n"
    "v_smfmac_f32_16x16x64_bf16 v[56:59], v[72:75], a[0:7], v6\n"
    "ds_read_b128 v[100:103], v7 offset:896\n"
    "s_waitcnt lgkmcnt(6)\n"
    "v_smfmac_f32_16x16x64_bf16 v[60:63], v[76:79], a[8:15], v6\n"
    "ds_read_b128 v[72:75], v7 offset:1024\n"
    "s_waitcnt lgkmcnt(6)\n"
    "v_smfmac_f32_16x16x64_bf16 v[56:59], v[80:83], a[16:23], v6\n"
    "ds_read_b128 v[76:79], v7 offset:1152\n"
    "s_waitcnt lgkmcnt(6)\n"
    "v_smfmac_f32_16x16x64_bf16 v[60:63], v[84:87], a[24:31], v6\n"
    "ds_read_b128 v[80:83], v7 offset:1280\n"
    "s_waitcnt lgkmcnt(6)\n"
    "v_smfmac_f32_16x16x64_bf16 v[56:59], v[88:91], a[32:39], v6\n"
    "ds_read_b128 v[84:87], v7 offset:1408\n"
    "s_waitcnt lgkmcnt(6)\n"
    "v_smfmac_f32_16x16x64_bf16 v[60:63], v[92:95], a[40:47], v6\n"
    "ds_read_b128 v[88:91], v7 offset:1536\n"
    "s_waitcnt lgkmcnt(6)\n"
    "v_smfmac_f32_16x16x64_bf16 v[56:59], v[96:99], a[48:55], v6\n"
    "ds_read_b128 v[92:95], v7 offset:1664\n"
    "s_waitcnt lgkmcnt(6)\n"
    "v_smfmac_f32_16x16x64_bf16 v[60:63], v[100:103], a[56:63], v6\n"
    "ds_read_b128 v[96:99], v7 offset:1792\n"
    "s_waitcnt lgkmcnt(6)\n"
    "v_smfmac_f32_16x16x64_bf16 v[56:59], v[72:75], a[64:71], v6\n"
    "ds_read_b128 v[100:103], v7 offset:1920\n"
    "s_waitcnt lgkmcnt(6)\n"
    "v_smfmac_f32_16x16x64_bf16 v[60:63], v[76:79], a[72:79], v6\n"
    "ds_read_b128 v[72:75], v7 offset:2048\n"
    "s_waitcnt lgkmcnt(6)\n"
    "v_smfmac_f32_16x16x64_bf16 v[56:59], v[80:83], a[80:87], v6\n"
    "ds_read_b128 v[76:79], v7 offset:2176\n"
    "s_waitcnt lgkmcnt(6)\n"
    "v_smfmac_f32_16x16x64_bf16 v[60:63], v[84:87], a[88:95], v6\n"
    "ds_read_b128 v[80:83], v7 offset:2304\n"
    "s_waitcnt lgkmcnt(6)\n"
    "v_smfmac_f32_16x16x64_bf16 v[56:59], v[88:91], a[96:103], v6\n"
    "ds_read_b128 v[84:87], v7 offset:2432\n"
    "s_waitcnt lgkmcnt(6)\n"
    "v_smfmac_f32_16x16x64_bf16 v[60:63], v[92:95], a[104:111], v6\n"
    "ds_read_b128 v[88:91], v7 offset:2560\n"
    "s_waitcnt lgkmcnt(6)\n"
    "v_smfmac_f32_16x16x64_bf16 v[56:59], v[96:99], a[112:119], v6\n"
    "ds_read_b128 v[92:95], v7 offset:2688\n"
    "s_waitcnt lgkmcnt(6)\n"
    "v_smfmac_f32_16x16x64_bf16 v[60:63], v[100:103], a[120:127], v6\n"
    "ds_read_b128 v[96:99], v7 offset:2816\n"
    "s_waitcnt lgkmcnt(6)\n"
    "v_smfmac_f32_16x16x64_bf16 v[56:59], v[72:75], a[128:135], v6\n"
    "ds_read_b128 v[100:103], v7 offset:2944\n"
    "s_waitcnt lgkmcnt(6)\n"
    "v_smfmac_f32_16x16x64_bf16 v[60:63], v[76:79], a[136:143], v6\n"
    "ds_read_b128 v[72:75], v7 offset:3072\n"
    "s_waitcnt lgkmcnt(6)\n"
    "v_smfmac_f32_16x16x64_bf16 v[56:59], v[80:83], a[144:151], v6\n"
    "ds_read_b128 v[76:79], v7 offset:3200\n"
    "s_waitcnt lgkmcnt(6)\n"
    "v_smfmac_f32_16x16x64_bf16 v[60:63], v[84:87], a[152:159], v6\n"
    "ds_read_b128 v[80:83], v7 offset:3328\n"
    "s_waitcnt lgkmcnt(6)\n"
    "v_smfmac_f32_16x16x64_bf16 v[56:59], v[88:91], a[160:167], v6\n"
    "ds_read_b128 v[84:87], v7 offset:3456\n"
    "s_waitcnt lgkmcnt(6)\n"
    "v_smfmac_f32_16x16x64_bf16 v[60:63], v[92:95], a[168:175], v6\n"
    "s_waitcnt lgkmcnt(5)\n"
    "v_smfmac_f32_16x16x64_bf16 v[56:59], v[96:99], a[176:183], v6\n"
    "s_waitcnt lgkmcnt(4)\n"
    "v_smfmac_f32_16x16x64_bf16 v[60:63], v[100:103], a[184:191], v6\n"
    "s_waitcnt lgkmcnt(3)\n"
    "v_smfmac_f32_16x16x64_bf16 v[56:59], v[72:75], a[192:199], v6\n"
    "s_waitcnt lgkmcnt(2)\n"
    "v_smfmac_f32_16x16x64_bf16 v[60:63], v[76:79], a[200:207], v6\n"
    "s_waitcnt lgkmcnt(1)\n"
    "v_smfmac_f32_16x16x64_bf16 v[56:59], v[80:83], a[208:215], v6\n"
    "s_waitcnt lgkmcnt(0)\n"
    "v_smfmac_f32_16x16x64_bf16 v[60:63], v[84:87], a[216:223], v6\n"
    "s_lshl_b32 s39, s70, 10\n"
    "s_andn2_b32 s61, s39, 0x1fff\n"
    "s_add_u32 s61, s61, s39\n"
    "s_add_u32 s61, s61, 263192576\n"
    "s_lshr_b32 s38, s61, 8\n"
    "s_xor_b32 s38, s38, s57\n"
    "s_and_b32 s38, s38, 0x2000\n"
    "s_or_b32 s61, s61, s38\n"
    "v_add_f32 v56, v56, v58\n"
    "v_add_f32 v60, v60, v62\n"
    "v_add_f32 v56, v56, v60\n"
    "v_cvt_pk_bf16_f32 v56, v56, v56\n"
    "v_lshlrev_b32 v56, 16, v56\n"
    "v_mul_f32 v56, v56, v52\n"
    "v_cvt_pk_bf16_f32 v56, v56, v56\n"
    "v_lshlrev_b32 v56, 16, v56\n"
    "v_add_f32 v56, v56, v46\n"
    "v_cvt_pk_bf16_f32 v56, v56, v56\n"
    "v_lshlrev_b32 v56, 16, v56\n"
    "v_add_f32 v56, v56, v45\n"
    "s_nop 1\n"
    "v_mov_b32_dpp v57, v56 quad_perm:[1,1,3,3] row_mask:0xf bank_mask:0xf\n"
    "v_cvt_pk_bf16_f32 v56, v56, v57\n"
    "s_cmp_eq_u32 s70, 64\n"
    "s_cbranch_scc0 LXP%=\n"
    "s_lshl_b32 s38, s71, 10\n"
    "buffer_store_dword v56, v10, s[24:27], s38 offen sc1\n"
    "s_branch LEND%=\n"
    "LXP%=:\n"
    "v_min_u32 v56, -2, v56\n"
    "buffer_store_dword v56, v10, s[16:19], s61 offen sc1\n"
    "LFIX%=:\n"
    "s_lshl_b64 vcc, -1, s43\n"
    "s_andn2_b64 s[66:67], s[64:65], vcc\n"
    "s_and_b64 s[64:65], s[64:65], vcc\n"
    "s_max_u32 s42, s42, s43\n"
    "LFL%=:\n"
    "s_cmp_eq_u64 s[66:67], 0\n"
    "s_cbranch_scc1 LPOLL%=\n"
    "s_waitcnt vmcnt(0)\n"
    "s_ff1_i32_b64 s62, s[66:67]\n"
    "s_bitset0_b64 s[66:67], s62\n"
    "s_lshl_b32 s90, s62, 5\n"
    "s_add_u32 s90, s90, s55\n"
    "s_lshl_b32 s39, s90, 10\n"
    "s_andn2_b32 s63, s39, 0x1fff\n"
    "s_add_u32 s63, s63, s39\n"
    "s_add_u32 s63, s63, 241172480\n"
    "s_lshr_b32 s38, s63, 8\n"
    "s_xor_b32 s38, s38, s57\n"
    "s_and_b32 s38, s38, 0x2000\n"
    "s_or_b32 s63, s63, s38\n"
    "s_lshl_b32 s90, s90, 10\n"
    "buffer_load_dwordx4 v[72:75], v0, s[16:19], s63 offen sc1\n"
    "s_ff1_i32_b64 s62, s[66:67]\n"
    "s_bitset0_b64 s[66:67], s62\n"
    "s_lshl_b32 s91, s62, 5\n"
    "s_add_u32 s91, s91, s55\n"
    "s_lshl_b32 s39, s91, 10\n"
    "s_andn2_b32 s63, s39, 0x1fff\n"
    "s_add_u32 s63, s63, s39\n"
    "s_add_u32 s63, s63, 241172480\n"
    "s_lshr_b32 s38, s63, 8\n"
    "s_xor_b32 s38, s38, s57\n"
    "s_and_b32 s38, s38, 0x2000\n"
    "s_or_b32 s63, s63, s38\n"
    "s_lshl_b32 s91, s91, 10\n"
    "buffer_load_dwordx4 v[76:79], v0, s[16:19], s63 offen sc1\n"
    "s_ff1_i32_b64 s62, s[66:67]\n"
    "s_bitset0_b64 s[66:67], s62\n"
    "s_lshl_b32 s92, s62, 5\n"
    "s_add_u32 s92, s92, s55\n"
    "s_lshl_b32 s39, s92, 10\n"
    "s_andn2_b32 s63, s39, 0x1fff\n"
    "s_add_u32 s63, s63, s39\n"
    "s_add_u32 s63, s63, 241172480\n"
    "s_lshr_b32 s38, s63, 8\n"
    "s_xor_b32 s38, s38, s57\n"
    "s_and_b32 s38, s38, 0x2000\n"
    "s_or_b32 s63, s63, s38\n"
    "s_lshl_b32 s92, s92, 10\n"
    "buffer_load_dwordx4 v[80:83], v0, s[16:19], s63 offen sc1\n"
    "s_ff1_i32_b64 s62, s[66:67]\n"
    "s_bitset0_b64 s[66:67], s62\n"
    "s_lshl_b32 s93, s62, 5\n"
    "s_add_u32 s93, s93, s55\n"
    "s_lshl_b32 s39, s93, 10\n"
    "s_andn2_b32 s63, s39, 0x1fff\n"
    "s_add_u32 s63, s63, s39\n"
    "s_add_u32 s63, s63, 241172480\n"
    "s_lshr_b32 s38, s63, 8\n"
    "s_xor_b32 s38, s38, s57\n"
    "s_and_b32 s38, s38, 0x2000\n"
    "s_or_b32 s63, s63, s38\n"
    "s_lshl_b32 s93, s93, 10\n"
    "buffer_load_dwordx4 v[84:87], v0, s[16:19], s63 offen sc1\n"
    "s_ff1_i32_b64 s62, s[66:67]\n"
    "s_bitset0_b64 s[66:67], s62\n"
    "s_lshl_b32 s94, s62, 5\n"
    "s_add_u32 s94, s94, s55\n"
    "s_lshl_b32 s39, s94, 10\n"
    "s_andn2_b32 s63, s39, 0x1fff\n"
    "s_add_u32 s63, s63, s39\n"
    "s_add_u32 s63, s63, 241172480\n"
    "s_lshr_b32 s38, s63, 8\n"
    "s_xor_b32 s38, s38, s57\n"
    "s_and_b32 s38, s38, 0x2000\n"
    "s_or_b32 s63, s63, s38\n"
    "s_lshl_b32 s94, s94, 10\n"
    "buffer_load_dwordx4 v[88:91], v0, s[16:19], s63 offen sc1\n"
    "s_ff1_i32_b64 s62, s[66:67]\n"
    "s_bitset0_b64 s[66:67], s62\n"
    "s_lshl_b32 s95, s62, 5\n"
    "s_add_u32 s95, s95, s55\n"
    "s_lshl_b32 s39, s95, 10\n"
    "s_andn2_b32 s63, s39, 0x1fff\n"
    "s_add_u32 s63, s63, s39\n"
    "s_add_u32 s63, s63, 241172480\n"
    "s_lshr_b32 s38, s63, 8\n"
    "s_xor_b32 s38, s38, s57\n"
    "s_and_b32 s38, s38, 0x2000\n"
    "s_or_b32 s63, s63, s38\n"
    "s_lshl_b32 s95, s95, 10\n"
    "buffer_load_dwordx4 v[92:95], v0, s[16:19], s63 offen sc1\n"
    "s_ff1_i32_b64 s62, s[66:67]\n"
    "s_bitset0_b64 s[66:67], s62\n"
    "s_lshl_b32 s96, s62, 5\n"
    "s_add_u32 s96, s96, s55\n"
    "s_lshl_b32 s39, s96, 10\n"
    "s_andn2_b32 s63, s39, 0x1fff\n"
    "s_add_u32 s63, s63, s39\n"
    "s_add_u32 s63, s63, 241172480\n"
    "s_lshr_b32 s38, s63, 8\n"
    "s_xor_b32 s38, s38, s57\n"
    "s_and_b32 s38, s38, 0x2000\n"
    "s_or_b32 s63, s63, s38\n"
    "s_lshl_b32 s96, s96, 10\n"
    "buffer_load_dwordx4 v[96:99], v0, s[16:19], s63 offen sc1\n"
    "s_ff1_i32_b64 s62, s[66:67]\n"
    "s_bitset0_b64 s[66:67], s62\n"
    "s_lshl_b32 s97, s62, 5\n"
    "s_add_u32 s97, s97, s55\n"
    "s_lshl_b32 s39, s97, 10\n"
    "s_andn2_b32 s63, s39, 0x1fff\n"
    "s_add_u32 s63, s63, s39\n"
    "s_add_u32 s63, s63, 241172480\n"
    "s_lshr_b32 s38, s63, 8\n"
    "s_xor_b32 s38, s38, s57\n"
    "s_and_b32 s38, s38, 0x2000\n"
    "s_or_b32 s63, s63, s38\n"
    "s_lshl_b32 s97, s97, 10\n"
    "buffer_load_dwordx4 v[100:103], v0, s[16:19], s63 offen sc1\n"
    "s_waitcnt vmcnt(7)\n"
    "buffer_store_dwordx4 v[72:75], v0, s[24:27], s90 offen offset:0 sc1\n"
    "s_waitcnt vmcnt(7)\n"
    "buffer_store_dwordx4 v[76:79], v0, s[24:27], s91 offen offset:0 sc1\n"
    "s_waitcnt vmcnt(7)\n"
    "buffer_store_dwordx4 v[80:83], v0, s[24:27], s92 offen offset:0 sc1\n"
    "s_waitcnt vmcnt(7)\n"
    "buffer_store_dwordx4 v[84:87], v0, s[24:27], s93 offen offset:0 sc1\n"
    "s_waitcnt vmcnt(7)\n"
    "buffer_store_dwordx4 v[88:91], v0, s[24:27], s94 offen offset:0 sc1\n"
    "s_waitcnt vmcnt(7)\n"
    "buffer_store_dwordx4 v[92:95], v0, s[24:27], s95 offen offset:0 sc1\n"
    "s_waitcnt vmcnt(7)\n"
    "buffer_store_dwordx4 v[96:99], v0, s[24:27], s96 offen offset:0 sc1\n"
    "s_waitcnt vmcnt(7)\n"
    "buffer_store_dwordx4 v[100:103], v0, s[24:27], s97 offen offset:0 sc1\n"
    "s_branch LFL%=\n"
    "LFAIL%=:\n"
    "buffer_store_dword v6, off, s[16:19], s59 sc1\n"
    "LEND%=:\n"
    "s_endpgm\n"
    :
    : "{v0}"(hv0), "{v1}"(hv1), "{v2}"(hv2), "{v3}"(hv3), "{v4}"(hv4), "{v5}"(hv5), "{v6}"(hv6), "{v7}"(hv7), "{v8}"(hv8), "{v9}"(hv9), "{v10}"(hv10), "{v11}"(hv11), "{v12}"(hv12), "{v13}"(hv13), "{v14}"(hv14), "{v15}"(hv15), "{v16}"(hv16), "{v17}"(hv17), "{v18}"(hv18), "{v19}"(hv19), "{v20}"(hv20), "{v21}"(hv21)
    : "v255", "a255", "s101", "vcc", "scc", "memory");
  }
  __builtin_unreachable();
}
#define NPROBE 5
#define PACE_ARG
#define PACE (Pace{4100,2840,300,2550,3070,2048,60})
#define BIG_LDS 163840
#define SLPB 600u
#define SLPA 600u
#define WARM 0
#define XB 5
#define KSIZE 82000
#define BOOT_BYTES 4096
__device__ __forceinline__ v4u first_state(int f, const u16* mem0) {
  v4u m0 = (v4u){~0u, ~0u, ~0u, ~0u};
  if (f >= 0 && f < 512) {
    const int tk = (f >> 3) & 7, n7 = (f & 7) ^ (tk >= 4 ? 4 : 0);
    const u16* p = mem0 + tk * DM + 64 * (f >> 6) + 32 * (n7 >> 2) + (n7 & 3);
#pragma unroll
    for (int d = 0; d < 4; ++d) m0[d] = (unsigned)p[8 * d] | ((unsigned)p[8 * d + 4] << 16);
    m0[3] = min(m0[3], 0xfffffffeu);
  }
  return m0;
}
__device__ __forceinline__ unsigned choice(int w, unsigned* bt, char* lds) {
  if (w == 0) *(volatile unsigned*)(lds + 8192) = wait_word(bt + 640 + (blockIdx.x & 7) * 32);
  __syncthreads();
  return __builtin_amdgcn_readfirstlane(*(volatile unsigned*)(lds + 8192)) & 1u;
}
__device__ __forceinline__ void boot2(int n, int w, unsigned* bt, char* lds, char* ws, const u16* mem0, const u16* Wq, const u16* Wk, const u16* Wv) {
  const rsrc_t rs = __builtin_amdgcn_make_buffer_rsrc((void*)ws, 0, ZWS_BYTES, 0x00020000);
  const int lane = lane_id(), t = n * 256 + w * 64 + lane;
  const v4u EMPTY = (v4u){~0u, ~0u, ~0u, ~0u}, m0 = first_state(t, mem0);
#pragma unroll
  for (int m = 0; m < WARM; ++m) {
    const rsrc_t rw = __builtin_amdgcn_make_buffer_rsrc((void*)(m == 0 ? Wv : (m == 1 ? Wk : Wq)), 0, DM * HD * 2, 0x00020000);
#pragma unroll
    for (int k = 0; k < 16; ++k) dma16(rw, lds + w * 1024, t * 16, k * 262144);
  }
  const unsigned cb = choice(w, bt, lds), c2 = (cb << 13) ^ 0x2000u;
#pragma unroll
  for (unsigned k = 0; k < 2; ++k) { const unsigned o = (k * 16384 + t) * 16; st4c(rs, o < (Z_SEND - Z_X2P) / 2 ? blk(Z_X2P, o, c2) : OOR, EMPTY); }
#pragma unroll
  for (int k = 0; k < 8; ++k) st4c(rs, blk(Z_CH + (1 + (k >> 2)) * MBY(2), ((k & 3) * 16384 + t) * 16, c2), (k & 3) == 2 ? m0 : EMPTY);
  vm0();
  __syncthreads();
  if (w == 0) {
    unsigned o = 0u;
    if (lane == 0) o = __hip_atomic_fetch_add((gu32*)(bt + 96), 1u, RLX_AGENT);
    if (__builtin_amdgcn_readfirstlane(o) == 63u) {
      const unsigned v = wait_word(bt + 128);
      if (lane < 4) __hip_atomic_store((gu32*)(bt + 256 + lane * 32), v, RLX_AGENT);
    }
  }
}
__device__ __forceinline__ void boot(int n, int w, unsigned* bt, char* lds, char* ws, const u16* x, const u16* mem0) {
  const rsrc_t rs = __builtin_amdgcn_make_buffer_rsrc((void*)ws, 0, ZWS_BYTES, 0x00020000), rx = __builtin_amdgcn_make_buffer_rsrc((void*)x, 0, S_TOK * DM * 2, 0x00020000);
  const int lane = lane_id(), t = n * 256 + w * 64 + lane;
  if (w == 0 && n < 32) {
    unsigned tm[2] = {~0u, ~0u};
    for (int q = 0; q < NPROBE; ++q) {
      const unsigned long long t0 = __builtin_amdgcn_s_memtime();
      asm volatile("s_waitcnt lgkmcnt(0)" ::: "memory");
      st1c(rs, lane ? OOR : Z_PART + (n & 3) * MBY(2) + ((q ^ n) & 1) * 8192 + n * 128, 0u);
      vm0();
      tm[q & 1] = min(tm[q & 1], (unsigned)(__builtin_amdgcn_s_memtime() - t0));
    }
    const unsigned d = (unsigned)min(max((int)(tm[0] - tm[1]), -200), 200);
    unsigned long long o = 0ull;
    if (lane == 0) o = __hip_atomic_fetch_add((gu64*)bt, ((unsigned long long)d << 32) | 1ull, RLX_AGENT);
    if (__builtin_amdgcn_readfirstlane((unsigned)o) == 31u && lane < 8)
      __hip_atomic_store((gu32*)(bt + 640 + lane * 32), (int)(__builtin_amdgcn_readfirstlane((unsigned)(o >> 32)) + d) > 0 ? 3u : 2u, RLX_AGENT);
  }
  const int v = n < 32 ? (w ? n * 3 + w - 1 : 2048) : n * 4 + w - 32;
  v4u y[6];
#pragma unroll
  for (int k = 0; k < 6; ++k) y[k] = ld4(rx, (k * 352 + v) * 1024 + lane * 16);
  const v4u EMPTY = (v4u){~0u, ~0u, ~0u, ~0u};
  const v4u m0 = first_state(t - 8192, mem0);
#pragma unroll
  for (unsigned k = 0; k < 2; ++k) { const unsigned o = (k * 24576 + t) * 16; st4c(rs, o < Z_ZEND - Z_FLG ? Z_FLG + o : OOR, (v4u){0u, 0u, 0u, 0u}); }
#pragma unroll
  for (int k = 0; k < 6; ++k) y[k] = ln_row(y[k]);
  const unsigned cb = choice(w, bt, lds), c1 = cb << 13;
#pragma unroll
  for (int k = 0; k < 6; ++k) st4c(rs, k * 352 + v < S_TOK ? blk(Z_XN1, (k * 352 + v) * 1024, c1) + lane * 16 : OOR, y[k]);
#pragma unroll
  for (int k = 0; k < 3; ++k) { const unsigned o = (k * 24576 + t) * 16; st4c(rs, o < MBY(1) ? blk(Z_CH, o, c1) : OOR, k == 1 ? m0 : EMPTY); }
  vm0();
  __syncthreads();
  if (w == 0) {
    unsigned o = 0u;
    if (lane == 0) o = __hip_atomic_fetch_add((gu32*)(bt + 32), 1u, RLX_AGENT);
    if (__builtin_amdgcn_readfirstlane(o) == 95u && lane < 4) __hip_atomic_store((gu32*)(bt + 128 + lane * 32), 2u | cb, RLX_AGENT);
  }
}

__global__ __launch_bounds__(256, 1) void main_kernel(unsigned* bt, char* ws, const u16* x, const u16* mem0, const u16* Wq, const u16* Wk, const u16* Wv, const u16* Wo, const u16* w_r,
                                                      const u16* wg, const u16* wu, const u16* wd, u16* out PACE_ARG) {
  extern __shared__ char lds[];
  const int b = blockIdx.x, xc = SW && (b & 3) == 3 ? (b & 7) ^ 4 : b & 7, i = b >> 3, w = __builtin_amdgcn_readfirstlane(threadIdx.x >> 6);
  unsigned* rdy = bt + 128 + xc * 32;
  {
    const unsigned o = min((unsigned)((i & 3) * 256 + threadIdx.x) * 128u, (unsigned)KSIZE);
    asm volatile("s_mov_b32 m0, 163584\n s_getpc_b64 s[20:21]\n global_load_lds_dword %0, s[20:21]" :: "v"(o) : "s20", "s21", "memory");
  }
  const unsigned off = w == 1 && (xc == 0 || xc == 4 || xc == 5) ? clock_offset() : 0u;
  if (xc == 4 || xc == 5) {
    const unsigned t0 = (unsigned)__builtin_amdgcn_s_memrealtime();
    boot2(i * 2 + xc - 4, w, bt, lds, ws, mem0, Wq, Wk, Wv);
    while ((unsigned)__builtin_amdgcn_s_memrealtime() - t0 < (xc == 4 ? SLPB : SLPA)) __builtin_amdgcn_s_sleep(2);
  }
  if (xc == 0 || xc == XB) chain8<false>(w, i, xc >> 2, off, rdy, ws, lds, Wq, Wk, Wv, Wo, PACE);
  if (xc == 9 - XB) chain8<true>(w, i, 2, off, rdy, ws, lds, Wq, Wk, Wv, Wo, PACE);
  if ((b & 7) <= 3) boot(xc < 3 ? 32 + i * 2 + xc - 1 : i, w, bt, lds, ws, x, mem0);
  if (xc <= 3) t_role8(w, xc - 1, i, bt, ws, xc == 1 ? Wk : (xc == 2 ? Wv : Wq));
  if ((i >> 3) != 2) o_role8(w, i >= 16 ? 1 : (xc == 7 ? 0 : 2), i >= 16 ? (i & 7) + (xc & 1) * 8 : i, rdy, ws, Wo);
  if (xc == 7) l_role8<true>(w, i - 16, rdy, ws, x, w_r, wg, wu, wd, out);
  l_role8<false>(w, i - 16, rdy, ws, x, w_r, wg, wu, wd, out);
}
extern "C" void kernel_launch(void* const* d_in, const int* in_sizes, int n_in, void* d_out, int out_size,
                              void* d_ws, size_t ws_size, hipStream_t stream) {
  char* ws = (char*)(((size_t)d_ws + BOOT_BYTES + MBY(2) - 1) & ~(size_t)(MBY(2) - 1));
  (void)hipFuncSetAttribute((const void*)main_kernel, hipFuncAttributeMaxDynamicSharedMemorySize, BIG_LDS);
  (void)hipMemsetAsync(d_ws, 0, BOOT_BYTES, stream);
  main_kernel<<<256, 256, BIG_LDS, stream>>>((unsigned*)d_ws, ws, (const u16*)d_in[0], (const u16*)d_in[1], (const u16*)d_in[2], (const u16*)d_in[3], (const u16*)d_in[4], (const u16*)d_in[5],
                                             (const u16*)d_in[8], (const u16*)d_in[9], (const u16*)d_in[10], (const u16*)d_in[11], (u16*)d_out);
}
```

```cpp
#include <hip/hip_runtime.h>
#include <hip/hip_bf16.h>
#include <stdint.h>

typedef unsigned short u16;
using bf16x8 = __attribute__((ext_vector_type(8))) short;
using f32x4 = __attribute__((ext_vector_type(4))) float;

#define S_TOK 2048
#define DM 512
#define NH 32
#define DH 128
#define HD 4096
#define MEMT 8
#define SEG 32
#define NSEG 64
#define FF 1792

__device__ __forceinline__ float bf2f(u16 v) { return __uint_as_float(((unsigned)v) << 16); }
__device__ __forceinline__ float rbf(float f) { return (float)(__bf16)f; }

template <int N> __device__ __forceinline__ float ror_add(float v) {
  return v + __int_as_float(__builtin_amdgcn_update_dpp(0, __float_as_int(v), 0x120 + N, 0xf, 0xf, false));
}

typedef unsigned v4u __attribute__((ext_vector_type(4)));
typedef unsigned v2u __attribute__((ext_vector_type(2)));
typedef unsigned v8u __attribute__((ext_vector_type(8)));
typedef __bf16 bfx8 __attribute__((ext_vector_type(8)));
typedef __bf16 bfx16 __attribute__((ext_vector_type(16)));
typedef short s16x4 __attribute__((ext_vector_type(4)));
typedef __attribute__((address_space(3))) s16x4 lds_s16x4;
#define AUX_SC1 16
#define SPINCAP (1u << 19)

__device__ __forceinline__ unsigned pk_bf16(float a, float b) {
  typedef __bf16 bfx2 __attribute__((ext_vector_type(2)));
  bfx2 v; v[0] = (__bf16)a; v[1] = (__bf16)b;
  return __builtin_bit_cast(unsigned, v);
}
__device__ __forceinline__ f32x4 mf32(v4u a, v4u b, f32x4 c) {
  return __builtin_amdgcn_mfma_f32_16x16x32_bf16(__builtin_bit_cast(bfx8, a), __builtin_bit_cast(bfx8, b), c, 0, 0, 0);
}
#define MBY(x) ((unsigned)(x) << 20)
#define SEGST 2621440u
#define Z_PART MBY(172)
#define Z_CH MBY(220)
#define Z_XN1 MBY(160)
#define Z_XN MBY(226)
#define Z_X2 MBY(230)
#define Z_X1 MBY(234)
#define Z_X3A MBY(242)
#define Z_FLG MBY(250)
#define Z_FT (Z_FLG)
#define Z_FO (Z_FT + 32u * 16384u)
#define Z_RS (Z_FO + 3u * 16384u)
#define Z_FP (Z_RS + 3u * 16384u)
#define Z_R1 (Z_FP + 3u * 16384u)
#define Z_DEC (Z_R1 + 16384u)
#define Z_ERR (Z_DEC + 16384u)
#define Z_ZEND (Z_ERR + 128u)
#define Z_X2P (MBY(251))
#define Z_HB (Z_X2P + 131072u)
#define Z_SEND (Z_HB + 33u * 16384u)
#define Z_DBG MBY(252)
#define ZWS_BYTES MBY(252)
#define OOR 0x80000000u
__host__ __device__ __forceinline__ unsigned nearof(unsigned o, unsigned c13) { return o | (((o >> 8) ^ c13) & 0x2000u); }
__host__ __device__ __forceinline__ unsigned blk(unsigned base, unsigned o, unsigned c13) { return nearof(base + (o >> 13 << 14), c13) + (o & 8191u); }
typedef __amdgpu_buffer_rsrc_t rsrc_t;
__device__ __forceinline__ v4u ld4(rsrc_t rs, unsigned off) { return __builtin_amdgcn_raw_buffer_load_b128(rs, off, 0, 0); }
__device__ __forceinline__ v4u ld4c(rsrc_t rs, unsigned off) { return __builtin_amdgcn_raw_buffer_load_b128(rs, off, 0, AUX_SC1); }
__device__ __forceinline__ v2u ld2c(rsrc_t rs, unsigned off) { return __builtin_amdgcn_raw_buffer_load_b64(rs, off, 0, AUX_SC1); }
__device__ __forceinline__ unsigned ld1c(rsrc_t rs, unsigned off) { return __builtin_amdgcn_raw_buffer_load_b32(rs, off, 0, AUX_SC1); }
__device__ __forceinline__ void st4c(rsrc_t rs, unsigned off, v4u v) { __builtin_amdgcn_raw_buffer_store_b128(v, rs, off, 0, AUX_SC1); }
__device__ __forceinline__ void st1c(rsrc_t rs, unsigned off, unsigned v) { __builtin_amdgcn_raw_buffer_store_b32(v, rs, off, 0, AUX_SC1); }
__device__ __forceinline__ v4u ld4s(rsrc_t rs, unsigned off, unsigned so) { return __builtin_amdgcn_raw_buffer_load_b128(rs, off, so, AUX_SC1); }
__device__ __forceinline__ void st4s(rsrc_t rs, unsigned off, unsigned so, v4u v) {
  __builtin_amdgcn_raw_buffer_store_b128(v, rs, off, so, AUX_SC1);
  asm volatile("s_nop 1" :: "v"(v) : "memory");
}
__device__ __forceinline__ void vm0() { asm volatile("s_waitcnt vmcnt(0)" ::: "memory"); }
#define XSTR 1040
#define PSLEEP 1
#define ZACC ((f32x4){0.f, 0.f, 0.f, 0.f})
__device__ __forceinline__ void mfw(bool first, f32x4& acc, const v4u& wa, const v4u& xb) {
  if (first) asm volatile("s_nop 1\n v_mfma_f32_16x16x32_bf16 %0, %1, %2, 0" : "=&v"(acc) : "a"(wa), "v"(xb));
  else asm volatile("s_nop 1\n v_mfma_f32_16x16x32_bf16 %0, %1, %2, %0" : "+v"(acc) : "a"(wa), "v"(xb));
}
__device__ __forceinline__ void mfence4(f32x4& a, f32x4& b, f32x4& c, f32x4& d) { asm volatile("s_nop 7\n s_nop 7" : "+v"(a), "+v"(b), "+v"(c), "+v"(d)); }
__device__ __forceinline__ void unpack8(v4u v, float (&x)[8]) {
#pragma unroll
  for (int i = 0; i < 4; ++i) { x[2 * i] = __uint_as_float(v[i] << 16); x[2 * i + 1] = __uint_as_float(v[i] & 0xffff0000u); }
}

__device__ __forceinline__ void mfxv(bool first, f32x4& acc, const v4u& xa, const v4u& wb) {
  if (first) asm volatile("s_nop 1\n v_mfma_f32_16x16x32_bf16 %0, %1, %2, 0" : "=&v"(acc) : "v"(xa), "v"(wb));
  else asm volatile("s_nop 1\n v_mfma_f32_16x16x32_bf16 %0, %1, %2, %0" : "+v"(acc) : "v"(xa), "v"(wb));
}
__device__ __forceinline__ int lane_id() { int l; asm volatile("v_mbcnt_lo_u32_b32 %0, -1, 0\n v_mbcnt_hi_u32_b32 %0, -1, %0" : "=v"(l)); return l; }
typedef __attribute__((address_space(1))) unsigned gu32;
typedef __attribute__((address_space(1))) unsigned long long gu64;
#define RLX_AGENT __ATOMIC_RELAXED, __HIP_MEMORY_SCOPE_AGENT
__device__ __forceinline__ unsigned wait_word(unsigned* p) {
  unsigned v = 0u;
  for (unsigned it = 0; it < SPINCAP && v == 0u; ++it) { v = __builtin_amdgcn_readfirstlane(__hip_atomic_load((gu32*)p, RLX_AGENT)); if (v == 0u) __builtin_amdgcn_s_sleep(2); }
  return v;
}
#define CL_K 122880
#define CL_Q (CL_K + 16384)
#define CL_MEM (CL_K + 24576)
#define CL_QX (CL_K + 32768)
#define CL_SEL (CL_QX + 2560 + 256)
#define CL_VX (CL_MEM + 2304)
#define CL_PX (CL_MEM + 4352)
#define CL_OX CL_QX
__device__ __forceinline__ v4u mkrs(const void* p, unsigned bytes) {
  return (v4u){(unsigned)(size_t)p, (unsigned)((size_t)p >> 32) & 0xffffu, bytes, 0x00020000u};
}
__device__ __forceinline__ unsigned clock_offset() {
  unsigned first = 0u, sum = 0u;
  for (int e = 0; e < 8; ++e) {
    unsigned long long r0, r, m;
    asm volatile("s_memrealtime %0\n s_waitcnt lgkmcnt(0)\n 1:\n s_memrealtime %1\n s_memtime %2\n s_waitcnt lgkmcnt(0)\n s_cmp_eq_u64 %0, %1\n s_cbranch_scc1 1b" : "=&s"(r0), "=&s"(r), "=&s"(m) :: "scc", "memory");
    const unsigned o = (unsigned)m - (unsigned)(r * 381ull >> 4);
    if (e == 0) first = o;
    sum += o - first;
  }
  return first + (unsigned)((int)sum >> 3);
}
struct Pace { int per, gat, late, rsd, fck, slip, xl; };
template <bool RST>
__device__ __forceinline__ void chain8(int w, int h, int V, unsigned off, const unsigned* bt, const unsigned* rdy, char* ws, char* lds, const u16* Wq, const u16* Wk, const u16* Wv, const u16* Wo, const Pace tm) {
  const int lane = lane_id(), c = lane & 15, g = lane >> 4, l16 = lane * 16;
  const unsigned c1 = 0u, cm = V ? c1 ^ 0x2000u : c1, ps = nearof(Z_CH + V * MBY(2), cm), mo = ps + MBY(1);
  const int gp = (h >> 1) & 3, sp = (h >> 3) * 2 + (h & 1);
  if (w == 0) {
    const unsigned one = (c & 1) ? 0x3f800000u : 0x3f80u;
    *(v4u*)(lds + CL_SEL + l16) = (v4u){(c >> 1) == 0 ? one : 0u, (c >> 1) == 1 ? one : 0u, (c >> 1) == 2 ? one : 0u, (c >> 1) == 3 ? one : 0u};
  }
  const int tk = 2 * (c >> 2) + (c & 1), hf = (c >> 1) & 1, j = 8 * (w >> 1) + (lane >> 3), q = (lane >> 2) & 3, p = lane & 3;
  const int pv[43] = {w, (int)ps, h * 16384, (int)mo, h * 16384 + w * 2048, (int)cm, (V == 1 ? 4 : 3) * 524288 + h * 16384 + (w & 1) * 4096 + (w >> 1) * 256, (int)cm,
                      (int)((nearof(Z_FO + V * 16384, cm) + h * 256) | OOR), (int)nearof(Z_FT + h * 16384, cm), (int)nearof(Z_RS + V * 16384, cm),
                      (int)(nearof(Z_CH + MBY(2), cm) + MBY(1)), -1, (int)nearof(Z_X2P, cm), 0, h * 4, (int)nearof(Z_RS + (V == 2 ? 1 : V) * 16384, cm), (int)off, (int)Z_ERR, (int)(Z_DBG + (V * 32 + h) * 4096),
                      (int)(size_t)Wq, (int)((size_t)Wq >> 32) & 0xffff, (int)(size_t)Wk, (int)((size_t)Wk >> 32) & 0xffff, (int)(size_t)Wv, (int)((size_t)Wv >> 32) & 0xffff, (int)(size_t)Wo, (int)((size_t)Wo >> 32) & 0xffff,
                      (int)(size_t)ws, (int)((size_t)ws >> 32) & 0xffff, (int)ZWS_BYTES, tm.per, h < 16 ? tm.rsd : tm.gat, -tm.late, 264 - min(max(tm.late - 230, 0) / 8, 63) * 4, tm.fck, (int)(size_t)rdy, (int)((size_t)rdy >> 32), tm.slip, tm.xl,
                      (int)(size_t)bt, (int)((size_t)bt >> 32), (int)blockIdx.x * 128 + (V ? 4 : 0)};
  int hv0 = l16, hv1 = hf ? 0xEEEE : 0x4444, hv2 = CL_MEM + tk * 128 + ((2 * g + hf) ^ (c >= 8 ? 4 : 0)) * 16;
  int hv3 = CL_QX + 2 * g * 288 + (32 * w + 2 * c) * 2, hv4 = hv3 - CL_QX + CL_MEM, hv5 = CL_QX + (c & 7) * 288 + g * 16, hv6 = CL_MEM + (2 * ((c >> 2) ^ (c & 2)) + (c & 1)) * 288 + g * 16;
  int hv7 = w * 30720 + l16, hv8 = CL_PX + w * 512 + g * 128 + (c & 7) * 16, hv9 = CL_K + w * 2048 + l16, hv10 = CL_VX + w * 512 + lane * 8, hv11 = CL_OX + (4 * w + g) * 128 + (c & 7) * 16, hv12 = CL_OX + (2 * g + hf) * 128 + tk * 16;
  int hv13 = j * 16384 + (w & 1) * 4096 + ((h + j) & 31) * 128 + (lane & 7) * 16;
  int hv14 = w ? CL_Q + (w & 1) * 4096 + l16 : ((lane & 0x28) ? OOR : sp * 1024 + (2 * gp + g) * 128 + ((lane & 7) ^ (gp >= 2 ? 4 : 0)) * 16), hv15 = CL_PX + (c >> 3) * 1024 + g * 128 + (c & 7) * 16, hv20 = 0;
  int hv16 = (w * 16 + g) * 8192 + h * 256 + (c ^ ((g & 1) * 2)) * 16, hv17 = (h * 128 + 16 * (w & 1) + 8 * (w >> 1)) * 1024 + l16;
  int hv18 = (g & 1) * 4480 + (q & 1) * 1088 + (2 * (g >> 1) + (q >> 1)) * 256 + 64 * w + 32 * (q >> 1) + 8 * p, hv19 = (lane >> 2) * 1040 + 256 * (w >> 1) + 32 * (w & 1) + 64 * p;
#pragma unroll
  for (int i = 0; i < 43; ++i) hv20 = lane == i ? pv[i] : hv20;
  if constexpr (RST) {
  asm volatile(
    "s_mov_b32 s33, 0\n"
    "s_nop 1\n"
    "v_mov_b32 v119, v20\n"
    "s_nop 0\n"
    "v_readlane_b32 s61, v119, 0\n"
    "v_readlane_b32 s46, v119, 1\n"
    "v_readlane_b32 s63, v119, 2\n"
    "v_readlane_b32 s48, v119, 3\n"
    "v_readlane_b32 s55, v119, 4\n"
    "v_readlane_b32 s56, v119, 5\n"
    "v_readlane_b32 s57, v119, 6\n"
    "v_readlane_b32 s74, v119, 7\n"
    "v_readlane_b32 s58, v119, 8\n"
    "v_readlane_b32 s59, v119, 9\n"
    "v_readlane_b32 s60, v119, 10\n"
    "v_readlane_b32 s67, v119, 11\n"
    "v_readlane_b32 s1, v119, 12\n"
    "v_readlane_b32 s69, v119, 13\n"
    "v_readlane_b32 s70, v119, 14\n"
    "v_readlane_b32 s71, v119, 15\n"
    "v_readlane_b32 s73, v119, 16\n"
    "v_readlane_b32 s45, v119, 17\n"
    "v_readlane_b32 s92, v119, 36\n"
    "v_readlane_b32 s93, v119, 37\n"
    "v_readlane_b32 s35, v119, 18\n"
    "v_readlane_b32 s75, v119, 19\n"
    "v_readlane_b32 s16, v119, 28\n"
    "v_readlane_b32 s17, v119, 29\n"
    "v_readlane_b32 s18, v119, 30\n"
    "v_readlane_b32 s8, v119, 31\n"
    "v_readlane_b32 s9, v119, 32\n"
    "v_readlane_b32 s10, v119, 33\n"
    "v_readlane_b32 s0, v119, 34\n"
    "v_readlane_b32 s12, v119, 35\n"
    "v_readlane_b32 s13, v119, 38\n"
    "v_readlane_b32 s21, v119, 39\n"
    "v_readlane_b32 s94, v119, 40\n"
    "v_readlane_b32 s95, v119, 41\n"
    "v_readlane_b32 s52, v119, 42\n"
    "s_mov_b32 s19, 0x20000\n"
    "v_readlane_b32 s84, v119, 20\n"
    "v_readlane_b32 s85, v119, 21\n"
    "v_readlane_b32 s86, v119, 22\n"
    "v_readlane_b32 s87, v119, 23\n"
    "v_readlane_b32 s88, v119, 24\n"
    "v_readlane_b32 s89, v119, 25\n"
    "v_readlane_b32 s90, v119, 26\n"
    "v_readlane_b32 s91, v119, 27\n"
    "s_mov_b32 s76, 0xff00ff00\n"
    "s_mov_b32 s77, 0xff00ff00\n"
    "s_mov_b32 s78, 0x00ff00ff\n"
    "s_mov_b32 s79, 0x00ff00ff\n"
    "s_mov_b32 s65, 0x3e0293ee\n"
    "s_mov_b32 s66, 0xfffefffe\n"
    "s_lshl_b32 s62, s61, 11\n"
    "s_lshl_b32 s11, s61, 10\n"
    "s_mov_b32 s36, 0\n"
    "s_mov_b32 s64, 0\n"
    "s_mov_b32 s50, 0\n"
    "s_lshl_b32 s72, s61, 2\n"
    "s_add_u32 s72, s72, 158208\n"
    "s_add_u32 s47, s46, 0x40000\n"
    "s_and_b32 s20, s61, 1\n"
    "s_lshl_b32 s20, s20, 7\n"
    "s_mov_b32 s14, 0x3f803f80\n"
    "s_mov_b32 s15, 0x3f803f80\n"
    "s_mov_b32 s82, 0x400000\n"
    "s_mov_b32 s83, 0x20000\n"
    "v_mov_b32 v112, v16\n"
    "v_mov_b32 v113, v17\n"
    "v_mov_b32 v114, v18\n"
    "v_mov_b32 v115, v19\n"
    "v_mov_b32 v117, 0\n"
    "v_mov_b32 v118, 0\n"
    "s_cmp_eq_u32 s61, 1\n"
    "s_mov_b32 s80, s90\n"
    "s_mov_b32 s81, s91\n"
    "s_cbranch_scc1 LCG%=\n"
    "s_cmp_lg_u32 s61, 0\n"
    "s_cbranch_scc1 LCF0_%=\n"
    "s_mov_b32 s80, s90\n"
    "s_mov_b32 s81, s91\n"
    "s_mul_i32 s38, s61, 4160\n"
    "s_add_u32 m0, s38, 4160\n"
    "s_mov_b32 s39, 16384\n"
    "buffer_load_dwordx4 v113, s[80:83], s39 offen lds\n"
    "s_add_u32 m0, s38, 5200\n"
    "s_mov_b32 s39, 17408\n"
    "buffer_load_dwordx4 v113, s[80:83], s39 offen lds\n"
    "s_add_u32 m0, s38, 6240\n"
    "s_mov_b32 s39, 20480\n"
    "buffer_load_dwordx4 v113, s[80:83], s39 offen lds\n"
    "s_add_u32 m0, s38, 7280\n"
    "s_mov_b32 s39, 21504\n"
    "buffer_load_dwordx4 v113, s[80:83], s39 offen lds\n"
    "LCF0_%=:\n"
    "s_mov_b32 s80, s90\n"
    "s_mov_b32 s81, s91\n"
    "s_mul_i32 s38, s61, 4160\n"
    "s_add_u32 m0, s38, 0\n"
    "s_mov_b32 s39, 0\n"
    "buffer_load_dwordx4 v113, s[80:83], s39 offen lds\n"
    "s_add_u32 m0, s38, 1040\n"
    "s_mov_b32 s39, 1024\n"
    "buffer_load_dwordx4 v113, s[80:83], s39 offen lds\n"
    "s_add_u32 m0, s38, 2080\n"
    "s_mov_b32 s39, 4096\n"
    "buffer_load_dwordx4 v113, s[80:83], s39 offen lds\n"
    "s_add_u32 m0, s38, 3120\n"
    "s_mov_b32 s39, 5120\n"
    "buffer_load_dwordx4 v113, s[80:83], s39 offen lds\n"
    "s_cmp_lg_u32 s61, 0\n"
    "s_cbranch_scc1 LCF1_%=\n"
    "s_mul_i32 s38, s61, 4160\n"
    "s_add_u32 m0, s38, 22080\n"
    "s_mov_b32 s39, 18432\n"
    "buffer_load_dwordx4 v113, s[80:83], s39 offen lds\n"
    "s_add_u32 m0, s38, 23120\n"
    "s_mov_b32 s39, 19456\n"
    "buffer_load_dwordx4 v113, s[80:83], s39 offen lds\n"
    "s_add_u32 m0, s38, 24160\n"
    "s_mov_b32 s39, 22528\n"
    "buffer_load_dwordx4 v113, s[80:83], s39 offen lds\n"
    "s_add_u32 m0, s38, 25200\n"
    "s_mov_b32 s39, 23552\n"
    "buffer_load_dwordx4 v113, s[80:83], s39 offen lds\n"
    "LCF1_%=:\n"
    "s_mul_i32 s38, s61, 4160\n"
    "s_add_u32 m0, s38, 17920\n"
    "s_mov_b32 s39, 2048\n"
    "buffer_load_dwordx4 v113, s[80:83], s39 offen lds\n"
    "s_add_u32 m0, s38, 18960\n"
    "s_mov_b32 s39, 3072\n"
    "buffer_load_dwordx4 v113, s[80:83], s39 offen lds\n"
    "s_add_u32 m0, s38, 20000\n"
    "s_mov_b32 s39, 6144\n"
    "buffer_load_dwordx4 v113, s[80:83], s39 offen lds\n"
    "s_add_u32 m0, s38, 21040\n"
    "s_mov_b32 s39, 7168\n"
    "buffer_load_dwordx4 v113, s[80:83], s39 offen lds\n"
    "s_cmp_lg_u32 s61, 0\n"
    "s_cbranch_scc1 LCF2_%=\n"
    "s_mul_i32 s38, s61, 4160\n"
    "s_add_u32 m0, s38, 40000\n"
    "s_mov_b32 s39, 49152\n"
    "buffer_load_dwordx4 v113, s[80:83], s39 offen lds\n"
    "s_add_u32 m0, s38, 41040\n"
    "s_mov_b32 s39, 50176\n"
    "buffer_load_dwordx4 v113, s[80:83], s39 offen lds\n"
    "s_add_u32 m0, s38, 42080\n"
    "s_mov_b32 s39, 53248\n"
    "buffer_load_dwordx4 v113, s[80:83], s39 offen lds\n"
    "s_add_u32 m0, s38, 43120\n"
    "s_mov_b32 s39, 54272\n"
    "buffer_load_dwordx4 v113, s[80:83], s39 offen lds\n"
    "LCF2_%=:\n"
    "s_mul_i32 s38, s61, 4160\n"
    "s_add_u32 m0, s38, 35840\n"
    "s_mov_b32 s39, 32768\n"
    "buffer_load_dwordx4 v113, s[80:83], s39 offen lds\n"
    "s_add_u32 m0, s38, 36880\n"
    "s_mov_b32 s39, 33792\n"
    "buffer_load_dwordx4 v113, s[80:83], s39 offen lds\n"
    "s_add_u32 m0, s38, 37920\n"
    "s_mov_b32 s39, 36864\n"
    "buffer_load_dwordx4 v113, s[80:83], s39 offen lds\n"
    "s_add_u32 m0, s38, 38960\n"
    "s_mov_b32 s39, 37888\n"
    "buffer_load_dwordx4 v113, s[80:83], s39 offen lds\n"
    "s_cmp_lg_u32 s61, 0\n"
    "s_cbranch_scc1 LCF3_%=\n"
    "s_mul_i32 s38, s61, 4160\n"
    "s_add_u32 m0, s38, 57920\n"
    "s_mov_b32 s39, 51200\n"
    "buffer_load_dwordx4 v113, s[80:83], s39 offen lds\n"
    "s_add_u32 m0, s38, 58960\n"
    "s_mov_b32 s39, 52224\n"
    "buffer_load_dwordx4 v113, s[80:83], s39 offen lds\n"
    "s_add_u32 m0, s38, 60000\n"
    "s_mov_b32 s39, 55296\n"
    "buffer_load_dwordx4 v113, s[80:83], s39 offen lds\n"
    "s_add_u32 m0, s38, 61040\n"
    "s_mov_b32 s39, 56320\n"
    "buffer_load_dwordx4 v113, s[80:83], s39 offen lds\n"
    "LCF3_%=:\n"
    "s_mul_i32 s38, s61, 4160\n"
    "s_add_u32 m0, s38, 53760\n"
    "s_mov_b32 s39, 34816\n"
    "buffer_load_dwordx4 v113, s[80:83], s39 offen lds\n"
    "s_add_u32 m0, s38, 54800\n"
    "s_mov_b32 s39, 35840\n"
    "buffer_load_dwordx4 v113, s[80:83], s39 offen lds\n"
    "s_add_u32 m0, s38, 55840\n"
    "s_mov_b32 s39, 38912\n"
    "buffer_load_dwordx4 v113, s[80:83], s39 offen lds\n"
    "s_add_u32 m0, s38, 56880\n"
    "s_mov_b32 s39, 39936\n"
    "buffer_load_dwordx4 v113, s[80:83], s39 offen lds\n"
    "LCG%=:\n"
    "s_waitcnt vmcnt(12)\n"
    "s_barrier\n"
    "s_add_u32 s38, s52, 40960\n"
    "v_mov_b32 v119, s38\n"
    "global_store_dword v119, v118, s[94:95] sc1\n"
    "s_mul_i32 s38, s61, 4160\n"
    "s_add_u32 m0, s38, 71680\n"
    "s_mov_b32 s39, 65536\n"
    "buffer_load_dwordx4 v113, s[80:83], s39 offen lds\n"
    "s_add_u32 m0, s38, 72720\n"
    "s_mov_b32 s39, 66560\n"
    "buffer_load_dwordx4 v113, s[80:83], s39 offen lds\n"
    "s_add_u32 m0, s38, 73760\n"
    "s_mov_b32 s39, 69632\n"
    "buffer_load_dwordx4 v113, s[80:83], s39 offen lds\n"
    "s_add_u32 m0, s38, 74800\n"
    "s_mov_b32 s39, 70656\n"
    "buffer_load_dwordx4 v113, s[80:83], s39 offen lds\n"
    "v_add_u32 v116, 0, v115\n"
    "ds_read_b64_tr_b16 v[168:169], v116 offset:0\n"
    "ds_read_b64_tr_b16 v[184:185], v116 offset:8\n"
    "ds_read_b64_tr_b16 v[200:201], v116 offset:16\n"
    "ds_read_b64_tr_b16 v[216:217], v116 offset:24\n"
    "ds_read_b64_tr_b16 v[232:233], v116 offset:512\n"
    "ds_read_b64_tr_b16 v[248:249], v116 offset:520\n"
    "ds_read_b64_tr_b16 v[104:105], v116 offset:528\n"
    "ds_read_b64_tr_b16 v[32:33], v116 offset:536\n"
    "s_waitcnt lgkmcnt(0)\n"
    "s_waitcnt vmcnt(13)\n"
    "s_barrier\n"
    "s_mul_i32 s38, s61, 4160\n"
    "s_add_u32 m0, s38, 89600\n"
    "s_mov_b32 s39, 67584\n"
    "buffer_load_dwordx4 v113, s[80:83], s39 offen lds\n"
    "s_add_u32 m0, s38, 90640\n"
    "s_mov_b32 s39, 68608\n"
    "buffer_load_dwordx4 v113, s[80:83], s39 offen lds\n"
    "s_add_u32 m0, s38, 91680\n"
    "s_mov_b32 s39, 71680\n"
    "buffer_load_dwordx4 v113, s[80:83], s39 offen lds\n"
    "s_add_u32 m0, s38, 92720\n"
    "s_mov_b32 s39, 72704\n"
    "buffer_load_dwordx4 v113, s[80:83], s39 offen lds\n"
    "v_add_u32 v116, 17920, v115\n"
    "ds_read_b64_tr_b16 v[170:171], v116 offset:0\n"
    "ds_read_b64_tr_b16 v[186:187], v116 offset:8\n"
    "ds_read_b64_tr_b16 v[202:203], v116 offset:16\n"
    "ds_read_b64_tr_b16 v[218:219], v116 offset:24\n"
    "ds_read_b64_tr_b16 v[234:235], v116 offset:512\n"
    "ds_read_b64_tr_b16 v[250:251], v116 offset:520\n"
    "ds_read_b64_tr_b16 v[106:107], v116 offset:528\n"
    "ds_read_b64_tr_b16 v[34:35], v116 offset:536\n"
    "s_waitcnt lgkmcnt(0)\n"
    "s_waitcnt vmcnt(13)\n"
    "s_barrier\n"
    "s_mul_i32 s38, s61, 4160\n"
    "s_add_u32 m0, s38, 0\n"
    "s_mov_b32 s39, 98304\n"
    "buffer_load_dwordx4 v113, s[80:83], s39 offen lds\n"
    "s_add_u32 m0, s38, 1040\n"
    "s_mov_b32 s39, 99328\n"
    "buffer_load_dwordx4 v113, s[80:83], s39 offen lds\n"
    "s_add_u32 m0, s38, 2080\n"
    "s_mov_b32 s39, 102400\n"
    "buffer_load_dwordx4 v113, s[80:83], s39 offen lds\n"
    "s_add_u32 m0, s38, 3120\n"
    "s_mov_b32 s39, 103424\n"
    "buffer_load_dwordx4 v113, s[80:83], s39 offen lds\n"
    "v_add_u32 v116, 35840, v115\n"
    "ds_read_b64_tr_b16 v[172:173], v116 offset:0\n"
    "ds_read_b64_tr_b16 v[188:189], v116 offset:8\n"
    "ds_read_b64_tr_b16 v[204:205], v116 offset:16\n"
    "ds_read_b64_tr_b16 v[220:221], v116 offset:24\n"
    "ds_read_b64_tr_b16 v[236:237], v116 offset:512\n"
    "ds_read_b64_tr_b16 v[252:253], v116 offset:520\n"
    "ds_read_b64_tr_b16 v[108:109], v116 offset:528\n"
    "ds_read_b64_tr_b16 v[36:37], v116 offset:536\n"
    "s_waitcnt lgkmcnt(0)\n"
    "s_waitcnt vmcnt(13)\n"
    "s_barrier\n"
    "s_mul_i32 s38, s61, 4160\n"
    "s_add_u32 m0, s38, 17920\n"
    "s_mov_b32 s39, 100352\n"
    "buffer_load_dwordx4 v113, s[80:83], s39 offen lds\n"
    "s_add_u32 m0, s38, 18960\n"
    "s_mov_b32 s39, 101376\n"
    "buffer_load_dwordx4 v113, s[80:83], s39 offen lds\n"
    "s_add_u32 m0, s38, 20000\n"
    "s_mov_b32 s39, 104448\n"
    "buffer_load_dwordx4 v113, s[80:83], s39 offen lds\n"
    "s_add_u32 m0, s38, 21040\n"
    "s_mov_b32 s39, 105472\n"
    "buffer_load_dwordx4 v113, s[80:83], s39 offen lds\n"
    "v_add_u32 v116, 53760, v115\n"
    "ds_read_b64_tr_b16 v[174:175], v116 offset:0\n"
    "ds_read_b64_tr_b16 v[190:191], v116 offset:8\n"
    "ds_read_b64_tr_b16 v[206:207], v116 offset:16\n"
    "ds_read_b64_tr_b16 v[222:223], v116 offset:24\n"
    "ds_read_b64_tr_b16 v[238:239], v116 offset:512\n"
    "ds_read_b64_tr_b16 v[254:255], v116 offset:520\n"
    "ds_read_b64_tr_b16 v[110:111], v116 offset:528\n"
    "ds_read_b64_tr_b16 v[38:39], v116 offset:536\n"
    "s_waitcnt lgkmcnt(0)\n"
    "s_waitcnt vmcnt(12)\n"
    "s_barrier\n"
    "s_mov_b32 s80, s86\n"
    "s_mov_b32 s81, s87\n"
    "s_mul_i32 s38, s61, 4480\n"
    "s_add_u32 m0, s38, 35840\n"
    "s_mov_b32 s39, 0\n"
    "buffer_load_dwordx4 v112, s[80:83], s39 offen lds\n"
    "s_add_u32 m0, s38, 36928\n"
    "s_mov_b32 s39, 32768\n"
    "buffer_load_dwordx4 v112, s[80:83], s39 offen lds\n"
    "s_add_u32 m0, s38, 38016\n"
    "s_mov_b32 s39, 65536\n"
    "buffer_load_dwordx4 v112, s[80:83], s39 offen lds\n"
    "s_add_u32 m0, s38, 39104\n"
    "s_mov_b32 s39, 98304\n"
    "buffer_load_dwordx4 v112, s[80:83], s39 offen lds\n"
    "v_add_u32 v116, 71680, v115\n"
    "ds_read_b64_tr_b16 v[176:177], v116 offset:0\n"
    "ds_read_b64_tr_b16 v[192:193], v116 offset:8\n"
    "ds_read_b64_tr_b16 v[208:209], v116 offset:16\n"
    "ds_read_b64_tr_b16 v[224:225], v116 offset:24\n"
    "ds_read_b64_tr_b16 v[240:241], v116 offset:512\n"
    "ds_read_b64_tr_b16 v[96:97], v116 offset:520\n"
    "ds_read_b64_tr_b16 v[24:25], v116 offset:528\n"
    "ds_read_b64_tr_b16 v[40:41], v116 offset:536\n"
    "s_waitcnt lgkmcnt(0)\n"
    "s_waitcnt vmcnt(12)\n"
    "s_barrier\n"
    "s_mul_i32 s38, s61, 4480\n"
    "s_add_u32 m0, s38, 53760\n"
    "s_mov_b32 s39, 524288\n"
    "buffer_load_dwordx4 v112, s[80:83], s39 offen lds\n"
    "s_add_u32 m0, s38, 54848\n"
    "s_mov_b32 s39, 557056\n"
    "buffer_load_dwordx4 v112, s[80:83], s39 offen lds\n"
    "s_add_u32 m0, s38, 55936\n"
    "s_mov_b32 s39, 589824\n"
    "buffer_load_dwordx4 v112, s[80:83], s39 offen lds\n"
    "s_add_u32 m0, s38, 57024\n"
    "s_mov_b32 s39, 622592\n"
    "buffer_load_dwordx4 v112, s[80:83], s39 offen lds\n"
    "v_add_u32 v116, 89600, v115\n"
    "ds_read_b64_tr_b16 v[178:179], v116 offset:0\n"
    "ds_read_b64_tr_b16 v[194:195], v116 offset:8\n"
    "ds_read_b64_tr_b16 v[210:211], v116 offset:16\n"
    "ds_read_b64_tr_b16 v[226:227], v116 offset:24\n"
    "ds_read_b64_tr_b16 v[242:243], v116 offset:512\n"
    "ds_read_b64_tr_b16 v[98:99], v116 offset:520\n"
    "ds_read_b64_tr_b16 v[26:27], v116 offset:528\n"
    "ds_read_b64_tr_b16 v[42:43], v116 offset:536\n"
    "s_waitcnt lgkmcnt(0)\n"
    "s_waitcnt vmcnt(12)\n"
    "s_barrier\n"
    "s_mul_i32 s38, s61, 4480\n"
    "s_add_u32 m0, s38, 71680\n"
    "s_mov_b32 s39, 1048576\n"
    "buffer_load_dwordx4 v112, s[80:83], s39 offen lds\n"
    "s_add_u32 m0, s38, 72768\n"
    "s_mov_b32 s39, 1081344\n"
    "buffer_load_dwordx4 v112, s[80:83], s39 offen lds\n"
    "s_add_u32 m0, s38, 73856\n"
    "s_mov_b32 s39, 1114112\n"
    "buffer_load_dwordx4 v112, s[80:83], s39 offen lds\n"
    "s_add_u32 m0, s38, 74944\n"
    "s_mov_b32 s39, 1146880\n"
    "buffer_load_dwordx4 v112, s[80:83], s39 offen lds\n"
    "v_add_u32 v116, 0, v115\n"
    "ds_read_b64_tr_b16 v[180:181], v116 offset:0\n"
    "ds_read_b64_tr_b16 v[196:197], v116 offset:8\n"
    "ds_read_b64_tr_b16 v[212:213], v116 offset:16\n"
    "ds_read_b64_tr_b16 v[228:229], v116 offset:24\n"
    "ds_read_b64_tr_b16 v[244:245], v116 offset:512\n"
    "ds_read_b64_tr_b16 v[100:101], v116 offset:520\n"
    "ds_read_b64_tr_b16 v[28:29], v116 offset:528\n"
    "ds_read_b64_tr_b16 v[44:45], v116 offset:536\n"
    "s_waitcnt lgkmcnt(0)\n"
    "s_waitcnt vmcnt(12)\n"
    "s_barrier\n"
    "s_add_u32 s42, s94, 4096\n"
    "s_addc_u32 s43, s95, 0\n"
    "global_load_dword v119, v118, s[42:43] sc1\n"
    "s_mul_i32 s38, s61, 4480\n"
    "s_add_u32 m0, s38, 89600\n"
    "s_mov_b32 s39, 1572864\n"
    "buffer_load_dwordx4 v112, s[80:83], s39 offen lds\n"
    "s_add_u32 m0, s38, 90688\n"
    "s_mov_b32 s39, 1605632\n"
    "buffer_load_dwordx4 v112, s[80:83], s39 offen lds\n"
    "s_add_u32 m0, s38, 91776\n"
    "s_mov_b32 s39, 1638400\n"
    "buffer_load_dwordx4 v112, s[80:83], s39 offen lds\n"
    "s_add_u32 m0, s38, 92864\n"
    "s_mov_b32 s39, 1671168\n"
    "buffer_load_dwordx4 v112, s[80:83], s39 offen lds\n"
    "v_add_u32 v116, 17920, v115\n"
    "ds_read_b64_tr_b16 v[182:183], v116 offset:0\n"
    "ds_read_b64_tr_b16 v[198:199], v116 offset:8\n"
    "ds_read_b64_tr_b16 v[214:215], v116 offset:16\n"
    "ds_read_b64_tr_b16 v[230:231], v116 offset:24\n"
    "ds_read_b64_tr_b16 v[246:247], v116 offset:512\n"
    "ds_read_b64_tr_b16 v[102:103], v116 offset:520\n"
    "ds_read_b64_tr_b16 v[30:31], v116 offset:528\n"
    "ds_read_b64_tr_b16 v[46:47], v116 offset:536\n"
    "ds_write_b128 v9, v[24:27] offset:0\n"
    "s_waitcnt lgkmcnt(2)\n"
    "ds_write_b128 v9, v[28:31] offset:1024\n"
    "ds_write_b128 v9, v[32:35] offset:8192\n"
    "ds_write_b128 v9, v[36:39] offset:9216\n"
    "ds_write_b128 v9, v[40:43] offset:16384\n"
    "s_waitcnt lgkmcnt(5)\n"
    "ds_write_b128 v9, v[44:47] offset:17408\n"
    "s_waitcnt lgkmcnt(0)\n"
    "s_waitcnt vmcnt(13)\n"
    "s_barrier\n"
    "s_mul_i32 s38, s61, 4480\n"
    "s_add_u32 m0, s38, 0\n"
    "s_mov_b32 s39, 2097152\n"
    "buffer_load_dwordx4 v112, s[80:83], s39 offen lds\n"
    "s_add_u32 m0, s38, 1088\n"
    "s_mov_b32 s39, 2129920\n"
    "buffer_load_dwordx4 v112, s[80:83], s39 offen lds\n"
    "s_add_u32 m0, s38, 2176\n"
    "s_mov_b32 s39, 2162688\n"
    "buffer_load_dwordx4 v112, s[80:83], s39 offen lds\n"
    "s_add_u32 m0, s38, 3264\n"
    "s_mov_b32 s39, 2195456\n"
    "buffer_load_dwordx4 v112, s[80:83], s39 offen lds\n"
    "v_add_u32 v116, 35840, v114\n"
    "ds_read_b64_tr_b16 v[64:65], v116 offset:0\n"
    "ds_read_b64_tr_b16 v[66:67], v116 offset:2176\n"
    "ds_read_b64_tr_b16 v[68:69], v116 offset:8960\n"
    "ds_read_b64_tr_b16 v[70:71], v116 offset:11136\n"
    "v_xor_b32 v116, 32, v116\n"
    "ds_read_b64_tr_b16 v[72:73], v116 offset:0\n"
    "ds_read_b64_tr_b16 v[74:75], v116 offset:2176\n"
    "ds_read_b64_tr_b16 v[76:77], v116 offset:8960\n"
    "ds_read_b64_tr_b16 v[78:79], v116 offset:11136\n"
    "s_waitcnt lgkmcnt(0)\n"
    "s_waitcnt vmcnt(13)\n"
    "s_barrier\n"
    "s_mul_i32 s38, s61, 4480\n"
    "s_add_u32 m0, s38, 17920\n"
    "s_mov_b32 s39, 2621440\n"
    "buffer_load_dwordx4 v112, s[80:83], s39 offen lds\n"
    "s_add_u32 m0, s38, 19008\n"
    "s_mov_b32 s39, 2654208\n"
    "buffer_load_dwordx4 v112, s[80:83], s39 offen lds\n"
    "s_add_u32 m0, s38, 20096\n"
    "s_mov_b32 s39, 2686976\n"
    "buffer_load_dwordx4 v112, s[80:83], s39 offen lds\n"
    "s_add_u32 m0, s38, 21184\n"
    "s_mov_b32 s39, 2719744\n"
    "buffer_load_dwordx4 v112, s[80:83], s39 offen lds\n"
    "v_add_u32 v116, 53760, v114\n"
    "ds_read_b64_tr_b16 v[80:81], v116 offset:0\n"
    "ds_read_b64_tr_b16 v[82:83], v116 offset:2176\n"
    "ds_read_b64_tr_b16 v[84:85], v116 offset:8960\n"
    "ds_read_b64_tr_b16 v[86:87], v116 offset:11136\n"
    "v_xor_b32 v116, 32, v116\n"
    "ds_read_b64_tr_b16 v[88:89], v116 offset:0\n"
    "ds_read_b64_tr_b16 v[90:91], v116 offset:2176\n"
    "ds_read_b64_tr_b16 v[92:93], v116 offset:8960\n"
    "ds_read_b64_tr_b16 v[94:95], v116 offset:11136\n"
    "s_waitcnt lgkmcnt(0)\n"
    "s_waitcnt vmcnt(13)\n"
    "s_barrier\n"
    "s_mul_i32 s38, s61, 4480\n"
    "s_add_u32 m0, s38, 35840\n"
    "s_mov_b32 s39, 3145728\n"
    "buffer_load_dwordx4 v112, s[80:83], s39 offen lds\n"
    "s_add_u32 m0, s38, 36928\n"
    "s_mov_b32 s39, 3178496\n"
    "buffer_load_dwordx4 v112, s[80:83], s39 offen lds\n"
    "s_add_u32 m0, s38, 38016\n"
    "s_mov_b32 s39, 3211264\n"
    "buffer_load_dwordx4 v112, s[80:83], s39 offen lds\n"
    "s_add_u32 m0, s38, 39104\n"
    "s_mov_b32 s39, 3244032\n"
    "buffer_load_dwordx4 v112, s[80:83], s39 offen lds\n"
    "v_add_u32 v116, 71680, v114\n"
    "ds_read_b64_tr_b16 a[80:81], v116 offset:0\n"
    "ds_read_b64_tr_b16 a[82:83], v116 offset:2176\n"
    "ds_read_b64_tr_b16 a[84:85], v116 offset:8960\n"
    "ds_read_b64_tr_b16 a[86:87], v116 offset:11136\n"
    "v_xor_b32 v116, 32, v116\n"
    "ds_read_b64_tr_b16 a[88:89], v116 offset:0\n"
    "ds_read_b64_tr_b16 a[90:91], v116 offset:2176\n"
    "ds_read_b64_tr_b16 a[92:93], v116 offset:8960\n"
    "ds_read_b64_tr_b16 a[94:95], v116 offset:11136\n"
    "s_waitcnt lgkmcnt(0)\n"
    "s_waitcnt vmcnt(12)\n"
    "s_barrier\n"
    "s_mul_i32 s38, s61, 4480\n"
    "s_add_u32 m0, s38, 53760\n"
    "s_mov_b32 s39, 3670016\n"
    "buffer_load_dwordx4 v112, s[80:83], s39 offen lds\n"
    "s_add_u32 m0, s38, 54848\n"
    "s_mov_b32 s39, 3702784\n"
    "buffer_load_dwordx4 v112, s[80:83], s39 offen lds\n"
    "s_add_u32 m0, s38, 55936\n"
    "s_mov_b32 s39, 3735552\n"
    "buffer_load_dwordx4 v112, s[80:83], s39 offen lds\n"
    "s_add_u32 m0, s38, 57024\n"
    "s_mov_b32 s39, 3768320\n"
    "buffer_load_dwordx4 v112, s[80:83], s39 offen lds\n"
    "v_add_u32 v116, 89600, v114\n"
    "ds_read_b64_tr_b16 a[96:97], v116 offset:0\n"
    "ds_read_b64_tr_b16 a[98:99], v116 offset:2176\n"
    "ds_read_b64_tr_b16 a[100:101], v116 offset:8960\n"
    "ds_read_b64_tr_b16 a[102:103], v116 offset:11136\n"
    "v_xor_b32 v116, 32, v116\n"
    "ds_read_b64_tr_b16 a[104:105], v116 offset:0\n"
    "ds_read_b64_tr_b16 a[106:107], v116 offset:2176\n"
    "ds_read_b64_tr_b16 a[108:109], v116 offset:8960\n"
    "ds_read_b64_tr_b16 a[110:111], v116 offset:11136\n"
    "s_waitcnt lgkmcnt(0)\n"
    "s_waitcnt vmcnt(12)\n"
    "s_barrier\n"
    "s_mov_b32 s80, s84\n"
    "s_mov_b32 s81, s85\n"
    "s_mul_i32 s38, s61, 4480\n"
    "s_add_u32 m0, s38, 71680\n"
    "s_mov_b32 s39, 0\n"
    "buffer_load_dwordx4 v112, s[80:83], s39 offen lds\n"
    "s_add_u32 m0, s38, 72768\n"
    "s_mov_b32 s39, 32768\n"
    "buffer_load_dwordx4 v112, s[80:83], s39 offen lds\n"
    "s_add_u32 m0, s38, 73856\n"
    "s_mov_b32 s39, 65536\n"
    "buffer_load_dwordx4 v112, s[80:83], s39 offen lds\n"
    "s_add_u32 m0, s38, 74944\n"
    "s_mov_b32 s39, 98304\n"
    "buffer_load_dwordx4 v112, s[80:83], s39 offen lds\n"
    "v_add_u32 v116, 0, v114\n"
    "ds_read_b64_tr_b16 a[112:113], v116 offset:0\n"
    "ds_read_b64_tr_b16 a[114:115], v116 offset:2176\n"
    "ds_read_b64_tr_b16 a[116:117], v116 offset:8960\n"
    "ds_read_b64_tr_b16 a[118:119], v116 offset:11136\n"
    "v_xor_b32 v116, 32, v116\n"
    "ds_read_b64_tr_b16 a[120:121], v116 offset:0\n"
    "ds_read_b64_tr_b16 a[122:123], v116 offset:2176\n"
    "ds_read_b64_tr_b16 a[124:125], v116 offset:8960\n"
    "ds_read_b64_tr_b16 a[126:127], v116 offset:11136\n"
    "s_waitcnt lgkmcnt(0)\n"
    "s_waitcnt vmcnt(12)\n"
    "s_barrier\n"
    "s_add_u32 s38, s52, 8192\n"
    "s_add_u32 s42, s94, s38\n"
    "s_addc_u32 s43, s95, 0\n"
    "global_store_dword v118, v119, s[42:43] sc1\n"
    "s_mul_i32 s38, s61, 4480\n"
    "s_add_u32 m0, s38, 89600\n"
    "s_mov_b32 s39, 524288\n"
    "buffer_load_dwordx4 v112, s[80:83], s39 offen lds\n"
    "s_add_u32 m0, s38, 90688\n"
    "s_mov_b32 s39, 557056\n"
    "buffer_load_dwordx4 v112, s[80:83], s39 offen lds\n"
    "s_add_u32 m0, s38, 91776\n"
    "s_mov_b32 s39, 589824\n"
    "buffer_load_dwordx4 v112, s[80:83], s39 offen lds\n"
    "s_add_u32 m0, s38, 92864\n"
    "s_mov_b32 s39, 622592\n"
    "buffer_load_dwordx4 v112, s[80:83], s39 offen lds\n"
    "v_add_u32 v116, 17920, v114\n"
    "ds_read_b64_tr_b16 a[128:129], v116 offset:0\n"
    "ds_read_b64_tr_b16 a[130:131], v116 offset:2176\n"
    "ds_read_b64_tr_b16 a[132:133], v116 offset:8960\n"
    "ds_read_b64_tr_b16 a[134:135], v116 offset:11136\n"
    "v_xor_b32 v116, 32, v116\n"
    "ds_read_b64_tr_b16 a[136:137], v116 offset:0\n"
    "ds_read_b64_tr_b16 a[138:139], v116 offset:2176\n"
    "ds_read_b64_tr_b16 a[140:141], v116 offset:8960\n"
    "ds_read_b64_tr_b16 a[142:143], v116 offset:11136\n"
    "s_waitcnt lgkmcnt(0)\n"
    "s_waitcnt vmcnt(13)\n"
    "s_barrier\n"
    "s_mul_i32 s38, s61, 4480\n"
    "s_add_u32 m0, s38, 0\n"
    "s_mov_b32 s39, 1048576\n"
    "buffer_load_dwordx4 v112, s[80:83], s39 offen lds\n"
    "s_add_u32 m0, s38, 1088\n"
    "s_mov_b32 s39, 1081344\n"
    "buffer_load_dwordx4 v112, s[80:83], s39 offen lds\n"
    "s_add_u32 m0, s38, 2176\n"
    "s_mov_b32 s39, 1114112\n"
    "buffer_load_dwordx4 v112, s[80:83], s39 offen lds\n"
    "s_add_u32 m0, s38, 3264\n"
    "s_mov_b32 s39, 1146880\n"
    "buffer_load_dwordx4 v112, s[80:83], s39 offen lds\n"
    "v_add_u32 v116, 35840, v114\n"
    "ds_read_b64_tr_b16 a[144:145], v116 offset:0\n"
    "ds_read_b64_tr_b16 a[146:147], v116 offset:2176\n"
    "ds_read_b64_tr_b16 a[148:149], v116 offset:8960\n"
    "ds_read_b64_tr_b16 a[150:151], v116 offset:11136\n"
    "v_xor_b32 v116, 32, v116\n"
    "ds_read_b64_tr_b16 a[152:153], v116 offset:0\n"
    "ds_read_b64_tr_b16 a[154:155], v116 offset:2176\n"
    "ds_read_b64_tr_b16 a[156:157], v116 offset:8960\n"
    "ds_read_b64_tr_b16 a[158:159], v116 offset:11136\n"
    "s_waitcnt lgkmcnt(0)\n"
    "s_waitcnt vmcnt(13)\n"
    "s_barrier\n"
    "s_mul_i32 s38, s61, 4480\n"
    "s_add_u32 m0, s38, 17920\n"
    "s_mov_b32 s39, 1572864\n"
    "buffer_load_dwordx4 v112, s[80:83], s39 offen lds\n"
    "s_add_u32 m0, s38, 19008\n"
    "s_mov_b32 s39, 1605632\n"
    "buffer_load_dwordx4 v112, s[80:83], s39 offen lds\n"
    "s_add_u32 m0, s38, 20096\n"
    "s_mov_b32 s39, 1638400\n"
    "buffer_load_dwordx4 v112, s[80:83], s39 offen lds\n"
    "s_add_u32 m0, s38, 21184\n"
    "s_mov_b32 s39, 1671168\n"
    "buffer_load_dwordx4 v112, s[80:83], s39 offen lds\n"
    "v_add_u32 v116, 53760, v114\n"
    "ds_read_b64_tr_b16 a[160:161], v116 offset:0\n"
    "ds_read_b64_tr_b16 a[162:163], v116 offset:2176\n"
    "ds_read_b64_tr_b16 a[164:165], v116 offset:8960\n"
    "ds_read_b64_tr_b16 a[166:167], v116 offset:11136\n"
    "v_xor_b32 v116, 32, v116\n"
    "ds_read_b64_tr_b16 a[168:169], v116 offset:0\n"
    "ds_read_b64_tr_b16 a[170:171], v116 offset:2176\n"
    "ds_read_b64_tr_b16 a[172:173], v116 offset:8960\n"
    "ds_read_b64_tr_b16 a[174:175], v116 offset:11136\n"
    "s_waitcnt lgkmcnt(0)\n"
    "s_waitcnt vmcnt(13)\n"
    "s_barrier\n"
    "s_mul_i32 s38, s61, 4480\n"
    "s_add_u32 m0, s38, 35840\n"
    "s_mov_b32 s39, 2097152\n"
    "buffer_load_dwordx4 v112, s[80:83], s39 offen lds\n"
    "s_add_u32 m0, s38, 36928\n"
    "s_mov_b32 s39, 2129920\n"
    "buffer_load_dwordx4 v112, s[80:83], s39 offen lds\n"
    "s_add_u32 m0, s38, 38016\n"
    "s_mov_b32 s39, 2162688\n"
    "buffer_load_dwordx4 v112, s[80:83], s39 offen lds\n"
    "s_add_u32 m0, s38, 39104\n"
    "s_mov_b32 s39, 2195456\n"
    "buffer_load_dwordx4 v112, s[80:83], s39 offen lds\n"
    "v_add_u32 v116, 71680, v114\n"
    "ds_read_b64_tr_b16 v[16:17], v116 offset:0\n"
    "ds_read_b64_tr_b16 v[18:19], v116 offset:2176\n"
    "ds_read_b64_tr_b16 v[20:21], v116 offset:8960\n"
    "ds_read_b64_tr_b16 v[22:23], v116 offset:11136\n"
    "v_xor_b32 v116, 32, v116\n"
    "ds_read_b64_tr_b16 v[24:25], v116 offset:0\n"
    "ds_read_b64_tr_b16 v[26:27], v116 offset:2176\n"
    "ds_read_b64_tr_b16 v[28:29], v116 offset:8960\n"
    "ds_read_b64_tr_b16 v[30:31], v116 offset:11136\n"
    "s_waitcnt lgkmcnt(0)\n"
    "s_waitcnt vmcnt(12)\n"
    "s_barrier\n"
    "s_mul_i32 s38, s61, 4480\n"
    "s_add_u32 m0, s38, 53760\n"
    "s_mov_b32 s39, 2621440\n"
    "buffer_load_dwordx4 v112, s[80:83], s39 offen lds\n"
    "s_add_u32 m0, s38, 54848\n"
    "s_mov_b32 s39, 2654208\n"
    "buffer_load_dwordx4 v112, s[80:83], s39 offen lds\n"
    "s_add_u32 m0, s38, 55936\n"
    "s_mov_b32 s39, 2686976\n"
    "buffer_load_dwordx4 v112, s[80:83], s39 offen lds\n"
    "s_add_u32 m0, s38, 57024\n"
    "s_mov_b32 s39, 2719744\n"
    "buffer_load_dwordx4 v112, s[80:83], s39 offen lds\n"
    "v_add_u32 v116, 89600, v114\n"
    "ds_read_b64_tr_b16 v[32:33], v116 offset:0\n"
    "ds_read_b64_tr_b16 v[34:35], v116 offset:2176\n"
    "ds_read_b64_tr_b16 v[36:37], v116 offset:8960\n"
    "ds_read_b64_tr_b16 v[38:39], v116 offset:11136\n"
    "v_xor_b32 v116, 32, v116\n"
    "ds_read_b64_tr_b16 v[40:41], v116 offset:0\n"
    "ds_read_b64_tr_b16 v[42:43], v116 offset:2176\n"
    "ds_read_b64_tr_b16 v[44:45], v116 offset:8960\n"
    "ds_read_b64_tr_b16 v[46:47], v116 offset:11136\n"
    "s_waitcnt lgkmcnt(0)\n"
    "s_waitcnt vmcnt(12)\n"
    "s_barrier\n"
    "s_mul_i32 s38, s61, 4480\n"
    "s_add_u32 m0, s38, 71680\n"
    "s_mov_b32 s39, 3145728\n"
    "buffer_load_dwordx4 v112, s[80:83], s39 offen lds\n"
    "s_add_u32 m0, s38, 72768\n"
    "s_mov_b32 s39, 3178496\n"
    "buffer_load_dwordx4 v112, s[80:83], s39 offen lds\n"
    "s_add_u32 m0, s38, 73856\n"
    "s_mov_b32 s39, 3211264\n"
    "buffer_load_dwordx4 v112, s[80:83], s39 offen lds\n"
    "s_add_u32 m0, s38, 74944\n"
    "s_mov_b32 s39, 3244032\n"
    "buffer_load_dwordx4 v112, s[80:83], s39 offen lds\n"
    "v_add_u32 v116, 0, v114\n"
    "ds_read_b64_tr_b16 v[48:49], v116 offset:0\n"
    "ds_read_b64_tr_b16 v[50:51], v116 offset:2176\n"
    "ds_read_b64_tr_b16 v[52:53], v116 offset:8960\n"
    "ds_read_b64_tr_b16 v[54:55], v116 offset:11136\n"
    "v_xor_b32 v116, 32, v116\n"
    "ds_read_b64_tr_b16 v[56:57], v116 offset:0\n"
    "ds_read_b64_tr_b16 v[58:59], v116 offset:2176\n"
    "ds_read_b64_tr_b16 v[60:61], v116 offset:8960\n"
    "ds_read_b64_tr_b16 v[62:63], v116 offset:11136\n"
    "s_waitcnt lgkmcnt(0)\n"
    "s_waitcnt vmcnt(12)\n"
    "s_barrier\n"
    "s_mul_i32 s38, s61, 4480\n"
    "s_add_u32 m0, s38, 89600\n"
    "s_mov_b32 s39, 3670016\n"
    "buffer_load_dwordx4 v112, s[80:83], s39 offen lds\n"
    "s_add_u32 m0, s38, 90688\n"
    "s_mov_b32 s39, 3702784\n"
    "buffer_load_dwordx4 v112, s[80:83], s39 offen lds\n"
    "s_add_u32 m0, s38, 91776\n"
    "s_mov_b32 s39, 3735552\n"
    "buffer_load_dwordx4 v112, s[80:83], s39 offen lds\n"
    "s_add_u32 m0, s38, 92864\n"
    "s_mov_b32 s39, 3768320\n"
    "buffer_load_dwordx4 v112, s[80:83], s39 offen lds\n"
    "v_add_u32 v116, 17920, v114\n"
    "ds_read_b64_tr_b16 a[0:1], v116 offset:0\n"
    "ds_read_b64_tr_b16 a[2:3], v116 offset:2176\n"
    "ds_read_b64_tr_b16 a[4:5], v116 offset:8960\n"
    "ds_read_b64_tr_b16 a[6:7], v116 offset:11136\n"
    "v_xor_b32 v116, 32, v116\n"
    "ds_read_b64_tr_b16 a[8:9], v116 offset:0\n"
    "ds_read_b64_tr_b16 a[10:11], v116 offset:2176\n"
    "ds_read_b64_tr_b16 a[12:13], v116 offset:8960\n"
    "ds_read_b64_tr_b16 a[14:15], v116 offset:11136\n"
    "s_waitcnt lgkmcnt(0)\n"
    "s_waitcnt vmcnt(12)\n"
    "s_barrier\n"
    "s_mov_b32 s80, s88\n"
    "s_mov_b32 s81, s89\n"
    "s_mul_i32 s38, s61, 4480\n"
    "s_add_u32 m0, s38, 0\n"
    "s_mov_b32 s39, 0\n"
    "buffer_load_dwordx4 v112, s[80:83], s39 offen lds\n"
    "s_add_u32 m0, s38, 1088\n"
    "s_mov_b32 s39, 32768\n"
    "buffer_load_dwordx4 v112, s[80:83], s39 offen lds\n"
    "s_add_u32 m0, s38, 2176\n"
    "s_mov_b32 s39, 65536\n"
    "buffer_load_dwordx4 v112, s[80:83], s39 offen lds\n"
    "s_add_u32 m0, s38, 3264\n"
    "s_mov_b32 s39, 98304\n"
    "buffer_load_dwordx4 v112, s[80:83], s39 offen lds\n"
    "v_add_u32 v116, 35840, v114\n"
    "ds_read_b64_tr_b16 a[16:17], v116 offset:0\n"
    "ds_read_b64_tr_b16 a[18:19], v116 offset:2176\n"
    "ds_read_b64_tr_b16 a[20:21], v116 offset:8960\n"
    "ds_read_b64_tr_b16 a[22:23], v116 offset:11136\n"
    "v_xor_b32 v116, 32, v116\n"
    "ds_read_b64_tr_b16 a[24:25], v116 offset:0\n"
    "ds_read_b64_tr_b16 a[26:27], v116 offset:2176\n"
    "ds_read_b64_tr_b16 a[28:29], v116 offset:8960\n"
    "ds_read_b64_tr_b16 a[30:31], v116 offset:11136\n"
    "s_waitcnt lgkmcnt(0)\n"
    "s_waitcnt vmcnt(12)\n"
    "s_barrier\n"
    "s_mul_i32 s38, s61, 4480\n"
    "s_add_u32 m0, s38, 17920\n"
    "s_mov_b32 s39, 524288\n"
    "buffer_load_dwordx4 v112, s[80:83], s39 offen lds\n"
    "s_add_u32 m0, s38, 19008\n"
    "s_mov_b32 s39, 557056\n"
    "buffer_load_dwordx4 v112, s[80:83], s39 offen lds\n"
    "s_add_u32 m0, s38, 20096\n"
    "s_mov_b32 s39, 589824\n"
    "buffer_load_dwordx4 v112, s[80:83], s39 offen lds\n"
    "s_add_u32 m0, s38, 21184\n"
    "s_mov_b32 s39, 622592\n"
    "buffer_load_dwordx4 v112, s[80:83], s39 offen lds\n"
    "v_add_u32 v116, 53760, v114\n"
    "ds_read_b64_tr_b16 a[32:33], v116 offset:0\n"
    "ds_read_b64_tr_b16 a[34:35], v116 offset:2176\n"
    "ds_read_b64_tr_b16 a[36:37], v116 offset:8960\n"
    "ds_read_b64_tr_b16 a[38:39], v116 offset:11136\n"
    "v_xor_b32 v116, 32, v116\n"
    "ds_read_b64_tr_b16 a[40:41], v116 offset:0\n"
    "ds_read_b64_tr_b16 a[42:43], v116 offset:2176\n"
    "ds_read_b64_tr_b16 a[44:45], v116 offset:8960\n"
    "ds_read_b64_tr_b16 a[46:47], v116 offset:11136\n"
    "s_waitcnt lgkmcnt(0)\n"
    "s_waitcnt vmcnt(12)\n"
    "s_barrier\n"
    "global_load_dword v117, v118, s[92:93] sc1\n"
    "s_mul_i32 s38, s61, 4480\n"
    "s_add_u32 m0, s38, 35840\n"
    "s_mov_b32 s39, 1048576\n"
    "buffer_load_dwordx4 v112, s[80:83], s39 offen lds\n"
    "s_add_u32 m0, s38, 36928\n"
    "s_mov_b32 s39, 1081344\n"
    "buffer_load_dwordx4 v112, s[80:83], s39 offen lds\n"
    "s_add_u32 m0, s38, 38016\n"
    "s_mov_b32 s39, 1114112\n"
    "buffer_load_dwordx4 v112, s[80:83], s39 offen lds\n"
    "s_add_u32 m0, s38, 39104\n"
    "s_mov_b32 s39, 1146880\n"
    "buffer_load_dwordx4 v112, s[80:83], s39 offen lds\n"
    "v_add_u32 v116, 71680, v114\n"
    "ds_read_b64_tr_b16 a[48:49], v116 offset:0\n"
    "ds_read_b64_tr_b16 a[50:51], v116 offset:2176\n"
    "ds_read_b64_tr_b16 a[52:53], v116 offset:8960\n"
    "ds_read_b64_tr_b16 a[54:55], v116 offset:11136\n"
    "v_xor_b32 v116, 32, v116\n"
    "ds_read_b64_tr_b16 a[56:57], v116 offset:0\n"
    "ds_read_b64_tr_b16 a[58:59], v116 offset:2176\n"
    "ds_read_b64_tr_b16 a[60:61], v116 offset:8960\n"
    "ds_read_b64_tr_b16 a[62:63], v116 offset:11136\n"
    "s_waitcnt lgkmcnt(0)\n"
    "s_waitcnt vmcnt(13)\n"
    "s_barrier\n"
    "s_mul_i32 s38, s61, 4480\n"
    "s_add_u32 m0, s38, 53760\n"
    "s_mov_b32 s39, 1572864\n"
    "buffer_load_dwordx4 v112, s[80:83], s39 offen lds\n"
    "s_add_u32 m0, s38, 54848\n"
    "s_mov_b32 s39, 1605632\n"
    "buffer_load_dwordx4 v112, s[80:83], s39 offen lds\n"
    "s_add_u32 m0, s38, 55936\n"
    "s_mov_b32 s39, 1638400\n"
    "buffer_load_dwordx4 v112, s[80:83], s39 offen lds\n"
    "s_add_u32 m0, s38, 57024\n"
    "s_mov_b32 s39, 1671168\n"
    "buffer_load_dwordx4 v112, s[80:83], s39 offen lds\n"
    "v_add_u32 v116, 89600, v114\n"
    "ds_read_b64_tr_b16 a[64:65], v116 offset:0\n"
    "ds_read_b64_tr_b16 a[66:67], v116 offset:2176\n"
    "ds_read_b64_tr_b16 a[68:69], v116 offset:8960\n"
    "ds_read_b64_tr_b16 a[70:71], v116 offset:11136\n"
    "v_xor_b32 v116, 32, v116\n"
    "ds_read_b64_tr_b16 a[72:73], v116 offset:0\n"
    "ds_read_b64_tr_b16 a[74:75], v116 offset:2176\n"
    "ds_read_b64_tr_b16 a[76:77], v116 offset:8960\n"
    "ds_read_b64_tr_b16 a[78:79], v116 offset:11136\n"
    "s_waitcnt lgkmcnt(0)\n"
    "s_waitcnt vmcnt(13)\n"
    "s_barrier\n"
    "s_mul_i32 s38, s61, 4480\n"
    "s_add_u32 m0, s38, 71680\n"
    "s_mov_b32 s39, 2097152\n"
    "buffer_load_dwordx4 v112, s[80:83], s39 offen lds\n"
    "s_add_u32 m0, s38, 72768\n"
    "s_mov_b32 s39, 2129920\n"
    "buffer_load_dwordx4 v112, s[80:83], s39 offen lds\n"
    "s_add_u32 m0, s38, 73856\n"
    "s_mov_b32 s39, 2162688\n"
    "buffer_load_dwordx4 v112, s[80:83], s39 offen lds\n"
    "s_add_u32 m0, s38, 74944\n"
    "s_mov_b32 s39, 2195456\n"
    "buffer_load_dwordx4 v112, s[80:83], s39 offen lds\n"
    "v_add_u32 v116, 0, v114\n"
    "ds_read_b64_tr_b16 a[176:177], v116 offset:0\n"
    "ds_read_b64_tr_b16 a[178:179], v116 offset:2176\n"
    "ds_read_b64_tr_b16 a[180:181], v116 offset:8960\n"
    "ds_read_b64_tr_b16 a[182:183], v116 offset:11136\n"
    "v_xor_b32 v116, 32, v116\n"
    "ds_read_b64_tr_b16 a[184:185], v116 offset:0\n"
    "ds_read_b64_tr_b16 a[186:187], v116 offset:2176\n"
    "ds_read_b64_tr_b16 a[188:189], v116 offset:8960\n"
    "ds_read_b64_tr_b16 a[190:191], v116 offset:11136\n"
    "s_waitcnt lgkmcnt(0)\n"
    "s_waitcnt vmcnt(13)\n"
    "s_barrier\n"
    "s_mul_i32 s38, s61, 4480\n"
    "s_add_u32 m0, s38, 89600\n"
    "s_mov_b32 s39, 2621440\n"
    "buffer_load_dwordx4 v112, s[80:83], s39 offen lds\n"
    "s_add_u32 m0, s38, 90688\n"
    "s_mov_b32 s39, 2654208\n"
    "buffer_load_dwordx4 v112, s[80:83], s39 offen lds\n"
    "s_add_u32 m0, s38, 91776\n"
    "s_mov_b32 s39, 2686976\n"
    "buffer_load_dwordx4 v112, s[80:83], s39 offen lds\n"
    "s_add_u32 m0, s38, 92864\n"
    "s_mov_b32 s39, 2719744\n"
    "buffer_load_dwordx4 v112, s[80:83], s39 offen lds\n"
    "v_add_u32 v116, 17920, v114\n"
    "ds_read_b64_tr_b16 a[192:193], v116 offset:0\n"
    "ds_read_b64_tr_b16 a[194:195], v116 offset:2176\n"
    "ds_read_b64_tr_b16 a[196:197], v116 offset:8960\n"
    "ds_read_b64_tr_b16 a[198:199], v116 offset:11136\n"
    "v_xor_b32 v116, 32, v116\n"
    "ds_read_b64_tr_b16 a[200:201], v116 offset:0\n"
    "ds_read_b64_tr_b16 a[202:203], v116 offset:2176\n"
    "ds_read_b64_tr_b16 a[204:205], v116 offset:8960\n"
    "ds_read_b64_tr_b16 a[206:207], v116 offset:11136\n"
    "s_waitcnt lgkmcnt(0)\n"
    "s_waitcnt vmcnt(12)\n"
    "s_barrier\n"
    "s_mul_i32 s38, s61, 4480\n"
    "s_add_u32 m0, s38, 0\n"
    "s_mov_b32 s39, 3145728\n"
    "buffer_load_dwordx4 v112, s[80:83], s39 offen lds\n"
    "s_add_u32 m0, s38, 1088\n"
    "s_mov_b32 s39, 3178496\n"
    "buffer_load_dwordx4 v112, s[80:83], s39 offen lds\n"
    "s_add_u32 m0, s38, 2176\n"
    "s_mov_b32 s39, 3211264\n"
    "buffer_load_dwordx4 v112, s[80:83], s39 offen lds\n"
    "s_add_u32 m0, s38, 3264\n"
    "s_mov_b32 s39, 3244032\n"
    "buffer_load_dwordx4 v112, s[80:83], s39 offen lds\n"
    "v_add_u32 v116, 35840, v114\n"
    "ds_read_b64_tr_b16 a[208:209], v116 offset:0\n"
    "ds_read_b64_tr_b16 a[210:211], v116 offset:2176\n"
    "ds_read_b64_tr_b16 a[212:213], v116 offset:8960\n"
    "ds_read_b64_tr_b16 a[214:215], v116 offset:11136\n"
    "v_xor_b32 v116, 32, v116\n"
    "ds_read_b64_tr_b16 a[216:217], v116 offset:0\n"
    "ds_read_b64_tr_b16 a[218:219], v116 offset:2176\n"
    "ds_read_b64_tr_b16 a[220:221], v116 offset:8960\n"
    "ds_read_b64_tr_b16 a[222:223], v116 offset:11136\n"
    "s_waitcnt lgkmcnt(0)\n"
    "s_waitcnt vmcnt(12)\n"
    "s_barrier\n"
    "s_mul_i32 s38, s61, 4480\n"
    "s_add_u32 m0, s38, 17920\n"
    "s_mov_b32 s39, 3670016\n"
    "buffer_load_dwordx4 v112, s[80:83], s39 offen lds\n"
    "s_add_u32 m0, s38, 19008\n"
    "s_mov_b32 s39, 3702784\n"
    "buffer_load_dwordx4 v112, s[80:83], s39 offen lds\n"
    "s_add_u32 m0, s38, 20096\n"
    "s_mov_b32 s39, 3735552\n"
    "buffer_load_dwordx4 v112, s[80:83], s39 offen lds\n"
    "s_add_u32 m0, s38, 21184\n"
    "s_mov_b32 s39, 3768320\n"
    "buffer_load_dwordx4 v112, s[80:83], s39 offen lds\n"
    "v_add_u32 v116, 53760, v114\n"
    "ds_read_b64_tr_b16 a[224:225], v116 offset:0\n"
    "ds_read_b64_tr_b16 a[226:227], v116 offset:2176\n"
    "ds_read_b64_tr_b16 a[228:229], v116 offset:8960\n"
    "ds_read_b64_tr_b16 a[230:231], v116 offset:11136\n"
    "v_xor_b32 v116, 32, v116\n"
    "ds_read_b64_tr_b16 a[232:233], v116 offset:0\n"
    "ds_read_b64_tr_b16 a[234:235], v116 offset:2176\n"
    "ds_read_b64_tr_b16 a[236:237], v116 offset:8960\n"
    "ds_read_b64_tr_b16 a[238:239], v116 offset:11136\n"
    "s_waitcnt lgkmcnt(0)\n"
    "s_waitcnt vmcnt(12)\n"
    "s_barrier\n"
    "v_add_u32 v116, 71680, v114\n"
    "ds_read_b64_tr_b16 a[240:241], v116 offset:0\n"
    "ds_read_b64_tr_b16 a[242:243], v116 offset:2176\n"
    "ds_read_b64_tr_b16 a[244:245], v116 offset:8960\n"
    "ds_read_b64_tr_b16 a[246:247], v116 offset:11136\n"
    "v_xor_b32 v116, 32, v116\n"
    "ds_read_b64_tr_b16 a[248:249], v116 offset:0\n"
    "ds_read_b64_tr_b16 a[250:251], v116 offset:2176\n"
    "ds_read_b64_tr_b16 a[252:253], v116 offset:8960\n"
    "ds_read_b64_tr_b16 a[254:255], v116 offset:11136\n"
    "s_waitcnt lgkmcnt(0)\n"
    "s_waitcnt vmcnt(8)\n"
    "s_barrier\n"
    "v_add_u32 v116, 89600, v114\n"
    "ds_read_b64_tr_b16 v[120:121], v116 offset:0\n"
    "ds_read_b64_tr_b16 v[122:123], v116 offset:2176\n"
    "ds_read_b64_tr_b16 v[124:125], v116 offset:8960\n"
    "ds_read_b64_tr_b16 v[126:127], v116 offset:11136\n"
    "v_xor_b32 v116, 32, v116\n"
    "ds_read_b64_tr_b16 v[128:129], v116 offset:0\n"
    "ds_read_b64_tr_b16 v[130:131], v116 offset:2176\n"
    "ds_read_b64_tr_b16 v[132:133], v116 offset:8960\n"
    "ds_read_b64_tr_b16 v[134:135], v116 offset:11136\n"
    "s_waitcnt lgkmcnt(0)\n"
    "s_mov_b32 s37, 0x80000\n"
    "LRY%=:\n"
    "v_readfirstlane_b32 s44, v117\n"
    "s_cmp_lg_u32 s44, 0\n"
    "s_cbranch_scc1 LRZ%=\n"
    "s_add_u32 s42, s94, 4096\n"
    "s_addc_u32 s43, s95, 0\n"
    "global_load_dword v119, v118, s[42:43] sc1\n"
    "global_load_dword v117, v118, s[92:93] sc1\n"
    "s_waitcnt vmcnt(0)\n"
    "s_add_u32 s38, s52, 8192\n"
    "s_add_u32 s42, s94, s38\n"
    "s_addc_u32 s43, s95, 0\n"
    "global_store_dword v118, v119, s[42:43] sc1\n"
    "s_sleep 4\n"
    "s_sub_u32 s37, s37, 1\n"
    "s_cbranch_scc0 LRY%=\n"
    "s_branch LFAIL%=\n"
    "LRZ%=:\n"
    "s_lshl_b32 s38, s44, 13\n"
    "s_and_b32 s38, s38, 0x2000\n"
    "s_xor_b32 s46, s46, s38\n"
    "s_xor_b32 s47, s47, s38\n"
    "s_xor_b32 s48, s48, s38\n"
    "s_xor_b32 s56, s56, s38\n"
    "s_xor_b32 s74, s74, s38\n"
    "s_xor_b32 s58, s58, s38\n"
    "s_xor_b32 s59, s59, s38\n"
    "s_xor_b32 s67, s67, s38\n"
    "s_xor_b32 s69, s69, s38\n"
    "s_sub_u32 s58, s58, 4\n"
    "s_mov_b64 exec, 7\n"
    "buffer_load_dword v119, v0, s[16:19], s59 offen sc1\n"
    "s_mov_b64 exec, -1\n"
    "s_waitcnt vmcnt(5)\n"
    "s_barrier\n"
    "v_add_u32 v116, 0, v114\n"
    "ds_read_b64_tr_b16 v[136:137], v116 offset:0\n"
    "ds_read_b64_tr_b16 v[138:139], v116 offset:2176\n"
    "ds_read_b64_tr_b16 v[140:141], v116 offset:8960\n"
    "ds_read_b64_tr_b16 v[142:143], v116 offset:11136\n"
    "v_xor_b32 v116, 32, v116\n"
    "ds_read_b64_tr_b16 v[144:145], v116 offset:0\n"
    "ds_read_b64_tr_b16 v[146:147], v116 offset:2176\n"
    "ds_read_b64_tr_b16 v[148:149], v116 offset:8960\n"
    "ds_read_b64_tr_b16 v[150:151], v116 offset:11136\n"
    "s_waitcnt lgkmcnt(0)\n"
    "s_waitcnt vmcnt(1)\n"
    "s_barrier\n"
    "v_add_u32 v116, 17920, v114\n"
    "ds_read_b64_tr_b16 v[152:153], v116 offset:0\n"
    "ds_read_b64_tr_b16 v[154:155], v116 offset:2176\n"
    "ds_read_b64_tr_b16 v[156:157], v116 offset:8960\n"
    "ds_read_b64_tr_b16 v[158:159], v116 offset:11136\n"
    "v_xor_b32 v116, 32, v116\n"
    "ds_read_b64_tr_b16 v[160:161], v116 offset:0\n"
    "ds_read_b64_tr_b16 v[162:163], v116 offset:2176\n"
    "ds_read_b64_tr_b16 v[164:165], v116 offset:8960\n"
    "ds_read_b64_tr_b16 v[166:167], v116 offset:11136\n"
    "s_waitcnt lgkmcnt(0)\n"
    "s_barrier\n"
    "ds_write_b128 v7, v[16:19] offset:0\n"
    "ds_write_b128 v7, v[20:23] offset:1024\n"
    "ds_write_b128 v7, v[24:27] offset:2048\n"
    "ds_write_b128 v7, v[28:31] offset:3072\n"
    "ds_write_b128 v7, v[32:35] offset:4096\n"
    "ds_write_b128 v7, v[36:39] offset:5120\n"
    "ds_write_b128 v7, v[40:43] offset:6144\n"
    "ds_write_b128 v7, v[44:47] offset:7168\n"
    "ds_write_b128 v7, v[48:51] offset:8192\n"
    "ds_write_b128 v7, v[52:55] offset:9216\n"
    "ds_write_b128 v7, v[56:59] offset:10240\n"
    "ds_write_b128 v7, v[60:63] offset:11264\n"
    "ds_write_b128 v7, v[64:67] offset:12288\n"
    "ds_write_b128 v7, v[68:71] offset:13312\n"
    "ds_write_b128 v7, v[72:75] offset:14336\n"
    "s_waitcnt lgkmcnt(11)\n"
    "ds_write_b128 v7, v[76:79] offset:15360\n"
    "ds_write_b128 v7, v[80:83] offset:16384\n"
    "ds_write_b128 v7, v[84:87] offset:17408\n"
    "ds_write_b128 v7, v[88:91] offset:18432\n"
    "s_waitcnt lgkmcnt(11)\n"
    "ds_write_b128 v7, v[92:95] offset:19456\n"
    "ds_write_b128 v7, v[96:99] offset:20480\n"
    "ds_write_b128 v7, v[100:103] offset:21504\n"
    "ds_write_b128 v7, v[104:107] offset:22528\n"
    "s_waitcnt lgkmcnt(11)\n"
    "ds_write_b128 v7, v[108:111] offset:23552\n"
    "s_waitcnt lgkmcnt(0)\n"
    "ds_read_b128 v[16:19], v9 offset:0\n"
    "ds_read_b128 v[20:23], v9 offset:1024\n"
    "ds_read_b128 v[24:27], v9 offset:8192\n"
    "ds_read_b128 v[28:31], v9 offset:9216\n"
    "ds_read_b128 v[32:35], v9 offset:16384\n"
    "ds_read_b128 v[36:39], v9 offset:17408\n"
    "s_waitcnt lgkmcnt(5)\n"
    "ds_write_b128 v7, v[16:19] offset:24576\n"
    "s_waitcnt lgkmcnt(5)\n"
    "ds_write_b128 v7, v[20:23] offset:25600\n"
    "s_waitcnt lgkmcnt(5)\n"
    "ds_write_b128 v7, v[24:27] offset:26624\n"
    "s_waitcnt lgkmcnt(5)\n"
    "ds_write_b128 v7, v[28:31] offset:27648\n"
    "s_waitcnt lgkmcnt(5)\n"
    "ds_write_b128 v7, v[32:35] offset:28672\n"
    "s_waitcnt lgkmcnt(5)\n"
    "ds_write_b128 v7, v[36:39] offset:29696\n"
    "v_lshrrev_b32 v64, 4, v0\n"
    "v_lshrrev_b32 v65, 4, v64\n"
    "v_and_b32 v64, 15, v64\n"
    "v_lshlrev_b32 v65, 2, v65\n"
    "v_sub_u32 v64, v64, v65\n"
    "s_and_b32 s38, s61, 1\n"
    "s_lshl_b32 s38, s38, 4\n"
    "v_add_u32 v64, s38, v64\n"
    "v_cmp_le_i32 s[80:81], 0, v64\n"
    "v_cmp_le_i32 s[82:83], 1, v64\n"
    "v_cmp_le_i32 s[84:85], 2, v64\n"
    "v_cmp_le_i32 s[86:87], 3, v64\n"
    "v_cmp_le_i32 s[88:89], 16, v64\n"
    "v_cmp_le_i32 s[90:91], 17, v64\n"
    "v_cmp_le_i32 s[92:93], 18, v64\n"
    "v_cmp_le_i32 s[94:95], 19, v64\n"
    "v_mov_b32 v64, 158224\n"
    "v_mov_b64 v[66:67], 0\n"
    "ds_write_b64 v64, v[66:67] offset:16\n"
    "ds_write_b64 v64, v[66:67] offset:32\n"
    "ds_write_b64 v64, v[66:67] offset:40\n"
    "s_mov_b32 s54, 0x80000000\n"
    "s_xor_b32 s46, s46, 0x80000\n"
    "s_waitcnt lgkmcnt(0)\n"
    "s_memtime s[40:41]\n"
    "s_waitcnt lgkmcnt(0)\n"
    "s_sub_u32 s41, s40, s45\n"
    "s_sub_u32 s42, s13, 1\n"
    "s_and_b32 s41, s41, s42\n"
    "s_sub_u32 s44, s40, s41\n"
    "s_sub_u32 s44, s44, 8192\n"
    "s_waitcnt vmcnt(0)\n"
    "v_mov_b32 v116, v119\n"
    "v_cmp_ne_u32 vcc, 0, v116\n"
    "v_readfirstlane_b32 s70, v116\n"
    "s_nop 0\n"
    "s_andn2_b32 s38, 7, vcc_lo\n"
    "s_cbranch_scc0 LS0K%=\n"
    "s_mov_b32 s37, 0x80000\n"
    "LS0W%=:\n"
    "s_mov_b64 exec, 7\n"
    "buffer_load_dword v116, v0, s[16:19], s59 offen sc1\n"
    "s_mov_b64 exec, -1\n"
    "s_waitcnt vmcnt(0)\n"
    "v_cmp_ne_u32 vcc, 0, v116\n"
    "v_readfirstlane_b32 s70, v116\n"
    "s_nop 0\n"
    "s_andn2_b32 s38, 7, vcc_lo\n"
    "s_cbranch_scc0 LS0K%=\n"
    "s_sleep 2\n"
    "s_sub_u32 s37, s37, 1\n"
    "s_cbranch_scc0 LS0W%=\n"
    "s_branch LFAIL%=\n"
    "LS0K%=:\n"
    "s_lshr_b32 s38, s70, 16\n"
    "s_cmp_gt_i32 s38, s1\n"
    "s_cbranch_scc1 LREC%=\n"
    "s_add_u32 s53, s50, s55\n"
    "s_mov_b32 s49, s48\n"
    "s_add_u32 s51, s53, 0\n"
    "s_lshr_b32 s38, s51, 8\n"
    "s_xor_b32 s38, s38, s56\n"
    "s_and_b32 s38, s38, 0x2000\n"
    "s_or_b32 s51, s51, s38\n"
    "s_add_u32 s52, s53, 524288\n"
    "s_lshr_b32 s38, s52, 8\n"
    "s_xor_b32 s38, s38, s56\n"
    "s_and_b32 s38, s38, 0x2000\n"
    "s_or_b32 s52, s52, s38\n"
    "s_add_u32 s53, s53, 1048576\n"
    "s_lshr_b32 s38, s53, 8\n"
    "s_xor_b32 s38, s38, s56\n"
    "s_and_b32 s38, s38, 0x2000\n"
    "s_or_b32 s53, s53, s38\n"
    "s_add_u32 s59, s59, 128\n"
    "s_barrier\n"
    "s_cmp_lg_u32 s61, 1\n"
    "s_cbranch_scc1 LPRE%=\n"
    "s_sub_u32 s51, s51, 2048\n"
    "s_mov_b32 m0, 122880\n"
    "s_nop 0\n"
    "buffer_load_dwordx4 v0, s[16:19], s51 offen offset:0 sc1 lds\n"
    "buffer_load_dwordx4 v0, s[16:19], s51 offen offset:1024 sc1 lds\n"
    "buffer_load_dwordx4 v0, s[16:19], s51 offen offset:2048 sc1 lds\n"
    "buffer_load_dwordx4 v0, s[16:19], s51 offen offset:3072 sc1 lds\n"
    "s_mov_b32 m0, 126976\n"
    "s_add_u32 s51, s51, 4096\n"
    "buffer_load_dwordx4 v0, s[16:19], s51 offen offset:0 sc1 lds\n"
    "buffer_load_dwordx4 v0, s[16:19], s51 offen offset:1024 sc1 lds\n"
    "buffer_load_dwordx4 v0, s[16:19], s51 offen offset:2048 sc1 lds\n"
    "buffer_load_dwordx4 v0, s[16:19], s51 offen offset:3072 sc1 lds\n"
    "s_memtime s[40:41]\n"
    "s_waitcnt lgkmcnt(0)\n"
    "s_sub_u32 s41, s40, s45\n"
    "s_sub_u32 s42, s13, 1\n"
    "s_and_b32 s41, s41, s42\n"
    "s_sub_u32 s44, s40, s41\n"
    "v_mov_b32 v112, 158220\n"
    "v_mov_b32 v113, s36\n"
    "v_mov_b32 v114, s44\n"
    "ds_write2_b32 v112, v113, v114 offset0:0 offset1:1\n"
    "s_waitcnt lgkmcnt(0)\n"
    "s_branch LPRE%=\n"
    "LBM%=:\n"
    "s_waitcnt lgkmcnt(0)\n"
    "s_barrier\n"
    "buffer_load_dwordx4 v0, s[16:19], s73 offen sc1 lds\n"
    "ds_read_b64 v[42:43], v41 offset:0\n"
    "ds_read_b128 v[32:35], v9 offset:16384\n"
    "ds_read_b128 v[36:39], v9 offset:17408\n"
    "s_waitcnt lgkmcnt(2)\n"
    "v_readfirstlane_b32 s39, v42\n"
    "v_readfirstlane_b32 s44, v43\n"
    "s_sub_u32 s38, s39, s44\n"
    "s_cmp_gt_i32 s38, 0\n"
    "s_cbranch_scc1 LSL%=\n"
    "s_add_u32 m0, s32, 131072\n"
    "s_nop 0\n"
    "buffer_load_dwordx4 v0, s[16:19], s52 offen offset:0 sc1 lds\n"
    "buffer_load_dwordx4 v0, s[16:19], s52 offen offset:1024 sc1 lds\n"
    "s_waitcnt lgkmcnt(0)\n"
    "s_cmp_lg_u32 s61, 1\n"
    "s_cbranch_scc1 LC1%=\n"
    "s_waitcnt vmcnt(3)\n"
    "LC1%=:\n"
    "s_barrier\n"
    "ds_read_b128 v[16:19], v2 offset:0\n"
    "ds_read_b128 v[20:23], v2 offset:1024\n"
    "ds_read_b128 v[24:27], v2 offset:2048\n"
    "s_add_u32 m0, s68, 139264\n"
    "s_nop 0\n"
    "buffer_load_dwordx4 v0, s[16:19], s53 offen offset:0 sc1 lds\n"
    "buffer_load_dwordx4 v0, s[16:19], s53 offen offset:1024 sc1 lds\n"
    "s_waitcnt lgkmcnt(2)\n"
    "v_smfmac_f32_16x16x64_bf16 v[48:51], v[16:19], v[64:71], v1\n"
    "ds_read_b128 v[28:31], v2 offset:3072\n"
    "v_smfmac_f32_16x16x64_bf16 v[52:55], v[16:19], v[72:79], v1\n"
    "s_waitcnt lgkmcnt(2)\n"
    "v_smfmac_f32_16x16x64_bf16 v[48:51], v[20:23], v[80:87], v1\n"
    "v_smfmac_f32_16x16x64_bf16 v[52:55], v[20:23], v[88:95], v1\n"
    "s_waitcnt lgkmcnt(1)\n"
    "v_smfmac_f32_16x16x64_bf16 v[48:51], v[24:27], v[96:103], v1\n"
    "ds_read_b128 v[64:67], v7 offset:12288\n"
    "v_smfmac_f32_16x16x64_bf16 v[52:55], v[24:27], v[104:111], v1\n"
    "ds_read_b128 v[68:71], v7 offset:13312\n"
    "s_waitcnt lgkmcnt(2)\n"
    "v_smfmac_f32_16x16x64_bf16 v[48:51], v[28:31], a[0:7], v1\n"
    "ds_read_b128 v[72:75], v7 offset:14336\n"
    "v_smfmac_f32_16x16x64_bf16 v[52:55], v[28:31], a[8:15], v1\n"
    "ds_read_b128 v[76:79], v7 offset:15360\n"
    "v_smfmac_f32_16x16x64_bf16 v[112:115], v[16:19], a[176:183], v1\n"
    "ds_read_b128 v[80:83], v7 offset:16384\n"
    "buffer_store_dwordx4 v[32:35], v40, s[16:19], s54 offen offset:0 sc1\n"
    "v_smfmac_f32_16x16x64_bf16 v[116:119], v[16:19], a[184:191], v1\n"
    "ds_read_b128 v[84:87], v7 offset:17408\n"
    "v_smfmac_f32_16x16x64_bf16 v[112:115], v[20:23], a[192:199], v1\n"
    "ds_read_b128 v[88:91], v7 offset:18432\n"
    "v_smfmac_f32_16x16x64_bf16 v[116:119], v[20:23], a[200:207], v1\n"
    "ds_read_b128 v[92:95], v7 offset:19456\n"
    "v_smfmac_f32_16x16x64_bf16 v[56:59], v[24:27], a[80:87], v1\n"
    "v_smfmac_f32_16x16x64_bf16 v[60:63], v[24:27], a[88:95], v1\n"
    "v_max3_u32 v111, v19, v23, v27\n"
    "buffer_store_dwordx4 v[36:39], v40, s[16:19], s54 offen offset:2048 sc1\n"
    "v_smfmac_f32_16x16x64_bf16 v[56:59], v[28:31], a[96:103], v1\n"
    "v_smfmac_f32_16x16x64_bf16 v[60:63], v[28:31], a[104:111], v1\n"
    "s_waitcnt vmcnt(6)\n"
    "s_barrier\n"
    "ds_read_b128 v[32:35], v2 offset:4096\n"
    "s_waitcnt lgkmcnt(7)\n"
    "v_smfmac_f32_16x16x64_bf16 v[56:59], v[16:19], v[64:71], v1\n"
    "ds_read_b128 v[36:39], v2 offset:5120\n"
    "s_waitcnt lgkmcnt(6)\n"
    "v_smfmac_f32_16x16x64_bf16 v[60:63], v[16:19], v[72:79], v1\n"
    "ds_read_b128 v[40:43], v2 offset:6144\n"
    "s_waitcnt lgkmcnt(5)\n"
    "v_smfmac_f32_16x16x64_bf16 v[56:59], v[20:23], v[80:87], v1\n"
    "ds_read_b128 v[44:47], v2 offset:7168\n"
    "s_waitcnt lgkmcnt(4)\n"
    "v_smfmac_f32_16x16x64_bf16 v[60:63], v[20:23], v[88:95], v1\n"
    "s_waitcnt lgkmcnt(3)\n"
    "v_smfmac_f32_16x16x64_bf16 v[48:51], v[32:35], a[16:23], v1\n"
    "v_max3_u32 v111, v111, v31, v35\n"
    "v_smfmac_f32_16x16x64_bf16 v[52:55], v[32:35], a[24:31], v1\n"
    "ds_read_b128 v[88:91], v9 offset:0\n"
    "s_waitcnt lgkmcnt(3)\n"
    "v_smfmac_f32_16x16x64_bf16 v[48:51], v[36:39], a[32:39], v1\n"
    "v_add_u32 v21, s20, v5\n"
    "v_smfmac_f32_16x16x64_bf16 v[52:55], v[36:39], a[40:47], v1\n"
    "ds_read_b128 v[92:95], v9 offset:1024\n"
    "s_waitcnt lgkmcnt(3)\n"
    "v_smfmac_f32_16x16x64_bf16 v[48:51], v[40:43], a[48:55], v1\n"
    "s_waitcnt lgkmcnt(2)\n"
    "v_max3_u32 v111, v111, v39, v43\n"
    "v_smfmac_f32_16x16x64_bf16 v[48:51], v[44:47], a[64:71], v1\n"
    "v_max_u32 v111, v111, v47\n"
    "v_smfmac_f32_16x16x64_bf16 v[52:55], v[40:43], a[56:63], v1\n"
    "v_cmp_eq_u32 vcc, -1, v111\n"
    "v_smfmac_f32_16x16x64_bf16 v[52:55], v[44:47], a[72:79], v1\n"
    "v_add_f32 v48, v48, v50\n"
    "v_smfmac_f32_16x16x64_bf16 v[56:59], v[32:35], a[112:119], v1\n"
    "v_add_f32 v49, v49, v51\n"
    "v_smfmac_f32_16x16x64_bf16 v[60:63], v[32:35], a[120:127], v1\n"
    "v_add_f32 v52, v52, v54\n"
    "v_add_f32 v53, v53, v55\n"
    "v_cvt_pk_bf16_f32 v48, v48, v52\n"
    "v_cvt_pk_bf16_f32 v49, v49, v53\n"
    "v_smfmac_f32_16x16x64_bf16 v[56:59], v[36:39], a[128:135], v1\n"
    "ds_write2_b32 v3, v48, v49 offset0:0 offset1:72\n"
    "v_smfmac_f32_16x16x64_bf16 v[60:63], v[36:39], a[136:143], v1\n"
    "s_cbranch_vccnz LMR%=\n"
    "v_smfmac_f32_16x16x64_bf16 v[56:59], v[40:43], a[144:151], v1\n"
    "v_mov_b32 v53, 0\n"
    "v_smfmac_f32_16x16x64_bf16 v[56:59], v[44:47], a[160:167], v1\n"
    "v_mov_b32 v54, 0\n"
    "v_smfmac_f32_16x16x64_bf16 v[60:63], v[40:43], a[152:159], v1\n"
    "s_waitcnt lgkmcnt(0)\n"
    "s_barrier\n"
    "ds_read_b128 v[80:83], v21 offset:0\n"
    "v_smfmac_f32_16x16x64_bf16 v[60:63], v[44:47], a[168:175], v1\n"
    "ds_read_b128 v[84:87], v21 offset:64\n"
    "v_add_f32 v56, v56, v58\n"
    "v_smfmac_f32_16x16x64_bf16 v[112:115], v[24:27], a[208:215], v1\n"
    "ds_read_b128 v[96:99], v5 offset:0\n"
    "v_add_f32 v57, v57, v59\n"
    "v_smfmac_f32_16x16x64_bf16 v[112:115], v[28:31], a[224:231], v1\n"
    "ds_read_b128 v[100:103], v5 offset:64\n"
    "s_waitcnt lgkmcnt(3)\n"
    "v_add_f32 v60, v60, v62\n"
    "v_mfma_f32_16x16x32_bf16 v[64:67], v[88:91], v[80:83], 0\n"
    "ds_read_b128 v[104:107], v5 offset:128\n"
    "s_waitcnt lgkmcnt(3)\n"
    "v_add_f32 v61, v61, v63\n"
    "v_cvt_pk_bf16_f32 v56, v56, v60\n"
    "v_mfma_f32_16x16x32_bf16 v[64:67], v[92:95], v[84:87], v[64:67]\n"
    "ds_read_b128 v[108:111], v5 offset:192\n"
    "v_cvt_pk_bf16_f32 v57, v57, v61\n"
    "v_mov_b64 v[50:51], 0\n"
    "v_smfmac_f32_16x16x64_bf16 v[112:115], v[32:35], a[240:247], v1\n"
    "ds_write2_b32 v4, v56, v57 offset0:0 offset1:72\n"
    "v_smfmac_f32_16x16x64_bf16 v[112:115], v[36:39], v[120:127], v1\n"
    "ds_write_b128 v8, v[64:67]\n"
    "v_smfmac_f32_16x16x64_bf16 v[112:115], v[40:43], v[136:143], v1\n"
    "v_mov_b64 v[60:61], s[14:15]\n"
    "v_smfmac_f32_16x16x64_bf16 v[112:115], v[44:47], v[152:159], v1\n"
    "v_mov_b64 v[62:63], s[14:15]\n"
    "v_smfmac_f32_16x16x64_bf16 v[116:119], v[24:27], a[216:223], v1\n"
    "v_smfmac_f32_16x16x64_bf16 v[116:119], v[28:31], a[232:239], v1\n"
    "s_waitcnt lgkmcnt(0)\n"
    "s_barrier\n"
    "ds_read_b128 v[64:67], v6 offset:0\n"
    "ds_read_b128 v[88:91], v15 offset:0\n"
    "v_smfmac_f32_16x16x64_bf16 v[116:119], v[32:35], a[248:255], v1\n"
    "ds_read_b128 v[92:95], v15 offset:512\n"
    "ds_read_b128 v[68:71], v6 offset:64\n"
    "v_smfmac_f32_16x16x64_bf16 v[116:119], v[36:39], v[128:135], v1\n"
    "ds_read_b128 v[72:75], v6 offset:128\n"
    "v_add_f32 v112, v112, v114\n"
    "v_smfmac_f32_16x16x64_bf16 v[116:119], v[40:43], v[144:151], v1\n"
    "ds_read_b128 v[76:79], v6 offset:192\n"
    "s_waitcnt lgkmcnt(5)\n"
    "v_add_f32 v113, v113, v115\n"
    "v_mfma_f32_16x16x32_bf16 v[16:19], v[64:67], v[96:99], 0\n"
    "s_waitcnt lgkmcnt(3)\n"
    "s_waitcnt lgkmcnt(2)\n"
    "v_add_f32 v88, v88, v92\n"
    "v_add_f32 v89, v89, v93\n"
    "v_mfma_f32_16x16x32_bf16 v[16:19], v[68:71], v[100:103], v[16:19]\n"
    "s_waitcnt lgkmcnt(1)\n"
    "v_add_f32 v90, v90, v94\n"
    "v_add_f32 v91, v91, v95\n"
    "v_mfma_f32_16x16x32_bf16 v[16:19], v[72:75], v[104:107], v[16:19]\n"
    "s_waitcnt lgkmcnt(0)\n"
    "v_max3_f32 v21, v88, v89, v90\n"
    "v_max_f32 v21, v21, v91\n"
    "v_mfma_f32_16x16x32_bf16 v[16:19], v[76:79], v[108:111], v[16:19]\n"
    "s_waitcnt vmcnt(4)\n"
    "ds_read_b128 v[80:83], v9 offset:8192\n"
    "v_max_f32_dpp v20, v21, v21 row_ror:8 row_mask:0xf bank_mask:0xf\n"
    "v_mov_b32 v21, v20\n"
    "v_smfmac_f32_16x16x64_bf16 v[116:119], v[44:47], v[160:167], v1\n"
    "ds_read_b128 v[84:87], v9 offset:9216\n"
    "v_permlane32_swap_b32 v20, v21\n"
    "v_max_f32 v20, v20, v21\n"
    "v_cvt_pk_bf16_f32 v112, v112, v113\n"
    "v_max3_f32 v21, v16, v17, v20\n"
    "v_max3_f32 v21, v18, v19, v21\n"
    "v_mov_b32 v22, v21\n"
    "s_nop 0\n"
    "v_add_f32 v116, v116, v118\n"
    "v_add_f32 v117, v117, v119\n"
    "v_permlane16_swap_b32 v21, v22\n"
    "v_max_f32 v21, v21, v22\n"
    "v_mul_f32 v21, 0xbe0293ee, v21\n"
    "v_fma_f32 v88, v88, s65, v21\n"
    "v_fma_f32 v89, v89, s65, v21\n"
    "v_fma_f32 v90, v90, s65, v21\n"
    "v_fma_f32 v91, v91, s65, v21\n"
    "v_fma_f32 v16, v16, s65, v21\n"
    "v_fma_f32 v17, v17, s65, v21\n"
    "v_exp_f32 v16, v16\n"
    "v_exp_f32 v17, v17\n"
    "v_exp_f32 v88, v88\n"
    "v_exp_f32 v89, v89\n"
    "v_cvt_pk_bf16_f32 v52, v16, v17\n"
    "v_cvt_pk_bf16_f32 v113, v116, v117\n"
    "v_cvt_pk_bf16_f32 v55, v16, v17\n"
    "v_cvt_pk_bf16_f32 v56, v88, v89\n"
    "v_mfma_f32_16x16x16_bf16 v[104:107], v[60:61], v[52:53], 0\n"
    "v_exp_f32 v90, v90\n"
    "v_mfma_f32_16x16x16_bf16 v[96:99], v[112:113], v[52:53], 0\n"
    "v_exp_f32 v91, v91\n"
    "v_mfma_f32_16x16x16_bf16 v[100:103], v[112:113], v[54:55], 0\n"
    "v_cvt_pk_bf16_f32 v57, v90, v91\n"
    "v_mov_b32_dpp v58, v56 row_shl:8 row_mask:0xf bank_mask:0xf bound_ctrl:1\n"
    "s_nop 0\n"
    "v_mov_b32_dpp v59, v57 row_shl:8 row_mask:0xf bank_mask:0xf bound_ctrl:1\n"
    "s_nop 1\n"
    "v_mfma_f32_16x16x32_bf16 v[104:107], v[60:63], v[56:59], v[104:107]\n"
    "ds_write_b64 v10, v[112:113]\n"
    "s_waitcnt lgkmcnt(2)\n"
    "v_mfma_f32_16x16x32_bf16 v[96:99], v[80:83], v[56:59], v[96:99]\n"
    "s_waitcnt lgkmcnt(1)\n"
    "v_mfma_f32_16x16x32_bf16 v[100:103], v[84:87], v[56:59], v[100:103]\n"
    "v_rcp_f32 v104, v104\n"
    "s_mov_b64 exec, s[78:79]\n"
    "s_nop 1\n"
    "v_pk_mul_f32 v[96:97], v[96:97], v[104:105] op_sel_hi:[1,0]\n"
    "v_pk_mul_f32 v[98:99], v[98:99], v[104:105] op_sel_hi:[1,0]\n"
    "v_cvt_pk_bf16_f32 v96, v96, v97\n"
    "v_cvt_pk_bf16_f32 v97, v98, v99\n"
    "ds_write_b64 v11, v[96:97] offset:0\n"
    "v_pk_mul_f32 v[100:101], v[100:101], v[104:105] op_sel_hi:[1,0]\n"
    "v_pk_mul_f32 v[102:103], v[102:103], v[104:105] op_sel_hi:[1,0]\n"
    "v_cvt_pk_bf16_f32 v98, v100, v101\n"
    "v_cvt_pk_bf16_f32 v99, v102, v103\n"
    "ds_write_b64 v11, v[98:99] offset:8\n"
    "s_mov_b64 exec, 7\n"
    "buffer_load_dword v116, v0, s[16:19], s59 offen sc1\n"
    "s_mov_b64 exec, -1\n"
    "v_mfma_f32_32x32x8_bf16 v[72:87], v[50:51], v[50:51], 0\n"
    "s_add_u32 s36, s36, 1\n"
    "s_cmp_eq_u32 s36, 64\n"
    "s_waitcnt lgkmcnt(0)\n"
    "s_waitcnt vmcnt(3)\n"
    "s_barrier\n"
    "ds_read_b128 v[104:107], v12 offset:0\n"
    "ds_read_b128 v[108:111], v12 offset:1024\n"
    "s_nop 4\n"
    "v_mfma_f32_32x32x8_bf16 v[48:63], v[72:73], v[72:73], 0\n"
    "s_cbranch_scc1 LEND%=\n"
    "s_waitcnt lgkmcnt(1)\n"
    "v_smfmac_f32_16x16x64_bf16 v[72:75], v[104:107], v[168:175], v1\n"
    "ds_read_b128 v[64:67], v7 offset:20480\n"
    "s_waitcnt lgkmcnt(1)\n"
    "v_smfmac_f32_16x16x64_bf16 v[72:75], v[108:111], v[176:183], v1\n"
    "ds_read_b128 v[68:71], v7 offset:21504\n"
    "v_smfmac_f32_16x16x64_bf16 v[76:79], v[104:107], v[184:191], v1\n"
    "ds_read_b128 v[16:19], v7 offset:22528\n"
    "v_smfmac_f32_16x16x64_bf16 v[76:79], v[108:111], v[192:199], v1\n"
    "ds_read_b128 v[20:23], v7 offset:23552\n"
    "v_add_f32 v72, v72, v74\n"
    "v_smfmac_f32_16x16x64_bf16 v[80:83], v[104:107], v[200:207], v1\n"
    "ds_read_b128 v[24:27], v7 offset:24576\n"
    "v_add_f32 v73, v73, v75\n"
    "v_smfmac_f32_16x16x64_bf16 v[80:83], v[108:111], v[208:215], v1\n"
    "ds_read_b128 v[28:31], v7 offset:25600\n"
    "v_add_f32 v76, v76, v78\n"
    "v_smfmac_f32_16x16x64_bf16 v[84:87], v[104:107], v[216:223], v1\n"
    "ds_read_b128 v[32:35], v7 offset:26624\n"
    "v_add_f32 v77, v77, v79\n"
    "v_smfmac_f32_16x16x64_bf16 v[84:87], v[108:111], v[224:231], v1\n"
    "ds_read_b128 v[36:39], v7 offset:27648\n"
    "v_cvt_pk_bf16_f32 v88, v72, v76\n"
    "v_cvt_pk_bf16_f32 v90, v73, v77\n"
    "v_smfmac_f32_16x16x64_bf16 v[48:51], v[104:107], v[232:239], v1\n"
    "ds_read_b128 v[40:43], v7 offset:28672\n"
    "v_add_f32 v80, v80, v82\n"
    "v_add_f32 v81, v81, v83\n"
    "v_smfmac_f32_16x16x64_bf16 v[48:51], v[108:111], v[240:247], v1\n"
    "ds_read_b128 v[44:47], v7 offset:29696\n"
    "v_add_f32 v85, v85, v87\n"
    "v_add_f32 v84, v84, v86\n"
    "v_cvt_pk_bf16_f32 v91, v81, v85\n"
    "v_cvt_pk_bf16_f32 v89, v80, v84\n"
    "v_and_or_b32 v91, v91, -2, s64\n"
    "buffer_store_dwordx4 v[88:91], v13, s[16:19], s46 offen sc1\n"
    "v_smfmac_f32_16x16x64_bf16 v[52:55], v[104:107], v[248:255], v1\n"
    "s_waitcnt lgkmcnt(8)\n"
    "v_smfmac_f32_16x16x64_bf16 v[52:55], v[108:111], v[64:71], v1\n"
    "s_waitcnt lgkmcnt(6)\n"
    "v_add_f32 v48, v48, v50\n"
    "v_smfmac_f32_16x16x64_bf16 v[56:59], v[104:107], v[16:23], v1\n"
    "s_waitcnt lgkmcnt(4)\n"
    "v_add_f32 v49, v49, v51\n"
    "v_smfmac_f32_16x16x64_bf16 v[56:59], v[108:111], v[24:31], v1\n"
    "s_waitcnt lgkmcnt(2)\n"
    "v_add_f32 v52, v52, v54\n"
    "v_add_f32 v53, v53, v55\n"
    "v_smfmac_f32_16x16x64_bf16 v[60:63], v[104:107], v[32:39], v1\n"
    "s_waitcnt lgkmcnt(0)\n"
    "v_cvt_pk_bf16_f32 v92, v48, v52\n"
    "v_cvt_pk_bf16_f32 v94, v49, v53\n"
    "v_smfmac_f32_16x16x64_bf16 v[60:63], v[108:111], v[40:47], v1\n"
    "v_add_f32 v56, v56, v58\n"
    "v_add_f32 v57, v57, v59\n"
    "s_nop 5\n"
    "v_add_f32 v61, v61, v63\n"
    "v_add_f32 v60, v60, v62\n"
    "v_cvt_pk_bf16_f32 v95, v57, v61\n"
    "v_cvt_pk_bf16_f32 v93, v56, v60\n"
    "v_and_or_b32 v95, v95, -2, s64\n"
    "buffer_store_dwordx4 v[92:95], v13, s[16:19], s47 offen sc1\n"
    "s_waitcnt vmcnt(3)\n"
    "v_mov_b32 v118, s72\n"
    "v_mov_b32 v119, s36\n"
    "ds_write_b32 v118, v119 offset:48\n"
    "s_add_u32 s54, s50, s57\n"
    "s_lshr_b32 s38, s54, 8\n"
    "s_xor_b32 s38, s38, s74\n"
    "s_and_b32 s38, s38, 0x2000\n"
    "s_or_b32 s54, s54, s38\n"
    "s_add_u32 s50, s50, 2621440\n"
    "s_add_u32 s48, s48, 16384\n"
    "s_cmp_eq_u32 s61, 1\n"
    "s_cbranch_scc1 LW1%=\n"
    "s_cmp_gt_u32 s61, 1\n"
    "s_cbranch_scc1 LW2%=\n"
    "s_waitcnt vmcnt(2)\n"
    "v_cmp_ne_u32 vcc, 0, v116\n"
    "v_readfirstlane_b32 s70, v116\n"
    "s_nop 0\n"
    "s_andn2_b32 s38, 7, vcc_lo\n"
    "s_cbranch_scc1 LE2%=\n"
    "s_lshr_b32 s38, s70, 16\n"
    "s_cmp_gt_i32 s38, s1\n"
    "s_cbranch_scc1 LREC%=\n"
    "LE2%=:\n"
    "s_add_u32 s73, s46, s63\n"
    "s_add_u32 s66, s73, 4096\n"
    "s_add_u32 s39, s44, s9\n"
    "s_waitcnt lgkmcnt(0)\n"
    "LUG%=:\n"
    "s_memtime s[40:41]\n"
    "s_waitcnt lgkmcnt(0)\n"
    "s_sub_u32 s40, s39, s40\n"
    "s_sub_u32 s41, s40, 253\n"
    "s_cmp_lt_u32 s41, 16384\n"
    "s_cbranch_scc1 LUG%=\n"
    "s_cmp_lt_u32 s40, 253\n"
    "s_cselect_b32 s40, s40, 0\n"
    "s_and_b32 s40, s40, 0xfc\n"
    "s_getpc_b64 s[42:43]\n"
    "s_sub_u32 s40, 272, s40\n"
    "s_add_u32 s42, s42, s40\n"
    "s_addc_u32 s43, s43, 0\n"
    "s_setpc_b64 s[42:43]\n"
    ".rept 63\n"
    "s_nop 0\n"
    ".endr\n"
    "buffer_load_dwordx4 v[16:19], v0, s[16:19], s73 offen offset:0 sc1\n"
    "buffer_load_dwordx4 v[20:23], v0, s[16:19], s73 offen offset:1024 sc1\n"
    "buffer_load_dwordx4 v[24:27], v0, s[16:19], s73 offen offset:2048 sc1\n"
    "buffer_load_dwordx4 v[28:31], v0, s[16:19], s73 offen offset:3072 sc1\n"
    "buffer_load_dwordx4 v[32:35], v0, s[16:19], s66 offen offset:0 sc1\n"
    "buffer_load_dwordx4 v[36:39], v0, s[16:19], s66 offen offset:1024 sc1\n"
    "buffer_load_dwordx4 v[40:43], v0, s[16:19], s66 offen offset:2048 sc1\n"
    "buffer_load_dwordx4 v[44:47], v0, s[16:19], s66 offen offset:3072 sc1\n"
    "ds_read_b128 v[48:51], v9 offset:35584\n"
    "ds_read_b128 v[64:67], v7 offset:0\n"
    "ds_read_b128 v[68:71], v7 offset:1024\n"
    "ds_read_b128 v[72:75], v7 offset:2048\n"
    "ds_read_b128 v[76:79], v7 offset:3072\n"
    "ds_read_b128 v[80:83], v7 offset:4096\n"
    "ds_read_b128 v[84:87], v7 offset:5120\n"
    "ds_read_b128 v[88:91], v7 offset:6144\n"
    "ds_read_b128 v[92:95], v7 offset:7168\n"
    "ds_read_b128 v[96:99], v7 offset:8192\n"
    "ds_read_b128 v[100:103], v7 offset:9216\n"
    "ds_read_b128 v[104:107], v7 offset:10240\n"
    "ds_read_b128 v[108:111], v7 offset:11264\n"
    "s_add_u32 s53, s50, s55\n"
    "s_mov_b32 s49, s48\n"
    "s_add_u32 s51, s53, 0\n"
    "s_lshr_b32 s38, s51, 8\n"
    "s_xor_b32 s38, s38, s56\n"
    "s_and_b32 s38, s38, 0x2000\n"
    "s_or_b32 s51, s51, s38\n"
    "s_add_u32 s52, s53, 524288\n"
    "s_lshr_b32 s38, s52, 8\n"
    "s_xor_b32 s38, s38, s56\n"
    "s_and_b32 s38, s38, 0x2000\n"
    "s_or_b32 s52, s52, s38\n"
    "s_add_u32 s53, s53, 1048576\n"
    "s_lshr_b32 s38, s53, 8\n"
    "s_xor_b32 s38, s38, s56\n"
    "s_and_b32 s38, s38, 0x2000\n"
    "s_or_b32 s53, s53, s38\n"
    "s_waitcnt lgkmcnt(12)\n"
    "s_waitcnt vmcnt(7)\n"
    "v_mfma_f32_16x16x32_bf16 v[52:55], v[48:51], v[16:19], 0\n"
    "s_waitcnt vmcnt(6)\n"
    "v_mfma_f32_16x16x32_bf16 v[52:55], v[48:51], v[20:23], v[52:55]\n"
    "s_waitcnt vmcnt(5)\n"
    "v_bitop3_b32 v60, v19, v23, s64 bitop3:0x7e\n"
    "v_mfma_f32_16x16x32_bf16 v[52:55], v[48:51], v[24:27], v[52:55]\n"
    "s_waitcnt vmcnt(4)\n"
    "v_mfma_f32_16x16x32_bf16 v[52:55], v[48:51], v[28:31], v[52:55]\n"
    "s_waitcnt vmcnt(3)\n"
    "v_bitop3_b32 v61, v27, v31, s64 bitop3:0x7e\n"
    "v_mfma_f32_16x16x32_bf16 v[56:59], v[48:51], v[32:35], 0\n"
    "s_waitcnt vmcnt(2)\n"
    "v_mfma_f32_16x16x32_bf16 v[56:59], v[48:51], v[36:39], v[56:59]\n"
    "v_bitop3_b32 v62, v35, v39, s64 bitop3:0x7e\n"
    "v_or3_b32 v60, v60, v61, v62\n"
    "v_add_f32_dpp v52, v52, v52 row_ror:8 row_mask:0xf bank_mask:0xf\n"
    "v_add_f32_dpp v53, v53, v53 row_ror:8 row_mask:0xf bank_mask:0xf\n"
    "v_add_f32_dpp v54, v54, v54 row_ror:8 row_mask:0xf bank_mask:0xf\n"
    "v_add_f32_dpp v55, v55, v55 row_ror:8 row_mask:0xf bank_mask:0xf\n"
    "v_cvt_pk_bf16_f32 v113, v54, v55\n"
    "v_cvt_pk_bf16_f32 v112, v52, v53\n"
    "s_waitcnt vmcnt(1)\n"
    "v_mfma_f32_16x16x32_bf16 v[56:59], v[48:51], v[40:43], v[56:59]\n"
    "s_waitcnt vmcnt(0)\n"
    "v_mfma_f32_16x16x32_bf16 v[56:59], v[48:51], v[44:47], v[56:59]\n"
    "v_bitop3_b32 v63, v43, v47, s64 bitop3:0x7e\n"
    "v_or_b32 v60, v60, v63\n"
    "v_and_b32 v61, 1, v60\n"
    "v_cmp_ne_u32 vcc, 0, v61\n"
    "s_cbranch_vccnz LGR%=\n"
    "s_nop 2\n"
    "v_add_f32_dpp v56, v56, v56 row_ror:8 row_mask:0xf bank_mask:0xf\n"
    "v_add_f32_dpp v57, v57, v57 row_ror:8 row_mask:0xf bank_mask:0xf\n"
    "v_add_f32_dpp v58, v58, v58 row_ror:8 row_mask:0xf bank_mask:0xf\n"
    "v_add_f32_dpp v59, v59, v59 row_ror:8 row_mask:0xf bank_mask:0xf\n"
    "v_cvt_pk_bf16_f32 v115, v58, v59\n"
    "v_cvt_pk_bf16_f32 v114, v56, v57\n"
    "v_min_u32 v115, -2, v115\n"
    "buffer_store_dwordx4 v[112:115], v14, s[16:19], s48 offen sc1\n"
    "ds_write_b32 v118, v119 offset:12\n"
    "v_cmp_ne_u32 vcc, 0, v116\n"
    "v_readfirstlane_b32 s70, v116\n"
    "s_nop 0\n"
    "s_andn2_b32 s38, 7, vcc_lo\n"
    "s_cbranch_scc0 LS1K%=\n"
    "s_mov_b32 s37, 0x80000\n"
    "LS1W%=:\n"
    "s_mov_b64 exec, 7\n"
    "buffer_load_dword v116, v0, s[16:19], s59 offen sc1\n"
    "s_mov_b64 exec, -1\n"
    "s_waitcnt vmcnt(0)\n"
    "v_cmp_ne_u32 vcc, 0, v116\n"
    "v_readfirstlane_b32 s70, v116\n"
    "s_nop 0\n"
    "s_andn2_b32 s38, 7, vcc_lo\n"
    "s_cbranch_scc0 LS1K%=\n"
    "s_sleep 2\n"
    "s_sub_u32 s37, s37, 1\n"
    "s_cbranch_scc0 LS1W%=\n"
    "s_branch LFAIL%=\n"
    "LS1K%=:\n"
    "s_lshr_b32 s38, s70, 16\n"
    "s_cmp_gt_i32 s38, s1\n"
    "s_cbranch_scc1 LREC%=\n"
    "s_mov_b32 s32, s62\n"
    "s_mov_b32 s68, s62\n"
    "s_xor_b32 s46, s46, 0x80000\n"
    "s_add_u32 s47, s46, 0x40000\n"
    "s_bfe_u32 s64, s36, 0x10001\n"
    "s_add_u32 s44, s44, s8\n"
    "s_add_u32 s59, s59, 128\n"
    "v_mov_b64 v[112:113], 0\n"
    "v_mov_b64 v[114:115], 0\n"
    "v_mov_b64 v[116:117], 0\n"
    "v_mov_b64 v[118:119], 0\n"
    "v_mfma_f32_32x32x8_bf16 v[48:63], v[112:113], v[112:113], 0\n"
    "v_lshrrev_b32 v40, 4, v0\n"
    "v_and_b32 v40, 0x30, v40\n"
    "v_lshl_add_u32 v40, v40, 4, v0\n"
    "v_mov_b32 v41, 158224\n"
    "s_add_u32 s73, s49, s11\n"
    "s_add_u32 s73, s73, 4096\n"
    "s_add_u32 m0, s11, 151552\n"
    "s_branch LBM%=\n"
    "LGR%=:\n"
    "s_add_u32 s33, s33, 1\n"
    "s_cmp_gt_u32 s33, 0x40000\n"
    "s_cbranch_scc1 LFAIL%=\n"
    "s_and_b32 s38, s33, 63\n"
    "s_cbranch_scc1 LE4%=\n"
    "s_mov_b64 exec, 7\n"
    "buffer_load_dword v116, v0, s[16:19], s59 offen sc1\n"
    "s_mov_b64 exec, -1\n"
    "s_waitcnt vmcnt(0)\n"
    "v_cmp_ne_u32 vcc, 0, v116\n"
    "v_readfirstlane_b32 s70, v116\n"
    "s_nop 0\n"
    "s_andn2_b32 s38, 7, vcc_lo\n"
    "s_cbranch_scc1 LE4%=\n"
    "s_lshr_b32 s38, s70, 16\n"
    "s_cmp_gt_i32 s38, s1\n"
    "s_cbranch_scc1 LREC%=\n"
    "LE4%=:\n"
    "s_add_u32 s44, s44, s13\n"
    "LGM%=:\n"
    "s_memtime s[40:41]\n"
    "s_add_u32 s42, s21, s9\n"
    "s_waitcnt lgkmcnt(0)\n"
    "s_sub_u32 s40, s40, s42\n"
    "s_sub_u32 s41, s44, s40\n"
    "s_cmp_ge_i32 s41, 0\n"
    "s_cbranch_scc1 LGMD%=\n"
    "LGMI%=:\n"
    "s_add_u32 s44, s44, s13\n"
    "s_add_u32 s41, s41, s13\n"
    "s_cbranch_scc0 LGMI%=\n"
    "s_branch LGM%=\n"
    "LGMD%=:\n"
    "s_add_u32 s39, s44, s9\n"
    "s_add_u32 s38, s44, s8\n"
    "v_mov_b32 v60, s72\n"
    "v_mov_b32 v61, s38\n"
    "ds_write_b32 v60, v61 offset:16\n"
    "s_branch LUG%=\n"
    "LMR%=:\n"
    "s_add_u32 s33, s33, 1\n"
    "s_cmp_gt_u32 s33, 0x40000\n"
    "s_cbranch_scc1 LFAIL%=\n"
    "s_add_u32 s39, s44, s13\n"
    "s_mov_b32 s54, 0x80000000\n"
    "LSL%=:\n"
    "s_mov_b32 s44, s39\n"
    "s_waitcnt lgkmcnt(0)\n"
    "s_barrier\n"
    "s_branch LPRE%=\n"
    "LW1%=:\n"
    "v_mov_b32 v64, 158240\n"
    "s_mov_b32 s37, 0x80000\n"
    "LOW%=:\n"
    "ds_read_b128 v[68:71], v64 offset:16\n"
    "s_waitcnt lgkmcnt(0)\n"
    "v_min3_u32 v68, v68, v70, v71\n"
    "v_cmp_eq_u32 vcc, s36, v68\n"
    "s_cbranch_vccnz LOK%=\n"
    "s_sleep 0\n"
    "s_sub_u32 s37, s37, 1\n"
    "s_cbranch_scc0 LOW%=\n"
    "s_branch LFAIL%=\n"
    "LOK%=:\n"
    "buffer_store_dword v1, off, s[16:19], s58 sc1\n"
    "s_add_u32 s58, s58, 4\n"
    "s_bitset0_b32 s58, 31\n"
    "s_waitcnt vmcnt(3)\n"
    "v_cmp_ne_u32 vcc, 0, v116\n"
    "v_readfirstlane_b32 s70, v116\n"
    "s_nop 0\n"
    "s_andn2_b32 s38, 7, vcc_lo\n"
    "s_cbranch_scc0 LS2K%=\n"
    "s_mov_b32 s37, 0x80000\n"
    "LS2W%=:\n"
    "s_mov_b64 exec, 7\n"
    "buffer_load_dword v116, v0, s[16:19], s59 offen sc1\n"
    "s_mov_b64 exec, -1\n"
    "s_waitcnt vmcnt(0)\n"
    "v_cmp_ne_u32 vcc, 0, v116\n"
    "v_readfirstlane_b32 s70, v116\n"
    "s_nop 0\n"
    "s_andn2_b32 s38, 7, vcc_lo\n"
    "s_cbranch_scc0 LS2K%=\n"
    "s_sleep 2\n"
    "s_sub_u32 s37, s37, 1\n"
    "s_cbranch_scc0 LS2W%=\n"
    "s_branch LFAIL%=\n"
    "LS2K%=:\n"
    "s_lshr_b32 s38, s70, 16\n"
    "s_cmp_gt_i32 s38, s1\n"
    "s_cbranch_scc1 LREC%=\n"
    "s_add_u32 s53, s50, s55\n"
    "s_mov_b32 s49, s48\n"
    "s_add_u32 s51, s53, 0\n"
    "s_lshr_b32 s38, s51, 8\n"
    "s_xor_b32 s38, s38, s56\n"
    "s_and_b32 s38, s38, 0x2000\n"
    "s_or_b32 s51, s51, s38\n"
    "s_add_u32 s52, s53, 524288\n"
    "s_lshr_b32 s38, s52, 8\n"
    "s_xor_b32 s38, s38, s56\n"
    "s_and_b32 s38, s38, 0x2000\n"
    "s_or_b32 s52, s52, s38\n"
    "s_add_u32 s53, s53, 1048576\n"
    "s_lshr_b32 s38, s53, 8\n"
    "s_xor_b32 s38, s38, s56\n"
    "s_and_b32 s38, s38, 0x2000\n"
    "s_or_b32 s53, s53, s38\n"
    "s_add_u32 s66, s44, s12\n"
    "s_mov_b32 s32, s62\n"
    "s_mov_b32 s68, s62\n"
    "s_xor_b32 s46, s46, 0x80000\n"
    "s_add_u32 s47, s46, 0x40000\n"
    "s_bfe_u32 s64, s36, 0x10001\n"
    "s_add_u32 s44, s44, s8\n"
    "s_add_u32 s59, s59, 128\n"
    "ds_read_b128 v[64:67], v7 offset:0\n"
    "ds_read_b128 v[68:71], v7 offset:1024\n"
    "ds_read_b128 v[72:75], v7 offset:2048\n"
    "ds_read_b128 v[76:79], v7 offset:3072\n"
    "ds_read_b128 v[80:83], v7 offset:4096\n"
    "ds_read_b128 v[84:87], v7 offset:5120\n"
    "ds_read_b128 v[88:91], v7 offset:6144\n"
    "ds_read_b128 v[92:95], v7 offset:7168\n"
    "ds_read_b128 v[96:99], v7 offset:8192\n"
    "ds_read_b128 v[100:103], v7 offset:9216\n"
    "ds_read_b128 v[104:107], v7 offset:10240\n"
    "ds_read_b128 v[108:111], v7 offset:11264\n"
    "v_mov_b64 v[112:113], 0\n"
    "v_mov_b64 v[114:115], 0\n"
    "v_mov_b64 v[116:117], 0\n"
    "v_mov_b64 v[118:119], 0\n"
    "v_mfma_f32_32x32x8_bf16 v[48:63], v[112:113], v[112:113], 0\n"
    "v_lshrrev_b32 v40, 4, v0\n"
    "v_and_b32 v40, 0x30, v40\n"
    "v_lshl_add_u32 v40, v40, 4, v0\n"
    "v_mov_b32 v41, 158224\n"
    "s_add_u32 s73, s49, s11\n"
    "s_add_u32 s73, s73, 4096\n"
    "s_add_u32 m0, s11, 151552\n"
    "s_mov_b32 s37, 0x80000\n"
    "LKW%=:\n"
    "ds_read_b64 v[16:17], v41 offset:16\n"
    "s_waitcnt lgkmcnt(0)\n"
    "v_min_u32 v16, v16, v17\n"
    "v_cmp_eq_u32 vcc, s36, v16\n"
    "s_cbranch_vccnz LKK%=\n"
    "s_sleep 1\n"
    "s_sub_u32 s37, s37, 1\n"
    "s_cbranch_scc0 LKW%=\n"
    "s_branch LFAIL%=\n"
    "LKK%=:\n"
    "LUF%=:\n"
    "s_memtime s[40:41]\n"
    "s_waitcnt lgkmcnt(0)\n"
    "s_sub_u32 s40, s66, s40\n"
    "s_sub_u32 s41, s40, 253\n"
    "s_cmp_lt_u32 s41, 16384\n"
    "s_cbranch_scc1 LUF%=\n"
    "s_cmp_lt_u32 s40, 253\n"
    "s_cselect_b32 s40, s40, 0\n"
    "s_and_b32 s40, s40, 0xfc\n"
    "s_getpc_b64 s[42:43]\n"
    "s_sub_u32 s40, 272, s40\n"
    "s_add_u32 s42, s42, s40\n"
    "s_addc_u32 s43, s43, 0\n"
    "s_setpc_b64 s[42:43]\n"
    ".rept 63\n"
    "s_nop 0\n"
    ".endr\n"
    "s_sub_u32 s51, s51, 2048\n"
    "s_mov_b32 m0, 122880\n"
    "s_nop 0\n"
    "buffer_load_dwordx4 v0, s[16:19], s51 offen offset:0 sc1 lds\n"
    "buffer_load_dwordx4 v0, s[16:19], s51 offen offset:1024 sc1 lds\n"
    "buffer_load_dwordx4 v0, s[16:19], s51 offen offset:2048 sc1 lds\n"
    "buffer_load_dwordx4 v0, s[16:19], s51 offen offset:3072 sc1 lds\n"
    "s_mov_b32 m0, 126976\n"
    "s_add_u32 s51, s51, 4096\n"
    "buffer_load_dwordx4 v0, s[16:19], s51 offen offset:0 sc1 lds\n"
    "buffer_load_dwordx4 v0, s[16:19], s51 offen offset:1024 sc1 lds\n"
    "buffer_load_dwordx4 v0, s[16:19], s51 offen offset:2048 sc1 lds\n"
    "buffer_load_dwordx4 v0, s[16:19], s51 offen offset:3072 sc1 lds\n"
    "LMN%=:\n"
    "LMM%=:\n"
    "s_memtime s[40:41]\n"
    "s_add_u32 s42, s21, s10\n"
    "s_waitcnt lgkmcnt(0)\n"
    "s_sub_u32 s40, s40, s42\n"
    "s_sub_u32 s41, s44, s40\n"
    "s_cmp_ge_i32 s41, 0\n"
    "s_cbranch_scc1 LMMD%=\n"
    "LMMI%=:\n"
    "s_add_u32 s44, s44, s13\n"
    "s_add_u32 s41, s41, s13\n"
    "s_cbranch_scc0 LMMI%=\n"
    "s_branch LMM%=\n"
    "LMMD%=:\n"
    "s_add_u32 s39, s44, s10\n"
    "v_mov_b32 v44, s72\n"
    "v_mov_b32 v45, s44\n"
    "ds_write_b32 v44, v45 offset:16\n"
    "s_mov_b32 m0, 147456\n"
    "s_mov_b32 s37, 64\n"
    "s_waitcnt lgkmcnt(0)\n"
    "LUL%=:\n"
    "s_memtime s[40:41]\n"
    "s_waitcnt lgkmcnt(0)\n"
    "s_sub_u32 s40, s39, s40\n"
    "s_sub_u32 s41, s40, 253\n"
    "s_cmp_lt_u32 s41, 16384\n"
    "s_cbranch_scc1 LUL%=\n"
    "s_cmp_lt_u32 s40, 253\n"
    "s_cselect_b32 s40, s40, 0\n"
    "s_and_b32 s40, s40, 0xfc\n"
    "s_getpc_b64 s[42:43]\n"
    "s_sub_u32 s40, 272, s40\n"
    "s_add_u32 s42, s42, s40\n"
    "s_addc_u32 s43, s43, 0\n"
    "s_setpc_b64 s[42:43]\n"
    ".rept 63\n"
    "s_nop 0\n"
    ".endr\n"
    "buffer_load_dwordx4 v0, s[16:19], s49 offen offset:0 sc1 lds\n"
    "buffer_load_dwordx4 v0, s[16:19], s49 offen offset:1024 sc1 lds\n"
    "buffer_load_dwordx4 v0, s[16:19], s49 offen offset:2048 sc1 lds\n"
    "buffer_load_dwordx4 v0, s[16:19], s49 offen offset:3072 sc1 lds\n"
    "s_mov_b32 m0, 152576\n"
    "s_getpc_b64 s[42:43]\n"
    "s_add_u32 s42, s42, s0\n"
    "s_addc_u32 s43, s43, 0\n"
    "s_setpc_b64 s[42:43]\n"
    ".rept 63\n"
    "s_nop 1\n"
    ".endr\n"
    "LDW%=:\n"
    "ds_read2_b32 v[46:47], v44 offset0:2 offset1:3\n"
    "s_waitcnt lgkmcnt(0)\n"
    "v_readfirstlane_b32 s39, v47\n"
    "v_readfirstlane_b32 s40, v46\n"
    "s_sub_u32 s38, s39, s44\n"
    "s_cmp_gt_i32 s38, 0\n"
    "s_cselect_b32 s44, s39, s44\n"
    "s_cbranch_scc1 LMN%=\n"
    "s_cmp_eq_u32 s40, s36\n"
    "s_cbranch_scc1 LBM%=\n"
    "s_sub_u32 s37, s37, 1\n"
    "s_cbranch_scc0 LDW%=\n"
    "s_branch LBM%=\n"
    "LW2%=:\n"
    "s_waitcnt vmcnt(2)\n"
    "v_cmp_ne_u32 vcc, 0, v116\n"
    "v_readfirstlane_b32 s70, v116\n"
    "s_nop 0\n"
    "s_andn2_b32 s38, 7, vcc_lo\n"
    "s_cbranch_scc1 LE3%=\n"
    "s_lshr_b32 s38, s70, 16\n"
    "s_cmp_gt_i32 s38, s1\n"
    "s_cbranch_scc1 LREC%=\n"
    "LE3%=:\n"
    "ds_read_b128 v[16:19], v14 offset:0\n"
    "v_add_u32 v118, 122880, v0\n"
    "ds_read_b128 v[32:35], v118 offset:0\n"
    "ds_read_b128 v[48:51], v118 offset:4096\n"
    "ds_read_b128 v[20:23], v14 offset:1024\n"
    "ds_read_b128 v[36:39], v118 offset:1024\n"
    "ds_read_b128 v[52:55], v118 offset:5120\n"
    "v_mov_b32 v111, 0xd0289000\n"
    "v_cndmask_b32 v80, v111, 0, s[80:81]\n"
    "v_cndmask_b32 v81, v111, 0, s[82:83]\n"
    "v_cndmask_b32 v82, v111, 0, s[84:85]\n"
    "v_cndmask_b32 v83, v111, 0, s[86:87]\n"
    "v_cndmask_b32 v84, v111, 0, s[88:89]\n"
    "v_cndmask_b32 v85, v111, 0, s[90:91]\n"
    "v_cndmask_b32 v86, v111, 0, s[92:93]\n"
    "v_cndmask_b32 v87, v111, 0, s[94:95]\n"
    "v_lshrrev_b32 v112, 1, v0\n"
    "v_add_u32 v112, 149760, v112\n"
    "ds_read_b128 v[24:27], v14 offset:2048\n"
    "ds_read_b128 v[40:43], v118 offset:2048\n"
    "ds_read_b128 v[56:59], v118 offset:6144\n"
    "s_waitcnt lgkmcnt(7)\n"
    "v_mfma_f32_16x16x32_bf16 v[80:83], v[32:35], v[16:19], v[80:83]\n"
    "ds_read_b128 v[28:31], v14 offset:3072\n"
    "ds_read_b128 v[44:47], v118 offset:3072\n"
    "s_waitcnt lgkmcnt(8)\n"
    "v_mfma_f32_16x16x32_bf16 v[84:87], v[48:51], v[16:19], v[84:87]\n"
    "ds_read_b128 v[60:63], v118 offset:7168\n"
    "s_waitcnt lgkmcnt(7)\n"
    "v_mov_b32 v114, s72\n"
    "v_mfma_f32_16x16x32_bf16 v[80:83], v[36:39], v[20:23], v[80:83]\n"
    "ds_read_b128 v[64:67], v6 offset:0\n"
    "ds_read_b128 v[68:71], v6 offset:64\n"
    "s_waitcnt lgkmcnt(8)\n"
    "v_mfma_f32_16x16x32_bf16 v[84:87], v[52:55], v[20:23], v[84:87]\n"
    "ds_read_b128 v[72:75], v6 offset:128\n"
    "s_waitcnt lgkmcnt(7)\n"
    "v_mov_b32 v105, 0\n"
    "v_mfma_f32_16x16x32_bf16 v[80:83], v[40:43], v[24:27], v[80:83]\n"
    "ds_read_b128 v[76:79], v6 offset:192\n"
    "ds_read_b64 v[92:93], v112 offset:0\n"
    "s_waitcnt lgkmcnt(8)\n"
    "v_mfma_f32_16x16x32_bf16 v[84:87], v[56:59], v[24:27], v[84:87]\n"
    "ds_read_b64 v[94:95], v112 offset:512\n"
    "s_waitcnt lgkmcnt(7)\n"
    "v_mov_b32 v106, 0\n"
    "v_mfma_f32_16x16x32_bf16 v[80:83], v[44:47], v[28:31], v[80:83]\n"
    "ds_read_b64 v[96:97], v112 offset:1024\n"
    "ds_read_b64 v[98:99], v112 offset:1536\n"
    "s_waitcnt lgkmcnt(8)\n"
    "v_mfma_f32_16x16x32_bf16 v[84:87], v[60:63], v[28:31], v[84:87]\n"
    "ds_write_b32 v114, v119 offset:24\n"
    "s_waitcnt lgkmcnt(8)\n"
    "v_mfma_f32_16x16x32_bf16 v[88:91], v[64:67], v[16:19], 0\n"
    "ds_read_b128 v[32:35], v118 offset:8192\n"
    "ds_read_b128 v[36:39], v118 offset:9216\n"
    "s_waitcnt lgkmcnt(9)\n"
    "v_mfma_f32_16x16x32_bf16 v[88:91], v[68:71], v[20:23], v[88:91]\n"
    "ds_read_b128 v[40:43], v118 offset:10240\n"
    "ds_read_b128 v[44:47], v118 offset:11264\n"
    "s_waitcnt lgkmcnt(10)\n"
    "v_mfma_f32_16x16x32_bf16 v[88:91], v[72:75], v[24:27], v[88:91]\n"
    "ds_read_b128 v[48:51], v118 offset:12288\n"
    "ds_read_b128 v[52:55], v118 offset:13312\n"
    "s_waitcnt lgkmcnt(11)\n"
    "v_max3_f32 v108, v80, v81, v82\n"
    "v_max3_f32 v109, v83, v84, v85\n"
    "v_mfma_f32_16x16x32_bf16 v[88:91], v[76:79], v[28:31], v[88:91]\n"
    "ds_read_b128 v[56:59], v118 offset:14336\n"
    "ds_read_b128 v[60:63], v118 offset:15360\n"
    "v_max3_f32 v108, v108, v86, v87\n"
    "v_max_f32 v108, v108, v109\n"
    "v_mov_b32 v109, v108\n"
    "s_nop 1\n"
    "v_permlane32_swap_b32 v108, v109\n"
    "v_max_f32 v108, v108, v109\n"
    "v_max3_f32 v108, v108, v88, v89\n"
    "v_max3_f32 v108, v108, v90, v91\n"
    "v_mov_b32 v109, v108\n"
    "s_nop 1\n"
    "v_permlane16_swap_b32 v108, v109\n"
    "v_max_f32 v108, v108, v109\n"
    "v_mul_f32 v108, 0xbe0293ee, v108\n"
    "v_pk_fma_f32 v[80:81], v[80:81], s[64:65], v[108:109] op_sel:[0,1,0] op_sel_hi:[1,1,0]\n"
    "v_pk_fma_f32 v[82:83], v[82:83], s[64:65], v[108:109] op_sel:[0,1,0] op_sel_hi:[1,1,0]\n"
    "v_pk_fma_f32 v[84:85], v[84:85], s[64:65], v[108:109] op_sel:[0,1,0] op_sel_hi:[1,1,0]\n"
    "v_pk_fma_f32 v[86:87], v[86:87], s[64:65], v[108:109] op_sel:[0,1,0] op_sel_hi:[1,1,0]\n"
    "v_pk_fma_f32 v[88:89], v[88:89], s[64:65], v[108:109] op_sel:[0,1,0] op_sel_hi:[1,1,0]\n"
    "v_exp_f32 v80, v80\n"
    "v_exp_f32 v81, v81\n"
    "v_exp_f32 v82, v82\n"
    "v_exp_f32 v83, v83\n"
    "v_exp_f32 v84, v84\n"
    "v_exp_f32 v85, v85\n"
    "v_exp_f32 v86, v86\n"
    "v_exp_f32 v87, v87\n"
    "v_exp_f32 v88, v88\n"
    "v_exp_f32 v89, v89\n"
    "v_pk_add_f32 v[108:109], v[80:81], v[82:83]\n"
    "v_pk_add_f32 v[110:111], v[84:85], v[86:87]\n"
    "v_pk_add_f32 v[108:109], v[108:109], v[88:89]\n"
    "v_pk_add_f32 v[108:109], v[108:109], v[110:111]\n"
    "v_add_f32 v108, v108, v109\n"
    "v_mov_b32 v109, v108\n"
    "s_nop 1\n"
    "v_permlane16_swap_b32 v108, v109\n"
    "v_add_f32 v108, v108, v109\n"
    "v_mov_b32 v109, v108\n"
    "s_nop 1\n"
    "v_permlane32_swap_b32 v108, v109\n"
    "v_add_f32 v108, v108, v109\n"
    "v_rcp_f32 v108, v108\n"
    "s_nop 0\n"
    "v_pk_mul_f32 v[80:81], v[80:81], v[108:109] op_sel_hi:[1,0]\n"
    "v_pk_mul_f32 v[82:83], v[82:83], v[108:109] op_sel_hi:[1,0]\n"
    "v_pk_mul_f32 v[84:85], v[84:85], v[108:109] op_sel_hi:[1,0]\n"
    "v_pk_mul_f32 v[86:87], v[86:87], v[108:109] op_sel_hi:[1,0]\n"
    "v_pk_mul_f32 v[88:89], v[88:89], v[108:109] op_sel_hi:[1,0]\n"
    "v_cvt_pk_bf16_f32 v100, v80, v81\n"
    "v_cvt_pk_bf16_f32 v101, v82, v83\n"
    "v_cvt_pk_bf16_f32 v102, v84, v85\n"
    "v_cvt_pk_bf16_f32 v103, v86, v87\n"
    "v_cvt_pk_bf16_f32 v104, v88, v89\n"
    "v_cvt_pk_bf16_f32 v107, v88, v89\n"
    "s_waitcnt lgkmcnt(7)\n"
    "v_mfma_f32_16x16x32_bf16 v[16:19], v[32:35], v[100:103], 0\n"
    "ds_read_b128 v[64:67], v7 offset:0\n"
    "s_waitcnt lgkmcnt(7)\n"
    "v_mfma_f32_16x16x32_bf16 v[20:23], v[36:39], v[100:103], 0\n"
    "ds_read_b128 v[68:71], v7 offset:1024\n"
    "v_mfma_f32_16x16x16_bf16 v[16:19], v[92:93], v[104:105], v[16:19]\n"
    "v_mfma_f32_16x16x16_bf16 v[20:23], v[92:93], v[106:107], v[20:23]\n"
    "s_waitcnt lgkmcnt(7)\n"
    "v_mfma_f32_16x16x32_bf16 v[24:27], v[40:43], v[100:103], 0\n"
    "ds_read_b128 v[72:75], v7 offset:2048\n"
    "s_waitcnt lgkmcnt(7)\n"
    "v_mfma_f32_16x16x32_bf16 v[28:31], v[44:47], v[100:103], 0\n"
    "ds_read_b128 v[76:79], v7 offset:3072\n"
    "v_cvt_pk_bf16_f32 v112, v16, v17\n"
    "v_cvt_pk_bf16_f32 v113, v18, v19\n"
    "v_mfma_f32_16x16x16_bf16 v[24:27], v[94:95], v[104:105], v[24:27]\n"
    "v_cvt_pk_bf16_f32 v114, v20, v21\n"
    "v_cvt_pk_bf16_f32 v115, v22, v23\n"
    "v_mfma_f32_16x16x16_bf16 v[28:31], v[94:95], v[106:107], v[28:31]\n"
    "ds_write_b128 v14, v[112:115] offset:0\n"
    "s_waitcnt lgkmcnt(8)\n"
    "v_mfma_f32_16x16x32_bf16 v[16:19], v[48:51], v[100:103], 0\n"
    "ds_read_b128 v[80:83], v7 offset:4096\n"
    "s_waitcnt lgkmcnt(8)\n"
    "v_mfma_f32_16x16x32_bf16 v[20:23], v[52:55], v[100:103], 0\n"
    "ds_read_b128 v[84:87], v7 offset:5120\n"
    "v_cvt_pk_bf16_f32 v108, v24, v25\n"
    "v_cvt_pk_bf16_f32 v109, v26, v27\n"
    "v_mfma_f32_16x16x16_bf16 v[16:19], v[96:97], v[104:105], v[16:19]\n"
    "v_cvt_pk_bf16_f32 v110, v28, v29\n"
    "v_cvt_pk_bf16_f32 v111, v30, v31\n"
    "v_mfma_f32_16x16x16_bf16 v[20:23], v[96:97], v[106:107], v[20:23]\n"
    "ds_write_b128 v14, v[108:111] offset:1024\n"
    "s_waitcnt lgkmcnt(9)\n"
    "v_mfma_f32_16x16x32_bf16 v[24:27], v[56:59], v[100:103], 0\n"
    "s_waitcnt lgkmcnt(8)\n"
    "v_mfma_f32_16x16x32_bf16 v[28:31], v[60:63], v[100:103], 0\n"
    "v_cvt_pk_bf16_f32 v112, v16, v17\n"
    "v_cvt_pk_bf16_f32 v113, v18, v19\n"
    "v_mfma_f32_16x16x16_bf16 v[24:27], v[98:99], v[104:105], v[24:27]\n"
    "v_cvt_pk_bf16_f32 v114, v20, v21\n"
    "v_cvt_pk_bf16_f32 v115, v22, v23\n"
    "v_mfma_f32_16x16x16_bf16 v[28:31], v[98:99], v[106:107], v[28:31]\n"
    "ds_write_b128 v14, v[112:115] offset:2048\n"
    "ds_read_b128 v[88:91], v7 offset:6144\n"
    "ds_read_b128 v[92:95], v7 offset:7168\n"
    "ds_read_b128 v[96:99], v7 offset:8192\n"
    "ds_read_b128 v[100:103], v7 offset:9216\n"
    "v_cvt_pk_bf16_f32 v108, v24, v25\n"
    "v_cvt_pk_bf16_f32 v109, v26, v27\n"
    "s_nop 0\n"
    "v_cvt_pk_bf16_f32 v110, v28, v29\n"
    "v_cvt_pk_bf16_f32 v111, v30, v31\n"
    "ds_write_b128 v14, v[108:111] offset:3072\n"
    "v_cmp_ne_u32 vcc, 0, v116\n"
    "v_readfirstlane_b32 s70, v116\n"
    "s_nop 0\n"
    "s_andn2_b32 s38, 7, vcc_lo\n"
    "s_cbranch_scc0 LS3K%=\n"
    "s_mov_b32 s37, 0x80000\n"
    "LS3W%=:\n"
    "s_mov_b64 exec, 7\n"
    "buffer_load_dword v116, v0, s[16:19], s59 offen sc1\n"
    "s_mov_b64 exec, -1\n"
    "s_waitcnt vmcnt(0)\n"
    "v_cmp_ne_u32 vcc, 0, v116\n"
    "v_readfirstlane_b32 s70, v116\n"
    "s_nop 0\n"
    "s_andn2_b32 s38, 7, vcc_lo\n"
    "s_cbranch_scc0 LS3K%=\n"
    "s_sleep 2\n"
    "s_sub_u32 s37, s37, 1\n"
    "s_cbranch_scc0 LS3W%=\n"
    "s_branch LFAIL%=\n"
    "LS3K%=:\n"
    "s_lshr_b32 s38, s70, 16\n"
    "s_cmp_gt_i32 s38, s1\n"
    "s_cbranch_scc1 LREC%=\n"
    "s_add_u32 s53, s50, s55\n"
    "s_mov_b32 s49, s48\n"
    "s_add_u32 s51, s53, 0\n"
    "s_lshr_b32 s38, s51, 8\n"
    "s_xor_b32 s38, s38, s56\n"
    "s_and_b32 s38, s38, 0x2000\n"
    "s_or_b32 s51, s51, s38\n"
    "s_add_u32 s52, s53, 524288\n"
    "s_lshr_b32 s38, s52, 8\n"
    "s_xor_b32 s38, s38, s56\n"
    "s_and_b32 s38, s38, 0x2000\n"
    "s_or_b32 s52, s52, s38\n"
    "s_add_u32 s53, s53, 1048576\n"
    "s_lshr_b32 s38, s53, 8\n"
    "s_xor_b32 s38, s38, s56\n"
    "s_and_b32 s38, s38, 0x2000\n"
    "s_or_b32 s53, s53, s38\n"
    "s_mov_b32 s32, s62\n"
    "s_mov_b32 s68, s62\n"
    "s_xor_b32 s46, s46, 0x80000\n"
    "s_add_u32 s47, s46, 0x40000\n"
    "s_bfe_u32 s64, s36, 0x10001\n"
    "s_add_u32 s44, s44, s8\n"
    "s_add_u32 s59, s59, 128\n"
    "s_waitcnt vmcnt(2)\n"
    "s_waitcnt lgkmcnt(0)\n"
    "ds_read_b128 v[104:107], v7 offset:10240\n"
    "ds_read_b128 v[108:111], v7 offset:11264\n"
    "v_mov_b64 v[112:113], 0\n"
    "v_mov_b64 v[114:115], 0\n"
    "v_mov_b64 v[116:117], 0\n"
    "v_mov_b64 v[118:119], 0\n"
    "v_mfma_f32_32x32x8_bf16 v[48:63], v[112:113], v[112:113], 0\n"
    "v_lshrrev_b32 v40, 4, v0\n"
    "v_and_b32 v40, 0x30, v40\n"
    "v_lshl_add_u32 v40, v40, 4, v0\n"
    "v_mov_b32 v41, 158224\n"
    "s_add_u32 s73, s49, s11\n"
    "s_add_u32 s73, s73, 4096\n"
    "s_add_u32 m0, s11, 151552\n"
    "s_branch LBM%=\n"
    "s_waitcnt lgkmcnt(0)\n"
    "LPRE%=:\n"
    "s_nop 7\n"
    "ds_read_b128 v[64:67], v7 offset:0\n"
    "ds_read_b128 v[68:71], v7 offset:1024\n"
    "ds_read_b128 v[72:75], v7 offset:2048\n"
    "ds_read_b128 v[76:79], v7 offset:3072\n"
    "ds_read_b128 v[80:83], v7 offset:4096\n"
    "ds_read_b128 v[84:87], v7 offset:5120\n"
    "ds_read_b128 v[88:91], v7 offset:6144\n"
    "ds_read_b128 v[92:95], v7 offset:7168\n"
    "ds_read_b128 v[96:99], v7 offset:8192\n"
    "ds_read_b128 v[100:103], v7 offset:9216\n"
    "ds_read_b128 v[104:107], v7 offset:10240\n"
    "ds_read_b128 v[108:111], v7 offset:11264\n"
    "v_mov_b64 v[112:113], 0\n"
    "v_mov_b64 v[114:115], 0\n"
    "v_mov_b64 v[116:117], 0\n"
    "v_mov_b64 v[118:119], 0\n"
    "v_mfma_f32_32x32x8_bf16 v[48:63], v[112:113], v[112:113], 0\n"
    "v_lshrrev_b32 v40, 4, v0\n"
    "v_and_b32 v40, 0x30, v40\n"
    "v_lshl_add_u32 v40, v40, 4, v0\n"
    "v_mov_b32 v41, 158224\n"
    "s_add_u32 s73, s49, s11\n"
    "s_add_u32 s73, s73, 4096\n"
    "s_add_u32 m0, s11, 151552\n"
    "s_cmp_lg_u32 s61, 1\n"
    "s_cbranch_scc1 LBM%=\n"
    "s_branch LMN%=\n"
    "LREC%=:\n"
    "s_mov_b32 s1, s38\n"
    "s_andn2_b32 s39, 31, s70\n"
    "s_add_u32 s53, s50, s55\n"
    "s_mov_b32 s49, s48\n"
    "s_add_u32 s51, s53, 0\n"
    "s_lshr_b32 s38, s51, 8\n"
    "s_xor_b32 s38, s38, s56\n"
    "s_and_b32 s38, s38, 0x2000\n"
    "s_or_b32 s51, s51, s38\n"
    "s_add_u32 s52, s53, 524288\n"
    "s_lshr_b32 s38, s52, 8\n"
    "s_xor_b32 s38, s38, s56\n"
    "s_and_b32 s38, s38, 0x2000\n"
    "s_or_b32 s52, s52, s38\n"
    "s_add_u32 s53, s53, 1048576\n"
    "s_lshr_b32 s38, s53, 8\n"
    "s_xor_b32 s38, s38, s56\n"
    "s_and_b32 s38, s38, 0x2000\n"
    "s_or_b32 s53, s53, s38\n"
    "s_mov_b32 s32, 30464\n"
    "s_mov_b32 s68, 22272\n"
    "s_xor_b32 s46, s46, 0x80000\n"
    "s_add_u32 s47, s46, 0x40000\n"
    "s_bfe_u32 s64, s36, 0x10001\n"
    "s_add_u32 s44, s44, 5150\n"
    "s_add_u32 s59, s59, 128\n"
    "s_lshl_b32 s49, s36, 14\n"
    "s_add_u32 s49, s49, s67\n"
    "s_andn2_b32 s70, s36, 7\n"
    "s_add_u32 s70, s70, s36\n"
    "s_lshl_b32 s70, s70, 10\n"
    "s_add_u32 s70, s70, s69\n"
    "s_nop 7\n"
    "s_waitcnt lgkmcnt(0)\n"
    "buffer_load_dwordx4 v[16:19], v0, s[16:19], s70 offen sc1\n"
    "s_barrier\n"
    "s_add_u32 s73, s49, s62\n"
    "s_add_u32 m0, s62, 147456\n"
    "s_nop 0\n"
    "buffer_load_dwordx4 v0, s[16:19], s73 offen offset:0 sc1 lds\n"
    "buffer_load_dwordx4 v0, s[16:19], s73 offen offset:1024 sc1 lds\n"
    "s_add_u32 m0, s62, 122880\n"
    "s_nop 0\n"
    "buffer_load_dwordx4 v0, s[16:19], s51 offen offset:0 sc1 lds\n"
    "buffer_load_dwordx4 v0, s[16:19], s51 offen offset:1024 sc1 lds\n"
    "s_add_u32 m0, s62, 131072\n"
    "s_nop 0\n"
    "buffer_load_dwordx4 v0, s[16:19], s52 offen offset:0 sc1 lds\n"
    "buffer_load_dwordx4 v0, s[16:19], s52 offen offset:1024 sc1 lds\n"
    "s_add_u32 m0, s62, 139264\n"
    "s_nop 0\n"
    "buffer_load_dwordx4 v0, s[16:19], s53 offen offset:0 sc1 lds\n"
    "buffer_load_dwordx4 v0, s[16:19], s53 offen offset:1024 sc1 lds\n"
    "ds_read_b128 v[64:67], v7 offset:0\n"
    "ds_read_b128 v[68:71], v7 offset:1024\n"
    "ds_read_b128 v[72:75], v7 offset:2048\n"
    "ds_read_b128 v[76:79], v7 offset:3072\n"
    "ds_read_b128 v[80:83], v7 offset:4096\n"
    "ds_read_b128 v[84:87], v7 offset:5120\n"
    "ds_read_b128 v[88:91], v7 offset:6144\n"
    "ds_read_b128 v[92:95], v7 offset:7168\n"
    "ds_read_b128 v[96:99], v7 offset:8192\n"
    "ds_read_b128 v[100:103], v7 offset:9216\n"
    "ds_read_b128 v[104:107], v7 offset:10240\n"
    "ds_read_b128 v[108:111], v7 offset:11264\n"
    "v_mov_b64 v[112:113], 0\n"
    "v_mov_b64 v[114:115], 0\n"
    "v_mov_b64 v[116:117], 0\n"
    "v_mov_b64 v[118:119], 0\n"
    "v_mfma_f32_32x32x8_bf16 v[48:63], v[112:113], v[112:113], 0\n"
    "v_mov_b32 v1, 0x4444\n"
    "s_mov_b32 vcc_lo, 0xaaaaaaaa\n"
    "s_mov_b32 vcc_hi, 0xaaaaaaaa\n"
    "s_mov_b64 exec, vcc\n"
    "v_mov_b32 v1, 0xeeee\n"
    "s_mov_b64 exec, -1\n"
    "v_lshrrev_b32 v46, 3, v0\n"
    "v_and_b32 v46, 0x58, v46\n"
    "v_bfe_u32 v47, v0, 8, 1\n"
    "v_mul_u32_u24 v47, 1032, v47\n"
    "v_add_u32 v46, v46, v47\n"
    "v_add_u32 v46, 161536, v46\n"
    "v_add_u32 v47, 162560, v0\n"
    "s_lshr_b32 s40, s39, 4\n"
    "s_lshl_b32 s41, s40, 12\n"
    "s_and_b32 s42, s39, 15\n"
    "s_lshl_b32 s42, s42, 4\n"
    "s_add_u32 s41, s41, s42\n"
    "s_add_u32 s41, s41, s11\n"
    "s_add_u32 s73, s41, 122880\n"
    "s_lshl_b32 s40, s40, 3\n"
    "s_and_b32 s41, s39, 3\n"
    "s_lshl_b32 s41, s41, 1\n"
    "s_add_u32 s40, s40, s41\n"
    "s_bfe_u32 s41, s39, 0x20002\n"
    "s_lshl_b32 s41, s41, 8\n"
    "s_add_u32 s40, s40, s41\n"
    "s_add_u32 s40, s40, s62\n"
    "s_add_u32 s66, s40, 131072\n"
    "s_mov_b32 s37, 0x80000\n"
    "s_waitcnt vmcnt(8)\n"
    "LXW%=:\n"
    "v_max3_u32 v36, v16, v17, v18\n"
    "v_max_u32 v36, v36, v19\n"
    "v_cmp_eq_u32 vcc, -1, v36\n"
    "s_cbranch_vccz LXK%=\n"
    "s_sleep 2\n"
    "buffer_load_dwordx4 v[16:19], v0, s[16:19], s70 offen sc1\n"
    "s_waitcnt vmcnt(0)\n"
    "s_sub_u32 s37, s37, 1\n"
    "s_cbranch_scc0 LXW%=\n"
    "s_branch LFAIL%=\n"
    "LXK%=:\n"
    "v_lshlrev_b32 v20, 16, v16\n"
    "v_and_b32 v21, 0xffff0000, v16\n"
    "v_readfirstlane_b32 s40, v20\n"
    "v_lshlrev_b32 v22, 16, v17\n"
    "v_and_b32 v23, 0xffff0000, v17\n"
    "v_lshlrev_b32 v24, 16, v18\n"
    "v_and_b32 v25, 0xffff0000, v18\n"
    "v_lshlrev_b32 v26, 16, v19\n"
    "v_and_b32 v27, 0xffff0000, v19\n"
    "v_pk_add_f32 v[28:29], v[20:21], s[40:41] op_sel_hi:[1,0] neg_lo:[0,1] neg_hi:[0,1]\n"
    "v_pk_add_f32 v[30:31], v[22:23], s[40:41] op_sel_hi:[1,0] neg_lo:[0,1] neg_hi:[0,1]\n"
    "v_pk_add_f32 v[32:33], v[24:25], s[40:41] op_sel_hi:[1,0] neg_lo:[0,1] neg_hi:[0,1]\n"
    "v_pk_add_f32 v[34:35], v[26:27], s[40:41] op_sel_hi:[1,0] neg_lo:[0,1] neg_hi:[0,1]\n"
    "v_pk_add_f32 v[36:37], v[28:29], v[30:31]\n"
    "v_pk_mul_f32 v[38:39], v[28:29], v[28:29]\n"
    "v_pk_add_f32 v[40:41], v[32:33], v[34:35]\n"
    "v_pk_fma_f32 v[38:39], v[30:31], v[30:31], v[38:39]\n"
    "v_pk_add_f32 v[36:37], v[36:37], v[40:41]\n"
    "v_pk_fma_f32 v[38:39], v[32:33], v[32:33], v[38:39]\n"
    "v_add_f32 v36, v36, v37\n"
    "v_pk_fma_f32 v[38:39], v[34:35], v[34:35], v[38:39]\n"
    "v_mov_b32 v41, 0x3b000000\n"
    "v_add_f32 v37, v38, v39\n"
    "v_add_f32_dpp v36, v36, v36 row_ror:8 row_mask:0xf bank_mask:0xf\n"
    "s_nop 0\n"
    "v_add_f32_dpp v37, v37, v37 row_ror:8 row_mask:0xf bank_mask:0xf\n"
    "v_add_f32_dpp v36, v36, v36 row_ror:4 row_mask:0xf bank_mask:0xf\n"
    "s_nop 0\n"
    "v_add_f32_dpp v37, v37, v37 row_ror:4 row_mask:0xf bank_mask:0xf\n"
    "v_add_f32_dpp v36, v36, v36 row_ror:2 row_mask:0xf bank_mask:0xf\n"
    "s_nop 0\n"
    "v_add_f32_dpp v37, v37, v37 row_ror:2 row_mask:0xf bank_mask:0xf\n"
    "v_add_f32_dpp v36, v36, v36 row_ror:1 row_mask:0xf bank_mask:0xf\n"
    "s_nop 0\n"
    "v_add_f32_dpp v37, v37, v37 row_ror:1 row_mask:0xf bank_mask:0xf\n"
    "s_nop 1\n"
    "v_permlane16_swap_b32 v36, v37\n"
    "v_add_f32 v36, v36, v37\n"
    "v_mov_b32 v37, v36\n"
    "s_nop 1\n"
    "v_permlane32_swap_b32 v36, v37\n"
    "v_add_f32 v36, v36, v37\n"
    "s_nop 0\n"
    "v_readlane_b32 s42, v36, 0\n"
    "v_readlane_b32 s43, v36, 16\n"
    "s_nop 0\n"
    "v_mul_f32 v36, s42, v41\n"
    "v_mul_f32 v37, s43, v41\n"
    "v_fma_f32 v37, -v36, v36, v37\n"
    "v_add_f32 v40, s40, v36\n"
    "v_max_f32 v37, 0, v37\n"
    "v_cvt_pk_bf16_f32 v36, v40, v37\n"
    "v_and_b32 v37, 0xffff0000, v36\n"
    "v_add_f32 v37, 0x3727c5ac, v37\n"
    "v_cvt_pk_bf16_f32 v37, v37, v37\n"
    "v_lshlrev_b32 v37, 16, v37\n"
    "v_sqrt_f32 v38, v37\n"
    "v_lshlrev_b32 v36, 16, v36\n"
    "v_cvt_pk_bf16_f32 v38, v38, v38\n"
    "v_lshlrev_b32 v38, 16, v38\n"
    "v_rcp_f32 v38, v38\n"
    "v_pk_add_f32 v[28:29], v[20:21], v[36:37] op_sel_hi:[1,0] neg_lo:[0,1] neg_hi:[0,1]\n"
    "v_pk_add_f32 v[30:31], v[22:23], v[36:37] op_sel_hi:[1,0] neg_lo:[0,1] neg_hi:[0,1]\n"
    "v_pk_add_f32 v[32:33], v[24:25], v[36:37] op_sel_hi:[1,0] neg_lo:[0,1] neg_hi:[0,1]\n"
    "v_pk_add_f32 v[34:35], v[26:27], v[36:37] op_sel_hi:[1,0] neg_lo:[0,1] neg_hi:[0,1]\n"
    "v_pk_mul_f32 v[28:29], v[28:29], v[38:39] op_sel_hi:[1,0]\n"
    "v_pk_mul_f32 v[30:31], v[30:31], v[38:39] op_sel_hi:[1,0]\n"
    "v_pk_mul_f32 v[32:33], v[32:33], v[38:39] op_sel_hi:[1,0]\n"
    "v_pk_mul_f32 v[34:35], v[34:35], v[38:39] op_sel_hi:[1,0]\n"
    "v_cvt_pk_bf16_f32 v40, v28, v29\n"
    "v_cvt_pk_bf16_f32 v41, v30, v31\n"
    "v_cvt_pk_bf16_f32 v42, v32, v33\n"
    "v_cvt_pk_bf16_f32 v43, v34, v35\n"
    "v_subrev_u32 v36, 1024, v47\n"
    "ds_write_b128 v36, v[40:43]\n"
    "ds_write2_b32 v47, v40, v41 offset0:1 offset1:2\n"
    "ds_write2_b32 v47, v42, v43 offset0:3 offset1:4\n"
    "s_waitcnt lgkmcnt(11)\n"
    "ds_read_b64_tr_b16 v[16:17], v46 offset:0\n"
    "ds_read_b64_tr_b16 v[18:19], v46 offset:32\n"
    "ds_read_b64_tr_b16 v[20:21], v46 offset:128\n"
    "ds_read_b64_tr_b16 v[22:23], v46 offset:160\n"
    "s_waitcnt lgkmcnt(11)\n"
    "ds_read_b64_tr_b16 v[24:25], v46 offset:256\n"
    "ds_read_b64_tr_b16 v[26:27], v46 offset:288\n"
    "ds_read_b64_tr_b16 v[28:29], v46 offset:384\n"
    "ds_read_b64_tr_b16 v[30:31], v46 offset:416\n"
    "s_waitcnt lgkmcnt(6)\n"
    "v_smfmac_f32_16x16x64_bf16 v[48:51], v[16:19], v[64:71], v1\n"
    "ds_read_b64_tr_b16 v[32:33], v46 offset:512\n"
    "v_smfmac_f32_16x16x64_bf16 v[52:55], v[16:19], v[72:79], v1\n"
    "ds_read_b64_tr_b16 v[34:35], v46 offset:544\n"
    "s_waitcnt lgkmcnt(6)\n"
    "v_smfmac_f32_16x16x64_bf16 v[48:51], v[20:23], v[80:87], v1\n"
    "ds_read_b64_tr_b16 v[36:37], v46 offset:640\n"
    "v_smfmac_f32_16x16x64_bf16 v[52:55], v[20:23], v[88:95], v1\n"
    "ds_read_b64_tr_b16 v[38:39], v46 offset:672\n"
    "s_waitcnt lgkmcnt(6)\n"
    "v_smfmac_f32_16x16x64_bf16 v[48:51], v[24:27], v[96:103], v1\n"
    "ds_read_b64_tr_b16 v[40:41], v46 offset:768\n"
    "v_smfmac_f32_16x16x64_bf16 v[52:55], v[24:27], v[104:111], v1\n"
    "ds_read_b64_tr_b16 v[42:43], v46 offset:800\n"
    "s_waitcnt lgkmcnt(6)\n"
    "v_smfmac_f32_16x16x64_bf16 v[48:51], v[28:31], a[0:7], v1\n"
    "ds_read_b64_tr_b16 v[44:45], v46 offset:896\n"
    "v_smfmac_f32_16x16x64_bf16 v[52:55], v[28:31], a[8:15], v1\n"
    "ds_read_b64_tr_b16 v[46:47], v46 offset:928\n"
    "s_waitcnt lgkmcnt(6)\n"
    "v_smfmac_f32_16x16x64_bf16 v[48:51], v[32:35], a[16:23], v1\n"
    "ds_read_b128 v[64:67], v7 offset:12288\n"
    "v_smfmac_f32_16x16x64_bf16 v[52:55], v[32:35], a[24:31], v1\n"
    "ds_read_b128 v[68:71], v7 offset:13312\n"
    "s_waitcnt lgkmcnt(6)\n"
    "v_lshrrev_b32 v104, 2, v0\n"
    "v_smfmac_f32_16x16x64_bf16 v[48:51], v[36:39], a[32:39], v1\n"
    "ds_read_b128 v[72:75], v7 offset:14336\n"
    "v_and_b32 v104, 12, v104\n"
    "v_smfmac_f32_16x16x64_bf16 v[52:55], v[36:39], a[40:47], v1\n"
    "ds_read_b128 v[76:79], v7 offset:15360\n"
    "s_waitcnt lgkmcnt(6)\n"
    "v_lshlrev_b32 v105, 2, v0\n"
    "v_smfmac_f32_16x16x64_bf16 v[48:51], v[40:43], a[48:55], v1\n"
    "ds_read_b128 v[80:83], v7 offset:16384\n"
    "v_and_b32 v105, 0x300, v105\n"
    "v_smfmac_f32_16x16x64_bf16 v[52:55], v[40:43], a[56:63], v1\n"
    "ds_read_b128 v[84:87], v7 offset:17408\n"
    "s_waitcnt lgkmcnt(6)\n"
    "v_add3_u32 v104, v104, v105, s73\n"
    "v_smfmac_f32_16x16x64_bf16 v[48:51], v[44:47], a[64:71], v1\n"
    "ds_read_b128 v[88:91], v7 offset:18432\n"
    "v_and_b32 v105, 0xf0, v0\n"
    "v_smfmac_f32_16x16x64_bf16 v[52:55], v[44:47], a[72:79], v1\n"
    "ds_read_b128 v[92:95], v7 offset:19456\n"
    "v_add_u32 v105, s66, v105\n"
    "v_smfmac_f32_16x16x64_bf16 v[56:59], v[24:27], a[80:87], v1\n"
    "v_smfmac_f32_16x16x64_bf16 v[60:63], v[24:27], a[88:95], v1\n"
    "v_smfmac_f32_16x16x64_bf16 v[56:59], v[28:31], a[96:103], v1\n"
    "v_add_f32 v48, v48, v49\n"
    "v_smfmac_f32_16x16x64_bf16 v[60:63], v[28:31], a[104:111], v1\n"
    "v_add_f32 v52, v52, v53\n"
    "v_smfmac_f32_16x16x64_bf16 v[56:59], v[32:35], a[112:119], v1\n"
    "v_cvt_pk_bf16_f32 v48, v48, v52\n"
    "v_smfmac_f32_16x16x64_bf16 v[60:63], v[32:35], a[120:127], v1\n"
    "v_smfmac_f32_16x16x64_bf16 v[56:59], v[36:39], a[128:135], v1\n"
    "v_smfmac_f32_16x16x64_bf16 v[60:63], v[36:39], a[136:143], v1\n"
    "v_smfmac_f32_16x16x64_bf16 v[56:59], v[40:43], a[144:151], v1\n"
    "v_smfmac_f32_16x16x64_bf16 v[60:63], v[40:43], a[152:159], v1\n"
    "v_smfmac_f32_16x16x64_bf16 v[56:59], v[44:47], a[160:167], v1\n"
    "v_smfmac_f32_16x16x64_bf16 v[60:63], v[44:47], a[168:175], v1\n"
    "s_waitcnt lgkmcnt(6)\n"
    "v_smfmac_f32_16x16x64_bf16 v[56:59], v[16:19], v[64:71], v1\n"
    "s_waitcnt lgkmcnt(4)\n"
    "v_smfmac_f32_16x16x64_bf16 v[60:63], v[16:19], v[72:79], v1\n"
    "s_waitcnt lgkmcnt(2)\n"
    "v_smfmac_f32_16x16x64_bf16 v[56:59], v[20:23], v[80:87], v1\n"
    "s_waitcnt lgkmcnt(0)\n"
    "v_smfmac_f32_16x16x64_bf16 v[60:63], v[20:23], v[88:95], v1\n"
    "v_smfmac_f32_16x16x64_bf16 v[112:115], v[16:19], a[176:183], v1\n"
    "v_smfmac_f32_16x16x64_bf16 v[116:119], v[16:19], a[184:191], v1\n"
    "ds_read_b128 v[64:67], v7 offset:0\n"
    "v_smfmac_f32_16x16x64_bf16 v[112:115], v[20:23], a[192:199], v1\n"
    "ds_read_b128 v[68:71], v7 offset:1024\n"
    "v_add_f32 v56, v56, v57\n"
    "v_smfmac_f32_16x16x64_bf16 v[116:119], v[20:23], a[200:207], v1\n"
    "ds_read_b128 v[72:75], v7 offset:2048\n"
    "v_add_f32 v60, v60, v61\n"
    "v_smfmac_f32_16x16x64_bf16 v[112:115], v[24:27], a[208:215], v1\n"
    "ds_read_b128 v[76:79], v7 offset:3072\n"
    "v_cvt_pk_bf16_f32 v56, v56, v60\n"
    "v_smfmac_f32_16x16x64_bf16 v[116:119], v[24:27], a[216:223], v1\n"
    "ds_read_b128 v[80:83], v7 offset:4096\n"
    "v_smfmac_f32_16x16x64_bf16 v[112:115], v[28:31], a[224:231], v1\n"
    "ds_read_b128 v[84:87], v7 offset:5120\n"
    "v_smfmac_f32_16x16x64_bf16 v[116:119], v[28:31], a[232:239], v1\n"
    "ds_read_b128 v[88:91], v7 offset:6144\n"
    "v_smfmac_f32_16x16x64_bf16 v[112:115], v[32:35], a[240:247], v1\n"
    "ds_read_b128 v[92:95], v7 offset:7168\n"
    "v_smfmac_f32_16x16x64_bf16 v[116:119], v[32:35], a[248:255], v1\n"
    "ds_read_b128 v[96:99], v7 offset:8192\n"
    "v_smfmac_f32_16x16x64_bf16 v[112:115], v[36:39], v[120:127], v1\n"
    "ds_read_b128 v[100:103], v7 offset:9216\n"
    "v_smfmac_f32_16x16x64_bf16 v[116:119], v[36:39], v[128:135], v1\n"
    "v_smfmac_f32_16x16x64_bf16 v[112:115], v[40:43], v[136:143], v1\n"
    "v_smfmac_f32_16x16x64_bf16 v[116:119], v[40:43], v[144:151], v1\n"
    "v_smfmac_f32_16x16x64_bf16 v[112:115], v[44:47], v[152:159], v1\n"
    "v_smfmac_f32_16x16x64_bf16 v[116:119], v[44:47], v[160:167], v1\n"
    "s_cmp_lg_u32 s61, 1\n"
    "s_cbranch_scc1 LRN%=\n"
    "LRM%=:\n"
    "s_memtime s[40:41]\n"
    "s_add_u32 s42, s21, 0\n"
    "s_waitcnt lgkmcnt(0)\n"
    "s_sub_u32 s40, s40, s42\n"
    "s_sub_u32 s41, s44, s40\n"
    "s_cmp_ge_i32 s41, 0\n"
    "s_cbranch_scc1 LRMD%=\n"
    "LRMI%=:\n"
    "s_add_u32 s44, s44, s13\n"
    "s_add_u32 s41, s41, s13\n"
    "s_cbranch_scc0 LRMI%=\n"
    "s_branch LRM%=\n"
    "LRMD%=:\n"
    "v_mov_b32 v106, 158220\n"
    "v_mov_b32 v108, s36\n"
    "v_mov_b32 v109, s44\n"
    "v_mov_b32 v110, s44\n"
    "ds_write2_b32 v106, v108, v109 offset0:0 offset1:1\n"
    "ds_write_b32 v106, v110 offset:8\n"
    "LRN%=:\n"
    "s_nop 7\n"
    "v_add_f32 v112, v112, v113\n"
    "v_add_f32 v116, v116, v117\n"
    "v_cvt_pk_bf16_f32 v112, v112, v116\n"
    "v_mov_b32 v1, 0x4444\n"
    "s_mov_b32 vcc_lo, 0xcccccccc\n"
    "s_mov_b32 vcc_hi, 0xcccccccc\n"
    "s_mov_b64 exec, vcc\n"
    "v_mov_b32 v1, 0xeeee\n"
    "s_mov_b64 exec, -1\n"
    "s_waitcnt vmcnt(0) lgkmcnt(0)\n"
    "s_barrier\n"
    "ds_write_b32 v104, v56\n"
    "ds_write_b32 v104, v48 offset:16384\n"
    "ds_write_b16 v105, v112\n"
    "ds_write_b16_d16_hi v105, v112 offset:1024\n"
    "s_waitcnt lgkmcnt(0)\n"
    "ds_read_b128 v[104:107], v7 offset:10240\n"
    "ds_read_b128 v[108:111], v7 offset:11264\n"
    "v_mov_b64 v[112:113], 0\n"
    "v_mov_b64 v[114:115], 0\n"
    "v_mov_b64 v[116:117], 0\n"
    "v_mov_b64 v[118:119], 0\n"
    "v_mfma_f32_32x32x8_bf16 v[48:63], v[112:113], v[112:113], 0\n"
    "v_lshrrev_b32 v40, 4, v0\n"
    "v_and_b32 v40, 0x30, v40\n"
    "v_lshl_add_u32 v40, v40, 4, v0\n"
    "v_mov_b32 v41, 158224\n"
    "s_add_u32 s73, s49, s11\n"
    "s_add_u32 s73, s73, 4096\n"
    "s_add_u32 m0, s11, 151552\n"
    "ds_read_b64 v[42:43], v41 offset:0\n"
    "s_waitcnt lgkmcnt(0)\n"
    "v_readfirstlane_b32 s44, v43\n"
    "s_branch LC1%=\n"
    "LEND%=:\n"
    "s_waitcnt vmcnt(1) lgkmcnt(0)\n"
    "s_barrier\n"
    "s_cmp_lg_u32 s61, 1\n"
    "s_cbranch_scc1 LE0%=\n"
    "buffer_store_dword v1, off, s[16:19], s58 sc1\n"
    "s_add_u32 s58, s58, 4\n"
    "s_bitset0_b32 s58, 31\n"
    "LE0%=:\n"
    "s_add_u32 s54, s50, s57\n"
    "s_lshr_b32 s38, s54, 8\n"
    "s_xor_b32 s38, s38, s74\n"
    "s_and_b32 s38, s38, 0x2000\n"
    "s_or_b32 s54, s54, s38\n"
    "s_cmp_lt_u32 s61, 2\n"
    "s_cbranch_scc1 LE1%=\n"
    "ds_read_b128 v[16:19], v14 offset:0\n"
    "v_add_u32 v118, 122880, v0\n"
    "ds_read_b128 v[32:35], v118 offset:0\n"
    "ds_read_b128 v[48:51], v118 offset:4096\n"
    "ds_read_b128 v[20:23], v14 offset:1024\n"
    "ds_read_b128 v[36:39], v118 offset:1024\n"
    "ds_read_b128 v[52:55], v118 offset:5120\n"
    "v_mov_b32 v111, 0xd0289000\n"
    "v_cndmask_b32 v80, v111, 0, s[80:81]\n"
    "v_cndmask_b32 v81, v111, 0, s[82:83]\n"
    "v_cndmask_b32 v82, v111, 0, s[84:85]\n"
    "v_cndmask_b32 v83, v111, 0, s[86:87]\n"
    "v_cndmask_b32 v84, v111, 0, s[88:89]\n"
    "v_cndmask_b32 v85, v111, 0, s[90:91]\n"
    "v_cndmask_b32 v86, v111, 0, s[92:93]\n"
    "v_cndmask_b32 v87, v111, 0, s[94:95]\n"
    "v_lshrrev_b32 v112, 1, v0\n"
    "v_add_u32 v112, 149760, v112\n"
    "ds_read_b128 v[24:27], v14 offset:2048\n"
    "ds_read_b128 v[40:43], v118 offset:2048\n"
    "ds_read_b128 v[56:59], v118 offset:6144\n"
    "s_waitcnt lgkmcnt(7)\n"
    "v_mfma_f32_16x16x32_bf16 v[80:83], v[32:35], v[16:19], v[80:83]\n"
    "ds_read_b128 v[28:31], v14 offset:3072\n"
    "ds_read_b128 v[44:47], v118 offset:3072\n"
    "s_waitcnt lgkmcnt(8)\n"
    "v_mfma_f32_16x16x32_bf16 v[84:87], v[48:51], v[16:19], v[84:87]\n"
    "ds_read_b128 v[60:63], v118 offset:7168\n"
    "s_waitcnt lgkmcnt(7)\n"
    "v_mov_b32 v114, s72\n"
    "v_mfma_f32_16x16x32_bf16 v[80:83], v[36:39], v[20:23], v[80:83]\n"
    "ds_read_b128 v[64:67], v6 offset:0\n"
    "ds_read_b128 v[68:71], v6 offset:64\n"
    "s_waitcnt lgkmcnt(8)\n"
    "v_mfma_f32_16x16x32_bf16 v[84:87], v[52:55], v[20:23], v[84:87]\n"
    "ds_read_b128 v[72:75], v6 offset:128\n"
    "s_waitcnt lgkmcnt(7)\n"
    "v_mov_b32 v105, 0\n"
    "v_mfma_f32_16x16x32_bf16 v[80:83], v[40:43], v[24:27], v[80:83]\n"
    "ds_read_b128 v[76:79], v6 offset:192\n"
    "ds_read_b64 v[92:93], v112 offset:0\n"
    "s_waitcnt lgkmcnt(8)\n"
    "v_mfma_f32_16x16x32_bf16 v[84:87], v[56:59], v[24:27], v[84:87]\n"
    "ds_read_b64 v[94:95], v112 offset:512\n"
    "s_waitcnt lgkmcnt(7)\n"
    "v_mov_b32 v106, 0\n"
    "v_mfma_f32_16x16x32_bf16 v[80:83], v[44:47], v[28:31], v[80:83]\n"
    "ds_read_b64 v[96:97], v112 offset:1024\n"
    "ds_read_b64 v[98:99], v112 offset:1536\n"
    "s_waitcnt lgkmcnt(8)\n"
    "v_mfma_f32_16x16x32_bf16 v[84:87], v[60:63], v[28:31], v[84:87]\n"
    "ds_write_b32 v114, v119 offset:24\n"
    "s_waitcnt lgkmcnt(8)\n"
    "v_mfma_f32_16x16x32_bf16 v[88:91], v[64:67], v[16:19], 0\n"
    "ds_read_b128 v[32:35], v118 offset:8192\n"
    "ds_read_b128 v[36:39], v118 offset:9216\n"
    "s_waitcnt lgkmcnt(9)\n"
    "v_mfma_f32_16x16x32_bf16 v[88:91], v[68:71], v[20:23], v[88:91]\n"
    "ds_read_b128 v[40:43], v118 offset:10240\n"
    "ds_read_b128 v[44:47], v118 offset:11264\n"
    "s_waitcnt lgkmcnt(10)\n"
    "v_mfma_f32_16x16x32_bf16 v[88:91], v[72:75], v[24:27], v[88:91]\n"
    "ds_read_b128 v[48:51], v118 offset:12288\n"
    "ds_read_b128 v[52:55], v118 offset:13312\n"
    "s_waitcnt lgkmcnt(11)\n"
    "v_max3_f32 v108, v80, v81, v82\n"
    "v_max3_f32 v109, v83, v84, v85\n"
    "v_mfma_f32_16x16x32_bf16 v[88:91], v[76:79], v[28:31], v[88:91]\n"
    "ds_read_b128 v[56:59], v118 offset:14336\n"
    "ds_read_b128 v[60:63], v118 offset:15360\n"
    "v_max3_f32 v108, v108, v86, v87\n"
    "v_max_f32 v108, v108, v109\n"
    "v_mov_b32 v109, v108\n"
    "s_nop 1\n"
    "v_permlane32_swap_b32 v108, v109\n"
    "v_max_f32 v108, v108, v109\n"
    "v_max3_f32 v108, v108, v88, v89\n"
    "v_max3_f32 v108, v108, v90, v91\n"
    "v_mov_b32 v109, v108\n"
    "s_nop 1\n"
    "v_permlane16_swap_b32 v108, v109\n"
    "v_max_f32 v108, v108, v109\n"
    "v_mul_f32 v108, 0xbe0293ee, v108\n"
    "v_pk_fma_f32 v[80:81], v[80:81], s[64:65], v[108:109] op_sel:[0,1,0] op_sel_hi:[1,1,0]\n"
    "v_pk_fma_f32 v[82:83], v[82:83], s[64:65], v[108:109] op_sel:[0,1,0] op_sel_hi:[1,1,0]\n"
    "v_pk_fma_f32 v[84:85], v[84:85], s[64:65], v[108:109] op_sel:[0,1,0] op_sel_hi:[1,1,0]\n"
    "v_pk_fma_f32 v[86:87], v[86:87], s[64:65], v[108:109] op_sel:[0,1,0] op_sel_hi:[1,1,0]\n"
    "v_pk_fma_f32 v[88:89], v[88:89], s[64:65], v[108:109] op_sel:[0,1,0] op_sel_hi:[1,1,0]\n"
    "v_exp_f32 v80, v80\n"
    "v_exp_f32 v81, v81\n"
    "v_exp_f32 v82, v82\n"
    "v_exp_f32 v83, v83\n"
    "v_exp_f32 v84, v84\n"
    "v_exp_f32 v85, v85\n"
    "v_exp_f32 v86, v86\n"
    "v_exp_f32 v87, v87\n"
    "v_exp_f32 v88, v88\n"
    "v_exp_f32 v89, v89\n"
    "v_pk_add_f32 v[108:109], v[80:81], v[82:83]\n"
    "v_pk_add_f32 v[110:111], v[84:85], v[86:87]\n"
    "v_pk_add_f32 v[108:109], v[108:109], v[88:89]\n"
    "v_pk_add_f32 v[108:109], v[108:109], v[110:111]\n"
    "v_add_f32 v108, v108, v109\n"
    "v_mov_b32 v109, v108\n"
    "s_nop 1\n"
    "v_permlane16_swap_b32 v108, v109\n"
    "v_add_f32 v108, v108, v109\n"
    "v_mov_b32 v109, v108\n"
    "s_nop 1\n"
    "v_permlane32_swap_b32 v108, v109\n"
    "v_add_f32 v108, v108, v109\n"
    "v_rcp_f32 v108, v108\n"
    "s_nop 0\n"
    "v_pk_mul_f32 v[80:81], v[80:81], v[108:109] op_sel_hi:[1,0]\n"
    "v_pk_mul_f32 v[82:83], v[82:83], v[108:109] op_sel_hi:[1,0]\n"
    "v_pk_mul_f32 v[84:85], v[84:85], v[108:109] op_sel_hi:[1,0]\n"
    "v_pk_mul_f32 v[86:87], v[86:87], v[108:109] op_sel_hi:[1,0]\n"
    "v_pk_mul_f32 v[88:89], v[88:89], v[108:109] op_sel_hi:[1,0]\n"
    "v_cvt_pk_bf16_f32 v100, v80, v81\n"
    "v_cvt_pk_bf16_f32 v101, v82, v83\n"
    "v_cvt_pk_bf16_f32 v102, v84, v85\n"
    "v_cvt_pk_bf16_f32 v103, v86, v87\n"
    "v_cvt_pk_bf16_f32 v104, v88, v89\n"
    "v_cvt_pk_bf16_f32 v107, v88, v89\n"
    "s_waitcnt lgkmcnt(7)\n"
    "v_mfma_f32_16x16x32_bf16 v[16:19], v[32:35], v[100:103], 0\n"
    "s_waitcnt lgkmcnt(6)\n"
    "v_mfma_f32_16x16x32_bf16 v[20:23], v[36:39], v[100:103], 0\n"
    "v_mfma_f32_16x16x16_bf16 v[16:19], v[92:93], v[104:105], v[16:19]\n"
    "v_mfma_f32_16x16x16_bf16 v[20:23], v[92:93], v[106:107], v[20:23]\n"
    "s_waitcnt lgkmcnt(5)\n"
    "v_mfma_f32_16x16x32_bf16 v[24:27], v[40:43], v[100:103], 0\n"
    "s_waitcnt lgkmcnt(4)\n"
    "v_mfma_f32_16x16x32_bf16 v[28:31], v[44:47], v[100:103], 0\n"
    "v_cvt_pk_bf16_f32 v112, v16, v17\n"
    "v_cvt_pk_bf16_f32 v113, v18, v19\n"
    "v_mfma_f32_16x16x16_bf16 v[24:27], v[94:95], v[104:105], v[24:27]\n"
    "v_cvt_pk_bf16_f32 v114, v20, v21\n"
    "v_cvt_pk_bf16_f32 v115, v22, v23\n"
    "v_mfma_f32_16x16x16_bf16 v[28:31], v[94:95], v[106:107], v[28:31]\n"
    "ds_write_b128 v14, v[112:115] offset:0\n"
    "s_waitcnt lgkmcnt(4)\n"
    "v_mfma_f32_16x16x32_bf16 v[16:19], v[48:51], v[100:103], 0\n"
    "s_waitcnt lgkmcnt(3)\n"
    "v_mfma_f32_16x16x32_bf16 v[20:23], v[52:55], v[100:103], 0\n"
    "v_cvt_pk_bf16_f32 v108, v24, v25\n"
    "v_cvt_pk_bf16_f32 v109, v26, v27\n"
    "v_mfma_f32_16x16x16_bf16 v[16:19], v[96:97], v[104:105], v[16:19]\n"
    "v_cvt_pk_bf16_f32 v110, v28, v29\n"
    "v_cvt_pk_bf16_f32 v111, v30, v31\n"
    "v_mfma_f32_16x16x16_bf16 v[20:23], v[96:97], v[106:107], v[20:23]\n"
    "ds_write_b128 v14, v[108:111] offset:1024\n"
    "s_waitcnt lgkmcnt(3)\n"
    "v_mfma_f32_16x16x32_bf16 v[24:27], v[56:59], v[100:103], 0\n"
    "s_waitcnt lgkmcnt(2)\n"
    "v_mfma_f32_16x16x32_bf16 v[28:31], v[60:63], v[100:103], 0\n"
    "v_cvt_pk_bf16_f32 v112, v16, v17\n"
    "v_cvt_pk_bf16_f32 v113, v18, v19\n"
    "v_mfma_f32_16x16x16_bf16 v[24:27], v[98:99], v[104:105], v[24:27]\n"
    "v_cvt_pk_bf16_f32 v114, v20, v21\n"
    "v_cvt_pk_bf16_f32 v115, v22, v23\n"
    "v_mfma_f32_16x16x16_bf16 v[28:31], v[98:99], v[106:107], v[28:31]\n"
    "ds_write_b128 v14, v[112:115] offset:2048\n"
    "s_nop 2\n"
    "v_cvt_pk_bf16_f32 v108, v24, v25\n"
    "v_cvt_pk_bf16_f32 v109, v26, v27\n"
    "s_nop 1\n"
    "v_cvt_pk_bf16_f32 v110, v28, v29\n"
    "v_cvt_pk_bf16_f32 v111, v30, v31\n"
    "ds_write_b128 v14, v[108:111] offset:3072\n"
    "s_waitcnt lgkmcnt(0)\n"
    "LE1%=:\n"
    "s_barrier\n"
    "ds_read_b128 v[32:35], v9 offset:16384\n"
    "ds_read_b128 v[36:39], v9 offset:17408\n"
    "v_lshrrev_b32 v40, 4, v0\n"
    "v_and_b32 v40, 0x30, v40\n"
    "v_lshl_add_u32 v40, v40, 4, v0\n"
    "s_waitcnt lgkmcnt(1)\n"
    "buffer_store_dwordx4 v[32:35], v40, s[16:19], s54 offen offset:0 sc1\n"
    "s_waitcnt lgkmcnt(0)\n"
    "buffer_store_dwordx4 v[36:39], v40, s[16:19], s54 offen offset:2048 sc1\n"
    "s_waitcnt vmcnt(0)\n"
    "s_barrier\n"
    "buffer_store_dword v1, off, s[16:19], s58 sc1\n"
    "s_endpgm\n"
    "LFAIL%=:\n"
    "buffer_store_dword v1, off, s[16:19], s35 sc1\n"
    "s_endpgm\n"
    :
    : "{v0}"(hv0), "{v1}"(hv1), "{v2}"(hv2), "{v3}"(hv3), "{v4}"(hv4), "{v5}"(hv5), "{v6}"(hv6), "{v7}"(hv7), "{v8}"(hv8), "{v9}"(hv9), "{v10}"(hv10), "{v11}"(hv11), "{v12}"(hv12), "{v13}"(hv13), "{v14}"(hv14), "{v15}"(hv15), "{v16}"(hv16), "{v17}"(hv17), "{v18}"(hv18), "{v19}"(hv19), "{v20}"(hv20)
    : "v255", "a255", "s101", "vcc", "scc", "memory");
  } else {
  asm volatile(
    "s_mov_b32 s33, 0\n"
    "s_nop 1\n"
    "v_mov_b32 v119, v20\n"
    "s_nop 0\n"
    "v_readlane_b32 s61, v119, 0\n"
    "v_readlane_b32 s46, v119, 1\n"
    "v_readlane_b32 s63, v119, 2\n"
    "v_readlane_b32 s48, v119, 3\n"
    "v_readlane_b32 s55, v119, 4\n"
    "v_readlane_b32 s56, v119, 5\n"
    "v_readlane_b32 s57, v119, 6\n"
    "v_readlane_b32 s74, v119, 7\n"
    "v_readlane_b32 s58, v119, 8\n"
    "v_readlane_b32 s59, v119, 9\n"
    "v_readlane_b32 s60, v119, 10\n"
    "v_readlane_b32 s67, v119, 11\n"
    "v_readlane_b32 s1, v119, 12\n"
    "v_readlane_b32 s69, v119, 13\n"
    "v_readlane_b32 s70, v119, 14\n"
    "v_readlane_b32 s71, v119, 15\n"
    "v_readlane_b32 s73, v119, 16\n"
    "v_readlane_b32 s45, v119, 17\n"
    "v_readlane_b32 s92, v119, 36\n"
    "v_readlane_b32 s93, v119, 37\n"
    "v_readlane_b32 s35, v119, 18\n"
    "v_readlane_b32 s75, v119, 19\n"
    "v_readlane_b32 s16, v119, 28\n"
    "v_readlane_b32 s17, v119, 29\n"
    "v_readlane_b32 s18, v119, 30\n"
    "v_readlane_b32 s8, v119, 31\n"
    "v_readlane_b32 s9, v119, 32\n"
    "v_readlane_b32 s10, v119, 33\n"
    "v_readlane_b32 s0, v119, 34\n"
    "v_readlane_b32 s12, v119, 35\n"
    "v_readlane_b32 s13, v119, 38\n"
    "v_readlane_b32 s21, v119, 39\n"
    "v_readlane_b32 s94, v119, 40\n"
    "v_readlane_b32 s95, v119, 41\n"
    "v_readlane_b32 s52, v119, 42\n"
    "s_mov_b32 s19, 0x20000\n"
    "v_readlane_b32 s84, v119, 20\n"
    "v_readlane_b32 s85, v119, 21\n"
    "v_readlane_b32 s86, v119, 22\n"
    "v_readlane_b32 s87, v119, 23\n"
    "v_readlane_b32 s88, v119, 24\n"
    "v_readlane_b32 s89, v119, 25\n"
    "v_readlane_b32 s90, v119, 26\n"
    "v_readlane_b32 s91, v119, 27\n"
    "s_mov_b32 s76, 0xff00ff00\n"
    "s_mov_b32 s77, 0xff00ff00\n"
    "s_mov_b32 s78, 0x00ff00ff\n"
    "s_mov_b32 s79, 0x00ff00ff\n"
    "s_mov_b32 s65, 0x3e0293ee\n"
    "s_mov_b32 s66, 0xfffefffe\n"
    "s_lshl_b32 s62, s61, 11\n"
    "s_lshl_b32 s11, s61, 10\n"
    "s_mov_b32 s36, 0\n"
    "s_mov_b32 s64, 0\n"
    "s_mov_b32 s50, 0\n"
    "s_lshl_b32 s72, s61, 2\n"
    "s_add_u32 s72, s72, 158208\n"
    "s_add_u32 s47, s46, 0x40000\n"
    "s_and_b32 s20, s61, 1\n"
    "s_lshl_b32 s20, s20, 7\n"
    "s_mov_b32 s14, 0x3f803f80\n"
    "s_mov_b32 s15, 0x3f803f80\n"
    "s_mov_b32 s82, 0x400000\n"
    "s_mov_b32 s83, 0x20000\n"
    "v_mov_b32 v112, v16\n"
    "v_mov_b32 v113, v17\n"
    "v_mov_b32 v114, v18\n"
    "v_mov_b32 v115, v19\n"
    "v_mov_b32 v117, 0\n"
    "v_mov_b32 v118, 0\n"
    "s_cmp_eq_u32 s61, 1\n"
    "s_mov_b32 s80, s90\n"
    "s_mov_b32 s81, s91\n"
    "s_cbranch_scc1 LCG%=\n"
    "s_cmp_lg_u32 s61, 0\n"
    "s_cbranch_scc1 LCF0_%=\n"
    "s_mov_b32 s80, s90\n"
    "s_mov_b32 s81, s91\n"
    "s_mul_i32 s38, s61, 4160\n"
    "s_add_u32 m0, s38, 4160\n"
    "s_mov_b32 s39, 16384\n"
    "buffer_load_dwordx4 v113, s[80:83], s39 offen lds\n"
    "s_add_u32 m0, s38, 5200\n"
    "s_mov_b32 s39, 17408\n"
    "buffer_load_dwordx4 v113, s[80:83], s39 offen lds\n"
    "s_add_u32 m0, s38, 6240\n"
    "s_mov_b32 s39, 20480\n"
    "buffer_load_dwordx4 v113, s[80:83], s39 offen lds\n"
    "s_add_u32 m0, s38, 7280\n"
    "s_mov_b32 s39, 21504\n"
    "buffer_load_dwordx4 v113, s[80:83], s39 offen lds\n"
    "LCF0_%=:\n"
    "s_mov_b32 s80, s90\n"
    "s_mov_b32 s81, s91\n"
    "s_mul_i32 s38, s61, 4160\n"
    "s_add_u32 m0, s38, 0\n"
    "s_mov_b32 s39, 0\n"
    "buffer_load_dwordx4 v113, s[80:83], s39 offen lds\n"
    "s_add_u32 m0, s38, 1040\n"
    "s_mov_b32 s39, 1024\n"
    "buffer_load_dwordx4 v113, s[80:83], s39 offen lds\n"
    "s_add_u32 m0, s38, 2080\n"
    "s_mov_b32 s39, 4096\n"
    "buffer_load_dwordx4 v113, s[80:83], s39 offen lds\n"
    "s_add_u32 m0, s38, 3120\n"
    "s_mov_b32 s39, 5120\n"
    "buffer_load_dwordx4 v113, s[80:83], s39 offen lds\n"
    "s_cmp_lg_u32 s61, 0\n"
    "s_cbranch_scc1 LCF1_%=\n"
    "s_mul_i32 s38, s61, 4160\n"
    "s_add_u32 m0, s38, 22080\n"
    "s_mov_b32 s39, 18432\n"
    "buffer_load_dwordx4 v113, s[80:83], s39 offen lds\n"
    "s_add_u32 m0, s38, 23120\n"
    "s_mov_b32 s39, 19456\n"
    "buffer_load_dwordx4 v113, s[80:83], s39 offen lds\n"
    "s_add_u32 m0, s38, 24160\n"
    "s_mov_b32 s39, 22528\n"
    "buffer_load_dwordx4 v113, s[80:83], s39 offen lds\n"
    "s_add_u32 m0, s38, 25200\n"
    "s_mov_b32 s39, 23552\n"
    "buffer_load_dwordx4 v113, s[80:83], s39 offen lds\n"
    "LCF1_%=:\n"
    "s_mul_i32 s38, s61, 4160\n"
    "s_add_u32 m0, s38, 17920\n"
    "s_mov_b32 s39, 2048\n"
    "buffer_load_dwordx4 v113, s[80:83], s39 offen lds\n"
    "s_add_u32 m0, s38, 18960\n"
    "s_mov_b32 s39, 3072\n"
    "buffer_load_dwordx4 v113, s[80:83], s39 offen lds\n"
    "s_add_u32 m0, s38, 20000\n"
    "s_mov_b32 s39, 6144\n"
    "buffer_load_dwordx4 v113, s[80:83], s39 offen lds\n"
    "s_add_u32 m0, s38, 21040\n"
    "s_mov_b32 s39, 7168\n"
    "buffer_load_dwordx4 v113, s[80:83], s39 offen lds\n"
    "s_cmp_lg_u32 s61, 0\n"
    "s_cbranch_scc1 LCF2_%=\n"
    "s_mul_i32 s38, s61, 4160\n"
    "s_add_u32 m0, s38, 40000\n"
    "s_mov_b32 s39, 49152\n"
    "buffer_load_dwordx4 v113, s[80:83], s39 offen lds\n"
    "s_add_u32 m0, s38, 41040\n"
    "s_mov_b32 s39, 50176\n"
    "buffer_load_dwordx4 v113, s[80:83], s39 offen lds\n"
    "s_add_u32 m0, s38, 42080\n"
    "s_mov_b32 s39, 53248\n"
    "buffer_load_dwordx4 v113, s[80:83], s39 offen lds\n"
    "s_add_u32 m0, s38, 43120\n"
    "s_mov_b32 s39, 54272\n"
    "buffer_load_dwordx4 v113, s[80:83], s39 offen lds\n"
    "LCF2_%=:\n"
    "s_mul_i32 s38, s61, 4160\n"
    "s_add_u32 m0, s38, 35840\n"
    "s_mov_b32 s39, 32768\n"
    "buffer_load_dwordx4 v113, s[80:83], s39 offen lds\n"
    "s_add_u32 m0, s38, 36880\n"
    "s_mov_b32 s39, 33792\n"
    "buffer_load_dwordx4 v113, s[80:83], s39 offen lds\n"
    "s_add_u32 m0, s38, 37920\n"
    "s_mov_b32 s39, 36864\n"
    "buffer_load_dwordx4 v113, s[80:83], s39 offen lds\n"
    "s_add_u32 m0, s38, 38960\n"
    "s_mov_b32 s39, 37888\n"
    "buffer_load_dwordx4 v113, s[80:83], s39 offen lds\n"
    "s_cmp_lg_u32 s61, 0\n"
    "s_cbranch_scc1 LCF3_%=\n"
    "s_mul_i32 s38, s61, 4160\n"
    "s_add_u32 m0, s38, 57920\n"
    "s_mov_b32 s39, 51200\n"
    "buffer_load_dwordx4 v113, s[80:83], s39 offen lds\n"
    "s_add_u32 m0, s38, 58960\n"
    "s_mov_b32 s39, 52224\n"
    "buffer_load_dwordx4 v113, s[80:83], s39 offen lds\n"
    "s_add_u32 m0, s38, 60000\n"
    "s_mov_b32 s39, 55296\n"
    "buffer_load_dwordx4 v113, s[80:83], s39 offen lds\n"
    "s_add_u32 m0, s38, 61040\n"
    "s_mov_b32 s39, 56320\n"
    "buffer_load_dwordx4 v113, s[80:83], s39 offen lds\n"
    "LCF3_%=:\n"
    "s_mul_i32 s38, s61, 4160\n"
    "s_add_u32 m0, s38, 53760\n"
    "s_mov_b32 s39, 34816\n"
    "buffer_load_dwordx4 v113, s[80:83], s39 offen lds\n"
    "s_add_u32 m0, s38, 54800\n"
    "s_mov_b32 s39, 35840\n"
    "buffer_load_dwordx4 v113, s[80:83], s39 offen lds\n"
    "s_add_u32 m0, s38, 55840\n"
    "s_mov_b32 s39, 38912\n"
    "buffer_load_dwordx4 v113, s[80:83], s39 offen lds\n"
    "s_add_u32 m0, s38, 56880\n"
    "s_mov_b32 s39, 39936\n"
    "buffer_load_dwordx4 v113, s[80:83], s39 offen lds\n"
    "LCG%=:\n"
    "s_waitcnt vmcnt(12)\n"
    "s_barrier\n"
    "s_add_u32 s38, s52, 40960\n"
    "v_mov_b32 v119, s38\n"
    "global_store_dword v119, v118, s[94:95] sc1\n"
    "s_mul_i32 s38, s61, 4160\n"
    "s_add_u32 m0, s38, 71680\n"
    "s_mov_b32 s39, 65536\n"
    "buffer_load_dwordx4 v113, s[80:83], s39 offen lds\n"
    "s_add_u32 m0, s38, 72720\n"
    "s_mov_b32 s39, 66560\n"
    "buffer_load_dwordx4 v113, s[80:83], s39 offen lds\n"
    "s_add_u32 m0, s38, 73760\n"
    "s_mov_b32 s39, 69632\n"
    "buffer_load_dwordx4 v113, s[80:83], s39 offen lds\n"
    "s_add_u32 m0, s38, 74800\n"
    "s_mov_b32 s39, 70656\n"
    "buffer_load_dwordx4 v113, s[80:83], s39 offen lds\n"
    "v_add_u32 v116, 0, v115\n"
    "ds_read_b64_tr_b16 v[168:169], v116 offset:0\n"
    "ds_read_b64_tr_b16 v[184:185], v116 offset:8\n"
    "ds_read_b64_tr_b16 v[200:201], v116 offset:16\n"
    "ds_read_b64_tr_b16 v[216:217], v116 offset:24\n"
    "ds_read_b64_tr_b16 v[232:233], v116 offset:512\n"
    "ds_read_b64_tr_b16 v[248:249], v116 offset:520\n"
    "ds_read_b64_tr_b16 v[104:105], v116 offset:528\n"
    "ds_read_b64_tr_b16 v[32:33], v116 offset:536\n"
    "s_waitcnt lgkmcnt(0)\n"
    "s_waitcnt vmcnt(13)\n"
    "s_barrier\n"
    "s_mul_i32 s38, s61, 4160\n"
    "s_add_u32 m0, s38, 89600\n"
    "s_mov_b32 s39, 67584\n"
    "buffer_load_dwordx4 v113, s[80:83], s39 offen lds\n"
    "s_add_u32 m0, s38, 90640\n"
    "s_mov_b32 s39, 68608\n"
    "buffer_load_dwordx4 v113, s[80:83], s39 offen lds\n"
    "s_add_u32 m0, s38, 91680\n"
    "s_mov_b32 s39, 71680\n"
    "buffer_load_dwordx4 v113, s[80:83], s39 offen lds\n"
    "s_add_u32 m0, s38, 92720\n"
    "s_mov_b32 s39, 72704\n"
    "buffer_load_dwordx4 v113, s[80:83], s39 offen lds\n"
    "v_add_u32 v116, 17920, v115\n"
    "ds_read_b64_tr_b16 v[170:171], v116 offset:0\n"
    "ds_read_b64_tr_b16 v[186:187], v116 offset:8\n"
    "ds_read_b64_tr_b16 v[202:203], v116 offset:16\n"
    "ds_read_b64_tr_b16 v[218:219], v116 offset:24\n"
    "ds_read_b64_tr_b16 v[234:235], v116 offset:512\n"
    "ds_read_b64_tr_b16 v[250:251], v116 offset:520\n"
    "ds_read_b64_tr_b16 v[106:107], v116 offset:528\n"
    "ds_read_b64_tr_b16 v[34:35], v116 offset:536\n"
    "s_waitcnt lgkmcnt(0)\n"
    "s_waitcnt vmcnt(13)\n"
    "s_barrier\n"
    "s_mul_i32 s38, s61, 4160\n"
    "s_add_u32 m0, s38, 0\n"
    "s_mov_b32 s39, 98304\n"
    "buffer_load_dwordx4 v113, s[80:83], s39 offen lds\n"
    "s_add_u32 m0, s38, 1040\n"
    "s_mov_b32 s39, 99328\n"
    "buffer_load_dwordx4 v113, s[80:83], s39 offen lds\n"
    "s_add_u32 m0, s38, 2080\n"
    "s_mov_b32 s39, 102400\n"
    "buffer_load_dwordx4 v113, s[80:83], s39 offen lds\n"
    "s_add_u32 m0, s38, 3120\n"
    "s_mov_b32 s39, 103424\n"
    "buffer_load_dwordx4 v113, s[80:83], s39 offen lds\n"
    "v_add_u32 v116, 35840, v115\n"
    "ds_read_b64_tr_b16 v[172:173], v116 offset:0\n"
    "ds_read_b64_tr_b16 v[188:189], v116 offset:8\n"
    "ds_read_b64_tr_b16 v[204:205], v116 offset:16\n"
    "ds_read_b64_tr_b16 v[220:221], v116 offset:24\n"
    "ds_read_b64_tr_b16 v[236:237], v116 offset:512\n"
    "ds_read_b64_tr_b16 v[252:253], v116 offset:520\n"
    "ds_read_b64_tr_b16 v[108:109], v116 offset:528\n"
    "ds_read_b64_tr_b16 v[36:37], v116 offset:536\n"
    "s_waitcnt lgkmcnt(0)\n"
    "s_waitcnt vmcnt(13)\n"
    "s_barrier\n"
    "s_mul_i32 s38, s61, 4160\n"
    "s_add_u32 m0, s38, 17920\n"
    "s_mov_b32 s39, 100352\n"
    "buffer_load_dwordx4 v113, s[80:83], s39 offen lds\n"
    "s_add_u32 m0, s38, 18960\n"
    "s_mov_b32 s39, 101376\n"
    "buffer_load_dwordx4 v113, s[80:83], s39 offen lds\n"
    "s_add_u32 m0, s38, 20000\n"
    "s_mov_b32 s39, 104448\n"
    "buffer_load_dwordx4 v113, s[80:83], s39 offen lds\n"
    "s_add_u32 m0, s38, 21040\n"
    "s_mov_b32 s39, 105472\n"
    "buffer_load_dwordx4 v113, s[80:83], s39 offen lds\n"
    "v_add_u32 v116, 53760, v115\n"
    "ds_read_b64_tr_b16 v[174:175], v116 offset:0\n"
    "ds_read_b64_tr_b16 v[190:191], v116 offset:8\n"
    "ds_read_b64_tr_b16 v[206:207], v116 offset:16\n"
    "ds_read_b64_tr_b16 v[222:223], v116 offset:24\n"
    "ds_read_b64_tr_b16 v[238:239], v116 offset:512\n"
    "ds_read_b64_tr_b16 v[254:255], v116 offset:520\n"
    "ds_read_b64_tr_b16 v[110:111], v116 offset:528\n"
    "ds_read_b64_tr_b16 v[38:39], v116 offset:536\n"
    "s_waitcnt lgkmcnt(0)\n"
    "s_waitcnt vmcnt(12)\n"
    "s_barrier\n"
    "s_mov_b32 s80, s88\n"
    "s_mov_b32 s81, s89\n"
    "s_mul_i32 s38, s61, 4480\n"
    "s_add_u32 m0, s38, 35840\n"
    "s_mov_b32 s39, 0\n"
    "buffer_load_dwordx4 v112, s[80:83], s39 offen lds\n"
    "s_add_u32 m0, s38, 36928\n"
    "s_mov_b32 s39, 32768\n"
    "buffer_load_dwordx4 v112, s[80:83], s39 offen lds\n"
    "s_add_u32 m0, s38, 38016\n"
    "s_mov_b32 s39, 65536\n"
    "buffer_load_dwordx4 v112, s[80:83], s39 offen lds\n"
    "s_add_u32 m0, s38, 39104\n"
    "s_mov_b32 s39, 98304\n"
    "buffer_load_dwordx4 v112, s[80:83], s39 offen lds\n"
    "v_add_u32 v116, 71680, v115\n"
    "ds_read_b64_tr_b16 v[176:177], v116 offset:0\n"
    "ds_read_b64_tr_b16 v[192:193], v116 offset:8\n"
    "ds_read_b64_tr_b16 v[208:209], v116 offset:16\n"
    "ds_read_b64_tr_b16 v[224:225], v116 offset:24\n"
    "ds_read_b64_tr_b16 v[240:241], v116 offset:512\n"
    "ds_read_b64_tr_b16 v[96:97], v116 offset:520\n"
    "ds_read_b64_tr_b16 v[24:25], v116 offset:528\n"
    "ds_read_b64_tr_b16 v[40:41], v116 offset:536\n"
    "s_waitcnt lgkmcnt(0)\n"
    "s_waitcnt vmcnt(12)\n"
    "s_barrier\n"
    "s_mul_i32 s38, s61, 4480\n"
    "s_add_u32 m0, s38, 53760\n"
    "s_mov_b32 s39, 524288\n"
    "buffer_load_dwordx4 v112, s[80:83], s39 offen lds\n"
    "s_add_u32 m0, s38, 54848\n"
    "s_mov_b32 s39, 557056\n"
    "buffer_load_dwordx4 v112, s[80:83], s39 offen lds\n"
    "s_add_u32 m0, s38, 55936\n"
    "s_mov_b32 s39, 589824\n"
    "buffer_load_dwordx4 v112, s[80:83], s39 offen lds\n"
    "s_add_u32 m0, s38, 57024\n"
    "s_mov_b32 s39, 622592\n"
    "buffer_load_dwordx4 v112, s[80:83], s39 offen lds\n"
    "v_add_u32 v116, 89600, v115\n"
    "ds_read_b64_tr_b16 v[178:179], v116 offset:0\n"
    "ds_read_b64_tr_b16 v[194:195], v116 offset:8\n"
    "ds_read_b64_tr_b16 v[210:211], v116 offset:16\n"
    "ds_read_b64_tr_b16 v[226:227], v116 offset:24\n"
    "ds_read_b64_tr_b16 v[242:243], v116 offset:512\n"
    "ds_read_b64_tr_b16 v[98:99], v116 offset:520\n"
    "ds_read_b64_tr_b16 v[26:27], v116 offset:528\n"
    "ds_read_b64_tr_b16 v[42:43], v116 offset:536\n"
    "s_waitcnt lgkmcnt(0)\n"
    "s_waitcnt vmcnt(12)\n"
    "s_barrier\n"
    "s_mul_i32 s38, s61, 4480\n"
    "s_add_u32 m0, s38, 71680\n"
    "s_mov_b32 s39, 1048576\n"
    "buffer_load_dwordx4 v112, s[80:83], s39 offen lds\n"
    "s_add_u32 m0, s38, 72768\n"
    "s_mov_b32 s39, 1081344\n"
    "buffer_load_dwordx4 v112, s[80:83], s39 offen lds\n"
    "s_add_u32 m0, s38, 73856\n"
    "s_mov_b32 s39, 1114112\n"
    "buffer_load_dwordx4 v112, s[80:83], s39 offen lds\n"
    "s_add_u32 m0, s38, 74944\n"
    "s_mov_b32 s39, 1146880\n"
    "buffer_load_dwordx4 v112, s[80:83], s39 offen lds\n"
    "v_add_u32 v116, 0, v115\n"
    "ds_read_b64_tr_b16 v[180:181], v116 offset:0\n"
    "ds_read_b64_tr_b16 v[196:197], v116 offset:8\n"
    "ds_read_b64_tr_b16 v[212:213], v116 offset:16\n"
    "ds_read_b64_tr_b16 v[228:229], v116 offset:24\n"
    "ds_read_b64_tr_b16 v[244:245], v116 offset:512\n"
    "ds_read_b64_tr_b16 v[100:101], v116 offset:520\n"
    "ds_read_b64_tr_b16 v[28:29], v116 offset:528\n"
    "ds_read_b64_tr_b16 v[44:45], v116 offset:536\n"
    "s_waitcnt lgkmcnt(0)\n"
    "s_waitcnt vmcnt(12)\n"
    "s_barrier\n"
    "s_add_u32 s42, s94, 4096\n"
    "s_addc_u32 s43, s95, 0\n"
    "global_load_dword v119, v118, s[42:43] sc1\n"
    "s_mul_i32 s38, s61, 4480\n"
    "s_add_u32 m0, s38, 89600\n"
    "s_mov_b32 s39, 1572864\n"
    "buffer_load_dwordx4 v112, s[80:83], s39 offen lds\n"
    "s_add_u32 m0, s38, 90688\n"
    "s_mov_b32 s39, 1605632\n"
    "buffer_load_dwordx4 v112, s[80:83], s39 offen lds\n"
    "s_add_u32 m0, s38, 91776\n"
    "s_mov_b32 s39, 1638400\n"
    "buffer_load_dwordx4 v112, s[80:83], s39 offen lds\n"
    "s_add_u32 m0, s38, 92864\n"
    "s_mov_b32 s39, 1671168\n"
    "buffer_load_dwordx4 v112, s[80:83], s39 offen lds\n"
    "v_add_u32 v116, 17920, v115\n"
    "ds_read_b64_tr_b16 v[182:183], v116 offset:0\n"
    "ds_read_b64_tr_b16 v[198:199], v116 offset:8\n"
    "ds_read_b64_tr_b16 v[214:215], v116 offset:16\n"
    "ds_read_b64_tr_b16 v[230:231], v116 offset:24\n"
    "ds_read_b64_tr_b16 v[246:247], v116 offset:512\n"
    "ds_read_b64_tr_b16 v[102:103], v116 offset:520\n"
    "ds_read_b64_tr_b16 v[30:31], v116 offset:528\n"
    "ds_read_b64_tr_b16 v[46:47], v116 offset:536\n"
    "ds_write_b128 v9, v[24:27] offset:0\n"
    "s_waitcnt lgkmcnt(2)\n"
    "ds_write_b128 v9, v[28:31] offset:1024\n"
    "ds_write_b128 v9, v[32:35] offset:8192\n"
    "ds_write_b128 v9, v[36:39] offset:9216\n"
    "ds_write_b128 v9, v[40:43] offset:16384\n"
    "s_waitcnt lgkmcnt(5)\n"
    "ds_write_b128 v9, v[44:47] offset:17408\n"
    "s_waitcnt lgkmcnt(0)\n"
    "s_waitcnt vmcnt(13)\n"
    "s_barrier\n"
    "s_mul_i32 s38, s61, 4480\n"
    "s_add_u32 m0, s38, 0\n"
    "s_mov_b32 s39, 2097152\n"
    "buffer_load_dwordx4 v112, s[80:83], s39 offen lds\n"
    "s_add_u32 m0, s38, 1088\n"
    "s_mov_b32 s39, 2129920\n"
    "buffer_load_dwordx4 v112, s[80:83], s39 offen lds\n"
    "s_add_u32 m0, s38, 2176\n"
    "s_mov_b32 s39, 2162688\n"
    "buffer_load_dwordx4 v112, s[80:83], s39 offen lds\n"
    "s_add_u32 m0, s38, 3264\n"
    "s_mov_b32 s39, 2195456\n"
    "buffer_load_dwordx4 v112, s[80:83], s39 offen lds\n"
    "v_add_u32 v116, 35840, v114\n"
    "ds_read_b64_tr_b16 a[176:177], v116 offset:0\n"
    "ds_read_b64_tr_b16 a[178:179], v116 offset:2176\n"
    "ds_read_b64_tr_b16 a[180:181], v116 offset:8960\n"
    "ds_read_b64_tr_b16 a[182:183], v116 offset:11136\n"
    "v_xor_b32 v116, 32, v116\n"
    "ds_read_b64_tr_b16 a[184:185], v116 offset:0\n"
    "ds_read_b64_tr_b16 a[186:187], v116 offset:2176\n"
    "ds_read_b64_tr_b16 a[188:189], v116 offset:8960\n"
    "ds_read_b64_tr_b16 a[190:191], v116 offset:11136\n"
    "s_waitcnt lgkmcnt(0)\n"
    "s_waitcnt vmcnt(13)\n"
    "s_barrier\n"
    "s_mul_i32 s38, s61, 4480\n"
    "s_add_u32 m0, s38, 17920\n"
    "s_mov_b32 s39, 2621440\n"
    "buffer_load_dwordx4 v112, s[80:83], s39 offen lds\n"
    "s_add_u32 m0, s38, 19008\n"
    "s_mov_b32 s39, 2654208\n"
    "buffer_load_dwordx4 v112, s[80:83], s39 offen lds\n"
    "s_add_u32 m0, s38, 20096\n"
    "s_mov_b32 s39, 2686976\n"
    "buffer_load_dwordx4 v112, s[80:83], s39 offen lds\n"
    "s_add_u32 m0, s38, 21184\n"
    "s_mov_b32 s39, 2719744\n"
    "buffer_load_dwordx4 v112, s[80:83], s39 offen lds\n"
    "v_add_u32 v116, 53760, v114\n"
    "ds_read_b64_tr_b16 a[192:193], v116 offset:0\n"
    "ds_read_b64_tr_b16 a[194:195], v116 offset:2176\n"
    "ds_read_b64_tr_b16 a[196:197], v116 offset:8960\n"
    "ds_read_b64_tr_b16 a[198:199], v116 offset:11136\n"
    "v_xor_b32 v116, 32, v116\n"
    "ds_read_b64_tr_b16 a[200:201], v116 offset:0\n"
    "ds_read_b64_tr_b16 a[202:203], v116 offset:2176\n"
    "ds_read_b64_tr_b16 a[204:205], v116 offset:8960\n"
    "ds_read_b64_tr_b16 a[206:207], v116 offset:11136\n"
    "s_waitcnt lgkmcnt(0)\n"
    "s_waitcnt vmcnt(13)\n"
    "s_barrier\n"
    "s_mul_i32 s38, s61, 4480\n"
    "s_add_u32 m0, s38, 35840\n"
    "s_mov_b32 s39, 3145728\n"
    "buffer_load_dwordx4 v112, s[80:83], s39 offen lds\n"
    "s_add_u32 m0, s38, 36928\n"
    "s_mov_b32 s39, 3178496\n"
    "buffer_load_dwordx4 v112, s[80:83], s39 offen lds\n"
    "s_add_u32 m0, s38, 38016\n"
    "s_mov_b32 s39, 3211264\n"
    "buffer_load_dwordx4 v112, s[80:83], s39 offen lds\n"
    "s_add_u32 m0, s38, 39104\n"
    "s_mov_b32 s39, 3244032\n"
    "buffer_load_dwordx4 v112, s[80:83], s39 offen lds\n"
    "v_add_u32 v116, 71680, v114\n"
    "ds_read_b64_tr_b16 a[208:209], v116 offset:0\n"
    "ds_read_b64_tr_b16 a[210:211], v116 offset:2176\n"
    "ds_read_b64_tr_b16 a[212:213], v116 offset:8960\n"
    "ds_read_b64_tr_b16 a[214:215], v116 offset:11136\n"
    "v_xor_b32 v116, 32, v116\n"
    "ds_read_b64_tr_b16 a[216:217], v116 offset:0\n"
    "ds_read_b64_tr_b16 a[218:219], v116 offset:2176\n"
    "ds_read_b64_tr_b16 a[220:221], v116 offset:8960\n"
    "ds_read_b64_tr_b16 a[222:223], v116 offset:11136\n"
    "s_waitcnt lgkmcnt(0)\n"
    "s_waitcnt vmcnt(12)\n"
    "s_barrier\n"
    "s_mul_i32 s38, s61, 4480\n"
    "s_add_u32 m0, s38, 53760\n"
    "s_mov_b32 s39, 3670016\n"
    "buffer_load_dwordx4 v112, s[80:83], s39 offen lds\n"
    "s_add_u32 m0, s38, 54848\n"
    "s_mov_b32 s39, 3702784\n"
    "buffer_load_dwordx4 v112, s[80:83], s39 offen lds\n"
    "s_add_u32 m0, s38, 55936\n"
    "s_mov_b32 s39, 3735552\n"
    "buffer_load_dwordx4 v112, s[80:83], s39 offen lds\n"
    "s_add_u32 m0, s38, 57024\n"
    "s_mov_b32 s39, 3768320\n"
    "buffer_load_dwordx4 v112, s[80:83], s39 offen lds\n"
    "v_add_u32 v116, 89600, v114\n"
    "ds_read_b64_tr_b16 a[224:225], v116 offset:0\n"
    "ds_read_b64_tr_b16 a[226:227], v116 offset:2176\n"
    "ds_read_b64_tr_b16 a[228:229], v116 offset:8960\n"
    "ds_read_b64_tr_b16 a[230:231], v116 offset:11136\n"
    "v_xor_b32 v116, 32, v116\n"
    "ds_read_b64_tr_b16 a[232:233], v116 offset:0\n"
    "ds_read_b64_tr_b16 a[234:235], v116 offset:2176\n"
    "ds_read_b64_tr_b16 a[236:237], v116 offset:8960\n"
    "ds_read_b64_tr_b16 a[238:239], v116 offset:11136\n"
    "s_waitcnt lgkmcnt(0)\n"
    "s_waitcnt vmcnt(12)\n"
    "s_barrier\n"
    "s_mov_b32 s80, s86\n"
    "s_mov_b32 s81, s87\n"
    "s_mul_i32 s38, s61, 4480\n"
    "s_add_u32 m0, s38, 71680\n"
    "s_mov_b32 s39, 0\n"
    "buffer_load_dwordx4 v112, s[80:83], s39 offen lds\n"
    "s_add_u32 m0, s38, 72768\n"
    "s_mov_b32 s39, 32768\n"
    "buffer_load_dwordx4 v112, s[80:83], s39 offen lds\n"
    "s_add_u32 m0, s38, 73856\n"
    "s_mov_b32 s39, 65536\n"
    "buffer_load_dwordx4 v112, s[80:83], s39 offen lds\n"
    "s_add_u32 m0, s38, 74944\n"
    "s_mov_b32 s39, 98304\n"
    "buffer_load_dwordx4 v112, s[80:83], s39 offen lds\n"
    "v_add_u32 v116, 0, v114\n"
    "ds_read_b64_tr_b16 a[240:241], v116 offset:0\n"
    "ds_read_b64_tr_b16 a[242:243], v116 offset:2176\n"
    "ds_read_b64_tr_b16 a[244:245], v116 offset:8960\n"
    "ds_read_b64_tr_b16 a[246:247], v116 offset:11136\n"
    "v_xor_b32 v116, 32, v116\n"
    "ds_read_b64_tr_b16 a[248:249], v116 offset:0\n"
    "ds_read_b64_tr_b16 a[250:251], v116 offset:2176\n"
    "ds_read_b64_tr_b16 a[252:253], v116 offset:8960\n"
    "ds_read_b64_tr_b16 a[254:255], v116 offset:11136\n"
    "s_waitcnt lgkmcnt(0)\n"
    "s_waitcnt vmcnt(12)\n"
    "s_barrier\n"
    "s_add_u32 s38, s52, 8192\n"
    "s_add_u32 s42, s94, s38\n"
    "s_addc_u32 s43, s95, 0\n"
    "global_store_dword v118, v119, s[42:43] sc1\n"
    "s_mul_i32 s38, s61, 4480\n"
    "s_add_u32 m0, s38, 89600\n"
    "s_mov_b32 s39, 524288\n"
    "buffer_load_dwordx4 v112, s[80:83], s39 offen lds\n"
    "s_add_u32 m0, s38, 90688\n"
    "s_mov_b32 s39, 557056\n"
    "buffer_load_dwordx4 v112, s[80:83], s39 offen lds\n"
    "s_add_u32 m0, s38, 91776\n"
    "s_mov_b32 s39, 589824\n"
    "buffer_load_dwordx4 v112, s[80:83], s39 offen lds\n"
    "s_add_u32 m0, s38, 92864\n"
    "s_mov_b32 s39, 622592\n"
    "buffer_load_dwordx4 v112, s[80:83], s39 offen lds\n"
    "v_add_u32 v116, 17920, v114\n"
    "ds_read_b64_tr_b16 v[120:121], v116 offset:0\n"
    "ds_read_b64_tr_b16 v[122:123], v116 offset:2176\n"
    "ds_read_b64_tr_b16 v[124:125], v116 offset:8960\n"
    "ds_read_b64_tr_b16 v[126:127], v116 offset:11136\n"
    "v_xor_b32 v116, 32, v116\n"
    "ds_read_b64_tr_b16 v[128:129], v116 offset:0\n"
    "ds_read_b64_tr_b16 v[130:131], v116 offset:2176\n"
    "ds_read_b64_tr_b16 v[132:133], v116 offset:8960\n"
    "ds_read_b64_tr_b16 v[134:135], v116 offset:11136\n"
    "s_waitcnt lgkmcnt(0)\n"
    "s_waitcnt vmcnt(13)\n"
    "s_barrier\n"
    "s_mul_i32 s38, s61, 4480\n"
    "s_add_u32 m0, s38, 0\n"
    "s_mov_b32 s39, 1048576\n"
    "buffer_load_dwordx4 v112, s[80:83], s39 offen lds\n"
    "s_add_u32 m0, s38, 1088\n"
    "s_mov_b32 s39, 1081344\n"
    "buffer_load_dwordx4 v112, s[80:83], s39 offen lds\n"
    "s_add_u32 m0, s38, 2176\n"
    "s_mov_b32 s39, 1114112\n"
    "buffer_load_dwordx4 v112, s[80:83], s39 offen lds\n"
    "s_add_u32 m0, s38, 3264\n"
    "s_mov_b32 s39, 1146880\n"
    "buffer_load_dwordx4 v112, s[80:83], s39 offen lds\n"
    "v_add_u32 v116, 35840, v114\n"
    "ds_read_b64_tr_b16 v[136:137], v116 offset:0\n"
    "ds_read_b64_tr_b16 v[138:139], v116 offset:2176\n"
    "ds_read_b64_tr_b16 v[140:141], v116 offset:8960\n"
    "ds_read_b64_tr_b16 v[142:143], v116 offset:11136\n"
    "v_xor_b32 v116, 32, v116\n"
    "ds_read_b64_tr_b16 v[144:145], v116 offset:0\n"
    "ds_read_b64_tr_b16 v[146:147], v116 offset:2176\n"
    "ds_read_b64_tr_b16 v[148:149], v116 offset:8960\n"
    "ds_read_b64_tr_b16 v[150:151], v116 offset:11136\n"
    "s_waitcnt lgkmcnt(0)\n"
    "s_waitcnt vmcnt(13)\n"
    "s_barrier\n"
    "s_mul_i32 s38, s61, 4480\n"
    "s_add_u32 m0, s38, 17920\n"
    "s_mov_b32 s39, 1572864\n"
    "buffer_load_dwordx4 v112, s[80:83], s39 offen lds\n"
    "s_add_u32 m0, s38, 19008\n"
    "s_mov_b32 s39, 1605632\n"
    "buffer_load_dwordx4 v112, s[80:83], s39 offen lds\n"
    "s_add_u32 m0, s38, 20096\n"
    "s_mov_b32 s39, 1638400\n"
    "buffer_load_dwordx4 v112, s[80:83], s39 offen lds\n"
    "s_add_u32 m0, s38, 21184\n"
    "s_mov_b32 s39, 1671168\n"
    "buffer_load_dwordx4 v112, s[80:83], s39 offen lds\n"
    "v_add_u32 v116, 53760, v114\n"
    "ds_read_b64_tr_b16 v[152:153], v116 offset:0\n"
    "ds_read_b64_tr_b16 v[154:155], v116 offset:2176\n"
    "ds_read_b64_tr_b16 v[156:157], v116 offset:8960\n"
    "ds_read_b64_tr_b16 v[158:159], v116 offset:11136\n"
    "v_xor_b32 v116, 32, v116\n"
    "ds_read_b64_tr_b16 v[160:161], v116 offset:0\n"
    "ds_read_b64_tr_b16 v[162:163], v116 offset:2176\n"
    "ds_read_b64_tr_b16 v[164:165], v116 offset:8960\n"
    "ds_read_b64_tr_b16 v[166:167], v116 offset:11136\n"
    "s_waitcnt lgkmcnt(0)\n"
    "s_waitcnt vmcnt(13)\n"
    "s_barrier\n"
    "s_mul_i32 s38, s61, 4480\n"
    "s_add_u32 m0, s38, 35840\n"
    "s_mov_b32 s39, 2097152\n"
    "buffer_load_dwordx4 v112, s[80:83], s39 offen lds\n"
    "s_add_u32 m0, s38, 36928\n"
    "s_mov_b32 s39, 2129920\n"
    "buffer_load_dwordx4 v112, s[80:83], s39 offen lds\n"
    "s_add_u32 m0, s38, 38016\n"
    "s_mov_b32 s39, 2162688\n"
    "buffer_load_dwordx4 v112, s[80:83], s39 offen lds\n"
    "s_add_u32 m0, s38, 39104\n"
    "s_mov_b32 s39, 2195456\n"
    "buffer_load_dwordx4 v112, s[80:83], s39 offen lds\n"
    "v_add_u32 v116, 71680, v114\n"
    "ds_read_b64_tr_b16 v[64:65], v116 offset:0\n"
    "ds_read_b64_tr_b16 v[66:67], v116 offset:2176\n"
    "ds_read_b64_tr_b16 v[68:69], v116 offset:8960\n"
    "ds_read_b64_tr_b16 v[70:71], v116 offset:11136\n"
    "v_xor_b32 v116, 32, v116\n"
    "ds_read_b64_tr_b16 v[72:73], v116 offset:0\n"
    "ds_read_b64_tr_b16 v[74:75], v116 offset:2176\n"
    "ds_read_b64_tr_b16 v[76:77], v116 offset:8960\n"
    "ds_read_b64_tr_b16 v[78:79], v116 offset:11136\n"
    "s_waitcnt lgkmcnt(0)\n"
    "s_waitcnt vmcnt(12)\n"
    "s_barrier\n"
    "s_mul_i32 s38, s61, 4480\n"
    "s_add_u32 m0, s38, 53760\n"
    "s_mov_b32 s39, 2621440\n"
    "buffer_load_dwordx4 v112, s[80:83], s39 offen lds\n"
    "s_add_u32 m0, s38, 54848\n"
    "s_mov_b32 s39, 2654208\n"
    "buffer_load_dwordx4 v112, s[80:83], s39 offen lds\n"
    "s_add_u32 m0, s38, 55936\n"
    "s_mov_b32 s39, 2686976\n"
    "buffer_load_dwordx4 v112, s[80:83], s39 offen lds\n"
    "s_add_u32 m0, s38, 57024\n"
    "s_mov_b32 s39, 2719744\n"
    "buffer_load_dwordx4 v112, s[80:83], s39 offen lds\n"
    "v_add_u32 v116, 89600, v114\n"
    "ds_read_b64_tr_b16 v[80:81], v116 offset:0\n"
    "ds_read_b64_tr_b16 v[82:83], v116 offset:2176\n"
    "ds_read_b64_tr_b16 v[84:85], v116 offset:8960\n"
    "ds_read_b64_tr_b16 v[86:87], v116 offset:11136\n"
    "v_xor_b32 v116, 32, v116\n"
    "ds_read_b64_tr_b16 v[88:89], v116 offset:0\n"
    "ds_read_b64_tr_b16 v[90:91], v116 offset:2176\n"
    "ds_read_b64_tr_b16 v[92:93], v116 offset:8960\n"
    "ds_read_b64_tr_b16 v[94:95], v116 offset:11136\n"
    "s_waitcnt lgkmcnt(0)\n"
    "s_waitcnt vmcnt(12)\n"
    "s_barrier\n"
    "s_mul_i32 s38, s61, 4480\n"
    "s_add_u32 m0, s38, 71680\n"
    "s_mov_b32 s39, 3145728\n"
    "buffer_load_dwordx4 v112, s[80:83], s39 offen lds\n"
    "s_add_u32 m0, s38, 72768\n"
    "s_mov_b32 s39, 3178496\n"
    "buffer_load_dwordx4 v112, s[80:83], s39 offen lds\n"
    "s_add_u32 m0, s38, 73856\n"
    "s_mov_b32 s39, 3211264\n"
    "buffer_load_dwordx4 v112, s[80:83], s39 offen lds\n"
    "s_add_u32 m0, s38, 74944\n"
    "s_mov_b32 s39, 3244032\n"
    "buffer_load_dwordx4 v112, s[80:83], s39 offen lds\n"
    "v_add_u32 v116, 0, v114\n"
    "ds_read_b64_tr_b16 a[80:81], v116 offset:0\n"
    "ds_read_b64_tr_b16 a[82:83], v116 offset:2176\n"
    "ds_read_b64_tr_b16 a[84:85], v116 offset:8960\n"
    "ds_read_b64_tr_b16 a[86:87], v116 offset:11136\n"
    "v_xor_b32 v116, 32, v116\n"
    "ds_read_b64_tr_b16 a[88:89], v116 offset:0\n"
    "ds_read_b64_tr_b16 a[90:91], v116 offset:2176\n"
    "ds_read_b64_tr_b16 a[92:93], v116 offset:8960\n"
    "ds_read_b64_tr_b16 a[94:95], v116 offset:11136\n"
    "s_waitcnt lgkmcnt(0)\n"
    "s_waitcnt vmcnt(12)\n"
    "s_barrier\n"
    "s_mul_i32 s38, s61, 4480\n"
    "s_add_u32 m0, s38, 89600\n"
    "s_mov_b32 s39, 3670016\n"
    "buffer_load_dwordx4 v112, s[80:83], s39 offen lds\n"
    "s_add_u32 m0, s38, 90688\n"
    "s_mov_b32 s39, 3702784\n"
    "buffer_load_dwordx4 v112, s[80:83], s39 offen lds\n"
    "s_add_u32 m0, s38, 91776\n"
    "s_mov_b32 s39, 3735552\n"
    "buffer_load_dwordx4 v112, s[80:83], s39 offen lds\n"
    "s_add_u32 m0, s38, 92864\n"
    "s_mov_b32 s39, 3768320\n"
    "buffer_load_dwordx4 v112, s[80:83], s39 offen lds\n"
    "v_add_u32 v116, 17920, v114\n"
    "ds_read_b64_tr_b16 a[96:97], v116 offset:0\n"
    "ds_read_b64_tr_b16 a[98:99], v116 offset:2176\n"
    "ds_read_b64_tr_b16 a[100:101], v116 offset:8960\n"
    "ds_read_b64_tr_b16 a[102:103], v116 offset:11136\n"
    "v_xor_b32 v116, 32, v116\n"
    "ds_read_b64_tr_b16 a[104:105], v116 offset:0\n"
    "ds_read_b64_tr_b16 a[106:107], v116 offset:2176\n"
    "ds_read_b64_tr_b16 a[108:109], v116 offset:8960\n"
    "ds_read_b64_tr_b16 a[110:111], v116 offset:11136\n"
    "s_waitcnt lgkmcnt(0)\n"
    "s_waitcnt vmcnt(12)\n"
    "s_barrier\n"
    "s_mov_b32 s80, s84\n"
    "s_mov_b32 s81, s85\n"
    "s_mul_i32 s38, s61, 4480\n"
    "s_add_u32 m0, s38, 0\n"
    "s_mov_b32 s39, 0\n"
    "buffer_load_dwordx4 v112, s[80:83], s39 offen lds\n"
    "s_add_u32 m0, s38, 1088\n"
    "s_mov_b32 s39, 32768\n"
    "buffer_load_dwordx4 v112, s[80:83], s39 offen lds\n"
    "s_add_u32 m0, s38, 2176\n"
    "s_mov_b32 s39, 65536\n"
    "buffer_load_dwordx4 v112, s[80:83], s39 offen lds\n"
    "s_add_u32 m0, s38, 3264\n"
    "s_mov_b32 s39, 98304\n"
    "buffer_load_dwordx4 v112, s[80:83], s39 offen lds\n"
    "v_add_u32 v116, 35840, v114\n"
    "ds_read_b64_tr_b16 a[112:113], v116 offset:0\n"
    "ds_read_b64_tr_b16 a[114:115], v116 offset:2176\n"
    "ds_read_b64_tr_b16 a[116:117], v116 offset:8960\n"
    "ds_read_b64_tr_b16 a[118:119], v116 offset:11136\n"
    "v_xor_b32 v116, 32, v116\n"
    "ds_read_b64_tr_b16 a[120:121], v116 offset:0\n"
    "ds_read_b64_tr_b16 a[122:123], v116 offset:2176\n"
    "ds_read_b64_tr_b16 a[124:125], v116 offset:8960\n"
    "ds_read_b64_tr_b16 a[126:127], v116 offset:11136\n"
    "s_waitcnt lgkmcnt(0)\n"
    "s_waitcnt vmcnt(12)\n"
    "s_barrier\n"
    "s_mul_i32 s38, s61, 4480\n"
    "s_add_u32 m0, s38, 17920\n"
    "s_mov_b32 s39, 524288\n"
    "buffer_load_dwordx4 v112, s[80:83], s39 offen lds\n"
    "s_add_u32 m0, s38, 19008\n"
    "s_mov_b32 s39, 557056\n"
    "buffer_load_dwordx4 v112, s[80:83], s39 offen lds\n"
    "s_add_u32 m0, s38, 20096\n"
    "s_mov_b32 s39, 589824\n"
    "buffer_load_dwordx4 v112, s[80:83], s39 offen lds\n"
    "s_add_u32 m0, s38, 21184\n"
    "s_mov_b32 s39, 622592\n"
    "buffer_load_dwordx4 v112, s[80:83], s39 offen lds\n"
    "v_add_u32 v116, 53760, v114\n"
    "ds_read_b64_tr_b16 a[128:129], v116 offset:0\n"
    "ds_read_b64_tr_b16 a[130:131], v116 offset:2176\n"
    "ds_read_b64_tr_b16 a[132:133], v116 offset:8960\n"
    "ds_read_b64_tr_b16 a[134:135], v116 offset:11136\n"
    "v_xor_b32 v116, 32, v116\n"
    "ds_read_b64_tr_b16 a[136:137], v116 offset:0\n"
    "ds_read_b64_tr_b16 a[138:139], v116 offset:2176\n"
    "ds_read_b64_tr_b16 a[140:141], v116 offset:8960\n"
    "ds_read_b64_tr_b16 a[142:143], v116 offset:11136\n"
    "s_waitcnt lgkmcnt(0)\n"
    "s_waitcnt vmcnt(12)\n"
    "s_barrier\n"
    "global_load_dword v117, v118, s[92:93] sc1\n"
    "s_mul_i32 s38, s61, 4480\n"
    "s_add_u32 m0, s38, 35840\n"
    "s_mov_b32 s39, 1048576\n"
    "buffer_load_dwordx4 v112, s[80:83], s39 offen lds\n"
    "s_add_u32 m0, s38, 36928\n"
    "s_mov_b32 s39, 1081344\n"
    "buffer_load_dwordx4 v112, s[80:83], s39 offen lds\n"
    "s_add_u32 m0, s38, 38016\n"
    "s_mov_b32 s39, 1114112\n"
    "buffer_load_dwordx4 v112, s[80:83], s39 offen lds\n"
    "s_add_u32 m0, s38, 39104\n"
    "s_mov_b32 s39, 1146880\n"
    "buffer_load_dwordx4 v112, s[80:83], s39 offen lds\n"
    "v_add_u32 v116, 71680, v114\n"
    "ds_read_b64_tr_b16 a[144:145], v116 offset:0\n"
    "ds_read_b64_tr_b16 a[146:147], v116 offset:2176\n"
    "ds_read_b64_tr_b16 a[148:149], v116 offset:8960\n"
    "ds_read_b64_tr_b16 a[150:151], v116 offset:11136\n"
    "v_xor_b32 v116, 32, v116\n"
    "ds_read_b64_tr_b16 a[152:153], v116 offset:0\n"
    "ds_read_b64_tr_b16 a[154:155], v116 offset:2176\n"
    "ds_read_b64_tr_b16 a[156:157], v116 offset:8960\n"
    "ds_read_b64_tr_b16 a[158:159], v116 offset:11136\n"
    "s_waitcnt lgkmcnt(0)\n"
    "s_waitcnt vmcnt(13)\n"
    "s_barrier\n"
    "s_mul_i32 s38, s61, 4480\n"
    "s_add_u32 m0, s38, 53760\n"
    "s_mov_b32 s39, 1572864\n"
    "buffer_load_dwordx4 v112, s[80:83], s39 offen lds\n"
    "s_add_u32 m0, s38, 54848\n"
    "s_mov_b32 s39, 1605632\n"
    "buffer_load_dwordx4 v112, s[80:83], s39 offen lds\n"
    "s_add_u32 m0, s38, 55936\n"
    "s_mov_b32 s39, 1638400\n"
    "buffer_load_dwordx4 v112, s[80:83], s39 offen lds\n"
    "s_add_u32 m0, s38, 57024\n"
    "s_mov_b32 s39, 1671168\n"
    "buffer_load_dwordx4 v112, s[80:83], s39 offen lds\n"
    "v_add_u32 v116, 89600, v114\n"
    "ds_read_b64_tr_b16 a[160:161], v116 offset:0\n"
    "ds_read_b64_tr_b16 a[162:163], v116 offset:2176\n"
    "ds_read_b64_tr_b16 a[164:165], v116 offset:8960\n"
    "ds_read_b64_tr_b16 a[166:167], v116 offset:11136\n"
    "v_xor_b32 v116, 32, v116\n"
    "ds_read_b64_tr_b16 a[168:169], v116 offset:0\n"
    "ds_read_b64_tr_b16 a[170:171], v116 offset:2176\n"
    "ds_read_b64_tr_b16 a[172:173], v116 offset:8960\n"
    "ds_read_b64_tr_b16 a[174:175], v116 offset:11136\n"
    "s_waitcnt lgkmcnt(0)\n"
    "s_waitcnt vmcnt(13)\n"
    "s_barrier\n"
    "s_mul_i32 s38, s61, 4480\n"
    "s_add_u32 m0, s38, 71680\n"
    "s_mov_b32 s39, 2097152\n"
    "buffer_load_dwordx4 v112, s[80:83], s39 offen lds\n"
    "s_add_u32 m0, s38, 72768\n"
    "s_mov_b32 s39, 2129920\n"
    "buffer_load_dwordx4 v112, s[80:83], s39 offen lds\n"
    "s_add_u32 m0, s38, 73856\n"
    "s_mov_b32 s39, 2162688\n"
    "buffer_load_dwordx4 v112, s[80:83], s39 offen lds\n"
    "s_add_u32 m0, s38, 74944\n"
    "s_mov_b32 s39, 2195456\n"
    "buffer_load_dwordx4 v112, s[80:83], s39 offen lds\n"
    "v_add_u32 v116, 0, v114\n"
    "ds_read_b64_tr_b16 v[16:17], v116 offset:0\n"
    "ds_read_b64_tr_b16 v[18:19], v116 offset:2176\n"
    "ds_read_b64_tr_b16 v[20:21], v116 offset:8960\n"
    "ds_read_b64_tr_b16 v[22:23], v116 offset:11136\n"
    "v_xor_b32 v116, 32, v116\n"
    "ds_read_b64_tr_b16 v[24:25], v116 offset:0\n"
    "ds_read_b64_tr_b16 v[26:27], v116 offset:2176\n"
    "ds_read_b64_tr_b16 v[28:29], v116 offset:8960\n"
    "ds_read_b64_tr_b16 v[30:31], v116 offset:11136\n"
    "s_waitcnt lgkmcnt(0)\n"
    "s_waitcnt vmcnt(13)\n"
    "s_barrier\n"
    "s_mul_i32 s38, s61, 4480\n"
    "s_add_u32 m0, s38, 89600\n"
    "s_mov_b32 s39, 2621440\n"
    "buffer_load_dwordx4 v112, s[80:83], s39 offen lds\n"
    "s_add_u32 m0, s38, 90688\n"
    "s_mov_b32 s39, 2654208\n"
    "buffer_load_dwordx4 v112, s[80:83], s39 offen lds\n"
    "s_add_u32 m0, s38, 91776\n"
    "s_mov_b32 s39, 2686976\n"
    "buffer_load_dwordx4 v112, s[80:83], s39 offen lds\n"
    "s_add_u32 m0, s38, 92864\n"
    "s_mov_b32 s39, 2719744\n"
    "buffer_load_dwordx4 v112, s[80:83], s39 offen lds\n"
    "v_add_u32 v116, 17920, v114\n"
    "ds_read_b64_tr_b16 v[32:33], v116 offset:0\n"
    "ds_read_b64_tr_b16 v[34:35], v116 offset:2176\n"
    "ds_read_b64_tr_b16 v[36:37], v116 offset:8960\n"
    "ds_read_b64_tr_b16 v[38:39], v116 offset:11136\n"
    "v_xor_b32 v116, 32, v116\n"
    "ds_read_b64_tr_b16 v[40:41], v116 offset:0\n"
    "ds_read_b64_tr_b16 v[42:43], v116 offset:2176\n"
    "ds_read_b64_tr_b16 v[44:45], v116 offset:8960\n"
    "ds_read_b64_tr_b16 v[46:47], v116 offset:11136\n"
    "s_waitcnt lgkmcnt(0)\n"
    "s_waitcnt vmcnt(12)\n"
    "s_barrier\n"
    "s_mul_i32 s38, s61, 4480\n"
    "s_add_u32 m0, s38, 0\n"
    "s_mov_b32 s39, 3145728\n"
    "buffer_load_dwordx4 v112, s[80:83], s39 offen lds\n"
    "s_add_u32 m0, s38, 1088\n"
    "s_mov_b32 s39, 3178496\n"
    "buffer_load_dwordx4 v112, s[80:83], s39 offen lds\n"
    "s_add_u32 m0, s38, 2176\n"
    "s_mov_b32 s39, 3211264\n"
    "buffer_load_dwordx4 v112, s[80:83], s39 offen lds\n"
    "s_add_u32 m0, s38, 3264\n"
    "s_mov_b32 s39, 3244032\n"
    "buffer_load_dwordx4 v112, s[80:83], s39 offen lds\n"
    "v_add_u32 v116, 35840, v114\n"
    "ds_read_b64_tr_b16 v[48:49], v116 offset:0\n"
    "ds_read_b64_tr_b16 v[50:51], v116 offset:2176\n"
    "ds_read_b64_tr_b16 v[52:53], v116 offset:8960\n"
    "ds_read_b64_tr_b16 v[54:55], v116 offset:11136\n"
    "v_xor_b32 v116, 32, v116\n"
    "ds_read_b64_tr_b16 v[56:57], v116 offset:0\n"
    "ds_read_b64_tr_b16 v[58:59], v116 offset:2176\n"
    "ds_read_b64_tr_b16 v[60:61], v116 offset:8960\n"
    "ds_read_b64_tr_b16 v[62:63], v116 offset:11136\n"
    "s_waitcnt lgkmcnt(0)\n"
    "s_waitcnt vmcnt(12)\n"
    "s_barrier\n"
    "s_mul_i32 s38, s61, 4480\n"
    "s_add_u32 m0, s38, 17920\n"
    "s_mov_b32 s39, 3670016\n"
    "buffer_load_dwordx4 v112, s[80:83], s39 offen lds\n"
    "s_add_u32 m0, s38, 19008\n"
    "s_mov_b32 s39, 3702784\n"
    "buffer_load_dwordx4 v112, s[80:83], s39 offen lds\n"
    "s_add_u32 m0, s38, 20096\n"
    "s_mov_b32 s39, 3735552\n"
    "buffer_load_dwordx4 v112, s[80:83], s39 offen lds\n"
    "s_add_u32 m0, s38, 21184\n"
    "s_mov_b32 s39, 3768320\n"
    "buffer_load_dwordx4 v112, s[80:83], s39 offen lds\n"
    "v_add_u32 v116, 53760, v114\n"
    "ds_read_b64_tr_b16 a[0:1], v116 offset:0\n"
    "ds_read_b64_tr_b16 a[2:3], v116 offset:2176\n"
    "ds_read_b64_tr_b16 a[4:5], v116 offset:8960\n"
    "ds_read_b64_tr_b16 a[6:7], v116 offset:11136\n"
    "v_xor_b32 v116, 32, v116\n"
    "ds_read_b64_tr_b16 a[8:9], v116 offset:0\n"
    "ds_read_b64_tr_b16 a[10:11], v116 offset:2176\n"
    "ds_read_b64_tr_b16 a[12:13], v116 offset:8960\n"
    "ds_read_b64_tr_b16 a[14:15], v116 offset:11136\n"
    "s_waitcnt lgkmcnt(0)\n"
    "s_waitcnt vmcnt(12)\n"
    "s_barrier\n"
    "v_add_u32 v116, 71680, v114\n"
    "ds_read_b64_tr_b16 a[16:17], v116 offset:0\n"
    "ds_read_b64_tr_b16 a[18:19], v116 offset:2176\n"
    "ds_read_b64_tr_b16 a[20:21], v116 offset:8960\n"
    "ds_read_b64_tr_b16 a[22:23], v116 offset:11136\n"
    "v_xor_b32 v116, 32, v116\n"
    "ds_read_b64_tr_b16 a[24:25], v116 offset:0\n"
    "ds_read_b64_tr_b16 a[26:27], v116 offset:2176\n"
    "ds_read_b64_tr_b16 a[28:29], v116 offset:8960\n"
    "ds_read_b64_tr_b16 a[30:31], v116 offset:11136\n"
    "s_waitcnt lgkmcnt(0)\n"
    "s_waitcnt vmcnt(8)\n"
    "s_barrier\n"
    "v_add_u32 v116, 89600, v114\n"
    "ds_read_b64_tr_b16 a[32:33], v116 offset:0\n"
    "ds_read_b64_tr_b16 a[34:35], v116 offset:2176\n"
    "ds_read_b64_tr_b16 a[36:37], v116 offset:8960\n"
    "ds_read_b64_tr_b16 a[38:39], v116 offset:11136\n"
    "v_xor_b32 v116, 32, v116\n"
    "ds_read_b64_tr_b16 a[40:41], v116 offset:0\n"
    "ds_read_b64_tr_b16 a[42:43], v116 offset:2176\n"
    "ds_read_b64_tr_b16 a[44:45], v116 offset:8960\n"
    "ds_read_b64_tr_b16 a[46:47], v116 offset:11136\n"
    "s_waitcnt lgkmcnt(0)\n"
    "s_mov_b32 s37, 0x80000\n"
    "LRY%=:\n"
    "v_readfirstlane_b32 s44, v117\n"
    "s_cmp_lg_u32 s44, 0\n"
    "s_cbranch_scc1 LRZ%=\n"
    "s_add_u32 s42, s94, 4096\n"
    "s_addc_u32 s43, s95, 0\n"
    "global_load_dword v119, v118, s[42:43] sc1\n"
    "global_load_dword v117, v118, s[92:93] sc1\n"
    "s_waitcnt vmcnt(0)\n"
    "s_add_u32 s38, s52, 8192\n"
    "s_add_u32 s42, s94, s38\n"
    "s_addc_u32 s43, s95, 0\n"
    "global_store_dword v118, v119, s[42:43] sc1\n"
    "s_sleep 4\n"
    "s_sub_u32 s37, s37, 1\n"
    "s_cbranch_scc0 LRY%=\n"
    "s_branch LFAIL%=\n"
    "LRZ%=:\n"
    "s_lshl_b32 s38, s44, 13\n"
    "s_and_b32 s38, s38, 0x2000\n"
    "s_xor_b32 s46, s46, s38\n"
    "s_xor_b32 s47, s47, s38\n"
    "s_xor_b32 s48, s48, s38\n"
    "s_xor_b32 s56, s56, s38\n"
    "s_xor_b32 s74, s74, s38\n"
    "s_xor_b32 s58, s58, s38\n"
    "s_xor_b32 s59, s59, s38\n"
    "s_xor_b32 s67, s67, s38\n"
    "s_xor_b32 s69, s69, s38\n"
    "s_sub_u32 s58, s58, 4\n"
    "s_mov_b64 exec, 7\n"
    "buffer_load_dword v119, v0, s[16:19], s59 offen sc1\n"
    "s_mov_b64 exec, -1\n"
    "s_waitcnt vmcnt(5)\n"
    "s_barrier\n"
    "v_add_u32 v116, 0, v114\n"
    "ds_read_b64_tr_b16 a[48:49], v116 offset:0\n"
    "ds_read_b64_tr_b16 a[50:51], v116 offset:2176\n"
    "ds_read_b64_tr_b16 a[52:53], v116 offset:8960\n"
    "ds_read_b64_tr_b16 a[54:55], v116 offset:11136\n"
    "v_xor_b32 v116, 32, v116\n"
    "ds_read_b64_tr_b16 a[56:57], v116 offset:0\n"
    "ds_read_b64_tr_b16 a[58:59], v116 offset:2176\n"
    "ds_read_b64_tr_b16 a[60:61], v116 offset:8960\n"
    "ds_read_b64_tr_b16 a[62:63], v116 offset:11136\n"
    "s_waitcnt lgkmcnt(0)\n"
    "s_waitcnt vmcnt(1)\n"
    "s_barrier\n"
    "v_add_u32 v116, 17920, v114\n"
    "ds_read_b64_tr_b16 a[64:65], v116 offset:0\n"
    "ds_read_b64_tr_b16 a[66:67], v116 offset:2176\n"
    "ds_read_b64_tr_b16 a[68:69], v116 offset:8960\n"
    "ds_read_b64_tr_b16 a[70:71], v116 offset:11136\n"
    "v_xor_b32 v116, 32, v116\n"
    "ds_read_b64_tr_b16 a[72:73], v116 offset:0\n"
    "ds_read_b64_tr_b16 a[74:75], v116 offset:2176\n"
    "ds_read_b64_tr_b16 a[76:77], v116 offset:8960\n"
    "ds_read_b64_tr_b16 a[78:79], v116 offset:11136\n"
    "s_waitcnt lgkmcnt(0)\n"
    "s_barrier\n"
    "ds_write_b128 v7, v[16:19] offset:0\n"
    "ds_write_b128 v7, v[20:23] offset:1024\n"
    "ds_write_b128 v7, v[24:27] offset:2048\n"
    "ds_write_b128 v7, v[28:31] offset:3072\n"
    "ds_write_b128 v7, v[32:35] offset:4096\n"
    "ds_write_b128 v7, v[36:39] offset:5120\n"
    "ds_write_b128 v7, v[40:43] offset:6144\n"
    "ds_write_b128 v7, v[44:47] offset:7168\n"
    "ds_write_b128 v7, v[48:51] offset:8192\n"
    "ds_write_b128 v7, v[52:55] offset:9216\n"
    "ds_write_b128 v7, v[56:59] offset:10240\n"
    "ds_write_b128 v7, v[60:63] offset:11264\n"
    "ds_write_b128 v7, v[64:67] offset:12288\n"
    "ds_write_b128 v7, v[68:71] offset:13312\n"
    "ds_write_b128 v7, v[72:75] offset:14336\n"
    "s_waitcnt lgkmcnt(11)\n"
    "ds_write_b128 v7, v[76:79] offset:15360\n"
    "ds_write_b128 v7, v[80:83] offset:16384\n"
    "ds_write_b128 v7, v[84:87] offset:17408\n"
    "ds_write_b128 v7, v[88:91] offset:18432\n"
    "s_waitcnt lgkmcnt(11)\n"
    "ds_write_b128 v7, v[92:95] offset:19456\n"
    "ds_write_b128 v7, v[96:99] offset:20480\n"
    "ds_write_b128 v7, v[100:103] offset:21504\n"
    "ds_write_b128 v7, v[104:107] offset:22528\n"
    "s_waitcnt lgkmcnt(11)\n"
    "ds_write_b128 v7, v[108:111] offset:23552\n"
    "s_waitcnt lgkmcnt(0)\n"
    "ds_read_b128 v[16:19], v9 offset:0\n"
    "ds_read_b128 v[20:23], v9 offset:1024\n"
    "ds_read_b128 v[24:27], v9 offset:8192\n"
    "ds_read_b128 v[28:31], v9 offset:9216\n"
    "ds_read_b128 v[32:35], v9 offset:16384\n"
    "ds_read_b128 v[36:39], v9 offset:17408\n"
    "s_waitcnt lgkmcnt(5)\n"
    "ds_write_b128 v7, v[16:19] offset:24576\n"
    "s_waitcnt lgkmcnt(5)\n"
    "ds_write_b128 v7, v[20:23] offset:25600\n"
    "s_waitcnt lgkmcnt(5)\n"
    "ds_write_b128 v7, v[24:27] offset:26624\n"
    "s_waitcnt lgkmcnt(5)\n"
    "ds_write_b128 v7, v[28:31] offset:27648\n"
    "s_waitcnt lgkmcnt(5)\n"
    "ds_write_b128 v7, v[32:35] offset:28672\n"
    "s_waitcnt lgkmcnt(5)\n"
    "ds_write_b128 v7, v[36:39] offset:29696\n"
    "v_lshrrev_b32 v64, 4, v0\n"
    "v_lshrrev_b32 v65, 4, v64\n"
    "v_and_b32 v64, 15, v64\n"
    "v_lshlrev_b32 v65, 2, v65\n"
    "v_sub_u32 v64, v64, v65\n"
    "s_and_b32 s38, s61, 1\n"
    "s_lshl_b32 s38, s38, 4\n"
    "v_add_u32 v64, s38, v64\n"
    "v_cmp_le_i32 s[80:81], 0, v64\n"
    "v_cmp_le_i32 s[82:83], 1, v64\n"
    "v_cmp_le_i32 s[84:85], 2, v64\n"
    "v_cmp_le_i32 s[86:87], 3, v64\n"
    "v_cmp_le_i32 s[88:89], 16, v64\n"
    "v_cmp_le_i32 s[90:91], 17, v64\n"
    "v_cmp_le_i32 s[92:93], 18, v64\n"
    "v_cmp_le_i32 s[94:95], 19, v64\n"
    "v_mov_b32 v64, 158224\n"
    "v_mov_b64 v[66:67], 0\n"
    "ds_write_b64 v64, v[66:67] offset:16\n"
    "ds_write_b64 v64, v[66:67] offset:32\n"
    "ds_write_b64 v64, v[66:67] offset:40\n"
    "s_mov_b32 s54, 0x80000000\n"
    "s_waitcnt vmcnt(0)\n"
    "v_mov_b32 v116, v119\n"
    "v_cmp_ne_u32 vcc, 0, v116\n"
    "v_readfirstlane_b32 s70, v116\n"
    "s_nop 0\n"
    "s_andn2_b32 s38, 7, vcc_lo\n"
    "s_cbranch_scc0 LS0K%=\n"
    "s_mov_b32 s37, 0x80000\n"
    "LS0W%=:\n"
    "s_mov_b64 exec, 7\n"
    "buffer_load_dword v116, v0, s[16:19], s59 offen sc1\n"
    "s_mov_b64 exec, -1\n"
    "s_waitcnt vmcnt(0)\n"
    "v_cmp_ne_u32 vcc, 0, v116\n"
    "v_readfirstlane_b32 s70, v116\n"
    "s_nop 0\n"
    "s_andn2_b32 s38, 7, vcc_lo\n"
    "s_cbranch_scc0 LS0K%=\n"
    "s_sleep 2\n"
    "s_sub_u32 s37, s37, 1\n"
    "s_cbranch_scc0 LS0W%=\n"
    "s_branch LFAIL%=\n"
    "LS0K%=:\n"
    "s_add_u32 s53, s50, s55\n"
    "s_mov_b32 s49, s48\n"
    "s_add_u32 s51, s53, 0\n"
    "s_lshr_b32 s38, s51, 8\n"
    "s_xor_b32 s38, s38, s56\n"
    "s_and_b32 s38, s38, 0x2000\n"
    "s_or_b32 s51, s51, s38\n"
    "s_add_u32 s52, s53, 524288\n"
    "s_lshr_b32 s38, s52, 8\n"
    "s_xor_b32 s38, s38, s56\n"
    "s_and_b32 s38, s38, 0x2000\n"
    "s_or_b32 s52, s52, s38\n"
    "s_add_u32 s53, s53, 1048576\n"
    "s_lshr_b32 s38, s53, 8\n"
    "s_xor_b32 s38, s38, s56\n"
    "s_and_b32 s38, s38, 0x2000\n"
    "s_or_b32 s53, s53, s38\n"
    "s_add_u32 s59, s59, 128\n"
    "s_waitcnt lgkmcnt(0)\n"
    "s_barrier\n"
    "s_cmp_lg_u32 s61, 1\n"
    "s_cbranch_scc1 LPRE%=\n"
    "s_sub_u32 s51, s51, 2048\n"
    "s_mov_b32 m0, 122880\n"
    "s_nop 0\n"
    "buffer_load_dwordx4 v0, s[16:19], s51 offen offset:0 sc1 lds\n"
    "buffer_load_dwordx4 v0, s[16:19], s51 offen offset:1024 sc1 lds\n"
    "buffer_load_dwordx4 v0, s[16:19], s51 offen offset:2048 sc1 lds\n"
    "buffer_load_dwordx4 v0, s[16:19], s51 offen offset:3072 sc1 lds\n"
    "s_mov_b32 m0, 126976\n"
    "s_add_u32 s51, s51, 4096\n"
    "buffer_load_dwordx4 v0, s[16:19], s51 offen offset:0 sc1 lds\n"
    "buffer_load_dwordx4 v0, s[16:19], s51 offen offset:1024 sc1 lds\n"
    "buffer_load_dwordx4 v0, s[16:19], s51 offen offset:2048 sc1 lds\n"
    "buffer_load_dwordx4 v0, s[16:19], s51 offen offset:3072 sc1 lds\n"
    "s_memtime s[40:41]\n"
    "s_waitcnt lgkmcnt(0)\n"
    "s_sub_u32 s41, s40, s45\n"
    "s_sub_u32 s42, s13, 1\n"
    "s_and_b32 s41, s41, s42\n"
    "s_sub_u32 s44, s40, s41\n"
    "v_mov_b32 v112, 158220\n"
    "v_mov_b32 v113, s36\n"
    "v_mov_b32 v114, s44\n"
    "ds_write2_b32 v112, v113, v114 offset0:0 offset1:1\n"
    "s_waitcnt lgkmcnt(0)\n"
    "s_branch LPRE%=\n"
    "LBM%=:\n"
    "s_waitcnt lgkmcnt(0)\n"
    "s_barrier\n"
    "buffer_load_dwordx4 v0, s[16:19], s73 offen sc1 lds\n"
    "ds_read_b64 v[42:43], v41 offset:0\n"
    "ds_read_b128 v[32:35], v9 offset:16384\n"
    "ds_read_b128 v[36:39], v9 offset:17408\n"
    "s_waitcnt lgkmcnt(2)\n"
    "v_readfirstlane_b32 s39, v42\n"
    "v_readfirstlane_b32 s44, v43\n"
    "s_sub_u32 s38, s39, s44\n"
    "s_cmp_gt_i32 s38, 0\n"
    "s_cbranch_scc1 LSL%=\n"
    "s_add_u32 m0, s62, 131072\n"
    "s_nop 0\n"
    "buffer_load_dwordx4 v0, s[16:19], s52 offen offset:0 sc1 lds\n"
    "buffer_load_dwordx4 v0, s[16:19], s52 offen offset:1024 sc1 lds\n"
    "s_waitcnt lgkmcnt(0)\n"
    "s_cmp_lg_u32 s61, 1\n"
    "s_cbranch_scc1 LC1%=\n"
    "s_waitcnt vmcnt(3)\n"
    "LC1%=:\n"
    "s_barrier\n"
    "ds_read_b128 v[16:19], v2 offset:0\n"
    "ds_read_b128 v[20:23], v2 offset:1024\n"
    "ds_read_b128 v[24:27], v2 offset:2048\n"
    "s_add_u32 m0, s62, 139264\n"
    "s_nop 0\n"
    "buffer_load_dwordx4 v0, s[16:19], s53 offen offset:0 sc1 lds\n"
    "buffer_load_dwordx4 v0, s[16:19], s53 offen offset:1024 sc1 lds\n"
    "s_waitcnt lgkmcnt(2)\n"
    "v_smfmac_f32_16x16x64_bf16 v[48:51], v[16:19], v[64:71], v1\n"
    "ds_read_b128 v[28:31], v2 offset:3072\n"
    "v_smfmac_f32_16x16x64_bf16 v[52:55], v[16:19], v[72:79], v1\n"
    "s_waitcnt lgkmcnt(2)\n"
    "v_smfmac_f32_16x16x64_bf16 v[48:51], v[20:23], v[80:87], v1\n"
    "v_smfmac_f32_16x16x64_bf16 v[52:55], v[20:23], v[88:95], v1\n"
    "s_waitcnt lgkmcnt(1)\n"
    "v_smfmac_f32_16x16x64_bf16 v[48:51], v[24:27], v[96:103], v1\n"
    "ds_read_b128 v[64:67], v7 offset:12288\n"
    "v_smfmac_f32_16x16x64_bf16 v[52:55], v[24:27], v[104:111], v1\n"
    "ds_read_b128 v[68:71], v7 offset:13312\n"
    "s_waitcnt lgkmcnt(2)\n"
    "v_smfmac_f32_16x16x64_bf16 v[48:51], v[28:31], a[0:7], v1\n"
    "ds_read_b128 v[72:75], v7 offset:14336\n"
    "v_smfmac_f32_16x16x64_bf16 v[52:55], v[28:31], a[8:15], v1\n"
    "ds_read_b128 v[76:79], v7 offset:15360\n"
    "v_smfmac_f32_16x16x64_bf16 v[112:115], v[16:19], a[176:183], v1\n"
    "ds_read_b128 v[80:83], v7 offset:16384\n"
    "buffer_store_dwordx4 v[32:35], v40, s[16:19], s54 offen offset:0 sc1\n"
    "v_smfmac_f32_16x16x64_bf16 v[116:119], v[16:19], a[184:191], v1\n"
    "ds_read_b128 v[84:87], v7 offset:17408\n"
    "v_smfmac_f32_16x16x64_bf16 v[112:115], v[20:23], a[192:199], v1\n"
    "ds_read_b128 v[88:91], v7 offset:18432\n"
    "v_smfmac_f32_16x16x64_bf16 v[116:119], v[20:23], a[200:207], v1\n"
    "ds_read_b128 v[92:95], v7 offset:19456\n"
    "v_smfmac_f32_16x16x64_bf16 v[56:59], v[24:27], a[80:87], v1\n"
    "v_smfmac_f32_16x16x64_bf16 v[60:63], v[24:27], a[88:95], v1\n"
    "v_max3_u32 v111, v19, v23, v27\n"
    "buffer_store_dwordx4 v[36:39], v40, s[16:19], s54 offen offset:2048 sc1\n"
    "v_smfmac_f32_16x16x64_bf16 v[56:59], v[28:31], a[96:103], v1\n"
    "v_smfmac_f32_16x16x64_bf16 v[60:63], v[28:31], a[104:111], v1\n"
    "s_waitcnt vmcnt(6)\n"
    "s_barrier\n"
    "ds_read_b128 v[32:35], v2 offset:4096\n"
    "s_waitcnt lgkmcnt(7)\n"
    "v_smfmac_f32_16x16x64_bf16 v[56:59], v[16:19], v[64:71], v1\n"
    "ds_read_b128 v[36:39], v2 offset:5120\n"
    "s_waitcnt lgkmcnt(6)\n"
    "v_smfmac_f32_16x16x64_bf16 v[60:63], v[16:19], v[72:79], v1\n"
    "ds_read_b128 v[40:43], v2 offset:6144\n"
    "s_waitcnt lgkmcnt(5)\n"
    "v_smfmac_f32_16x16x64_bf16 v[56:59], v[20:23], v[80:87], v1\n"
    "ds_read_b128 v[44:47], v2 offset:7168\n"
    "s_waitcnt lgkmcnt(4)\n"
    "v_smfmac_f32_16x16x64_bf16 v[60:63], v[20:23], v[88:95], v1\n"
    "s_waitcnt lgkmcnt(3)\n"
    "v_smfmac_f32_16x16x64_bf16 v[48:51], v[32:35], a[16:23], v1\n"
    "v_max3_u32 v111, v111, v31, v35\n"
    "v_smfmac_f32_16x16x64_bf16 v[52:55], v[32:35], a[24:31], v1\n"
    "ds_read_b128 v[88:91], v9 offset:0\n"
    "s_waitcnt lgkmcnt(3)\n"
    "v_smfmac_f32_16x16x64_bf16 v[48:51], v[36:39], a[32:39], v1\n"
    "v_add_u32 v21, s20, v5\n"
    "v_smfmac_f32_16x16x64_bf16 v[52:55], v[36:39], a[40:47], v1\n"
    "ds_read_b128 v[92:95], v9 offset:1024\n"
    "s_waitcnt lgkmcnt(3)\n"
    "v_smfmac_f32_16x16x64_bf16 v[48:51], v[40:43], a[48:55], v1\n"
    "s_waitcnt lgkmcnt(2)\n"
    "v_max3_u32 v111, v111, v39, v43\n"
    "v_smfmac_f32_16x16x64_bf16 v[48:51], v[44:47], a[64:71], v1\n"
    "v_max_u32 v111, v111, v47\n"
    "v_smfmac_f32_16x16x64_bf16 v[52:55], v[40:43], a[56:63], v1\n"
    "v_cmp_eq_u32 vcc, -1, v111\n"
    "v_smfmac_f32_16x16x64_bf16 v[52:55], v[44:47], a[72:79], v1\n"
    "v_add_f32 v48, v48, v50\n"
    "v_smfmac_f32_16x16x64_bf16 v[56:59], v[32:35], a[112:119], v1\n"
    "v_add_f32 v49, v49, v51\n"
    "v_smfmac_f32_16x16x64_bf16 v[60:63], v[32:35], a[120:127], v1\n"
    "v_add_f32 v52, v52, v54\n"
    "v_add_f32 v53, v53, v55\n"
    "v_cvt_pk_bf16_f32 v48, v48, v52\n"
    "v_cvt_pk_bf16_f32 v49, v49, v53\n"
    "v_smfmac_f32_16x16x64_bf16 v[56:59], v[36:39], a[128:135], v1\n"
    "ds_write2_b32 v3, v48, v49 offset0:0 offset1:72\n"
    "v_smfmac_f32_16x16x64_bf16 v[60:63], v[36:39], a[136:143], v1\n"
    "s_cbranch_vccnz LMR%=\n"
    "v_smfmac_f32_16x16x64_bf16 v[56:59], v[40:43], a[144:151], v1\n"
    "v_mov_b32 v53, 0\n"
    "v_smfmac_f32_16x16x64_bf16 v[56:59], v[44:47], a[160:167], v1\n"
    "v_mov_b32 v54, 0\n"
    "v_smfmac_f32_16x16x64_bf16 v[60:63], v[40:43], a[152:159], v1\n"
    "s_waitcnt lgkmcnt(0)\n"
    "s_barrier\n"
    "ds_read_b128 v[80:83], v21 offset:0\n"
    "v_smfmac_f32_16x16x64_bf16 v[60:63], v[44:47], a[168:175], v1\n"
    "ds_read_b128 v[84:87], v21 offset:64\n"
    "v_add_f32 v56, v56, v58\n"
    "v_smfmac_f32_16x16x64_bf16 v[112:115], v[24:27], a[208:215], v1\n"
    "ds_read_b128 v[96:99], v5 offset:0\n"
    "v_add_f32 v57, v57, v59\n"
    "v_smfmac_f32_16x16x64_bf16 v[112:115], v[28:31], a[224:231], v1\n"
    "ds_read_b128 v[100:103], v5 offset:64\n"
    "s_waitcnt lgkmcnt(3)\n"
    "v_add_f32 v60, v60, v62\n"
    "v_mfma_f32_16x16x32_bf16 v[64:67], v[88:91], v[80:83], 0\n"
    "ds_read_b128 v[104:107], v5 offset:128\n"
    "s_waitcnt lgkmcnt(3)\n"
    "v_add_f32 v61, v61, v63\n"
    "v_cvt_pk_bf16_f32 v56, v56, v60\n"
    "v_mfma_f32_16x16x32_bf16 v[64:67], v[92:95], v[84:87], v[64:67]\n"
    "ds_read_b128 v[108:111], v5 offset:192\n"
    "v_cvt_pk_bf16_f32 v57, v57, v61\n"
    "v_mov_b64 v[50:51], 0\n"
    "v_smfmac_f32_16x16x64_bf16 v[112:115], v[32:35], a[240:247], v1\n"
    "ds_write2_b32 v4, v56, v57 offset0:0 offset1:72\n"
    "v_smfmac_f32_16x16x64_bf16 v[112:115], v[36:39], v[120:127], v1\n"
    "ds_write_b128 v8, v[64:67]\n"
    "v_smfmac_f32_16x16x64_bf16 v[112:115], v[40:43], v[136:143], v1\n"
    "v_mov_b64 v[60:61], s[14:15]\n"
    "v_smfmac_f32_16x16x64_bf16 v[112:115], v[44:47], v[152:159], v1\n"
    "v_mov_b64 v[62:63], s[14:15]\n"
    "v_smfmac_f32_16x16x64_bf16 v[116:119], v[24:27], a[216:223], v1\n"
    "v_smfmac_f32_16x16x64_bf16 v[116:119], v[28:31], a[232:239], v1\n"
    "s_waitcnt lgkmcnt(0)\n"
    "s_barrier\n"
    "ds_read_b128 v[64:67], v6 offset:0\n"
    "ds_read_b128 v[88:91], v15 offset:0\n"
    "v_smfmac_f32_16x16x64_bf16 v[116:119], v[32:35], a[248:255], v1\n"
    "ds_read_b128 v[92:95], v15 offset:512\n"
    "ds_read_b128 v[68:71], v6 offset:64\n"
    "v_smfmac_f32_16x16x64_bf16 v[116:119], v[36:39], v[128:135], v1\n"
    "ds_read_b128 v[72:75], v6 offset:128\n"
    "v_add_f32 v112, v112, v114\n"
    "v_smfmac_f32_16x16x64_bf16 v[116:119], v[40:43], v[144:151], v1\n"
    "ds_read_b128 v[76:79], v6 offset:192\n"
    "s_waitcnt lgkmcnt(5)\n"
    "v_add_f32 v113, v113, v115\n"
    "v_mfma_f32_16x16x32_bf16 v[16:19], v[64:67], v[96:99], 0\n"
    "s_waitcnt lgkmcnt(3)\n"
    "s_waitcnt lgkmcnt(2)\n"
    "v_add_f32 v88, v88, v92\n"
    "v_add_f32 v89, v89, v93\n"
    "v_mfma_f32_16x16x32_bf16 v[16:19], v[68:71], v[100:103], v[16:19]\n"
    "s_waitcnt lgkmcnt(1)\n"
    "v_add_f32 v90, v90, v94\n"
    "v_add_f32 v91, v91, v95\n"
    "v_mfma_f32_16x16x32_bf16 v[16:19], v[72:75], v[104:107], v[16:19]\n"
    "s_waitcnt lgkmcnt(0)\n"
    "v_max3_f32 v21, v88, v89, v90\n"
    "v_max_f32 v21, v21, v91\n"
    "v_mfma_f32_16x16x32_bf16 v[16:19], v[76:79], v[108:111], v[16:19]\n"
    "s_waitcnt vmcnt(4)\n"
    "ds_read_b128 v[80:83], v9 offset:8192\n"
    "v_max_f32_dpp v20, v21, v21 row_ror:8 row_mask:0xf bank_mask:0xf\n"
    "v_mov_b32 v21, v20\n"
    "v_smfmac_f32_16x16x64_bf16 v[116:119], v[44:47], v[160:167], v1\n"
    "ds_read_b128 v[84:87], v9 offset:9216\n"
    "v_permlane32_swap_b32 v20, v21\n"
    "v_max_f32 v20, v20, v21\n"
    "v_cvt_pk_bf16_f32 v112, v112, v113\n"
    "v_max3_f32 v21, v16, v17, v20\n"
    "v_max3_f32 v21, v18, v19, v21\n"
    "v_mov_b32 v22, v21\n"
    "s_nop 0\n"
    "v_add_f32 v116, v116, v118\n"
    "v_add_f32 v117, v117, v119\n"
    "v_permlane16_swap_b32 v21, v22\n"
    "v_max_f32 v21, v21, v22\n"
    "v_mul_f32 v21, 0xbe0293ee, v21\n"
    "v_fma_f32 v88, v88, s65, v21\n"
    "v_fma_f32 v89, v89, s65, v21\n"
    "v_fma_f32 v90, v90, s65, v21\n"
    "v_fma_f32 v91, v91, s65, v21\n"
    "v_fma_f32 v16, v16, s65, v21\n"
    "v_fma_f32 v17, v17, s65, v21\n"
    "v_exp_f32 v16, v16\n"
    "v_exp_f32 v17, v17\n"
    "v_exp_f32 v88, v88\n"
    "v_exp_f32 v89, v89\n"
    "v_cvt_pk_bf16_f32 v52, v16, v17\n"
    "v_cvt_pk_bf16_f32 v113, v116, v117\n"
    "v_cvt_pk_bf16_f32 v55, v16, v17\n"
    "v_cvt_pk_bf16_f32 v56, v88, v89\n"
    "v_mfma_f32_16x16x16_bf16 v[104:107], v[60:61], v[52:53], 0\n"
    "v_exp_f32 v90, v90\n"
    "v_mfma_f32_16x16x16_bf16 v[96:99], v[112:113], v[52:53], 0\n"
    "v_exp_f32 v91, v91\n"
    "v_mfma_f32_16x16x16_bf16 v[100:103], v[112:113], v[54:55], 0\n"
    "v_cvt_pk_bf16_f32 v57, v90, v91\n"
    "v_mov_b32_dpp v58, v56 row_shl:8 row_mask:0xf bank_mask:0xf bound_ctrl:1\n"
    "s_nop 0\n"
    "v_mov_b32_dpp v59, v57 row_shl:8 row_mask:0xf bank_mask:0xf bound_ctrl:1\n"
    "s_nop 1\n"
    "v_mfma_f32_16x16x32_bf16 v[104:107], v[60:63], v[56:59], v[104:107]\n"
    "ds_write_b64 v10, v[112:113]\n"
    "s_waitcnt lgkmcnt(2)\n"
    "v_mfma_f32_16x16x32_bf16 v[96:99], v[80:83], v[56:59], v[96:99]\n"
    "s_waitcnt lgkmcnt(1)\n"
    "v_mfma_f32_16x16x32_bf16 v[100:103], v[84:87], v[56:59], v[100:103]\n"
    "v_rcp_f32 v104, v104\n"
    "s_mov_b64 exec, s[78:79]\n"
    "s_nop 1\n"
    "v_pk_mul_f32 v[96:97], v[96:97], v[104:105] op_sel_hi:[1,0]\n"
    "v_pk_mul_f32 v[98:99], v[98:99], v[104:105] op_sel_hi:[1,0]\n"
    "v_cvt_pk_bf16_f32 v96, v96, v97\n"
    "v_cvt_pk_bf16_f32 v97, v98, v99\n"
    "ds_write_b64 v11, v[96:97] offset:0\n"
    "v_pk_mul_f32 v[100:101], v[100:101], v[104:105] op_sel_hi:[1,0]\n"
    "v_pk_mul_f32 v[102:103], v[102:103], v[104:105] op_sel_hi:[1,0]\n"
    "v_cvt_pk_bf16_f32 v98, v100, v101\n"
    "v_cvt_pk_bf16_f32 v99, v102, v103\n"
    "ds_write_b64 v11, v[98:99] offset:8\n"
    "s_mov_b64 exec, 7\n"
    "buffer_load_dword v116, v0, s[16:19], s59 offen sc1\n"
    "s_mov_b64 exec, -1\n"
    "v_mfma_f32_32x32x8_bf16 v[72:87], v[50:51], v[50:51], 0\n"
    "s_add_u32 s36, s36, 1\n"
    "s_cmp_eq_u32 s36, 64\n"
    "s_waitcnt lgkmcnt(0)\n"
    "s_waitcnt vmcnt(3)\n"
    "s_barrier\n"
    "ds_read_b128 v[104:107], v12 offset:0\n"
    "ds_read_b128 v[108:111], v12 offset:1024\n"
    "s_nop 4\n"
    "v_mfma_f32_32x32x8_bf16 v[48:63], v[72:73], v[72:73], 0\n"
    "s_cbranch_scc1 LEND%=\n"
    "s_waitcnt lgkmcnt(1)\n"
    "v_smfmac_f32_16x16x64_bf16 v[72:75], v[104:107], v[168:175], v1\n"
    "ds_read_b128 v[64:67], v7 offset:20480\n"
    "s_waitcnt lgkmcnt(1)\n"
    "v_smfmac_f32_16x16x64_bf16 v[72:75], v[108:111], v[176:183], v1\n"
    "ds_read_b128 v[68:71], v7 offset:21504\n"
    "v_smfmac_f32_16x16x64_bf16 v[76:79], v[104:107], v[184:191], v1\n"
    "ds_read_b128 v[16:19], v7 offset:22528\n"
    "v_smfmac_f32_16x16x64_bf16 v[76:79], v[108:111], v[192:199], v1\n"
    "ds_read_b128 v[20:23], v7 offset:23552\n"
    "v_add_f32 v72, v72, v74\n"
    "v_smfmac_f32_16x16x64_bf16 v[80:83], v[104:107], v[200:207], v1\n"
    "ds_read_b128 v[24:27], v7 offset:24576\n"
    "v_add_f32 v73, v73, v75\n"
    "v_smfmac_f32_16x16x64_bf16 v[80:83], v[108:111], v[208:215], v1\n"
    "ds_read_b128 v[28:31], v7 offset:25600\n"
    "v_add_f32 v76, v76, v78\n"
    "v_smfmac_f32_16x16x64_bf16 v[84:87], v[104:107], v[216:223], v1\n"
    "ds_read_b128 v[32:35], v7 offset:26624\n"
    "v_add_f32 v77, v77, v79\n"
    "v_smfmac_f32_16x16x64_bf16 v[84:87], v[108:111], v[224:231], v1\n"
    "ds_read_b128 v[36:39], v7 offset:27648\n"
    "v_cvt_pk_bf16_f32 v88, v72, v76\n"
    "v_cvt_pk_bf16_f32 v90, v73, v77\n"
    "v_smfmac_f32_16x16x64_bf16 v[48:51], v[104:107], v[232:239], v1\n"
    "ds_read_b128 v[40:43], v7 offset:28672\n"
    "v_add_f32 v80, v80, v82\n"
    "v_add_f32 v81, v81, v83\n"
    "v_smfmac_f32_16x16x64_bf16 v[48:51], v[108:111], v[240:247], v1\n"
    "ds_read_b128 v[44:47], v7 offset:29696\n"
    "v_add_f32 v85, v85, v87\n"
    "v_add_f32 v84, v84, v86\n"
    "v_cvt_pk_bf16_f32 v91, v81, v85\n"
    "v_cvt_pk_bf16_f32 v89, v80, v84\n"
    "v_and_or_b32 v91, v91, -2, s64\n"
    "buffer_store_dwordx4 v[88:91], v13, s[16:19], s46 offen sc1\n"
    "v_smfmac_f32_16x16x64_bf16 v[52:55], v[104:107], v[248:255], v1\n"
    "s_waitcnt lgkmcnt(8)\n"
    "v_smfmac_f32_16x16x64_bf16 v[52:55], v[108:111], v[64:71], v1\n"
    "s_waitcnt lgkmcnt(6)\n"
    "v_add_f32 v48, v48, v50\n"
    "v_smfmac_f32_16x16x64_bf16 v[56:59], v[104:107], v[16:23], v1\n"
    "s_waitcnt lgkmcnt(4)\n"
    "v_add_f32 v49, v49, v51\n"
    "v_smfmac_f32_16x16x64_bf16 v[56:59], v[108:111], v[24:31], v1\n"
    "s_waitcnt lgkmcnt(2)\n"
    "v_add_f32 v52, v52, v54\n"
    "v_add_f32 v53, v53, v55\n"
    "v_smfmac_f32_16x16x64_bf16 v[60:63], v[104:107], v[32:39], v1\n"
    "s_waitcnt lgkmcnt(0)\n"
    "v_cvt_pk_bf16_f32 v92, v48, v52\n"
    "v_cvt_pk_bf16_f32 v94, v49, v53\n"
    "v_smfmac_f32_16x16x64_bf16 v[60:63], v[108:111], v[40:47], v1\n"
    "v_add_f32 v56, v56, v58\n"
    "v_add_f32 v57, v57, v59\n"
    "s_nop 5\n"
    "v_add_f32 v61, v61, v63\n"
    "v_add_f32 v60, v60, v62\n"
    "v_cvt_pk_bf16_f32 v95, v57, v61\n"
    "v_cvt_pk_bf16_f32 v93, v56, v60\n"
    "v_and_or_b32 v95, v95, -2, s64\n"
    "buffer_store_dwordx4 v[92:95], v13, s[16:19], s47 offen sc1\n"
    "s_waitcnt vmcnt(3)\n"
    "v_mov_b32 v118, s72\n"
    "v_mov_b32 v119, s36\n"
    "ds_write_b32 v118, v119 offset:48\n"
    "s_add_u32 s54, s50, s57\n"
    "s_lshr_b32 s38, s54, 8\n"
    "s_xor_b32 s38, s38, s74\n"
    "s_and_b32 s38, s38, 0x2000\n"
    "s_or_b32 s54, s54, s38\n"
    "s_add_u32 s50, s50, 2621440\n"
    "s_add_u32 s48, s48, 16384\n"
    "s_cmp_eq_u32 s61, 1\n"
    "s_cbranch_scc1 LW1%=\n"
    "s_cmp_gt_u32 s61, 1\n"
    "s_cbranch_scc1 LW2%=\n"
    "s_add_u32 s73, s46, s63\n"
    "s_add_u32 s66, s73, 4096\n"
    "s_add_u32 s39, s44, s9\n"
    "s_waitcnt lgkmcnt(0)\n"
    "LUG%=:\n"
    "s_memtime s[40:41]\n"
    "s_waitcnt lgkmcnt(0)\n"
    "s_sub_u32 s40, s39, s40\n"
    "s_sub_u32 s41, s40, 253\n"
    "s_cmp_lt_u32 s41, 16384\n"
    "s_cbranch_scc1 LUG%=\n"
    "s_cmp_lt_u32 s40, 253\n"
    "s_cselect_b32 s40, s40, 0\n"
    "s_and_b32 s40, s40, 0xfc\n"
    "s_getpc_b64 s[42:43]\n"
    "s_sub_u32 s40, 272, s40\n"
    "s_add_u32 s42, s42, s40\n"
    "s_addc_u32 s43, s43, 0\n"
    "s_setpc_b64 s[42:43]\n"
    ".rept 63\n"
    "s_nop 0\n"
    ".endr\n"
    "buffer_load_dwordx4 v[16:19], v0, s[16:19], s73 offen offset:0 sc1\n"
    "buffer_load_dwordx4 v[20:23], v0, s[16:19], s73 offen offset:1024 sc1\n"
    "buffer_load_dwordx4 v[24:27], v0, s[16:19], s73 offen offset:2048 sc1\n"
    "buffer_load_dwordx4 v[28:31], v0, s[16:19], s73 offen offset:3072 sc1\n"
    "buffer_load_dwordx4 v[32:35], v0, s[16:19], s66 offen offset:0 sc1\n"
    "buffer_load_dwordx4 v[36:39], v0, s[16:19], s66 offen offset:1024 sc1\n"
    "buffer_load_dwordx4 v[40:43], v0, s[16:19], s66 offen offset:2048 sc1\n"
    "buffer_load_dwordx4 v[44:47], v0, s[16:19], s66 offen offset:3072 sc1\n"
    "ds_read_b128 v[48:51], v9 offset:35584\n"
    "ds_read_b128 v[64:67], v7 offset:0\n"
    "ds_read_b128 v[68:71], v7 offset:1024\n"
    "ds_read_b128 v[72:75], v7 offset:2048\n"
    "ds_read_b128 v[76:79], v7 offset:3072\n"
    "ds_read_b128 v[80:83], v7 offset:4096\n"
    "ds_read_b128 v[84:87], v7 offset:5120\n"
    "ds_read_b128 v[88:91], v7 offset:6144\n"
    "ds_read_b128 v[92:95], v7 offset:7168\n"
    "ds_read_b128 v[96:99], v7 offset:8192\n"
    "ds_read_b128 v[100:103], v7 offset:9216\n"
    "ds_read_b128 v[104:107], v7 offset:10240\n"
    "ds_read_b128 v[108:111], v7 offset:11264\n"
    "s_add_u32 s53, s50, s55\n"
    "s_mov_b32 s49, s48\n"
    "s_add_u32 s51, s53, 0\n"
    "s_lshr_b32 s38, s51, 8\n"
    "s_xor_b32 s38, s38, s56\n"
    "s_and_b32 s38, s38, 0x2000\n"
    "s_or_b32 s51, s51, s38\n"
    "s_add_u32 s52, s53, 524288\n"
    "s_lshr_b32 s38, s52, 8\n"
    "s_xor_b32 s38, s38, s56\n"
    "s_and_b32 s38, s38, 0x2000\n"
    "s_or_b32 s52, s52, s38\n"
    "s_add_u32 s53, s53, 1048576\n"
    "s_lshr_b32 s38, s53, 8\n"
    "s_xor_b32 s38, s38, s56\n"
    "s_and_b32 s38, s38, 0x2000\n"
    "s_or_b32 s53, s53, s38\n"
    "s_waitcnt lgkmcnt(12)\n"
    "s_waitcnt vmcnt(7)\n"
    "v_mfma_f32_16x16x32_bf16 v[52:55], v[48:51], v[16:19], 0\n"
    "s_waitcnt vmcnt(6)\n"
    "v_mfma_f32_16x16x32_bf16 v[52:55], v[48:51], v[20:23], v[52:55]\n"
    "s_waitcnt vmcnt(5)\n"
    "v_bitop3_b32 v60, v19, v23, s64 bitop3:0x7e\n"
    "v_mfma_f32_16x16x32_bf16 v[52:55], v[48:51], v[24:27], v[52:55]\n"
    "s_waitcnt vmcnt(4)\n"
    "v_mfma_f32_16x16x32_bf16 v[52:55], v[48:51], v[28:31], v[52:55]\n"
    "s_waitcnt vmcnt(3)\n"
    "v_bitop3_b32 v61, v27, v31, s64 bitop3:0x7e\n"
    "v_mfma_f32_16x16x32_bf16 v[56:59], v[48:51], v[32:35], 0\n"
    "s_waitcnt vmcnt(2)\n"
    "v_mfma_f32_16x16x32_bf16 v[56:59], v[48:51], v[36:39], v[56:59]\n"
    "v_bitop3_b32 v62, v35, v39, s64 bitop3:0x7e\n"
    "v_or3_b32 v60, v60, v61, v62\n"
    "v_add_f32_dpp v52, v52, v52 row_ror:8 row_mask:0xf bank_mask:0xf\n"
    "v_add_f32_dpp v53, v53, v53 row_ror:8 row_mask:0xf bank_mask:0xf\n"
    "v_add_f32_dpp v54, v54, v54 row_ror:8 row_mask:0xf bank_mask:0xf\n"
    "v_add_f32_dpp v55, v55, v55 row_ror:8 row_mask:0xf bank_mask:0xf\n"
    "v_cvt_pk_bf16_f32 v113, v54, v55\n"
    "v_cvt_pk_bf16_f32 v112, v52, v53\n"
    "s_waitcnt vmcnt(1)\n"
    "v_mfma_f32_16x16x32_bf16 v[56:59], v[48:51], v[40:43], v[56:59]\n"
    "s_waitcnt vmcnt(0)\n"
    "v_mfma_f32_16x16x32_bf16 v[56:59], v[48:51], v[44:47], v[56:59]\n"
    "v_bitop3_b32 v63, v43, v47, s64 bitop3:0x7e\n"
    "v_or_b32 v60, v60, v63\n"
    "v_and_b32 v61, 1, v60\n"
    "v_cmp_ne_u32 vcc, 0, v61\n"
    "s_cbranch_vccnz LGR%=\n"
    "s_nop 2\n"
    "v_add_f32_dpp v56, v56, v56 row_ror:8 row_mask:0xf bank_mask:0xf\n"
    "v_add_f32_dpp v57, v57, v57 row_ror:8 row_mask:0xf bank_mask:0xf\n"
    "v_add_f32_dpp v58, v58, v58 row_ror:8 row_mask:0xf bank_mask:0xf\n"
    "v_add_f32_dpp v59, v59, v59 row_ror:8 row_mask:0xf bank_mask:0xf\n"
    "v_cvt_pk_bf16_f32 v115, v58, v59\n"
    "v_cvt_pk_bf16_f32 v114, v56, v57\n"
    "v_min_u32 v115, -2, v115\n"
    "buffer_store_dwordx4 v[112:115], v14, s[16:19], s48 offen sc1\n"
    "ds_write_b32 v118, v119 offset:12\n"
    "s_xor_b32 s46, s46, 0x80000\n"
    "s_add_u32 s47, s46, 0x40000\n"
    "s_bfe_u32 s64, s36, 0x10001\n"
    "s_add_u32 s44, s44, s8\n"
    "s_add_u32 s59, s59, 128\n"
    "v_mov_b64 v[112:113], 0\n"
    "v_mov_b64 v[114:115], 0\n"
    "v_mov_b64 v[116:117], 0\n"
    "v_mov_b64 v[118:119], 0\n"
    "v_mfma_f32_32x32x8_bf16 v[48:63], v[112:113], v[112:113], 0\n"
    "v_lshrrev_b32 v40, 4, v0\n"
    "v_and_b32 v40, 0x30, v40\n"
    "v_lshl_add_u32 v40, v40, 4, v0\n"
    "v_mov_b32 v41, 158224\n"
    "s_add_u32 s73, s49, s11\n"
    "s_add_u32 s73, s73, 4096\n"
    "s_add_u32 m0, s11, 151552\n"
    "s_branch LBM%=\n"
    "LGR%=:\n"
    "s_add_u32 s33, s33, 1\n"
    "s_cmp_gt_u32 s33, 0x40000\n"
    "s_cbranch_scc1 LFAIL%=\n"
    "s_add_u32 s44, s44, s13\n"
    "LGM%=:\n"
    "s_memtime s[40:41]\n"
    "s_add_u32 s42, s21, s9\n"
    "s_waitcnt lgkmcnt(0)\n"
    "s_sub_u32 s40, s40, s42\n"
    "s_sub_u32 s41, s44, s40\n"
    "s_cmp_ge_i32 s41, 0\n"
    "s_cbranch_scc1 LGMD%=\n"
    "LGMI%=:\n"
    "s_add_u32 s44, s44, s13\n"
    "s_add_u32 s41, s41, s13\n"
    "s_cbranch_scc0 LGMI%=\n"
    "s_branch LGM%=\n"
    "LGMD%=:\n"
    "s_add_u32 s39, s44, s9\n"
    "s_add_u32 s38, s44, s8\n"
    "v_mov_b32 v60, s72\n"
    "v_mov_b32 v61, s38\n"
    "ds_write_b32 v60, v61 offset:16\n"
    "s_branch LUG%=\n"
    "LMR%=:\n"
    "s_add_u32 s33, s33, 1\n"
    "s_cmp_gt_u32 s33, 0x40000\n"
    "s_cbranch_scc1 LFAIL%=\n"
    "s_add_u32 s39, s44, s13\n"
    "s_mov_b32 s54, 0x80000000\n"
    "LSL%=:\n"
    "s_mov_b32 s44, s39\n"
    "s_waitcnt lgkmcnt(0)\n"
    "s_barrier\n"
    "s_branch LPRE%=\n"
    "LW1%=:\n"
    "v_mov_b32 v64, 158240\n"
    "s_mov_b32 s37, 0x80000\n"
    "LOW%=:\n"
    "ds_read_b128 v[68:71], v64 offset:16\n"
    "s_waitcnt lgkmcnt(0)\n"
    "v_min3_u32 v68, v68, v70, v71\n"
    "v_cmp_eq_u32 vcc, s36, v68\n"
    "s_cbranch_vccnz LOK%=\n"
    "s_sleep 0\n"
    "s_sub_u32 s37, s37, 1\n"
    "s_cbranch_scc0 LOW%=\n"
    "s_branch LFAIL%=\n"
    "LOK%=:\n"
    "buffer_store_dword v1, off, s[16:19], s58 sc1\n"
    "s_add_u32 s58, s58, 4\n"
    "s_bitset0_b32 s58, 31\n"
    "s_waitcnt vmcnt(3)\n"
    "v_cmp_ne_u32 vcc, 0, v116\n"
    "v_readfirstlane_b32 s70, v116\n"
    "s_nop 0\n"
    "s_andn2_b32 s38, 7, vcc_lo\n"
    "s_cbranch_scc0 LS2K%=\n"
    "s_mov_b32 s37, 0x80000\n"
    "LS2W%=:\n"
    "s_mov_b64 exec, 7\n"
    "buffer_load_dword v116, v0, s[16:19], s59 offen sc1\n"
    "s_mov_b64 exec, -1\n"
    "s_waitcnt vmcnt(0)\n"
    "v_cmp_ne_u32 vcc, 0, v116\n"
    "v_readfirstlane_b32 s70, v116\n"
    "s_nop 0\n"
    "s_andn2_b32 s38, 7, vcc_lo\n"
    "s_cbranch_scc0 LS2K%=\n"
    "s_sleep 2\n"
    "s_sub_u32 s37, s37, 1\n"
    "s_cbranch_scc0 LS2W%=\n"
    "s_branch LFAIL%=\n"
    "LS2K%=:\n"
    "s_add_u32 s53, s50, s55\n"
    "s_mov_b32 s49, s48\n"
    "s_add_u32 s51, s53, 0\n"
    "s_lshr_b32 s38, s51, 8\n"
    "s_xor_b32 s38, s38, s56\n"
    "s_and_b32 s38, s38, 0x2000\n"
    "s_or_b32 s51, s51, s38\n"
    "s_add_u32 s52, s53, 524288\n"
    "s_lshr_b32 s38, s52, 8\n"
    "s_xor_b32 s38, s38, s56\n"
    "s_and_b32 s38, s38, 0x2000\n"
    "s_or_b32 s52, s52, s38\n"
    "s_add_u32 s53, s53, 1048576\n"
    "s_lshr_b32 s38, s53, 8\n"
    "s_xor_b32 s38, s38, s56\n"
    "s_and_b32 s38, s38, 0x2000\n"
    "s_or_b32 s53, s53, s38\n"
    "s_add_u32 s66, s44, s12\n"
    "s_xor_b32 s46, s46, 0x80000\n"
    "s_add_u32 s47, s46, 0x40000\n"
    "s_bfe_u32 s64, s36, 0x10001\n"
    "s_add_u32 s44, s44, s8\n"
    "s_add_u32 s59, s59, 128\n"
    "ds_read_b128 v[64:67], v7 offset:0\n"
    "ds_read_b128 v[68:71], v7 offset:1024\n"
    "ds_read_b128 v[72:75], v7 offset:2048\n"
    "ds_read_b128 v[76:79], v7 offset:3072\n"
    "ds_read_b128 v[80:83], v7 offset:4096\n"
    "ds_read_b128 v[84:87], v7 offset:5120\n"
    "ds_read_b128 v[88:91], v7 offset:6144\n"
    "ds_read_b128 v[92:95], v7 offset:7168\n"
    "ds_read_b128 v[96:99], v7 offset:8192\n"
    "ds_read_b128 v[100:103], v7 offset:9216\n"
    "ds_read_b128 v[104:107], v7 offset:10240\n"
    "ds_read_b128 v[108:111], v7 offset:11264\n"
    "v_mov_b64 v[112:113], 0\n"
    "v_mov_b64 v[114:115], 0\n"
    "v_mov_b64 v[116:117], 0\n"
    "v_mov_b64 v[118:119], 0\n"
    "v_mfma_f32_32x32x8_bf16 v[48:63], v[112:113], v[112:113], 0\n"
    "v_lshrrev_b32 v40, 4, v0\n"
    "v_and_b32 v40, 0x30, v40\n"
    "v_lshl_add_u32 v40, v40, 4, v0\n"
    "v_mov_b32 v41, 158224\n"
    "s_add_u32 s73, s49, s11\n"
    "s_add_u32 s73, s73, 4096\n"
    "s_add_u32 m0, s11, 151552\n"
    "s_mov_b32 s37, 0x80000\n"
    "LKW%=:\n"
    "ds_read_b64 v[16:17], v41 offset:16\n"
    "s_waitcnt lgkmcnt(0)\n"
    "v_min_u32 v16, v16, v17\n"
    "v_cmp_eq_u32 vcc, s36, v16\n"
    "s_cbranch_vccnz LKK%=\n"
    "s_sleep 1\n"
    "s_sub_u32 s37, s37, 1\n"
    "s_cbranch_scc0 LKW%=\n"
    "s_branch LFAIL%=\n"
    "LKK%=:\n"
    "LUF%=:\n"
    "s_memtime s[40:41]\n"
    "s_waitcnt lgkmcnt(0)\n"
    "s_sub_u32 s40, s66, s40\n"
    "s_sub_u32 s41, s40, 253\n"
    "s_cmp_lt_u32 s41, 16384\n"
    "s_cbranch_scc1 LUF%=\n"
    "s_cmp_lt_u32 s40, 253\n"
    "s_cselect_b32 s40, s40, 0\n"
    "s_and_b32 s40, s40, 0xfc\n"
    "s_getpc_b64 s[42:43]\n"
    "s_sub_u32 s40, 272, s40\n"
    "s_add_u32 s42, s42, s40\n"
    "s_addc_u32 s43, s43, 0\n"
    "s_setpc_b64 s[42:43]\n"
    ".rept 63\n"
    "s_nop 0\n"
    ".endr\n"
    "s_sub_u32 s51, s51, 2048\n"
    "s_mov_b32 m0, 122880\n"
    "s_nop 0\n"
    "buffer_load_dwordx4 v0, s[16:19], s51 offen offset:0 sc1 lds\n"
    "buffer_load_dwordx4 v0, s[16:19], s51 offen offset:1024 sc1 lds\n"
    "buffer_load_dwordx4 v0, s[16:19], s51 offen offset:2048 sc1 lds\n"
    "buffer_load_dwordx4 v0, s[16:19], s51 offen offset:3072 sc1 lds\n"
    "s_mov_b32 m0, 126976\n"
    "s_add_u32 s51, s51, 4096\n"
    "buffer_load_dwordx4 v0, s[16:19], s51 offen offset:0 sc1 lds\n"
    "buffer_load_dwordx4 v0, s[16:19], s51 offen offset:1024 sc1 lds\n"
    "buffer_load_dwordx4 v0, s[16:19], s51 offen offset:2048 sc1 lds\n"
    "buffer_load_dwordx4 v0, s[16:19], s51 offen offset:3072 sc1 lds\n"
    "LMN%=:\n"
    "LMM%=:\n"
    "s_memtime s[40:41]\n"
    "s_add_u32 s42, s21, s10\n"
    "s_waitcnt lgkmcnt(0)\n"
    "s_sub_u32 s40, s40, s42\n"
    "s_sub_u32 s41, s44, s40\n"
    "s_cmp_ge_i32 s41, 0\n"
    "s_cbranch_scc1 LMMD%=\n"
    "LMMI%=:\n"
    "s_add_u32 s44, s44, s13\n"
    "s_add_u32 s41, s41, s13\n"
    "s_cbranch_scc0 LMMI%=\n"
    "s_branch LMM%=\n"
    "LMMD%=:\n"
    "s_add_u32 s39, s44, s10\n"
    "v_mov_b32 v44, s72\n"
    "v_mov_b32 v45, s44\n"
    "ds_write_b32 v44, v45 offset:16\n"
    "s_mov_b32 m0, 147456\n"
    "s_mov_b32 s37, 64\n"
    "s_waitcnt lgkmcnt(0)\n"
    "LUL%=:\n"
    "s_memtime s[40:41]\n"
    "s_waitcnt lgkmcnt(0)\n"
    "s_sub_u32 s40, s39, s40\n"
    "s_sub_u32 s41, s40, 253\n"
    "s_cmp_lt_u32 s41, 16384\n"
    "s_cbranch_scc1 LUL%=\n"
    "s_cmp_lt_u32 s40, 253\n"
    "s_cselect_b32 s40, s40, 0\n"
    "s_and_b32 s40, s40, 0xfc\n"
    "s_getpc_b64 s[42:43]\n"
    "s_sub_u32 s40, 272, s40\n"
    "s_add_u32 s42, s42, s40\n"
    "s_addc_u32 s43, s43, 0\n"
    "s_setpc_b64 s[42:43]\n"
    ".rept 63\n"
    "s_nop 0\n"
    ".endr\n"
    "buffer_load_dwordx4 v0, s[16:19], s49 offen offset:0 sc1 lds\n"
    "buffer_load_dwordx4 v0, s[16:19], s49 offen offset:1024 sc1 lds\n"
    "buffer_load_dwordx4 v0, s[16:19], s49 offen offset:2048 sc1 lds\n"
    "buffer_load_dwordx4 v0, s[16:19], s49 offen offset:3072 sc1 lds\n"
    "s_mov_b32 m0, 152576\n"
    "s_getpc_b64 s[42:43]\n"
    "s_add_u32 s42, s42, s0\n"
    "s_addc_u32 s43, s43, 0\n"
    "s_setpc_b64 s[42:43]\n"
    ".rept 63\n"
    "s_nop 1\n"
    ".endr\n"
    "LDW%=:\n"
    "ds_read2_b32 v[46:47], v44 offset0:2 offset1:3\n"
    "s_waitcnt lgkmcnt(0)\n"
    "v_readfirstlane_b32 s39, v47\n"
    "v_readfirstlane_b32 s40, v46\n"
    "s_sub_u32 s38, s39, s44\n"
    "s_cmp_gt_i32 s38, 0\n"
    "s_cselect_b32 s44, s39, s44\n"
    "s_cbranch_scc1 LMN%=\n"
    "s_cmp_eq_u32 s40, s36\n"
    "s_cbranch_scc1 LBM%=\n"
    "s_sub_u32 s37, s37, 1\n"
    "s_cbranch_scc0 LDW%=\n"
    "s_branch LBM%=\n"
    "LW2%=:\n"
    "ds_read_b128 v[16:19], v14 offset:0\n"
    "v_add_u32 v118, 122880, v0\n"
    "ds_read_b128 v[32:35], v118 offset:0\n"
    "ds_read_b128 v[48:51], v118 offset:4096\n"
    "ds_read_b128 v[20:23], v14 offset:1024\n"
    "ds_read_b128 v[36:39], v118 offset:1024\n"
    "ds_read_b128 v[52:55], v118 offset:5120\n"
    "v_mov_b32 v111, 0xd0289000\n"
    "v_cndmask_b32 v80, v111, 0, s[80:81]\n"
    "v_cndmask_b32 v81, v111, 0, s[82:83]\n"
    "v_cndmask_b32 v82, v111, 0, s[84:85]\n"
    "v_cndmask_b32 v83, v111, 0, s[86:87]\n"
    "v_cndmask_b32 v84, v111, 0, s[88:89]\n"
    "v_cndmask_b32 v85, v111, 0, s[90:91]\n"
    "v_cndmask_b32 v86, v111, 0, s[92:93]\n"
    "v_cndmask_b32 v87, v111, 0, s[94:95]\n"
    "v_lshrrev_b32 v112, 1, v0\n"
    "v_add_u32 v112, 149760, v112\n"
    "ds_read_b128 v[24:27], v14 offset:2048\n"
    "ds_read_b128 v[40:43], v118 offset:2048\n"
    "ds_read_b128 v[56:59], v118 offset:6144\n"
    "s_waitcnt lgkmcnt(7)\n"
    "v_mfma_f32_16x16x32_bf16 v[80:83], v[32:35], v[16:19], v[80:83]\n"
    "ds_read_b128 v[28:31], v14 offset:3072\n"
    "ds_read_b128 v[44:47], v118 offset:3072\n"
    "s_waitcnt lgkmcnt(8)\n"
    "v_mfma_f32_16x16x32_bf16 v[84:87], v[48:51], v[16:19], v[84:87]\n"
    "ds_read_b128 v[60:63], v118 offset:7168\n"
    "s_waitcnt lgkmcnt(7)\n"
    "v_mov_b32 v114, s72\n"
    "v_mfma_f32_16x16x32_bf16 v[80:83], v[36:39], v[20:23], v[80:83]\n"
    "ds_read_b128 v[64:67], v6 offset:0\n"
    "ds_read_b128 v[68:71], v6 offset:64\n"
    "s_waitcnt lgkmcnt(8)\n"
    "v_mfma_f32_16x16x32_bf16 v[84:87], v[52:55], v[20:23], v[84:87]\n"
    "ds_read_b128 v[72:75], v6 offset:128\n"
    "s_waitcnt lgkmcnt(7)\n"
    "v_mov_b32 v105, 0\n"
    "v_mfma_f32_16x16x32_bf16 v[80:83], v[40:43], v[24:27], v[80:83]\n"
    "ds_read_b128 v[76:79], v6 offset:192\n"
    "ds_read_b64 v[92:93], v112 offset:0\n"
    "s_waitcnt lgkmcnt(8)\n"
    "v_mfma_f32_16x16x32_bf16 v[84:87], v[56:59], v[24:27], v[84:87]\n"
    "ds_read_b64 v[94:95], v112 offset:512\n"
    "s_waitcnt lgkmcnt(7)\n"
    "v_mov_b32 v106, 0\n"
    "v_mfma_f32_16x16x32_bf16 v[80:83], v[44:47], v[28:31], v[80:83]\n"
    "ds_read_b64 v[96:97], v112 offset:1024\n"
    "ds_read_b64 v[98:99], v112 offset:1536\n"
    "s_waitcnt lgkmcnt(8)\n"
    "v_mfma_f32_16x16x32_bf16 v[84:87], v[60:63], v[28:31], v[84:87]\n"
    "ds_write_b32 v114, v119 offset:24\n"
    "s_waitcnt lgkmcnt(8)\n"
    "v_mfma_f32_16x16x32_bf16 v[88:91], v[64:67], v[16:19], 0\n"
    "ds_read_b128 v[32:35], v118 offset:8192\n"
    "ds_read_b128 v[36:39], v118 offset:9216\n"
    "s_waitcnt lgkmcnt(9)\n"
    "v_mfma_f32_16x16x32_bf16 v[88:91], v[68:71], v[20:23], v[88:91]\n"
    "ds_read_b128 v[40:43], v118 offset:10240\n"
    "ds_read_b128 v[44:47], v118 offset:11264\n"
    "s_waitcnt lgkmcnt(10)\n"
    "v_mfma_f32_16x16x32_bf16 v[88:91], v[72:75], v[24:27], v[88:91]\n"
    "ds_read_b128 v[48:51], v118 offset:12288\n"
    "ds_read_b128 v[52:55], v118 offset:13312\n"
    "s_waitcnt lgkmcnt(11)\n"
    "v_max3_f32 v108, v80, v81, v82\n"
    "v_max3_f32 v109, v83, v84, v85\n"
    "v_mfma_f32_16x16x32_bf16 v[88:91], v[76:79], v[28:31], v[88:91]\n"
    "ds_read_b128 v[56:59], v118 offset:14336\n"
    "ds_read_b128 v[60:63], v118 offset:15360\n"
    "v_max3_f32 v108, v108, v86, v87\n"
    "v_max_f32 v108, v108, v109\n"
    "v_mov_b32 v109, v108\n"
    "s_nop 1\n"
    "v_permlane32_swap_b32 v108, v109\n"
    "v_max_f32 v108, v108, v109\n"
    "v_max3_f32 v108, v108, v88, v89\n"
    "v_max3_f32 v108, v108, v90, v91\n"
    "v_mov_b32 v109, v108\n"
    "s_nop 1\n"
    "v_permlane16_swap_b32 v108, v109\n"
    "v_max_f32 v108, v108, v109\n"
    "v_mul_f32 v108, 0xbe0293ee, v108\n"
    "v_pk_fma_f32 v[80:81], v[80:81], s[64:65], v[108:109] op_sel:[0,1,0] op_sel_hi:[1,1,0]\n"
    "v_pk_fma_f32 v[82:83], v[82:83], s[64:65], v[108:109] op_sel:[0,1,0] op_sel_hi:[1,1,0]\n"
    "v_pk_fma_f32 v[84:85], v[84:85], s[64:65], v[108:109] op_sel:[0,1,0] op_sel_hi:[1,1,0]\n"
    "v_pk_fma_f32 v[86:87], v[86:87], s[64:65], v[108:109] op_sel:[0,1,0] op_sel_hi:[1,1,0]\n"
    "v_pk_fma_f32 v[88:89], v[88:89], s[64:65], v[108:109] op_sel:[0,1,0] op_sel_hi:[1,1,0]\n"
    "v_exp_f32 v80, v80\n"
    "v_exp_f32 v81, v81\n"
    "v_exp_f32 v82, v82\n"
    "v_exp_f32 v83, v83\n"
    "v_exp_f32 v84, v84\n"
    "v_exp_f32 v85, v85\n"
    "v_exp_f32 v86, v86\n"
    "v_exp_f32 v87, v87\n"
    "v_exp_f32 v88, v88\n"
    "v_exp_f32 v89, v89\n"
    "v_pk_add_f32 v[108:109], v[80:81], v[82:83]\n"
    "v_pk_add_f32 v[110:111], v[84:85], v[86:87]\n"
    "v_pk_add_f32 v[108:109], v[108:109], v[88:89]\n"
    "v_pk_add_f32 v[108:109], v[108:109], v[110:111]\n"
    "v_add_f32 v108, v108, v109\n"
    "v_mov_b32 v109, v108\n"
    "s_nop 1\n"
    "v_permlane16_swap_b32 v108, v109\n"
    "v_add_f32 v108, v108, v109\n"
    "v_mov_b32 v109, v108\n"
    "s_nop 1\n"
    "v_permlane32_swap_b32 v108, v109\n"
    "v_add_f32 v108, v108, v109\n"
    "v_rcp_f32 v108, v108\n"
    "s_nop 0\n"
    "v_pk_mul_f32 v[80:81], v[80:81], v[108:109] op_sel_hi:[1,0]\n"
    "v_pk_mul_f32 v[82:83], v[82:83], v[108:109] op_sel_hi:[1,0]\n"
    "v_pk_mul_f32 v[84:85], v[84:85], v[108:109] op_sel_hi:[1,0]\n"
    "v_pk_mul_f32 v[86:87], v[86:87], v[108:109] op_sel_hi:[1,0]\n"
    "v_pk_mul_f32 v[88:89], v[88:89], v[108:109] op_sel_hi:[1,0]\n"
    "v_cvt_pk_bf16_f32 v100, v80, v81\n"
    "v_cvt_pk_bf16_f32 v101, v82, v83\n"
    "v_cvt_pk_bf16_f32 v102, v84, v85\n"
    "v_cvt_pk_bf16_f32 v103, v86, v87\n"
    "v_cvt_pk_bf16_f32 v104, v88, v89\n"
    "v_cvt_pk_bf16_f32 v107, v88, v89\n"
    "s_waitcnt lgkmcnt(7)\n"
    "v_mfma_f32_16x16x32_bf16 v[16:19], v[32:35], v[100:103], 0\n"
    "ds_read_b128 v[64:67], v7 offset:0\n"
    "s_waitcnt lgkmcnt(7)\n"
    "v_mfma_f32_16x16x32_bf16 v[20:23], v[36:39], v[100:103], 0\n"
    "ds_read_b128 v[68:71], v7 offset:1024\n"
    "v_mfma_f32_16x16x16_bf16 v[16:19], v[92:93], v[104:105], v[16:19]\n"
    "v_mfma_f32_16x16x16_bf16 v[20:23], v[92:93], v[106:107], v[20:23]\n"
    "s_waitcnt lgkmcnt(7)\n"
    "v_mfma_f32_16x16x32_bf16 v[24:27], v[40:43], v[100:103], 0\n"
    "ds_read_b128 v[72:75], v7 offset:2048\n"
    "s_waitcnt lgkmcnt(7)\n"
    "v_mfma_f32_16x16x32_bf16 v[28:31], v[44:47], v[100:103], 0\n"
    "ds_read_b128 v[76:79], v7 offset:3072\n"
    "v_cvt_pk_bf16_f32 v112, v16, v17\n"
    "v_cvt_pk_bf16_f32 v113, v18, v19\n"
    "v_mfma_f32_16x16x16_bf16 v[24:27], v[94:95], v[104:105], v[24:27]\n"
    "v_cvt_pk_bf16_f32 v114, v20, v21\n"
    "v_cvt_pk_bf16_f32 v115, v22, v23\n"
    "v_mfma_f32_16x16x16_bf16 v[28:31], v[94:95], v[106:107], v[28:31]\n"
    "ds_write_b128 v14, v[112:115] offset:0\n"
    "s_waitcnt lgkmcnt(8)\n"
    "v_mfma_f32_16x16x32_bf16 v[16:19], v[48:51], v[100:103], 0\n"
    "ds_read_b128 v[80:83], v7 offset:4096\n"
    "s_waitcnt lgkmcnt(8)\n"
    "v_mfma_f32_16x16x32_bf16 v[20:23], v[52:55], v[100:103], 0\n"
    "ds_read_b128 v[84:87], v7 offset:5120\n"
    "v_cvt_pk_bf16_f32 v108, v24, v25\n"
    "v_cvt_pk_bf16_f32 v109, v26, v27\n"
    "v_mfma_f32_16x16x16_bf16 v[16:19], v[96:97], v[104:105], v[16:19]\n"
    "v_cvt_pk_bf16_f32 v110, v28, v29\n"
    "v_cvt_pk_bf16_f32 v111, v30, v31\n"
    "v_mfma_f32_16x16x16_bf16 v[20:23], v[96:97], v[106:107], v[20:23]\n"
    "ds_write_b128 v14, v[108:111] offset:1024\n"
    "s_waitcnt lgkmcnt(9)\n"
    "v_mfma_f32_16x16x32_bf16 v[24:27], v[56:59], v[100:103], 0\n"
    "s_waitcnt lgkmcnt(8)\n"
    "v_mfma_f32_16x16x32_bf16 v[28:31], v[60:63], v[100:103], 0\n"
    "v_cvt_pk_bf16_f32 v112, v16, v17\n"
    "v_cvt_pk_bf16_f32 v113, v18, v19\n"
    "v_mfma_f32_16x16x16_bf16 v[24:27], v[98:99], v[104:105], v[24:27]\n"
    "v_cvt_pk_bf16_f32 v114, v20, v21\n"
    "v_cvt_pk_bf16_f32 v115, v22, v23\n"
    "v_mfma_f32_16x16x16_bf16 v[28:31], v[98:99], v[106:107], v[28:31]\n"
    "ds_write_b128 v14, v[112:115] offset:2048\n"
    "ds_read_b128 v[88:91], v7 offset:6144\n"
    "ds_read_b128 v[92:95], v7 offset:7168\n"
    "ds_read_b128 v[96:99], v7 offset:8192\n"
    "ds_read_b128 v[100:103], v7 offset:9216\n"
    "v_cvt_pk_bf16_f32 v108, v24, v25\n"
    "v_cvt_pk_bf16_f32 v109, v26, v27\n"
    "s_nop 0\n"
    "v_cvt_pk_bf16_f32 v110, v28, v29\n"
    "v_cvt_pk_bf16_f32 v111, v30, v31\n"
    "ds_write_b128 v14, v[108:111] offset:3072\n"
    "s_add_u32 s53, s50, s55\n"
    "s_mov_b32 s49, s48\n"
    "s_add_u32 s51, s53, 0\n"
    "s_lshr_b32 s38, s51, 8\n"
    "s_xor_b32 s38, s38, s56\n"
    "s_and_b32 s38, s38, 0x2000\n"
    "s_or_b32 s51, s51, s38\n"
    "s_add_u32 s52, s53, 524288\n"
    "s_lshr_b32 s38, s52, 8\n"
    "s_xor_b32 s38, s38, s56\n"
    "s_and_b32 s38, s38, 0x2000\n"
    "s_or_b32 s52, s52, s38\n"
    "s_add_u32 s53, s53, 1048576\n"
    "s_lshr_b32 s38, s53, 8\n"
    "s_xor_b32 s38, s38, s56\n"
    "s_and_b32 s38, s38, 0x2000\n"
    "s_or_b32 s53, s53, s38\n"
    "s_xor_b32 s46, s46, 0x80000\n"
    "s_add_u32 s47, s46, 0x40000\n"
    "s_bfe_u32 s64, s36, 0x10001\n"
    "s_add_u32 s44, s44, s8\n"
    "s_add_u32 s59, s59, 128\n"
    "s_waitcnt vmcnt(2)\n"
    "s_waitcnt lgkmcnt(0)\n"
    "ds_read_b128 v[104:107], v7 offset:10240\n"
    "ds_read_b128 v[108:111], v7 offset:11264\n"
    "v_mov_b64 v[112:113], 0\n"
    "v_mov_b64 v[114:115], 0\n"
    "s_waitcnt vmcnt(2)\n"
    "v_mov_b64 v[116:117], 0\n"
    "v_mov_b64 v[118:119], 0\n"
    "v_mfma_f32_32x32x8_bf16 v[48:63], v[112:113], v[112:113], 0\n"
    "v_lshrrev_b32 v40, 4, v0\n"
    "v_and_b32 v40, 0x30, v40\n"
    "v_lshl_add_u32 v40, v40, 4, v0\n"
    "v_mov_b32 v41, 158224\n"
    "s_add_u32 s73, s49, s11\n"
    "s_add_u32 s73, s73, 4096\n"
    "s_add_u32 m0, s11, 151552\n"
    "s_branch LBM%=\n"
    "s_waitcnt lgkmcnt(0)\n"
    "LPRE%=:\n"
    "s_nop 7\n"
    "ds_read_b128 v[64:67], v7 offset:0\n"
    "ds_read_b128 v[68:71], v7 offset:1024\n"
    "ds_read_b128 v[72:75], v7 offset:2048\n"
    "ds_read_b128 v[76:79], v7 offset:3072\n"
    "ds_read_b128 v[80:83], v7 offset:4096\n"
    "ds_read_b128 v[84:87], v7 offset:5120\n"
    "ds_read_b128 v[88:91], v7 offset:6144\n"
    "ds_read_b128 v[92:95], v7 offset:7168\n"
    "ds_read_b128 v[96:99], v7 offset:8192\n"
    "ds_read_b128 v[100:103], v7 offset:9216\n"
    "ds_read_b128 v[104:107], v7 offset:10240\n"
    "ds_read_b128 v[108:111], v7 offset:11264\n"
    "v_mov_b64 v[112:113], 0\n"
    "v_mov_b64 v[114:115], 0\n"
    "v_mov_b64 v[116:117], 0\n"
    "v_mov_b64 v[118:119], 0\n"
    "v_mfma_f32_32x32x8_bf16 v[48:63], v[112:113], v[112:113], 0\n"
    "v_lshrrev_b32 v40, 4, v0\n"
    "v_and_b32 v40, 0x30, v40\n"
    "v_lshl_add_u32 v40, v40, 4, v0\n"
    "v_mov_b32 v41, 158224\n"
    "s_add_u32 s73, s49, s11\n"
    "s_add_u32 s73, s73, 4096\n"
    "s_add_u32 m0, s11, 151552\n"
    "s_cmp_lg_u32 s61, 1\n"
    "s_cbranch_scc1 LBM%=\n"
    "s_branch LMN%=\n"
    "LEND%=:\n"
    "s_waitcnt vmcnt(1) lgkmcnt(0)\n"
    "s_barrier\n"
    "s_cmp_lg_u32 s61, 1\n"
    "s_cbranch_scc1 LE0%=\n"
    "buffer_store_dword v1, off, s[16:19], s58 sc1\n"
    "s_add_u32 s58, s58, 4\n"
    "s_bitset0_b32 s58, 31\n"
    "LE0%=:\n"
    "s_add_u32 s54, s50, s57\n"
    "s_lshr_b32 s38, s54, 8\n"
    "s_xor_b32 s38, s38, s74\n"
    "s_and_b32 s38, s38, 0x2000\n"
    "s_or_b32 s54, s54, s38\n"
    "s_cmp_lt_u32 s61, 2\n"
    "s_cbranch_scc1 LE1%=\n"
    "ds_read_b128 v[16:19], v14 offset:0\n"
    "v_add_u32 v118, 122880, v0\n"
    "ds_read_b128 v[32:35], v118 offset:0\n"
    "ds_read_b128 v[48:51], v118 offset:4096\n"
    "ds_read_b128 v[20:23], v14 offset:1024\n"
    "ds_read_b128 v[36:39], v118 offset:1024\n"
    "ds_read_b128 v[52:55], v118 offset:5120\n"
    "v_mov_b32 v111, 0xd0289000\n"
    "v_cndmask_b32 v80, v111, 0, s[80:81]\n"
    "v_cndmask_b32 v81, v111, 0, s[82:83]\n"
    "v_cndmask_b32 v82, v111, 0, s[84:85]\n"
    "v_cndmask_b32 v83, v111, 0, s[86:87]\n"
    "v_cndmask_b32 v84, v111, 0, s[88:89]\n"
    "v_cndmask_b32 v85, v111, 0, s[90:91]\n"
    "v_cndmask_b32 v86, v111, 0, s[92:93]\n"
    "v_cndmask_b32 v87, v111, 0, s[94:95]\n"
    "v_lshrrev_b32 v112, 1, v0\n"
    "v_add_u32 v112, 149760, v112\n"
    "ds_read_b128 v[24:27], v14 offset:2048\n"
    "ds_read_b128 v[40:43], v118 offset:2048\n"
    "ds_read_b128 v[56:59], v118 offset:6144\n"
    "s_waitcnt lgkmcnt(7)\n"
    "v_mfma_f32_16x16x32_bf16 v[80:83], v[32:35], v[16:19], v[80:83]\n"
    "ds_read_b128 v[28:31], v14 offset:3072\n"
    "ds_read_b128 v[44:47], v118 offset:3072\n"
    "s_waitcnt lgkmcnt(8)\n"
    "v_mfma_f32_16x16x32_bf16 v[84:87], v[48:51], v[16:19], v[84:87]\n"
    "ds_read_b128 v[60:63], v118 offset:7168\n"
    "s_waitcnt lgkmcnt(7)\n"
    "v_mov_b32 v114, s72\n"
    "v_mfma_f32_16x16x32_bf16 v[80:83], v[36:39], v[20:23], v[80:83]\n"
    "ds_read_b128 v[64:67], v6 offset:0\n"
    "ds_read_b128 v[68:71], v6 offset:64\n"
    "s_waitcnt lgkmcnt(8)\n"
    "v_mfma_f32_16x16x32_bf16 v[84:87], v[52:55], v[20:23], v[84:87]\n"
    "ds_read_b128 v[72:75], v6 offset:128\n"
    "s_waitcnt lgkmcnt(7)\n"
    "v_mov_b32 v105, 0\n"
    "v_mfma_f32_16x16x32_bf16 v[80:83], v[40:43], v[24:27], v[80:83]\n"
    "ds_read_b128 v[76:79], v6 offset:192\n"
    "ds_read_b64 v[92:93], v112 offset:0\n"
    "s_waitcnt lgkmcnt(8)\n"
    "v_mfma_f32_16x16x32_bf16 v[84:87], v[56:59], v[24:27], v[84:87]\n"
    "ds_read_b64 v[94:95], v112 offset:512\n"
    "s_waitcnt lgkmcnt(7)\n"
    "v_mov_b32 v106, 0\n"
    "v_mfma_f32_16x16x32_bf16 v[80:83], v[44:47], v[28:31], v[80:83]\n"
    "ds_read_b64 v[96:97], v112 offset:1024\n"
    "ds_read_b64 v[98:99], v112 offset:1536\n"
    "s_waitcnt lgkmcnt(8)\n"
    "v_mfma_f32_16x16x32_bf16 v[84:87], v[60:63], v[28:31], v[84:87]\n"
    "ds_write_b32 v114, v119 offset:24\n"
    "s_waitcnt lgkmcnt(8)\n"
    "v_mfma_f32_16x16x32_bf16 v[88:91], v[64:67], v[16:19], 0\n"
    "ds_read_b128 v[32:35], v118 offset:8192\n"
    "ds_read_b128 v[36:39], v118 offset:9216\n"
    "s_waitcnt lgkmcnt(9)\n"
    "v_mfma_f32_16x16x32_bf16 v[88:91], v[68:71], v[20:23], v[88:91]\n"
    "ds_read_b128 v[40:43], v118 offset:10240\n"
    "ds_read_b128 v[44:47], v118 offset:11264\n"
    "s_waitcnt lgkmcnt(10)\n"
    "v_mfma_f32_16x16x32_bf16 v[88:91], v[72:75], v[24:27], v[88:91]\n"
    "ds_read_b128 v[48:51], v118 offset:12288\n"
    "ds_read_b128 v[52:55], v118 offset:13312\n"
    "s_waitcnt lgkmcnt(11)\n"
    "v_max3_f32 v108, v80, v81, v82\n"
    "v_max3_f32 v109, v83, v84, v85\n"
    "v_mfma_f32_16x16x32_bf16 v[88:91], v[76:79], v[28:31], v[88:91]\n"
    "ds_read_b128 v[56:59], v118 offset:14336\n"
    "ds_read_b128 v[60:63], v118 offset:15360\n"
    "v_max3_f32 v108, v108, v86, v87\n"
    "v_max_f32 v108, v108, v109\n"
    "v_mov_b32 v109, v108\n"
    "s_nop 1\n"
    "v_permlane32_swap_b32 v108, v109\n"
    "v_max_f32 v108, v108, v109\n"
    "v_max3_f32 v108, v108, v88, v89\n"
    "v_max3_f32 v108, v108, v90, v91\n"
    "v_mov_b32 v109, v108\n"
    "s_nop 1\n"
    "v_permlane16_swap_b32 v108, v109\n"
    "v_max_f32 v108, v108, v109\n"
    "v_mul_f32 v108, 0xbe0293ee, v108\n"
    "v_pk_fma_f32 v[80:81], v[80:81], s[64:65], v[108:109] op_sel:[0,1,0] op_sel_hi:[1,1,0]\n"
    "v_pk_fma_f32 v[82:83], v[82:83], s[64:65], v[108:109] op_sel:[0,1,0] op_sel_hi:[1,1,0]\n"
    "v_pk_fma_f32 v[84:85], v[84:85], s[64:65], v[108:109] op_sel:[0,1,0] op_sel_hi:[1,1,0]\n"
    "v_pk_fma_f32 v[86:87], v[86:87], s[64:65], v[108:109] op_sel:[0,1,0] op_sel_hi:[1,1,0]\n"
    "v_pk_fma_f32 v[88:89], v[88:89], s[64:65], v[108:109] op_sel:[0,1,0] op_sel_hi:[1,1,0]\n"
    "v_exp_f32 v80, v80\n"
    "v_exp_f32 v81, v81\n"
    "v_exp_f32 v82, v82\n"
    "v_exp_f32 v83, v83\n"
    "v_exp_f32 v84, v84\n"
    "v_exp_f32 v85, v85\n"
    "v_exp_f32 v86, v86\n"
    "v_exp_f32 v87, v87\n"
    "v_exp_f32 v88, v88\n"
    "v_exp_f32 v89, v89\n"
    "v_pk_add_f32 v[108:109], v[80:81], v[82:83]\n"
    "v_pk_add_f32 v[110:111], v[84:85], v[86:87]\n"
    "v_pk_add_f32 v[108:109], v[108:109], v[88:89]\n"
    "v_pk_add_f32 v[108:109], v[108:109], v[110:111]\n"
    "v_add_f32 v108, v108, v109\n"
    "v_mov_b32 v109, v108\n"
    "s_nop 1\n"
    "v_permlane16_swap_b32 v108, v109\n"
    "v_add_f32 v108, v108, v109\n"
    "v_mov_b32 v109, v108\n"
    "s_nop 1\n"
    "v_permlane32_swap_b32 v108, v109\n"
    "v_add_f32 v108, v108, v109\n"
    "v_rcp_f32 v108, v108\n"
    "s_nop 0\n"
    "v_pk_mul_f32 v[80:81], v[80:81], v[108:109] op_sel_hi:[1,0]\n"
    "v_pk_mul_f32 v[82:83], v[82:83], v[108:109] op_sel_hi:[1,0]\n"
    "v_pk_mul_f32 v[84:85], v[84:85], v[108:109] op_sel_hi:[1,0]\n"
    "v_pk_mul_f32 v[86:87], v[86:87], v[108:109] op_sel_hi:[1,0]\n"
    "v_pk_mul_f32 v[88:89], v[88:89], v[108:109] op_sel_hi:[1,0]\n"
    "v_cvt_pk_bf16_f32 v100, v80, v81\n"
    "v_cvt_pk_bf16_f32 v101, v82, v83\n"
    "v_cvt_pk_bf16_f32 v102, v84, v85\n"
    "v_cvt_pk_bf16_f32 v103, v86, v87\n"
    "v_cvt_pk_bf16_f32 v104, v88, v89\n"
    "v_cvt_pk_bf16_f32 v107, v88, v89\n"
    "s_waitcnt lgkmcnt(7)\n"
    "v_mfma_f32_16x16x32_bf16 v[16:19], v[32:35], v[100:103], 0\n"
    "s_waitcnt lgkmcnt(6)\n"
    "v_mfma_f32_16x16x32_bf16 v[20:23], v[36:39], v[100:103], 0\n"
    "v_mfma_f32_16x16x16_bf16 v[16:19], v[92:93], v[104:105], v[16:19]\n"
    "v_mfma_f32_16x16x16_bf16 v[20:23], v[92:93], v[106:107], v[20:23]\n"
    "s_waitcnt lgkmcnt(5)\n"
    "v_mfma_f32_16x16x32_bf16 v[24:27], v[40:43], v[100:103], 0\n"
    "s_waitcnt lgkmcnt(4)\n"
    "v_mfma_f32_16x16x32_bf16 v[28:31], v[44:47], v[100:103], 0\n"
    "v_cvt_pk_bf16_f32 v112, v16, v17\n"
    "v_cvt_pk_bf16_f32 v113, v18, v19\n"
    "v_mfma_f32_16x16x16_bf16 v[24:27], v[94:95], v[104:105], v[24:27]\n"
    "v_cvt_pk_bf16_f32 v114, v20, v21\n"
    "v_cvt_pk_bf16_f32 v115, v22, v23\n"
    "v_mfma_f32_16x16x16_bf16 v[28:31], v[94:95], v[106:107], v[28:31]\n"
    "ds_write_b128 v14, v[112:115] offset:0\n"
    "s_waitcnt lgkmcnt(4)\n"
    "v_mfma_f32_16x16x32_bf16 v[16:19], v[48:51], v[100:103], 0\n"
    "s_waitcnt lgkmcnt(3)\n"
    "v_mfma_f32_16x16x32_bf16 v[20:23], v[52:55], v[100:103], 0\n"
    "v_cvt_pk_bf16_f32 v108, v24, v25\n"
    "v_cvt_pk_bf16_f32 v109, v26, v27\n"
    "v_mfma_f32_16x16x16_bf16 v[16:19], v[96:97], v[104:105], v[16:19]\n"
    "v_cvt_pk_bf16_f32 v110, v28, v29\n"
    "v_cvt_pk_bf16_f32 v111, v30, v31\n"
    "v_mfma_f32_16x16x16_bf16 v[20:23], v[96:97], v[106:107], v[20:23]\n"
    "ds_write_b128 v14, v[108:111] offset:1024\n"
    "s_waitcnt lgkmcnt(3)\n"
    "v_mfma_f32_16x16x32_bf16 v[24:27], v[56:59], v[100:103], 0\n"
    "s_waitcnt lgkmcnt(2)\n"
    "v_mfma_f32_16x16x32_bf16 v[28:31], v[60:63], v[100:103], 0\n"
    "v_cvt_pk_bf16_f32 v112, v16, v17\n"
    "v_cvt_pk_bf16_f32 v113, v18, v19\n"
    "v_mfma_f32_16x16x16_bf16 v[24:27], v[98:99], v[104:105], v[24:27]\n"
    "v_cvt_pk_bf16_f32 v114, v20, v21\n"
    "v_cvt_pk_bf16_f32 v115, v22, v23\n"
    "v_mfma_f32_16x16x16_bf16 v[28:31], v[98:99], v[106:107], v[28:31]\n"
    "ds_write_b128 v14, v[112:115] offset:2048\n"
    "s_nop 2\n"
    "v_cvt_pk_bf16_f32 v108, v24, v25\n"
    "v_cvt_pk_bf16_f32 v109, v26, v27\n"
    "s_nop 1\n"
    "v_cvt_pk_bf16_f32 v110, v28, v29\n"
    "v_cvt_pk_bf16_f32 v111, v30, v31\n"
    "ds_write_b128 v14, v[108:111] offset:3072\n"
    "s_waitcnt lgkmcnt(0)\n"
    "LE1%=:\n"
    "s_barrier\n"
    "ds_read_b128 v[32:35], v9 offset:16384\n"
    "ds_read_b128 v[36:39], v9 offset:17408\n"
    "v_lshrrev_b32 v40, 4, v0\n"
    "v_and_b32 v40, 0x30, v40\n"
    "v_lshl_add_u32 v40, v40, 4, v0\n"
    "s_waitcnt lgkmcnt(1)\n"
    "buffer_store_dwordx4 v[32:35], v40, s[16:19], s54 offen offset:0 sc1\n"
    "s_waitcnt lgkmcnt(0)\n"
    "buffer_store_dwordx4 v[36:39], v40, s[16:19], s54 offen offset:2048 sc1\n"
    "s_waitcnt vmcnt(0)\n"
    "s_barrier\n"
    "buffer_store_dword v1, off, s[16:19], s58 sc1\n"
    "s_endpgm\n"
    "LFAIL%=:\n"
    "buffer_store_dword v1, off, s[16:19], s35 sc1\n"
    "s_endpgm\n"
    :
    : "{v0}"(hv0), "{v1}"(hv1), "{v2}"(hv2), "{v3}"(hv3), "{v4}"(hv4), "{v5}"(hv5), "{v6}"(hv6), "{v7}"(hv7), "{v8}"(hv8), "{v9}"(hv9), "{v10}"(hv10), "{v11}"(hv11), "{v12}"(hv12), "{v13}"(hv13), "{v14}"(hv14), "{v15}"(hv15), "{v16}"(hv16), "{v17}"(hv17), "{v18}"(hv18), "{v19}"(hv19), "{v20}"(hv20)
    : "v255", "a255", "s101", "vcc", "scc", "memory");
  }
  __builtin_unreachable();
}
#define SW 1
#define ODL 9
#define ROLL 2
#define MEMFENCE() asm volatile("" ::: "memory")
__device__ __forceinline__ void t_role8(int w, int m, int i, bool gated, const unsigned* rdy, char* ws, const u16* W) {
  const int lane = lane_id(), g = lane >> 4, q = (lane >> 2) & 3, p = lane & 3;
  const unsigned me = w ? OOR : 0u, c1 = 0u, c2 = 0x2000u;
  const int h = i & 30, par = i & 1, ob = m * 524288 + h * 16384 + (m == 1 ? w * 2048 : w * 1024);
  const int pv[24] = {w, (int)Z_XN1 + par * 65536 + w * 16384, (int)Z_XN + w * 16384, ob, (int)c1, (int)nearof(Z_FT + h * 16384, c1) + m * 16, (int)nearof(Z_FT + h * 16384, c2) + m * 16,
                      (int)((nearof(Z_DEC, c1) + 4096 + m * 512 + par * 4) | me), (int)blockIdx.x * 128 + (gated ? 4 : 0), gated ? 640 + m * 128 : 40960 + (int)blockIdx.x * 128, 4096 + ((int)blockIdx.x & 7) * 128, (int)(nearof(Z_FO + 32768, c2) + h * 256), (int)Z_ERR, m, par,
                      (int)(size_t)W, (int)((size_t)W >> 32) & 0xffff, (int)(size_t)ws, (int)((size_t)ws >> 32) & 0xffff, (int)ZWS_BYTES, (int)((Z_DBG + MBY(1) + (m * 32 + i) * 4096) | (w == 2 ? 0u : OOR)),
                      (int)(size_t)rdy, (int)((size_t)rdy >> 32), (int)((SW ? nearof(Z_DEC, c1) + 1536 : nearof(Z_DEC, c2) + 1024) | (w == 1 ? 0u : OOR))};
  int hv0 = lane * 16, hv1 = (lane & 15) * 1056 + g * 16, hv2 = 0;
  int hv3 = (w * 8 + (lane >> 5)) * 8192 + h * 256 + ((lane & 31) ^ ((lane >> 5) * 2)) * 16, hv4 = g * 4480 + (q >> 1) * 1088 + (q & 1) * 544 + 64 * w + 8 * p, hv5 = 0;
#pragma unroll
  for (int j = 0; j < 24; ++j) hv5 = lane == j ? pv[j] : hv5;
  asm volatile(
    "s_nop 1\n"
    "v_readlane_b32 s36, v5, 0\n"
    "v_readlane_b32 s45, v5, 1\n"
    "v_readlane_b32 s46, v5, 2\n"
    "v_readlane_b32 s47, v5, 3\n"
    "v_readlane_b32 s48, v5, 4\n"
    "v_readlane_b32 s50, v5, 5\n"
    "v_readlane_b32 s51, v5, 6\n"
    "v_readlane_b32 s53, v5, 7\n"
    "v_readlane_b32 s54, v5, 8\n"
    "v_readlane_b32 s55, v5, 9\n"
    "v_readlane_b32 s56, v5, 10\n"
    "v_readlane_b32 s57, v5, 11\n"
    "v_readlane_b32 s58, v5, 12\n"
    "v_readlane_b32 s61, v5, 13\n"
    "v_readlane_b32 s40, v5, 14\n"
    "v_readlane_b32 s20, v5, 15\n"
    "v_readlane_b32 s21, v5, 16\n"
    "v_readlane_b32 s16, v5, 17\n"
    "v_readlane_b32 s17, v5, 18\n"
    "v_readlane_b32 s18, v5, 19\n"
    "v_readlane_b32 s65, v5, 20\n"
    "v_readlane_b32 s24, v5, 21\n"
    "v_readlane_b32 s25, v5, 22\n"
    "v_readlane_b32 s26, v5, 23\n"
    "v_mov_b32 v5, 1\n"
    "s_mov_b32 s19, 0x20000\n"
    "s_mov_b32 s22, 0x400000\n"
    "s_mov_b32 s23, 0x20000\n"
    "s_xor_b32 s49, s48, 0x2000\n"
    "s_mul_i32 s44, s36, 8448\n"
    "s_mul_i32 s39, s36, 4480\n"
    "s_mov_b32 s43, 0\n"
    "s_mov_b32 s27, 0\n"
    "s_mov_b32 s41, s40\n"
    "s_mov_b32 s42, s40\n"
    "s_mov_b32 s52, 0x80000000\n"
    "s_cmp_eq_u32 s36, 1\n"
    "s_cselect_b32 s68, 0, 0x80000000\n"
    "s_add_u32 s38, s54, 40960\n"
    "v_mov_b32 v12, s38\n"
    "v_mov_b32 v13, 0\n"
    "global_store_dword v12, v13, s[24:25] sc1\n"
    "s_add_u32 s38, s54, 8192\n"
    "v_mov_b32 v14, s38\n"
    "v_mov_b32 v15, s56\n"
    "v_mov_b32 v16, s55\n"
    "s_add_u32 m0, s39, 0\n"
    "s_mov_b32 s38, 0\n"
    "buffer_load_dwordx4 v3, s[20:23], s38 offen lds\n"
    "s_add_u32 m0, s39, 1088\n"
    "s_mov_b32 s38, 16384\n"
    "buffer_load_dwordx4 v3, s[20:23], s38 offen lds\n"
    "s_add_u32 m0, s39, 2176\n"
    "s_mov_b32 s38, 32768\n"
    "buffer_load_dwordx4 v3, s[20:23], s38 offen lds\n"
    "s_add_u32 m0, s39, 3264\n"
    "s_mov_b32 s38, 49152\n"
    "buffer_load_dwordx4 v3, s[20:23], s38 offen lds\n"
    "s_add_u32 m0, s39, 17920\n"
    "s_mov_b32 s38, 262144\n"
    "buffer_load_dwordx4 v3, s[20:23], s38 offen lds\n"
    "s_add_u32 m0, s39, 19008\n"
    "s_mov_b32 s38, 278528\n"
    "buffer_load_dwordx4 v3, s[20:23], s38 offen lds\n"
    "s_add_u32 m0, s39, 20096\n"
    "s_mov_b32 s38, 294912\n"
    "buffer_load_dwordx4 v3, s[20:23], s38 offen lds\n"
    "s_add_u32 m0, s39, 21184\n"
    "s_mov_b32 s38, 311296\n"
    "buffer_load_dwordx4 v3, s[20:23], s38 offen lds\n"
    "s_add_u32 m0, s39, 35840\n"
    "s_mov_b32 s38, 524288\n"
    "buffer_load_dwordx4 v3, s[20:23], s38 offen lds\n"
    "s_add_u32 m0, s39, 36928\n"
    "s_mov_b32 s38, 540672\n"
    "buffer_load_dwordx4 v3, s[20:23], s38 offen lds\n"
    "s_add_u32 m0, s39, 38016\n"
    "s_mov_b32 s38, 557056\n"
    "buffer_load_dwordx4 v3, s[20:23], s38 offen lds\n"
    "s_add_u32 m0, s39, 39104\n"
    "s_mov_b32 s38, 573440\n"
    "buffer_load_dwordx4 v3, s[20:23], s38 offen lds\n"
    "s_add_u32 m0, s39, 53760\n"
    "s_mov_b32 s38, 786432\n"
    "buffer_load_dwordx4 v3, s[20:23], s38 offen lds\n"
    "s_add_u32 m0, s39, 54848\n"
    "s_mov_b32 s38, 802816\n"
    "buffer_load_dwordx4 v3, s[20:23], s38 offen lds\n"
    "s_add_u32 m0, s39, 55936\n"
    "s_mov_b32 s38, 819200\n"
    "buffer_load_dwordx4 v3, s[20:23], s38 offen lds\n"
    "s_add_u32 m0, s39, 57024\n"
    "s_mov_b32 s38, 835584\n"
    "buffer_load_dwordx4 v3, s[20:23], s38 offen lds\n"
    "s_add_u32 m0, s39, 71680\n"
    "s_mov_b32 s38, 1048576\n"
    "buffer_load_dwordx4 v3, s[20:23], s38 offen lds\n"
    "s_add_u32 m0, s39, 72768\n"
    "s_mov_b32 s38, 1064960\n"
    "buffer_load_dwordx4 v3, s[20:23], s38 offen lds\n"
    "s_add_u32 m0, s39, 73856\n"
    "s_mov_b32 s38, 1081344\n"
    "buffer_load_dwordx4 v3, s[20:23], s38 offen lds\n"
    "s_add_u32 m0, s39, 74944\n"
    "s_mov_b32 s38, 1097728\n"
    "buffer_load_dwordx4 v3, s[20:23], s38 offen lds\n"
    "s_add_u32 m0, s39, 89600\n"
    "s_mov_b32 s38, 1310720\n"
    "buffer_load_dwordx4 v3, s[20:23], s38 offen lds\n"
    "s_add_u32 m0, s39, 90688\n"
    "s_mov_b32 s38, 1327104\n"
    "buffer_load_dwordx4 v3, s[20:23], s38 offen lds\n"
    "s_add_u32 m0, s39, 91776\n"
    "s_mov_b32 s38, 1343488\n"
    "buffer_load_dwordx4 v3, s[20:23], s38 offen lds\n"
    "s_add_u32 m0, s39, 92864\n"
    "s_mov_b32 s38, 1359872\n"
    "buffer_load_dwordx4 v3, s[20:23], s38 offen lds\n"
    "s_add_u32 m0, s39, 107520\n"
    "s_mov_b32 s38, 1572864\n"
    "buffer_load_dwordx4 v3, s[20:23], s38 offen lds\n"
    "s_add_u32 m0, s39, 108608\n"
    "s_mov_b32 s38, 1589248\n"
    "buffer_load_dwordx4 v3, s[20:23], s38 offen lds\n"
    "s_add_u32 m0, s39, 109696\n"
    "s_mov_b32 s38, 1605632\n"
    "buffer_load_dwordx4 v3, s[20:23], s38 offen lds\n"
    "s_add_u32 m0, s39, 110784\n"
    "s_mov_b32 s38, 1622016\n"
    "buffer_load_dwordx4 v3, s[20:23], s38 offen lds\n"
    "s_waitcnt vmcnt(24)\n"
    "s_barrier\n"
    "s_add_u32 m0, s39, 125440\n"
    "s_mov_b32 s38, 1835008\n"
    "buffer_load_dwordx4 v3, s[20:23], s38 offen lds\n"
    "s_add_u32 m0, s39, 126528\n"
    "s_mov_b32 s38, 1851392\n"
    "buffer_load_dwordx4 v3, s[20:23], s38 offen lds\n"
    "s_add_u32 m0, s39, 127616\n"
    "s_mov_b32 s38, 1867776\n"
    "buffer_load_dwordx4 v3, s[20:23], s38 offen lds\n"
    "s_add_u32 m0, s39, 128704\n"
    "s_mov_b32 s38, 1884160\n"
    "buffer_load_dwordx4 v3, s[20:23], s38 offen lds\n"
    "v_add_u32 v8, 0, v4\n"
    "v_xor_b32 v9, 32, v8\n"
    "ds_read_b64_tr_b16 a[0:1], v8 offset:0\n"
    "ds_read_b64_tr_b16 a[2:3], v8 offset:2176\n"
    "ds_read_b64_tr_b16 a[4:5], v9 offset:0\n"
    "ds_read_b64_tr_b16 a[6:7], v9 offset:2176\n"
    "ds_read_b64_tr_b16 a[128:129], v8 offset:256\n"
    "ds_read_b64_tr_b16 a[130:131], v8 offset:2432\n"
    "ds_read_b64_tr_b16 a[132:133], v9 offset:256\n"
    "ds_read_b64_tr_b16 a[134:135], v9 offset:2432\n"
    "s_waitcnt lgkmcnt(0)\n"
    "s_waitcnt vmcnt(24)\n"
    "s_barrier\n"
    "s_add_u32 m0, s39, 0\n"
    "s_mov_b32 s38, 2097152\n"
    "buffer_load_dwordx4 v3, s[20:23], s38 offen lds\n"
    "s_add_u32 m0, s39, 1088\n"
    "s_mov_b32 s38, 2113536\n"
    "buffer_load_dwordx4 v3, s[20:23], s38 offen lds\n"
    "s_add_u32 m0, s39, 2176\n"
    "s_mov_b32 s38, 2129920\n"
    "buffer_load_dwordx4 v3, s[20:23], s38 offen lds\n"
    "s_add_u32 m0, s39, 3264\n"
    "s_mov_b32 s38, 2146304\n"
    "buffer_load_dwordx4 v3, s[20:23], s38 offen lds\n"
    "v_add_u32 v8, 17920, v4\n"
    "v_xor_b32 v9, 32, v8\n"
    "ds_read_b64_tr_b16 a[8:9], v8 offset:0\n"
    "ds_read_b64_tr_b16 a[10:11], v8 offset:2176\n"
    "ds_read_b64_tr_b16 a[12:13], v9 offset:0\n"
    "ds_read_b64_tr_b16 a[14:15], v9 offset:2176\n"
    "ds_read_b64_tr_b16 a[136:137], v8 offset:256\n"
    "ds_read_b64_tr_b16 a[138:139], v8 offset:2432\n"
    "ds_read_b64_tr_b16 a[140:141], v9 offset:256\n"
    "ds_read_b64_tr_b16 a[142:143], v9 offset:2432\n"
    "s_waitcnt lgkmcnt(0)\n"
    "s_waitcnt vmcnt(24)\n"
    "s_barrier\n"
    "s_add_u32 m0, s39, 17920\n"
    "s_mov_b32 s38, 2359296\n"
    "buffer_load_dwordx4 v3, s[20:23], s38 offen lds\n"
    "s_add_u32 m0, s39, 19008\n"
    "s_mov_b32 s38, 2375680\n"
    "buffer_load_dwordx4 v3, s[20:23], s38 offen lds\n"
    "s_add_u32 m0, s39, 20096\n"
    "s_mov_b32 s38, 2392064\n"
    "buffer_load_dwordx4 v3, s[20:23], s38 offen lds\n"
    "s_add_u32 m0, s39, 21184\n"
    "s_mov_b32 s38, 2408448\n"
    "buffer_load_dwordx4 v3, s[20:23], s38 offen lds\n"
    "v_add_u32 v8, 35840, v4\n"
    "v_xor_b32 v9, 32, v8\n"
    "ds_read_b64_tr_b16 a[16:17], v8 offset:0\n"
    "ds_read_b64_tr_b16 a[18:19], v8 offset:2176\n"
    "ds_read_b64_tr_b16 a[20:21], v9 offset:0\n"
    "ds_read_b64_tr_b16 a[22:23], v9 offset:2176\n"
    "ds_read_b64_tr_b16 a[144:145], v8 offset:256\n"
    "ds_read_b64_tr_b16 a[146:147], v8 offset:2432\n"
    "ds_read_b64_tr_b16 a[148:149], v9 offset:256\n"
    "ds_read_b64_tr_b16 a[150:151], v9 offset:2432\n"
    "s_waitcnt lgkmcnt(0)\n"
    "s_waitcnt vmcnt(24)\n"
    "s_barrier\n"
    "s_add_u32 m0, s39, 35840\n"
    "s_mov_b32 s38, 2621440\n"
    "buffer_load_dwordx4 v3, s[20:23], s38 offen lds\n"
    "s_add_u32 m0, s39, 36928\n"
    "s_mov_b32 s38, 2637824\n"
    "buffer_load_dwordx4 v3, s[20:23], s38 offen lds\n"
    "s_add_u32 m0, s39, 38016\n"
    "s_mov_b32 s38, 2654208\n"
    "buffer_load_dwordx4 v3, s[20:23], s38 offen lds\n"
    "s_add_u32 m0, s39, 39104\n"
    "s_mov_b32 s38, 2670592\n"
    "buffer_load_dwordx4 v3, s[20:23], s38 offen lds\n"
    "v_add_u32 v8, 53760, v4\n"
    "v_xor_b32 v9, 32, v8\n"
    "ds_read_b64_tr_b16 a[24:25], v8 offset:0\n"
    "ds_read_b64_tr_b16 a[26:27], v8 offset:2176\n"
    "ds_read_b64_tr_b16 a[28:29], v9 offset:0\n"
    "ds_read_b64_tr_b16 a[30:31], v9 offset:2176\n"
    "ds_read_b64_tr_b16 a[152:153], v8 offset:256\n"
    "ds_read_b64_tr_b16 a[154:155], v8 offset:2432\n"
    "ds_read_b64_tr_b16 a[156:157], v9 offset:256\n"
    "ds_read_b64_tr_b16 a[158:159], v9 offset:2432\n"
    "s_waitcnt lgkmcnt(0)\n"
    "s_waitcnt vmcnt(24)\n"
    "s_barrier\n"
    "s_add_u32 m0, s39, 53760\n"
    "s_mov_b32 s38, 2883584\n"
    "buffer_load_dwordx4 v3, s[20:23], s38 offen lds\n"
    "s_add_u32 m0, s39, 54848\n"
    "s_mov_b32 s38, 2899968\n"
    "buffer_load_dwordx4 v3, s[20:23], s38 offen lds\n"
    "s_add_u32 m0, s39, 55936\n"
    "s_mov_b32 s38, 2916352\n"
    "buffer_load_dwordx4 v3, s[20:23], s38 offen lds\n"
    "s_add_u32 m0, s39, 57024\n"
    "s_mov_b32 s38, 2932736\n"
    "buffer_load_dwordx4 v3, s[20:23], s38 offen lds\n"
    "v_add_u32 v8, 71680, v4\n"
    "v_xor_b32 v9, 32, v8\n"
    "ds_read_b64_tr_b16 a[32:33], v8 offset:0\n"
    "ds_read_b64_tr_b16 a[34:35], v8 offset:2176\n"
    "ds_read_b64_tr_b16 a[36:37], v9 offset:0\n"
    "ds_read_b64_tr_b16 a[38:39], v9 offset:2176\n"
    "ds_read_b64_tr_b16 a[160:161], v8 offset:256\n"
    "ds_read_b64_tr_b16 a[162:163], v8 offset:2432\n"
    "ds_read_b64_tr_b16 a[164:165], v9 offset:256\n"
    "ds_read_b64_tr_b16 a[166:167], v9 offset:2432\n"
    "s_waitcnt lgkmcnt(0)\n"
    "s_waitcnt vmcnt(24)\n"
    "s_barrier\n"
    "global_load_dword v7, v15, s[24:25] sc1\n"
    "s_add_u32 m0, s39, 71680\n"
    "s_mov_b32 s38, 3145728\n"
    "buffer_load_dwordx4 v3, s[20:23], s38 offen lds\n"
    "s_add_u32 m0, s39, 72768\n"
    "s_mov_b32 s38, 3162112\n"
    "buffer_load_dwordx4 v3, s[20:23], s38 offen lds\n"
    "s_add_u32 m0, s39, 73856\n"
    "s_mov_b32 s38, 3178496\n"
    "buffer_load_dwordx4 v3, s[20:23], s38 offen lds\n"
    "s_add_u32 m0, s39, 74944\n"
    "s_mov_b32 s38, 3194880\n"
    "buffer_load_dwordx4 v3, s[20:23], s38 offen lds\n"
    "v_add_u32 v8, 89600, v4\n"
    "v_xor_b32 v9, 32, v8\n"
    "ds_read_b64_tr_b16 a[40:41], v8 offset:0\n"
    "ds_read_b64_tr_b16 a[42:43], v8 offset:2176\n"
    "ds_read_b64_tr_b16 a[44:45], v9 offset:0\n"
    "ds_read_b64_tr_b16 a[46:47], v9 offset:2176\n"
    "ds_read_b64_tr_b16 a[168:169], v8 offset:256\n"
    "ds_read_b64_tr_b16 a[170:171], v8 offset:2432\n"
    "ds_read_b64_tr_b16 a[172:173], v9 offset:256\n"
    "ds_read_b64_tr_b16 a[174:175], v9 offset:2432\n"
    "s_waitcnt lgkmcnt(0)\n"
    "s_waitcnt vmcnt(25)\n"
    "s_barrier\n"
    "s_add_u32 m0, s39, 89600\n"
    "s_mov_b32 s38, 3407872\n"
    "buffer_load_dwordx4 v3, s[20:23], s38 offen lds\n"
    "s_add_u32 m0, s39, 90688\n"
    "s_mov_b32 s38, 3424256\n"
    "buffer_load_dwordx4 v3, s[20:23], s38 offen lds\n"
    "s_add_u32 m0, s39, 91776\n"
    "s_mov_b32 s38, 3440640\n"
    "buffer_load_dwordx4 v3, s[20:23], s38 offen lds\n"
    "s_add_u32 m0, s39, 92864\n"
    "s_mov_b32 s38, 3457024\n"
    "buffer_load_dwordx4 v3, s[20:23], s38 offen lds\n"
    "v_add_u32 v8, 107520, v4\n"
    "v_xor_b32 v9, 32, v8\n"
    "ds_read_b64_tr_b16 a[48:49], v8 offset:0\n"
    "ds_read_b64_tr_b16 a[50:51], v8 offset:2176\n"
    "ds_read_b64_tr_b16 a[52:53], v9 offset:0\n"
    "ds_read_b64_tr_b16 a[54:55], v9 offset:2176\n"
    "ds_read_b64_tr_b16 a[176:177], v8 offset:256\n"
    "ds_read_b64_tr_b16 a[178:179], v8 offset:2432\n"
    "ds_read_b64_tr_b16 a[180:181], v9 offset:256\n"
    "ds_read_b64_tr_b16 a[182:183], v9 offset:2432\n"
    "s_waitcnt lgkmcnt(0)\n"
    "s_waitcnt vmcnt(25)\n"
    "s_barrier\n"
    "s_add_u32 m0, s39, 107520\n"
    "s_mov_b32 s38, 3670016\n"
    "buffer_load_dwordx4 v3, s[20:23], s38 offen lds\n"
    "s_add_u32 m0, s39, 108608\n"
    "s_mov_b32 s38, 3686400\n"
    "buffer_load_dwordx4 v3, s[20:23], s38 offen lds\n"
    "s_add_u32 m0, s39, 109696\n"
    "s_mov_b32 s38, 3702784\n"
    "buffer_load_dwordx4 v3, s[20:23], s38 offen lds\n"
    "s_add_u32 m0, s39, 110784\n"
    "s_mov_b32 s38, 3719168\n"
    "buffer_load_dwordx4 v3, s[20:23], s38 offen lds\n"
    "v_add_u32 v8, 125440, v4\n"
    "v_xor_b32 v9, 32, v8\n"
    "ds_read_b64_tr_b16 a[56:57], v8 offset:0\n"
    "ds_read_b64_tr_b16 a[58:59], v8 offset:2176\n"
    "ds_read_b64_tr_b16 a[60:61], v9 offset:0\n"
    "ds_read_b64_tr_b16 a[62:63], v9 offset:2176\n"
    "ds_read_b64_tr_b16 a[184:185], v8 offset:256\n"
    "ds_read_b64_tr_b16 a[186:187], v8 offset:2432\n"
    "ds_read_b64_tr_b16 a[188:189], v9 offset:256\n"
    "ds_read_b64_tr_b16 a[190:191], v9 offset:2432\n"
    "s_waitcnt lgkmcnt(0)\n"
    "s_waitcnt vmcnt(25)\n"
    "s_barrier\n"
    "s_add_u32 m0, s39, 125440\n"
    "s_mov_b32 s38, 3932160\n"
    "buffer_load_dwordx4 v3, s[20:23], s38 offen lds\n"
    "s_add_u32 m0, s39, 126528\n"
    "s_mov_b32 s38, 3948544\n"
    "buffer_load_dwordx4 v3, s[20:23], s38 offen lds\n"
    "s_add_u32 m0, s39, 127616\n"
    "s_mov_b32 s38, 3964928\n"
    "buffer_load_dwordx4 v3, s[20:23], s38 offen lds\n"
    "s_add_u32 m0, s39, 128704\n"
    "s_mov_b32 s38, 3981312\n"
    "buffer_load_dwordx4 v3, s[20:23], s38 offen lds\n"
    "v_add_u32 v8, 0, v4\n"
    "v_xor_b32 v9, 32, v8\n"
    "ds_read_b64_tr_b16 a[64:65], v8 offset:0\n"
    "ds_read_b64_tr_b16 a[66:67], v8 offset:2176\n"
    "ds_read_b64_tr_b16 a[68:69], v9 offset:0\n"
    "ds_read_b64_tr_b16 a[70:71], v9 offset:2176\n"
    "ds_read_b64_tr_b16 a[192:193], v8 offset:256\n"
    "ds_read_b64_tr_b16 a[194:195], v8 offset:2432\n"
    "ds_read_b64_tr_b16 a[196:197], v9 offset:256\n"
    "ds_read_b64_tr_b16 a[198:199], v9 offset:2432\n"
    "s_waitcnt lgkmcnt(0)\n"
    "s_waitcnt vmcnt(25)\n"
    "s_barrier\n"
    "v_add_u32 v8, 17920, v4\n"
    "v_xor_b32 v9, 32, v8\n"
    "ds_read_b64_tr_b16 a[72:73], v8 offset:0\n"
    "ds_read_b64_tr_b16 a[74:75], v8 offset:2176\n"
    "ds_read_b64_tr_b16 a[76:77], v9 offset:0\n"
    "ds_read_b64_tr_b16 a[78:79], v9 offset:2176\n"
    "ds_read_b64_tr_b16 a[200:201], v8 offset:256\n"
    "ds_read_b64_tr_b16 a[202:203], v8 offset:2432\n"
    "ds_read_b64_tr_b16 a[204:205], v9 offset:256\n"
    "ds_read_b64_tr_b16 a[206:207], v9 offset:2432\n"
    "s_waitcnt lgkmcnt(0)\n"
    "s_waitcnt vmcnt(21)\n"
    "s_barrier\n"
    "v_add_u32 v8, 35840, v4\n"
    "v_xor_b32 v9, 32, v8\n"
    "ds_read_b64_tr_b16 a[80:81], v8 offset:0\n"
    "ds_read_b64_tr_b16 a[82:83], v8 offset:2176\n"
    "ds_read_b64_tr_b16 a[84:85], v9 offset:0\n"
    "ds_read_b64_tr_b16 a[86:87], v9 offset:2176\n"
    "ds_read_b64_tr_b16 a[208:209], v8 offset:256\n"
    "ds_read_b64_tr_b16 a[210:211], v8 offset:2432\n"
    "ds_read_b64_tr_b16 a[212:213], v9 offset:256\n"
    "ds_read_b64_tr_b16 a[214:215], v9 offset:2432\n"
    "s_waitcnt lgkmcnt(0)\n"
    "s_waitcnt vmcnt(17)\n"
    "s_barrier\n"
    "v_add_u32 v8, 53760, v4\n"
    "v_xor_b32 v9, 32, v8\n"
    "ds_read_b64_tr_b16 a[88:89], v8 offset:0\n"
    "ds_read_b64_tr_b16 a[90:91], v8 offset:2176\n"
    "ds_read_b64_tr_b16 a[92:93], v9 offset:0\n"
    "ds_read_b64_tr_b16 a[94:95], v9 offset:2176\n"
    "ds_read_b64_tr_b16 a[216:217], v8 offset:256\n"
    "ds_read_b64_tr_b16 a[218:219], v8 offset:2432\n"
    "ds_read_b64_tr_b16 a[220:221], v9 offset:256\n"
    "ds_read_b64_tr_b16 a[222:223], v9 offset:2432\n"
    "s_waitcnt lgkmcnt(0)\n"
    "s_waitcnt vmcnt(12)\n"
    "s_barrier\n"
    "v_add_u32 v8, 71680, v4\n"
    "v_xor_b32 v9, 32, v8\n"
    "ds_read_b64_tr_b16 a[96:97], v8 offset:0\n"
    "ds_read_b64_tr_b16 a[98:99], v8 offset:2176\n"
    "ds_read_b64_tr_b16 a[100:101], v9 offset:0\n"
    "ds_read_b64_tr_b16 a[102:103], v9 offset:2176\n"
    "ds_read_b64_tr_b16 a[224:225], v8 offset:256\n"
    "ds_read_b64_tr_b16 a[226:227], v8 offset:2432\n"
    "ds_read_b64_tr_b16 a[228:229], v9 offset:256\n"
    "ds_read_b64_tr_b16 a[230:231], v9 offset:2432\n"
    "s_waitcnt lgkmcnt(0)\n"
    "s_waitcnt vmcnt(8)\n"
    "s_barrier\n"
    "global_store_dword v14, v7, s[24:25] sc1\n"
    "v_add_u32 v8, 89600, v4\n"
    "v_xor_b32 v9, 32, v8\n"
    "ds_read_b64_tr_b16 a[104:105], v8 offset:0\n"
    "ds_read_b64_tr_b16 a[106:107], v8 offset:2176\n"
    "ds_read_b64_tr_b16 a[108:109], v9 offset:0\n"
    "ds_read_b64_tr_b16 a[110:111], v9 offset:2176\n"
    "ds_read_b64_tr_b16 a[232:233], v8 offset:256\n"
    "ds_read_b64_tr_b16 a[234:235], v8 offset:2432\n"
    "ds_read_b64_tr_b16 a[236:237], v9 offset:256\n"
    "ds_read_b64_tr_b16 a[238:239], v9 offset:2432\n"
    "s_waitcnt lgkmcnt(0)\n"
    "s_waitcnt vmcnt(5)\n"
    "s_barrier\n"
    "v_add_u32 v8, 107520, v4\n"
    "v_xor_b32 v9, 32, v8\n"
    "ds_read_b64_tr_b16 a[112:113], v8 offset:0\n"
    "ds_read_b64_tr_b16 a[114:115], v8 offset:2176\n"
    "ds_read_b64_tr_b16 a[116:117], v9 offset:0\n"
    "ds_read_b64_tr_b16 a[118:119], v9 offset:2176\n"
    "ds_read_b64_tr_b16 a[240:241], v8 offset:256\n"
    "ds_read_b64_tr_b16 a[242:243], v8 offset:2432\n"
    "ds_read_b64_tr_b16 a[244:245], v9 offset:256\n"
    "ds_read_b64_tr_b16 a[246:247], v9 offset:2432\n"
    "s_waitcnt lgkmcnt(0)\n"
    "s_waitcnt vmcnt(1)\n"
    "s_barrier\n"
    "v_add_u32 v8, 125440, v4\n"
    "v_xor_b32 v9, 32, v8\n"
    "ds_read_b64_tr_b16 a[120:121], v8 offset:0\n"
    "ds_read_b64_tr_b16 a[122:123], v8 offset:2176\n"
    "ds_read_b64_tr_b16 a[124:125], v9 offset:0\n"
    "ds_read_b64_tr_b16 a[126:127], v9 offset:2176\n"
    "ds_read_b64_tr_b16 a[248:249], v8 offset:256\n"
    "ds_read_b64_tr_b16 a[250:251], v8 offset:2432\n"
    "ds_read_b64_tr_b16 a[252:253], v9 offset:256\n"
    "ds_read_b64_tr_b16 a[254:255], v9 offset:2432\n"
    "s_waitcnt lgkmcnt(0)\n"
    "s_mov_b32 s37, 0x80000\n"
    "LRY%=:\n"
    "global_load_dword v6, v16, s[24:25] sc1\n"
    "global_load_dword v7, v15, s[24:25] sc1\n"
    "s_waitcnt vmcnt(0)\n"
    "v_readfirstlane_b32 s60, v6\n"
    "s_cmp_lg_u32 s60, 0\n"
    "s_cbranch_scc1 LRZ%=\n"
    "global_store_dword v14, v7, s[24:25] sc1\n"
    "s_sleep 4\n"
    "s_sub_u32 s37, s37, 1\n"
    "s_cbranch_scc0 LRY%=\n"
    "s_endpgm\n"
    "LRZ%=:\n"
    "s_lshl_b32 s38, s60, 13\n"
    "s_and_b32 s38, s38, 0x2000\n"
    "s_xor_b32 s48, s48, s38\n"
    "s_xor_b32 s49, s49, s38\n"
    "s_xor_b32 s50, s50, s38\n"
    "s_xor_b32 s51, s51, s38\n"
    "s_xor_b32 s53, s53, s38\n"
    "s_xor_b32 s26, s26, s38\n"
    "s_barrier\n"
    "v_bfe_u32 v9, v0, 4, 1\n"
    "v_lshlrev_b32 v4, 8, v9\n"
    "v_lshlrev_b32 v9, 14, v9\n"
    "s_mov_b32 s63, s45\n"
    "s_lshr_b32 s38, s63, 8\n"
    "s_xor_b32 s38, s38, s48\n"
    "s_and_b32 s38, s38, 0x2000\n"
    "s_or_b32 s63, s63, s38\n"
    "s_add_u32 s39, s44, s43\n"
    "s_add_u32 s69, s63, 4096\n"
    "s_add_u32 m0, s39, 0\n"
    "s_nop 0\n"
    "buffer_load_dwordx4 v0, s[16:19], s63 offen offset:0 sc1 lds\n"
    "s_add_u32 m0, s39, 32\n"
    "s_nop 0\n"
    "buffer_load_dwordx4 v0, s[16:19], s63 offen offset:1024 sc1 lds\n"
    "s_add_u32 m0, s39, 64\n"
    "s_nop 0\n"
    "buffer_load_dwordx4 v0, s[16:19], s63 offen offset:2048 sc1 lds\n"
    "s_add_u32 m0, s39, 96\n"
    "s_nop 0\n"
    "buffer_load_dwordx4 v0, s[16:19], s63 offen offset:3072 sc1 lds\n"
    "s_add_u32 m0, s39, 4224\n"
    "s_nop 0\n"
    "buffer_load_dwordx4 v0, s[16:19], s69 offen offset:0 sc1 lds\n"
    "s_add_u32 m0, s39, 4256\n"
    "s_nop 0\n"
    "buffer_load_dwordx4 v0, s[16:19], s69 offen offset:1024 sc1 lds\n"
    "s_add_u32 m0, s39, 4288\n"
    "s_nop 0\n"
    "buffer_load_dwordx4 v0, s[16:19], s69 offen offset:2048 sc1 lds\n"
    "s_add_u32 m0, s39, 4320\n"
    "s_nop 0\n"
    "buffer_load_dwordx4 v0, s[16:19], s69 offen offset:3072 sc1 lds\n"
    "s_add_u32 s45, s45, 131072\n"
    "v_mov_b32 v10, 101376\n"
    "v_lshl_add_u32 v11, s36, 3, v10\n"
    "LT%=:\n"
    "s_cmp_lg_u32 s36, 0\n"
    "s_cbranch_scc1 LB%=\n"
    "s_mov_b32 s37, 0x80000\n"
    "LP%=:\n"
    "buffer_load_dword v6, off, s[16:19], s53 sc1\n"
    "s_waitcnt vmcnt(0)\n"
    "v_readfirstlane_b32 s38, v6\n"
    "s_mov_b32 s60, 3\n"
    "s_cmp_ge_u32 s41, 64\n"
    "s_cbranch_scc1 LD%=\n"
    "s_mov_b32 s60, 2\n"
    "s_cmp_lg_u32 s38, 0\n"
    "s_cbranch_scc1 LD%=\n"
    "s_add_u32 s38, s41, 8\n"
    "s_cmp_lt_u32 s41, 2\n"
    "s_cselect_b32 s38, 12, s38\n"
    "s_min_u32 s38, s38, 64\n"
    "s_mov_b32 s60, 1\n"
    "s_cmp_lt_u32 s40, s38\n"
    "s_cbranch_scc1 LD%=\n"
    "s_mov_b32 s60, 0\n"
    "s_cmp_lt_u32 s52, 0x80000000\n"
    "s_cbranch_scc1 LD%=\n"
    "s_sleep 1\n"
    "s_sub_u32 s37, s37, 1\n"
    "s_cbranch_scc0 LP%=\n"
    "buffer_store_dword v10, off, s[16:19], s58 sc1\n"
    "s_mov_b32 s60, 3\n"
    "LD%=:\n"
    "v_mov_b32 v6, s60\n"
    "ds_write_b32 v10, v6\n"
    "LB%=:\n"
    "s_waitcnt vmcnt(0) lgkmcnt(0)\n"
    "s_barrier\n"
    "ds_read_b32 v6, v10\n"
    "v_readfirstlane_b32 s38, v5\n"
    "s_or_b32 s52, s52, s68\n"
    "s_or_b32 s38, s38, s68\n"
    "s_cbranch_scc0 LK0_%=\n"
    "LJ0_%=:\n"
    "buffer_store_dword v5, v9, s[16:19], s52 offen sc1\n"
    "s_mov_b32 s52, 0x80000000\n"
    "s_lshl_b32 s63, s41, 16\n"
    "s_add_u32 s63, s63, s46\n"
    "s_lshr_b32 s38, s63, 8\n"
    "s_xor_b32 s38, s38, s48\n"
    "s_and_b32 s38, s38, 0x2000\n"
    "s_or_b32 s63, s63, s38\n"
    "s_waitcnt lgkmcnt(0)\n"
    "v_readfirstlane_b32 s60, v6\n"
    "s_cmp_eq_u32 s60, 1\n"
    "s_cbranch_scc1 L1%=\n"
    "s_cmp_eq_u32 s60, 2\n"
    "s_cbranch_scc0 LX%=\n"
    "s_add_u32 s39, s44, 67584\n"
    "s_add_u32 s69, s63, 4096\n"
    "s_add_u32 m0, s39, 0\n"
    "s_nop 0\n"
    "buffer_load_dwordx4 v0, s[16:19], s63 offen offset:0 sc1 lds\n"
    "s_add_u32 m0, s39, 32\n"
    "s_nop 0\n"
    "buffer_load_dwordx4 v0, s[16:19], s63 offen offset:1024 sc1 lds\n"
    "s_add_u32 m0, s39, 64\n"
    "s_nop 0\n"
    "buffer_load_dwordx4 v0, s[16:19], s63 offen offset:2048 sc1 lds\n"
    "s_add_u32 m0, s39, 96\n"
    "s_nop 0\n"
    "buffer_load_dwordx4 v0, s[16:19], s63 offen offset:3072 sc1 lds\n"
    "s_add_u32 m0, s39, 4224\n"
    "s_nop 0\n"
    "buffer_load_dwordx4 v0, s[16:19], s69 offen offset:0 sc1 lds\n"
    "s_add_u32 m0, s39, 4256\n"
    "s_nop 0\n"
    "buffer_load_dwordx4 v0, s[16:19], s69 offen offset:1024 sc1 lds\n"
    "s_add_u32 m0, s39, 4288\n"
    "s_nop 0\n"
    "buffer_load_dwordx4 v0, s[16:19], s69 offen offset:2048 sc1 lds\n"
    "s_add_u32 m0, s39, 4320\n"
    "s_nop 0\n"
    "buffer_load_dwordx4 v0, s[16:19], s69 offen offset:3072 sc1 lds\n"
    "s_waitcnt vmcnt(0)\n"
    "L2%=:\n"
    "s_mul_i32 s59, s41, 2621440\n"
    "s_add_u32 s59, s59, s47\n"
    "s_lshr_b32 s38, s59, 8\n"
    "s_xor_b32 s38, s38, s49\n"
    "s_and_b32 s38, s38, 0x2000\n"
    "s_or_b32 s59, s59, s38\n"
    "s_lshl_b32 s64, s41, 7\n"
    "s_add_u32 s64, s64, s51\n"
    "s_lshl_b32 s67, s41, 2\n"
    "s_add_u32 s67, s67, s26\n"
    "s_add_u32 s41, s41, 2\n"
    "s_add_u32 s53, s53, 8\n"
    "s_mov_b32 s60, 2\n"
    "LG%=:\n"
    "v_add_u32 v8, 67584, v1\n"
    "s_waitcnt vmcnt(2)\n"
    "s_barrier\n"
    "s_cmp_eq_u32 s61, 1\n"
    "s_cbranch_scc1 LHV%=\n"
    "s_add_u32 s62, s59, 4096\n"
    "ds_read_b128 v[20:23], v8 offset:0\n"
    "ds_read_b128 v[24:27], v8 offset:16896\n"
    "ds_read_b128 v[28:31], v8 offset:64\n"
    "ds_read_b128 v[32:35], v8 offset:16960\n"
    "ds_read_b128 v[36:39], v8 offset:128\n"
    "ds_read_b128 v[40:43], v8 offset:17024\n"
    "ds_read_b128 v[44:47], v8 offset:192\n"
    "ds_read_b128 v[48:51], v8 offset:17088\n"
    "s_waitcnt lgkmcnt(7)\n"
    "v_mfma_f32_16x16x32_bf16 v[64:67], a[0:3], v[20:23], 0\n"
    "ds_read_b128 v[52:55], v8 offset:256\n"
    "s_waitcnt lgkmcnt(7)\n"
    "v_mfma_f32_16x16x32_bf16 v[68:71], a[0:3], v[24:27], 0\n"
    "ds_read_b128 v[56:59], v8 offset:17152\n"
    "v_max3_u32 v6, v23, v27, v23\n"
    "v_mfma_f32_16x16x32_bf16 v[76:79], a[4:7], v[24:27], 0\n"
    "v_mfma_f32_16x16x32_bf16 v[72:75], a[4:7], v[20:23], 0\n"
    "s_waitcnt lgkmcnt(7)\n"
    "v_mfma_f32_16x16x32_bf16 v[64:67], a[8:11], v[28:31], v[64:67]\n"
    "ds_read_b128 v[20:23], v8 offset:320\n"
    "s_waitcnt lgkmcnt(7)\n"
    "v_mfma_f32_16x16x32_bf16 v[68:71], a[8:11], v[32:35], v[68:71]\n"
    "ds_read_b128 v[24:27], v8 offset:17216\n"
    "v_max3_u32 v6, v31, v35, v6\n"
    "v_mfma_f32_16x16x32_bf16 v[76:79], a[12:15], v[32:35], v[76:79]\n"
    "v_mfma_f32_16x16x32_bf16 v[72:75], a[12:15], v[28:31], v[72:75]\n"
    "s_waitcnt lgkmcnt(7)\n"
    "v_mfma_f32_16x16x32_bf16 v[64:67], a[16:19], v[36:39], v[64:67]\n"
    "ds_read_b128 v[28:31], v8 offset:384\n"
    "s_waitcnt lgkmcnt(7)\n"
    "v_mfma_f32_16x16x32_bf16 v[68:71], a[16:19], v[40:43], v[68:71]\n"
    "ds_read_b128 v[32:35], v8 offset:17280\n"
    "v_max3_u32 v6, v39, v43, v6\n"
    "v_mfma_f32_16x16x32_bf16 v[76:79], a[20:23], v[40:43], v[76:79]\n"
    "v_mfma_f32_16x16x32_bf16 v[72:75], a[20:23], v[36:39], v[72:75]\n"
    "s_waitcnt lgkmcnt(7)\n"
    "v_mfma_f32_16x16x32_bf16 v[64:67], a[24:27], v[44:47], v[64:67]\n"
    "ds_read_b128 v[36:39], v8 offset:448\n"
    "s_waitcnt lgkmcnt(7)\n"
    "v_mfma_f32_16x16x32_bf16 v[68:71], a[24:27], v[48:51], v[68:71]\n"
    "ds_read_b128 v[40:43], v8 offset:17344\n"
    "v_max3_u32 v6, v47, v51, v6\n"
    "v_mfma_f32_16x16x32_bf16 v[76:79], a[28:31], v[48:51], v[76:79]\n"
    "v_mfma_f32_16x16x32_bf16 v[72:75], a[28:31], v[44:47], v[72:75]\n"
    "s_waitcnt lgkmcnt(7)\n"
    "v_mfma_f32_16x16x32_bf16 v[64:67], a[32:35], v[52:55], v[64:67]\n"
    "ds_read_b128 v[44:47], v8 offset:512\n"
    "s_waitcnt lgkmcnt(7)\n"
    "v_mfma_f32_16x16x32_bf16 v[68:71], a[32:35], v[56:59], v[68:71]\n"
    "ds_read_b128 v[48:51], v8 offset:17408\n"
    "v_max3_u32 v6, v55, v59, v6\n"
    "v_mfma_f32_16x16x32_bf16 v[76:79], a[36:39], v[56:59], v[76:79]\n"
    "v_mfma_f32_16x16x32_bf16 v[72:75], a[36:39], v[52:55], v[72:75]\n"
    "s_waitcnt lgkmcnt(7)\n"
    "v_mfma_f32_16x16x32_bf16 v[64:67], a[40:43], v[20:23], v[64:67]\n"
    "ds_read_b128 v[52:55], v8 offset:576\n"
    "s_waitcnt lgkmcnt(7)\n"
    "v_mfma_f32_16x16x32_bf16 v[68:71], a[40:43], v[24:27], v[68:71]\n"
    "ds_read_b128 v[56:59], v8 offset:17472\n"
    "v_max3_u32 v6, v23, v27, v6\n"
    "v_mfma_f32_16x16x32_bf16 v[76:79], a[44:47], v[24:27], v[76:79]\n"
    "v_mfma_f32_16x16x32_bf16 v[72:75], a[44:47], v[20:23], v[72:75]\n"
    "s_waitcnt lgkmcnt(7)\n"
    "v_mfma_f32_16x16x32_bf16 v[64:67], a[48:51], v[28:31], v[64:67]\n"
    "ds_read_b128 v[20:23], v8 offset:640\n"
    "s_waitcnt lgkmcnt(7)\n"
    "v_mfma_f32_16x16x32_bf16 v[68:71], a[48:51], v[32:35], v[68:71]\n"
    "ds_read_b128 v[24:27], v8 offset:17536\n"
    "v_max3_u32 v6, v31, v35, v6\n"
    "v_mfma_f32_16x16x32_bf16 v[76:79], a[52:55], v[32:35], v[76:79]\n"
    "v_mfma_f32_16x16x32_bf16 v[72:75], a[52:55], v[28:31], v[72:75]\n"
    "s_waitcnt lgkmcnt(7)\n"
    "v_mfma_f32_16x16x32_bf16 v[64:67], a[56:59], v[36:39], v[64:67]\n"
    "ds_read_b128 v[28:31], v8 offset:704\n"
    "s_waitcnt lgkmcnt(7)\n"
    "v_mfma_f32_16x16x32_bf16 v[68:71], a[56:59], v[40:43], v[68:71]\n"
    "ds_read_b128 v[32:35], v8 offset:17600\n"
    "v_max3_u32 v6, v39, v43, v6\n"
    "v_mfma_f32_16x16x32_bf16 v[76:79], a[60:63], v[40:43], v[76:79]\n"
    "v_mfma_f32_16x16x32_bf16 v[72:75], a[60:63], v[36:39], v[72:75]\n"
    "s_waitcnt lgkmcnt(7)\n"
    "v_mfma_f32_16x16x32_bf16 v[64:67], a[64:67], v[44:47], v[64:67]\n"
    "ds_read_b128 v[36:39], v8 offset:768\n"
    "s_waitcnt lgkmcnt(7)\n"
    "v_mfma_f32_16x16x32_bf16 v[68:71], a[64:67], v[48:51], v[68:71]\n"
    "ds_read_b128 v[40:43], v8 offset:17664\n"
    "v_max3_u32 v6, v47, v51, v6\n"
    "v_mfma_f32_16x16x32_bf16 v[76:79], a[68:71], v[48:51], v[76:79]\n"
    "v_mfma_f32_16x16x32_bf16 v[72:75], a[68:71], v[44:47], v[72:75]\n"
    "s_waitcnt lgkmcnt(7)\n"
    "v_mfma_f32_16x16x32_bf16 v[64:67], a[72:75], v[52:55], v[64:67]\n"
    "ds_read_b128 v[44:47], v8 offset:832\n"
    "s_waitcnt lgkmcnt(7)\n"
    "v_mfma_f32_16x16x32_bf16 v[68:71], a[72:75], v[56:59], v[68:71]\n"
    "ds_read_b128 v[48:51], v8 offset:17728\n"
    "v_max3_u32 v6, v55, v59, v6\n"
    "v_mfma_f32_16x16x32_bf16 v[76:79], a[76:79], v[56:59], v[76:79]\n"
    "v_mfma_f32_16x16x32_bf16 v[72:75], a[76:79], v[52:55], v[72:75]\n"
    "s_waitcnt lgkmcnt(7)\n"
    "v_mfma_f32_16x16x32_bf16 v[64:67], a[80:83], v[20:23], v[64:67]\n"
    "ds_read_b128 v[52:55], v8 offset:896\n"
    "s_waitcnt lgkmcnt(7)\n"
    "v_mfma_f32_16x16x32_bf16 v[68:71], a[80:83], v[24:27], v[68:71]\n"
    "ds_read_b128 v[56:59], v8 offset:17792\n"
    "v_max3_u32 v6, v23, v27, v6\n"
    "v_mfma_f32_16x16x32_bf16 v[76:79], a[84:87], v[24:27], v[76:79]\n"
    "v_mfma_f32_16x16x32_bf16 v[72:75], a[84:87], v[20:23], v[72:75]\n"
    "s_waitcnt lgkmcnt(7)\n"
    "v_mfma_f32_16x16x32_bf16 v[64:67], a[88:91], v[28:31], v[64:67]\n"
    "ds_read_b128 v[20:23], v8 offset:960\n"
    "s_waitcnt lgkmcnt(7)\n"
    "v_mfma_f32_16x16x32_bf16 v[68:71], a[88:91], v[32:35], v[68:71]\n"
    "ds_read_b128 v[24:27], v8 offset:17856\n"
    "v_max3_u32 v6, v31, v35, v6\n"
    "v_mfma_f32_16x16x32_bf16 v[76:79], a[92:95], v[32:35], v[76:79]\n"
    "v_mfma_f32_16x16x32_bf16 v[72:75], a[92:95], v[28:31], v[72:75]\n"
    "s_waitcnt lgkmcnt(7)\n"
    "v_mfma_f32_16x16x32_bf16 v[64:67], a[96:99], v[36:39], v[64:67]\n"
    "ds_read_b128 v[28:31], v8 offset:0\n"
    "s_waitcnt lgkmcnt(7)\n"
    "v_mfma_f32_16x16x32_bf16 v[68:71], a[96:99], v[40:43], v[68:71]\n"
    "ds_read_b128 v[32:35], v8 offset:16896\n"
    "s_waitcnt vmcnt(0)\n"
    "s_barrier\n"
    "v_readfirstlane_b32 s38, v5\n"
    "s_or_b32 s52, s52, s68\n"
    "s_or_b32 s38, s38, s68\n"
    "s_cbranch_scc0 LK1_%=\n"
    "LJ1_%=:\n"
    "buffer_store_dword v5, v9, s[16:19], s52 offen sc1\n"
    "s_mov_b32 s52, 0x80000000\n"
    "v_mfma_f32_16x16x32_bf16 v[76:79], a[100:103], v[40:43], v[76:79]\n"
    "v_max3_u32 v6, v39, v43, v6\n"
    "v_mfma_f32_16x16x32_bf16 v[72:75], a[100:103], v[36:39], v[72:75]\n"
    "s_waitcnt lgkmcnt(7)\n"
    "v_mfma_f32_16x16x32_bf16 v[64:67], a[104:107], v[44:47], v[64:67]\n"
    "ds_read_b128 v[36:39], v8 offset:64\n"
    "s_waitcnt lgkmcnt(7)\n"
    "v_mfma_f32_16x16x32_bf16 v[68:71], a[104:107], v[48:51], v[68:71]\n"
    "ds_read_b128 v[40:43], v8 offset:16960\n"
    "v_max3_u32 v6, v47, v51, v6\n"
    "v_mfma_f32_16x16x32_bf16 v[76:79], a[108:111], v[48:51], v[76:79]\n"
    "v_mfma_f32_16x16x32_bf16 v[72:75], a[108:111], v[44:47], v[72:75]\n"
    "s_waitcnt lgkmcnt(7)\n"
    "v_mfma_f32_16x16x32_bf16 v[64:67], a[112:115], v[52:55], v[64:67]\n"
    "ds_read_b128 v[44:47], v8 offset:128\n"
    "s_waitcnt lgkmcnt(7)\n"
    "v_mfma_f32_16x16x32_bf16 v[68:71], a[112:115], v[56:59], v[68:71]\n"
    "ds_read_b128 v[48:51], v8 offset:17024\n"
    "v_max3_u32 v6, v55, v59, v6\n"
    "v_mfma_f32_16x16x32_bf16 v[76:79], a[116:119], v[56:59], v[76:79]\n"
    "v_mfma_f32_16x16x32_bf16 v[72:75], a[116:119], v[52:55], v[72:75]\n"
    "s_waitcnt lgkmcnt(7)\n"
    "v_mfma_f32_16x16x32_bf16 v[64:67], a[120:123], v[20:23], v[64:67]\n"
    "ds_read_b128 v[52:55], v8 offset:192\n"
    "s_waitcnt lgkmcnt(7)\n"
    "v_mfma_f32_16x16x32_bf16 v[68:71], a[120:123], v[24:27], v[68:71]\n"
    "ds_read_b128 v[56:59], v8 offset:17088\n"
    "v_max3_u32 v6, v23, v27, v6\n"
    "v_mfma_f32_16x16x32_bf16 v[76:79], a[124:127], v[24:27], v[76:79]\n"
    "v_mfma_f32_16x16x32_bf16 v[72:75], a[124:127], v[20:23], v[72:75]\n"
    "s_waitcnt lgkmcnt(7)\n"
    "v_mfma_f32_16x16x32_bf16 v[80:83], a[128:131], v[28:31], 0\n"
    "ds_read_b128 v[20:23], v8 offset:256\n"
    "s_waitcnt lgkmcnt(7)\n"
    "v_mfma_f32_16x16x32_bf16 v[84:87], a[128:131], v[32:35], 0\n"
    "ds_read_b128 v[24:27], v8 offset:17152\n"
    "v_cmp_eq_u32 vcc, -1, v6\n"
    "v_mfma_f32_16x16x32_bf16 v[92:95], a[132:135], v[32:35], 0\n"
    "s_cbranch_vccnz LRD%=\n"
    "v_mfma_f32_16x16x32_bf16 v[88:91], a[132:135], v[28:31], 0\n"
    "s_waitcnt lgkmcnt(7)\n"
    "v_mfma_f32_16x16x32_bf16 v[80:83], a[136:139], v[36:39], v[80:83]\n"
    "ds_read_b128 v[28:31], v8 offset:320\n"
    "s_waitcnt lgkmcnt(7)\n"
    "v_mfma_f32_16x16x32_bf16 v[84:87], a[136:139], v[40:43], v[84:87]\n"
    "ds_read_b128 v[32:35], v8 offset:17216\n"
    "v_cvt_pk_bf16_f32 v12, v64, v72\n"
    "v_mfma_f32_16x16x32_bf16 v[92:95], a[140:143], v[40:43], v[92:95]\n"
    "v_cvt_pk_bf16_f32 v13, v65, v73\n"
    "v_mfma_f32_16x16x32_bf16 v[88:91], a[140:143], v[36:39], v[88:91]\n"
    "s_waitcnt lgkmcnt(7)\n"
    "v_cvt_pk_bf16_f32 v14, v66, v74\n"
    "v_mfma_f32_16x16x32_bf16 v[80:83], a[144:147], v[44:47], v[80:83]\n"
    "ds_read_b128 v[36:39], v8 offset:384\n"
    "s_waitcnt lgkmcnt(7)\n"
    "v_mfma_f32_16x16x32_bf16 v[84:87], a[144:147], v[48:51], v[84:87]\n"
    "ds_read_b128 v[40:43], v8 offset:17280\n"
    "v_cvt_pk_bf16_f32 v15, v67, v75\n"
    "v_mfma_f32_16x16x32_bf16 v[92:95], a[148:151], v[48:51], v[92:95]\n"
    "buffer_store_dwordx4 v[12:15], v0, s[16:19], s59 offen sc1\n"
    "v_mfma_f32_16x16x32_bf16 v[88:91], a[148:151], v[44:47], v[88:91]\n"
    "s_waitcnt lgkmcnt(7)\n"
    "v_cvt_pk_bf16_f32 v16, v68, v76\n"
    "v_mfma_f32_16x16x32_bf16 v[80:83], a[152:155], v[52:55], v[80:83]\n"
    "ds_read_b128 v[44:47], v8 offset:448\n"
    "s_waitcnt lgkmcnt(7)\n"
    "v_mfma_f32_16x16x32_bf16 v[84:87], a[152:155], v[56:59], v[84:87]\n"
    "ds_read_b128 v[48:51], v8 offset:17344\n"
    "v_cvt_pk_bf16_f32 v17, v69, v77\n"
    "v_mfma_f32_16x16x32_bf16 v[92:95], a[156:159], v[56:59], v[92:95]\n"
    "v_cvt_pk_bf16_f32 v18, v70, v78\n"
    "v_mfma_f32_16x16x32_bf16 v[88:91], a[156:159], v[52:55], v[88:91]\n"
    "s_waitcnt lgkmcnt(7)\n"
    "v_cvt_pk_bf16_f32 v19, v71, v79\n"
    "v_mfma_f32_16x16x32_bf16 v[80:83], a[160:163], v[20:23], v[80:83]\n"
    "ds_read_b128 v[52:55], v8 offset:512\n"
    "s_waitcnt lgkmcnt(7)\n"
    "v_mfma_f32_16x16x32_bf16 v[84:87], a[160:163], v[24:27], v[84:87]\n"
    "ds_read_b128 v[56:59], v8 offset:17408\n"
    "buffer_store_dwordx4 v[16:19], v0, s[16:19], s62 offen sc1\n"
    "v_mfma_f32_16x16x32_bf16 v[92:95], a[164:167], v[24:27], v[92:95]\n"
    "s_add_u32 s59, s59, 16384\n"
    "s_add_u32 s62, s62, 16384\n"
    "v_mfma_f32_16x16x32_bf16 v[88:91], a[164:167], v[20:23], v[88:91]\n"
    "s_waitcnt lgkmcnt(7)\n"
    "v_mfma_f32_16x16x32_bf16 v[80:83], a[168:171], v[28:31], v[80:83]\n"
    "ds_read_b128 v[20:23], v8 offset:576\n"
    "s_waitcnt lgkmcnt(7)\n"
    "v_mfma_f32_16x16x32_bf16 v[84:87], a[168:171], v[32:35], v[84:87]\n"
    "ds_read_b128 v[24:27], v8 offset:17472\n"
    "v_mfma_f32_16x16x32_bf16 v[92:95], a[172:175], v[32:35], v[92:95]\n"
    "v_mfma_f32_16x16x32_bf16 v[88:91], a[172:175], v[28:31], v[88:91]\n"
    "s_waitcnt lgkmcnt(7)\n"
    "v_mfma_f32_16x16x32_bf16 v[80:83], a[176:179], v[36:39], v[80:83]\n"
    "ds_read_b128 v[28:31], v8 offset:640\n"
    "s_waitcnt lgkmcnt(7)\n"
    "v_mfma_f32_16x16x32_bf16 v[84:87], a[176:179], v[40:43], v[84:87]\n"
    "ds_read_b128 v[32:35], v8 offset:17536\n"
    "v_mfma_f32_16x16x32_bf16 v[92:95], a[180:183], v[40:43], v[92:95]\n"
    "v_mfma_f32_16x16x32_bf16 v[88:91], a[180:183], v[36:39], v[88:91]\n"
    "s_waitcnt lgkmcnt(7)\n"
    "v_mfma_f32_16x16x32_bf16 v[80:83], a[184:187], v[44:47], v[80:83]\n"
    "ds_read_b128 v[36:39], v8 offset:704\n"
    "s_waitcnt lgkmcnt(7)\n"
    "v_mfma_f32_16x16x32_bf16 v[84:87], a[184:187], v[48:51], v[84:87]\n"
    "ds_read_b128 v[40:43], v8 offset:17600\n"
    "v_mfma_f32_16x16x32_bf16 v[92:95], a[188:191], v[48:51], v[92:95]\n"
    "v_mfma_f32_16x16x32_bf16 v[88:91], a[188:191], v[44:47], v[88:91]\n"
    "s_waitcnt lgkmcnt(7)\n"
    "v_mfma_f32_16x16x32_bf16 v[80:83], a[192:195], v[52:55], v[80:83]\n"
    "ds_read_b128 v[44:47], v8 offset:768\n"
    "s_waitcnt lgkmcnt(7)\n"
    "v_mfma_f32_16x16x32_bf16 v[84:87], a[192:195], v[56:59], v[84:87]\n"
    "ds_read_b128 v[48:51], v8 offset:17664\n"
    "buffer_load_dword v5, off, s[16:19], s67 sc1\n"
    "v_mfma_f32_16x16x32_bf16 v[92:95], a[196:199], v[56:59], v[92:95]\n"
    "v_mfma_f32_16x16x32_bf16 v[88:91], a[196:199], v[52:55], v[88:91]\n"
    "s_waitcnt lgkmcnt(7)\n"
    "v_mfma_f32_16x16x32_bf16 v[80:83], a[200:203], v[20:23], v[80:83]\n"
    "ds_read_b128 v[52:55], v8 offset:832\n"
    "s_waitcnt lgkmcnt(7)\n"
    "v_mfma_f32_16x16x32_bf16 v[84:87], a[200:203], v[24:27], v[84:87]\n"
    "ds_read_b128 v[56:59], v8 offset:17728\n"
    "v_mfma_f32_16x16x32_bf16 v[92:95], a[204:207], v[24:27], v[92:95]\n"
    "v_mfma_f32_16x16x32_bf16 v[88:91], a[204:207], v[20:23], v[88:91]\n"
    "s_waitcnt lgkmcnt(7)\n"
    "v_mfma_f32_16x16x32_bf16 v[80:83], a[208:211], v[28:31], v[80:83]\n"
    "ds_read_b128 v[20:23], v8 offset:896\n"
    "s_waitcnt lgkmcnt(7)\n"
    "v_mfma_f32_16x16x32_bf16 v[84:87], a[208:211], v[32:35], v[84:87]\n"
    "ds_read_b128 v[24:27], v8 offset:17792\n"
    "v_mfma_f32_16x16x32_bf16 v[92:95], a[212:215], v[32:35], v[92:95]\n"
    "v_mfma_f32_16x16x32_bf16 v[88:91], a[212:215], v[28:31], v[88:91]\n"
    "s_waitcnt lgkmcnt(7)\n"
    "v_mfma_f32_16x16x32_bf16 v[80:83], a[216:219], v[36:39], v[80:83]\n"
    "ds_read_b128 v[28:31], v8 offset:960\n"
    "s_waitcnt lgkmcnt(7)\n"
    "v_mfma_f32_16x16x32_bf16 v[84:87], a[216:219], v[40:43], v[84:87]\n"
    "ds_read_b128 v[32:35], v8 offset:17856\n"
    "v_mfma_f32_16x16x32_bf16 v[92:95], a[220:223], v[40:43], v[92:95]\n"
    "v_mfma_f32_16x16x32_bf16 v[88:91], a[220:223], v[36:39], v[88:91]\n"
    "s_waitcnt lgkmcnt(7)\n"
    "v_mfma_f32_16x16x32_bf16 v[80:83], a[224:227], v[44:47], v[80:83]\n"
    "s_waitcnt lgkmcnt(6)\n"
    "v_mfma_f32_16x16x32_bf16 v[84:87], a[224:227], v[48:51], v[84:87]\n"
    "v_mfma_f32_16x16x32_bf16 v[92:95], a[228:231], v[48:51], v[92:95]\n"
    "v_mfma_f32_16x16x32_bf16 v[88:91], a[228:231], v[44:47], v[88:91]\n"
    "s_waitcnt lgkmcnt(5)\n"
    "v_mfma_f32_16x16x32_bf16 v[80:83], a[232:235], v[52:55], v[80:83]\n"
    "s_waitcnt lgkmcnt(4)\n"
    "v_mfma_f32_16x16x32_bf16 v[84:87], a[232:235], v[56:59], v[84:87]\n"
    "v_mfma_f32_16x16x32_bf16 v[92:95], a[236:239], v[56:59], v[92:95]\n"
    "v_mfma_f32_16x16x32_bf16 v[88:91], a[236:239], v[52:55], v[88:91]\n"
    "s_waitcnt lgkmcnt(3)\n"
    "v_mfma_f32_16x16x32_bf16 v[80:83], a[240:243], v[20:23], v[80:83]\n"
    "s_waitcnt lgkmcnt(2)\n"
    "v_mfma_f32_16x16x32_bf16 v[84:87], a[240:243], v[24:27], v[84:87]\n"
    "v_mfma_f32_16x16x32_bf16 v[92:95], a[244:247], v[24:27], v[92:95]\n"
    "v_mfma_f32_16x16x32_bf16 v[88:91], a[244:247], v[20:23], v[88:91]\n"
    "s_waitcnt lgkmcnt(1)\n"
    "v_mfma_f32_16x16x32_bf16 v[80:83], a[248:251], v[28:31], v[80:83]\n"
    "s_waitcnt lgkmcnt(0)\n"
    "v_mfma_f32_16x16x32_bf16 v[84:87], a[248:251], v[32:35], v[84:87]\n"
    "v_mfma_f32_16x16x32_bf16 v[92:95], a[252:255], v[32:35], v[92:95]\n"
    "v_mfma_f32_16x16x32_bf16 v[88:91], a[252:255], v[28:31], v[88:91]\n"
    "s_nop 7\n"
    "v_cvt_pk_bf16_f32 v12, v80, v88\n"
    "v_cvt_pk_bf16_f32 v13, v81, v89\n"
    "v_cvt_pk_bf16_f32 v14, v82, v90\n"
    "v_cvt_pk_bf16_f32 v15, v83, v91\n"
    "buffer_store_dwordx4 v[12:15], v0, s[16:19], s59 offen sc1\n"
    "v_cvt_pk_bf16_f32 v16, v84, v92\n"
    "v_cvt_pk_bf16_f32 v17, v85, v93\n"
    "v_cvt_pk_bf16_f32 v18, v86, v94\n"
    "v_cvt_pk_bf16_f32 v19, v87, v95\n"
    "buffer_store_dwordx4 v[16:19], v0, s[16:19], s62 offen sc1\n"
    "s_mov_b32 s52, s64\n"
    "s_branch LHE%=\n"
    "LHV%=:\n"
    "s_add_u32 s62, s59, 1024\n"
    "ds_read_b128 v[20:23], v8 offset:0\n"
    "ds_read_b128 v[24:27], v8 offset:16896\n"
    "ds_read_b128 v[28:31], v8 offset:64\n"
    "ds_read_b128 v[32:35], v8 offset:16960\n"
    "ds_read_b128 v[36:39], v8 offset:128\n"
    "ds_read_b128 v[40:43], v8 offset:17024\n"
    "ds_read_b128 v[44:47], v8 offset:192\n"
    "ds_read_b128 v[48:51], v8 offset:17088\n"
    "s_waitcnt lgkmcnt(7)\n"
    "v_mfma_f32_16x16x32_bf16 v[64:67], v[20:23], a[0:3], 0\n"
    "ds_read_b128 v[52:55], v8 offset:256\n"
    "s_waitcnt lgkmcnt(7)\n"
    "v_mfma_f32_16x16x32_bf16 v[68:71], v[24:27], a[0:3], 0\n"
    "ds_read_b128 v[56:59], v8 offset:17152\n"
    "v_max3_u32 v6, v23, v27, v23\n"
    "v_mfma_f32_16x16x32_bf16 v[76:79], v[24:27], a[4:7], 0\n"
    "v_mfma_f32_16x16x32_bf16 v[72:75], v[20:23], a[4:7], 0\n"
    "s_waitcnt lgkmcnt(7)\n"
    "v_mfma_f32_16x16x32_bf16 v[64:67], v[28:31], a[8:11], v[64:67]\n"
    "ds_read_b128 v[20:23], v8 offset:320\n"
    "s_waitcnt lgkmcnt(7)\n"
    "v_mfma_f32_16x16x32_bf16 v[68:71], v[32:35], a[8:11], v[68:71]\n"
    "ds_read_b128 v[24:27], v8 offset:17216\n"
    "v_max3_u32 v6, v31, v35, v6\n"
    "v_mfma_f32_16x16x32_bf16 v[76:79], v[32:35], a[12:15], v[76:79]\n"
    "v_mfma_f32_16x16x32_bf16 v[72:75], v[28:31], a[12:15], v[72:75]\n"
    "s_waitcnt lgkmcnt(7)\n"
    "v_mfma_f32_16x16x32_bf16 v[64:67], v[36:39], a[16:19], v[64:67]\n"
    "ds_read_b128 v[28:31], v8 offset:384\n"
    "s_waitcnt lgkmcnt(7)\n"
    "v_mfma_f32_16x16x32_bf16 v[68:71], v[40:43], a[16:19], v[68:71]\n"
    "ds_read_b128 v[32:35], v8 offset:17280\n"
    "v_max3_u32 v6, v39, v43, v6\n"
    "v_mfma_f32_16x16x32_bf16 v[76:79], v[40:43], a[20:23], v[76:79]\n"
    "v_mfma_f32_16x16x32_bf16 v[72:75], v[36:39], a[20:23], v[72:75]\n"
    "s_waitcnt lgkmcnt(7)\n"
    "v_mfma_f32_16x16x32_bf16 v[64:67], v[44:47], a[24:27], v[64:67]\n"
    "ds_read_b128 v[36:39], v8 offset:448\n"
    "s_waitcnt lgkmcnt(7)\n"
    "v_mfma_f32_16x16x32_bf16 v[68:71], v[48:51], a[24:27], v[68:71]\n"
    "ds_read_b128 v[40:43], v8 offset:17344\n"
    "v_max3_u32 v6, v47, v51, v6\n"
    "v_mfma_f32_16x16x32_bf16 v[76:79], v[48:51], a[28:31], v[76:79]\n"
    "v_mfma_f32_16x16x32_bf16 v[72:75], v[44:47], a[28:31], v[72:75]\n"
    "s_waitcnt lgkmcnt(7)\n"
    "v_mfma_f32_16x16x32_bf16 v[64:67], v[52:55], a[32:35], v[64:67]\n"
    "ds_read_b128 v[44:47], v8 offset:512\n"
    "s_waitcnt lgkmcnt(7)\n"
    "v_mfma_f32_16x16x32_bf16 v[68:71], v[56:59], a[32:35], v[68:71]\n"
    "ds_read_b128 v[48:51], v8 offset:17408\n"
    "v_max3_u32 v6, v55, v59, v6\n"
    "v_mfma_f32_16x16x32_bf16 v[76:79], v[56:59], a[36:39], v[76:79]\n"
    "v_mfma_f32_16x16x32_bf16 v[72:75], v[52:55], a[36:39], v[72:75]\n"
    "s_waitcnt lgkmcnt(7)\n"
    "v_mfma_f32_16x16x32_bf16 v[64:67], v[20:23], a[40:43], v[64:67]\n"
    "ds_read_b128 v[52:55], v8 offset:576\n"
    "s_waitcnt lgkmcnt(7)\n"
    "v_mfma_f32_16x16x32_bf16 v[68:71], v[24:27], a[40:43], v[68:71]\n"
    "ds_read_b128 v[56:59], v8 offset:17472\n"
    "v_max3_u32 v6, v23, v27, v6\n"
    "v_mfma_f32_16x16x32_bf16 v[76:79], v[24:27], a[44:47], v[76:79]\n"
    "v_mfma_f32_16x16x32_bf16 v[72:75], v[20:23], a[44:47], v[72:75]\n"
    "s_waitcnt lgkmcnt(7)\n"
    "v_mfma_f32_16x16x32_bf16 v[64:67], v[28:31], a[48:51], v[64:67]\n"
    "ds_read_b128 v[20:23], v8 offset:640\n"
    "s_waitcnt lgkmcnt(7)\n"
    "v_mfma_f32_16x16x32_bf16 v[68:71], v[32:35], a[48:51], v[68:71]\n"
    "ds_read_b128 v[24:27], v8 offset:17536\n"
    "v_max3_u32 v6, v31, v35, v6\n"
    "v_mfma_f32_16x16x32_bf16 v[76:79], v[32:35], a[52:55], v[76:79]\n"
    "v_mfma_f32_16x16x32_bf16 v[72:75], v[28:31], a[52:55], v[72:75]\n"
    "s_waitcnt lgkmcnt(7)\n"
    "v_mfma_f32_16x16x32_bf16 v[64:67], v[36:39], a[56:59], v[64:67]\n"
    "ds_read_b128 v[28:31], v8 offset:704\n"
    "s_waitcnt lgkmcnt(7)\n"
    "v_mfma_f32_16x16x32_bf16 v[68:71], v[40:43], a[56:59], v[68:71]\n"
    "ds_read_b128 v[32:35], v8 offset:17600\n"
    "v_max3_u32 v6, v39, v43, v6\n"
    "v_mfma_f32_16x16x32_bf16 v[76:79], v[40:43], a[60:63], v[76:79]\n"
    "v_mfma_f32_16x16x32_bf16 v[72:75], v[36:39], a[60:63], v[72:75]\n"
    "s_waitcnt lgkmcnt(7)\n"
    "v_mfma_f32_16x16x32_bf16 v[64:67], v[44:47], a[64:67], v[64:67]\n"
    "ds_read_b128 v[36:39], v8 offset:768\n"
    "s_waitcnt lgkmcnt(7)\n"
    "v_mfma_f32_16x16x32_bf16 v[68:71], v[48:51], a[64:67], v[68:71]\n"
    "ds_read_b128 v[40:43], v8 offset:17664\n"
    "v_max3_u32 v6, v47, v51, v6\n"
    "v_mfma_f32_16x16x32_bf16 v[76:79], v[48:51], a[68:71], v[76:79]\n"
    "v_mfma_f32_16x16x32_bf16 v[72:75], v[44:47], a[68:71], v[72:75]\n"
    "s_waitcnt lgkmcnt(7)\n"
    "v_mfma_f32_16x16x32_bf16 v[64:67], v[52:55], a[72:75], v[64:67]\n"
    "ds_read_b128 v[44:47], v8 offset:832\n"
    "s_waitcnt lgkmcnt(7)\n"
    "v_mfma_f32_16x16x32_bf16 v[68:71], v[56:59], a[72:75], v[68:71]\n"
    "ds_read_b128 v[48:51], v8 offset:17728\n"
    "v_max3_u32 v6, v55, v59, v6\n"
    "v_mfma_f32_16x16x32_bf16 v[76:79], v[56:59], a[76:79], v[76:79]\n"
    "v_mfma_f32_16x16x32_bf16 v[72:75], v[52:55], a[76:79], v[72:75]\n"
    "s_waitcnt lgkmcnt(7)\n"
    "v_mfma_f32_16x16x32_bf16 v[64:67], v[20:23], a[80:83], v[64:67]\n"
    "ds_read_b128 v[52:55], v8 offset:896\n"
    "s_waitcnt lgkmcnt(7)\n"
    "v_mfma_f32_16x16x32_bf16 v[68:71], v[24:27], a[80:83], v[68:71]\n"
    "ds_read_b128 v[56:59], v8 offset:17792\n"
    "v_max3_u32 v6, v23, v27, v6\n"
    "v_mfma_f32_16x16x32_bf16 v[76:79], v[24:27], a[84:87], v[76:79]\n"
    "v_mfma_f32_16x16x32_bf16 v[72:75], v[20:23], a[84:87], v[72:75]\n"
    "s_waitcnt lgkmcnt(7)\n"
    "v_mfma_f32_16x16x32_bf16 v[64:67], v[28:31], a[88:91], v[64:67]\n"
    "ds_read_b128 v[20:23], v8 offset:960\n"
    "s_waitcnt lgkmcnt(7)\n"
    "v_mfma_f32_16x16x32_bf16 v[68:71], v[32:35], a[88:91], v[68:71]\n"
    "ds_read_b128 v[24:27], v8 offset:17856\n"
    "v_max3_u32 v6, v31, v35, v6\n"
    "v_mfma_f32_16x16x32_bf16 v[76:79], v[32:35], a[92:95], v[76:79]\n"
    "v_mfma_f32_16x16x32_bf16 v[72:75], v[28:31], a[92:95], v[72:75]\n"
    "s_waitcnt lgkmcnt(7)\n"
    "v_mfma_f32_16x16x32_bf16 v[64:67], v[36:39], a[96:99], v[64:67]\n"
    "ds_read_b128 v[28:31], v8 offset:0\n"
    "s_waitcnt lgkmcnt(7)\n"
    "v_mfma_f32_16x16x32_bf16 v[68:71], v[40:43], a[96:99], v[68:71]\n"
    "ds_read_b128 v[32:35], v8 offset:16896\n"
    "s_waitcnt vmcnt(0)\n"
    "s_barrier\n"
    "v_readfirstlane_b32 s38, v5\n"
    "s_or_b32 s52, s52, s68\n"
    "s_or_b32 s38, s38, s68\n"
    "s_cbranch_scc0 LK2_%=\n"
    "LJ2_%=:\n"
    "buffer_store_dword v5, v9, s[16:19], s52 offen sc1\n"
    "s_mov_b32 s52, 0x80000000\n"
    "v_mfma_f32_16x16x32_bf16 v[76:79], v[40:43], a[100:103], v[76:79]\n"
    "v_max3_u32 v6, v39, v43, v6\n"
    "v_mfma_f32_16x16x32_bf16 v[72:75], v[36:39], a[100:103], v[72:75]\n"
    "s_waitcnt lgkmcnt(7)\n"
    "v_mfma_f32_16x16x32_bf16 v[64:67], v[44:47], a[104:107], v[64:67]\n"
    "ds_read_b128 v[36:39], v8 offset:64\n"
    "s_waitcnt lgkmcnt(7)\n"
    "v_mfma_f32_16x16x32_bf16 v[68:71], v[48:51], a[104:107], v[68:71]\n"
    "ds_read_b128 v[40:43], v8 offset:16960\n"
    "v_max3_u32 v6, v47, v51, v6\n"
    "v_mfma_f32_16x16x32_bf16 v[76:79], v[48:51], a[108:111], v[76:79]\n"
    "v_mfma_f32_16x16x32_bf16 v[72:75], v[44:47], a[108:111], v[72:75]\n"
    "s_waitcnt lgkmcnt(7)\n"
    "v_mfma_f32_16x16x32_bf16 v[64:67], v[52:55], a[112:115], v[64:67]\n"
    "ds_read_b128 v[44:47], v8 offset:128\n"
    "s_waitcnt lgkmcnt(7)\n"
    "v_mfma_f32_16x16x32_bf16 v[68:71], v[56:59], a[112:115], v[68:71]\n"
    "ds_read_b128 v[48:51], v8 offset:17024\n"
    "v_max3_u32 v6, v55, v59, v6\n"
    "v_mfma_f32_16x16x32_bf16 v[76:79], v[56:59], a[116:119], v[76:79]\n"
    "v_mfma_f32_16x16x32_bf16 v[72:75], v[52:55], a[116:119], v[72:75]\n"
    "s_waitcnt lgkmcnt(7)\n"
    "v_mfma_f32_16x16x32_bf16 v[64:67], v[20:23], a[120:123], v[64:67]\n"
    "ds_read_b128 v[52:55], v8 offset:192\n"
    "s_waitcnt lgkmcnt(7)\n"
    "v_mfma_f32_16x16x32_bf16 v[68:71], v[24:27], a[120:123], v[68:71]\n"
    "ds_read_b128 v[56:59], v8 offset:17088\n"
    "v_max3_u32 v6, v23, v27, v6\n"
    "v_mfma_f32_16x16x32_bf16 v[76:79], v[24:27], a[124:127], v[76:79]\n"
    "v_mfma_f32_16x16x32_bf16 v[72:75], v[20:23], a[124:127], v[72:75]\n"
    "s_waitcnt lgkmcnt(7)\n"
    "v_mfma_f32_16x16x32_bf16 v[80:83], v[28:31], a[128:131], 0\n"
    "ds_read_b128 v[20:23], v8 offset:256\n"
    "s_waitcnt lgkmcnt(7)\n"
    "v_mfma_f32_16x16x32_bf16 v[84:87], v[32:35], a[128:131], 0\n"
    "ds_read_b128 v[24:27], v8 offset:17152\n"
    "v_cmp_eq_u32 vcc, -1, v6\n"
    "v_mfma_f32_16x16x32_bf16 v[92:95], v[32:35], a[132:135], 0\n"
    "s_cbranch_vccnz LRD%=\n"
    "v_mfma_f32_16x16x32_bf16 v[88:91], v[28:31], a[132:135], 0\n"
    "s_waitcnt lgkmcnt(7)\n"
    "v_mfma_f32_16x16x32_bf16 v[80:83], v[36:39], a[136:139], v[80:83]\n"
    "ds_read_b128 v[28:31], v8 offset:320\n"
    "s_waitcnt lgkmcnt(7)\n"
    "v_mfma_f32_16x16x32_bf16 v[84:87], v[40:43], a[136:139], v[84:87]\n"
    "ds_read_b128 v[32:35], v8 offset:17216\n"
    "v_cvt_pk_bf16_f32 v12, v64, v65\n"
    "v_mfma_f32_16x16x32_bf16 v[92:95], v[40:43], a[140:143], v[92:95]\n"
    "v_cvt_pk_bf16_f32 v13, v66, v67\n"
    "v_mfma_f32_16x16x32_bf16 v[88:91], v[36:39], a[140:143], v[88:91]\n"
    "s_waitcnt lgkmcnt(7)\n"
    "v_cvt_pk_bf16_f32 v14, v68, v69\n"
    "v_mfma_f32_16x16x32_bf16 v[80:83], v[44:47], a[144:147], v[80:83]\n"
    "ds_read_b128 v[36:39], v8 offset:384\n"
    "s_waitcnt lgkmcnt(7)\n"
    "v_mfma_f32_16x16x32_bf16 v[84:87], v[48:51], a[144:147], v[84:87]\n"
    "ds_read_b128 v[40:43], v8 offset:17280\n"
    "v_cvt_pk_bf16_f32 v15, v70, v71\n"
    "v_mfma_f32_16x16x32_bf16 v[92:95], v[48:51], a[148:151], v[92:95]\n"
    "buffer_store_dwordx4 v[12:15], v0, s[16:19], s59 offen sc1\n"
    "v_mfma_f32_16x16x32_bf16 v[88:91], v[44:47], a[148:151], v[88:91]\n"
    "s_waitcnt lgkmcnt(7)\n"
    "v_cvt_pk_bf16_f32 v16, v72, v73\n"
    "v_mfma_f32_16x16x32_bf16 v[80:83], v[52:55], a[152:155], v[80:83]\n"
    "ds_read_b128 v[44:47], v8 offset:448\n"
    "s_waitcnt lgkmcnt(7)\n"
    "v_mfma_f32_16x16x32_bf16 v[84:87], v[56:59], a[152:155], v[84:87]\n"
    "ds_read_b128 v[48:51], v8 offset:17344\n"
    "v_cvt_pk_bf16_f32 v17, v74, v75\n"
    "v_mfma_f32_16x16x32_bf16 v[92:95], v[56:59], a[156:159], v[92:95]\n"
    "v_cvt_pk_bf16_f32 v18, v76, v77\n"
    "v_mfma_f32_16x16x32_bf16 v[88:91], v[52:55], a[156:159], v[88:91]\n"
    "s_waitcnt lgkmcnt(7)\n"
    "v_cvt_pk_bf16_f32 v19, v78, v79\n"
    "v_mfma_f32_16x16x32_bf16 v[80:83], v[20:23], a[160:163], v[80:83]\n"
    "ds_read_b128 v[52:55], v8 offset:512\n"
    "s_waitcnt lgkmcnt(7)\n"
    "v_mfma_f32_16x16x32_bf16 v[84:87], v[24:27], a[160:163], v[84:87]\n"
    "ds_read_b128 v[56:59], v8 offset:17408\n"
    "buffer_store_dwordx4 v[16:19], v0, s[16:19], s62 offen sc1\n"
    "v_mfma_f32_16x16x32_bf16 v[92:95], v[24:27], a[164:167], v[92:95]\n"
    "s_add_u32 s59, s59, 16384\n"
    "s_add_u32 s62, s62, 16384\n"
    "v_mfma_f32_16x16x32_bf16 v[88:91], v[20:23], a[164:167], v[88:91]\n"
    "s_waitcnt lgkmcnt(7)\n"
    "v_mfma_f32_16x16x32_bf16 v[80:83], v[28:31], a[168:171], v[80:83]\n"
    "ds_read_b128 v[20:23], v8 offset:576\n"
    "s_waitcnt lgkmcnt(7)\n"
    "v_mfma_f32_16x16x32_bf16 v[84:87], v[32:35], a[168:171], v[84:87]\n"
    "ds_read_b128 v[24:27], v8 offset:17472\n"
    "v_mfma_f32_16x16x32_bf16 v[92:95], v[32:35], a[172:175], v[92:95]\n"
    "v_mfma_f32_16x16x32_bf16 v[88:91], v[28:31], a[172:175], v[88:91]\n"
    "s_waitcnt lgkmcnt(7)\n"
    "v_mfma_f32_16x16x32_bf16 v[80:83], v[36:39], a[176:179], v[80:83]\n"
    "ds_read_b128 v[28:31], v8 offset:640\n"
    "s_waitcnt lgkmcnt(7)\n"
    "v_mfma_f32_16x16x32_bf16 v[84:87], v[40:43], a[176:179], v[84:87]\n"
    "ds_read_b128 v[32:35], v8 offset:17536\n"
    "v_mfma_f32_16x16x32_bf16 v[92:95], v[40:43], a[180:183], v[92:95]\n"
    "v_mfma_f32_16x16x32_bf16 v[88:91], v[36:39], a[180:183], v[88:91]\n"
    "s_waitcnt lgkmcnt(7)\n"
    "v_mfma_f32_16x16x32_bf16 v[80:83], v[44:47], a[184:187], v[80:83]\n"
    "ds_read_b128 v[36:39], v8 offset:704\n"
    "s_waitcnt lgkmcnt(7)\n"
    "v_mfma_f32_16x16x32_bf16 v[84:87], v[48:51], a[184:187], v[84:87]\n"
    "ds_read_b128 v[40:43], v8 offset:17600\n"
    "v_mfma_f32_16x16x32_bf16 v[92:95], v[48:51], a[188:191], v[92:95]\n"
    "v_mfma_f32_16x16x32_bf16 v[88:91], v[44:47], a[188:191], v[88:91]\n"
    "s_waitcnt lgkmcnt(7)\n"
    "v_mfma_f32_16x16x32_bf16 v[80:83], v[52:55], a[192:195], v[80:83]\n"
    "ds_read_b128 v[44:47], v8 offset:768\n"
    "s_waitcnt lgkmcnt(7)\n"
    "v_mfma_f32_16x16x32_bf16 v[84:87], v[56:59], a[192:195], v[84:87]\n"
    "ds_read_b128 v[48:51], v8 offset:17664\n"
    "buffer_load_dword v5, off, s[16:19], s67 sc1\n"
    "v_mfma_f32_16x16x32_bf16 v[92:95], v[56:59], a[196:199], v[92:95]\n"
    "v_mfma_f32_16x16x32_bf16 v[88:91], v[52:55], a[196:199], v[88:91]\n"
    "s_waitcnt lgkmcnt(7)\n"
    "v_mfma_f32_16x16x32_bf16 v[80:83], v[20:23], a[200:203], v[80:83]\n"
    "ds_read_b128 v[52:55], v8 offset:832\n"
    "s_waitcnt lgkmcnt(7)\n"
    "v_mfma_f32_16x16x32_bf16 v[84:87], v[24:27], a[200:203], v[84:87]\n"
    "ds_read_b128 v[56:59], v8 offset:17728\n"
    "v_mfma_f32_16x16x32_bf16 v[92:95], v[24:27], a[204:207], v[92:95]\n"
    "v_mfma_f32_16x16x32_bf16 v[88:91], v[20:23], a[204:207], v[88:91]\n"
    "s_waitcnt lgkmcnt(7)\n"
    "v_mfma_f32_16x16x32_bf16 v[80:83], v[28:31], a[208:211], v[80:83]\n"
    "ds_read_b128 v[20:23], v8 offset:896\n"
    "s_waitcnt lgkmcnt(7)\n"
    "v_mfma_f32_16x16x32_bf16 v[84:87], v[32:35], a[208:211], v[84:87]\n"
    "ds_read_b128 v[24:27], v8 offset:17792\n"
    "v_mfma_f32_16x16x32_bf16 v[92:95], v[32:35], a[212:215], v[92:95]\n"
    "v_mfma_f32_16x16x32_bf16 v[88:91], v[28:31], a[212:215], v[88:91]\n"
    "s_waitcnt lgkmcnt(7)\n"
    "v_mfma_f32_16x16x32_bf16 v[80:83], v[36:39], a[216:219], v[80:83]\n"
    "ds_read_b128 v[28:31], v8 offset:960\n"
    "s_waitcnt lgkmcnt(7)\n"
    "v_mfma_f32_16x16x32_bf16 v[84:87], v[40:43], a[216:219], v[84:87]\n"
    "ds_read_b128 v[32:35], v8 offset:17856\n"
    "v_mfma_f32_16x16x32_bf16 v[92:95], v[40:43], a[220:223], v[92:95]\n"
    "v_mfma_f32_16x16x32_bf16 v[88:91], v[36:39], a[220:223], v[88:91]\n"
    "s_waitcnt lgkmcnt(7)\n"
    "v_mfma_f32_16x16x32_bf16 v[80:83], v[44:47], a[224:227], v[80:83]\n"
    "s_waitcnt lgkmcnt(6)\n"
    "v_mfma_f32_16x16x32_bf16 v[84:87], v[48:51], a[224:227], v[84:87]\n"
    "v_mfma_f32_16x16x32_bf16 v[92:95], v[48:51], a[228:231], v[92:95]\n"
    "v_mfma_f32_16x16x32_bf16 v[88:91], v[44:47], a[228:231], v[88:91]\n"
    "s_waitcnt lgkmcnt(5)\n"
    "v_mfma_f32_16x16x32_bf16 v[80:83], v[52:55], a[232:235], v[80:83]\n"
    "s_waitcnt lgkmcnt(4)\n"
    "v_mfma_f32_16x16x32_bf16 v[84:87], v[56:59], a[232:235], v[84:87]\n"
    "v_mfma_f32_16x16x32_bf16 v[92:95], v[56:59], a[236:239], v[92:95]\n"
    "v_mfma_f32_16x16x32_bf16 v[88:91], v[52:55], a[236:239], v[88:91]\n"
    "s_waitcnt lgkmcnt(3)\n"
    "v_mfma_f32_16x16x32_bf16 v[80:83], v[20:23], a[240:243], v[80:83]\n"
    "s_waitcnt lgkmcnt(2)\n"
    "v_mfma_f32_16x16x32_bf16 v[84:87], v[24:27], a[240:243], v[84:87]\n"
    "v_mfma_f32_16x16x32_bf16 v[92:95], v[24:27], a[244:247], v[92:95]\n"
    "v_mfma_f32_16x16x32_bf16 v[88:91], v[20:23], a[244:247], v[88:91]\n"
    "s_waitcnt lgkmcnt(1)\n"
    "v_mfma_f32_16x16x32_bf16 v[80:83], v[28:31], a[248:251], v[80:83]\n"
    "s_waitcnt lgkmcnt(0)\n"
    "v_mfma_f32_16x16x32_bf16 v[84:87], v[32:35], a[248:251], v[84:87]\n"
    "v_mfma_f32_16x16x32_bf16 v[92:95], v[32:35], a[252:255], v[92:95]\n"
    "v_mfma_f32_16x16x32_bf16 v[88:91], v[28:31], a[252:255], v[88:91]\n"
    "v_cvt_pk_bf16_f32 v12, v80, v81\n"
    "v_cvt_pk_bf16_f32 v13, v82, v83\n"
    "v_cvt_pk_bf16_f32 v14, v84, v85\n"
    "v_cvt_pk_bf16_f32 v15, v86, v87\n"
    "buffer_store_dwordx4 v[12:15], v0, s[16:19], s59 offen sc1\n"
    "s_nop 2\n"
    "v_cvt_pk_bf16_f32 v16, v88, v89\n"
    "v_cvt_pk_bf16_f32 v17, v90, v91\n"
    "v_cvt_pk_bf16_f32 v18, v92, v93\n"
    "v_cvt_pk_bf16_f32 v19, v94, v95\n"
    "buffer_store_dwordx4 v[16:19], v0, s[16:19], s62 offen sc1\n"
    "s_mov_b32 s52, s64\n"
    "LHE%=:\n"
    "s_cmp_ge_u32 s40, 64\n"
    "s_cbranch_scc1 LT%=\n"
    "L1%=:\n"
    "v_add_u32 v8, s43, v1\n"
    "s_xor_b32 s43, s43, 33792\n"
    "s_mov_b32 s63, s45\n"
    "s_lshr_b32 s38, s63, 8\n"
    "s_xor_b32 s38, s38, s48\n"
    "s_and_b32 s38, s38, 0x2000\n"
    "s_or_b32 s63, s63, s38\n"
    "s_add_u32 s39, s44, s43\n"
    "s_add_u32 s69, s63, 4096\n"
    "s_add_u32 m0, s39, 0\n"
    "s_nop 0\n"
    "buffer_load_dwordx4 v0, s[16:19], s63 offen offset:0 sc1 lds\n"
    "s_add_u32 m0, s39, 32\n"
    "s_nop 0\n"
    "buffer_load_dwordx4 v0, s[16:19], s63 offen offset:1024 sc1 lds\n"
    "s_add_u32 m0, s39, 64\n"
    "s_nop 0\n"
    "buffer_load_dwordx4 v0, s[16:19], s63 offen offset:2048 sc1 lds\n"
    "s_add_u32 m0, s39, 96\n"
    "s_nop 0\n"
    "buffer_load_dwordx4 v0, s[16:19], s63 offen offset:3072 sc1 lds\n"
    "s_add_u32 m0, s39, 4224\n"
    "s_nop 0\n"
    "buffer_load_dwordx4 v0, s[16:19], s69 offen offset:0 sc1 lds\n"
    "s_add_u32 m0, s39, 4256\n"
    "s_nop 0\n"
    "buffer_load_dwordx4 v0, s[16:19], s69 offen offset:1024 sc1 lds\n"
    "s_add_u32 m0, s39, 4288\n"
    "s_nop 0\n"
    "buffer_load_dwordx4 v0, s[16:19], s69 offen offset:2048 sc1 lds\n"
    "s_add_u32 m0, s39, 4320\n"
    "s_nop 0\n"
    "buffer_load_dwordx4 v0, s[16:19], s69 offen offset:3072 sc1 lds\n"
    "s_add_u32 s45, s45, 131072\n"
    "s_mul_i32 s59, s40, 2621440\n"
    "s_add_u32 s59, s59, s47\n"
    "s_lshr_b32 s38, s59, 8\n"
    "s_xor_b32 s38, s38, s48\n"
    "s_and_b32 s38, s38, 0x2000\n"
    "s_or_b32 s59, s59, s38\n"
    "s_lshl_b32 s64, s40, 7\n"
    "s_add_u32 s64, s64, s50\n"
    "s_add_u32 s40, s40, 2\n"
    "s_cmp_eq_u32 s61, 1\n"
    "s_cbranch_scc1 LIV%=\n"
    "s_add_u32 s62, s59, 4096\n"
    "ds_read_b128 v[20:23], v8 offset:0\n"
    "ds_read_b128 v[24:27], v8 offset:16896\n"
    "ds_read_b128 v[28:31], v8 offset:64\n"
    "ds_read_b128 v[32:35], v8 offset:16960\n"
    "ds_read_b128 v[36:39], v8 offset:128\n"
    "ds_read_b128 v[40:43], v8 offset:17024\n"
    "ds_read_b128 v[44:47], v8 offset:192\n"
    "ds_read_b128 v[48:51], v8 offset:17088\n"
    "s_waitcnt lgkmcnt(7)\n"
    "v_mfma_f32_16x16x32_bf16 v[64:67], a[0:3], v[20:23], 0\n"
    "ds_read_b128 v[52:55], v8 offset:256\n"
    "s_waitcnt lgkmcnt(7)\n"
    "v_mfma_f32_16x16x32_bf16 v[68:71], a[0:3], v[24:27], 0\n"
    "ds_read_b128 v[56:59], v8 offset:17152\n"
    "v_mfma_f32_16x16x32_bf16 v[76:79], a[4:7], v[24:27], 0\n"
    "v_mfma_f32_16x16x32_bf16 v[72:75], a[4:7], v[20:23], 0\n"
    "s_waitcnt lgkmcnt(7)\n"
    "v_mfma_f32_16x16x32_bf16 v[64:67], a[8:11], v[28:31], v[64:67]\n"
    "ds_read_b128 v[20:23], v8 offset:320\n"
    "s_waitcnt lgkmcnt(7)\n"
    "v_mfma_f32_16x16x32_bf16 v[68:71], a[8:11], v[32:35], v[68:71]\n"
    "ds_read_b128 v[24:27], v8 offset:17216\n"
    "v_mfma_f32_16x16x32_bf16 v[76:79], a[12:15], v[32:35], v[76:79]\n"
    "v_mfma_f32_16x16x32_bf16 v[72:75], a[12:15], v[28:31], v[72:75]\n"
    "s_waitcnt lgkmcnt(7)\n"
    "v_mfma_f32_16x16x32_bf16 v[64:67], a[16:19], v[36:39], v[64:67]\n"
    "ds_read_b128 v[28:31], v8 offset:384\n"
    "s_waitcnt lgkmcnt(7)\n"
    "v_mfma_f32_16x16x32_bf16 v[68:71], a[16:19], v[40:43], v[68:71]\n"
    "ds_read_b128 v[32:35], v8 offset:17280\n"
    "v_mfma_f32_16x16x32_bf16 v[76:79], a[20:23], v[40:43], v[76:79]\n"
    "v_mfma_f32_16x16x32_bf16 v[72:75], a[20:23], v[36:39], v[72:75]\n"
    "s_waitcnt lgkmcnt(7)\n"
    "v_mfma_f32_16x16x32_bf16 v[64:67], a[24:27], v[44:47], v[64:67]\n"
    "ds_read_b128 v[36:39], v8 offset:448\n"
    "s_waitcnt lgkmcnt(7)\n"
    "v_mfma_f32_16x16x32_bf16 v[68:71], a[24:27], v[48:51], v[68:71]\n"
    "ds_read_b128 v[40:43], v8 offset:17344\n"
    "v_mfma_f32_16x16x32_bf16 v[76:79], a[28:31], v[48:51], v[76:79]\n"
    "v_mfma_f32_16x16x32_bf16 v[72:75], a[28:31], v[44:47], v[72:75]\n"
    "s_waitcnt lgkmcnt(7)\n"
    "v_mfma_f32_16x16x32_bf16 v[64:67], a[32:35], v[52:55], v[64:67]\n"
    "ds_read_b128 v[44:47], v8 offset:512\n"
    "s_waitcnt lgkmcnt(7)\n"
    "v_mfma_f32_16x16x32_bf16 v[68:71], a[32:35], v[56:59], v[68:71]\n"
    "ds_read_b128 v[48:51], v8 offset:17408\n"
    "v_mfma_f32_16x16x32_bf16 v[76:79], a[36:39], v[56:59], v[76:79]\n"
    "v_mfma_f32_16x16x32_bf16 v[72:75], a[36:39], v[52:55], v[72:75]\n"
    "s_waitcnt lgkmcnt(7)\n"
    "v_mfma_f32_16x16x32_bf16 v[64:67], a[40:43], v[20:23], v[64:67]\n"
    "ds_read_b128 v[52:55], v8 offset:576\n"
    "s_waitcnt lgkmcnt(7)\n"
    "v_mfma_f32_16x16x32_bf16 v[68:71], a[40:43], v[24:27], v[68:71]\n"
    "ds_read_b128 v[56:59], v8 offset:17472\n"
    "v_mfma_f32_16x16x32_bf16 v[76:79], a[44:47], v[24:27], v[76:79]\n"
    "v_mfma_f32_16x16x32_bf16 v[72:75], a[44:47], v[20:23], v[72:75]\n"
    "s_waitcnt lgkmcnt(7)\n"
    "v_mfma_f32_16x16x32_bf16 v[64:67], a[48:51], v[28:31], v[64:67]\n"
    "ds_read_b128 v[20:23], v8 offset:640\n"
    "s_waitcnt lgkmcnt(7)\n"
    "v_mfma_f32_16x16x32_bf16 v[68:71], a[48:51], v[32:35], v[68:71]\n"
    "ds_read_b128 v[24:27], v8 offset:17536\n"
    "v_mfma_f32_16x16x32_bf16 v[76:79], a[52:55], v[32:35], v[76:79]\n"
    "v_mfma_f32_16x16x32_bf16 v[72:75], a[52:55], v[28:31], v[72:75]\n"
    "s_waitcnt lgkmcnt(7)\n"
    "v_mfma_f32_16x16x32_bf16 v[64:67], a[56:59], v[36:39], v[64:67]\n"
    "ds_read_b128 v[28:31], v8 offset:704\n"
    "s_waitcnt lgkmcnt(7)\n"
    "v_mfma_f32_16x16x32_bf16 v[68:71], a[56:59], v[40:43], v[68:71]\n"
    "ds_read_b128 v[32:35], v8 offset:17600\n"
    "v_mfma_f32_16x16x32_bf16 v[76:79], a[60:63], v[40:43], v[76:79]\n"
    "v_mfma_f32_16x16x32_bf16 v[72:75], a[60:63], v[36:39], v[72:75]\n"
    "s_waitcnt lgkmcnt(7)\n"
    "v_mfma_f32_16x16x32_bf16 v[64:67], a[64:67], v[44:47], v[64:67]\n"
    "ds_read_b128 v[36:39], v8 offset:768\n"
    "s_waitcnt lgkmcnt(7)\n"
    "v_mfma_f32_16x16x32_bf16 v[68:71], a[64:67], v[48:51], v[68:71]\n"
    "ds_read_b128 v[40:43], v8 offset:17664\n"
    "v_mfma_f32_16x16x32_bf16 v[76:79], a[68:71], v[48:51], v[76:79]\n"
    "v_mfma_f32_16x16x32_bf16 v[72:75], a[68:71], v[44:47], v[72:75]\n"
    "s_waitcnt lgkmcnt(7)\n"
    "v_mfma_f32_16x16x32_bf16 v[64:67], a[72:75], v[52:55], v[64:67]\n"
    "ds_read_b128 v[44:47], v8 offset:832\n"
    "s_waitcnt lgkmcnt(7)\n"
    "v_mfma_f32_16x16x32_bf16 v[68:71], a[72:75], v[56:59], v[68:71]\n"
    "ds_read_b128 v[48:51], v8 offset:17728\n"
    "v_mfma_f32_16x16x32_bf16 v[76:79], a[76:79], v[56:59], v[76:79]\n"
    "v_mfma_f32_16x16x32_bf16 v[72:75], a[76:79], v[52:55], v[72:75]\n"
    "s_waitcnt lgkmcnt(7)\n"
    "v_mfma_f32_16x16x32_bf16 v[64:67], a[80:83], v[20:23], v[64:67]\n"
    "ds_read_b128 v[52:55], v8 offset:896\n"
    "s_waitcnt lgkmcnt(7)\n"
    "v_mfma_f32_16x16x32_bf16 v[68:71], a[80:83], v[24:27], v[68:71]\n"
    "ds_read_b128 v[56:59], v8 offset:17792\n"
    "v_mfma_f32_16x16x32_bf16 v[76:79], a[84:87], v[24:27], v[76:79]\n"
    "v_mfma_f32_16x16x32_bf16 v[72:75], a[84:87], v[20:23], v[72:75]\n"
    "s_waitcnt lgkmcnt(7)\n"
    "v_mfma_f32_16x16x32_bf16 v[64:67], a[88:91], v[28:31], v[64:67]\n"
    "ds_read_b128 v[20:23], v8 offset:960\n"
    "s_waitcnt lgkmcnt(7)\n"
    "v_mfma_f32_16x16x32_bf16 v[68:71], a[88:91], v[32:35], v[68:71]\n"
    "ds_read_b128 v[24:27], v8 offset:17856\n"
    "v_mfma_f32_16x16x32_bf16 v[76:79], a[92:95], v[32:35], v[76:79]\n"
    "v_mfma_f32_16x16x32_bf16 v[72:75], a[92:95], v[28:31], v[72:75]\n"
    "s_waitcnt lgkmcnt(7)\n"
    "v_mfma_f32_16x16x32_bf16 v[64:67], a[96:99], v[36:39], v[64:67]\n"
    "ds_read_b128 v[28:31], v8 offset:0\n"
    "s_waitcnt lgkmcnt(7)\n"
    "v_mfma_f32_16x16x32_bf16 v[68:71], a[96:99], v[40:43], v[68:71]\n"
    "ds_read_b128 v[32:35], v8 offset:16896\n"
    "s_waitcnt vmcnt(8)\n"
    "s_barrier\n"
    "v_readfirstlane_b32 s38, v5\n"
    "s_or_b32 s52, s52, s68\n"
    "s_or_b32 s38, s38, s68\n"
    "s_cbranch_scc0 LK3_%=\n"
    "LJ3_%=:\n"
    "buffer_store_dword v5, v9, s[16:19], s52 offen sc1\n"
    "s_mov_b32 s52, 0x80000000\n"
    "v_mfma_f32_16x16x32_bf16 v[76:79], a[100:103], v[40:43], v[76:79]\n"
    "v_mfma_f32_16x16x32_bf16 v[72:75], a[100:103], v[36:39], v[72:75]\n"
    "s_waitcnt lgkmcnt(7)\n"
    "v_mfma_f32_16x16x32_bf16 v[64:67], a[104:107], v[44:47], v[64:67]\n"
    "ds_read_b128 v[36:39], v8 offset:64\n"
    "s_waitcnt lgkmcnt(7)\n"
    "v_mfma_f32_16x16x32_bf16 v[68:71], a[104:107], v[48:51], v[68:71]\n"
    "ds_read_b128 v[40:43], v8 offset:16960\n"
    "v_mfma_f32_16x16x32_bf16 v[76:79], a[108:111], v[48:51], v[76:79]\n"
    "v_mfma_f32_16x16x32_bf16 v[72:75], a[108:111], v[44:47], v[72:75]\n"
    "s_waitcnt lgkmcnt(7)\n"
    "v_mfma_f32_16x16x32_bf16 v[64:67], a[112:115], v[52:55], v[64:67]\n"
    "ds_read_b128 v[44:47], v8 offset:128\n"
    "s_waitcnt lgkmcnt(7)\n"
    "v_mfma_f32_16x16x32_bf16 v[68:71], a[112:115], v[56:59], v[68:71]\n"
    "ds_read_b128 v[48:51], v8 offset:17024\n"
    "v_mfma_f32_16x16x32_bf16 v[76:79], a[116:119], v[56:59], v[76:79]\n"
    "v_mfma_f32_16x16x32_bf16 v[72:75], a[116:119], v[52:55], v[72:75]\n"
    "s_waitcnt lgkmcnt(7)\n"
    "v_mfma_f32_16x16x32_bf16 v[64:67], a[120:123], v[20:23], v[64:67]\n"
    "ds_read_b128 v[52:55], v8 offset:192\n"
    "s_waitcnt lgkmcnt(7)\n"
    "v_mfma_f32_16x16x32_bf16 v[68:71], a[120:123], v[24:27], v[68:71]\n"
    "ds_read_b128 v[56:59], v8 offset:17088\n"
    "v_mfma_f32_16x16x32_bf16 v[76:79], a[124:127], v[24:27], v[76:79]\n"
    "v_mfma_f32_16x16x32_bf16 v[72:75], a[124:127], v[20:23], v[72:75]\n"
    "s_waitcnt lgkmcnt(7)\n"
    "v_mfma_f32_16x16x32_bf16 v[80:83], a[128:131], v[28:31], 0\n"
    "ds_read_b128 v[20:23], v8 offset:256\n"
    "s_waitcnt lgkmcnt(7)\n"
    "v_mfma_f32_16x16x32_bf16 v[84:87], a[128:131], v[32:35], 0\n"
    "ds_read_b128 v[24:27], v8 offset:17152\n"
    "buffer_load_dword v6, off, s[16:19], s53 sc1\n"
    "v_mfma_f32_16x16x32_bf16 v[92:95], a[132:135], v[32:35], 0\n"
    "v_mfma_f32_16x16x32_bf16 v[88:91], a[132:135], v[28:31], 0\n"
    "s_waitcnt lgkmcnt(7)\n"
    "v_mfma_f32_16x16x32_bf16 v[80:83], a[136:139], v[36:39], v[80:83]\n"
    "ds_read_b128 v[28:31], v8 offset:320\n"
    "s_waitcnt lgkmcnt(7)\n"
    "v_mfma_f32_16x16x32_bf16 v[84:87], a[136:139], v[40:43], v[84:87]\n"
    "ds_read_b128 v[32:35], v8 offset:17216\n"
    "v_cvt_pk_bf16_f32 v12, v64, v72\n"
    "v_mfma_f32_16x16x32_bf16 v[92:95], a[140:143], v[40:43], v[92:95]\n"
    "v_cvt_pk_bf16_f32 v13, v65, v73\n"
    "v_mfma_f32_16x16x32_bf16 v[88:91], a[140:143], v[36:39], v[88:91]\n"
    "s_waitcnt lgkmcnt(7)\n"
    "v_cvt_pk_bf16_f32 v14, v66, v74\n"
    "v_mfma_f32_16x16x32_bf16 v[80:83], a[144:147], v[44:47], v[80:83]\n"
    "ds_read_b128 v[36:39], v8 offset:384\n"
    "s_waitcnt lgkmcnt(7)\n"
    "v_mfma_f32_16x16x32_bf16 v[84:87], a[144:147], v[48:51], v[84:87]\n"
    "ds_read_b128 v[40:43], v8 offset:17280\n"
    "v_cvt_pk_bf16_f32 v15, v67, v75\n"
    "v_mfma_f32_16x16x32_bf16 v[92:95], a[148:151], v[48:51], v[92:95]\n"
    "buffer_store_dwordx4 v[12:15], v0, s[16:19], s59 offen sc1\n"
    "v_mfma_f32_16x16x32_bf16 v[88:91], a[148:151], v[44:47], v[88:91]\n"
    "s_waitcnt lgkmcnt(7)\n"
    "v_cvt_pk_bf16_f32 v16, v68, v76\n"
    "v_mfma_f32_16x16x32_bf16 v[80:83], a[152:155], v[52:55], v[80:83]\n"
    "ds_read_b128 v[44:47], v8 offset:448\n"
    "s_waitcnt lgkmcnt(7)\n"
    "v_mfma_f32_16x16x32_bf16 v[84:87], a[152:155], v[56:59], v[84:87]\n"
    "ds_read_b128 v[48:51], v8 offset:17344\n"
    "v_cvt_pk_bf16_f32 v17, v69, v77\n"
    "v_mfma_f32_16x16x32_bf16 v[92:95], a[156:159], v[56:59], v[92:95]\n"
    "v_cvt_pk_bf16_f32 v18, v70, v78\n"
    "v_mfma_f32_16x16x32_bf16 v[88:91], a[156:159], v[52:55], v[88:91]\n"
    "s_waitcnt lgkmcnt(7)\n"
    "v_cvt_pk_bf16_f32 v19, v71, v79\n"
    "v_mfma_f32_16x16x32_bf16 v[80:83], a[160:163], v[20:23], v[80:83]\n"
    "ds_read_b128 v[52:55], v8 offset:512\n"
    "s_waitcnt lgkmcnt(7)\n"
    "v_mfma_f32_16x16x32_bf16 v[84:87], a[160:163], v[24:27], v[84:87]\n"
    "ds_read_b128 v[56:59], v8 offset:17408\n"
    "buffer_store_dwordx4 v[16:19], v0, s[16:19], s62 offen sc1\n"
    "v_mfma_f32_16x16x32_bf16 v[92:95], a[164:167], v[24:27], v[92:95]\n"
    "s_add_u32 s59, s59, 16384\n"
    "s_add_u32 s62, s62, 16384\n"
    "v_mfma_f32_16x16x32_bf16 v[88:91], a[164:167], v[20:23], v[88:91]\n"
    "s_waitcnt lgkmcnt(7)\n"
    "v_mfma_f32_16x16x32_bf16 v[80:83], a[168:171], v[28:31], v[80:83]\n"
    "ds_read_b128 v[20:23], v8 offset:576\n"
    "s_waitcnt lgkmcnt(7)\n"
    "v_mfma_f32_16x16x32_bf16 v[84:87], a[168:171], v[32:35], v[84:87]\n"
    "ds_read_b128 v[24:27], v8 offset:17472\n"
    "v_mfma_f32_16x16x32_bf16 v[92:95], a[172:175], v[32:35], v[92:95]\n"
    "v_mfma_f32_16x16x32_bf16 v[88:91], a[172:175], v[28:31], v[88:91]\n"
    "s_waitcnt lgkmcnt(7)\n"
    "v_mfma_f32_16x16x32_bf16 v[80:83], a[176:179], v[36:39], v[80:83]\n"
    "ds_read_b128 v[28:31], v8 offset:640\n"
    "s_waitcnt lgkmcnt(7)\n"
    "v_mfma_f32_16x16x32_bf16 v[84:87], a[176:179], v[40:43], v[84:87]\n"
    "ds_read_b128 v[32:35], v8 offset:17536\n"
    "v_mfma_f32_16x16x32_bf16 v[92:95], a[180:183], v[40:43], v[92:95]\n"
    "v_mfma_f32_16x16x32_bf16 v[88:91], a[180:183], v[36:39], v[88:91]\n"
    "s_waitcnt lgkmcnt(7)\n"
    "v_mfma_f32_16x16x32_bf16 v[80:83], a[184:187], v[44:47], v[80:83]\n"
    "ds_read_b128 v[36:39], v8 offset:704\n"
    "s_waitcnt lgkmcnt(7)\n"
    "v_mfma_f32_16x16x32_bf16 v[84:87], a[184:187], v[48:51], v[84:87]\n"
    "ds_read_b128 v[40:43], v8 offset:17600\n"
    "s_waitcnt vmcnt(2)\n"
    "ds_write_b32 v11, v6 offset:64\n"
    "v_mfma_f32_16x16x32_bf16 v[92:95], a[188:191], v[48:51], v[92:95]\n"
    "v_mfma_f32_16x16x32_bf16 v[88:91], a[188:191], v[44:47], v[88:91]\n"
    "s_waitcnt lgkmcnt(8)\n"
    "v_mfma_f32_16x16x32_bf16 v[80:83], a[192:195], v[52:55], v[80:83]\n"
    "ds_read_b128 v[44:47], v8 offset:768\n"
    "s_waitcnt lgkmcnt(8)\n"
    "v_mfma_f32_16x16x32_bf16 v[84:87], a[192:195], v[56:59], v[84:87]\n"
    "ds_read_b128 v[48:51], v8 offset:17664\n"
    "v_mfma_f32_16x16x32_bf16 v[92:95], a[196:199], v[56:59], v[92:95]\n"
    "v_mfma_f32_16x16x32_bf16 v[88:91], a[196:199], v[52:55], v[88:91]\n"
    "s_waitcnt lgkmcnt(8)\n"
    "v_mfma_f32_16x16x32_bf16 v[80:83], a[200:203], v[20:23], v[80:83]\n"
    "ds_read_b128 v[52:55], v8 offset:832\n"
    "s_waitcnt lgkmcnt(8)\n"
    "v_mfma_f32_16x16x32_bf16 v[84:87], a[200:203], v[24:27], v[84:87]\n"
    "ds_read_b128 v[56:59], v8 offset:17728\n"
    "s_waitcnt lgkmcnt(2)\n"
    "s_barrier\n"
    "ds_read_b32 v6, v10 offset:64\n"
    "v_mfma_f32_16x16x32_bf16 v[92:95], a[204:207], v[24:27], v[92:95]\n"
    "v_mfma_f32_16x16x32_bf16 v[88:91], a[204:207], v[20:23], v[88:91]\n"
    "v_mfma_f32_16x16x32_bf16 v[80:83], a[208:211], v[28:31], v[80:83]\n"
    "ds_read_b128 v[20:23], v8 offset:896\n"
    "v_mfma_f32_16x16x32_bf16 v[84:87], a[208:211], v[32:35], v[84:87]\n"
    "ds_read_b128 v[24:27], v8 offset:17792\n"
    "s_lshl_b32 s63, s41, 16\n"
    "s_add_u32 s63, s63, s46\n"
    "s_lshr_b32 s38, s63, 8\n"
    "s_xor_b32 s38, s38, s48\n"
    "s_and_b32 s38, s38, 0x2000\n"
    "s_or_b32 s63, s63, s38\n"
    "v_mfma_f32_16x16x32_bf16 v[92:95], a[212:215], v[32:35], v[92:95]\n"
    "v_mfma_f32_16x16x32_bf16 v[88:91], a[212:215], v[28:31], v[88:91]\n"
    "s_waitcnt lgkmcnt(2)\n"
    "v_readfirstlane_b32 s60, v6\n"
    "s_cmp_lg_u32 s60, 0\n"
    "s_cbranch_scc1 LIKA%=\n"
    "v_mfma_f32_16x16x32_bf16 v[80:83], a[216:219], v[36:39], v[80:83]\n"
    "ds_read_b128 v[28:31], v8 offset:960\n"
    "v_mfma_f32_16x16x32_bf16 v[84:87], a[216:219], v[40:43], v[84:87]\n"
    "ds_read_b128 v[32:35], v8 offset:17856\n"
    "v_mfma_f32_16x16x32_bf16 v[92:95], a[220:223], v[40:43], v[92:95]\n"
    "v_mfma_f32_16x16x32_bf16 v[88:91], a[220:223], v[36:39], v[88:91]\n"
    "v_mfma_f32_16x16x32_bf16 v[80:83], a[224:227], v[44:47], v[80:83]\n"
    "v_mfma_f32_16x16x32_bf16 v[84:87], a[224:227], v[48:51], v[84:87]\n"
    "v_mfma_f32_16x16x32_bf16 v[92:95], a[228:231], v[48:51], v[92:95]\n"
    "v_mfma_f32_16x16x32_bf16 v[88:91], a[228:231], v[44:47], v[88:91]\n"
    "v_mfma_f32_16x16x32_bf16 v[80:83], a[232:235], v[52:55], v[80:83]\n"
    "v_mfma_f32_16x16x32_bf16 v[84:87], a[232:235], v[56:59], v[84:87]\n"
    "v_mfma_f32_16x16x32_bf16 v[92:95], a[236:239], v[56:59], v[92:95]\n"
    "v_mfma_f32_16x16x32_bf16 v[88:91], a[236:239], v[52:55], v[88:91]\n"
    "s_waitcnt lgkmcnt(3)\n"
    "v_mfma_f32_16x16x32_bf16 v[80:83], a[240:243], v[20:23], v[80:83]\n"
    "s_waitcnt lgkmcnt(2)\n"
    "v_mfma_f32_16x16x32_bf16 v[84:87], a[240:243], v[24:27], v[84:87]\n"
    "v_mfma_f32_16x16x32_bf16 v[92:95], a[244:247], v[24:27], v[92:95]\n"
    "v_mfma_f32_16x16x32_bf16 v[88:91], a[244:247], v[20:23], v[88:91]\n"
    "s_waitcnt lgkmcnt(1)\n"
    "v_mfma_f32_16x16x32_bf16 v[80:83], a[248:251], v[28:31], v[80:83]\n"
    "s_waitcnt lgkmcnt(0)\n"
    "v_mfma_f32_16x16x32_bf16 v[84:87], a[248:251], v[32:35], v[84:87]\n"
    "v_mfma_f32_16x16x32_bf16 v[92:95], a[252:255], v[32:35], v[92:95]\n"
    "v_mfma_f32_16x16x32_bf16 v[88:91], a[252:255], v[28:31], v[88:91]\n"
    "s_branch LIKB%=\n"
    "LIKA%=:\n"
    "v_mfma_f32_16x16x32_bf16 v[80:83], a[216:219], v[36:39], v[80:83]\n"
    "ds_read_b128 v[28:31], v8 offset:960\n"
    "v_mfma_f32_16x16x32_bf16 v[84:87], a[216:219], v[40:43], v[84:87]\n"
    "ds_read_b128 v[32:35], v8 offset:17856\n"
    "s_add_u32 s39, s44, 67584\n"
    "s_add_u32 s69, s63, 4096\n"
    "v_mfma_f32_16x16x32_bf16 v[92:95], a[220:223], v[40:43], v[92:95]\n"
    "s_add_u32 m0, s39, 0\n"
    "s_nop 0\n"
    "buffer_load_dwordx4 v0, s[16:19], s63 offen offset:0 sc1 lds\n"
    "v_mfma_f32_16x16x32_bf16 v[88:91], a[220:223], v[36:39], v[88:91]\n"
    "v_mfma_f32_16x16x32_bf16 v[80:83], a[224:227], v[44:47], v[80:83]\n"
    "v_mfma_f32_16x16x32_bf16 v[84:87], a[224:227], v[48:51], v[84:87]\n"
    "s_add_u32 m0, s39, 32\n"
    "s_nop 0\n"
    "buffer_load_dwordx4 v0, s[16:19], s63 offen offset:1024 sc1 lds\n"
    "v_mfma_f32_16x16x32_bf16 v[92:95], a[228:231], v[48:51], v[92:95]\n"
    "s_add_u32 m0, s39, 64\n"
    "s_nop 0\n"
    "buffer_load_dwordx4 v0, s[16:19], s63 offen offset:2048 sc1 lds\n"
    "v_mfma_f32_16x16x32_bf16 v[88:91], a[228:231], v[44:47], v[88:91]\n"
    "v_mfma_f32_16x16x32_bf16 v[80:83], a[232:235], v[52:55], v[80:83]\n"
    "v_mfma_f32_16x16x32_bf16 v[84:87], a[232:235], v[56:59], v[84:87]\n"
    "s_add_u32 m0, s39, 96\n"
    "s_nop 0\n"
    "buffer_load_dwordx4 v0, s[16:19], s63 offen offset:3072 sc1 lds\n"
    "v_mfma_f32_16x16x32_bf16 v[92:95], a[236:239], v[56:59], v[92:95]\n"
    "s_add_u32 m0, s39, 4224\n"
    "s_nop 0\n"
    "buffer_load_dwordx4 v0, s[16:19], s69 offen offset:0 sc1 lds\n"
    "v_mfma_f32_16x16x32_bf16 v[88:91], a[236:239], v[52:55], v[88:91]\n"
    "s_waitcnt lgkmcnt(3)\n"
    "v_mfma_f32_16x16x32_bf16 v[80:83], a[240:243], v[20:23], v[80:83]\n"
    "s_waitcnt lgkmcnt(2)\n"
    "v_mfma_f32_16x16x32_bf16 v[84:87], a[240:243], v[24:27], v[84:87]\n"
    "s_add_u32 m0, s39, 4256\n"
    "s_nop 0\n"
    "buffer_load_dwordx4 v0, s[16:19], s69 offen offset:1024 sc1 lds\n"
    "v_mfma_f32_16x16x32_bf16 v[92:95], a[244:247], v[24:27], v[92:95]\n"
    "s_add_u32 m0, s39, 4288\n"
    "s_nop 0\n"
    "buffer_load_dwordx4 v0, s[16:19], s69 offen offset:2048 sc1 lds\n"
    "v_mfma_f32_16x16x32_bf16 v[88:91], a[244:247], v[20:23], v[88:91]\n"
    "s_waitcnt lgkmcnt(1)\n"
    "v_mfma_f32_16x16x32_bf16 v[80:83], a[248:251], v[28:31], v[80:83]\n"
    "s_waitcnt lgkmcnt(0)\n"
    "v_mfma_f32_16x16x32_bf16 v[84:87], a[248:251], v[32:35], v[84:87]\n"
    "s_add_u32 m0, s39, 4320\n"
    "s_nop 0\n"
    "buffer_load_dwordx4 v0, s[16:19], s69 offen offset:3072 sc1 lds\n"
    "v_mfma_f32_16x16x32_bf16 v[92:95], a[252:255], v[32:35], v[92:95]\n"
    "v_mfma_f32_16x16x32_bf16 v[88:91], a[252:255], v[28:31], v[88:91]\n"
    "LIKB%=:\n"
    "s_nop 7\n"
    "v_cvt_pk_bf16_f32 v12, v80, v88\n"
    "v_cvt_pk_bf16_f32 v13, v81, v89\n"
    "v_cvt_pk_bf16_f32 v14, v82, v90\n"
    "v_cvt_pk_bf16_f32 v15, v83, v91\n"
    "buffer_store_dwordx4 v[12:15], v0, s[16:19], s59 offen sc1\n"
    "v_cvt_pk_bf16_f32 v16, v84, v92\n"
    "v_cvt_pk_bf16_f32 v17, v85, v93\n"
    "v_cvt_pk_bf16_f32 v18, v86, v94\n"
    "v_cvt_pk_bf16_f32 v19, v87, v95\n"
    "buffer_store_dwordx4 v[16:19], v0, s[16:19], s62 offen sc1\n"
    "s_mov_b32 s52, s64\n"
    "s_branch LIE%=\n"
    "LIV%=:\n"
    "s_add_u32 s62, s59, 1024\n"
    "ds_read_b128 v[20:23], v8 offset:0\n"
    "ds_read_b128 v[24:27], v8 offset:16896\n"
    "ds_read_b128 v[28:31], v8 offset:64\n"
    "ds_read_b128 v[32:35], v8 offset:16960\n"
    "ds_read_b128 v[36:39], v8 offset:128\n"
    "ds_read_b128 v[40:43], v8 offset:17024\n"
    "ds_read_b128 v[44:47], v8 offset:192\n"
    "ds_read_b128 v[48:51], v8 offset:17088\n"
    "s_waitcnt lgkmcnt(7)\n"
    "v_mfma_f32_16x16x32_bf16 v[64:67], v[20:23], a[0:3], 0\n"
    "ds_read_b128 v[52:55], v8 offset:256\n"
    "s_waitcnt lgkmcnt(7)\n"
    "v_mfma_f32_16x16x32_bf16 v[68:71], v[24:27], a[0:3], 0\n"
    "ds_read_b128 v[56:59], v8 offset:17152\n"
    "v_mfma_f32_16x16x32_bf16 v[76:79], v[24:27], a[4:7], 0\n"
    "v_mfma_f32_16x16x32_bf16 v[72:75], v[20:23], a[4:7], 0\n"
    "s_waitcnt lgkmcnt(7)\n"
    "v_mfma_f32_16x16x32_bf16 v[64:67], v[28:31], a[8:11], v[64:67]\n"
    "ds_read_b128 v[20:23], v8 offset:320\n"
    "s_waitcnt lgkmcnt(7)\n"
    "v_mfma_f32_16x16x32_bf16 v[68:71], v[32:35], a[8:11], v[68:71]\n"
    "ds_read_b128 v[24:27], v8 offset:17216\n"
    "v_mfma_f32_16x16x32_bf16 v[76:79], v[32:35], a[12:15], v[76:79]\n"
    "v_mfma_f32_16x16x32_bf16 v[72:75], v[28:31], a[12:15], v[72:75]\n"
    "s_waitcnt lgkmcnt(7)\n"
    "v_mfma_f32_16x16x32_bf16 v[64:67], v[36:39], a[16:19], v[64:67]\n"
    "ds_read_b128 v[28:31], v8 offset:384\n"
    "s_waitcnt lgkmcnt(7)\n"
    "v_mfma_f32_16x16x32_bf16 v[68:71], v[40:43], a[16:19], v[68:71]\n"
    "ds_read_b128 v[32:35], v8 offset:17280\n"
    "v_mfma_f32_16x16x32_bf16 v[76:79], v[40:43], a[20:23], v[76:79]\n"
    "v_mfma_f32_16x16x32_bf16 v[72:75], v[36:39], a[20:23], v[72:75]\n"
    "s_waitcnt lgkmcnt(7)\n"
    "v_mfma_f32_16x16x32_bf16 v[64:67], v[44:47], a[24:27], v[64:67]\n"
    "ds_read_b128 v[36:39], v8 offset:448\n"
    "s_waitcnt lgkmcnt(7)\n"
    "v_mfma_f32_16x16x32_bf16 v[68:71], v[48:51], a[24:27], v[68:71]\n"
    "ds_read_b128 v[40:43], v8 offset:17344\n"
    "v_mfma_f32_16x16x32_bf16 v[76:79], v[48:51], a[28:31], v[76:79]\n"
    "v_mfma_f32_16x16x32_bf16 v[72:75], v[44:47], a[28:31], v[72:75]\n"
    "s_waitcnt lgkmcnt(7)\n"
    "v_mfma_f32_16x16x32_bf16 v[64:67], v[52:55], a[32:35], v[64:67]\n"
    "ds_read_b128 v[44:47], v8 offset:512\n"
    "s_waitcnt lgkmcnt(7)\n"
    "v_mfma_f32_16x16x32_bf16 v[68:71], v[56:59], a[32:35], v[68:71]\n"
    "ds_read_b128 v[48:51], v8 offset:17408\n"
    "v_mfma_f32_16x16x32_bf16 v[76:79], v[56:59], a[36:39], v[76:79]\n"
    "v_mfma_f32_16x16x32_bf16 v[72:75], v[52:55], a[36:39], v[72:75]\n"
    "s_waitcnt lgkmcnt(7)\n"
    "v_mfma_f32_16x16x32_bf16 v[64:67], v[20:23], a[40:43], v[64:67]\n"
    "ds_read_b128 v[52:55], v8 offset:576\n"
    "s_waitcnt lgkmcnt(7)\n"
    "v_mfma_f32_16x16x32_bf16 v[68:71], v[24:27], a[40:43], v[68:71]\n"
    "ds_read_b128 v[56:59], v8 offset:17472\n"
    "v_mfma_f32_16x16x32_bf16 v[76:79], v[24:27], a[44:47], v[76:79]\n"
    "v_mfma_f32_16x16x32_bf16 v[72:75], v[20:23], a[44:47], v[72:75]\n"
    "s_waitcnt lgkmcnt(7)\n"
    "v_mfma_f32_16x16x32_bf16 v[64:67], v[28:31], a[48:51], v[64:67]\n"
    "ds_read_b128 v[20:23], v8 offset:640\n"
    "s_waitcnt lgkmcnt(7)\n"
    "v_mfma_f32_16x16x32_bf16 v[68:71], v[32:35], a[48:51], v[68:71]\n"
    "ds_read_b128 v[24:27], v8 offset:17536\n"
    "v_mfma_f32_16x16x32_bf16 v[76:79], v[32:35], a[52:55], v[76:79]\n"
    "v_mfma_f32_16x16x32_bf16 v[72:75], v[28:31], a[52:55], v[72:75]\n"
    "s_waitcnt lgkmcnt(7)\n"
    "v_mfma_f32_16x16x32_bf16 v[64:67], v[36:39], a[56:59], v[64:67]\n"
    "ds_read_b128 v[28:31], v8 offset:704\n"
    "s_waitcnt lgkmcnt(7)\n"
    "v_mfma_f32_16x16x32_bf16 v[68:71], v[40:43], a[56:59], v[68:71]\n"
    "ds_read_b128 v[32:35], v8 offset:17600\n"
    "v_mfma_f32_16x16x32_bf16 v[76:79], v[40:43], a[60:63], v[76:79]\n"
    "v_mfma_f32_16x16x32_bf16 v[72:75], v[36:39], a[60:63], v[72:75]\n"
    "s_waitcnt lgkmcnt(7)\n"
    "v_mfma_f32_16x16x32_bf16 v[64:67], v[44:47], a[64:67], v[64:67]\n"
    "ds_read_b128 v[36:39], v8 offset:768\n"
    "s_waitcnt lgkmcnt(7)\n"
    "v_mfma_f32_16x16x32_bf16 v[68:71], v[48:51], a[64:67], v[68:71]\n"
    "ds_read_b128 v[40:43], v8 offset:17664\n"
    "v_mfma_f32_16x16x32_bf16 v[76:79], v[48:51], a[68:71], v[76:79]\n"
    "v_mfma_f32_16x16x32_bf16 v[72:75], v[44:47], a[68:71], v[72:75]\n"
    "s_waitcnt lgkmcnt(7)\n"
    "v_mfma_f32_16x16x32_bf16 v[64:67], v[52:55], a[72:75], v[64:67]\n"
    "ds_read_b128 v[44:47], v8 offset:832\n"
    "s_waitcnt lgkmcnt(7)\n"
    "v_mfma_f32_16x16x32_bf16 v[68:71], v[56:59], a[72:75], v[68:71]\n"
    "ds_read_b128 v[48:51], v8 offset:17728\n"
    "v_mfma_f32_16x16x32_bf16 v[76:79], v[56:59], a[76:79], v[76:79]\n"
    "v_mfma_f32_16x16x32_bf16 v[72:75], v[52:55], a[76:79], v[72:75]\n"
    "s_waitcnt lgkmcnt(7)\n"
    "v_mfma_f32_16x16x32_bf16 v[64:67], v[20:23], a[80:83], v[64:67]\n"
    "ds_read_b128 v[52:55], v8 offset:896\n"
    "s_waitcnt lgkmcnt(7)\n"
    "v_mfma_f32_16x16x32_bf16 v[68:71], v[24:27], a[80:83], v[68:71]\n"
    "ds_read_b128 v[56:59], v8 offset:17792\n"
    "v_mfma_f32_16x16x32_bf16 v[76:79], v[24:27], a[84:87], v[76:79]\n"
    "v_mfma_f32_16x16x32_bf16 v[72:75], v[20:23], a[84:87], v[72:75]\n"
    "s_waitcnt lgkmcnt(7)\n"
    "v_mfma_f32_16x16x32_bf16 v[64:67], v[28:31], a[88:91], v[64:67]\n"
    "ds_read_b128 v[20:23], v8 offset:960\n"
    "s_waitcnt lgkmcnt(7)\n"
    "v_mfma_f32_16x16x32_bf16 v[68:71], v[32:35], a[88:91], v[68:71]\n"
    "ds_read_b128 v[24:27], v8 offset:17856\n"
    "v_mfma_f32_16x16x32_bf16 v[76:79], v[32:35], a[92:95], v[76:79]\n"
    "v_mfma_f32_16x16x32_bf16 v[72:75], v[28:31], a[92:95], v[72:75]\n"
    "s_waitcnt lgkmcnt(7)\n"
    "v_mfma_f32_16x16x32_bf16 v[64:67], v[36:39], a[96:99], v[64:67]\n"
    "ds_read_b128 v[28:31], v8 offset:0\n"
    "s_waitcnt lgkmcnt(7)\n"
    "v_mfma_f32_16x16x32_bf16 v[68:71], v[40:43], a[96:99], v[68:71]\n"
    "ds_read_b128 v[32:35], v8 offset:16896\n"
    "s_waitcnt vmcnt(8)\n"
    "s_barrier\n"
    "v_readfirstlane_b32 s38, v5\n"
    "s_or_b32 s52, s52, s68\n"
    "s_or_b32 s38, s38, s68\n"
    "s_cbranch_scc0 LK4_%=\n"
    "LJ4_%=:\n"
    "buffer_store_dword v5, v9, s[16:19], s52 offen sc1\n"
    "s_mov_b32 s52, 0x80000000\n"
    "v_mfma_f32_16x16x32_bf16 v[76:79], v[40:43], a[100:103], v[76:79]\n"
    "v_mfma_f32_16x16x32_bf16 v[72:75], v[36:39], a[100:103], v[72:75]\n"
    "s_waitcnt lgkmcnt(7)\n"
    "v_mfma_f32_16x16x32_bf16 v[64:67], v[44:47], a[104:107], v[64:67]\n"
    "ds_read_b128 v[36:39], v8 offset:64\n"
    "s_waitcnt lgkmcnt(7)\n"
    "v_mfma_f32_16x16x32_bf16 v[68:71], v[48:51], a[104:107], v[68:71]\n"
    "ds_read_b128 v[40:43], v8 offset:16960\n"
    "v_mfma_f32_16x16x32_bf16 v[76:79], v[48:51], a[108:111], v[76:79]\n"
    "v_mfma_f32_16x16x32_bf16 v[72:75], v[44:47], a[108:111], v[72:75]\n"
    "s_waitcnt lgkmcnt(7)\n"
    "v_mfma_f32_16x16x32_bf16 v[64:67], v[52:55], a[112:115], v[64:67]\n"
    "ds_read_b128 v[44:47], v8 offset:128\n"
    "s_waitcnt lgkmcnt(7)\n"
    "v_mfma_f32_16x16x32_bf16 v[68:71], v[56:59], a[112:115], v[68:71]\n"
    "ds_read_b128 v[48:51], v8 offset:17024\n"
    "v_mfma_f32_16x16x32_bf16 v[76:79], v[56:59], a[116:119], v[76:79]\n"
    "v_mfma_f32_16x16x32_bf16 v[72:75], v[52:55], a[116:119], v[72:75]\n"
    "s_waitcnt lgkmcnt(7)\n"
    "v_mfma_f32_16x16x32_bf16 v[64:67], v[20:23], a[120:123], v[64:67]\n"
    "ds_read_b128 v[52:55], v8 offset:192\n"
    "s_waitcnt lgkmcnt(7)\n"
    "v_mfma_f32_16x16x32_bf16 v[68:71], v[24:27], a[120:123], v[68:71]\n"
    "ds_read_b128 v[56:59], v8 offset:17088\n"
    "v_mfma_f32_16x16x32_bf16 v[76:79], v[24:27], a[124:127], v[76:79]\n"
    "v_mfma_f32_16x16x32_bf16 v[72:75], v[20:23], a[124:127], v[72:75]\n"
    "s_waitcnt lgkmcnt(7)\n"
    "v_mfma_f32_16x16x32_bf16 v[80:83], v[28:31], a[128:131], 0\n"
    "ds_read_b128 v[20:23], v8 offset:256\n"
    "s_waitcnt lgkmcnt(7)\n"
    "v_mfma_f32_16x16x32_bf16 v[84:87], v[32:35], a[128:131], 0\n"
    "ds_read_b128 v[24:27], v8 offset:17152\n"
    "buffer_load_dword v6, off, s[16:19], s53 sc1\n"
    "v_mfma_f32_16x16x32_bf16 v[92:95], v[32:35], a[132:135], 0\n"
    "v_mfma_f32_16x16x32_bf16 v[88:91], v[28:31], a[132:135], 0\n"
    "s_waitcnt lgkmcnt(7)\n"
    "v_mfma_f32_16x16x32_bf16 v[80:83], v[36:39], a[136:139], v[80:83]\n"
    "ds_read_b128 v[28:31], v8 offset:320\n"
    "s_waitcnt lgkmcnt(7)\n"
    "v_mfma_f32_16x16x32_bf16 v[84:87], v[40:43], a[136:139], v[84:87]\n"
    "ds_read_b128 v[32:35], v8 offset:17216\n"
    "v_cvt_pk_bf16_f32 v12, v64, v65\n"
    "v_mfma_f32_16x16x32_bf16 v[92:95], v[40:43], a[140:143], v[92:95]\n"
    "v_cvt_pk_bf16_f32 v13, v66, v67\n"
    "v_mfma_f32_16x16x32_bf16 v[88:91], v[36:39], a[140:143], v[88:91]\n"
    "s_waitcnt lgkmcnt(7)\n"
    "v_cvt_pk_bf16_f32 v14, v68, v69\n"
    "v_mfma_f32_16x16x32_bf16 v[80:83], v[44:47], a[144:147], v[80:83]\n"
    "ds_read_b128 v[36:39], v8 offset:384\n"
    "s_waitcnt lgkmcnt(7)\n"
    "v_mfma_f32_16x16x32_bf16 v[84:87], v[48:51], a[144:147], v[84:87]\n"
    "ds_read_b128 v[40:43], v8 offset:17280\n"
    "v_cvt_pk_bf16_f32 v15, v70, v71\n"
    "v_mfma_f32_16x16x32_bf16 v[92:95], v[48:51], a[148:151], v[92:95]\n"
    "buffer_store_dwordx4 v[12:15], v0, s[16:19], s59 offen sc1\n"
    "v_mfma_f32_16x16x32_bf16 v[88:91], v[44:47], a[148:151], v[88:91]\n"
    "s_waitcnt lgkmcnt(7)\n"
    "v_cvt_pk_bf16_f32 v16, v72, v73\n"
    "v_mfma_f32_16x16x32_bf16 v[80:83], v[52:55], a[152:155], v[80:83]\n"
    "ds_read_b128 v[44:47], v8 offset:448\n"
    "s_waitcnt lgkmcnt(7)\n"
    "v_mfma_f32_16x16x32_bf16 v[84:87], v[56:59], a[152:155], v[84:87]\n"
    "ds_read_b128 v[48:51], v8 offset:17344\n"
    "v_cvt_pk_bf16_f32 v17, v74, v75\n"
    "v_mfma_f32_16x16x32_bf16 v[92:95], v[56:59], a[156:159], v[92:95]\n"
    "v_cvt_pk_bf16_f32 v18, v76, v77\n"
    "v_mfma_f32_16x16x32_bf16 v[88:91], v[52:55], a[156:159], v[88:91]\n"
    "s_waitcnt lgkmcnt(7)\n"
    "v_cvt_pk_bf16_f32 v19, v78, v79\n"
    "v_mfma_f32_16x16x32_bf16 v[80:83], v[20:23], a[160:163], v[80:83]\n"
    "ds_read_b128 v[52:55], v8 offset:512\n"
    "s_waitcnt lgkmcnt(7)\n"
    "v_mfma_f32_16x16x32_bf16 v[84:87], v[24:27], a[160:163], v[84:87]\n"
    "ds_read_b128 v[56:59], v8 offset:17408\n"
    "buffer_store_dwordx4 v[16:19], v0, s[16:19], s62 offen sc1\n"
    "v_mfma_f32_16x16x32_bf16 v[92:95], v[24:27], a[164:167], v[92:95]\n"
    "s_add_u32 s59, s59, 16384\n"
    "s_add_u32 s62, s62, 16384\n"
    "v_mfma_f32_16x16x32_bf16 v[88:91], v[20:23], a[164:167], v[88:91]\n"
    "s_waitcnt lgkmcnt(7)\n"
    "v_mfma_f32_16x16x32_bf16 v[80:83], v[28:31], a[168:171], v[80:83]\n"
    "ds_read_b128 v[20:23], v8 offset:576\n"
    "s_waitcnt lgkmcnt(7)\n"
    "v_mfma_f32_16x16x32_bf16 v[84:87], v[32:35], a[168:171], v[84:87]\n"
    "ds_read_b128 v[24:27], v8 offset:17472\n"
    "v_mfma_f32_16x16x32_bf16 v[92:95], v[32:35], a[172:175], v[92:95]\n"
    "v_mfma_f32_16x16x32_bf16 v[88:91], v[28:31], a[172:175], v[88:91]\n"
    "s_waitcnt lgkmcnt(7)\n"
    "v_mfma_f32_16x16x32_bf16 v[80:83], v[36:39], a[176:179], v[80:83]\n"
    "ds_read_b128 v[28:31], v8 offset:640\n"
    "s_waitcnt lgkmcnt(7)\n"
    "v_mfma_f32_16x16x32_bf16 v[84:87], v[40:43], a[176:179], v[84:87]\n"
    "ds_read_b128 v[32:35], v8 offset:17536\n"
    "v_mfma_f32_16x16x32_bf16 v[92:95], v[40:43], a[180:183], v[92:95]\n"
    "v_mfma_f32_16x16x32_bf16 v[88:91], v[36:39], a[180:183], v[88:91]\n"
    "s_waitcnt lgkmcnt(7)\n"
    "v_mfma_f32_16x16x32_bf16 v[80:83], v[44:47], a[184:187], v[80:83]\n"
    "ds_read_b128 v[36:39], v8 offset:704\n"
    "s_waitcnt lgkmcnt(7)\n"
    "v_mfma_f32_16x16x32_bf16 v[84:87], v[48:51], a[184:187], v[84:87]\n"
    "ds_read_b128 v[40:43], v8 offset:17600\n"
    "s_waitcnt vmcnt(2)\n"
    "ds_write_b32 v11, v6 offset:64\n"
    "v_mfma_f32_16x16x32_bf16 v[92:95], v[48:51], a[188:191], v[92:95]\n"
    "v_mfma_f32_16x16x32_bf16 v[88:91], v[44:47], a[188:191], v[88:91]\n"
    "s_waitcnt lgkmcnt(8)\n"
    "v_mfma_f32_16x16x32_bf16 v[80:83], v[52:55], a[192:195], v[80:83]\n"
    "ds_read_b128 v[44:47], v8 offset:768\n"
    "s_waitcnt lgkmcnt(8)\n"
    "v_mfma_f32_16x16x32_bf16 v[84:87], v[56:59], a[192:195], v[84:87]\n"
    "ds_read_b128 v[48:51], v8 offset:17664\n"
    "v_mfma_f32_16x16x32_bf16 v[92:95], v[56:59], a[196:199], v[92:95]\n"
    "v_mfma_f32_16x16x32_bf16 v[88:91], v[52:55], a[196:199], v[88:91]\n"
    "s_waitcnt lgkmcnt(8)\n"
    "v_mfma_f32_16x16x32_bf16 v[80:83], v[20:23], a[200:203], v[80:83]\n"
    "ds_read_b128 v[52:55], v8 offset:832\n"
    "s_waitcnt lgkmcnt(8)\n"
    "v_mfma_f32_16x16x32_bf16 v[84:87], v[24:27], a[200:203], v[84:87]\n"
    "ds_read_b128 v[56:59], v8 offset:17728\n"
    "s_waitcnt lgkmcnt(2)\n"
    "s_barrier\n"
    "ds_read_b32 v6, v10 offset:64\n"
    "v_mfma_f32_16x16x32_bf16 v[92:95], v[24:27], a[204:207], v[92:95]\n"
    "v_mfma_f32_16x16x32_bf16 v[88:91], v[20:23], a[204:207], v[88:91]\n"
    "v_mfma_f32_16x16x32_bf16 v[80:83], v[28:31], a[208:211], v[80:83]\n"
    "ds_read_b128 v[20:23], v8 offset:896\n"
    "v_mfma_f32_16x16x32_bf16 v[84:87], v[32:35], a[208:211], v[84:87]\n"
    "ds_read_b128 v[24:27], v8 offset:17792\n"
    "s_lshl_b32 s63, s41, 16\n"
    "s_add_u32 s63, s63, s46\n"
    "s_lshr_b32 s38, s63, 8\n"
    "s_xor_b32 s38, s38, s48\n"
    "s_and_b32 s38, s38, 0x2000\n"
    "s_or_b32 s63, s63, s38\n"
    "v_mfma_f32_16x16x32_bf16 v[92:95], v[32:35], a[212:215], v[92:95]\n"
    "v_mfma_f32_16x16x32_bf16 v[88:91], v[28:31], a[212:215], v[88:91]\n"
    "s_waitcnt lgkmcnt(2)\n"
    "v_readfirstlane_b32 s60, v6\n"
    "s_cmp_lg_u32 s60, 0\n"
    "s_cbranch_scc1 LITA%=\n"
    "v_mfma_f32_16x16x32_bf16 v[80:83], v[36:39], a[216:219], v[80:83]\n"
    "ds_read_b128 v[28:31], v8 offset:960\n"
    "v_mfma_f32_16x16x32_bf16 v[84:87], v[40:43], a[216:219], v[84:87]\n"
    "ds_read_b128 v[32:35], v8 offset:17856\n"
    "v_mfma_f32_16x16x32_bf16 v[92:95], v[40:43], a[220:223], v[92:95]\n"
    "v_mfma_f32_16x16x32_bf16 v[88:91], v[36:39], a[220:223], v[88:91]\n"
    "v_mfma_f32_16x16x32_bf16 v[80:83], v[44:47], a[224:227], v[80:83]\n"
    "v_mfma_f32_16x16x32_bf16 v[84:87], v[48:51], a[224:227], v[84:87]\n"
    "v_mfma_f32_16x16x32_bf16 v[92:95], v[48:51], a[228:231], v[92:95]\n"
    "v_mfma_f32_16x16x32_bf16 v[88:91], v[44:47], a[228:231], v[88:91]\n"
    "v_mfma_f32_16x16x32_bf16 v[80:83], v[52:55], a[232:235], v[80:83]\n"
    "v_mfma_f32_16x16x32_bf16 v[84:87], v[56:59], a[232:235], v[84:87]\n"
    "v_mfma_f32_16x16x32_bf16 v[92:95], v[56:59], a[236:239], v[92:95]\n"
    "v_mfma_f32_16x16x32_bf16 v[88:91], v[52:55], a[236:239], v[88:91]\n"
    "s_waitcnt lgkmcnt(3)\n"
    "v_mfma_f32_16x16x32_bf16 v[80:83], v[20:23], a[240:243], v[80:83]\n"
    "s_waitcnt lgkmcnt(2)\n"
    "v_mfma_f32_16x16x32_bf16 v[84:87], v[24:27], a[240:243], v[84:87]\n"
    "v_mfma_f32_16x16x32_bf16 v[92:95], v[24:27], a[244:247], v[92:95]\n"
    "v_mfma_f32_16x16x32_bf16 v[88:91], v[20:23], a[244:247], v[88:91]\n"
    "s_waitcnt lgkmcnt(1)\n"
    "v_mfma_f32_16x16x32_bf16 v[80:83], v[28:31], a[248:251], v[80:83]\n"
    "s_waitcnt lgkmcnt(0)\n"
    "v_mfma_f32_16x16x32_bf16 v[84:87], v[32:35], a[248:251], v[84:87]\n"
    "v_mfma_f32_16x16x32_bf16 v[92:95], v[32:35], a[252:255], v[92:95]\n"
    "v_mfma_f32_16x16x32_bf16 v[88:91], v[28:31], a[252:255], v[88:91]\n"
    "s_branch LITB%=\n"
    "LITA%=:\n"
    "v_mfma_f32_16x16x32_bf16 v[80:83], v[36:39], a[216:219], v[80:83]\n"
    "ds_read_b128 v[28:31], v8 offset:960\n"
    "v_mfma_f32_16x16x32_bf16 v[84:87], v[40:43], a[216:219], v[84:87]\n"
    "ds_read_b128 v[32:35], v8 offset:17856\n"
    "s_add_u32 s39, s44, 67584\n"
    "s_add_u32 s69, s63, 4096\n"
    "v_mfma_f32_16x16x32_bf16 v[92:95], v[40:43], a[220:223], v[92:95]\n"
    "s_add_u32 m0, s39, 0\n"
    "s_nop 0\n"
    "buffer_load_dwordx4 v0, s[16:19], s63 offen offset:0 sc1 lds\n"
    "v_mfma_f32_16x16x32_bf16 v[88:91], v[36:39], a[220:223], v[88:91]\n"
    "v_mfma_f32_16x16x32_bf16 v[80:83], v[44:47], a[224:227], v[80:83]\n"
    "v_mfma_f32_16x16x32_bf16 v[84:87], v[48:51], a[224:227], v[84:87]\n"
    "s_add_u32 m0, s39, 32\n"
    "s_nop 0\n"
    "buffer_load_dwordx4 v0, s[16:19], s63 offen offset:1024 sc1 lds\n"
    "v_mfma_f32_16x16x32_bf16 v[92:95], v[48:51], a[228:231], v[92:95]\n"
    "s_add_u32 m0, s39, 64\n"
    "s_nop 0\n"
    "buffer_load_dwordx4 v0, s[16:19], s63 offen offset:2048 sc1 lds\n"
    "v_mfma_f32_16x16x32_bf16 v[88:91], v[44:47], a[228:231], v[88:91]\n"
    "v_mfma_f32_16x16x32_bf16 v[80:83], v[52:55], a[232:235], v[80:83]\n"
    "v_mfma_f32_16x16x32_bf16 v[84:87], v[56:59], a[232:235], v[84:87]\n"
    "s_add_u32 m0, s39, 96\n"
    "s_nop 0\n"
    "buffer_load_dwordx4 v0, s[16:19], s63 offen offset:3072 sc1 lds\n"
    "v_mfma_f32_16x16x32_bf16 v[92:95], v[56:59], a[236:239], v[92:95]\n"
    "s_add_u32 m0, s39, 4224\n"
    "s_nop 0\n"
    "buffer_load_dwordx4 v0, s[16:19], s69 offen offset:0 sc1 lds\n"
    "v_mfma_f32_16x16x32_bf16 v[88:91], v[52:55], a[236:239], v[88:91]\n"
    "s_waitcnt lgkmcnt(3)\n"
    "v_mfma_f32_16x16x32_bf16 v[80:83], v[20:23], a[240:243], v[80:83]\n"
    "s_waitcnt lgkmcnt(2)\n"
    "v_mfma_f32_16x16x32_bf16 v[84:87], v[24:27], a[240:243], v[84:87]\n"
    "s_add_u32 m0, s39, 4256\n"
    "s_nop 0\n"
    "buffer_load_dwordx4 v0, s[16:19], s69 offen offset:1024 sc1 lds\n"
    "v_mfma_f32_16x16x32_bf16 v[92:95], v[24:27], a[244:247], v[92:95]\n"
    "s_add_u32 m0, s39, 4288\n"
    "s_nop 0\n"
    "buffer_load_dwordx4 v0, s[16:19], s69 offen offset:2048 sc1 lds\n"
    "v_mfma_f32_16x16x32_bf16 v[88:91], v[20:23], a[244:247], v[88:91]\n"
    "s_waitcnt lgkmcnt(1)\n"
    "v_mfma_f32_16x16x32_bf16 v[80:83], v[28:31], a[248:251], v[80:83]\n"
    "s_waitcnt lgkmcnt(0)\n"
    "v_mfma_f32_16x16x32_bf16 v[84:87], v[32:35], a[248:251], v[84:87]\n"
    "s_add_u32 m0, s39, 4320\n"
    "s_nop 0\n"
    "buffer_load_dwordx4 v0, s[16:19], s69 offen offset:3072 sc1 lds\n"
    "v_mfma_f32_16x16x32_bf16 v[92:95], v[32:35], a[252:255], v[92:95]\n"
    "v_mfma_f32_16x16x32_bf16 v[88:91], v[28:31], a[252:255], v[88:91]\n"
    "LITB%=:\n"
    "s_nop 7\n"
    "v_cvt_pk_bf16_f32 v12, v80, v81\n"
    "v_cvt_pk_bf16_f32 v13, v82, v83\n"
    "v_cvt_pk_bf16_f32 v14, v84, v85\n"
    "v_cvt_pk_bf16_f32 v15, v86, v87\n"
    "buffer_store_dwordx4 v[12:15], v0, s[16:19], s59 offen sc1\n"
    "v_cvt_pk_bf16_f32 v16, v88, v89\n"
    "v_cvt_pk_bf16_f32 v17, v90, v91\n"
    "v_cvt_pk_bf16_f32 v18, v92, v93\n"
    "v_cvt_pk_bf16_f32 v19, v94, v95\n"
    "buffer_store_dwordx4 v[16:19], v0, s[16:19], s62 offen sc1\n"
    "s_mov_b32 s52, s64\n"
    "LIE%=:\n"
    "s_cmp_lg_u32 s60, 0\n"
    "s_cbranch_scc1 L2%=\n"
    "s_branch LT%=\n"
    "LX%=:\n"
    "s_barrier\n"
    "s_cmp_eq_u32 s60, 0\n"
    "s_cbranch_scc1 LT%=\n"
    "s_branch LE%=\n"
    "LRD%=:\n"
    "s_waitcnt lgkmcnt(0)\n"
    "s_barrier\n"
    "s_add_u32 s27, s27, 1\n"
    "s_cmp_gt_u32 s27, 0x40000\n"
    "s_cbranch_scc1 LE%=\n"
    "s_add_u32 s39, s44, 67584\n"
    "s_add_u32 s69, s63, 4096\n"
    "s_add_u32 m0, s39, 0\n"
    "s_nop 0\n"
    "buffer_load_dwordx4 v0, s[16:19], s63 offen offset:0 sc1 lds\n"
    "s_add_u32 m0, s39, 32\n"
    "s_nop 0\n"
    "buffer_load_dwordx4 v0, s[16:19], s63 offen offset:1024 sc1 lds\n"
    "s_add_u32 m0, s39, 64\n"
    "s_nop 0\n"
    "buffer_load_dwordx4 v0, s[16:19], s63 offen offset:2048 sc1 lds\n"
    "s_add_u32 m0, s39, 96\n"
    "s_nop 0\n"
    "buffer_load_dwordx4 v0, s[16:19], s63 offen offset:3072 sc1 lds\n"
    "s_add_u32 m0, s39, 4224\n"
    "s_nop 0\n"
    "buffer_load_dwordx4 v0, s[16:19], s69 offen offset:0 sc1 lds\n"
    "s_add_u32 m0, s39, 4256\n"
    "s_nop 0\n"
    "buffer_load_dwordx4 v0, s[16:19], s69 offen offset:1024 sc1 lds\n"
    "s_add_u32 m0, s39, 4288\n"
    "s_nop 0\n"
    "buffer_load_dwordx4 v0, s[16:19], s69 offen offset:2048 sc1 lds\n"
    "s_add_u32 m0, s39, 4320\n"
    "s_nop 0\n"
    "buffer_load_dwordx4 v0, s[16:19], s69 offen offset:3072 sc1 lds\n"
    "s_waitcnt vmcnt(0)\n"
    "s_branch LG%=\n"
    "LK0_%=:\n"
    "s_mov_b32 s37, 0x80000\n"
    "LL0_%=:\n"
    "buffer_load_dword v5, off, s[16:19], s67 sc1\n"
    "s_waitcnt vmcnt(0)\n"
    "v_readfirstlane_b32 s38, v5\n"
    "s_cmp_lg_u32 s38, 0\n"
    "s_cbranch_scc1 LJ0_%=\n"
    "s_sleep 1\n"
    "s_sub_u32 s37, s37, 1\n"
    "s_cbranch_scc0 LL0_%=\n"
    "s_endpgm\n"
    "LK1_%=:\n"
    "s_mov_b32 s37, 0x80000\n"
    "LL1_%=:\n"
    "buffer_load_dword v5, off, s[16:19], s67 sc1\n"
    "s_waitcnt vmcnt(0)\n"
    "v_readfirstlane_b32 s38, v5\n"
    "s_cmp_lg_u32 s38, 0\n"
    "s_cbranch_scc1 LJ1_%=\n"
    "s_sleep 1\n"
    "s_sub_u32 s37, s37, 1\n"
    "s_cbranch_scc0 LL1_%=\n"
    "s_endpgm\n"
    "LK2_%=:\n"
    "s_mov_b32 s37, 0x80000\n"
    "LL2_%=:\n"
    "buffer_load_dword v5, off, s[16:19], s67 sc1\n"
    "s_waitcnt vmcnt(0)\n"
    "v_readfirstlane_b32 s38, v5\n"
    "s_cmp_lg_u32 s38, 0\n"
    "s_cbranch_scc1 LJ2_%=\n"
    "s_sleep 1\n"
    "s_sub_u32 s37, s37, 1\n"
    "s_cbranch_scc0 LL2_%=\n"
    "s_endpgm\n"
    "LK3_%=:\n"
    "s_mov_b32 s37, 0x80000\n"
    "LL3_%=:\n"
    "buffer_load_dword v5, off, s[16:19], s67 sc1\n"
    "s_waitcnt vmcnt(0)\n"
    "v_readfirstlane_b32 s38, v5\n"
    "s_cmp_lg_u32 s38, 0\n"
    "s_cbranch_scc1 LJ3_%=\n"
    "s_sleep 1\n"
    "s_sub_u32 s37, s37, 1\n"
    "s_cbranch_scc0 LL3_%=\n"
    "s_endpgm\n"
    "LK4_%=:\n"
    "s_mov_b32 s37, 0x80000\n"
    "LL4_%=:\n"
    "buffer_load_dword v5, off, s[16:19], s67 sc1\n"
    "s_waitcnt vmcnt(0)\n"
    "v_readfirstlane_b32 s38, v5\n"
    "s_cmp_lg_u32 s38, 0\n"
    "s_cbranch_scc1 LJ4_%=\n"
    "s_sleep 1\n"
    "s_sub_u32 s37, s37, 1\n"
    "s_cbranch_scc0 LL4_%=\n"
    "s_endpgm\n"
    "LE%=:\n"
    "s_endpgm\n"
    :
    : "{v0}"(hv0), "{v1}"(hv1), "{v2}"(hv2), "{v3}"(hv3), "{v4}"(hv4), "{v5}"(hv5)
    : "v255", "a255", "s101", "vcc", "scc", "memory");
  __builtin_unreachable();
}
__device__ __forceinline__ void o_role8(int w, int V, int u, const unsigned* rdy, char* ws, const u16* Wo) {
  const int lane = lane_id(), g = lane >> 4, q = (lane >> 2) & 3, p = lane & 3, hg = u >> 2, cs = u & 3;
  const unsigned me = w == 1 ? 0u : OOR, c1 = 0u, co = V || !SW ? 0x2000u : 0u;
  const int pv[19] = {w, (V == 1 ? 4 : 3) * 524288 + hg * 131072 + w * 2048, (int)(V ? c1 ^ 0x2000u : c1), (int)co, (int)((nearof(Z_FO + V * 16384, V ? c1 ^ 0x2000u : c1) + hg * 2048) | (w ? OOR : 0u)), (int)((nearof(Z_FP + V * 16384, co) + u * 256) | me),
                      (int)(Z_PART + V * MBY(16)) + u * 16384 + w * 2048, (int)Z_ERR, (int)(size_t)Wo, (int)((size_t)Wo >> 32) & 0xffff, (int)(size_t)ws, (int)((size_t)ws >> 32) & 0xffff, (int)ZWS_BYTES,
                      (int)((Z_DBG + MBY(2) + (V * 16 + u) * 1024) | me), (int)(size_t)rdy, (int)((size_t)rdy >> 32), V ? ODL : 0, (int)(u ? OOR & ~me : 4096u), V < ROLL ? 3 : 63};
  int hv0 = lane * 16, hv1 = 131072 + (lane & 31) * 272 + w * 64 + (lane >> 5) * 16, hv2 = 131072 + (8 * w + g) * 272 + (lane & 15) * 16;
  int hv3 = (hg * 1024 + w * 128 + g) * 1024 + cs * 256 + ((lane & 15) ^ (g * 4)) * 16, hv4 = (4 * (g >> 1) + q) * 256 + 64 * (w ^ q) + 32 * (g & 1) + 16 * (p & 1) + 8 * (p >> 1), hv5 = 0;
#pragma unroll
  for (int i = 0; i < 19; ++i) hv5 = lane == i ? pv[i] : hv5;
  asm volatile(
    "s_nop 1\n"
    "v_readlane_b32 s36, v5, 0\n"
    "v_readlane_b32 s41, v5, 1\n"
    "v_readlane_b32 s42, v5, 2\n"
    "v_readlane_b32 s43, v5, 3\n"
    "v_readlane_b32 s44, v5, 4\n"
    "v_readlane_b32 s45, v5, 5\n"
    "v_readlane_b32 s46, v5, 6\n"
    "v_readlane_b32 s47, v5, 7\n"
    "v_readlane_b32 s20, v5, 8\n"
    "v_readlane_b32 s21, v5, 9\n"
    "v_readlane_b32 s16, v5, 10\n"
    "v_readlane_b32 s17, v5, 11\n"
    "v_readlane_b32 s18, v5, 12\n"
    "v_readlane_b32 s51, v5, 13\n"
    "v_readlane_b32 s24, v5, 14\n"
    "v_readlane_b32 s25, v5, 15\n"
    "v_readlane_b32 s53, v5, 16\n"
    "v_readlane_b32 s52, v5, 17\n"
    "v_readlane_b32 s55, v5, 18\n"
    "v_mov_b32 v5, 1\n"
    "s_mov_b32 s19, 0x20000\n"
    "s_mov_b32 s22, 0x400000\n"
    "s_mov_b32 s23, 0x20000\n"
    "s_lshl_b32 s39, s36, 15\n"
    "s_lshl_b32 s49, s36, 11\n"
    "s_mov_b32 s40, 0\n"
    "s_add_u32 m0, s39, 0\n"
    "s_mov_b32 s38, 0\n"
    "buffer_load_dwordx4 v3, s[20:23], s38 offen lds\n"
    "s_add_u32 m0, s39, 1024\n"
    "s_mov_b32 s38, 4096\n"
    "buffer_load_dwordx4 v3, s[20:23], s38 offen lds\n"
    "s_add_u32 m0, s39, 2048\n"
    "s_mov_b32 s38, 8192\n"
    "buffer_load_dwordx4 v3, s[20:23], s38 offen lds\n"
    "s_add_u32 m0, s39, 3072\n"
    "s_mov_b32 s38, 12288\n"
    "buffer_load_dwordx4 v3, s[20:23], s38 offen lds\n"
    "s_add_u32 m0, s39, 4096\n"
    "s_mov_b32 s38, 16384\n"
    "buffer_load_dwordx4 v3, s[20:23], s38 offen lds\n"
    "s_add_u32 m0, s39, 5120\n"
    "s_mov_b32 s38, 20480\n"
    "buffer_load_dwordx4 v3, s[20:23], s38 offen lds\n"
    "s_add_u32 m0, s39, 6144\n"
    "s_mov_b32 s38, 24576\n"
    "buffer_load_dwordx4 v3, s[20:23], s38 offen lds\n"
    "s_add_u32 m0, s39, 7168\n"
    "s_mov_b32 s38, 28672\n"
    "buffer_load_dwordx4 v3, s[20:23], s38 offen lds\n"
    "s_add_u32 m0, s39, 8192\n"
    "s_mov_b32 s38, 32768\n"
    "buffer_load_dwordx4 v3, s[20:23], s38 offen lds\n"
    "s_add_u32 m0, s39, 9216\n"
    "s_mov_b32 s38, 36864\n"
    "buffer_load_dwordx4 v3, s[20:23], s38 offen lds\n"
    "s_add_u32 m0, s39, 10240\n"
    "s_mov_b32 s38, 40960\n"
    "buffer_load_dwordx4 v3, s[20:23], s38 offen lds\n"
    "s_add_u32 m0, s39, 11264\n"
    "s_mov_b32 s38, 45056\n"
    "buffer_load_dwordx4 v3, s[20:23], s38 offen lds\n"
    "s_add_u32 m0, s39, 12288\n"
    "s_mov_b32 s38, 49152\n"
    "buffer_load_dwordx4 v3, s[20:23], s38 offen lds\n"
    "s_add_u32 m0, s39, 13312\n"
    "s_mov_b32 s38, 53248\n"
    "buffer_load_dwordx4 v3, s[20:23], s38 offen lds\n"
    "s_add_u32 m0, s39, 14336\n"
    "s_mov_b32 s38, 57344\n"
    "buffer_load_dwordx4 v3, s[20:23], s38 offen lds\n"
    "s_add_u32 m0, s39, 15360\n"
    "s_mov_b32 s38, 61440\n"
    "buffer_load_dwordx4 v3, s[20:23], s38 offen lds\n"
    "s_add_u32 m0, s39, 16384\n"
    "s_mov_b32 s38, 65536\n"
    "buffer_load_dwordx4 v3, s[20:23], s38 offen lds\n"
    "s_add_u32 m0, s39, 17408\n"
    "s_mov_b32 s38, 69632\n"
    "buffer_load_dwordx4 v3, s[20:23], s38 offen lds\n"
    "s_add_u32 m0, s39, 18432\n"
    "s_mov_b32 s38, 73728\n"
    "buffer_load_dwordx4 v3, s[20:23], s38 offen lds\n"
    "s_add_u32 m0, s39, 19456\n"
    "s_mov_b32 s38, 77824\n"
    "buffer_load_dwordx4 v3, s[20:23], s38 offen lds\n"
    "s_add_u32 m0, s39, 20480\n"
    "s_mov_b32 s38, 81920\n"
    "buffer_load_dwordx4 v3, s[20:23], s38 offen lds\n"
    "s_add_u32 m0, s39, 21504\n"
    "s_mov_b32 s38, 86016\n"
    "buffer_load_dwordx4 v3, s[20:23], s38 offen lds\n"
    "s_add_u32 m0, s39, 22528\n"
    "s_mov_b32 s38, 90112\n"
    "buffer_load_dwordx4 v3, s[20:23], s38 offen lds\n"
    "s_add_u32 m0, s39, 23552\n"
    "s_mov_b32 s38, 94208\n"
    "buffer_load_dwordx4 v3, s[20:23], s38 offen lds\n"
    "s_add_u32 m0, s39, 24576\n"
    "s_mov_b32 s38, 98304\n"
    "buffer_load_dwordx4 v3, s[20:23], s38 offen lds\n"
    "s_add_u32 m0, s39, 25600\n"
    "s_mov_b32 s38, 102400\n"
    "buffer_load_dwordx4 v3, s[20:23], s38 offen lds\n"
    "s_add_u32 m0, s39, 26624\n"
    "s_mov_b32 s38, 106496\n"
    "buffer_load_dwordx4 v3, s[20:23], s38 offen lds\n"
    "s_add_u32 m0, s39, 27648\n"
    "s_mov_b32 s38, 110592\n"
    "buffer_load_dwordx4 v3, s[20:23], s38 offen lds\n"
    "s_add_u32 m0, s39, 28672\n"
    "s_mov_b32 s38, 114688\n"
    "buffer_load_dwordx4 v3, s[20:23], s38 offen lds\n"
    "s_add_u32 m0, s39, 29696\n"
    "s_mov_b32 s38, 118784\n"
    "buffer_load_dwordx4 v3, s[20:23], s38 offen lds\n"
    "s_add_u32 m0, s39, 30720\n"
    "s_mov_b32 s38, 122880\n"
    "buffer_load_dwordx4 v3, s[20:23], s38 offen lds\n"
    "s_add_u32 m0, s39, 31744\n"
    "s_mov_b32 s38, 126976\n"
    "buffer_load_dwordx4 v3, s[20:23], s38 offen lds\n"
    "s_waitcnt vmcnt(0)\n"
    "s_barrier\n"
    "v_add_u32 v7, 0, v4\n"
    "ds_read_b64_tr_b16 a[0:1], v7 offset:0\n"
    "ds_read_b64_tr_b16 a[2:3], v7 offset:4096\n"
    "ds_read_b64_tr_b16 a[4:5], v7 offset:2048\n"
    "ds_read_b64_tr_b16 a[6:7], v7 offset:6144\n"
    "ds_read_b64_tr_b16 a[8:9], v7 offset:8192\n"
    "ds_read_b64_tr_b16 a[10:11], v7 offset:12288\n"
    "ds_read_b64_tr_b16 a[12:13], v7 offset:10240\n"
    "ds_read_b64_tr_b16 a[14:15], v7 offset:14336\n"
    "ds_read_b64_tr_b16 a[16:17], v7 offset:16384\n"
    "ds_read_b64_tr_b16 a[18:19], v7 offset:20480\n"
    "ds_read_b64_tr_b16 a[20:21], v7 offset:18432\n"
    "ds_read_b64_tr_b16 a[22:23], v7 offset:22528\n"
    "ds_read_b64_tr_b16 a[24:25], v7 offset:24576\n"
    "ds_read_b64_tr_b16 a[26:27], v7 offset:28672\n"
    "ds_read_b64_tr_b16 a[28:29], v7 offset:26624\n"
    "s_waitcnt lgkmcnt(11)\n"
    "ds_read_b64_tr_b16 a[30:31], v7 offset:30720\n"
    "ds_read_b64_tr_b16 a[32:33], v7 offset:32768\n"
    "ds_read_b64_tr_b16 a[34:35], v7 offset:36864\n"
    "ds_read_b64_tr_b16 a[36:37], v7 offset:34816\n"
    "s_waitcnt lgkmcnt(11)\n"
    "ds_read_b64_tr_b16 a[38:39], v7 offset:38912\n"
    "ds_read_b64_tr_b16 a[40:41], v7 offset:40960\n"
    "ds_read_b64_tr_b16 a[42:43], v7 offset:45056\n"
    "ds_read_b64_tr_b16 a[44:45], v7 offset:43008\n"
    "s_waitcnt lgkmcnt(11)\n"
    "ds_read_b64_tr_b16 a[46:47], v7 offset:47104\n"
    "ds_read_b64_tr_b16 a[48:49], v7 offset:49152\n"
    "ds_read_b64_tr_b16 a[50:51], v7 offset:53248\n"
    "ds_read_b64_tr_b16 a[52:53], v7 offset:51200\n"
    "s_waitcnt lgkmcnt(11)\n"
    "ds_read_b64_tr_b16 a[54:55], v7 offset:55296\n"
    "ds_read_b64_tr_b16 a[56:57], v7 offset:57344\n"
    "ds_read_b64_tr_b16 a[58:59], v7 offset:61440\n"
    "ds_read_b64_tr_b16 a[60:61], v7 offset:59392\n"
    "s_waitcnt lgkmcnt(11)\n"
    "ds_read_b64_tr_b16 a[62:63], v7 offset:63488\n"
    "s_waitcnt lgkmcnt(0)\n"
    "v_add_u32 v7, 65536, v4\n"
    "ds_read_b64_tr_b16 a[64:65], v7 offset:0\n"
    "ds_read_b64_tr_b16 a[66:67], v7 offset:4096\n"
    "ds_read_b64_tr_b16 a[68:69], v7 offset:2048\n"
    "ds_read_b64_tr_b16 a[70:71], v7 offset:6144\n"
    "ds_read_b64_tr_b16 a[72:73], v7 offset:8192\n"
    "ds_read_b64_tr_b16 a[74:75], v7 offset:12288\n"
    "ds_read_b64_tr_b16 a[76:77], v7 offset:10240\n"
    "ds_read_b64_tr_b16 a[78:79], v7 offset:14336\n"
    "ds_read_b64_tr_b16 a[80:81], v7 offset:16384\n"
    "ds_read_b64_tr_b16 a[82:83], v7 offset:20480\n"
    "ds_read_b64_tr_b16 a[84:85], v7 offset:18432\n"
    "ds_read_b64_tr_b16 a[86:87], v7 offset:22528\n"
    "ds_read_b64_tr_b16 a[88:89], v7 offset:24576\n"
    "ds_read_b64_tr_b16 a[90:91], v7 offset:28672\n"
    "ds_read_b64_tr_b16 a[92:93], v7 offset:26624\n"
    "s_waitcnt lgkmcnt(11)\n"
    "ds_read_b64_tr_b16 a[94:95], v7 offset:30720\n"
    "ds_read_b64_tr_b16 a[96:97], v7 offset:32768\n"
    "ds_read_b64_tr_b16 a[98:99], v7 offset:36864\n"
    "ds_read_b64_tr_b16 a[100:101], v7 offset:34816\n"
    "s_waitcnt lgkmcnt(11)\n"
    "ds_read_b64_tr_b16 a[102:103], v7 offset:38912\n"
    "ds_read_b64_tr_b16 a[104:105], v7 offset:40960\n"
    "ds_read_b64_tr_b16 a[106:107], v7 offset:45056\n"
    "ds_read_b64_tr_b16 a[108:109], v7 offset:43008\n"
    "s_waitcnt lgkmcnt(11)\n"
    "ds_read_b64_tr_b16 a[110:111], v7 offset:47104\n"
    "ds_read_b64_tr_b16 a[112:113], v7 offset:49152\n"
    "ds_read_b64_tr_b16 a[114:115], v7 offset:53248\n"
    "ds_read_b64_tr_b16 a[116:117], v7 offset:51200\n"
    "s_waitcnt lgkmcnt(11)\n"
    "ds_read_b64_tr_b16 a[118:119], v7 offset:55296\n"
    "ds_read_b64_tr_b16 a[120:121], v7 offset:57344\n"
    "ds_read_b64_tr_b16 a[122:123], v7 offset:61440\n"
    "ds_read_b64_tr_b16 a[124:125], v7 offset:59392\n"
    "s_waitcnt lgkmcnt(11)\n"
    "ds_read_b64_tr_b16 a[126:127], v7 offset:63488\n"
    "s_waitcnt lgkmcnt(0)\n"
    "s_barrier\n"
    "s_add_u32 m0, s39, 0\n"
    "s_mov_b32 s38, 524288\n"
    "buffer_load_dwordx4 v3, s[20:23], s38 offen lds\n"
    "s_add_u32 m0, s39, 1024\n"
    "s_mov_b32 s38, 528384\n"
    "buffer_load_dwordx4 v3, s[20:23], s38 offen lds\n"
    "s_add_u32 m0, s39, 2048\n"
    "s_mov_b32 s38, 532480\n"
    "buffer_load_dwordx4 v3, s[20:23], s38 offen lds\n"
    "s_add_u32 m0, s39, 3072\n"
    "s_mov_b32 s38, 536576\n"
    "buffer_load_dwordx4 v3, s[20:23], s38 offen lds\n"
    "s_add_u32 m0, s39, 4096\n"
    "s_mov_b32 s38, 540672\n"
    "buffer_load_dwordx4 v3, s[20:23], s38 offen lds\n"
    "s_add_u32 m0, s39, 5120\n"
    "s_mov_b32 s38, 544768\n"
    "buffer_load_dwordx4 v3, s[20:23], s38 offen lds\n"
    "s_add_u32 m0, s39, 6144\n"
    "s_mov_b32 s38, 548864\n"
    "buffer_load_dwordx4 v3, s[20:23], s38 offen lds\n"
    "s_add_u32 m0, s39, 7168\n"
    "s_mov_b32 s38, 552960\n"
    "buffer_load_dwordx4 v3, s[20:23], s38 offen lds\n"
    "s_add_u32 m0, s39, 8192\n"
    "s_mov_b32 s38, 557056\n"
    "buffer_load_dwordx4 v3, s[20:23], s38 offen lds\n"
    "s_add_u32 m0, s39, 9216\n"
    "s_mov_b32 s38, 561152\n"
    "buffer_load_dwordx4 v3, s[20:23], s38 offen lds\n"
    "s_add_u32 m0, s39, 10240\n"
    "s_mov_b32 s38, 565248\n"
    "buffer_load_dwordx4 v3, s[20:23], s38 offen lds\n"
    "s_add_u32 m0, s39, 11264\n"
    "s_mov_b32 s38, 569344\n"
    "buffer_load_dwordx4 v3, s[20:23], s38 offen lds\n"
    "s_add_u32 m0, s39, 12288\n"
    "s_mov_b32 s38, 573440\n"
    "buffer_load_dwordx4 v3, s[20:23], s38 offen lds\n"
    "s_add_u32 m0, s39, 13312\n"
    "s_mov_b32 s38, 577536\n"
    "buffer_load_dwordx4 v3, s[20:23], s38 offen lds\n"
    "s_add_u32 m0, s39, 14336\n"
    "s_mov_b32 s38, 581632\n"
    "buffer_load_dwordx4 v3, s[20:23], s38 offen lds\n"
    "s_add_u32 m0, s39, 15360\n"
    "s_mov_b32 s38, 585728\n"
    "buffer_load_dwordx4 v3, s[20:23], s38 offen lds\n"
    "s_add_u32 m0, s39, 16384\n"
    "s_mov_b32 s38, 589824\n"
    "buffer_load_dwordx4 v3, s[20:23], s38 offen lds\n"
    "s_add_u32 m0, s39, 17408\n"
    "s_mov_b32 s38, 593920\n"
    "buffer_load_dwordx4 v3, s[20:23], s38 offen lds\n"
    "s_add_u32 m0, s39, 18432\n"
    "s_mov_b32 s38, 598016\n"
    "buffer_load_dwordx4 v3, s[20:23], s38 offen lds\n"
    "s_add_u32 m0, s39, 19456\n"
    "s_mov_b32 s38, 602112\n"
    "buffer_load_dwordx4 v3, s[20:23], s38 offen lds\n"
    "s_add_u32 m0, s39, 20480\n"
    "s_mov_b32 s38, 606208\n"
    "buffer_load_dwordx4 v3, s[20:23], s38 offen lds\n"
    "s_add_u32 m0, s39, 21504\n"
    "s_mov_b32 s38, 610304\n"
    "buffer_load_dwordx4 v3, s[20:23], s38 offen lds\n"
    "s_add_u32 m0, s39, 22528\n"
    "s_mov_b32 s38, 614400\n"
    "buffer_load_dwordx4 v3, s[20:23], s38 offen lds\n"
    "s_add_u32 m0, s39, 23552\n"
    "s_mov_b32 s38, 618496\n"
    "buffer_load_dwordx4 v3, s[20:23], s38 offen lds\n"
    "s_add_u32 m0, s39, 24576\n"
    "s_mov_b32 s38, 622592\n"
    "buffer_load_dwordx4 v3, s[20:23], s38 offen lds\n"
    "s_add_u32 m0, s39, 25600\n"
    "s_mov_b32 s38, 626688\n"
    "buffer_load_dwordx4 v3, s[20:23], s38 offen lds\n"
    "s_add_u32 m0, s39, 26624\n"
    "s_mov_b32 s38, 630784\n"
    "buffer_load_dwordx4 v3, s[20:23], s38 offen lds\n"
    "s_add_u32 m0, s39, 27648\n"
    "s_mov_b32 s38, 634880\n"
    "buffer_load_dwordx4 v3, s[20:23], s38 offen lds\n"
    "s_add_u32 m0, s39, 28672\n"
    "s_mov_b32 s38, 638976\n"
    "buffer_load_dwordx4 v3, s[20:23], s38 offen lds\n"
    "s_add_u32 m0, s39, 29696\n"
    "s_mov_b32 s38, 643072\n"
    "buffer_load_dwordx4 v3, s[20:23], s38 offen lds\n"
    "s_add_u32 m0, s39, 30720\n"
    "s_mov_b32 s38, 647168\n"
    "buffer_load_dwordx4 v3, s[20:23], s38 offen lds\n"
    "s_add_u32 m0, s39, 31744\n"
    "s_mov_b32 s38, 651264\n"
    "buffer_load_dwordx4 v3, s[20:23], s38 offen lds\n"
    "s_waitcnt vmcnt(0)\n"
    "s_barrier\n"
    "v_add_u32 v7, 0, v4\n"
    "ds_read_b64_tr_b16 a[128:129], v7 offset:0\n"
    "ds_read_b64_tr_b16 a[130:131], v7 offset:4096\n"
    "ds_read_b64_tr_b16 a[132:133], v7 offset:2048\n"
    "ds_read_b64_tr_b16 a[134:135], v7 offset:6144\n"
    "ds_read_b64_tr_b16 a[136:137], v7 offset:8192\n"
    "ds_read_b64_tr_b16 a[138:139], v7 offset:12288\n"
    "ds_read_b64_tr_b16 a[140:141], v7 offset:10240\n"
    "ds_read_b64_tr_b16 a[142:143], v7 offset:14336\n"
    "ds_read_b64_tr_b16 a[144:145], v7 offset:16384\n"
    "ds_read_b64_tr_b16 a[146:147], v7 offset:20480\n"
    "ds_read_b64_tr_b16 a[148:149], v7 offset:18432\n"
    "ds_read_b64_tr_b16 a[150:151], v7 offset:22528\n"
    "ds_read_b64_tr_b16 a[152:153], v7 offset:24576\n"
    "ds_read_b64_tr_b16 a[154:155], v7 offset:28672\n"
    "ds_read_b64_tr_b16 a[156:157], v7 offset:26624\n"
    "s_waitcnt lgkmcnt(11)\n"
    "ds_read_b64_tr_b16 a[158:159], v7 offset:30720\n"
    "ds_read_b64_tr_b16 a[160:161], v7 offset:32768\n"
    "ds_read_b64_tr_b16 a[162:163], v7 offset:36864\n"
    "ds_read_b64_tr_b16 a[164:165], v7 offset:34816\n"
    "s_waitcnt lgkmcnt(11)\n"
    "ds_read_b64_tr_b16 a[166:167], v7 offset:38912\n"
    "ds_read_b64_tr_b16 a[168:169], v7 offset:40960\n"
    "ds_read_b64_tr_b16 a[170:171], v7 offset:45056\n"
    "ds_read_b64_tr_b16 a[172:173], v7 offset:43008\n"
    "s_waitcnt lgkmcnt(11)\n"
    "ds_read_b64_tr_b16 a[174:175], v7 offset:47104\n"
    "ds_read_b64_tr_b16 a[176:177], v7 offset:49152\n"
    "ds_read_b64_tr_b16 a[178:179], v7 offset:53248\n"
    "ds_read_b64_tr_b16 a[180:181], v7 offset:51200\n"
    "s_waitcnt lgkmcnt(11)\n"
    "ds_read_b64_tr_b16 a[182:183], v7 offset:55296\n"
    "ds_read_b64_tr_b16 a[184:185], v7 offset:57344\n"
    "ds_read_b64_tr_b16 a[186:187], v7 offset:61440\n"
    "ds_read_b64_tr_b16 a[188:189], v7 offset:59392\n"
    "s_waitcnt lgkmcnt(11)\n"
    "ds_read_b64_tr_b16 a[190:191], v7 offset:63488\n"
    "s_waitcnt lgkmcnt(0)\n"
    "v_add_u32 v7, 65536, v4\n"
    "ds_read_b64_tr_b16 a[192:193], v7 offset:0\n"
    "ds_read_b64_tr_b16 a[194:195], v7 offset:4096\n"
    "ds_read_b64_tr_b16 a[196:197], v7 offset:2048\n"
    "ds_read_b64_tr_b16 a[198:199], v7 offset:6144\n"
    "ds_read_b64_tr_b16 a[200:201], v7 offset:8192\n"
    "ds_read_b64_tr_b16 a[202:203], v7 offset:12288\n"
    "ds_read_b64_tr_b16 a[204:205], v7 offset:10240\n"
    "ds_read_b64_tr_b16 a[206:207], v7 offset:14336\n"
    "ds_read_b64_tr_b16 a[208:209], v7 offset:16384\n"
    "ds_read_b64_tr_b16 a[210:211], v7 offset:20480\n"
    "ds_read_b64_tr_b16 a[212:213], v7 offset:18432\n"
    "ds_read_b64_tr_b16 a[214:215], v7 offset:22528\n"
    "ds_read_b64_tr_b16 a[216:217], v7 offset:24576\n"
    "ds_read_b64_tr_b16 a[218:219], v7 offset:28672\n"
    "ds_read_b64_tr_b16 a[220:221], v7 offset:26624\n"
    "s_waitcnt lgkmcnt(11)\n"
    "ds_read_b64_tr_b16 a[222:223], v7 offset:30720\n"
    "ds_read_b64_tr_b16 a[224:225], v7 offset:32768\n"
    "ds_read_b64_tr_b16 a[226:227], v7 offset:36864\n"
    "ds_read_b64_tr_b16 a[228:229], v7 offset:34816\n"
    "s_waitcnt lgkmcnt(11)\n"
    "ds_read_b64_tr_b16 a[230:231], v7 offset:38912\n"
    "ds_read_b64_tr_b16 a[232:233], v7 offset:40960\n"
    "ds_read_b64_tr_b16 a[234:235], v7 offset:45056\n"
    "ds_read_b64_tr_b16 a[236:237], v7 offset:43008\n"
    "s_waitcnt lgkmcnt(11)\n"
    "ds_read_b64_tr_b16 a[238:239], v7 offset:47104\n"
    "ds_read_b64_tr_b16 a[240:241], v7 offset:49152\n"
    "ds_read_b64_tr_b16 a[242:243], v7 offset:53248\n"
    "ds_read_b64_tr_b16 a[244:245], v7 offset:51200\n"
    "s_waitcnt lgkmcnt(11)\n"
    "ds_read_b64_tr_b16 a[246:247], v7 offset:55296\n"
    "ds_read_b64_tr_b16 a[248:249], v7 offset:57344\n"
    "ds_read_b64_tr_b16 a[250:251], v7 offset:61440\n"
    "ds_read_b64_tr_b16 a[252:253], v7 offset:59392\n"
    "s_waitcnt lgkmcnt(11)\n"
    "ds_read_b64_tr_b16 a[254:255], v7 offset:63488\n"
    "s_waitcnt lgkmcnt(0)\n"
    "s_barrier\n"
    "v_mov_b32 v7, 0\n"
    "s_mov_b32 s37, 0x80000\n"
    "LRY%=:\n"
    "global_load_dword v6, v7, s[24:25] sc1\n"
    "s_waitcnt vmcnt(0)\n"
    "v_readfirstlane_b32 s38, v6\n"
    "s_cmp_lg_u32 s38, 0\n"
    "s_cbranch_scc1 LRZ%=\n"
    "s_sleep 8\n"
    "s_sub_u32 s37, s37, 1\n"
    "s_cbranch_scc0 LRY%=\n"
    "s_endpgm\n"
    "LRZ%=:\n"
    "s_lshl_b32 s38, s38, 13\n"
    "s_and_b32 s38, s38, 0x2000\n"
    "s_xor_b32 s42, s42, s38\n"
    "s_xor_b32 s43, s43, s38\n"
    "s_xor_b32 s44, s44, s38\n"
    "s_xor_b32 s45, s45, s38\n"
    "LDL%=:\n"
    "s_sub_u32 s53, s53, 1\n"
    "s_cbranch_scc1 LDM%=\n"
    "s_sleep 37\n"
    "s_branch LDL%=\n"
    "LDM%=:\n"
    "v_mov_b32 v80, -1\n"
    "v_mov_b32 v81, -1\n"
    "v_mov_b32 v82, -1\n"
    "v_mov_b32 v83, -1\n"
    "s_mov_b32 s54, s46\n"
    "s_mov_b32 s37, s55\n"
    "LF%=:\n"
    "s_lshr_b32 s38, s54, 8\n"
    "s_xor_b32 s38, s38, s43\n"
    "s_and_b32 s38, s38, 0x2000\n"
    "s_or_b32 s50, s54, s38\n"
    "buffer_store_dwordx4 v[80:83], v0, s[16:19], s50 offen offset:0 sc1\n"
    "buffer_store_dwordx4 v[80:83], v0, s[16:19], s50 offen offset:1024 sc1\n"
    "s_waitcnt vmcnt(24)\n"
    "s_add_u32 s54, s54, 262144\n"
    "s_sub_u32 s37, s37, 1\n"
    "s_cbranch_scc0 LF%=\n"
    "s_waitcnt vmcnt(0)\n"
    "s_barrier\n"
    "buffer_store_dword v5, off, s[16:19], s45 sc1\n"
    "s_add_u32 s45, s45, s52\n"
    "v_lshlrev_b32 v7, 4, v0\n"
    "v_and_b32 v7, 0x700, v7\n"
    "v_mov_b32 v9, 139776\n"
    "v_lshl_add_u32 v10, s36, 2, v9\n"
    "s_lshr_b32 s38, s41, 8\n"
    "s_xor_b32 s38, s38, s42\n"
    "s_and_b32 s38, s38, 0x2000\n"
    "s_or_b32 s48, s41, s38\n"
    "LT%=:\n"
    "s_cmp_lg_u32 s36, 0\n"
    "s_cbranch_scc1 LB%=\n"
    "s_mov_b32 s37, 0x80000\n"
    "LP%=:\n"
    "v_mov_b32 v6, 1\n"
    "s_mov_b64 exec, 0xff\n"
    "v_mov_b32 v6, 0\n"
    "buffer_atomic_or v6, v7, s[16:19], s44 offen sc0 sc1\n"
    "s_mov_b64 exec, -1\n"
    "s_waitcnt vmcnt(0)\n"
    "v_cmp_eq_u32 vcc, 0, v6\n"
    "s_cbranch_vccz LB%=\n"
    "s_sleep 1\n"
    "s_sub_u32 s37, s37, 1\n"
    "s_cbranch_scc0 LP%=\n"
    "buffer_store_dword v5, off, s[16:19], s47 sc1\n"
    "s_endpgm\n"
    "LB%=:\n"
    "s_barrier\n"
    "LC%=:\n"
    "s_add_u32 m0, s49, 0\n"
    "s_nop 0\n"
    "buffer_load_dwordx4 v0, s[16:19], s48 offen offset:0 sc1 lds\n"
    "buffer_load_dwordx4 v0, s[16:19], s48 offen offset:1024 sc1 lds\n"
    "s_add_u32 s48, s48, 16384\n"
    "s_add_u32 m0, s49, 8192\n"
    "s_nop 0\n"
    "buffer_load_dwordx4 v0, s[16:19], s48 offen offset:0 sc1 lds\n"
    "buffer_load_dwordx4 v0, s[16:19], s48 offen offset:1024 sc1 lds\n"
    "s_add_u32 s48, s48, 16384\n"
    "s_add_u32 m0, s49, 16384\n"
    "s_nop 0\n"
    "buffer_load_dwordx4 v0, s[16:19], s48 offen offset:0 sc1 lds\n"
    "buffer_load_dwordx4 v0, s[16:19], s48 offen offset:1024 sc1 lds\n"
    "s_add_u32 s48, s48, 16384\n"
    "s_add_u32 m0, s49, 24576\n"
    "s_nop 0\n"
    "buffer_load_dwordx4 v0, s[16:19], s48 offen offset:0 sc1 lds\n"
    "buffer_load_dwordx4 v0, s[16:19], s48 offen offset:1024 sc1 lds\n"
    "s_add_u32 s48, s48, 16384\n"
    "LG%=:\n"
    "s_lshr_b32 s38, s46, 8\n"
    "s_xor_b32 s38, s38, s43\n"
    "s_and_b32 s38, s38, 0x2000\n"
    "s_or_b32 s50, s46, s38\n"
    "s_add_u32 s41, s41, 2621440\n"
    "s_add_u32 s46, s46, 262144\n"
    "s_add_u32 s44, s44, 4\n"
    "s_add_u32 s40, s40, 1\n"
    "s_waitcnt vmcnt(6)\n"
    "s_barrier\n"
    "ds_read_b128 v[20:23], v0 offset:0\n"
    "ds_read_b128 v[24:27], v0 offset:1024\n"
    "ds_read_b128 v[28:31], v0 offset:2048\n"
    "ds_read_b128 v[32:35], v0 offset:3072\n"
    "ds_read_b128 v[36:39], v0 offset:4096\n"
    "ds_read_b128 v[40:43], v0 offset:5120\n"
    "s_waitcnt lgkmcnt(5)\n"
    "v_mfma_f32_32x32x16_bf16 v[64:79], a[0:3], v[20:23], 0\n"
    "s_add_u32 m0, s49, 32768\n"
    "s_nop 0\n"
    "buffer_load_dwordx4 v0, s[16:19], s48 offen offset:0 sc1 lds\n"
    "ds_read_b128 v[44:47], v0 offset:6144\n"
    "s_waitcnt lgkmcnt(5)\n"
    "v_mfma_f32_32x32x16_bf16 v[64:79], a[4:7], v[24:27], v[64:79]\n"
    "ds_read_b128 v[48:51], v0 offset:7168\n"
    "s_waitcnt lgkmcnt(5)\n"
    "v_mfma_f32_32x32x16_bf16 v[64:79], a[8:11], v[28:31], v[64:79]\n"
    "s_waitcnt vmcnt(5)\n"
    "s_barrier\n"
    "ds_read_b128 v[20:23], v0 offset:8192\n"
    "s_waitcnt lgkmcnt(5)\n"
    "v_mfma_f32_32x32x16_bf16 v[64:79], a[12:15], v[32:35], v[64:79]\n"
    "buffer_load_dwordx4 v0, s[16:19], s48 offen offset:1024 sc1 lds\n"
    "s_add_u32 s48, s48, 16384\n"
    "ds_read_b128 v[24:27], v0 offset:9216\n"
    "s_waitcnt lgkmcnt(5)\n"
    "v_mfma_f32_32x32x16_bf16 v[64:79], a[16:19], v[36:39], v[64:79]\n"
    "ds_read_b128 v[28:31], v0 offset:10240\n"
    "s_waitcnt lgkmcnt(5)\n"
    "v_mfma_f32_32x32x16_bf16 v[64:79], a[20:23], v[40:43], v[64:79]\n"
    "ds_read_b128 v[32:35], v0 offset:11264\n"
    "s_waitcnt lgkmcnt(5)\n"
    "v_mfma_f32_32x32x16_bf16 v[64:79], a[24:27], v[44:47], v[64:79]\n"
    "s_add_u32 m0, s49, 40960\n"
    "s_nop 0\n"
    "buffer_load_dwordx4 v0, s[16:19], s48 offen offset:0 sc1 lds\n"
    "ds_read_b128 v[36:39], v0 offset:12288\n"
    "s_waitcnt lgkmcnt(5)\n"
    "v_mfma_f32_32x32x16_bf16 v[64:79], a[28:31], v[48:51], v[64:79]\n"
    "ds_read_b128 v[40:43], v0 offset:13312\n"
    "s_waitcnt lgkmcnt(5)\n"
    "v_mfma_f32_32x32x16_bf16 v[64:79], a[32:35], v[20:23], v[64:79]\n"
    "ds_read_b128 v[44:47], v0 offset:14336\n"
    "s_waitcnt lgkmcnt(5)\n"
    "v_mfma_f32_32x32x16_bf16 v[64:79], a[36:39], v[24:27], v[64:79]\n"
    "buffer_load_dwordx4 v0, s[16:19], s48 offen offset:1024 sc1 lds\n"
    "s_add_u32 s48, s48, 16384\n"
    "ds_read_b128 v[48:51], v0 offset:15360\n"
    "s_waitcnt lgkmcnt(5)\n"
    "v_mfma_f32_32x32x16_bf16 v[64:79], a[40:43], v[28:31], v[64:79]\n"
    "s_waitcnt vmcnt(6)\n"
    "s_barrier\n"
    "ds_read_b128 v[20:23], v0 offset:16384\n"
    "s_waitcnt lgkmcnt(5)\n"
    "v_mfma_f32_32x32x16_bf16 v[64:79], a[44:47], v[32:35], v[64:79]\n"
    "ds_read_b128 v[24:27], v0 offset:17408\n"
    "s_waitcnt lgkmcnt(5)\n"
    "v_mfma_f32_32x32x16_bf16 v[64:79], a[48:51], v[36:39], v[64:79]\n"
    "s_add_u32 m0, s49, 49152\n"
    "s_nop 0\n"
    "buffer_load_dwordx4 v0, s[16:19], s48 offen offset:0 sc1 lds\n"
    "ds_read_b128 v[28:31], v0 offset:18432\n"
    "s_waitcnt lgkmcnt(5)\n"
    "v_mfma_f32_32x32x16_bf16 v[64:79], a[52:55], v[40:43], v[64:79]\n"
    "ds_read_b128 v[32:35], v0 offset:19456\n"
    "s_waitcnt lgkmcnt(5)\n"
    "v_mfma_f32_32x32x16_bf16 v[64:79], a[56:59], v[44:47], v[64:79]\n"
    "ds_read_b128 v[36:39], v0 offset:20480\n"
    "s_waitcnt lgkmcnt(5)\n"
    "v_mfma_f32_32x32x16_bf16 v[64:79], a[60:63], v[48:51], v[64:79]\n"
    "buffer_load_dwordx4 v0, s[16:19], s48 offen offset:1024 sc1 lds\n"
    "s_add_u32 s48, s48, 16384\n"
    "ds_read_b128 v[40:43], v0 offset:21504\n"
    "s_waitcnt lgkmcnt(5)\n"
    "v_mfma_f32_32x32x16_bf16 v[64:79], a[64:67], v[20:23], v[64:79]\n"
    "ds_read_b128 v[44:47], v0 offset:22528\n"
    "s_waitcnt lgkmcnt(5)\n"
    "v_mfma_f32_32x32x16_bf16 v[64:79], a[68:71], v[24:27], v[64:79]\n"
    "ds_read_b128 v[48:51], v0 offset:23552\n"
    "s_waitcnt lgkmcnt(5)\n"
    "v_mfma_f32_32x32x16_bf16 v[64:79], a[72:75], v[28:31], v[64:79]\n"
    "s_add_u32 m0, s49, 57344\n"
    "s_nop 0\n"
    "buffer_load_dwordx4 v0, s[16:19], s48 offen offset:0 sc1 lds\n"
    "s_waitcnt vmcnt(7)\n"
    "s_barrier\n"
    "ds_read_b128 v[20:23], v0 offset:24576\n"
    "s_waitcnt lgkmcnt(5)\n"
    "v_mfma_f32_32x32x16_bf16 v[64:79], a[76:79], v[32:35], v[64:79]\n"
    "ds_read_b128 v[24:27], v0 offset:25600\n"
    "s_waitcnt lgkmcnt(5)\n"
    "v_mfma_f32_32x32x16_bf16 v[64:79], a[80:83], v[36:39], v[64:79]\n"
    "ds_read_b128 v[28:31], v0 offset:26624\n"
    "s_waitcnt lgkmcnt(5)\n"
    "v_mfma_f32_32x32x16_bf16 v[64:79], a[84:87], v[40:43], v[64:79]\n"
    "buffer_load_dwordx4 v0, s[16:19], s48 offen offset:1024 sc1 lds\n"
    "s_add_u32 s48, s48, 16384\n"
    "s_lshr_b32 s38, s41, 8\n"
    "s_xor_b32 s38, s38, s42\n"
    "s_and_b32 s38, s38, 0x2000\n"
    "s_or_b32 s48, s41, s38\n"
    "ds_read_b128 v[32:35], v0 offset:27648\n"
    "s_waitcnt lgkmcnt(5)\n"
    "v_mfma_f32_32x32x16_bf16 v[64:79], a[88:91], v[44:47], v[64:79]\n"
    "ds_read_b128 v[36:39], v0 offset:28672\n"
    "s_waitcnt lgkmcnt(5)\n"
    "v_mfma_f32_32x32x16_bf16 v[64:79], a[92:95], v[48:51], v[64:79]\n"
    "ds_read_b128 v[40:43], v0 offset:29696\n"
    "s_waitcnt lgkmcnt(5)\n"
    "v_mfma_f32_32x32x16_bf16 v[64:79], a[96:99], v[20:23], v[64:79]\n"
    "ds_read_b128 v[44:47], v0 offset:30720\n"
    "s_waitcnt lgkmcnt(5)\n"
    "v_mfma_f32_32x32x16_bf16 v[64:79], a[100:103], v[24:27], v[64:79]\n"
    "ds_read_b128 v[48:51], v0 offset:31744\n"
    "s_waitcnt lgkmcnt(5)\n"
    "v_mfma_f32_32x32x16_bf16 v[64:79], a[104:107], v[28:31], v[64:79]\n"
    "v_mov_b32 v6, 1\n"
    "s_mov_b64 exec, 0xff\n"
    "v_mov_b32 v6, 0\n"
    "buffer_atomic_or v6, v7, s[16:19], s44 offen sc0 sc1\n"
    "s_mov_b64 exec, -1\n"
    "s_waitcnt vmcnt(7)\n"
    "s_barrier\n"
    "ds_read_b128 v[20:23], v0 offset:32768\n"
    "s_waitcnt lgkmcnt(5)\n"
    "v_mfma_f32_32x32x16_bf16 v[64:79], a[108:111], v[32:35], v[64:79]\n"
    "ds_read_b128 v[24:27], v0 offset:33792\n"
    "s_waitcnt lgkmcnt(5)\n"
    "v_mfma_f32_32x32x16_bf16 v[64:79], a[112:115], v[36:39], v[64:79]\n"
    "ds_read_b128 v[28:31], v0 offset:34816\n"
    "s_waitcnt lgkmcnt(5)\n"
    "v_mfma_f32_32x32x16_bf16 v[64:79], a[116:119], v[40:43], v[64:79]\n"
    "ds_read_b128 v[32:35], v0 offset:35840\n"
    "s_waitcnt lgkmcnt(5)\n"
    "v_mfma_f32_32x32x16_bf16 v[64:79], a[120:123], v[44:47], v[64:79]\n"
    "ds_read_b128 v[36:39], v0 offset:36864\n"
    "s_waitcnt lgkmcnt(5)\n"
    "v_mfma_f32_32x32x16_bf16 v[64:79], a[124:127], v[48:51], v[64:79]\n"
    "ds_read_b128 v[40:43], v0 offset:37888\n"
    "s_waitcnt lgkmcnt(5)\n"
    "v_mfma_f32_32x32x16_bf16 v[64:79], a[128:131], v[20:23], v[64:79]\n"
    "ds_read_b128 v[44:47], v0 offset:38912\n"
    "s_waitcnt lgkmcnt(5)\n"
    "v_mfma_f32_32x32x16_bf16 v[64:79], a[132:135], v[24:27], v[64:79]\n"
    "ds_read_b128 v[48:51], v0 offset:39936\n"
    "s_waitcnt lgkmcnt(5)\n"
    "v_mfma_f32_32x32x16_bf16 v[64:79], a[136:139], v[28:31], v[64:79]\n"
    "s_waitcnt vmcnt(5)\n"
    "s_barrier\n"
    "ds_read_b128 v[20:23], v0 offset:40960\n"
    "s_waitcnt lgkmcnt(5)\n"
    "v_mfma_f32_32x32x16_bf16 v[64:79], a[140:143], v[32:35], v[64:79]\n"
    "ds_read_b128 v[24:27], v0 offset:41984\n"
    "s_waitcnt lgkmcnt(5)\n"
    "v_mfma_f32_32x32x16_bf16 v[64:79], a[144:147], v[36:39], v[64:79]\n"
    "ds_read_b128 v[28:31], v0 offset:43008\n"
    "s_waitcnt lgkmcnt(5)\n"
    "v_mfma_f32_32x32x16_bf16 v[64:79], a[148:151], v[40:43], v[64:79]\n"
    "ds_read_b128 v[32:35], v0 offset:44032\n"
    "s_waitcnt lgkmcnt(5)\n"
    "v_mfma_f32_32x32x16_bf16 v[64:79], a[152:155], v[44:47], v[64:79]\n"
    "ds_read_b128 v[36:39], v0 offset:45056\n"
    "s_waitcnt lgkmcnt(5)\n"
    "v_mfma_f32_32x32x16_bf16 v[64:79], a[156:159], v[48:51], v[64:79]\n"
    "ds_read_b128 v[40:43], v0 offset:46080\n"
    "s_waitcnt lgkmcnt(5)\n"
    "v_mfma_f32_32x32x16_bf16 v[64:79], a[160:163], v[20:23], v[64:79]\n"
    "ds_read_b128 v[44:47], v0 offset:47104\n"
    "s_waitcnt lgkmcnt(5)\n"
    "v_mfma_f32_32x32x16_bf16 v[64:79], a[164:167], v[24:27], v[64:79]\n"
    "ds_read_b128 v[48:51], v0 offset:48128\n"
    "s_waitcnt lgkmcnt(5)\n"
    "v_mfma_f32_32x32x16_bf16 v[64:79], a[168:171], v[28:31], v[64:79]\n"
    "s_waitcnt vmcnt(3)\n"
    "s_barrier\n"
    "ds_read_b128 v[20:23], v0 offset:49152\n"
    "s_waitcnt lgkmcnt(5)\n"
    "v_mfma_f32_32x32x16_bf16 v[64:79], a[172:175], v[32:35], v[64:79]\n"
    "ds_read_b128 v[24:27], v0 offset:50176\n"
    "s_waitcnt lgkmcnt(5)\n"
    "v_mfma_f32_32x32x16_bf16 v[64:79], a[176:179], v[36:39], v[64:79]\n"
    "ds_read_b128 v[28:31], v0 offset:51200\n"
    "s_waitcnt lgkmcnt(5)\n"
    "v_mfma_f32_32x32x16_bf16 v[64:79], a[180:183], v[40:43], v[64:79]\n"
    "s_waitcnt vmcnt(0)\n"
    "v_cmp_eq_u32 vcc, 0, v6\n"
    "s_cmp_eq_u32 s40, 64\n"
    "s_cselect_b32 s38, 1, 0\n"
    "s_nop 1\n"
    "s_or_b32 s38, s38, vcc_lo\n"
    "v_mov_b32 v6, s38\n"
    "ds_write_b32 v10, v6\n"
    "ds_read_b128 v[32:35], v0 offset:52224\n"
    "s_waitcnt lgkmcnt(6)\n"
    "v_mfma_f32_32x32x16_bf16 v[64:79], a[184:187], v[44:47], v[64:79]\n"
    "ds_read_b128 v[36:39], v0 offset:53248\n"
    "s_waitcnt lgkmcnt(6)\n"
    "v_mfma_f32_32x32x16_bf16 v[64:79], a[188:191], v[48:51], v[64:79]\n"
    "ds_read_b128 v[40:43], v0 offset:54272\n"
    "s_waitcnt lgkmcnt(6)\n"
    "v_mfma_f32_32x32x16_bf16 v[64:79], a[192:195], v[20:23], v[64:79]\n"
    "ds_read_b128 v[44:47], v0 offset:55296\n"
    "s_waitcnt lgkmcnt(6)\n"
    "v_mfma_f32_32x32x16_bf16 v[64:79], a[196:199], v[24:27], v[64:79]\n"
    "ds_read_b128 v[48:51], v0 offset:56320\n"
    "s_waitcnt lgkmcnt(6)\n"
    "v_mfma_f32_32x32x16_bf16 v[64:79], a[200:203], v[28:31], v[64:79]\n"
    "s_add_u32 s55, s55, 1\n"
    "s_lshr_b32 s38, s54, 8\n"
    "s_xor_b32 s38, s38, s43\n"
    "s_and_b32 s38, s38, 0x2000\n"
    "s_or_b32 s39, s54, s38\n"
    "s_cmp_lt_u32 s55, 64\n"
    "s_cselect_b32 s39, s39, 0x80000000\n"
    "s_add_u32 s54, s54, 262144\n"
    "s_barrier\n"
    "ds_read_b128 v[20:23], v0 offset:57344\n"
    "ds_read_b32 v6, v9\n"
    "s_waitcnt lgkmcnt(6)\n"
    "v_mfma_f32_32x32x16_bf16 v[64:79], a[204:207], v[32:35], v[64:79]\n"
    "buffer_store_dwordx4 v[80:83], v0, s[16:19], s39 offen offset:0 sc1\n"
    "ds_read_b128 v[24:27], v0 offset:58368\n"
    "s_waitcnt lgkmcnt(6)\n"
    "v_mfma_f32_32x32x16_bf16 v[64:79], a[208:211], v[36:39], v[64:79]\n"
    "ds_read_b128 v[28:31], v0 offset:59392\n"
    "s_waitcnt lgkmcnt(6)\n"
    "v_mfma_f32_32x32x16_bf16 v[64:79], a[212:215], v[40:43], v[64:79]\n"
    "buffer_store_dwordx4 v[80:83], v0, s[16:19], s39 offen offset:1024 sc1\n"
    "ds_read_b128 v[32:35], v0 offset:60416\n"
    "s_waitcnt lgkmcnt(6)\n"
    "v_mfma_f32_32x32x16_bf16 v[64:79], a[216:219], v[44:47], v[64:79]\n"
    "ds_read_b128 v[36:39], v0 offset:61440\n"
    "s_waitcnt lgkmcnt(6)\n"
    "v_mfma_f32_32x32x16_bf16 v[64:79], a[220:223], v[48:51], v[64:79]\n"
    "ds_read_b128 v[40:43], v0 offset:62464\n"
    "s_waitcnt lgkmcnt(5)\n"
    "v_readfirstlane_b32 s52, v6\n"
    "s_cmp_eq_u32 s52, 0\n"
    "s_cbranch_scc1 LH%=\n"
    "v_mfma_f32_32x32x16_bf16 v[64:79], a[224:227], v[20:23], v[64:79]\n"
    "ds_read_b128 v[44:47], v0 offset:63488\n"
    "s_waitcnt lgkmcnt(5)\n"
    "v_mfma_f32_32x32x16_bf16 v[64:79], a[228:231], v[24:27], v[64:79]\n"
    "ds_read_b128 v[48:51], v0 offset:64512\n"
    "s_waitcnt lgkmcnt(5)\n"
    "v_mfma_f32_32x32x16_bf16 v[64:79], a[232:235], v[28:31], v[64:79]\n"
    "s_waitcnt lgkmcnt(4)\n"
    "v_mfma_f32_32x32x16_bf16 v[64:79], a[236:239], v[32:35], v[64:79]\n"
    "s_waitcnt lgkmcnt(3)\n"
    "v_mfma_f32_32x32x16_bf16 v[64:79], a[240:243], v[36:39], v[64:79]\n"
    "s_waitcnt lgkmcnt(2)\n"
    "v_mfma_f32_32x32x16_bf16 v[64:79], a[244:247], v[40:43], v[64:79]\n"
    "s_waitcnt lgkmcnt(1)\n"
    "v_mfma_f32_32x32x16_bf16 v[64:79], a[248:251], v[44:47], v[64:79]\n"
    "s_waitcnt lgkmcnt(0)\n"
    "v_mfma_f32_32x32x16_bf16 v[64:79], a[252:255], v[48:51], v[64:79]\n"
    "s_branch LI%=\n"
    "LH%=:\n"
    "v_mfma_f32_32x32x16_bf16 v[64:79], a[224:227], v[20:23], v[64:79]\n"
    "ds_read_b128 v[44:47], v0 offset:63488\n"
    "s_add_u32 m0, s49, 0\n"
    "s_nop 0\n"
    "buffer_load_dwordx4 v0, s[16:19], s48 offen offset:0 sc1 lds\n"
    "s_waitcnt lgkmcnt(5)\n"
    "v_mfma_f32_32x32x16_bf16 v[64:79], a[228:231], v[24:27], v[64:79]\n"
    "ds_read_b128 v[48:51], v0 offset:64512\n"
    "buffer_load_dwordx4 v0, s[16:19], s48 offen offset:1024 sc1 lds\n"
    "s_add_u32 s48, s48, 16384\n"
    "s_waitcnt lgkmcnt(5)\n"
    "v_mfma_f32_32x32x16_bf16 v[64:79], a[232:235], v[28:31], v[64:79]\n"
    "s_add_u32 m0, s49, 8192\n"
    "s_nop 0\n"
    "buffer_load_dwordx4 v0, s[16:19], s48 offen offset:0 sc1 lds\n"
    "s_waitcnt lgkmcnt(4)\n"
    "v_mfma_f32_32x32x16_bf16 v[64:79], a[236:239], v[32:35], v[64:79]\n"
    "buffer_load_dwordx4 v0, s[16:19], s48 offen offset:1024 sc1 lds\n"
    "s_add_u32 s48, s48, 16384\n"
    "s_waitcnt lgkmcnt(3)\n"
    "v_mfma_f32_32x32x16_bf16 v[64:79], a[240:243], v[36:39], v[64:79]\n"
    "s_add_u32 m0, s49, 16384\n"
    "s_nop 0\n"
    "buffer_load_dwordx4 v0, s[16:19], s48 offen offset:0 sc1 lds\n"
    "s_waitcnt lgkmcnt(2)\n"
    "v_mfma_f32_32x32x16_bf16 v[64:79], a[244:247], v[40:43], v[64:79]\n"
    "buffer_load_dwordx4 v0, s[16:19], s48 offen offset:1024 sc1 lds\n"
    "s_add_u32 s48, s48, 16384\n"
    "s_waitcnt lgkmcnt(1)\n"
    "v_mfma_f32_32x32x16_bf16 v[64:79], a[248:251], v[44:47], v[64:79]\n"
    "s_add_u32 m0, s49, 24576\n"
    "s_nop 0\n"
    "buffer_load_dwordx4 v0, s[16:19], s48 offen offset:0 sc1 lds\n"
    "s_waitcnt lgkmcnt(0)\n"
    "v_mfma_f32_32x32x16_bf16 v[64:79], a[252:255], v[48:51], v[64:79]\n"
    "buffer_load_dwordx4 v0, s[16:19], s48 offen offset:1024 sc1 lds\n"
    "s_add_u32 s48, s48, 16384\n"
    "LI%=:\n"
    "s_nop 7\n"
    "s_nop 3\n"
    "v_cvt_pk_bf16_f32 v12, v64, v65\n"
    "v_cvt_pk_bf16_f32 v13, v66, v67\n"
    "v_cvt_pk_bf16_f32 v14, v68, v69\n"
    "v_cvt_pk_bf16_f32 v15, v70, v71\n"
    "v_min_u32 v15, -2, v15\n"
    "ds_write_b128 v1, v[12:15] offset:0\n"
    "v_cvt_pk_bf16_f32 v16, v72, v73\n"
    "v_cvt_pk_bf16_f32 v17, v74, v75\n"
    "v_cvt_pk_bf16_f32 v18, v76, v77\n"
    "v_cvt_pk_bf16_f32 v19, v78, v79\n"
    "v_min_u32 v19, -2, v19\n"
    "ds_write_b128 v1, v[16:19] offset:32\n"
    "v_mov_b32 v6, 1\n"
    "s_mov_b64 exec, 0xff\n"
    "v_mov_b32 v6, 0\n"
    "buffer_atomic_or v6, v7, s[16:19], s44 offen sc0 sc1\n"
    "s_mov_b64 exec, -1\n"
    "s_waitcnt lgkmcnt(0)\n"
    "s_barrier\n"
    "ds_read_b128 v[12:15], v2 offset:0\n"
    "ds_read_b128 v[16:19], v2 offset:1088\n"
    "s_waitcnt lgkmcnt(1)\n"
    "buffer_store_dwordx4 v[12:15], v0, s[16:19], s50 offen offset:0 sc1\n"
    "s_waitcnt lgkmcnt(0)\n"
    "buffer_store_dwordx4 v[16:19], v0, s[16:19], s50 offen offset:1024 sc1\n"
    "buffer_store_dword v5, off, s[16:19], s45 sc1\n"
    "s_add_u32 s45, s45, 4\n"
    "s_waitcnt vmcnt(3)\n"
    "s_cmp_eq_u32 s52, 0\n"
    "s_cbranch_scc1 LG%=\n"
    "s_cmp_eq_u32 s40, 64\n"
    "s_cbranch_scc1 LE%=\n"
    "s_cmp_lg_u32 s36, 0\n"
    "s_cbranch_scc1 LB%=\n"
    "v_cmp_eq_u32 vcc, 0, v6\n"
    "s_cbranch_vccz LB%=\n"
    "s_branch LT%=\n"
    "LE%=:\n"
    "s_endpgm\n"
    :
    : "{v0}"(hv0), "{v1}"(hv1), "{v2}"(hv2), "{v3}"(hv3), "{v4}"(hv4), "{v5}"(hv5)
    : "v255", "a255", "s101", "vcc", "scc", "memory");
  __builtin_unreachable();
}

typedef float f2 __attribute__((ext_vector_type(2)));
__device__ __forceinline__ unsigned cvt2(f2 v) { unsigned p; asm("v_cvt_pk_bf16_f32 %0, %1, %2" : "=v"(p) : "v"(v[0]), "v"(v[1])); return p; }
__device__ __forceinline__ f2 up2(unsigned p) { return (f2){__uint_as_float(p << 16), __uint_as_float(p & 0xffff0000u)}; }
__device__ __forceinline__ void row_sum2(float& x, float& y) {
  x = ror_add<8>(x); y = ror_add<8>(y); x = ror_add<4>(x); y = ror_add<4>(y); x = ror_add<2>(x); y = ror_add<2>(y); x = ror_add<1>(x); y = ror_add<1>(y);
  auto q = __builtin_amdgcn_permlane16_swap(__float_as_uint(x), __float_as_uint(y), false, false);
  float t = __uint_as_float(q[0]) + __uint_as_float(q[1]);
  auto s = __builtin_amdgcn_permlane32_swap(__float_as_uint(t), __float_as_uint(t), false, false);
  t = __uint_as_float(s[0]) + __uint_as_float(s[1]);
  x = __int_as_float(__builtin_amdgcn_readlane(__float_as_int(t), 0)); y = __int_as_float(__builtin_amdgcn_readlane(__float_as_int(t), 16));
}
__device__ __forceinline__ void ln8(const f2 (&x)[4], f2 (&y)[4]) {
  const float c0 = __int_as_float(__builtin_amdgcn_readfirstlane(__float_as_int(x[0][0])));
  const f2 d0 = x[0] - c0, d1 = x[1] - c0, d2 = x[2] - c0, d3 = x[3] - c0;
  const f2 s2 = (d0 + d1) + (d2 + d3), v2 = d3 * d3 + (d2 * d2 + (d1 * d1 + d0 * d0));
  float s = s2[0] + s2[1], vs = v2[0] + v2[1];
  row_sum2(s, vs);
  s *= 1.f / DM;
  const f2 mv = up2(cvt2((f2){c0 + s, fmaxf(__builtin_fmaf(-s, s, vs * (1.f / DM)), 0.f)}));
  const float rc = __builtin_amdgcn_rcpf(rbf(__builtin_amdgcn_sqrtf(rbf(mv[1] + 1e-5f))));
#pragma unroll
  for (int i = 0; i < 4; ++i) y[i] = (x[i] - mv[0]) * rc;
}
__device__ __forceinline__ v4u ln_row(v4u q) {
  f2 x[4], y[4];
#pragma unroll
  for (int i = 0; i < 4; ++i) x[i] = up2(q[i]);
  ln8(x, y);
  return (v4u){cvt2(y[0]), cvt2(y[1]), cvt2(y[2]), cvt2(y[3])};
}
__device__ __forceinline__ void dma16(rsrc_t r, char* lp, unsigned vo, unsigned so) { __builtin_amdgcn_raw_ptr_buffer_load_lds(r, (__attribute__((address_space(3))) void*)lp, 16, vo, so, 0, 0); }
#define PTR(p) (int)(size_t)(p), (int)((size_t)(p) >> 32) & 0xffff
template <bool L1>
__device__ __forceinline__ void l_role8(int w, int u, const unsigned* rdy, char* ws, const u16* x, const u16* w_r, const u16* wg, const u16* wu, const u16* wd, u16* out) {
  const int lane = lane_id(), c = lane & 15, g = lane >> 4, q = (lane >> 2) & 3, r = u * 4 + w, t0 = L1 ? (w < 2 ? 3 * w : 2 * w + 2) : w;
  const unsigned c1 = 0u, c2 = 0x2000u;
  const unsigned zd1 = nearof(Z_DEC, c1), zd2 = nearof(Z_DEC, c2), cl = L1 && SW ? c1 : c2, fp = nearof(Z_FP + (L1 ? 0 : 16384 + (g & 1) * 16384), cl), zdl = SW ? zd1 + 1024 : zd2 + 512;
  const int pv[22] = {w, r, (int)nearof(Z_R1, cl), (int)Z_ERR, PTR(ws), (int)ZWS_BYTES, PTR(x), PTR(out), PTR(wg), PTR(wu), PTR(wd), PTR(rdy), PTR(w_r), (int)(Z_DBG + MBY(3)) + ((L1 ? 0 : 32) + r) * 16};
  const int one = (c >> 2) != g ? 0 : ((c & 1) ? 0x3f800000 : 0x3f80), trow = 16 * (g >> 1) + 8 * (q >> 1) + 4 * (g & 1) + (q & 1);
  int hv0 = lane * 16, hv1 = g * 16384 + r * 256 + c * 16, hv2 = lane < 32 ? fp + 4096 + (L1 ? g * 4 : 0) : (L1 ? zdl : zd2), hv3 = g < (L1 ? 2 : 1) ? 4 : 0, hv4 = g == 1 ? 4 : 0, hv5 = g >= 2 ? 4 : 0;
  int hv6 = (c & 2) ? 0xEEEE : 0x4444, hv7 = w * 4096 + g * 32 + (c & 2) * 8, hv8 = w * 4096 + lane * 16;
  int hv9 = !(c & 1) && g < (L1 ? (w < 2 ? 3 : 2) : 1) ? (L1 ? u * 320 : 2560 + u * 128) + (t0 + g) * 32 + c * 2 : OOR, hv10 = (lane & 0x31) == 0 ? r * 32 + c * 2 : OOR;
  int hv11 = lane < 6 ? (lane < 3 ? zd1 + lane * 512 : (lane == 3 ? zd2 : zdl + (lane - 4) * 512)) : OOR, hv12 = (c & 2) ? 0 : one, hv13 = (c & 2) ? one : 0;
  int hv14 = trow * (L1 ? 320 : 128) + t0 * 32 + (lane & 3) * 8, hv15 = 0, hv21 = fp + c * 256;
#define VO(i) ((w * 16 + (i * 64 + lane) / 20) * (FF * 2) + u * 320 + ((i * 64 + lane) % 20) * 16)
  int hv16 = L1 ? VO(0) : (w * 16 + (lane >> 3)) * (FF * 2) + 2560 + u * 128 + (lane & 7) * 16, hv17 = L1 ? VO(1) : (w * 16 + (lane >> 3)) * (DM * 2) + u * 128 + (lane & 7) * 16;
  int hv18 = L1 ? VO(2) : r * 32 + c * 2, hv19 = VO(3), hv20 = VO(4);
#pragma unroll
  for (int i = 0; i < 22; ++i) hv15 = lane == i ? pv[i] : hv15;
  if constexpr (L1) {
  asm volatile(
    "s_nop 1\n"
    "v_readlane_b32 s36, v15, 0\n"
    "v_readlane_b32 s55, v15, 1\n"
    "v_readlane_b32 s58, v15, 2\n"
    "v_readlane_b32 s59, v15, 3\n"
    "v_readlane_b32 s16, v15, 4\n"
    "v_readlane_b32 s17, v15, 5\n"
    "v_readlane_b32 s18, v15, 6\n"
    "v_readlane_b32 s20, v15, 7\n"
    "v_readlane_b32 s21, v15, 8\n"
    "v_readlane_b32 s24, v15, 9\n"
    "v_readlane_b32 s25, v15, 10\n"
    "v_readlane_b32 s90, v15, 11\n"
    "v_readlane_b32 s91, v15, 12\n"
    "v_readlane_b32 s92, v15, 13\n"
    "v_readlane_b32 s93, v15, 14\n"
    "v_readlane_b32 s94, v15, 15\n"
    "v_readlane_b32 s95, v15, 16\n"
    "v_readlane_b32 s96, v15, 17\n"
    "v_readlane_b32 s97, v15, 18\n"
    "v_readlane_b32 s98, v15, 19\n"
    "v_readlane_b32 s99, v15, 20\n"
    "v_readlane_b32 s72, v15, 21\n"
    "s_mov_b32 s19, 0x20000\n"
    "s_mov_b32 s23, 0x20000\n"
    "s_mov_b32 s27, 0x20000\n"
    "s_mov_b32 s31, 0x20000\n"
    "s_mov_b32 s22, 0x200000\n"
    "s_mov_b32 s26, 0x200000\n"
    "s_mov_b32 s30, 1835008\n"
    "s_nop 3\n"
    "global_load_dwordx4 v[116:119], v0, s[98:99]\n"
    "s_mul_i32 s62, s36, 5120\n"
    "s_mov_b32 s28, s90\n"
    "s_mov_b32 s29, s91\n"
    "s_add_u32 m0, s62, 0\n"
    "s_mov_b32 s38, 0\n"
    "buffer_load_dwordx4 v16, s[28:31], s38 offen lds\n"
    "s_add_u32 m0, s62, 1024\n"
    "s_mov_b32 s38, 0\n"
    "buffer_load_dwordx4 v17, s[28:31], s38 offen lds\n"
    "s_add_u32 m0, s62, 2048\n"
    "s_mov_b32 s38, 0\n"
    "buffer_load_dwordx4 v18, s[28:31], s38 offen lds\n"
    "s_add_u32 m0, s62, 3072\n"
    "s_mov_b32 s38, 0\n"
    "buffer_load_dwordx4 v19, s[28:31], s38 offen lds\n"
    "s_add_u32 m0, s62, 4096\n"
    "s_mov_b32 s38, 0\n"
    "buffer_load_dwordx4 v20, s[28:31], s38 offen lds\n"
    "s_mov_b32 s28, s92\n"
    "s_mov_b32 s29, s93\n"
    "s_add_u32 m0, s62, 20480\n"
    "s_mov_b32 s38, 0\n"
    "buffer_load_dwordx4 v16, s[28:31], s38 offen lds\n"
    "s_add_u32 m0, s62, 21504\n"
    "s_mov_b32 s38, 0\n"
    "buffer_load_dwordx4 v17, s[28:31], s38 offen lds\n"
    "s_add_u32 m0, s62, 22528\n"
    "s_mov_b32 s38, 0\n"
    "buffer_load_dwordx4 v18, s[28:31], s38 offen lds\n"
    "s_add_u32 m0, s62, 23552\n"
    "s_mov_b32 s38, 0\n"
    "buffer_load_dwordx4 v19, s[28:31], s38 offen lds\n"
    "s_add_u32 m0, s62, 24576\n"
    "s_mov_b32 s38, 0\n"
    "buffer_load_dwordx4 v20, s[28:31], s38 offen lds\n"
    "s_mov_b32 s28, s90\n"
    "s_mov_b32 s29, s91\n"
    "s_add_u32 m0, s62, 40960\n"
    "s_mov_b32 s38, 229376\n"
    "buffer_load_dwordx4 v16, s[28:31], s38 offen lds\n"
    "s_add_u32 m0, s62, 41984\n"
    "s_mov_b32 s38, 229376\n"
    "buffer_load_dwordx4 v17, s[28:31], s38 offen lds\n"
    "s_add_u32 m0, s62, 43008\n"
    "s_mov_b32 s38, 229376\n"
    "buffer_load_dwordx4 v18, s[28:31], s38 offen lds\n"
    "s_add_u32 m0, s62, 44032\n"
    "s_mov_b32 s38, 229376\n"
    "buffer_load_dwordx4 v19, s[28:31], s38 offen lds\n"
    "s_add_u32 m0, s62, 45056\n"
    "s_mov_b32 s38, 229376\n"
    "buffer_load_dwordx4 v20, s[28:31], s38 offen lds\n"
    "s_mov_b32 s28, s92\n"
    "s_mov_b32 s29, s93\n"
    "s_add_u32 m0, s62, 61440\n"
    "s_mov_b32 s38, 229376\n"
    "buffer_load_dwordx4 v16, s[28:31], s38 offen lds\n"
    "s_add_u32 m0, s62, 62464\n"
    "s_mov_b32 s38, 229376\n"
    "buffer_load_dwordx4 v17, s[28:31], s38 offen lds\n"
    "s_add_u32 m0, s62, 63488\n"
    "s_mov_b32 s38, 229376\n"
    "buffer_load_dwordx4 v18, s[28:31], s38 offen lds\n"
    "s_add_u32 m0, s62, 64512\n"
    "s_mov_b32 s38, 229376\n"
    "buffer_load_dwordx4 v19, s[28:31], s38 offen lds\n"
    "s_add_u32 m0, s62, 65536\n"
    "s_mov_b32 s38, 229376\n"
    "buffer_load_dwordx4 v20, s[28:31], s38 offen lds\n"
    "s_mov_b32 s28, s90\n"
    "s_mov_b32 s29, s91\n"
    "s_add_u32 m0, s62, 81920\n"
    "s_mov_b32 s38, 458752\n"
    "buffer_load_dwordx4 v16, s[28:31], s38 offen lds\n"
    "s_add_u32 m0, s62, 82944\n"
    "s_mov_b32 s38, 458752\n"
    "buffer_load_dwordx4 v17, s[28:31], s38 offen lds\n"
    "s_add_u32 m0, s62, 83968\n"
    "s_mov_b32 s38, 458752\n"
    "buffer_load_dwordx4 v18, s[28:31], s38 offen lds\n"
    "s_add_u32 m0, s62, 84992\n"
    "s_mov_b32 s38, 458752\n"
    "buffer_load_dwordx4 v19, s[28:31], s38 offen lds\n"
    "s_add_u32 m0, s62, 86016\n"
    "s_mov_b32 s38, 458752\n"
    "buffer_load_dwordx4 v20, s[28:31], s38 offen lds\n"
    "s_mov_b32 s28, s92\n"
    "s_mov_b32 s29, s93\n"
    "s_add_u32 m0, s62, 102400\n"
    "s_mov_b32 s38, 458752\n"
    "buffer_load_dwordx4 v16, s[28:31], s38 offen lds\n"
    "s_add_u32 m0, s62, 103424\n"
    "s_mov_b32 s38, 458752\n"
    "buffer_load_dwordx4 v17, s[28:31], s38 offen lds\n"
    "s_add_u32 m0, s62, 104448\n"
    "s_mov_b32 s38, 458752\n"
    "buffer_load_dwordx4 v18, s[28:31], s38 offen lds\n"
    "s_add_u32 m0, s62, 105472\n"
    "s_mov_b32 s38, 458752\n"
    "buffer_load_dwordx4 v19, s[28:31], s38 offen lds\n"
    "s_add_u32 m0, s62, 106496\n"
    "s_mov_b32 s38, 458752\n"
    "buffer_load_dwordx4 v20, s[28:31], s38 offen lds\n"
    "s_waitcnt vmcnt(25)\n"
    "s_barrier\n"
    "s_mov_b32 s28, s90\n"
    "s_mov_b32 s29, s91\n"
    "s_add_u32 m0, s62, 122880\n"
    "s_mov_b32 s38, 688128\n"
    "buffer_load_dwordx4 v16, s[28:31], s38 offen lds\n"
    "s_add_u32 m0, s62, 123904\n"
    "s_mov_b32 s38, 688128\n"
    "buffer_load_dwordx4 v17, s[28:31], s38 offen lds\n"
    "s_add_u32 m0, s62, 124928\n"
    "s_mov_b32 s38, 688128\n"
    "buffer_load_dwordx4 v18, s[28:31], s38 offen lds\n"
    "s_add_u32 m0, s62, 125952\n"
    "s_mov_b32 s38, 688128\n"
    "buffer_load_dwordx4 v19, s[28:31], s38 offen lds\n"
    "s_add_u32 m0, s62, 126976\n"
    "s_mov_b32 s38, 688128\n"
    "buffer_load_dwordx4 v20, s[28:31], s38 offen lds\n"
    "v_add_u32 v48, 0, v14\n"
    "ds_read_b64_tr_b16 a[0:1], v48 offset:0\n"
    "ds_read_b64_tr_b16 a[2:3], v48 offset:640\n"
    "ds_read_b64_tr_b16 a[4:5], v48 offset:10240\n"
    "ds_read_b64_tr_b16 a[6:7], v48 offset:10880\n"
    "ds_read_b64_tr_b16 a[16:17], v48 offset:32\n"
    "ds_read_b64_tr_b16 a[18:19], v48 offset:672\n"
    "ds_read_b64_tr_b16 a[20:21], v48 offset:10272\n"
    "ds_read_b64_tr_b16 a[22:23], v48 offset:10912\n"
    "ds_read_b64_tr_b16 v[128:129], v48 offset:64\n"
    "ds_read_b64_tr_b16 v[130:131], v48 offset:704\n"
    "ds_read_b64_tr_b16 v[132:133], v48 offset:10304\n"
    "ds_read_b64_tr_b16 v[134:135], v48 offset:10944\n"
    "s_waitcnt lgkmcnt(0)\n"
    "s_waitcnt vmcnt(25)\n"
    "s_barrier\n"
    "s_mov_b32 s28, s92\n"
    "s_mov_b32 s29, s93\n"
    "s_add_u32 m0, s62, 0\n"
    "s_mov_b32 s38, 688128\n"
    "buffer_load_dwordx4 v16, s[28:31], s38 offen lds\n"
    "s_add_u32 m0, s62, 1024\n"
    "s_mov_b32 s38, 688128\n"
    "buffer_load_dwordx4 v17, s[28:31], s38 offen lds\n"
    "s_add_u32 m0, s62, 2048\n"
    "s_mov_b32 s38, 688128\n"
    "buffer_load_dwordx4 v18, s[28:31], s38 offen lds\n"
    "s_add_u32 m0, s62, 3072\n"
    "s_mov_b32 s38, 688128\n"
    "buffer_load_dwordx4 v19, s[28:31], s38 offen lds\n"
    "s_add_u32 m0, s62, 4096\n"
    "s_mov_b32 s38, 688128\n"
    "buffer_load_dwordx4 v20, s[28:31], s38 offen lds\n"
    "v_add_u32 v48, 20480, v14\n"
    "ds_read_b64_tr_b16 a[8:9], v48 offset:0\n"
    "ds_read_b64_tr_b16 a[10:11], v48 offset:640\n"
    "ds_read_b64_tr_b16 a[12:13], v48 offset:10240\n"
    "ds_read_b64_tr_b16 a[14:15], v48 offset:10880\n"
    "ds_read_b64_tr_b16 a[24:25], v48 offset:32\n"
    "ds_read_b64_tr_b16 a[26:27], v48 offset:672\n"
    "ds_read_b64_tr_b16 a[28:29], v48 offset:10272\n"
    "ds_read_b64_tr_b16 a[30:31], v48 offset:10912\n"
    "ds_read_b64_tr_b16 v[136:137], v48 offset:64\n"
    "ds_read_b64_tr_b16 v[138:139], v48 offset:704\n"
    "ds_read_b64_tr_b16 v[140:141], v48 offset:10304\n"
    "ds_read_b64_tr_b16 v[142:143], v48 offset:10944\n"
    "s_waitcnt lgkmcnt(0)\n"
    "s_waitcnt vmcnt(25)\n"
    "s_barrier\n"
    "s_mov_b32 s28, s90\n"
    "s_mov_b32 s29, s91\n"
    "s_add_u32 m0, s62, 20480\n"
    "s_mov_b32 s38, 917504\n"
    "buffer_load_dwordx4 v16, s[28:31], s38 offen lds\n"
    "s_add_u32 m0, s62, 21504\n"
    "s_mov_b32 s38, 917504\n"
    "buffer_load_dwordx4 v17, s[28:31], s38 offen lds\n"
    "s_add_u32 m0, s62, 22528\n"
    "s_mov_b32 s38, 917504\n"
    "buffer_load_dwordx4 v18, s[28:31], s38 offen lds\n"
    "s_add_u32 m0, s62, 23552\n"
    "s_mov_b32 s38, 917504\n"
    "buffer_load_dwordx4 v19, s[28:31], s38 offen lds\n"
    "s_add_u32 m0, s62, 24576\n"
    "s_mov_b32 s38, 917504\n"
    "buffer_load_dwordx4 v20, s[28:31], s38 offen lds\n"
    "v_add_u32 v48, 40960, v14\n"
    "ds_read_b64_tr_b16 a[32:33], v48 offset:0\n"
    "ds_read_b64_tr_b16 a[34:35], v48 offset:640\n"
    "ds_read_b64_tr_b16 a[36:37], v48 offset:10240\n"
    "ds_read_b64_tr_b16 a[38:39], v48 offset:10880\n"
    "ds_read_b64_tr_b16 a[48:49], v48 offset:32\n"
    "ds_read_b64_tr_b16 a[50:51], v48 offset:672\n"
    "ds_read_b64_tr_b16 a[52:53], v48 offset:10272\n"
    "ds_read_b64_tr_b16 a[54:55], v48 offset:10912\n"
    "ds_read_b64_tr_b16 v[144:145], v48 offset:64\n"
    "ds_read_b64_tr_b16 v[146:147], v48 offset:704\n"
    "ds_read_b64_tr_b16 v[148:149], v48 offset:10304\n"
    "ds_read_b64_tr_b16 v[150:151], v48 offset:10944\n"
    "s_waitcnt lgkmcnt(0)\n"
    "s_waitcnt vmcnt(25)\n"
    "s_barrier\n"
    "s_mov_b32 s28, s92\n"
    "s_mov_b32 s29, s93\n"
    "s_add_u32 m0, s62, 40960\n"
    "s_mov_b32 s38, 917504\n"
    "buffer_load_dwordx4 v16, s[28:31], s38 offen lds\n"
    "s_add_u32 m0, s62, 41984\n"
    "s_mov_b32 s38, 917504\n"
    "buffer_load_dwordx4 v17, s[28:31], s38 offen lds\n"
    "s_add_u32 m0, s62, 43008\n"
    "s_mov_b32 s38, 917504\n"
    "buffer_load_dwordx4 v18, s[28:31], s38 offen lds\n"
    "s_add_u32 m0, s62, 44032\n"
    "s_mov_b32 s38, 917504\n"
    "buffer_load_dwordx4 v19, s[28:31], s38 offen lds\n"
    "s_add_u32 m0, s62, 45056\n"
    "s_mov_b32 s38, 917504\n"
    "buffer_load_dwordx4 v20, s[28:31], s38 offen lds\n"
    "v_add_u32 v48, 61440, v14\n"
    "ds_read_b64_tr_b16 a[40:41], v48 offset:0\n"
    "ds_read_b64_tr_b16 a[42:43], v48 offset:640\n"
    "ds_read_b64_tr_b16 a[44:45], v48 offset:10240\n"
    "ds_read_b64_tr_b16 a[46:47], v48 offset:10880\n"
    "ds_read_b64_tr_b16 a[56:57], v48 offset:32\n"
    "ds_read_b64_tr_b16 a[58:59], v48 offset:672\n"
    "ds_read_b64_tr_b16 a[60:61], v48 offset:10272\n"
    "ds_read_b64_tr_b16 a[62:63], v48 offset:10912\n"
    "ds_read_b64_tr_b16 v[152:153], v48 offset:64\n"
    "ds_read_b64_tr_b16 v[154:155], v48 offset:704\n"
    "ds_read_b64_tr_b16 v[156:157], v48 offset:10304\n"
    "ds_read_b64_tr_b16 v[158:159], v48 offset:10944\n"
    "s_waitcnt lgkmcnt(0)\n"
    "s_waitcnt vmcnt(25)\n"
    "s_barrier\n"
    "s_mov_b32 s28, s90\n"
    "s_mov_b32 s29, s91\n"
    "s_add_u32 m0, s62, 61440\n"
    "s_mov_b32 s38, 1146880\n"
    "buffer_load_dwordx4 v16, s[28:31], s38 offen lds\n"
    "s_add_u32 m0, s62, 62464\n"
    "s_mov_b32 s38, 1146880\n"
    "buffer_load_dwordx4 v17, s[28:31], s38 offen lds\n"
    "s_add_u32 m0, s62, 63488\n"
    "s_mov_b32 s38, 1146880\n"
    "buffer_load_dwordx4 v18, s[28:31], s38 offen lds\n"
    "s_add_u32 m0, s62, 64512\n"
    "s_mov_b32 s38, 1146880\n"
    "buffer_load_dwordx4 v19, s[28:31], s38 offen lds\n"
    "s_add_u32 m0, s62, 65536\n"
    "s_mov_b32 s38, 1146880\n"
    "buffer_load_dwordx4 v20, s[28:31], s38 offen lds\n"
    "v_add_u32 v48, 81920, v14\n"
    "ds_read_b64_tr_b16 a[64:65], v48 offset:0\n"
    "ds_read_b64_tr_b16 a[66:67], v48 offset:640\n"
    "ds_read_b64_tr_b16 a[68:69], v48 offset:10240\n"
    "ds_read_b64_tr_b16 a[70:71], v48 offset:10880\n"
    "ds_read_b64_tr_b16 a[80:81], v48 offset:32\n"
    "ds_read_b64_tr_b16 a[82:83], v48 offset:672\n"
    "ds_read_b64_tr_b16 a[84:85], v48 offset:10272\n"
    "ds_read_b64_tr_b16 a[86:87], v48 offset:10912\n"
    "ds_read_b64_tr_b16 v[160:161], v48 offset:64\n"
    "ds_read_b64_tr_b16 v[162:163], v48 offset:704\n"
    "ds_read_b64_tr_b16 v[164:165], v48 offset:10304\n"
    "ds_read_b64_tr_b16 v[166:167], v48 offset:10944\n"
    "s_waitcnt lgkmcnt(0)\n"
    "s_waitcnt vmcnt(25)\n"
    "s_barrier\n"
    "s_mov_b32 s28, s92\n"
    "s_mov_b32 s29, s93\n"
    "s_add_u32 m0, s62, 81920\n"
    "s_mov_b32 s38, 1146880\n"
    "buffer_load_dwordx4 v16, s[28:31], s38 offen lds\n"
    "s_add_u32 m0, s62, 82944\n"
    "s_mov_b32 s38, 1146880\n"
    "buffer_load_dwordx4 v17, s[28:31], s38 offen lds\n"
    "s_add_u32 m0, s62, 83968\n"
    "s_mov_b32 s38, 1146880\n"
    "buffer_load_dwordx4 v18, s[28:31], s38 offen lds\n"
    "s_add_u32 m0, s62, 84992\n"
    "s_mov_b32 s38, 1146880\n"
    "buffer_load_dwordx4 v19, s[28:31], s38 offen lds\n"
    "s_add_u32 m0, s62, 86016\n"
    "s_mov_b32 s38, 1146880\n"
    "buffer_load_dwordx4 v20, s[28:31], s38 offen lds\n"
    "v_add_u32 v48, 102400, v14\n"
    "ds_read_b64_tr_b16 a[72:73], v48 offset:0\n"
    "ds_read_b64_tr_b16 a[74:75], v48 offset:640\n"
    "ds_read_b64_tr_b16 a[76:77], v48 offset:10240\n"
    "ds_read_b64_tr_b16 a[78:79], v48 offset:10880\n"
    "ds_read_b64_tr_b16 a[88:89], v48 offset:32\n"
    "ds_read_b64_tr_b16 a[90:91], v48 offset:672\n"
    "ds_read_b64_tr_b16 a[92:93], v48 offset:10272\n"
    "ds_read_b64_tr_b16 a[94:95], v48 offset:10912\n"
    "ds_read_b64_tr_b16 v[168:169], v48 offset:64\n"
    "ds_read_b64_tr_b16 v[170:171], v48 offset:704\n"
    "ds_read_b64_tr_b16 v[172:173], v48 offset:10304\n"
    "ds_read_b64_tr_b16 v[174:175], v48 offset:10944\n"
    "s_waitcnt lgkmcnt(0)\n"
    "s_waitcnt vmcnt(25)\n"
    "s_barrier\n"
    "s_mov_b32 s28, s90\n"
    "s_mov_b32 s29, s91\n"
    "s_add_u32 m0, s62, 102400\n"
    "s_mov_b32 s38, 1376256\n"
    "buffer_load_dwordx4 v16, s[28:31], s38 offen lds\n"
    "s_add_u32 m0, s62, 103424\n"
    "s_mov_b32 s38, 1376256\n"
    "buffer_load_dwordx4 v17, s[28:31], s38 offen lds\n"
    "s_add_u32 m0, s62, 104448\n"
    "s_mov_b32 s38, 1376256\n"
    "buffer_load_dwordx4 v18, s[28:31], s38 offen lds\n"
    "s_add_u32 m0, s62, 105472\n"
    "s_mov_b32 s38, 1376256\n"
    "buffer_load_dwordx4 v19, s[28:31], s38 offen lds\n"
    "s_add_u32 m0, s62, 106496\n"
    "s_mov_b32 s38, 1376256\n"
    "buffer_load_dwordx4 v20, s[28:31], s38 offen lds\n"
    "v_add_u32 v48, 122880, v14\n"
    "ds_read_b64_tr_b16 a[96:97], v48 offset:0\n"
    "ds_read_b64_tr_b16 a[98:99], v48 offset:640\n"
    "ds_read_b64_tr_b16 a[100:101], v48 offset:10240\n"
    "ds_read_b64_tr_b16 a[102:103], v48 offset:10880\n"
    "ds_read_b64_tr_b16 a[112:113], v48 offset:32\n"
    "ds_read_b64_tr_b16 a[114:115], v48 offset:672\n"
    "ds_read_b64_tr_b16 a[116:117], v48 offset:10272\n"
    "ds_read_b64_tr_b16 a[118:119], v48 offset:10912\n"
    "ds_read_b64_tr_b16 v[176:177], v48 offset:64\n"
    "ds_read_b64_tr_b16 v[178:179], v48 offset:704\n"
    "ds_read_b64_tr_b16 v[180:181], v48 offset:10304\n"
    "ds_read_b64_tr_b16 v[182:183], v48 offset:10944\n"
    "s_waitcnt lgkmcnt(0)\n"
    "s_waitcnt vmcnt(25)\n"
    "s_barrier\n"
    "s_mov_b32 s28, s92\n"
    "s_mov_b32 s29, s93\n"
    "s_add_u32 m0, s62, 122880\n"
    "s_mov_b32 s38, 1376256\n"
    "buffer_load_dwordx4 v16, s[28:31], s38 offen lds\n"
    "s_add_u32 m0, s62, 123904\n"
    "s_mov_b32 s38, 1376256\n"
    "buffer_load_dwordx4 v17, s[28:31], s38 offen lds\n"
    "s_add_u32 m0, s62, 124928\n"
    "s_mov_b32 s38, 1376256\n"
    "buffer_load_dwordx4 v18, s[28:31], s38 offen lds\n"
    "s_add_u32 m0, s62, 125952\n"
    "s_mov_b32 s38, 1376256\n"
    "buffer_load_dwordx4 v19, s[28:31], s38 offen lds\n"
    "s_add_u32 m0, s62, 126976\n"
    "s_mov_b32 s38, 1376256\n"
    "buffer_load_dwordx4 v20, s[28:31], s38 offen lds\n"
    "v_add_u32 v48, 0, v14\n"
    "ds_read_b64_tr_b16 a[104:105], v48 offset:0\n"
    "ds_read_b64_tr_b16 a[106:107], v48 offset:640\n"
    "ds_read_b64_tr_b16 a[108:109], v48 offset:10240\n"
    "ds_read_b64_tr_b16 a[110:111], v48 offset:10880\n"
    "ds_read_b64_tr_b16 a[120:121], v48 offset:32\n"
    "ds_read_b64_tr_b16 a[122:123], v48 offset:672\n"
    "ds_read_b64_tr_b16 a[124:125], v48 offset:10272\n"
    "ds_read_b64_tr_b16 a[126:127], v48 offset:10912\n"
    "ds_read_b64_tr_b16 v[184:185], v48 offset:64\n"
    "ds_read_b64_tr_b16 v[186:187], v48 offset:704\n"
    "ds_read_b64_tr_b16 v[188:189], v48 offset:10304\n"
    "ds_read_b64_tr_b16 v[190:191], v48 offset:10944\n"
    "s_waitcnt lgkmcnt(0)\n"
    "s_waitcnt vmcnt(25)\n"
    "s_barrier\n"
    "s_mov_b32 s28, s90\n"
    "s_mov_b32 s29, s91\n"
    "s_add_u32 m0, s62, 0\n"
    "s_mov_b32 s38, 1605632\n"
    "buffer_load_dwordx4 v16, s[28:31], s38 offen lds\n"
    "s_add_u32 m0, s62, 1024\n"
    "s_mov_b32 s38, 1605632\n"
    "buffer_load_dwordx4 v17, s[28:31], s38 offen lds\n"
    "s_add_u32 m0, s62, 2048\n"
    "s_mov_b32 s38, 1605632\n"
    "buffer_load_dwordx4 v18, s[28:31], s38 offen lds\n"
    "s_add_u32 m0, s62, 3072\n"
    "s_mov_b32 s38, 1605632\n"
    "buffer_load_dwordx4 v19, s[28:31], s38 offen lds\n"
    "s_add_u32 m0, s62, 4096\n"
    "s_mov_b32 s38, 1605632\n"
    "buffer_load_dwordx4 v20, s[28:31], s38 offen lds\n"
    "v_add_u32 v48, 20480, v14\n"
    "ds_read_b64_tr_b16 a[128:129], v48 offset:0\n"
    "ds_read_b64_tr_b16 a[130:131], v48 offset:640\n"
    "ds_read_b64_tr_b16 a[132:133], v48 offset:10240\n"
    "ds_read_b64_tr_b16 a[134:135], v48 offset:10880\n"
    "ds_read_b64_tr_b16 a[144:145], v48 offset:32\n"
    "ds_read_b64_tr_b16 a[146:147], v48 offset:672\n"
    "ds_read_b64_tr_b16 a[148:149], v48 offset:10272\n"
    "ds_read_b64_tr_b16 a[150:151], v48 offset:10912\n"
    "ds_read_b64_tr_b16 v[192:193], v48 offset:64\n"
    "ds_read_b64_tr_b16 v[194:195], v48 offset:704\n"
    "ds_read_b64_tr_b16 v[196:197], v48 offset:10304\n"
    "ds_read_b64_tr_b16 v[198:199], v48 offset:10944\n"
    "s_waitcnt lgkmcnt(0)\n"
    "s_waitcnt vmcnt(25)\n"
    "s_barrier\n"
    "s_mov_b32 s28, s92\n"
    "s_mov_b32 s29, s93\n"
    "s_add_u32 m0, s62, 20480\n"
    "s_mov_b32 s38, 1605632\n"
    "buffer_load_dwordx4 v16, s[28:31], s38 offen lds\n"
    "s_add_u32 m0, s62, 21504\n"
    "s_mov_b32 s38, 1605632\n"
    "buffer_load_dwordx4 v17, s[28:31], s38 offen lds\n"
    "s_add_u32 m0, s62, 22528\n"
    "s_mov_b32 s38, 1605632\n"
    "buffer_load_dwordx4 v18, s[28:31], s38 offen lds\n"
    "s_add_u32 m0, s62, 23552\n"
    "s_mov_b32 s38, 1605632\n"
    "buffer_load_dwordx4 v19, s[28:31], s38 offen lds\n"
    "s_add_u32 m0, s62, 24576\n"
    "s_mov_b32 s38, 1605632\n"
    "buffer_load_dwordx4 v20, s[28:31], s38 offen lds\n"
    "v_add_u32 v48, 40960, v14\n"
    "ds_read_b64_tr_b16 a[136:137], v48 offset:0\n"
    "ds_read_b64_tr_b16 a[138:139], v48 offset:640\n"
    "ds_read_b64_tr_b16 a[140:141], v48 offset:10240\n"
    "ds_read_b64_tr_b16 a[142:143], v48 offset:10880\n"
    "ds_read_b64_tr_b16 a[152:153], v48 offset:32\n"
    "ds_read_b64_tr_b16 a[154:155], v48 offset:672\n"
    "ds_read_b64_tr_b16 a[156:157], v48 offset:10272\n"
    "ds_read_b64_tr_b16 a[158:159], v48 offset:10912\n"
    "ds_read_b64_tr_b16 v[200:201], v48 offset:64\n"
    "ds_read_b64_tr_b16 v[202:203], v48 offset:704\n"
    "ds_read_b64_tr_b16 v[204:205], v48 offset:10304\n"
    "ds_read_b64_tr_b16 v[206:207], v48 offset:10944\n"
    "s_waitcnt lgkmcnt(0)\n"
    "s_waitcnt vmcnt(25)\n"
    "s_barrier\n"
    "v_add_u32 v48, 61440, v14\n"
    "ds_read_b64_tr_b16 a[160:161], v48 offset:0\n"
    "ds_read_b64_tr_b16 a[162:163], v48 offset:640\n"
    "ds_read_b64_tr_b16 a[164:165], v48 offset:10240\n"
    "ds_read_b64_tr_b16 a[166:167], v48 offset:10880\n"
    "ds_read_b64_tr_b16 a[176:177], v48 offset:32\n"
    "ds_read_b64_tr_b16 a[178:179], v48 offset:672\n"
    "ds_read_b64_tr_b16 a[180:181], v48 offset:10272\n"
    "ds_read_b64_tr_b16 a[182:183], v48 offset:10912\n"
    "ds_read_b64_tr_b16 v[208:209], v48 offset:64\n"
    "ds_read_b64_tr_b16 v[210:211], v48 offset:704\n"
    "ds_read_b64_tr_b16 v[212:213], v48 offset:10304\n"
    "ds_read_b64_tr_b16 v[214:215], v48 offset:10944\n"
    "s_waitcnt lgkmcnt(0)\n"
    "s_waitcnt vmcnt(20)\n"
    "s_barrier\n"
    "v_add_u32 v48, 81920, v14\n"
    "ds_read_b64_tr_b16 a[168:169], v48 offset:0\n"
    "ds_read_b64_tr_b16 a[170:171], v48 offset:640\n"
    "ds_read_b64_tr_b16 a[172:173], v48 offset:10240\n"
    "ds_read_b64_tr_b16 a[174:175], v48 offset:10880\n"
    "ds_read_b64_tr_b16 a[184:185], v48 offset:32\n"
    "ds_read_b64_tr_b16 a[186:187], v48 offset:672\n"
    "ds_read_b64_tr_b16 a[188:189], v48 offset:10272\n"
    "ds_read_b64_tr_b16 a[190:191], v48 offset:10912\n"
    "ds_read_b64_tr_b16 v[216:217], v48 offset:64\n"
    "ds_read_b64_tr_b16 v[218:219], v48 offset:704\n"
    "ds_read_b64_tr_b16 v[220:221], v48 offset:10304\n"
    "ds_read_b64_tr_b16 v[222:223], v48 offset:10944\n"
    "s_waitcnt lgkmcnt(0)\n"
    "s_waitcnt vmcnt(15)\n"
    "s_barrier\n"
    "v_add_u32 v48, 102400, v14\n"
    "ds_read_b64_tr_b16 a[192:193], v48 offset:0\n"
    "ds_read_b64_tr_b16 a[194:195], v48 offset:640\n"
    "ds_read_b64_tr_b16 a[196:197], v48 offset:10240\n"
    "ds_read_b64_tr_b16 a[198:199], v48 offset:10880\n"
    "ds_read_b64_tr_b16 a[208:209], v48 offset:32\n"
    "ds_read_b64_tr_b16 a[210:211], v48 offset:672\n"
    "ds_read_b64_tr_b16 a[212:213], v48 offset:10272\n"
    "ds_read_b64_tr_b16 a[214:215], v48 offset:10912\n"
    "ds_read_b64_tr_b16 v[224:225], v48 offset:64\n"
    "ds_read_b64_tr_b16 v[226:227], v48 offset:704\n"
    "ds_read_b64_tr_b16 v[228:229], v48 offset:10304\n"
    "ds_read_b64_tr_b16 v[230:231], v48 offset:10944\n"
    "s_waitcnt lgkmcnt(0)\n"
    "s_waitcnt vmcnt(10)\n"
    "s_barrier\n"
    "v_add_u32 v48, 122880, v14\n"
    "ds_read_b64_tr_b16 a[200:201], v48 offset:0\n"
    "ds_read_b64_tr_b16 a[202:203], v48 offset:640\n"
    "ds_read_b64_tr_b16 a[204:205], v48 offset:10240\n"
    "ds_read_b64_tr_b16 a[206:207], v48 offset:10880\n"
    "ds_read_b64_tr_b16 a[216:217], v48 offset:32\n"
    "ds_read_b64_tr_b16 a[218:219], v48 offset:672\n"
    "ds_read_b64_tr_b16 a[220:221], v48 offset:10272\n"
    "ds_read_b64_tr_b16 a[222:223], v48 offset:10912\n"
    "ds_read_b64_tr_b16 v[232:233], v48 offset:64\n"
    "ds_read_b64_tr_b16 v[234:235], v48 offset:704\n"
    "ds_read_b64_tr_b16 v[236:237], v48 offset:10304\n"
    "ds_read_b64_tr_b16 v[238:239], v48 offset:10944\n"
    "s_waitcnt lgkmcnt(0)\n"
    "s_waitcnt vmcnt(5)\n"
    "s_barrier\n"
    "v_add_u32 v48, 0, v14\n"
    "ds_read_b64_tr_b16 a[224:225], v48 offset:0\n"
    "ds_read_b64_tr_b16 a[226:227], v48 offset:640\n"
    "ds_read_b64_tr_b16 a[228:229], v48 offset:10240\n"
    "ds_read_b64_tr_b16 a[230:231], v48 offset:10880\n"
    "ds_read_b64_tr_b16 a[240:241], v48 offset:32\n"
    "ds_read_b64_tr_b16 a[242:243], v48 offset:672\n"
    "ds_read_b64_tr_b16 a[244:245], v48 offset:10272\n"
    "ds_read_b64_tr_b16 a[246:247], v48 offset:10912\n"
    "ds_read_b64_tr_b16 v[240:241], v48 offset:64\n"
    "ds_read_b64_tr_b16 v[242:243], v48 offset:704\n"
    "ds_read_b64_tr_b16 v[244:245], v48 offset:10304\n"
    "ds_read_b64_tr_b16 v[246:247], v48 offset:10944\n"
    "s_waitcnt lgkmcnt(0)\n"
    "s_waitcnt vmcnt(0)\n"
    "s_barrier\n"
    "v_add_u32 v48, 20480, v14\n"
    "ds_read_b64_tr_b16 a[232:233], v48 offset:0\n"
    "ds_read_b64_tr_b16 a[234:235], v48 offset:640\n"
    "ds_read_b64_tr_b16 a[236:237], v48 offset:10240\n"
    "ds_read_b64_tr_b16 a[238:239], v48 offset:10880\n"
    "ds_read_b64_tr_b16 a[248:249], v48 offset:32\n"
    "ds_read_b64_tr_b16 a[250:251], v48 offset:672\n"
    "ds_read_b64_tr_b16 a[252:253], v48 offset:10272\n"
    "ds_read_b64_tr_b16 a[254:255], v48 offset:10912\n"
    "ds_read_b64_tr_b16 v[248:249], v48 offset:64\n"
    "ds_read_b64_tr_b16 v[250:251], v48 offset:704\n"
    "ds_read_b64_tr_b16 v[252:253], v48 offset:10304\n"
    "ds_read_b64_tr_b16 v[254:255], v48 offset:10944\n"
    "s_waitcnt lgkmcnt(0)\n"
    "s_barrier\n"
    "v_lshlrev_b32 v32, 16, v116\n"
    "v_and_b32 v33, 0xffff0000, v116\n"
    "v_lshlrev_b32 v34, 16, v117\n"
    "v_and_b32 v35, 0xffff0000, v117\n"
    "v_lshlrev_b32 v36, 16, v118\n"
    "v_and_b32 v37, 0xffff0000, v118\n"
    "v_lshlrev_b32 v38, 16, v119\n"
    "v_and_b32 v39, 0xffff0000, v119\n"
    "v_mov_b32 v24, v12\n"
    "v_mov_b32 v25, v13\n"
    "v_mov_b32 v30, v12\n"
    "v_mov_b32 v31, v13\n"
    "v_mov_b32 v26, 0\n"
    "v_mov_b32 v27, 0\n"
    "v_mov_b32 v28, 0\n"
    "v_mov_b32 v29, 0\n"
    "v_mov_b32 v42, 0\n"
    "v_mov_b32 v43, 0\n"
    "v_mov_b32 v48, 0\n"
    "v_mov_b32 v44, 0x3b000000\n"
    "s_mov_b32 s40, 0\n"
    "s_mov_b32 s41, 0\n"
    "s_mov_b32 s42, 64\n"
    "s_mov_b32 s43, 0\n"
    "s_mov_b32 s44, 0\n"
    "s_mov_b32 s45, 0\n"
    "s_mov_b32 s46, -1\n"
    "s_mov_b32 s47, -1\n"
    "s_mov_b32 s48, 0\n"
    "s_mov_b32 s51, 0\n"
    "s_mov_b32 s49, 64\n"
    "s_mov_b32 s37, 0\n"
    "s_mov_b64 s[64:65], 0\n"
    "s_mov_b32 s79, 0\n"
    "s_mov_b32 s74, 0xffff0000\n"
    "s_mov_b32 s75, 0\n"
    "s_mov_b32 s76, 0xaaaaaaaa\n"
    "s_mov_b32 s77, 0xaaaaaaaa\n"
    "LRY%=:\n"
    "global_load_dword v40, v48, s[96:97] sc1\n"
    "s_waitcnt vmcnt(0)\n"
    "v_readfirstlane_b32 s56, v40\n"
    "s_cmp_lg_u32 s56, 0\n"
    "s_cbranch_scc1 LRZ%=\n"
    "s_sleep 8\n"
    "s_add_u32 s37, s37, 1\n"
    "s_cmp_lt_u32 s37, 0x80000\n"
    "s_cbranch_scc1 LRY%=\n"
    "s_branch LFAIL%=\n"
    "LRZ%=:\n"
    "s_lshl_b32 s56, s56, 13\n"
    "s_and_b32 s56, s56, 0x2000\n"
    "s_xor_b32 s57, s56, 0x2000\n"
    "s_xor_b32 s58, s58, s56\n"
    "v_xor_b32 v2, s56, v2\n"
    "v_xor_b32 v11, s56, v11\n"
    "v_xor_b32 v12, s56, v21\n"
    "s_cmp_eq_u32 s55, 0\n"
    "s_cselect_b32 s38, 0, 0x80000000\n"
    "s_add_u32 s38, s38, 4096\n"
    "v_add_u32 v13, s38, v11\n"
    "v_mov_b32 v16, -1\n"
    "v_mov_b32 v17, -1\n"
    "v_mov_b32 v18, -1\n"
    "v_mov_b32 v19, -1\n"
    "s_mov_b32 s60, s55\n"
    "LEM%=:\n"
    "s_lshl_b32 s39, s60, 10\n"
    "s_andn2_b32 s61, s39, 0x1fff\n"
    "s_add_u32 s61, s61, s39\n"
    "s_add_u32 s61, s61, 236978176\n"
    "s_lshr_b32 s38, s61, 8\n"
    "s_xor_b32 s38, s38, s56\n"
    "s_and_b32 s38, s38, 0x2000\n"
    "s_or_b32 s61, s61, s38\n"
    "buffer_store_dwordx4 v[16:19], v0, s[16:19], s61 offen sc1\n"
    "s_add_u32 s60, s60, 32\n"
    "s_cmp_lt_u32 s60, 128\n"
    "s_cbranch_scc1 LEM%=\n"
    "s_add_u32 s70, s58, 4224\n"
    "v_mov_b32 v72, 1\n"
    "s_waitcnt vmcnt(0)\n"
    "s_mov_b64 exec, 1\n"
    "buffer_atomic_add v72, off, s[16:19], s70 sc1\n"
    "s_mov_b64 exec, -1\n"
    "s_cmp_lg_u32 s55, 0\n"
    "s_cbranch_scc1 LPR%=\n"
    "LEW%=:\n"
    "buffer_load_dword v40, off, s[16:19], s70 sc1\n"
    "s_waitcnt vmcnt(0)\n"
    "v_cmp_eq_u32 vcc, 32, v40\n"
    "s_cbranch_vccnz LPR%=\n"
    "s_sleep 8\n"
    "s_add_u32 s37, s37, 1\n"
    "s_cmp_lt_u32 s37, 0x80000\n"
    "s_cbranch_scc1 LEW%=\n"
    "s_branch LFAIL%=\n"
    "LPR%=:\n"
    "buffer_load_dword v40, v12, s[16:19], 0 offen sc1\n"
    "s_waitcnt vmcnt(0)\n"
    "v_cmp_eq_u32 vcc, 0, v40\n"
    "s_cbranch_vccz LPOLL%=\n"
    "s_sleep 8\n"
    "s_add_u32 s37, s37, 1\n"
    "s_cmp_lt_u32 s37, 0x80000\n"
    "s_cbranch_scc1 LPR%=\n"
    "s_branch LFAIL%=\n"
    "LIDLE%=:\n"
    "s_add_u32 s37, s37, 1\n"
    "s_cmp_gt_u32 s37, 0x100000\n"
    "s_cbranch_scc1 LFAIL%=\n"
    "s_sleep 1\n"
    "LPOLL%=:\n"
    "v_mad_u32_u24 v41, v3, s41, v2\n"
    "v_mad_u32_u24 v41, v5, s40, v41\n"
    "buffer_load_dword v40, v41, s[16:19], 0 offen sc1\n"
    "s_waitcnt vmcnt(0)\n"
    "LTOP%=:\n"
    "v_readlane_b32 s52, v40, 32\n"
    "v_readlane_b32 s53, v40, 0\n"
    "v_readlane_b32 s54, v40, 16\n"
    "s_cmp_lg_u32 s52, 0\n"
    "s_cbranch_scc1 LDEC%=\n"
    "LPICK%=:\n"
    "s_cmp_eq_u32 s53, 0\n"
    "s_mov_b32 s50, -1\n"
    "s_cbranch_scc1 LNONE%=\n"
    "s_mov_b32 s50, s41\n"
    "s_add_u32 s41, s41, 1\n"
    "s_lshl_b32 s62, s50, 18\n"
    "s_add_u32 s62, s62, 180355072\n"
    "s_lshl_b32 s63, s50, 5\n"
    "s_add_u32 s63, s63, s55\n"
    "s_lshl_b32 s63, s63, 10\n"
    "s_lshr_b32 s38, s62, 8\n"
    "s_xor_b32 s38, s38, s56\n"
    "s_and_b32 s38, s38, 0x2000\n"
    "s_or_b32 s62, s62, s38\n"
    "buffer_load_dwordx4 v[116:119], v0, s[20:23], s63 offen\n"
    "buffer_load_dwordx4 v[100:103], v1, s[16:19], s62 offen sc1\n"
    "s_add_u32 s62, s62, 0x10000\n"
    "buffer_load_dwordx4 v[104:107], v1, s[16:19], s62 offen sc1\n"
    "s_add_u32 s62, s62, 0x10000\n"
    "buffer_load_dwordx4 v[108:111], v1, s[16:19], s62 offen sc1\n"
    "s_add_u32 s62, s62, 0x10000\n"
    "buffer_load_dwordx4 v[112:115], v1, s[16:19], s62 offen sc1\n"
    "s_cmp_ge_i32 s47, 0\n"
    "s_cbranch_scc1 LJOB%=\n"
    "s_lshl_b32 s60, s50, 5\n"
    "s_add_u32 s60, s60, s55\n"
    "s_lshl_b32 s39, s60, 11\n"
    "s_andn2_b32 s32, s39, 0x1fff\n"
    "s_add_u32 s32, s32, s39\n"
    "s_add_u32 s32, s32, 245366784\n"
    "s_lshr_b32 s38, s32, 8\n"
    "s_xor_b32 s38, s38, s56\n"
    "s_and_b32 s38, s38, 0x2000\n"
    "s_or_b32 s32, s32, s38\n"
    "s_lshl_b32 s39, s60, 10\n"
    "s_andn2_b32 s78, s39, 0x1fff\n"
    "s_add_u32 s78, s78, s39\n"
    "s_add_u32 s78, s78, 236978176\n"
    "s_lshr_b32 s38, s78, 8\n"
    "s_xor_b32 s38, s38, s56\n"
    "s_and_b32 s38, s38, 0x2000\n"
    "s_or_b32 s78, s78, s38\n"
    "s_lshl_b32 s39, s60, 10\n"
    "s_andn2_b32 s34, s39, 0x1fff\n"
    "s_add_u32 s34, s34, s39\n"
    "s_add_u32 s34, s34, 237240320\n"
    "s_lshr_b32 s38, s34, 8\n"
    "s_xor_b32 s38, s38, s56\n"
    "s_and_b32 s38, s38, 0x2000\n"
    "s_or_b32 s34, s34, s38\n"
    "s_cmp_lt_u32 s50, 60\n"
    "s_cselect_b32 s34, s34, 0x80000000\n"
    "s_mov_b32 s33, s60\n"
    "s_waitcnt vmcnt(4)\n"
    "v_lshlrev_b32 v64, 16, v116\n"
    "v_and_b32 v65, 0xffff0000, v116\n"
    "v_lshlrev_b32 v66, 16, v117\n"
    "v_and_b32 v67, 0xffff0000, v117\n"
    "v_lshlrev_b32 v68, 16, v118\n"
    "v_and_b32 v69, 0xffff0000, v118\n"
    "v_lshlrev_b32 v70, 16, v119\n"
    "v_and_b32 v71, 0xffff0000, v119\n"
    "s_waitcnt vmcnt(3)\n"
    "v_mfma_f32_16x16x32_bf16 v[64:67], v[24:27], v[100:103], v[64:67]\n"
    "v_mfma_f32_16x16x32_bf16 v[68:71], v[28:31], v[100:103], v[68:71]\n"
    "s_waitcnt vmcnt(2)\n"
    "v_mfma_f32_16x16x32_bf16 v[64:67], v[24:27], v[104:107], v[64:67]\n"
    "v_mfma_f32_16x16x32_bf16 v[68:71], v[28:31], v[104:107], v[68:71]\n"
    "s_waitcnt vmcnt(1)\n"
    "v_mfma_f32_16x16x32_bf16 v[64:67], v[24:27], v[108:111], v[64:67]\n"
    "v_mfma_f32_16x16x32_bf16 v[68:71], v[28:31], v[108:111], v[68:71]\n"
    "v_max3_u32 v48, v103, v107, v111\n"
    "s_waitcnt vmcnt(0)\n"
    "v_mfma_f32_16x16x32_bf16 v[64:67], v[24:27], v[112:115], v[64:67]\n"
    "v_mfma_f32_16x16x32_bf16 v[68:71], v[28:31], v[112:115], v[68:71]\n"
    "v_max_u32 v48, v48, v115\n"
    "v_cmp_eq_u32 vcc, -1, v48\n"
    "s_mov_b32 s47, s50\n"
    "s_mov_b32 s48, s51\n"
    "s_mov_b32 s50, -1\n"
    "s_cbranch_vccnz LEARLY%=\n"
    "LNXT%=:\n"
    "s_mov_b32 s53, s54\n"
    "s_cmp_lg_u32 s54, 0\n"
    "s_cbranch_scc1 LPICK%=\n"
    "LJOB%=:\n"
    "v_cvt_pk_bf16_f32 v124, v64, v65\n"
    "v_lshlrev_b32 v72, 16, v124\n"
    "v_cvt_pk_bf16_f32 v125, v66, v67\n"
    "v_readfirstlane_b32 s68, v72\n"
    "v_cvt_pk_bf16_f32 v126, v68, v69\n"
    "v_cvt_pk_bf16_f32 v127, v70, v71\n"
    "v_and_b32 v73, 0xffff0000, v124\n"
    "v_lshlrev_b32 v74, 16, v125\n"
    "v_and_b32 v75, 0xffff0000, v125\n"
    "v_lshlrev_b32 v76, 16, v126\n"
    "v_and_b32 v77, 0xffff0000, v126\n"
    "v_lshlrev_b32 v78, 16, v127\n"
    "v_and_b32 v79, 0xffff0000, v127\n"
    "v_pk_add_f32 v[92:93], v[72:73], s[68:69] op_sel_hi:[1,0] neg_lo:[0,1] neg_hi:[0,1]\n"
    "v_pk_add_f32 v[94:95], v[74:75], s[68:69] op_sel_hi:[1,0] neg_lo:[0,1] neg_hi:[0,1]\n"
    "v_pk_add_f32 v[96:97], v[76:77], s[68:69] op_sel_hi:[1,0] neg_lo:[0,1] neg_hi:[0,1]\n"
    "v_pk_add_f32 v[98:99], v[78:79], s[68:69] op_sel_hi:[1,0] neg_lo:[0,1] neg_hi:[0,1]\n"
    "v_pk_add_f32 v[48:49], v[92:93], v[94:95]\n"
    "v_pk_mul_f32 v[50:51], v[92:93], v[92:93]\n"
    "v_pk_add_f32 v[52:53], v[96:97], v[98:99]\n"
    "v_pk_fma_f32 v[50:51], v[94:95], v[94:95], v[50:51]\n"
    "v_pk_add_f32 v[48:49], v[48:49], v[52:53]\n"
    "v_pk_fma_f32 v[50:51], v[96:97], v[96:97], v[50:51]\n"
    "v_add_f32 v48, v48, v49\n"
    "v_pk_fma_f32 v[50:51], v[98:99], v[98:99], v[50:51]\n"
    "v_mov_b32 v52, s68\n"
    "v_add_f32 v49, v50, v51\n"
    "v_add_f32_dpp v48, v48, v48 row_ror:8 row_mask:0xf bank_mask:0xf\n"
    "s_nop 0\n"
    "v_add_f32_dpp v49, v49, v49 row_ror:8 row_mask:0xf bank_mask:0xf\n"
    "v_add_f32_dpp v48, v48, v48 row_ror:4 row_mask:0xf bank_mask:0xf\n"
    "s_nop 0\n"
    "v_add_f32_dpp v49, v49, v49 row_ror:4 row_mask:0xf bank_mask:0xf\n"
    "v_add_f32_dpp v48, v48, v48 row_ror:2 row_mask:0xf bank_mask:0xf\n"
    "s_nop 0\n"
    "v_add_f32_dpp v49, v49, v49 row_ror:2 row_mask:0xf bank_mask:0xf\n"
    "v_add_f32_dpp v48, v48, v48 row_ror:1 row_mask:0xf bank_mask:0xf\n"
    "s_nop 0\n"
    "v_add_f32_dpp v49, v49, v49 row_ror:1 row_mask:0xf bank_mask:0xf\n"
    "s_nop 1\n"
    "v_permlane16_swap_b32 v48, v49\n"
    "v_add_f32 v48, v48, v49\n"
    "v_mov_b32 v49, v48\n"
    "s_nop 1\n"
    "v_permlane32_swap_b32 v48, v49\n"
    "v_add_f32 v48, v48, v49\n"
    "s_nop 0\n"
    "v_readlane_b32 s62, v48, 0\n"
    "v_readlane_b32 s63, v48, 16\n"
    "s_nop 0\n"
    "v_mul_f32 v48, s62, v44\n"
    "v_mul_f32 v49, s63, v44\n"
    "v_fma_f32 v49, -v48, v48, v49\n"
    "v_add_f32 v52, v52, v48\n"
    "v_max_f32 v49, 0, v49\n"
    "v_cvt_pk_bf16_f32 v48, v52, v49\n"
    "v_and_b32 v49, 0xffff0000, v48\n"
    "v_add_f32 v49, 0x3727c5ac, v49\n"
    "v_cvt_pk_bf16_f32 v49, v49, v49\n"
    "v_lshlrev_b32 v49, 16, v49\n"
    "v_sqrt_f32 v50, v49\n"
    "v_lshlrev_b32 v48, 16, v48\n"
    "v_cvt_pk_bf16_f32 v50, v50, v50\n"
    "v_lshlrev_b32 v50, 16, v50\n"
    "v_rcp_f32 v50, v50\n"
    "v_pk_add_f32 v[92:93], v[72:73], v[48:49] op_sel_hi:[1,0] neg_lo:[0,1] neg_hi:[0,1]\n"
    "v_pk_add_f32 v[94:95], v[74:75], v[48:49] op_sel_hi:[1,0] neg_lo:[0,1] neg_hi:[0,1]\n"
    "v_pk_add_f32 v[96:97], v[76:77], v[48:49] op_sel_hi:[1,0] neg_lo:[0,1] neg_hi:[0,1]\n"
    "v_pk_add_f32 v[98:99], v[78:79], v[48:49] op_sel_hi:[1,0] neg_lo:[0,1] neg_hi:[0,1]\n"
    "v_cvt_pk_bf16_f32 v52, v92, v93\n"
    "v_cvt_pk_bf16_f32 v53, v94, v95\n"
    "v_cvt_pk_bf16_f32 v54, v96, v97\n"
    "v_cvt_pk_bf16_f32 v55, v98, v99\n"
    "v_lshlrev_b32 v92, 16, v52\n"
    "v_and_b32 v93, 0xffff0000, v52\n"
    "v_lshlrev_b32 v94, 16, v53\n"
    "v_and_b32 v95, 0xffff0000, v53\n"
    "v_lshlrev_b32 v96, 16, v54\n"
    "v_and_b32 v97, 0xffff0000, v54\n"
    "v_lshlrev_b32 v98, 16, v55\n"
    "v_and_b32 v99, 0xffff0000, v55\n"
    "v_pk_mul_f32 v[80:81], v[92:93], v[50:51] op_sel_hi:[1,0]\n"
    "v_pk_mul_f32 v[82:83], v[94:95], v[50:51] op_sel_hi:[1,0]\n"
    "v_pk_mul_f32 v[84:85], v[96:97], v[50:51] op_sel_hi:[1,0]\n"
    "v_pk_mul_f32 v[86:87], v[98:99], v[50:51] op_sel_hi:[1,0]\n"
    "v_cvt_pk_bf16_f32 v88, v80, v81\n"
    "v_cvt_pk_bf16_f32 v89, v82, v83\n"
    "v_cvt_pk_bf16_f32 v90, v84, v85\n"
    "v_cvt_pk_bf16_f32 v91, v86, v87\n"
    "v_lshlrev_b32 v80, 16, v88\n"
    "v_and_b32 v81, 0xffff0000, v88\n"
    "v_lshlrev_b32 v82, 16, v89\n"
    "v_and_b32 v83, 0xffff0000, v89\n"
    "v_lshlrev_b32 v84, 16, v90\n"
    "v_and_b32 v85, 0xffff0000, v90\n"
    "v_lshlrev_b32 v86, 16, v91\n"
    "v_and_b32 v87, 0xffff0000, v91\n"
    "v_pk_add_f32 v[72:73], v[72:73], v[80:81]\n"
    "v_pk_add_f32 v[74:75], v[74:75], v[82:83]\n"
    "v_pk_add_f32 v[76:77], v[76:77], v[84:85]\n"
    "v_pk_add_f32 v[78:79], v[78:79], v[86:87]\n"
    "v_cvt_pk_bf16_f32 v60, v72, v73\n"
    "v_cvt_pk_bf16_f32 v61, v74, v75\n"
    "v_cvt_pk_bf16_f32 v62, v76, v77\n"
    "v_cvt_pk_bf16_f32 v63, v78, v79\n"
    "v_lshlrev_b32 v72, 16, v60\n"
    "v_and_b32 v73, 0xffff0000, v60\n"
    "v_lshlrev_b32 v74, 16, v61\n"
    "v_and_b32 v75, 0xffff0000, v61\n"
    "v_lshlrev_b32 v76, 16, v62\n"
    "v_and_b32 v77, 0xffff0000, v62\n"
    "v_lshlrev_b32 v78, 16, v63\n"
    "v_and_b32 v79, 0xffff0000, v63\n"
    "s_lshl_b32 s70, s47, 6\n"
    "s_add_u32 s70, s70, s58\n"
    "v_pk_add_f32 v[92:93], v[72:73], s[68:69] op_sel_hi:[1,0] neg_lo:[0,1] neg_hi:[0,1]\n"
    "v_pk_add_f32 v[94:95], v[74:75], s[68:69] op_sel_hi:[1,0] neg_lo:[0,1] neg_hi:[0,1]\n"
    "v_pk_add_f32 v[96:97], v[76:77], s[68:69] op_sel_hi:[1,0] neg_lo:[0,1] neg_hi:[0,1]\n"
    "v_pk_add_f32 v[98:99], v[78:79], s[68:69] op_sel_hi:[1,0] neg_lo:[0,1] neg_hi:[0,1]\n"
    "v_pk_add_f32 v[48:49], v[92:93], v[94:95]\n"
    "v_pk_mul_f32 v[50:51], v[92:93], v[92:93]\n"
    "v_pk_add_f32 v[52:53], v[96:97], v[98:99]\n"
    "v_pk_fma_f32 v[50:51], v[94:95], v[94:95], v[50:51]\n"
    "v_pk_add_f32 v[48:49], v[48:49], v[52:53]\n"
    "v_pk_fma_f32 v[50:51], v[96:97], v[96:97], v[50:51]\n"
    "v_add_f32 v48, v48, v49\n"
    "v_pk_fma_f32 v[50:51], v[98:99], v[98:99], v[50:51]\n"
    "v_mov_b32 v52, s68\n"
    "v_add_f32 v49, v50, v51\n"
    "v_add_f32_dpp v48, v48, v48 row_ror:8 row_mask:0xf bank_mask:0xf\n"
    "s_nop 0\n"
    "v_add_f32_dpp v49, v49, v49 row_ror:8 row_mask:0xf bank_mask:0xf\n"
    "v_pk_mul_f32 v[56:57], v[80:81], v[32:33]\n"
    "v_add_f32_dpp v48, v48, v48 row_ror:4 row_mask:0xf bank_mask:0xf\n"
    "v_add_f32_dpp v49, v49, v49 row_ror:4 row_mask:0xf bank_mask:0xf\n"
    "v_pk_fma_f32 v[56:57], v[82:83], v[34:35], v[56:57]\n"
    "v_add_f32_dpp v48, v48, v48 row_ror:2 row_mask:0xf bank_mask:0xf\n"
    "v_add_f32_dpp v49, v49, v49 row_ror:2 row_mask:0xf bank_mask:0xf\n"
    "v_pk_fma_f32 v[56:57], v[84:85], v[36:37], v[56:57]\n"
    "v_add_f32_dpp v48, v48, v48 row_ror:1 row_mask:0xf bank_mask:0xf\n"
    "v_add_f32_dpp v49, v49, v49 row_ror:1 row_mask:0xf bank_mask:0xf\n"
    "v_pk_fma_f32 v[56:57], v[86:87], v[38:39], v[56:57]\n"
    "s_nop 0\n"
    "v_permlane16_swap_b32 v48, v49\n"
    "v_add_f32 v48, v48, v49\n"
    "v_mov_b32 v49, v48\n"
    "s_nop 1\n"
    "v_permlane32_swap_b32 v48, v49\n"
    "v_add_f32 v48, v48, v49\n"
    "s_nop 0\n"
    "v_readlane_b32 s62, v48, 0\n"
    "v_readlane_b32 s63, v48, 16\n"
    "s_nop 0\n"
    "v_mul_f32 v48, s62, v44\n"
    "v_mul_f32 v49, s63, v44\n"
    "v_fma_f32 v49, -v48, v48, v49\n"
    "v_add_f32 v52, v52, v48\n"
    "v_max_f32 v49, 0, v49\n"
    "v_cvt_pk_bf16_f32 v48, v52, v49\n"
    "v_and_b32 v49, 0xffff0000, v48\n"
    "v_add_f32 v49, 0x3727c5ac, v49\n"
    "v_cvt_pk_bf16_f32 v49, v49, v49\n"
    "v_lshlrev_b32 v49, 16, v49\n"
    "v_sqrt_f32 v50, v49\n"
    "v_lshlrev_b32 v48, 16, v48\n"
    "v_cvt_pk_bf16_f32 v50, v50, v50\n"
    "v_lshlrev_b32 v50, 16, v50\n"
    "v_rcp_f32 v50, v50\n"
    "v_pk_add_f32 v[92:93], v[72:73], v[48:49] op_sel_hi:[1,0] neg_lo:[0,1] neg_hi:[0,1]\n"
    "v_pk_add_f32 v[94:95], v[74:75], v[48:49] op_sel_hi:[1,0] neg_lo:[0,1] neg_hi:[0,1]\n"
    "v_pk_add_f32 v[96:97], v[76:77], v[48:49] op_sel_hi:[1,0] neg_lo:[0,1] neg_hi:[0,1]\n"
    "v_pk_add_f32 v[98:99], v[78:79], v[48:49] op_sel_hi:[1,0] neg_lo:[0,1] neg_hi:[0,1]\n"
    "v_pk_mul_f32 v[64:65], v[92:93], v[50:51] op_sel_hi:[1,0]\n"
    "v_pk_mul_f32 v[66:67], v[94:95], v[50:51] op_sel_hi:[1,0]\n"
    "v_pk_mul_f32 v[68:69], v[96:97], v[50:51] op_sel_hi:[1,0]\n"
    "v_pk_mul_f32 v[70:71], v[98:99], v[50:51] op_sel_hi:[1,0]\n"
    "v_cvt_pk_bf16_f32 v95, v70, v71\n"
    "v_cvt_pk_bf16_f32 v92, v64, v65\n"
    "v_cvt_pk_bf16_f32 v93, v66, v67\n"
    "v_cvt_pk_bf16_f32 v94, v68, v69\n"
    "v_min_u32 v95, -2, v95\n"
    "buffer_store_dwordx4 v[92:95], v0, s[16:19], s78 offen offset:0 sc1\n"
    "s_lshl_b32 s39, s47, 2\n"
    "s_mov_b64 exec, 7\n"
    "buffer_store_dword v6, v13, s[16:19], s39 offen sc1\n"
    "s_mov_b64 exec, -1\n"
    "v_add_f32_dpp v56, v56, v56 row_ror:8 row_mask:0xf bank_mask:0xf\n"
    "v_add_f32_dpp v57, v57, v57 row_ror:8 row_mask:0xf bank_mask:0xf\n"
    "s_nop 0\n"
    "v_add_f32_dpp v56, v56, v56 row_ror:4 row_mask:0xf bank_mask:0xf\n"
    "v_add_f32_dpp v57, v57, v57 row_ror:4 row_mask:0xf bank_mask:0xf\n"
    "s_nop 0\n"
    "v_add_f32_dpp v56, v56, v56 row_ror:2 row_mask:0xf bank_mask:0xf\n"
    "v_add_f32_dpp v57, v57, v57 row_ror:2 row_mask:0xf bank_mask:0xf\n"
    "s_nop 0\n"
    "v_add_f32_dpp v56, v56, v56 row_ror:1 row_mask:0xf bank_mask:0xf\n"
    "v_add_f32_dpp v57, v57, v57 row_ror:1 row_mask:0xf bank_mask:0xf\n"
    "s_nop 1\n"
    "v_permlane16_swap_b32 v56, v57\n"
    "v_add_f32 v56, v56, v57\n"
    "v_mov_b32 v57, v56\n"
    "s_nop 1\n"
    "v_permlane32_swap_b32 v56, v57\n"
    "v_add_f32 v56, v56, v57\n"
    "s_nop 0\n"
    "v_readlane_b32 s62, v56, 0\n"
    "v_readlane_b32 s63, v56, 16\n"
    "s_sub_u32 s39, 0xffff, s33\n"
    "v_mov_b32 v56, s62\n"
    "v_add_f32 v56, s63, v56\n"
    "v_cvt_pk_bf16_f32 v56, v56, v56\n"
    "v_lshlrev_b32 v56, 16, v56\n"
    "v_cmp_o_f32 vcc, v56, v56\n"
    "v_ashrrev_i32 v57, 31, v56\n"
    "v_or_b32 v57, 0x80000000, v57\n"
    "v_xor_b32 v56, v56, v57\n"
    "v_cndmask_b32 v56, 0, v56, vcc\n"
    "v_and_b32 v56, 0xffff0000, v56\n"
    "v_or_b32 v56, s39, v56\n"
    "s_mov_b64 exec, 1\n"
    "buffer_atomic_umax v56, off, s[16:19], s70 offset:4 sc1\n"
    "s_mov_b64 exec, -1\n"
    "buffer_store_dwordx4 v[88:91], v0, s[16:19], s32 offen offset:1024 sc1\n"
    "buffer_store_dwordx4 v[124:127], v0, s[16:19], s32 offen offset:0 sc1\n"
    "s_add_u32 s61, s78, 4194304\n"
    "buffer_store_dwordx4 v[60:63], v0, s[16:19], s61 offen offset:0 sc1\n"
    "buffer_store_dwordx4 v[16:19], v0, s[16:19], s34 offen offset:0 sc1\n"
    "v_mov_b64 v[120:121], 1\n"
    "v_mad_u32_u24 v41, v3, s41, v2\n"
    "v_mad_u32_u24 v41, v5, s40, v41\n"
    "s_nop 7\n"
    "s_nop 7\n"
    "s_nop 7\n"
    "buffer_load_dword v40, v41, s[16:19], 0 offen sc1\n"
    "s_mov_b32 s73, s47\n"
    "s_mov_b64 exec, 1\n"
    "s_waitcnt vmcnt(1)\n"
    "buffer_atomic_add_x2 v[120:121], off, s[16:19], s70 sc0 sc1\n"
    "s_mov_b64 exec, -1\n"
    "s_mov_b32 s47, -1\n"
    "s_mov_b32 s37, 0\n"
    "s_cmp_ge_i32 s50, 0\n"
    "s_cbranch_scc1 LAB%=\n"
    "s_waitcnt vmcnt(1)\n"
    "v_readlane_b32 s52, v40, 32\n"
    "v_readlane_b32 s53, v40, 0\n"
    "v_readlane_b32 s54, v40, 16\n"
    "s_cmp_eq_u32 s52, 0\n"
    "s_cselect_b32 s38, s53, 0\n"
    "s_cmp_lg_u32 s38, 0\n"
    "s_cbranch_scc1 LAC%=\n"
    "buffer_load_dword v40, v41, s[16:19], 0 offen sc1\n"
    "s_waitcnt vmcnt(1)\n"
    "v_readfirstlane_b32 s38, v120\n"
    "v_readfirstlane_b32 s52, v121\n"
    "s_lshl_b32 s39, s73, 2\n"
    "s_cmp_eq_u32 s38, 31\n"
    "v_mov_b32 v120, s52\n"
    "s_cselect_b32 s39, s39, 0x80000000\n"
    "s_cselect_b32 s73, s73, -1\n"
    "s_mov_b64 exec, 0x38\n"
    "buffer_store_dword v120, v11, s[16:19], s39 offen sc1\n"
    "s_mov_b64 exec, -1\n"
    "s_waitcnt vmcnt(1)\n"
    "LAD%=:\n"
    "s_cmp_lg_u32 s40, s73\n"
    "s_cbranch_scc1 LTOP%=\n"
    "v_writelane_b32 v40, s52, 32\n"
    "s_nop 0\n"
    "s_branch LTOP%=\n"
    "LAC%=:\n"
    "s_mov_b32 s50, s41\n"
    "s_add_u32 s41, s41, 1\n"
    "s_lshl_b32 s62, s50, 18\n"
    "s_add_u32 s62, s62, 180355072\n"
    "s_lshl_b32 s63, s50, 5\n"
    "s_add_u32 s63, s63, s55\n"
    "s_lshl_b32 s63, s63, 10\n"
    "s_lshr_b32 s38, s62, 8\n"
    "s_xor_b32 s38, s38, s56\n"
    "s_and_b32 s38, s38, 0x2000\n"
    "s_or_b32 s62, s62, s38\n"
    "buffer_load_dwordx4 v[116:119], v0, s[20:23], s63 offen\n"
    "buffer_load_dwordx4 v[100:103], v1, s[16:19], s62 offen sc1\n"
    "s_add_u32 s62, s62, 0x10000\n"
    "buffer_load_dwordx4 v[104:107], v1, s[16:19], s62 offen sc1\n"
    "s_add_u32 s62, s62, 0x10000\n"
    "buffer_load_dwordx4 v[108:111], v1, s[16:19], s62 offen sc1\n"
    "s_add_u32 s62, s62, 0x10000\n"
    "buffer_load_dwordx4 v[112:115], v1, s[16:19], s62 offen sc1\n"
    "s_waitcnt vmcnt(5)\n"
    "v_readfirstlane_b32 s38, v120\n"
    "v_readfirstlane_b32 s52, v121\n"
    "s_lshl_b32 s39, s73, 2\n"
    "s_cmp_eq_u32 s38, 31\n"
    "v_mov_b32 v120, s52\n"
    "s_cselect_b32 s39, s39, 0x80000000\n"
    "s_cselect_b32 s73, s73, -1\n"
    "s_mov_b64 exec, 0x38\n"
    "buffer_store_dword v120, v11, s[16:19], s39 offen sc1\n"
    "s_mov_b64 exec, -1\n"
    "s_cmp_lg_u32 s40, s73\n"
    "s_cbranch_scc1 LAE%=\n"
    "s_lshr_b32 s38, s52, 16\n"
    "s_cmp_gt_i32 s38, s46\n"
    "s_cbranch_scc0 LAE%=\n"
    "s_sub_u32 s41, s41, 1\n"
    "s_waitcnt vmcnt(1)\n"
    "s_branch LDEC%=\n"
    "LAE%=:\n"
    "s_lshl_b32 s60, s50, 5\n"
    "s_add_u32 s60, s60, s55\n"
    "s_lshl_b32 s39, s60, 11\n"
    "s_andn2_b32 s32, s39, 0x1fff\n"
    "s_add_u32 s32, s32, s39\n"
    "s_add_u32 s32, s32, 245366784\n"
    "s_lshr_b32 s38, s32, 8\n"
    "s_xor_b32 s38, s38, s56\n"
    "s_and_b32 s38, s38, 0x2000\n"
    "s_or_b32 s32, s32, s38\n"
    "s_lshl_b32 s39, s60, 10\n"
    "s_andn2_b32 s78, s39, 0x1fff\n"
    "s_add_u32 s78, s78, s39\n"
    "s_add_u32 s78, s78, 236978176\n"
    "s_lshr_b32 s38, s78, 8\n"
    "s_xor_b32 s38, s38, s56\n"
    "s_and_b32 s38, s38, 0x2000\n"
    "s_or_b32 s78, s78, s38\n"
    "s_lshl_b32 s39, s60, 10\n"
    "s_andn2_b32 s34, s39, 0x1fff\n"
    "s_add_u32 s34, s34, s39\n"
    "s_add_u32 s34, s34, 237240320\n"
    "s_lshr_b32 s38, s34, 8\n"
    "s_xor_b32 s38, s38, s56\n"
    "s_and_b32 s38, s38, 0x2000\n"
    "s_or_b32 s34, s34, s38\n"
    "s_cmp_lt_u32 s50, 60\n"
    "s_cselect_b32 s34, s34, 0x80000000\n"
    "s_mov_b32 s33, s60\n"
    "s_waitcnt vmcnt(5)\n"
    "v_lshlrev_b32 v64, 16, v116\n"
    "v_and_b32 v65, 0xffff0000, v116\n"
    "v_lshlrev_b32 v66, 16, v117\n"
    "v_and_b32 v67, 0xffff0000, v117\n"
    "v_lshlrev_b32 v68, 16, v118\n"
    "v_and_b32 v69, 0xffff0000, v118\n"
    "v_lshlrev_b32 v70, 16, v119\n"
    "v_and_b32 v71, 0xffff0000, v119\n"
    "s_waitcnt vmcnt(4)\n"
    "v_mfma_f32_16x16x32_bf16 v[64:67], v[24:27], v[100:103], v[64:67]\n"
    "v_mfma_f32_16x16x32_bf16 v[68:71], v[28:31], v[100:103], v[68:71]\n"
    "s_waitcnt vmcnt(3)\n"
    "v_mfma_f32_16x16x32_bf16 v[64:67], v[24:27], v[104:107], v[64:67]\n"
    "v_mfma_f32_16x16x32_bf16 v[68:71], v[28:31], v[104:107], v[68:71]\n"
    "s_waitcnt vmcnt(2)\n"
    "v_mfma_f32_16x16x32_bf16 v[64:67], v[24:27], v[108:111], v[64:67]\n"
    "v_mfma_f32_16x16x32_bf16 v[68:71], v[28:31], v[108:111], v[68:71]\n"
    "v_max3_u32 v48, v103, v107, v111\n"
    "s_waitcnt vmcnt(1)\n"
    "v_mfma_f32_16x16x32_bf16 v[64:67], v[24:27], v[112:115], v[64:67]\n"
    "v_mfma_f32_16x16x32_bf16 v[68:71], v[28:31], v[112:115], v[68:71]\n"
    "v_max_u32 v48, v48, v115\n"
    "v_cmp_eq_u32 vcc, -1, v48\n"
    "s_mov_b32 s47, s50\n"
    "s_mov_b32 s48, s51\n"
    "s_mov_b32 s50, -1\n"
    "s_cbranch_vccnz LEARLY%=\n"
    "s_branch LNXT%=\n"
    "LAB%=:\n"
    "s_waitcnt vmcnt(0)\n"
    "v_readfirstlane_b32 s38, v120\n"
    "v_readfirstlane_b32 s52, v121\n"
    "s_lshl_b32 s39, s73, 2\n"
    "s_cmp_eq_u32 s38, 31\n"
    "v_mov_b32 v120, s52\n"
    "s_cselect_b32 s39, s39, 0x80000000\n"
    "s_cselect_b32 s73, s73, -1\n"
    "s_mov_b64 exec, 0x38\n"
    "buffer_store_dword v120, v11, s[16:19], s39 offen sc1\n"
    "s_mov_b64 exec, -1\n"
    "s_lshl_b32 s60, s50, 5\n"
    "s_add_u32 s60, s60, s55\n"
    "s_lshl_b32 s39, s60, 11\n"
    "s_andn2_b32 s32, s39, 0x1fff\n"
    "s_add_u32 s32, s32, s39\n"
    "s_add_u32 s32, s32, 245366784\n"
    "s_lshr_b32 s38, s32, 8\n"
    "s_xor_b32 s38, s38, s56\n"
    "s_and_b32 s38, s38, 0x2000\n"
    "s_or_b32 s32, s32, s38\n"
    "s_lshl_b32 s39, s60, 10\n"
    "s_andn2_b32 s78, s39, 0x1fff\n"
    "s_add_u32 s78, s78, s39\n"
    "s_add_u32 s78, s78, 236978176\n"
    "s_lshr_b32 s38, s78, 8\n"
    "s_xor_b32 s38, s38, s56\n"
    "s_and_b32 s38, s38, 0x2000\n"
    "s_or_b32 s78, s78, s38\n"
    "s_lshl_b32 s39, s60, 10\n"
    "s_andn2_b32 s34, s39, 0x1fff\n"
    "s_add_u32 s34, s34, s39\n"
    "s_add_u32 s34, s34, 237240320\n"
    "s_lshr_b32 s38, s34, 8\n"
    "s_xor_b32 s38, s38, s56\n"
    "s_and_b32 s38, s38, 0x2000\n"
    "s_or_b32 s34, s34, s38\n"
    "s_cmp_lt_u32 s50, 60\n"
    "s_cselect_b32 s34, s34, 0x80000000\n"
    "s_mov_b32 s33, s60\n"
    "v_lshlrev_b32 v64, 16, v116\n"
    "v_and_b32 v65, 0xffff0000, v116\n"
    "v_lshlrev_b32 v66, 16, v117\n"
    "v_and_b32 v67, 0xffff0000, v117\n"
    "v_lshlrev_b32 v68, 16, v118\n"
    "v_and_b32 v69, 0xffff0000, v118\n"
    "v_lshlrev_b32 v70, 16, v119\n"
    "v_and_b32 v71, 0xffff0000, v119\n"
    "v_mfma_f32_16x16x32_bf16 v[64:67], v[24:27], v[100:103], v[64:67]\n"
    "s_nop 0\n"
    "v_mfma_f32_16x16x32_bf16 v[68:71], v[28:31], v[100:103], v[68:71]\n"
    "v_mfma_f32_16x16x32_bf16 v[64:67], v[24:27], v[104:107], v[64:67]\n"
    "v_mfma_f32_16x16x32_bf16 v[68:71], v[28:31], v[104:107], v[68:71]\n"
    "v_mfma_f32_16x16x32_bf16 v[64:67], v[24:27], v[108:111], v[64:67]\n"
    "v_mfma_f32_16x16x32_bf16 v[68:71], v[28:31], v[108:111], v[68:71]\n"
    "v_max3_u32 v48, v103, v107, v111\n"
    "v_mfma_f32_16x16x32_bf16 v[64:67], v[24:27], v[112:115], v[64:67]\n"
    "v_mfma_f32_16x16x32_bf16 v[68:71], v[28:31], v[112:115], v[68:71]\n"
    "v_max_u32 v48, v48, v115\n"
    "v_cmp_eq_u32 vcc, -1, v48\n"
    "s_mov_b32 s47, s50\n"
    "s_mov_b32 s48, s51\n"
    "s_mov_b32 s50, -1\n"
    "s_cbranch_vccnz LEARLY%=\n"
    "s_branch LAD%=\n"
    "LNONE%=:\n"
    "s_cmp_ge_i32 s47, 0\n"
    "s_cbranch_scc1 LJOB%=\n"
    "s_branch LIDLE%=\n"
    "LEARLY%=:\n"
    "s_mov_b32 s41, s47\n"
    "s_mov_b32 s53, 1\n"
    "s_mov_b32 s54, 0\n"
    "s_mov_b32 s47, -1\n"
    "s_and_b32 s38, s37, 3\n"
    "s_cmp_eq_u32 s38, 3\n"
    "s_cbranch_scc1 LIDLE%=\n"
    "s_sleep 2\n"
    "s_add_u32 s37, s37, 1\n"
    "s_cmp_lt_u32 s37, 0x80000\n"
    "s_cbranch_scc1 LPICK%=\n"
    "s_branch LFAIL%=\n"
    "LDEC%=:\n"
    "s_mov_b32 s70, s40\n"
    "s_cmp_lt_u32 s40, 64\n"
    "s_cbranch_scc1 LDF%=\n"
    "s_add_u32 s40, s40, 1\n"
    "s_cmp_eq_u32 s52, s45\n"
    "s_mov_b32 s45, s52\n"
    "s_cbranch_scc0 LMLP%=\n"
    "s_cmp_eq_u32 s70, 65\n"
    "s_cbranch_scc0 LMLP%=\n"
    "s_branch LEND%=\n"
    "LDF%=:\n"
    "s_add_u32 s40, s40, 1\n"
    "s_mov_b32 s37, 0\n"
    "s_lshr_b32 s38, s52, 16\n"
    "s_cmp_gt_i32 s38, s46\n"
    "s_cselect_b32 s70, s70, -1\n"
    "s_cselect_b32 s46, s38, s46\n"
    "s_cmp_lt_i32 s70, 0\n"
    "s_cbranch_scc1 LPICK%=\n"
    "s_mov_b32 s43, s70\n"
    "s_andn2_b32 s44, 0x7ff, s52\n"
    "LMLP%=:\n"
    "s_andn2_b32 s71, 0x7ff, s52\n"
    "s_lshr_b32 s38, s71, 5\n"
    "s_cmp_ge_u32 s38, s43\n"
    "s_cselect_b32 s63, s56, s57\n"
    "s_cselect_b32 s38, s70, 0\n"
    "s_cmp_ge_u32 s38, 64\n"
    "s_cselect_b32 s63, s57, s63\n"
    "s_cselect_b32 s62, 8388608, 0\n"
    "s_add_u32 s62, s62, 245366784\n"
    "s_lshl_b32 s39, s71, 11\n"
    "s_andn2_b32 s61, s39, 0x1fff\n"
    "s_add_u32 s61, s61, s39\n"
    "s_add_u32 s61, s61, s62\n"
    "s_lshr_b32 s38, s61, 8\n"
    "s_xor_b32 s38, s38, s63\n"
    "s_and_b32 s38, s38, 0x2000\n"
    "s_or_b32 s61, s61, s38\n"
    "buffer_load_dwordx4 v[20:23], v0, s[16:19], s61 offen offset:1024 sc1\n"
    "s_lshl_b32 s39, s70, 12\n"
    "s_andn2_b32 s61, s39, 0x1fff\n"
    "s_add_u32 s61, s61, s39\n"
    "s_add_u32 s61, s61, 263323648\n"
    "s_lshr_b32 s38, s61, 8\n"
    "s_xor_b32 s38, s38, s57\n"
    "s_and_b32 s38, s38, 0x2000\n"
    "s_or_b32 s61, s61, s38\n"
    "v_mov_b64 v[48:49], 0\n"
    "v_mov_b64 v[50:51], 0\n"
    "v_mov_b64 v[52:53], 0\n"
    "v_mov_b64 v[54:55], 0\n"
    "v_mov_b64 v[56:57], 0\n"
    "v_mov_b64 v[58:59], 0\n"
    "v_mov_b64 v[60:61], 0\n"
    "v_mov_b64 v[62:63], 0\n"
    "v_mov_b64 v[120:121], 0\n"
    "v_mov_b64 v[122:123], 0\n"
    "v_mov_b64 v[124:125], 0\n"
    "v_mov_b64 v[126:127], 0\n"
    "s_mov_b32 s50, -1\n"
    "s_cmp_lt_i32 s47, 0\n"
    "s_cselect_b32 s38, s53, 0\n"
    "s_cmp_lg_u32 s38, 0\n"
    "s_cbranch_scc1 LM1%=\n"
    "s_waitcnt vmcnt(0)\n"
    "s_branch LM2%=\n"
    "LM1%=:\n"
    "s_mov_b32 s50, s41\n"
    "s_add_u32 s41, s41, 1\n"
    "s_lshl_b32 s62, s50, 18\n"
    "s_add_u32 s62, s62, 180355072\n"
    "s_lshl_b32 s63, s50, 5\n"
    "s_add_u32 s63, s63, s55\n"
    "s_lshl_b32 s63, s63, 10\n"
    "s_lshr_b32 s38, s62, 8\n"
    "s_xor_b32 s38, s38, s56\n"
    "s_and_b32 s38, s38, 0x2000\n"
    "s_or_b32 s62, s62, s38\n"
    "buffer_load_dwordx4 v[116:119], v0, s[20:23], s63 offen\n"
    "buffer_load_dwordx4 v[100:103], v1, s[16:19], s62 offen sc1\n"
    "s_add_u32 s62, s62, 0x10000\n"
    "buffer_load_dwordx4 v[104:107], v1, s[16:19], s62 offen sc1\n"
    "s_add_u32 s62, s62, 0x10000\n"
    "buffer_load_dwordx4 v[108:111], v1, s[16:19], s62 offen sc1\n"
    "s_add_u32 s62, s62, 0x10000\n"
    "buffer_load_dwordx4 v[112:115], v1, s[16:19], s62 offen sc1\n"
    "s_waitcnt vmcnt(5)\n"
    "LM2%=:\n"
    "ds_write_b128 v8, v[20:23]\n"
    "ds_read_b128 v[72:75], v7 offset:0\n"
    "ds_read_b128 v[76:79], v7 offset:128\n"
    "ds_read_b128 v[80:83], v7 offset:256\n"
    "ds_read_b128 v[84:87], v7 offset:384\n"
    "ds_read_b128 v[88:91], v7 offset:512\n"
    "ds_read_b128 v[92:95], v7 offset:640\n"
    "ds_read_b128 v[96:99], v7 offset:768\n"
    "ds_read_b128 v[20:23], v7 offset:896\n"
    "s_waitcnt lgkmcnt(7)\n"
    "v_smfmac_f32_16x16x64_bf16 v[48:51], v[72:75], a[0:7], v6\n"
    "v_smfmac_f32_16x16x64_bf16 v[52:55], v[72:75], a[8:15], v6\n"
    "v_smfmac_f32_16x16x64_bf16 v[56:59], v[72:75], a[16:23], v6\n"
    "v_smfmac_f32_16x16x64_bf16 v[60:63], v[72:75], a[24:31], v6\n"
    "v_smfmac_f32_16x16x64_bf16 v[120:123], v[72:75], v[128:135], v6\n"
    "v_smfmac_f32_16x16x64_bf16 v[124:127], v[72:75], v[136:143], v6\n"
    "s_waitcnt lgkmcnt(6)\n"
    "v_smfmac_f32_16x16x64_bf16 v[48:51], v[76:79], a[32:39], v6\n"
    "v_smfmac_f32_16x16x64_bf16 v[52:55], v[76:79], a[40:47], v6\n"
    "v_smfmac_f32_16x16x64_bf16 v[56:59], v[76:79], a[48:55], v6\n"
    "v_smfmac_f32_16x16x64_bf16 v[60:63], v[76:79], a[56:63], v6\n"
    "v_smfmac_f32_16x16x64_bf16 v[120:123], v[76:79], v[144:151], v6\n"
    "v_smfmac_f32_16x16x64_bf16 v[124:127], v[76:79], v[152:159], v6\n"
    "s_waitcnt lgkmcnt(5)\n"
    "v_smfmac_f32_16x16x64_bf16 v[48:51], v[80:83], a[64:71], v6\n"
    "v_smfmac_f32_16x16x64_bf16 v[52:55], v[80:83], a[72:79], v6\n"
    "v_smfmac_f32_16x16x64_bf16 v[56:59], v[80:83], a[80:87], v6\n"
    "v_smfmac_f32_16x16x64_bf16 v[60:63], v[80:83], a[88:95], v6\n"
    "v_smfmac_f32_16x16x64_bf16 v[120:123], v[80:83], v[160:167], v6\n"
    "v_smfmac_f32_16x16x64_bf16 v[124:127], v[80:83], v[168:175], v6\n"
    "s_waitcnt lgkmcnt(4)\n"
    "v_smfmac_f32_16x16x64_bf16 v[48:51], v[84:87], a[96:103], v6\n"
    "v_smfmac_f32_16x16x64_bf16 v[52:55], v[84:87], a[104:111], v6\n"
    "v_smfmac_f32_16x16x64_bf16 v[56:59], v[84:87], a[112:119], v6\n"
    "v_smfmac_f32_16x16x64_bf16 v[60:63], v[84:87], a[120:127], v6\n"
    "v_smfmac_f32_16x16x64_bf16 v[120:123], v[84:87], v[176:183], v6\n"
    "v_smfmac_f32_16x16x64_bf16 v[124:127], v[84:87], v[184:191], v6\n"
    "v_mad_u32_u24 v41, v3, s41, v2\n"
    "v_mad_u32_u24 v41, v5, s40, v41\n"
    "buffer_load_dword v40, v41, s[16:19], 0 offen sc1\n"
    "s_waitcnt lgkmcnt(3)\n"
    "v_smfmac_f32_16x16x64_bf16 v[48:51], v[88:91], a[128:135], v6\n"
    "v_smfmac_f32_16x16x64_bf16 v[52:55], v[88:91], a[136:143], v6\n"
    "v_smfmac_f32_16x16x64_bf16 v[56:59], v[88:91], a[144:151], v6\n"
    "v_smfmac_f32_16x16x64_bf16 v[60:63], v[88:91], a[152:159], v6\n"
    "v_smfmac_f32_16x16x64_bf16 v[120:123], v[88:91], v[192:199], v6\n"
    "v_smfmac_f32_16x16x64_bf16 v[124:127], v[88:91], v[200:207], v6\n"
    "s_waitcnt lgkmcnt(2)\n"
    "v_smfmac_f32_16x16x64_bf16 v[48:51], v[92:95], a[160:167], v6\n"
    "v_smfmac_f32_16x16x64_bf16 v[52:55], v[92:95], a[168:175], v6\n"
    "v_smfmac_f32_16x16x64_bf16 v[56:59], v[92:95], a[176:183], v6\n"
    "v_smfmac_f32_16x16x64_bf16 v[60:63], v[92:95], a[184:191], v6\n"
    "v_smfmac_f32_16x16x64_bf16 v[120:123], v[92:95], v[208:215], v6\n"
    "v_smfmac_f32_16x16x64_bf16 v[124:127], v[92:95], v[216:223], v6\n"
    "s_waitcnt lgkmcnt(1)\n"
    "v_smfmac_f32_16x16x64_bf16 v[48:51], v[96:99], a[192:199], v6\n"
    "v_smfmac_f32_16x16x64_bf16 v[52:55], v[96:99], a[200:207], v6\n"
    "v_smfmac_f32_16x16x64_bf16 v[56:59], v[96:99], a[208:215], v6\n"
    "v_smfmac_f32_16x16x64_bf16 v[60:63], v[96:99], a[216:223], v6\n"
    "v_smfmac_f32_16x16x64_bf16 v[120:123], v[96:99], v[224:231], v6\n"
    "v_smfmac_f32_16x16x64_bf16 v[124:127], v[96:99], v[232:239], v6\n"
    "s_waitcnt lgkmcnt(0)\n"
    "v_smfmac_f32_16x16x64_bf16 v[48:51], v[20:23], a[224:231], v6\n"
    "v_smfmac_f32_16x16x64_bf16 v[52:55], v[20:23], a[232:239], v6\n"
    "v_smfmac_f32_16x16x64_bf16 v[56:59], v[20:23], a[240:247], v6\n"
    "v_smfmac_f32_16x16x64_bf16 v[60:63], v[20:23], a[248:255], v6\n"
    "v_smfmac_f32_16x16x64_bf16 v[120:123], v[20:23], v[240:247], v6\n"
    "v_smfmac_f32_16x16x64_bf16 v[124:127], v[20:23], v[248:255], v6\n"
    "v_add_f32 v48, v48, v50\n"
    "v_add_f32 v52, v52, v54\n"
    "v_add_f32 v56, v56, v58\n"
    "v_add_f32 v60, v60, v62\n"
    "v_cndmask_b32 v48, v48, v56, s[74:75]\n"
    "v_cndmask_b32 v52, v52, v60, s[74:75]\n"
    "s_mov_b32 vcc_lo, -1\n"
    "s_mov_b32 vcc_hi, 0\n"
    "v_add_f32 v120, v120, v122\n"
    "v_add_f32 v124, v124, v126\n"
    "v_cndmask_b32 v120, v120, v48, vcc\n"
    "v_cndmask_b32 v124, v124, v52, vcc\n"
    "v_cvt_pk_bf16_f32 v124, v120, v124\n"
    "v_lshlrev_b32 v120, 16, v124\n"
    "v_mul_f32 v121, 0xbfb8aa3b, v120\n"
    "v_exp_f32 v121, v121\n"
    "v_and_b32 v124, 0xffff0000, v124\n"
    "v_add_f32 v121, 1.0, v121\n"
    "v_rcp_f32 v121, v121\n"
    "s_nop 0\n"
    "v_mul_f32 v120, v120, v121\n"
    "v_cvt_pk_bf16_f32 v120, v120, v120\n"
    "v_lshlrev_b32 v120, 16, v120\n"
    "v_mul_f32 v120, v120, v124\n"
    "s_nop 1\n"
    "v_mov_b32_dpp v121, v120 quad_perm:[1,1,3,3] row_mask:0xf bank_mask:0xf\n"
    "v_cvt_pk_bf16_f32 v120, v120, v121\n"
    "v_min_u32 v120, -2, v120\n"
    "buffer_store_dword v120, v9, s[16:19], s61 offen sc1\n"
    "s_cmp_eq_u32 s70, 65\n"
    "s_cbranch_scc1 LEND%=\n"
    "s_waitcnt vmcnt(1)\n"
    "s_cmp_lt_i32 s50, 0\n"
    "s_cbranch_scc1 LTOP%=\n"
    "s_lshl_b32 s60, s50, 5\n"
    "s_add_u32 s60, s60, s55\n"
    "s_lshl_b32 s39, s60, 11\n"
    "s_andn2_b32 s32, s39, 0x1fff\n"
    "s_add_u32 s32, s32, s39\n"
    "s_add_u32 s32, s32, 245366784\n"
    "s_lshr_b32 s38, s32, 8\n"
    "s_xor_b32 s38, s38, s56\n"
    "s_and_b32 s38, s38, 0x2000\n"
    "s_or_b32 s32, s32, s38\n"
    "s_lshl_b32 s39, s60, 10\n"
    "s_andn2_b32 s78, s39, 0x1fff\n"
    "s_add_u32 s78, s78, s39\n"
    "s_add_u32 s78, s78, 236978176\n"
    "s_lshr_b32 s38, s78, 8\n"
    "s_xor_b32 s38, s38, s56\n"
    "s_and_b32 s38, s38, 0x2000\n"
    "s_or_b32 s78, s78, s38\n"
    "s_lshl_b32 s39, s60, 10\n"
    "s_andn2_b32 s34, s39, 0x1fff\n"
    "s_add_u32 s34, s34, s39\n"
    "s_add_u32 s34, s34, 237240320\n"
    "s_lshr_b32 s38, s34, 8\n"
    "s_xor_b32 s38, s38, s56\n"
    "s_and_b32 s38, s38, 0x2000\n"
    "s_or_b32 s34, s34, s38\n"
    "s_cmp_lt_u32 s50, 60\n"
    "s_cselect_b32 s34, s34, 0x80000000\n"
    "s_mov_b32 s33, s60\n"
    "v_lshlrev_b32 v64, 16, v116\n"
    "v_and_b32 v65, 0xffff0000, v116\n"
    "v_lshlrev_b32 v66, 16, v117\n"
    "v_and_b32 v67, 0xffff0000, v117\n"
    "v_lshlrev_b32 v68, 16, v118\n"
    "v_and_b32 v69, 0xffff0000, v118\n"
    "v_lshlrev_b32 v70, 16, v119\n"
    "v_and_b32 v71, 0xffff0000, v119\n"
    "v_mfma_f32_16x16x32_bf16 v[64:67], v[24:27], v[100:103], v[64:67]\n"
    "s_nop 0\n"
    "v_mfma_f32_16x16x32_bf16 v[68:71], v[28:31], v[100:103], v[68:71]\n"
    "v_mfma_f32_16x16x32_bf16 v[64:67], v[24:27], v[104:107], v[64:67]\n"
    "v_mfma_f32_16x16x32_bf16 v[68:71], v[28:31], v[104:107], v[68:71]\n"
    "v_mfma_f32_16x16x32_bf16 v[64:67], v[24:27], v[108:111], v[64:67]\n"
    "v_mfma_f32_16x16x32_bf16 v[68:71], v[28:31], v[108:111], v[68:71]\n"
    "v_max3_u32 v48, v103, v107, v111\n"
    "v_mfma_f32_16x16x32_bf16 v[64:67], v[24:27], v[112:115], v[64:67]\n"
    "v_mfma_f32_16x16x32_bf16 v[68:71], v[28:31], v[112:115], v[68:71]\n"
    "v_max_u32 v48, v48, v115\n"
    "v_cmp_eq_u32 vcc, -1, v48\n"
    "s_mov_b32 s47, s50\n"
    "s_mov_b32 s48, s51\n"
    "s_mov_b32 s50, -1\n"
    "s_cbranch_vccnz LEARLY%=\n"
    "s_branch LTOP%=\n"
    "LFAIL%=:\n"
    "buffer_store_dword v6, off, s[16:19], s59 sc1\n"
    "LEND%=:\n"
    "s_endpgm\n"
    :
    : "{v0}"(hv0), "{v1}"(hv1), "{v2}"(hv2), "{v3}"(hv3), "{v4}"(hv4), "{v5}"(hv5), "{v6}"(hv6), "{v7}"(hv7), "{v8}"(hv8), "{v9}"(hv9), "{v10}"(hv10), "{v11}"(hv11), "{v12}"(hv12), "{v13}"(hv13), "{v14}"(hv14), "{v15}"(hv15), "{v16}"(hv16), "{v17}"(hv17), "{v18}"(hv18), "{v19}"(hv19), "{v20}"(hv20), "{v21}"(hv21)
    : "v255", "a255", "s101", "vcc", "scc", "memory");
  } else {
  asm volatile(
    "s_nop 1\n"
    "v_readlane_b32 s36, v15, 0\n"
    "v_readlane_b32 s55, v15, 1\n"
    "v_readlane_b32 s58, v15, 2\n"
    "v_readlane_b32 s59, v15, 3\n"
    "v_readlane_b32 s16, v15, 4\n"
    "v_readlane_b32 s17, v15, 5\n"
    "v_readlane_b32 s18, v15, 6\n"
    "v_readlane_b32 s20, v15, 7\n"
    "v_readlane_b32 s21, v15, 8\n"
    "v_readlane_b32 s24, v15, 9\n"
    "v_readlane_b32 s25, v15, 10\n"
    "v_readlane_b32 s90, v15, 11\n"
    "v_readlane_b32 s91, v15, 12\n"
    "v_readlane_b32 s92, v15, 13\n"
    "v_readlane_b32 s93, v15, 14\n"
    "v_readlane_b32 s94, v15, 15\n"
    "v_readlane_b32 s95, v15, 16\n"
    "v_readlane_b32 s96, v15, 17\n"
    "v_readlane_b32 s97, v15, 18\n"
    "v_readlane_b32 s98, v15, 19\n"
    "v_readlane_b32 s99, v15, 20\n"
    "v_readlane_b32 s72, v15, 21\n"
    "s_mov_b32 s19, 0x20000\n"
    "s_mov_b32 s23, 0x20000\n"
    "s_mov_b32 s27, 0x20000\n"
    "s_mov_b32 s31, 0x20000\n"
    "s_mov_b32 s22, 0x200000\n"
    "s_mov_b32 s26, 0x200000\n"
    "s_mov_b32 s30, 1835008\n"
    "s_nop 3\n"
    "global_load_dwordx4 v[116:119], v0, s[98:99]\n"
    "s_lshl_b32 s62, s36, 11\n"
    "s_mov_b32 s28, s90\n"
    "s_mov_b32 s29, s91\n"
    "s_add_u32 m0, s62, 0\n"
    "s_mov_b32 s38, 0\n"
    "buffer_load_dwordx4 v16, s[28:31], s38 offen lds\n"
    "s_add_u32 m0, s62, 1024\n"
    "s_mov_b32 s38, 28672\n"
    "buffer_load_dwordx4 v16, s[28:31], s38 offen lds\n"
    "s_add_u32 m0, s62, 8192\n"
    "s_mov_b32 s38, 229376\n"
    "buffer_load_dwordx4 v16, s[28:31], s38 offen lds\n"
    "s_add_u32 m0, s62, 9216\n"
    "s_mov_b32 s38, 258048\n"
    "buffer_load_dwordx4 v16, s[28:31], s38 offen lds\n"
    "s_mov_b32 s28, s90\n"
    "s_mov_b32 s29, s91\n"
    "s_add_u32 m0, s62, 16384\n"
    "s_mov_b32 s38, 458752\n"
    "buffer_load_dwordx4 v16, s[28:31], s38 offen lds\n"
    "s_add_u32 m0, s62, 17408\n"
    "s_mov_b32 s38, 487424\n"
    "buffer_load_dwordx4 v16, s[28:31], s38 offen lds\n"
    "s_add_u32 m0, s62, 24576\n"
    "s_mov_b32 s38, 688128\n"
    "buffer_load_dwordx4 v16, s[28:31], s38 offen lds\n"
    "s_add_u32 m0, s62, 25600\n"
    "s_mov_b32 s38, 716800\n"
    "buffer_load_dwordx4 v16, s[28:31], s38 offen lds\n"
    "s_mov_b32 s28, s90\n"
    "s_mov_b32 s29, s91\n"
    "s_add_u32 m0, s62, 32768\n"
    "s_mov_b32 s38, 917504\n"
    "buffer_load_dwordx4 v16, s[28:31], s38 offen lds\n"
    "s_add_u32 m0, s62, 33792\n"
    "s_mov_b32 s38, 946176\n"
    "buffer_load_dwordx4 v16, s[28:31], s38 offen lds\n"
    "s_add_u32 m0, s62, 40960\n"
    "s_mov_b32 s38, 1146880\n"
    "buffer_load_dwordx4 v16, s[28:31], s38 offen lds\n"
    "s_add_u32 m0, s62, 41984\n"
    "s_mov_b32 s38, 1175552\n"
    "buffer_load_dwordx4 v16, s[28:31], s38 offen lds\n"
    "s_mov_b32 s28, s90\n"
    "s_mov_b32 s29, s91\n"
    "s_add_u32 m0, s62, 49152\n"
    "s_mov_b32 s38, 1376256\n"
    "buffer_load_dwordx4 v16, s[28:31], s38 offen lds\n"
    "s_add_u32 m0, s62, 50176\n"
    "s_mov_b32 s38, 1404928\n"
    "buffer_load_dwordx4 v16, s[28:31], s38 offen lds\n"
    "s_add_u32 m0, s62, 57344\n"
    "s_mov_b32 s38, 1605632\n"
    "buffer_load_dwordx4 v16, s[28:31], s38 offen lds\n"
    "s_add_u32 m0, s62, 58368\n"
    "s_mov_b32 s38, 1634304\n"
    "buffer_load_dwordx4 v16, s[28:31], s38 offen lds\n"
    "s_mov_b32 s28, s92\n"
    "s_mov_b32 s29, s93\n"
    "s_add_u32 m0, s62, 65536\n"
    "s_mov_b32 s38, 0\n"
    "buffer_load_dwordx4 v16, s[28:31], s38 offen lds\n"
    "s_add_u32 m0, s62, 66560\n"
    "s_mov_b32 s38, 28672\n"
    "buffer_load_dwordx4 v16, s[28:31], s38 offen lds\n"
    "s_add_u32 m0, s62, 73728\n"
    "s_mov_b32 s38, 229376\n"
    "buffer_load_dwordx4 v16, s[28:31], s38 offen lds\n"
    "s_add_u32 m0, s62, 74752\n"
    "s_mov_b32 s38, 258048\n"
    "buffer_load_dwordx4 v16, s[28:31], s38 offen lds\n"
    "s_mov_b32 s28, s92\n"
    "s_mov_b32 s29, s93\n"
    "s_add_u32 m0, s62, 81920\n"
    "s_mov_b32 s38, 458752\n"
    "buffer_load_dwordx4 v16, s[28:31], s38 offen lds\n"
    "s_add_u32 m0, s62, 82944\n"
    "s_mov_b32 s38, 487424\n"
    "buffer_load_dwordx4 v16, s[28:31], s38 offen lds\n"
    "s_add_u32 m0, s62, 90112\n"
    "s_mov_b32 s38, 688128\n"
    "buffer_load_dwordx4 v16, s[28:31], s38 offen lds\n"
    "s_add_u32 m0, s62, 91136\n"
    "s_mov_b32 s38, 716800\n"
    "buffer_load_dwordx4 v16, s[28:31], s38 offen lds\n"
    "s_mov_b32 s28, s92\n"
    "s_mov_b32 s29, s93\n"
    "s_add_u32 m0, s62, 98304\n"
    "s_mov_b32 s38, 917504\n"
    "buffer_load_dwordx4 v16, s[28:31], s38 offen lds\n"
    "s_add_u32 m0, s62, 99328\n"
    "s_mov_b32 s38, 946176\n"
    "buffer_load_dwordx4 v16, s[28:31], s38 offen lds\n"
    "s_add_u32 m0, s62, 106496\n"
    "s_mov_b32 s38, 1146880\n"
    "buffer_load_dwordx4 v16, s[28:31], s38 offen lds\n"
    "s_add_u32 m0, s62, 107520\n"
    "s_mov_b32 s38, 1175552\n"
    "buffer_load_dwordx4 v16, s[28:31], s38 offen lds\n"
    "s_waitcnt vmcnt(24)\n"
    "s_barrier\n"
    "s_mov_b32 s28, s92\n"
    "s_mov_b32 s29, s93\n"
    "s_add_u32 m0, s62, 114688\n"
    "s_mov_b32 s38, 1376256\n"
    "buffer_load_dwordx4 v16, s[28:31], s38 offen lds\n"
    "s_add_u32 m0, s62, 115712\n"
    "s_mov_b32 s38, 1404928\n"
    "buffer_load_dwordx4 v16, s[28:31], s38 offen lds\n"
    "s_add_u32 m0, s62, 122880\n"
    "s_mov_b32 s38, 1605632\n"
    "buffer_load_dwordx4 v16, s[28:31], s38 offen lds\n"
    "s_add_u32 m0, s62, 123904\n"
    "s_mov_b32 s38, 1634304\n"
    "buffer_load_dwordx4 v16, s[28:31], s38 offen lds\n"
    "v_add_u32 v48, 0, v14\n"
    "ds_read_b64_tr_b16 v[128:129], v48 offset:0\n"
    "ds_read_b64_tr_b16 v[130:131], v48 offset:256\n"
    "ds_read_b64_tr_b16 v[132:133], v48 offset:4096\n"
    "ds_read_b64_tr_b16 v[134:135], v48 offset:4352\n"
    "ds_read_b64_tr_b16 v[144:145], v48 offset:8192\n"
    "ds_read_b64_tr_b16 v[146:147], v48 offset:8448\n"
    "ds_read_b64_tr_b16 v[148:149], v48 offset:12288\n"
    "ds_read_b64_tr_b16 v[150:151], v48 offset:12544\n"
    "s_waitcnt lgkmcnt(0)\n"
    "s_waitcnt vmcnt(24)\n"
    "s_barrier\n"
    "s_mov_b32 s28, s94\n"
    "s_mov_b32 s29, s95\n"
    "s_add_u32 m0, s62, 0\n"
    "s_mov_b32 s38, 0\n"
    "buffer_load_dwordx4 v17, s[28:31], s38 offen lds\n"
    "s_add_u32 m0, s62, 1024\n"
    "s_mov_b32 s38, 8192\n"
    "buffer_load_dwordx4 v17, s[28:31], s38 offen lds\n"
    "s_add_u32 m0, s62, 8192\n"
    "s_mov_b32 s38, 65536\n"
    "buffer_load_dwordx4 v17, s[28:31], s38 offen lds\n"
    "s_add_u32 m0, s62, 9216\n"
    "s_mov_b32 s38, 73728\n"
    "buffer_load_dwordx4 v17, s[28:31], s38 offen lds\n"
    "v_add_u32 v48, 16384, v14\n"
    "ds_read_b64_tr_b16 v[160:161], v48 offset:0\n"
    "ds_read_b64_tr_b16 v[162:163], v48 offset:256\n"
    "ds_read_b64_tr_b16 v[164:165], v48 offset:4096\n"
    "ds_read_b64_tr_b16 v[166:167], v48 offset:4352\n"
    "ds_read_b64_tr_b16 v[176:177], v48 offset:8192\n"
    "ds_read_b64_tr_b16 v[178:179], v48 offset:8448\n"
    "ds_read_b64_tr_b16 v[180:181], v48 offset:12288\n"
    "ds_read_b64_tr_b16 v[182:183], v48 offset:12544\n"
    "s_waitcnt lgkmcnt(0)\n"
    "s_waitcnt vmcnt(24)\n"
    "s_barrier\n"
    "s_mov_b32 s28, s94\n"
    "s_mov_b32 s29, s95\n"
    "s_add_u32 m0, s62, 16384\n"
    "s_mov_b32 s38, 131072\n"
    "buffer_load_dwordx4 v17, s[28:31], s38 offen lds\n"
    "s_add_u32 m0, s62, 17408\n"
    "s_mov_b32 s38, 139264\n"
    "buffer_load_dwordx4 v17, s[28:31], s38 offen lds\n"
    "s_add_u32 m0, s62, 24576\n"
    "s_mov_b32 s38, 196608\n"
    "buffer_load_dwordx4 v17, s[28:31], s38 offen lds\n"
    "s_add_u32 m0, s62, 25600\n"
    "s_mov_b32 s38, 204800\n"
    "buffer_load_dwordx4 v17, s[28:31], s38 offen lds\n"
    "v_add_u32 v48, 32768, v14\n"
    "ds_read_b64_tr_b16 v[192:193], v48 offset:0\n"
    "ds_read_b64_tr_b16 v[194:195], v48 offset:256\n"
    "ds_read_b64_tr_b16 v[196:197], v48 offset:4096\n"
    "ds_read_b64_tr_b16 v[198:199], v48 offset:4352\n"
    "ds_read_b64_tr_b16 v[208:209], v48 offset:8192\n"
    "ds_read_b64_tr_b16 v[210:211], v48 offset:8448\n"
    "ds_read_b64_tr_b16 v[212:213], v48 offset:12288\n"
    "ds_read_b64_tr_b16 v[214:215], v48 offset:12544\n"
    "s_waitcnt lgkmcnt(0)\n"
    "s_waitcnt vmcnt(24)\n"
    "s_barrier\n"
    "s_mov_b32 s28, s94\n"
    "s_mov_b32 s29, s95\n"
    "s_add_u32 m0, s62, 32768\n"
    "s_mov_b32 s38, 262144\n"
    "buffer_load_dwordx4 v17, s[28:31], s38 offen lds\n"
    "s_add_u32 m0, s62, 33792\n"
    "s_mov_b32 s38, 270336\n"
    "buffer_load_dwordx4 v17, s[28:31], s38 offen lds\n"
    "s_add_u32 m0, s62, 40960\n"
    "s_mov_b32 s38, 327680\n"
    "buffer_load_dwordx4 v17, s[28:31], s38 offen lds\n"
    "s_add_u32 m0, s62, 41984\n"
    "s_mov_b32 s38, 335872\n"
    "buffer_load_dwordx4 v17, s[28:31], s38 offen lds\n"
    "v_add_u32 v48, 49152, v14\n"
    "ds_read_b64_tr_b16 v[224:225], v48 offset:0\n"
    "ds_read_b64_tr_b16 v[226:227], v48 offset:256\n"
    "ds_read_b64_tr_b16 v[228:229], v48 offset:4096\n"
    "ds_read_b64_tr_b16 v[230:231], v48 offset:4352\n"
    "ds_read_b64_tr_b16 v[240:241], v48 offset:8192\n"
    "ds_read_b64_tr_b16 v[242:243], v48 offset:8448\n"
    "ds_read_b64_tr_b16 v[244:245], v48 offset:12288\n"
    "ds_read_b64_tr_b16 v[246:247], v48 offset:12544\n"
    "s_waitcnt lgkmcnt(0)\n"
    "s_waitcnt vmcnt(24)\n"
    "s_barrier\n"
    "s_mov_b32 s28, s94\n"
    "s_mov_b32 s29, s95\n"
    "s_add_u32 m0, s62, 49152\n"
    "s_mov_b32 s38, 393216\n"
    "buffer_load_dwordx4 v17, s[28:31], s38 offen lds\n"
    "s_add_u32 m0, s62, 50176\n"
    "s_mov_b32 s38, 401408\n"
    "buffer_load_dwordx4 v17, s[28:31], s38 offen lds\n"
    "s_add_u32 m0, s62, 57344\n"
    "s_mov_b32 s38, 458752\n"
    "buffer_load_dwordx4 v17, s[28:31], s38 offen lds\n"
    "s_add_u32 m0, s62, 58368\n"
    "s_mov_b32 s38, 466944\n"
    "buffer_load_dwordx4 v17, s[28:31], s38 offen lds\n"
    "v_add_u32 v48, 65536, v14\n"
    "ds_read_b64_tr_b16 v[136:137], v48 offset:0\n"
    "ds_read_b64_tr_b16 v[138:139], v48 offset:256\n"
    "ds_read_b64_tr_b16 v[140:141], v48 offset:4096\n"
    "ds_read_b64_tr_b16 v[142:143], v48 offset:4352\n"
    "ds_read_b64_tr_b16 v[152:153], v48 offset:8192\n"
    "ds_read_b64_tr_b16 v[154:155], v48 offset:8448\n"
    "ds_read_b64_tr_b16 v[156:157], v48 offset:12288\n"
    "ds_read_b64_tr_b16 v[158:159], v48 offset:12544\n"
    "s_waitcnt lgkmcnt(0)\n"
    "s_waitcnt vmcnt(24)\n"
    "s_barrier\n"
    "s_mov_b32 s28, s94\n"
    "s_mov_b32 s29, s95\n"
    "s_add_u32 m0, s62, 65536\n"
    "s_mov_b32 s38, 524288\n"
    "buffer_load_dwordx4 v17, s[28:31], s38 offen lds\n"
    "s_add_u32 m0, s62, 66560\n"
    "s_mov_b32 s38, 532480\n"
    "buffer_load_dwordx4 v17, s[28:31], s38 offen lds\n"
    "s_add_u32 m0, s62, 73728\n"
    "s_mov_b32 s38, 589824\n"
    "buffer_load_dwordx4 v17, s[28:31], s38 offen lds\n"
    "s_add_u32 m0, s62, 74752\n"
    "s_mov_b32 s38, 598016\n"
    "buffer_load_dwordx4 v17, s[28:31], s38 offen lds\n"
    "v_add_u32 v48, 81920, v14\n"
    "ds_read_b64_tr_b16 v[168:169], v48 offset:0\n"
    "ds_read_b64_tr_b16 v[170:171], v48 offset:256\n"
    "ds_read_b64_tr_b16 v[172:173], v48 offset:4096\n"
    "ds_read_b64_tr_b16 v[174:175], v48 offset:4352\n"
    "ds_read_b64_tr_b16 v[184:185], v48 offset:8192\n"
    "ds_read_b64_tr_b16 v[186:187], v48 offset:8448\n"
    "ds_read_b64_tr_b16 v[188:189], v48 offset:12288\n"
    "ds_read_b64_tr_b16 v[190:191], v48 offset:12544\n"
    "s_waitcnt lgkmcnt(0)\n"
    "s_waitcnt vmcnt(24)\n"
    "s_barrier\n"
    "s_mov_b32 s28, s94\n"
    "s_mov_b32 s29, s95\n"
    "s_add_u32 m0, s62, 81920\n"
    "s_mov_b32 s38, 655360\n"
    "buffer_load_dwordx4 v17, s[28:31], s38 offen lds\n"
    "s_add_u32 m0, s62, 82944\n"
    "s_mov_b32 s38, 663552\n"
    "buffer_load_dwordx4 v17, s[28:31], s38 offen lds\n"
    "s_add_u32 m0, s62, 90112\n"
    "s_mov_b32 s38, 720896\n"
    "buffer_load_dwordx4 v17, s[28:31], s38 offen lds\n"
    "s_add_u32 m0, s62, 91136\n"
    "s_mov_b32 s38, 729088\n"
    "buffer_load_dwordx4 v17, s[28:31], s38 offen lds\n"
    "v_add_u32 v48, 98304, v14\n"
    "ds_read_b64_tr_b16 v[200:201], v48 offset:0\n"
    "ds_read_b64_tr_b16 v[202:203], v48 offset:256\n"
    "ds_read_b64_tr_b16 v[204:205], v48 offset:4096\n"
    "ds_read_b64_tr_b16 v[206:207], v48 offset:4352\n"
    "ds_read_b64_tr_b16 v[216:217], v48 offset:8192\n"
    "ds_read_b64_tr_b16 v[218:219], v48 offset:8448\n"
    "ds_read_b64_tr_b16 v[220:221], v48 offset:12288\n"
    "ds_read_b64_tr_b16 v[222:223], v48 offset:12544\n"
    "s_waitcnt lgkmcnt(0)\n"
    "s_waitcnt vmcnt(24)\n"
    "s_barrier\n"
    "s_mov_b32 s28, s94\n"
    "s_mov_b32 s29, s95\n"
    "s_add_u32 m0, s62, 98304\n"
    "s_mov_b32 s38, 786432\n"
    "buffer_load_dwordx4 v17, s[28:31], s38 offen lds\n"
    "s_add_u32 m0, s62, 99328\n"
    "s_mov_b32 s38, 794624\n"
    "buffer_load_dwordx4 v17, s[28:31], s38 offen lds\n"
    "s_add_u32 m0, s62, 106496\n"
    "s_mov_b32 s38, 851968\n"
    "buffer_load_dwordx4 v17, s[28:31], s38 offen lds\n"
    "s_add_u32 m0, s62, 107520\n"
    "s_mov_b32 s38, 860160\n"
    "buffer_load_dwordx4 v17, s[28:31], s38 offen lds\n"
    "v_add_u32 v48, 114688, v14\n"
    "ds_read_b64_tr_b16 v[232:233], v48 offset:0\n"
    "ds_read_b64_tr_b16 v[234:235], v48 offset:256\n"
    "ds_read_b64_tr_b16 v[236:237], v48 offset:4096\n"
    "ds_read_b64_tr_b16 v[238:239], v48 offset:4352\n"
    "ds_read_b64_tr_b16 v[248:249], v48 offset:8192\n"
    "ds_read_b64_tr_b16 v[250:251], v48 offset:8448\n"
    "ds_read_b64_tr_b16 v[252:253], v48 offset:12288\n"
    "ds_read_b64_tr_b16 v[254:255], v48 offset:12544\n"
    "s_waitcnt lgkmcnt(0)\n"
    "s_waitcnt vmcnt(24)\n"
    "s_barrier\n"
    "s_mov_b32 s28, s94\n"
    "s_mov_b32 s29, s95\n"
    "s_add_u32 m0, s62, 114688\n"
    "s_mov_b32 s38, 917504\n"
    "buffer_load_dwordx4 v17, s[28:31], s38 offen lds\n"
    "s_add_u32 m0, s62, 115712\n"
    "s_mov_b32 s38, 925696\n"
    "buffer_load_dwordx4 v17, s[28:31], s38 offen lds\n"
    "s_add_u32 m0, s62, 122880\n"
    "s_mov_b32 s38, 983040\n"
    "buffer_load_dwordx4 v17, s[28:31], s38 offen lds\n"
    "s_add_u32 m0, s62, 123904\n"
    "s_mov_b32 s38, 991232\n"
    "buffer_load_dwordx4 v17, s[28:31], s38 offen lds\n"
    "v_add_u32 v48, 0, v14\n"
    "ds_read_b64_tr_b16 a[0:1], v48 offset:0\n"
    "ds_read_b64_tr_b16 a[2:3], v48 offset:256\n"
    "ds_read_b64_tr_b16 a[4:5], v48 offset:4096\n"
    "ds_read_b64_tr_b16 a[6:7], v48 offset:4352\n"
    "ds_read_b64_tr_b16 a[8:9], v48 offset:8192\n"
    "ds_read_b64_tr_b16 a[10:11], v48 offset:8448\n"
    "ds_read_b64_tr_b16 a[12:13], v48 offset:12288\n"
    "ds_read_b64_tr_b16 a[14:15], v48 offset:12544\n"
    "s_waitcnt lgkmcnt(0)\n"
    "s_waitcnt vmcnt(24)\n"
    "s_barrier\n"
    "s_mov_b32 s28, s94\n"
    "s_mov_b32 s29, s95\n"
    "s_add_u32 m0, s62, 0\n"
    "s_mov_b32 s38, 1048576\n"
    "buffer_load_dwordx4 v17, s[28:31], s38 offen lds\n"
    "s_add_u32 m0, s62, 1024\n"
    "s_mov_b32 s38, 1056768\n"
    "buffer_load_dwordx4 v17, s[28:31], s38 offen lds\n"
    "s_add_u32 m0, s62, 8192\n"
    "s_mov_b32 s38, 1114112\n"
    "buffer_load_dwordx4 v17, s[28:31], s38 offen lds\n"
    "s_add_u32 m0, s62, 9216\n"
    "s_mov_b32 s38, 1122304\n"
    "buffer_load_dwordx4 v17, s[28:31], s38 offen lds\n"
    "v_add_u32 v48, 16384, v14\n"
    "ds_read_b64_tr_b16 a[16:17], v48 offset:0\n"
    "ds_read_b64_tr_b16 a[18:19], v48 offset:256\n"
    "ds_read_b64_tr_b16 a[20:21], v48 offset:4096\n"
    "ds_read_b64_tr_b16 a[22:23], v48 offset:4352\n"
    "ds_read_b64_tr_b16 a[24:25], v48 offset:8192\n"
    "ds_read_b64_tr_b16 a[26:27], v48 offset:8448\n"
    "ds_read_b64_tr_b16 a[28:29], v48 offset:12288\n"
    "ds_read_b64_tr_b16 a[30:31], v48 offset:12544\n"
    "s_waitcnt lgkmcnt(0)\n"
    "s_waitcnt vmcnt(24)\n"
    "s_barrier\n"
    "s_mov_b32 s28, s94\n"
    "s_mov_b32 s29, s95\n"
    "s_add_u32 m0, s62, 16384\n"
    "s_mov_b32 s38, 1179648\n"
    "buffer_load_dwordx4 v17, s[28:31], s38 offen lds\n"
    "s_add_u32 m0, s62, 17408\n"
    "s_mov_b32 s38, 1187840\n"
    "buffer_load_dwordx4 v17, s[28:31], s38 offen lds\n"
    "s_add_u32 m0, s62, 24576\n"
    "s_mov_b32 s38, 1245184\n"
    "buffer_load_dwordx4 v17, s[28:31], s38 offen lds\n"
    "s_add_u32 m0, s62, 25600\n"
    "s_mov_b32 s38, 1253376\n"
    "buffer_load_dwordx4 v17, s[28:31], s38 offen lds\n"
    "v_add_u32 v48, 32768, v14\n"
    "ds_read_b64_tr_b16 a[32:33], v48 offset:0\n"
    "ds_read_b64_tr_b16 a[34:35], v48 offset:256\n"
    "ds_read_b64_tr_b16 a[36:37], v48 offset:4096\n"
    "ds_read_b64_tr_b16 a[38:39], v48 offset:4352\n"
    "ds_read_b64_tr_b16 a[40:41], v48 offset:8192\n"
    "ds_read_b64_tr_b16 a[42:43], v48 offset:8448\n"
    "ds_read_b64_tr_b16 a[44:45], v48 offset:12288\n"
    "ds_read_b64_tr_b16 a[46:47], v48 offset:12544\n"
    "s_waitcnt lgkmcnt(0)\n"
    "s_waitcnt vmcnt(24)\n"
    "s_barrier\n"
    "s_mov_b32 s28, s94\n"
    "s_mov_b32 s29, s95\n"
    "s_add_u32 m0, s62, 32768\n"
    "s_mov_b32 s38, 1310720\n"
    "buffer_load_dwordx4 v17, s[28:31], s38 offen lds\n"
    "s_add_u32 m0, s62, 33792\n"
    "s_mov_b32 s38, 1318912\n"
    "buffer_load_dwordx4 v17, s[28:31], s38 offen lds\n"
    "s_add_u32 m0, s62, 40960\n"
    "s_mov_b32 s38, 1376256\n"
    "buffer_load_dwordx4 v17, s[28:31], s38 offen lds\n"
    "s_add_u32 m0, s62, 41984\n"
    "s_mov_b32 s38, 1384448\n"
    "buffer_load_dwordx4 v17, s[28:31], s38 offen lds\n"
    "v_add_u32 v48, 49152, v14\n"
    "ds_read_b64_tr_b16 a[48:49], v48 offset:0\n"
    "ds_read_b64_tr_b16 a[50:51], v48 offset:256\n"
    "ds_read_b64_tr_b16 a[52:53], v48 offset:4096\n"
    "ds_read_b64_tr_b16 a[54:55], v48 offset:4352\n"
    "ds_read_b64_tr_b16 a[56:57], v48 offset:8192\n"
    "ds_read_b64_tr_b16 a[58:59], v48 offset:8448\n"
    "ds_read_b64_tr_b16 a[60:61], v48 offset:12288\n"
    "ds_read_b64_tr_b16 a[62:63], v48 offset:12544\n"
    "s_waitcnt lgkmcnt(0)\n"
    "s_waitcnt vmcnt(24)\n"
    "s_barrier\n"
    "s_mov_b32 s28, s94\n"
    "s_mov_b32 s29, s95\n"
    "s_add_u32 m0, s62, 49152\n"
    "s_mov_b32 s38, 1441792\n"
    "buffer_load_dwordx4 v17, s[28:31], s38 offen lds\n"
    "s_add_u32 m0, s62, 50176\n"
    "s_mov_b32 s38, 1449984\n"
    "buffer_load_dwordx4 v17, s[28:31], s38 offen lds\n"
    "s_add_u32 m0, s62, 57344\n"
    "s_mov_b32 s38, 1507328\n"
    "buffer_load_dwordx4 v17, s[28:31], s38 offen lds\n"
    "s_add_u32 m0, s62, 58368\n"
    "s_mov_b32 s38, 1515520\n"
    "buffer_load_dwordx4 v17, s[28:31], s38 offen lds\n"
    "v_add_u32 v48, 65536, v14\n"
    "ds_read_b64_tr_b16 a[64:65], v48 offset:0\n"
    "ds_read_b64_tr_b16 a[66:67], v48 offset:256\n"
    "ds_read_b64_tr_b16 a[68:69], v48 offset:4096\n"
    "ds_read_b64_tr_b16 a[70:71], v48 offset:4352\n"
    "ds_read_b64_tr_b16 a[72:73], v48 offset:8192\n"
    "ds_read_b64_tr_b16 a[74:75], v48 offset:8448\n"
    "ds_read_b64_tr_b16 a[76:77], v48 offset:12288\n"
    "ds_read_b64_tr_b16 a[78:79], v48 offset:12544\n"
    "s_waitcnt lgkmcnt(0)\n"
    "s_waitcnt vmcnt(24)\n"
    "s_barrier\n"
    "s_mov_b32 s28, s94\n"
    "s_mov_b32 s29, s95\n"
    "s_add_u32 m0, s62, 65536\n"
    "s_mov_b32 s38, 1572864\n"
    "buffer_load_dwordx4 v17, s[28:31], s38 offen lds\n"
    "s_add_u32 m0, s62, 66560\n"
    "s_mov_b32 s38, 1581056\n"
    "buffer_load_dwordx4 v17, s[28:31], s38 offen lds\n"
    "s_add_u32 m0, s62, 73728\n"
    "s_mov_b32 s38, 1638400\n"
    "buffer_load_dwordx4 v17, s[28:31], s38 offen lds\n"
    "s_add_u32 m0, s62, 74752\n"
    "s_mov_b32 s38, 1646592\n"
    "buffer_load_dwordx4 v17, s[28:31], s38 offen lds\n"
    "v_add_u32 v48, 81920, v14\n"
    "ds_read_b64_tr_b16 a[80:81], v48 offset:0\n"
    "ds_read_b64_tr_b16 a[82:83], v48 offset:256\n"
    "ds_read_b64_tr_b16 a[84:85], v48 offset:4096\n"
    "ds_read_b64_tr_b16 a[86:87], v48 offset:4352\n"
    "ds_read_b64_tr_b16 a[88:89], v48 offset:8192\n"
    "ds_read_b64_tr_b16 a[90:91], v48 offset:8448\n"
    "ds_read_b64_tr_b16 a[92:93], v48 offset:12288\n"
    "ds_read_b64_tr_b16 a[94:95], v48 offset:12544\n"
    "s_waitcnt lgkmcnt(0)\n"
    "s_waitcnt vmcnt(24)\n"
    "s_barrier\n"
    "s_mov_b32 s28, s94\n"
    "s_mov_b32 s29, s95\n"
    "s_add_u32 m0, s62, 81920\n"
    "s_mov_b32 s38, 1703936\n"
    "buffer_load_dwordx4 v17, s[28:31], s38 offen lds\n"
    "s_add_u32 m0, s62, 82944\n"
    "s_mov_b32 s38, 1712128\n"
    "buffer_load_dwordx4 v17, s[28:31], s38 offen lds\n"
    "s_add_u32 m0, s62, 90112\n"
    "s_mov_b32 s38, 1769472\n"
    "buffer_load_dwordx4 v17, s[28:31], s38 offen lds\n"
    "s_add_u32 m0, s62, 91136\n"
    "s_mov_b32 s38, 1777664\n"
    "buffer_load_dwordx4 v17, s[28:31], s38 offen lds\n"
    "v_add_u32 v48, 98304, v14\n"
    "ds_read_b64_tr_b16 a[96:97], v48 offset:0\n"
    "ds_read_b64_tr_b16 a[98:99], v48 offset:256\n"
    "ds_read_b64_tr_b16 a[100:101], v48 offset:4096\n"
    "ds_read_b64_tr_b16 a[102:103], v48 offset:4352\n"
    "ds_read_b64_tr_b16 a[104:105], v48 offset:8192\n"
    "ds_read_b64_tr_b16 a[106:107], v48 offset:8448\n"
    "ds_read_b64_tr_b16 a[108:109], v48 offset:12288\n"
    "ds_read_b64_tr_b16 a[110:111], v48 offset:12544\n"
    "s_waitcnt lgkmcnt(0)\n"
    "s_waitcnt vmcnt(24)\n"
    "s_barrier\n"
    "v_add_u32 v48, 114688, v14\n"
    "ds_read_b64_tr_b16 a[112:113], v48 offset:0\n"
    "ds_read_b64_tr_b16 a[114:115], v48 offset:256\n"
    "ds_read_b64_tr_b16 a[116:117], v48 offset:4096\n"
    "ds_read_b64_tr_b16 a[118:119], v48 offset:4352\n"
    "ds_read_b64_tr_b16 a[120:121], v48 offset:8192\n"
    "ds_read_b64_tr_b16 a[122:123], v48 offset:8448\n"
    "ds_read_b64_tr_b16 a[124:125], v48 offset:12288\n"
    "ds_read_b64_tr_b16 a[126:127], v48 offset:12544\n"
    "s_waitcnt lgkmcnt(0)\n"
    "s_waitcnt vmcnt(20)\n"
    "s_barrier\n"
    "v_add_u32 v48, 0, v14\n"
    "ds_read_b64_tr_b16 a[128:129], v48 offset:0\n"
    "ds_read_b64_tr_b16 a[130:131], v48 offset:256\n"
    "ds_read_b64_tr_b16 a[132:133], v48 offset:4096\n"
    "ds_read_b64_tr_b16 a[134:135], v48 offset:4352\n"
    "ds_read_b64_tr_b16 a[136:137], v48 offset:8192\n"
    "ds_read_b64_tr_b16 a[138:139], v48 offset:8448\n"
    "ds_read_b64_tr_b16 a[140:141], v48 offset:12288\n"
    "ds_read_b64_tr_b16 a[142:143], v48 offset:12544\n"
    "s_waitcnt lgkmcnt(0)\n"
    "s_waitcnt vmcnt(16)\n"
    "s_barrier\n"
    "v_add_u32 v48, 16384, v14\n"
    "ds_read_b64_tr_b16 a[144:145], v48 offset:0\n"
    "ds_read_b64_tr_b16 a[146:147], v48 offset:256\n"
    "ds_read_b64_tr_b16 a[148:149], v48 offset:4096\n"
    "ds_read_b64_tr_b16 a[150:151], v48 offset:4352\n"
    "ds_read_b64_tr_b16 a[152:153], v48 offset:8192\n"
    "ds_read_b64_tr_b16 a[154:155], v48 offset:8448\n"
    "ds_read_b64_tr_b16 a[156:157], v48 offset:12288\n"
    "ds_read_b64_tr_b16 a[158:159], v48 offset:12544\n"
    "s_waitcnt lgkmcnt(0)\n"
    "s_waitcnt vmcnt(12)\n"
    "s_barrier\n"
    "v_add_u32 v48, 32768, v14\n"
    "ds_read_b64_tr_b16 a[160:161], v48 offset:0\n"
    "ds_read_b64_tr_b16 a[162:163], v48 offset:256\n"
    "ds_read_b64_tr_b16 a[164:165], v48 offset:4096\n"
    "ds_read_b64_tr_b16 a[166:167], v48 offset:4352\n"
    "ds_read_b64_tr_b16 a[168:169], v48 offset:8192\n"
    "ds_read_b64_tr_b16 a[170:171], v48 offset:8448\n"
    "ds_read_b64_tr_b16 a[172:173], v48 offset:12288\n"
    "ds_read_b64_tr_b16 a[174:175], v48 offset:12544\n"
    "s_waitcnt lgkmcnt(0)\n"
    "s_waitcnt vmcnt(8)\n"
    "s_barrier\n"
    "v_add_u32 v48, 49152, v14\n"
    "ds_read_b64_tr_b16 a[176:177], v48 offset:0\n"
    "ds_read_b64_tr_b16 a[178:179], v48 offset:256\n"
    "ds_read_b64_tr_b16 a[180:181], v48 offset:4096\n"
    "ds_read_b64_tr_b16 a[182:183], v48 offset:4352\n"
    "ds_read_b64_tr_b16 a[184:185], v48 offset:8192\n"
    "ds_read_b64_tr_b16 a[186:187], v48 offset:8448\n"
    "ds_read_b64_tr_b16 a[188:189], v48 offset:12288\n"
    "ds_read_b64_tr_b16 a[190:191], v48 offset:12544\n"
    "s_waitcnt lgkmcnt(0)\n"
    "s_waitcnt vmcnt(4)\n"
    "s_barrier\n"
    "v_add_u32 v48, 65536, v14\n"
    "ds_read_b64_tr_b16 a[192:193], v48 offset:0\n"
    "ds_read_b64_tr_b16 a[194:195], v48 offset:256\n"
    "ds_read_b64_tr_b16 a[196:197], v48 offset:4096\n"
    "ds_read_b64_tr_b16 a[198:199], v48 offset:4352\n"
    "ds_read_b64_tr_b16 a[200:201], v48 offset:8192\n"
    "ds_read_b64_tr_b16 a[202:203], v48 offset:8448\n"
    "ds_read_b64_tr_b16 a[204:205], v48 offset:12288\n"
    "ds_read_b64_tr_b16 a[206:207], v48 offset:12544\n"
    "s_waitcnt lgkmcnt(0)\n"
    "s_waitcnt vmcnt(0)\n"
    "s_barrier\n"
    "v_add_u32 v48, 81920, v14\n"
    "ds_read_b64_tr_b16 a[208:209], v48 offset:0\n"
    "ds_read_b64_tr_b16 a[210:211], v48 offset:256\n"
    "ds_read_b64_tr_b16 a[212:213], v48 offset:4096\n"
    "ds_read_b64_tr_b16 a[214:215], v48 offset:4352\n"
    "ds_read_b64_tr_b16 a[216:217], v48 offset:8192\n"
    "ds_read_b64_tr_b16 a[218:219], v48 offset:8448\n"
    "ds_read_b64_tr_b16 a[220:221], v48 offset:12288\n"
    "ds_read_b64_tr_b16 a[222:223], v48 offset:12544\n"
    "s_waitcnt lgkmcnt(0)\n"
    "s_barrier\n"
    "v_lshlrev_b32 v32, 16, v116\n"
    "v_and_b32 v33, 0xffff0000, v116\n"
    "v_lshlrev_b32 v34, 16, v117\n"
    "v_and_b32 v35, 0xffff0000, v117\n"
    "v_lshlrev_b32 v36, 16, v118\n"
    "v_and_b32 v37, 0xffff0000, v118\n"
    "v_lshlrev_b32 v38, 16, v119\n"
    "v_and_b32 v39, 0xffff0000, v119\n"
    "v_mov_b32 v24, v12\n"
    "v_mov_b32 v25, v13\n"
    "v_mov_b32 v30, v12\n"
    "v_mov_b32 v31, v13\n"
    "v_mov_b32 v26, 0\n"
    "v_mov_b32 v27, 0\n"
    "v_mov_b32 v28, 0\n"
    "v_mov_b32 v29, 0\n"
    "v_mov_b32 v42, 0\n"
    "v_mov_b32 v43, 0\n"
    "v_mov_b32 v48, 0\n"
    "v_mov_b32 v44, 0x3b000000\n"
    "s_mov_b32 s40, 0\n"
    "s_mov_b32 s41, 0\n"
    "s_mov_b32 s42, 0\n"
    "s_mov_b32 s43, 0\n"
    "s_mov_b32 s44, 0\n"
    "s_mov_b32 s45, 0\n"
    "s_mov_b32 s46, -1\n"
    "s_mov_b32 s47, -1\n"
    "s_mov_b32 s48, 0\n"
    "s_mov_b32 s51, 0\n"
    "s_mov_b32 s49, 64\n"
    "s_mov_b32 s37, 0\n"
    "s_mov_b64 s[64:65], 0\n"
    "s_mov_b32 s79, 0\n"
    "s_mov_b32 s74, 0xffff0000\n"
    "s_mov_b32 s75, 0\n"
    "s_mov_b32 s76, 0xaaaaaaaa\n"
    "s_mov_b32 s77, 0xaaaaaaaa\n"
    "LRY%=:\n"
    "global_load_dword v40, v48, s[96:97] sc1\n"
    "s_waitcnt vmcnt(0)\n"
    "v_readfirstlane_b32 s56, v40\n"
    "s_cmp_lg_u32 s56, 0\n"
    "s_cbranch_scc1 LRZ%=\n"
    "s_sleep 8\n"
    "s_add_u32 s37, s37, 1\n"
    "s_cmp_lt_u32 s37, 0x80000\n"
    "s_cbranch_scc1 LRY%=\n"
    "s_branch LFAIL%=\n"
    "LRZ%=:\n"
    "s_lshl_b32 s56, s56, 13\n"
    "s_and_b32 s56, s56, 0x2000\n"
    "s_xor_b32 s57, s56, 0x2000\n"
    "s_xor_b32 s58, s58, s56\n"
    "v_xor_b32 v2, s56, v2\n"
    "v_xor_b32 v11, s56, v11\n"
    "v_xor_b32 v12, s56, v21\n"
    "s_cmp_eq_u32 s55, 0\n"
    "s_cselect_b32 s38, 0, 0x80000000\n"
    "s_add_u32 s38, s38, 4096\n"
    "v_add_u32 v13, s38, v11\n"
    "LPR%=:\n"
    "buffer_load_dword v40, v12, s[16:19], 0 offen sc1\n"
    "s_waitcnt vmcnt(0)\n"
    "v_cmp_eq_u32 vcc, 0, v40\n"
    "s_cbranch_vccz LPOLL%=\n"
    "s_sleep 8\n"
    "s_add_u32 s37, s37, 1\n"
    "s_cmp_lt_u32 s37, 0x80000\n"
    "s_cbranch_scc1 LPR%=\n"
    "s_branch LFAIL%=\n"
    "LIDLE%=:\n"
    "s_cmp_eq_u32 s40, 64\n"
    "s_cselect_b32 s38, 59, 64\n"
    "s_cmp_ge_u32 s42, s38\n"
    "s_cselect_b32 s38, s41, 0\n"
    "s_cmp_eq_u32 s38, 64\n"
    "s_cselect_b32 s38, s40, 0\n"
    "s_cmp_eq_u32 s38, s49\n"
    "s_cbranch_scc1 LBEST%=\n"
    "s_add_u32 s37, s37, 1\n"
    "s_cmp_gt_u32 s37, 0x100000\n"
    "s_cbranch_scc1 LFAIL%=\n"
    "s_sleep 1\n"
    "LPOLL%=:\n"
    "v_mad_u32_u24 v41, v3, s41, v2\n"
    "v_mad_u32_u24 v41, v4, s42, v41\n"
    "v_mad_u32_u24 v41, v5, s40, v41\n"
    "buffer_load_dword v40, v41, s[16:19], 0 offen sc1\n"
    "s_waitcnt vmcnt(0)\n"
    "LTOP%=:\n"
    "v_readlane_b32 s52, v40, 32\n"
    "v_readlane_b32 s53, v40, 0\n"
    "v_readlane_b32 s54, v40, 16\n"
    "s_cmp_lg_u32 s52, 0\n"
    "s_cbranch_scc1 LDEC%=\n"
    "LPICK%=:\n"
    "s_cmp_ge_u32 s41, s43\n"
    "s_cselect_b32 s38, s40, 0\n"
    "s_cmp_ge_u32 s38, 64\n"
    "s_cselect_b32 s41, 64, s41\n"
    "s_cselect_b32 s53, 0, s53\n"
    "s_cmp_lt_u32 s42, s41\n"
    "s_cselect_b32 s51, s54, 0\n"
    "s_or_b32 s38, s51, s53\n"
    "s_mov_b32 s50, -1\n"
    "s_cbranch_scc0 LNONE%=\n"
    "s_cmp_lg_u32 s51, 0\n"
    "s_cselect_b32 s50, s42, s41\n"
    "s_addc_u32 s42, s42, 0\n"
    "s_add_u32 s41, s41, 1\n"
    "s_sub_u32 s41, s41, s51\n"
    "s_lshl_b32 s60, s50, 5\n"
    "s_add_u32 s60, s60, s55\n"
    "s_cmp_eq_u32 s60, s44\n"
    "s_cselect_b32 s38, s51, 0\n"
    "s_cmp_lg_u32 s38, 0\n"
    "s_cselect_b32 s60, s50, s60\n"
    "s_cselect_b32 s62, 22020096, 0\n"
    "s_cselect_b32 s61, s57, s56\n"
    "s_add_u32 s62, s62, 241172480\n"
    "s_lshl_b32 s39, s60, 10\n"
    "s_andn2_b32 s63, s39, 0x1fff\n"
    "s_add_u32 s63, s63, s39\n"
    "s_add_u32 s63, s63, s62\n"
    "s_lshr_b32 s38, s63, 8\n"
    "s_xor_b32 s38, s38, s61\n"
    "s_and_b32 s38, s38, 0x2000\n"
    "s_or_b32 s63, s63, s38\n"
    "s_lshl_b32 s62, s51, 6\n"
    "s_add_u32 s62, s62, s50\n"
    "s_lshl_b32 s62, s62, 18\n"
    "s_add_u32 s62, s62, 197132288\n"
    "s_lshr_b32 s38, s62, 8\n"
    "s_xor_b32 s38, s38, s57\n"
    "s_and_b32 s38, s38, 0x2000\n"
    "s_or_b32 s62, s62, s38\n"
    "buffer_load_dwordx4 v[116:119], v0, s[16:19], s63 offen sc1\n"
    "buffer_load_dwordx4 v[100:103], v1, s[16:19], s62 offen sc1\n"
    "s_add_u32 s62, s62, 0x10000\n"
    "buffer_load_dwordx4 v[104:107], v1, s[16:19], s62 offen sc1\n"
    "s_add_u32 s62, s62, 0x10000\n"
    "buffer_load_dwordx4 v[108:111], v1, s[16:19], s62 offen sc1\n"
    "s_add_u32 s62, s62, 0x10000\n"
    "buffer_load_dwordx4 v[112:115], v1, s[16:19], s62 offen sc1\n"
    "s_cmp_ge_i32 s47, 0\n"
    "s_cbranch_scc1 LJOB%=\n"
    "s_lshl_b32 s60, s50, 5\n"
    "s_add_u32 s60, s60, s55\n"
    "s_cmp_lg_u32 s51, 0\n"
    "s_cselect_b32 s62, 8388608, 0\n"
    "s_cselect_b32 s63, s57, s57\n"
    "s_add_u32 s62, s62, 245366784\n"
    "s_lshl_b32 s39, s60, 11\n"
    "s_andn2_b32 s32, s39, 0x1fff\n"
    "s_add_u32 s32, s32, s39\n"
    "s_add_u32 s32, s32, s62\n"
    "s_lshr_b32 s38, s32, 8\n"
    "s_xor_b32 s38, s38, s63\n"
    "s_and_b32 s38, s38, 0x2000\n"
    "s_or_b32 s32, s32, s38\n"
    "s_mov_b32 s33, s60\n"
    "s_waitcnt vmcnt(4)\n"
    "v_lshlrev_b32 v64, 16, v116\n"
    "v_and_b32 v65, 0xffff0000, v116\n"
    "v_lshlrev_b32 v66, 16, v117\n"
    "v_and_b32 v67, 0xffff0000, v117\n"
    "v_lshlrev_b32 v68, 16, v118\n"
    "v_and_b32 v69, 0xffff0000, v118\n"
    "v_lshlrev_b32 v70, 16, v119\n"
    "v_and_b32 v71, 0xffff0000, v119\n"
    "s_waitcnt vmcnt(3)\n"
    "v_mfma_f32_16x16x32_bf16 v[64:67], v[24:27], v[100:103], v[64:67]\n"
    "v_mfma_f32_16x16x32_bf16 v[68:71], v[28:31], v[100:103], v[68:71]\n"
    "s_waitcnt vmcnt(2)\n"
    "v_mfma_f32_16x16x32_bf16 v[64:67], v[24:27], v[104:107], v[64:67]\n"
    "v_mfma_f32_16x16x32_bf16 v[68:71], v[28:31], v[104:107], v[68:71]\n"
    "s_waitcnt vmcnt(1)\n"
    "v_mfma_f32_16x16x32_bf16 v[64:67], v[24:27], v[108:111], v[64:67]\n"
    "v_mfma_f32_16x16x32_bf16 v[68:71], v[28:31], v[108:111], v[68:71]\n"
    "v_max3_u32 v48, v103, v107, v111\n"
    "s_waitcnt vmcnt(0)\n"
    "v_mfma_f32_16x16x32_bf16 v[64:67], v[24:27], v[112:115], v[64:67]\n"
    "v_mfma_f32_16x16x32_bf16 v[68:71], v[28:31], v[112:115], v[68:71]\n"
    "v_max_u32 v48, v48, v115\n"
    "v_cmp_eq_u32 vcc, -1, v48\n"
    "s_mov_b32 s47, s50\n"
    "s_mov_b32 s48, s51\n"
    "s_mov_b32 s50, -1\n"
    "s_cbranch_vccnz LEARLY%=\n"
    "LNXT%=:\n"
    "LJOB%=:\n"
    "v_mad_u32_u24 v41, v3, s41, v2\n"
    "v_mad_u32_u24 v41, v4, s42, v41\n"
    "v_mad_u32_u24 v41, v5, s40, v41\n"
    "buffer_load_dword v40, v41, s[16:19], 0 offen sc1\n"
    "v_cvt_pk_bf16_f32 v124, v64, v65\n"
    "v_lshlrev_b32 v72, 16, v124\n"
    "v_cvt_pk_bf16_f32 v125, v66, v67\n"
    "v_readfirstlane_b32 s68, v72\n"
    "v_cvt_pk_bf16_f32 v126, v68, v69\n"
    "v_cvt_pk_bf16_f32 v127, v70, v71\n"
    "v_and_b32 v73, 0xffff0000, v124\n"
    "v_lshlrev_b32 v74, 16, v125\n"
    "v_and_b32 v75, 0xffff0000, v125\n"
    "v_lshlrev_b32 v76, 16, v126\n"
    "v_and_b32 v77, 0xffff0000, v126\n"
    "v_lshlrev_b32 v78, 16, v127\n"
    "v_and_b32 v79, 0xffff0000, v127\n"
    "v_pk_add_f32 v[92:93], v[72:73], s[68:69] op_sel_hi:[1,0] neg_lo:[0,1] neg_hi:[0,1]\n"
    "v_pk_add_f32 v[94:95], v[74:75], s[68:69] op_sel_hi:[1,0] neg_lo:[0,1] neg_hi:[0,1]\n"
    "v_pk_add_f32 v[96:97], v[76:77], s[68:69] op_sel_hi:[1,0] neg_lo:[0,1] neg_hi:[0,1]\n"
    "v_pk_add_f32 v[98:99], v[78:79], s[68:69] op_sel_hi:[1,0] neg_lo:[0,1] neg_hi:[0,1]\n"
    "v_pk_add_f32 v[48:49], v[92:93], v[94:95]\n"
    "v_pk_mul_f32 v[50:51], v[92:93], v[92:93]\n"
    "v_pk_add_f32 v[52:53], v[96:97], v[98:99]\n"
    "v_pk_fma_f32 v[50:51], v[94:95], v[94:95], v[50:51]\n"
    "v_pk_add_f32 v[48:49], v[48:49], v[52:53]\n"
    "v_pk_fma_f32 v[50:51], v[96:97], v[96:97], v[50:51]\n"
    "v_add_f32 v48, v48, v49\n"
    "v_pk_fma_f32 v[50:51], v[98:99], v[98:99], v[50:51]\n"
    "v_mov_b32 v52, s68\n"
    "v_add_f32 v49, v50, v51\n"
    "v_add_f32_dpp v48, v48, v48 row_ror:8 row_mask:0xf bank_mask:0xf\n"
    "s_nop 0\n"
    "v_add_f32_dpp v49, v49, v49 row_ror:8 row_mask:0xf bank_mask:0xf\n"
    "buffer_store_dwordx4 v[124:127], v0, s[16:19], s32 offen offset:0 sc1\n"
    "v_add_f32_dpp v48, v48, v48 row_ror:4 row_mask:0xf bank_mask:0xf\n"
    "v_add_f32_dpp v49, v49, v49 row_ror:4 row_mask:0xf bank_mask:0xf\n"
    "s_nop 0\n"
    "v_add_f32_dpp v48, v48, v48 row_ror:2 row_mask:0xf bank_mask:0xf\n"
    "v_add_f32_dpp v49, v49, v49 row_ror:2 row_mask:0xf bank_mask:0xf\n"
    "s_nop 0\n"
    "v_add_f32_dpp v48, v48, v48 row_ror:1 row_mask:0xf bank_mask:0xf\n"
    "v_add_f32_dpp v49, v49, v49 row_ror:1 row_mask:0xf bank_mask:0xf\n"
    "s_nop 1\n"
    "v_permlane16_swap_b32 v48, v49\n"
    "v_add_f32 v48, v48, v49\n"
    "v_mov_b32 v49, v48\n"
    "s_nop 1\n"
    "v_permlane32_swap_b32 v48, v49\n"
    "v_add_f32 v48, v48, v49\n"
    "s_nop 0\n"
    "v_readlane_b32 s62, v48, 0\n"
    "v_readlane_b32 s63, v48, 16\n"
    "s_nop 0\n"
    "v_mul_f32 v48, s62, v44\n"
    "v_mul_f32 v49, s63, v44\n"
    "v_fma_f32 v49, -v48, v48, v49\n"
    "v_add_f32 v52, v52, v48\n"
    "v_max_f32 v49, 0, v49\n"
    "v_cvt_pk_bf16_f32 v48, v52, v49\n"
    "v_and_b32 v49, 0xffff0000, v48\n"
    "v_add_f32 v49, 0x3727c5ac, v49\n"
    "v_cvt_pk_bf16_f32 v49, v49, v49\n"
    "v_lshlrev_b32 v49, 16, v49\n"
    "v_sqrt_f32 v50, v49\n"
    "v_lshlrev_b32 v48, 16, v48\n"
    "v_cvt_pk_bf16_f32 v50, v50, v50\n"
    "v_lshlrev_b32 v50, 16, v50\n"
    "v_rcp_f32 v50, v50\n"
    "v_pk_add_f32 v[92:93], v[72:73], v[48:49] op_sel_hi:[1,0] neg_lo:[0,1] neg_hi:[0,1]\n"
    "v_pk_add_f32 v[94:95], v[74:75], v[48:49] op_sel_hi:[1,0] neg_lo:[0,1] neg_hi:[0,1]\n"
    "v_pk_add_f32 v[96:97], v[76:77], v[48:49] op_sel_hi:[1,0] neg_lo:[0,1] neg_hi:[0,1]\n"
    "v_pk_add_f32 v[98:99], v[78:79], v[48:49] op_sel_hi:[1,0] neg_lo:[0,1] neg_hi:[0,1]\n"
    "v_cvt_pk_bf16_f32 v52, v92, v93\n"
    "v_cvt_pk_bf16_f32 v53, v94, v95\n"
    "v_cvt_pk_bf16_f32 v54, v96, v97\n"
    "v_cvt_pk_bf16_f32 v55, v98, v99\n"
    "v_lshlrev_b32 v92, 16, v52\n"
    "v_and_b32 v93, 0xffff0000, v52\n"
    "v_lshlrev_b32 v94, 16, v53\n"
    "v_and_b32 v95, 0xffff0000, v53\n"
    "v_lshlrev_b32 v96, 16, v54\n"
    "v_and_b32 v97, 0xffff0000, v54\n"
    "v_lshlrev_b32 v98, 16, v55\n"
    "v_and_b32 v99, 0xffff0000, v55\n"
    "v_pk_mul_f32 v[80:81], v[92:93], v[50:51] op_sel_hi:[1,0]\n"
    "v_pk_mul_f32 v[82:83], v[94:95], v[50:51] op_sel_hi:[1,0]\n"
    "v_pk_mul_f32 v[84:85], v[96:97], v[50:51] op_sel_hi:[1,0]\n"
    "v_pk_mul_f32 v[86:87], v[98:99], v[50:51] op_sel_hi:[1,0]\n"
    "v_cvt_pk_bf16_f32 v88, v80, v81\n"
    "v_cvt_pk_bf16_f32 v89, v82, v83\n"
    "v_cvt_pk_bf16_f32 v90, v84, v85\n"
    "v_cvt_pk_bf16_f32 v91, v86, v87\n"
    "v_lshlrev_b32 v80, 16, v88\n"
    "v_and_b32 v81, 0xffff0000, v88\n"
    "v_lshlrev_b32 v82, 16, v89\n"
    "v_and_b32 v83, 0xffff0000, v89\n"
    "v_lshlrev_b32 v84, 16, v90\n"
    "v_and_b32 v85, 0xffff0000, v90\n"
    "v_lshlrev_b32 v86, 16, v91\n"
    "v_and_b32 v87, 0xffff0000, v91\n"
    "v_pk_add_f32 v[72:73], v[72:73], v[80:81]\n"
    "v_pk_add_f32 v[74:75], v[74:75], v[82:83]\n"
    "v_pk_add_f32 v[76:77], v[76:77], v[84:85]\n"
    "v_pk_add_f32 v[78:79], v[78:79], v[86:87]\n"
    "buffer_store_dwordx4 v[88:91], v0, s[16:19], s32 offen offset:1024 sc1\n"
    "v_pk_mul_f32 v[56:57], v[80:81], v[32:33]\n"
    "v_pk_fma_f32 v[56:57], v[82:83], v[34:35], v[56:57]\n"
    "v_pk_fma_f32 v[56:57], v[84:85], v[36:37], v[56:57]\n"
    "v_pk_fma_f32 v[56:57], v[86:87], v[38:39], v[56:57]\n"
    "v_cvt_pk_bf16_f32 v60, v72, v73\n"
    "v_cvt_pk_bf16_f32 v61, v74, v75\n"
    "v_cvt_pk_bf16_f32 v62, v76, v77\n"
    "v_cvt_pk_bf16_f32 v63, v78, v79\n"
    "s_cmp_ge_u32 s47, s43\n"
    "s_cselect_b32 s38, 1, 0\n"
    "s_cmp_eq_u32 s38, s48\n"
    "s_cbranch_scc0 LKEEP%=\n"
    "s_lshl_b32 s61, s33, 10\n"
    "buffer_store_dwordx4 v[60:63], v0, s[24:27], s61 offen offset:0 sc1\n"
    "s_branch LKEPT%=\n"
    "LKEEP%=:\n"
    "s_lshl_b32 s39, s33, 10\n"
    "s_andn2_b32 s61, s39, 0x1fff\n"
    "s_add_u32 s61, s61, s39\n"
    "s_add_u32 s61, s61, 241172480\n"
    "s_lshr_b32 s38, s61, 8\n"
    "s_xor_b32 s38, s38, s57\n"
    "s_and_b32 s38, s38, 0x2000\n"
    "s_or_b32 s61, s61, s38\n"
    "s_lshl_b32 s38, s48, 31\n"
    "s_or_b32 s61, s61, s38\n"
    "buffer_store_dwordx4 v[60:63], v0, s[16:19], s61 offen offset:0 sc1\n"
    "s_xor_b32 s66, s48, 1\n"
    "s_mov_b32 s67, 0\n"
    "s_lshl_b64 s[66:67], s[66:67], s47\n"
    "s_or_b64 s[64:65], s[64:65], s[66:67]\n"
    "LKEPT%=:\n"
    "v_add_f32_dpp v56, v56, v56 row_ror:8 row_mask:0xf bank_mask:0xf\n"
    "v_add_f32_dpp v57, v57, v57 row_ror:8 row_mask:0xf bank_mask:0xf\n"
    "s_nop 0\n"
    "v_add_f32_dpp v56, v56, v56 row_ror:4 row_mask:0xf bank_mask:0xf\n"
    "v_add_f32_dpp v57, v57, v57 row_ror:4 row_mask:0xf bank_mask:0xf\n"
    "s_nop 0\n"
    "v_add_f32_dpp v56, v56, v56 row_ror:2 row_mask:0xf bank_mask:0xf\n"
    "v_add_f32_dpp v57, v57, v57 row_ror:2 row_mask:0xf bank_mask:0xf\n"
    "s_nop 0\n"
    "v_add_f32_dpp v56, v56, v56 row_ror:1 row_mask:0xf bank_mask:0xf\n"
    "v_add_f32_dpp v57, v57, v57 row_ror:1 row_mask:0xf bank_mask:0xf\n"
    "s_nop 1\n"
    "v_permlane16_swap_b32 v56, v57\n"
    "v_add_f32 v56, v56, v57\n"
    "v_mov_b32 v57, v56\n"
    "s_nop 1\n"
    "v_permlane32_swap_b32 v56, v57\n"
    "v_add_f32 v56, v56, v57\n"
    "s_nop 0\n"
    "v_readlane_b32 s62, v56, 0\n"
    "v_readlane_b32 s63, v56, 16\n"
    "s_sub_u32 s39, 0xffff, s33\n"
    "v_mov_b32 v56, s62\n"
    "v_add_f32 v56, s63, v56\n"
    "v_cvt_pk_bf16_f32 v56, v56, v56\n"
    "v_lshlrev_b32 v56, 16, v56\n"
    "v_cmp_o_f32 vcc, v56, v56\n"
    "v_ashrrev_i32 v57, 31, v56\n"
    "v_or_b32 v57, 0x80000000, v57\n"
    "v_xor_b32 v56, v56, v57\n"
    "v_cndmask_b32 v56, 0, v56, vcc\n"
    "v_and_b32 v56, 0xffff0000, v56\n"
    "v_or_b32 v56, s39, v56\n"
    "s_lshl_b64 exec, 1, s47\n"
    "s_cmp_lg_u32 s48, 0\n"
    "s_cselect_b64 vcc, -1, 0\n"
    "v_cndmask_b32 v42, v42, v56, vcc\n"
    "v_cndmask_b32 v43, v56, v43, vcc\n"
    "s_mov_b64 exec, -1\n"
    "s_waitcnt vmcnt(3)\n"
    "s_mov_b32 s47, -1\n"
    "s_mov_b32 s37, 0\n"
    "s_cmp_lt_i32 s50, 0\n"
    "s_cbranch_scc1 LTOP%=\n"
    "s_lshl_b32 s60, s50, 5\n"
    "s_add_u32 s60, s60, s55\n"
    "s_cmp_lg_u32 s51, 0\n"
    "s_cselect_b32 s62, 8388608, 0\n"
    "s_cselect_b32 s63, s57, s57\n"
    "s_add_u32 s62, s62, 245366784\n"
    "s_lshl_b32 s39, s60, 11\n"
    "s_andn2_b32 s32, s39, 0x1fff\n"
    "s_add_u32 s32, s32, s39\n"
    "s_add_u32 s32, s32, s62\n"
    "s_lshr_b32 s38, s32, 8\n"
    "s_xor_b32 s38, s38, s63\n"
    "s_and_b32 s38, s38, 0x2000\n"
    "s_or_b32 s32, s32, s38\n"
    "s_mov_b32 s33, s60\n"
    "v_lshlrev_b32 v64, 16, v116\n"
    "v_and_b32 v65, 0xffff0000, v116\n"
    "v_lshlrev_b32 v66, 16, v117\n"
    "v_and_b32 v67, 0xffff0000, v117\n"
    "v_lshlrev_b32 v68, 16, v118\n"
    "v_and_b32 v69, 0xffff0000, v118\n"
    "v_lshlrev_b32 v70, 16, v119\n"
    "v_and_b32 v71, 0xffff0000, v119\n"
    "v_mfma_f32_16x16x32_bf16 v[64:67], v[24:27], v[100:103], v[64:67]\n"
    "s_nop 0\n"
    "v_mfma_f32_16x16x32_bf16 v[68:71], v[28:31], v[100:103], v[68:71]\n"
    "v_mfma_f32_16x16x32_bf16 v[64:67], v[24:27], v[104:107], v[64:67]\n"
    "v_mfma_f32_16x16x32_bf16 v[68:71], v[28:31], v[104:107], v[68:71]\n"
    "v_mfma_f32_16x16x32_bf16 v[64:67], v[24:27], v[108:111], v[64:67]\n"
    "v_mfma_f32_16x16x32_bf16 v[68:71], v[28:31], v[108:111], v[68:71]\n"
    "v_max3_u32 v48, v103, v107, v111\n"
    "v_mfma_f32_16x16x32_bf16 v[64:67], v[24:27], v[112:115], v[64:67]\n"
    "v_mfma_f32_16x16x32_bf16 v[68:71], v[28:31], v[112:115], v[68:71]\n"
    "v_max_u32 v48, v48, v115\n"
    "v_cmp_eq_u32 vcc, -1, v48\n"
    "s_mov_b32 s47, s50\n"
    "s_mov_b32 s48, s51\n"
    "s_mov_b32 s50, -1\n"
    "s_cbranch_vccnz LEARLY%=\n"
    "s_branch LTOP%=\n"
    "LNONE%=:\n"
    "s_cmp_ge_i32 s47, 0\n"
    "s_cbranch_scc1 LJOB%=\n"
    "s_branch LIDLE%=\n"
    "LEARLY%=:\n"
    "s_sub_u32 s42, s42, s48\n"
    "s_add_u32 s41, s41, s48\n"
    "s_sub_u32 s41, s41, 1\n"
    "s_mov_b32 s54, s48\n"
    "s_xor_b32 s53, s48, 1\n"
    "s_mov_b32 s47, -1\n"
    "s_and_b32 s38, s37, 3\n"
    "s_cmp_eq_u32 s38, 3\n"
    "s_cbranch_scc1 LIDLE%=\n"
    "s_sleep 2\n"
    "s_add_u32 s37, s37, 1\n"
    "s_cmp_lt_u32 s37, 0x80000\n"
    "s_cbranch_scc1 LPICK%=\n"
    "s_branch LFAIL%=\n"
    "LBEST%=:\n"
    "s_lshl_b64 vcc, -1, s43\n"
    "s_nop 0\n"
    "v_cndmask_b32 v48, v43, v42, vcc\n"
    "s_nop 1\n"
    "v_max_u32_dpp v48, v48, v48 row_ror:8 row_mask:0xf bank_mask:0xf\n"
    "s_nop 1\n"
    "v_max_u32_dpp v48, v48, v48 row_ror:4 row_mask:0xf bank_mask:0xf\n"
    "s_nop 1\n"
    "v_max_u32_dpp v48, v48, v48 row_ror:2 row_mask:0xf bank_mask:0xf\n"
    "s_nop 1\n"
    "v_max_u32_dpp v48, v48, v48 row_ror:1 row_mask:0xf bank_mask:0xf\n"
    "v_mov_b32 v49, v48\n"
    "s_nop 1\n"
    "v_permlane16_swap_b32 v48, v49\n"
    "v_max_u32 v48, v48, v49\n"
    "v_mov_b32 v49, v48\n"
    "s_nop 1\n"
    "v_permlane32_swap_b32 v48, v49\n"
    "v_max_u32 v48, v48, v49\n"
    "v_max_u32 v48, 1, v48\n"
    "s_lshl_b32 s70, s40, 6\n"
    "s_add_u32 s70, s70, s58\n"
    "s_add_u32 s49, s49, 1\n"
    "s_mov_b64 exec, 1\n"
    "buffer_atomic_umax v48, off, s[16:19], s70 offset:4 sc1\n"
    "s_mov_b64 exec, -1\n"
    "v_mov_b64 v[120:121], 1\n"
    "v_mad_u32_u24 v41, v3, s41, v2\n"
    "v_mad_u32_u24 v41, v4, s42, v41\n"
    "v_mad_u32_u24 v41, v5, s40, v41\n"
    "s_mov_b64 exec, 1\n"
    "s_waitcnt vmcnt(0)\n"
    "buffer_atomic_add_x2 v[120:121], off, s[16:19], s70 sc0 sc1\n"
    "s_mov_b64 exec, -1\n"
    "buffer_load_dword v40, v41, s[16:19], 0 offen sc1\n"
    "s_lshl_b32 s39, s40, 2\n"
    "s_waitcnt vmcnt(1)\n"
    "v_readfirstlane_b32 s38, v120\n"
    "v_readfirstlane_b32 s62, v121\n"
    "s_cmp_lg_u32 s38, 31\n"
    "s_cbranch_scc1 LAS%=\n"
    "v_mov_b32 v120, s62\n"
    "s_mov_b64 exec, 0x38\n"
    "buffer_store_dword v120, v11, s[16:19], s39 offen sc1\n"
    "s_mov_b64 exec, -1\n"
    "s_waitcnt vmcnt(1)\n"
    "s_cmp_lg_u32 s40, s40\n"
    "s_cbranch_scc1 LASX%=\n"
    "v_writelane_b32 v40, s62, 32\n"
    "s_branch LASX%=\n"
    "LAS%=:\n"
    "s_waitcnt vmcnt(0)\n"
    "LASX%=:\n"
    "s_branch LTOP%=\n"
    "LDEC%=:\n"
    "s_mov_b32 s70, s40\n"
    "s_cmp_lt_u32 s40, 64\n"
    "s_cbranch_scc1 LDF%=\n"
    "s_add_u32 s40, s40, 1\n"
    "s_cmp_eq_u32 s52, s45\n"
    "s_mov_b32 s45, s52\n"
    "s_cbranch_scc0 LMLP%=\n"
    "s_cmp_eq_u32 s70, 65\n"
    "s_cbranch_scc0 LMLP%=\n"
    "s_branch LOUT%=\n"
    "LDF%=:\n"
    "s_add_u32 s40, s40, 1\n"
    "s_mov_b32 s37, 0\n"
    "s_lshr_b32 s38, s52, 16\n"
    "s_cmp_gt_i32 s38, s46\n"
    "s_cselect_b32 s70, s70, -1\n"
    "s_cselect_b32 s46, s38, s46\n"
    "s_cmp_lt_i32 s70, 0\n"
    "s_cbranch_scc1 LPICK%=\n"
    "s_mov_b32 s43, s70\n"
    "s_andn2_b32 s44, 0x7ff, s52\n"
    "LMLP%=:\n"
    "s_andn2_b32 s71, 0x7ff, s52\n"
    "s_lshr_b32 s38, s71, 5\n"
    "s_cmp_ge_u32 s38, s43\n"
    "s_cselect_b32 s63, s56, s57\n"
    "s_cselect_b32 s38, s70, 0\n"
    "s_cmp_ge_u32 s38, 64\n"
    "s_cselect_b32 s63, s57, s63\n"
    "s_cselect_b32 s62, 8388608, 0\n"
    "s_add_u32 s62, s62, 245366784\n"
    "s_lshl_b32 s39, s71, 11\n"
    "s_andn2_b32 s61, s39, 0x1fff\n"
    "s_add_u32 s61, s61, s39\n"
    "s_add_u32 s61, s61, s62\n"
    "s_lshr_b32 s38, s61, 8\n"
    "s_xor_b32 s38, s38, s63\n"
    "s_and_b32 s38, s38, 0x2000\n"
    "s_or_b32 s61, s61, s38\n"
    "buffer_load_dwordx4 v[116:119], v0, s[16:19], s61 offen offset:1024 sc1\n"
    "v_and_b32 v48, -4, v18\n"
    "buffer_load_dword v45, v48, s[16:19], s61 offen sc1\n"
    "buffer_load_dword v46, v48, s[16:19], s61 offen offset:1024 sc1\n"
    "s_lshl_b32 s39, s70, 12\n"
    "s_andn2_b32 s61, s39, 0x1fff\n"
    "s_add_u32 s61, s61, s39\n"
    "s_add_u32 s61, s61, 263323648\n"
    "s_lshr_b32 s38, s61, 8\n"
    "s_xor_b32 s38, s38, s57\n"
    "s_and_b32 s38, s38, 0x2000\n"
    "s_or_b32 s61, s61, s38\n"
    "v_mov_b64 v[104:105], 0\n"
    "v_mov_b64 v[106:107], 0\n"
    "v_mov_b64 v[108:109], 0\n"
    "v_mov_b64 v[110:111], 0\n"
    "s_waitcnt vmcnt(2)\n"
    "ds_write_b128 v8, v[116:119]\n"
    "ds_read_b128 v[72:75], v7 offset:0\n"
    "ds_read_b128 v[76:79], v7 offset:128\n"
    "ds_read_b128 v[80:83], v7 offset:256\n"
    "ds_read_b128 v[84:87], v7 offset:384\n"
    "ds_read_b128 v[88:91], v7 offset:512\n"
    "ds_read_b128 v[92:95], v7 offset:640\n"
    "ds_read_b128 v[96:99], v7 offset:768\n"
    "ds_read_b128 v[100:103], v7 offset:896\n"
    "s_waitcnt lgkmcnt(7)\n"
    "v_smfmac_f32_16x16x64_bf16 v[104:107], v[72:75], v[128:135], v6\n"
    "v_smfmac_f32_16x16x64_bf16 v[108:111], v[72:75], v[136:143], v6\n"
    "s_waitcnt lgkmcnt(6)\n"
    "v_smfmac_f32_16x16x64_bf16 v[104:107], v[76:79], v[144:151], v6\n"
    "v_smfmac_f32_16x16x64_bf16 v[108:111], v[76:79], v[152:159], v6\n"
    "s_waitcnt lgkmcnt(5)\n"
    "v_smfmac_f32_16x16x64_bf16 v[104:107], v[80:83], v[160:167], v6\n"
    "v_smfmac_f32_16x16x64_bf16 v[108:111], v[80:83], v[168:175], v6\n"
    "s_waitcnt lgkmcnt(4)\n"
    "v_smfmac_f32_16x16x64_bf16 v[104:107], v[84:87], v[176:183], v6\n"
    "v_smfmac_f32_16x16x64_bf16 v[108:111], v[84:87], v[184:191], v6\n"
    "s_waitcnt lgkmcnt(3)\n"
    "v_smfmac_f32_16x16x64_bf16 v[104:107], v[88:91], v[192:199], v6\n"
    "v_smfmac_f32_16x16x64_bf16 v[108:111], v[88:91], v[200:207], v6\n"
    "s_waitcnt lgkmcnt(2)\n"
    "v_smfmac_f32_16x16x64_bf16 v[104:107], v[92:95], v[208:215], v6\n"
    "v_smfmac_f32_16x16x64_bf16 v[108:111], v[92:95], v[216:223], v6\n"
    "s_waitcnt lgkmcnt(1)\n"
    "v_smfmac_f32_16x16x64_bf16 v[104:107], v[96:99], v[224:231], v6\n"
    "v_smfmac_f32_16x16x64_bf16 v[108:111], v[96:99], v[232:239], v6\n"
    "s_waitcnt lgkmcnt(0)\n"
    "v_smfmac_f32_16x16x64_bf16 v[104:107], v[100:103], v[240:247], v6\n"
    "v_smfmac_f32_16x16x64_bf16 v[108:111], v[100:103], v[248:255], v6\n"
    "s_nop 3\n"
    "v_add_f32 v104, v104, v106\n"
    "s_nop 2\n"
    "v_add_f32 v108, v108, v110\n"
    "v_cvt_pk_bf16_f32 v108, v104, v108\n"
    "v_lshlrev_b32 v104, 16, v108\n"
    "v_mul_f32 v105, 0xbfb8aa3b, v104\n"
    "v_exp_f32 v105, v105\n"
    "v_and_b32 v108, 0xffff0000, v108\n"
    "v_add_f32 v105, 1.0, v105\n"
    "v_rcp_f32 v105, v105\n"
    "s_nop 0\n"
    "v_mul_f32 v104, v104, v105\n"
    "v_cvt_pk_bf16_f32 v104, v104, v104\n"
    "v_lshlrev_b32 v104, 16, v104\n"
    "v_mul_f32 v104, v104, v108\n"
    "s_nop 1\n"
    "v_mov_b32_dpp v105, v104 quad_perm:[1,1,3,3] row_mask:0xf bank_mask:0xf\n"
    "v_cvt_pk_bf16_f32 v104, v104, v105\n"
    "v_min_u32 v104, -2, v104\n"
    "buffer_store_dword v104, v9, s[16:19], s61 offen sc1\n"
    "s_lshr_b32 s38, s52, 16\n"
    "s_bitcmp1_b32 s38, 15\n"
    "s_cselect_b32 s39, 0, 0x7fff\n"
    "s_or_b32 s39, s39, 0x8000\n"
    "s_xor_b32 s38, s38, s39\n"
    "s_lshl_b32 s38, s38, 16\n"
    "v_mov_b32 v52, 0xbfb8aa3b\n"
    "v_mul_f32 v52, s38, v52\n"
    "v_exp_f32 v52, v52\n"
    "s_nop 0\n"
    "v_add_f32 v52, 1.0, v52\n"
    "v_rcp_f32 v52, v52\n"
    "s_nop 0\n"
    "v_cvt_pk_bf16_f32 v52, v52, v52\n"
    "v_lshlrev_b32 v52, 16, v52\n"
    "s_waitcnt vmcnt(2)\n"
    "v_lshlrev_b32 v48, 16, v45\n"
    "v_and_b32 v45, 0xffff0000, v45\n"
    "v_cndmask_b32 v45, v48, v45, s[76:77]\n"
    "s_waitcnt vmcnt(1)\n"
    "v_lshlrev_b32 v48, 16, v46\n"
    "v_and_b32 v46, 0xffff0000, v46\n"
    "v_cndmask_b32 v46, v48, v46, s[76:77]\n"
    "v_mov_b32 v116, 0\n"
    "v_mov_b32 v117, 0\n"
    "v_mov_b32 v118, 0\n"
    "v_mov_b32 v119, 0\n"
    "v_mov_b64 v[56:57], 0\n"
    "v_mov_b64 v[58:59], 0\n"
    "v_mov_b64 v[60:61], 0\n"
    "v_mov_b64 v[62:63], 0\n"
    "s_sleep 6\n"
    "LH%=:\n"
    "buffer_load_dwordx4 v[104:107], v0, s[16:19], s61 offen offset:0 sc1\n"
    "buffer_load_dwordx4 v[108:111], v0, s[16:19], s61 offen offset:1024 sc1\n"
    "buffer_load_dwordx4 v[112:115], v0, s[16:19], s61 offen offset:2048 sc1\n"
    "s_mov_b32 exec_hi, 0\n"
    "buffer_load_dwordx4 v[116:119], v0, s[16:19], s61 offen offset:3072 sc1\n"
    "s_mov_b32 exec_hi, -1\n"
    "s_waitcnt vmcnt(0)\n"
    "v_max3_u32 v48, v104, v105, v106\n"
    "v_max3_u32 v48, v48, v107, v108\n"
    "v_max3_u32 v48, v48, v109, v110\n"
    "v_max3_u32 v48, v48, v111, v112\n"
    "v_max3_u32 v48, v48, v113, v114\n"
    "v_max3_u32 v48, v48, v115, v116\n"
    "v_max3_u32 v48, v48, v117, v118\n"
    "v_max_u32 v48, v48, v119\n"
    "v_cmp_eq_u32 vcc, -1, v48\n"
    "s_cbranch_vccz LHK%=\n"
    "s_sleep 2\n"
    "s_add_u32 s37, s37, 1\n"
    "s_cmp_lt_u32 s37, 0x80000\n"
    "s_cbranch_scc1 LH%=\n"
    "s_branch LFAIL%=\n"
    "LHK%=:\n"
    "ds_write_b128 v8, v[104:107] offset:0\n"
    "ds_write_b128 v8, v[108:111] offset:1024\n"
    "ds_write_b128 v8, v[112:115] offset:2048\n"
    "ds_write_b128 v8, v[116:119] offset:3072\n"
    "ds_read_b128 v[72:75], v7 offset:0\n"
    "ds_read_b128 v[76:79], v7 offset:128\n"
    "ds_read_b128 v[80:83], v7 offset:256\n"
    "ds_read_b128 v[84:87], v7 offset:384\n"
    "ds_read_b128 v[88:91], v7 offset:512\n"
    "ds_read_b128 v[92:95], v7 offset:640\n"
    "ds_read_b128 v[96:99], v7 offset:768\n"
    "s_waitcnt lgkmcnt(6)\n"
    "v_smfmac_f32_16x16x64_bf16 v[56:59], v[72:75], a[0:7], v6\n"
    "ds_read_b128 v[100:103], v7 offset:896\n"
    "s_waitcnt lgkmcnt(6)\n"
    "v_smfmac_f32_16x16x64_bf16 v[60:63], v[76:79], a[8:15], v6\n"
    "ds_read_b128 v[72:75], v7 offset:1024\n"
    "s_waitcnt lgkmcnt(6)\n"
    "v_smfmac_f32_16x16x64_bf16 v[56:59], v[80:83], a[16:23], v6\n"
    "ds_read_b128 v[76:79], v7 offset:1152\n"
    "s_waitcnt lgkmcnt(6)\n"
    "v_smfmac_f32_16x16x64_bf16 v[60:63], v[84:87], a[24:31], v6\n"
    "ds_read_b128 v[80:83], v7 offset:1280\n"
    "s_waitcnt lgkmcnt(6)\n"
    "v_smfmac_f32_16x16x64_bf16 v[56:59], v[88:91], a[32:39], v6\n"
    "ds_read_b128 v[84:87], v7 offset:1408\n"
    "s_waitcnt lgkmcnt(6)\n"
    "v_smfmac_f32_16x16x64_bf16 v[60:63], v[92:95], a[40:47], v6\n"
    "ds_read_b128 v[88:91], v7 offset:1536\n"
    "s_waitcnt lgkmcnt(6)\n"
    "v_smfmac_f32_16x16x64_bf16 v[56:59], v[96:99], a[48:55], v6\n"
    "ds_read_b128 v[92:95], v7 offset:1664\n"
    "s_waitcnt lgkmcnt(6)\n"
    "v_smfmac_f32_16x16x64_bf16 v[60:63], v[100:103], a[56:63], v6\n"
    "ds_read_b128 v[96:99], v7 offset:1792\n"
    "s_waitcnt lgkmcnt(6)\n"
    "v_smfmac_f32_16x16x64_bf16 v[56:59], v[72:75], a[64:71], v6\n"
    "ds_read_b128 v[100:103], v7 offset:1920\n"
    "s_waitcnt lgkmcnt(6)\n"
    "v_smfmac_f32_16x16x64_bf16 v[60:63], v[76:79], a[72:79], v6\n"
    "ds_read_b128 v[72:75], v7 offset:2048\n"
    "s_waitcnt lgkmcnt(6)\n"
    "v_smfmac_f32_16x16x64_bf16 v[56:59], v[80:83], a[80:87], v6\n"
    "ds_read_b128 v[76:79], v7 offset:2176\n"
    "s_waitcnt lgkmcnt(6)\n"
    "v_smfmac_f32_16x16x64_bf16 v[60:63], v[84:87], a[88:95], v6\n"
    "ds_read_b128 v[80:83], v7 offset:2304\n"
    "s_waitcnt lgkmcnt(6)\n"
    "v_smfmac_f32_16x16x64_bf16 v[56:59], v[88:91], a[96:103], v6\n"
    "ds_read_b128 v[84:87], v7 offset:2432\n"
    "s_waitcnt lgkmcnt(6)\n"
    "v_smfmac_f32_16x16x64_bf16 v[60:63], v[92:95], a[104:111], v6\n"
    "ds_read_b128 v[88:91], v7 offset:2560\n"
    "s_waitcnt lgkmcnt(6)\n"
    "v_smfmac_f32_16x16x64_bf16 v[56:59], v[96:99], a[112:119], v6\n"
    "ds_read_b128 v[92:95], v7 offset:2688\n"
    "s_waitcnt lgkmcnt(6)\n"
    "v_smfmac_f32_16x16x64_bf16 v[60:63], v[100:103], a[120:127], v6\n"
    "ds_read_b128 v[96:99], v7 offset:2816\n"
    "s_waitcnt lgkmcnt(6)\n"
    "v_smfmac_f32_16x16x64_bf16 v[56:59], v[72:75], a[128:135], v6\n"
    "ds_read_b128 v[100:103], v7 offset:2944\n"
    "s_waitcnt lgkmcnt(6)\n"
    "v_smfmac_f32_16x16x64_bf16 v[60:63], v[76:79], a[136:143], v6\n"
    "ds_read_b128 v[72:75], v7 offset:3072\n"
    "s_waitcnt lgkmcnt(6)\n"
    "v_smfmac_f32_16x16x64_bf16 v[56:59], v[80:83], a[144:151], v6\n"
    "ds_read_b128 v[76:79], v7 offset:3200\n"
    "s_waitcnt lgkmcnt(6)\n"
    "v_smfmac_f32_16x16x64_bf16 v[60:63], v[84:87], a[152:159], v6\n"
    "ds_read_b128 v[80:83], v7 offset:3328\n"
    "s_waitcnt lgkmcnt(6)\n"
    "v_smfmac_f32_16x16x64_bf16 v[56:59], v[88:91], a[160:167], v6\n"
    "ds_read_b128 v[84:87], v7 offset:3456\n"
    "s_waitcnt lgkmcnt(6)\n"
    "v_smfmac_f32_16x16x64_bf16 v[60:63], v[92:95], a[168:175], v6\n"
    "s_waitcnt lgkmcnt(5)\n"
    "v_smfmac_f32_16x16x64_bf16 v[56:59], v[96:99], a[176:183], v6\n"
    "s_waitcnt lgkmcnt(4)\n"
    "v_smfmac_f32_16x16x64_bf16 v[60:63], v[100:103], a[184:191], v6\n"
    "s_waitcnt lgkmcnt(3)\n"
    "v_smfmac_f32_16x16x64_bf16 v[56:59], v[72:75], a[192:199], v6\n"
    "s_waitcnt lgkmcnt(2)\n"
    "v_smfmac_f32_16x16x64_bf16 v[60:63], v[76:79], a[200:207], v6\n"
    "s_waitcnt lgkmcnt(1)\n"
    "v_smfmac_f32_16x16x64_bf16 v[56:59], v[80:83], a[208:215], v6\n"
    "s_waitcnt lgkmcnt(0)\n"
    "v_smfmac_f32_16x16x64_bf16 v[60:63], v[84:87], a[216:223], v6\n"
    "s_lshl_b32 s39, s70, 10\n"
    "s_andn2_b32 s61, s39, 0x1fff\n"
    "s_add_u32 s61, s61, s39\n"
    "s_add_u32 s61, s61, 263192576\n"
    "s_lshr_b32 s38, s61, 8\n"
    "s_xor_b32 s38, s38, s57\n"
    "s_and_b32 s38, s38, 0x2000\n"
    "s_or_b32 s61, s61, s38\n"
    "v_add_f32 v56, v56, v58\n"
    "v_add_f32 v60, v60, v62\n"
    "v_add_f32 v56, v56, v60\n"
    "v_cvt_pk_bf16_f32 v56, v56, v56\n"
    "v_lshlrev_b32 v56, 16, v56\n"
    "v_mul_f32 v56, v56, v52\n"
    "v_cvt_pk_bf16_f32 v56, v56, v56\n"
    "v_lshlrev_b32 v56, 16, v56\n"
    "v_add_f32 v56, v56, v46\n"
    "v_cvt_pk_bf16_f32 v56, v56, v56\n"
    "v_lshlrev_b32 v56, 16, v56\n"
    "v_add_f32 v56, v56, v45\n"
    "s_nop 1\n"
    "v_mov_b32_dpp v57, v56 quad_perm:[1,1,3,3] row_mask:0xf bank_mask:0xf\n"
    "v_cvt_pk_bf16_f32 v56, v56, v57\n"
    "s_cmp_lt_u32 s70, 64\n"
    "s_cbranch_scc1 LXP%=\n"
    "v_mov_b32 v47, v56\n"
    "s_cmp_eq_u32 s70, 64\n"
    "s_cbranch_scc1 LPOLL%=\n"
    "LOUT%=:\n"
    "s_lshl_b32 s38, s71, 10\n"
    "buffer_store_dword v47, v10, s[24:27], s38 offen sc1\n"
    "s_branch LEND%=\n"
    "LXP%=:\n"
    "v_min_u32 v56, -2, v56\n"
    "buffer_store_dword v56, v10, s[16:19], s61 offen sc1\n"
    "LFIX%=:\n"
    "s_lshl_b64 vcc, -1, s43\n"
    "s_andn2_b64 s[66:67], s[64:65], vcc\n"
    "s_and_b64 s[64:65], s[64:65], vcc\n"
    "s_max_u32 s42, s42, s43\n"
    "LFL%=:\n"
    "s_cmp_eq_u64 s[66:67], 0\n"
    "s_cbranch_scc1 LPOLL%=\n"
    "s_waitcnt vmcnt(0)\n"
    "s_ff1_i32_b64 s62, s[66:67]\n"
    "s_bitset0_b64 s[66:67], s62\n"
    "s_lshl_b32 s90, s62, 5\n"
    "s_add_u32 s90, s90, s55\n"
    "s_lshl_b32 s39, s90, 10\n"
    "s_andn2_b32 s63, s39, 0x1fff\n"
    "s_add_u32 s63, s63, s39\n"
    "s_add_u32 s63, s63, 241172480\n"
    "s_lshr_b32 s38, s63, 8\n"
    "s_xor_b32 s38, s38, s57\n"
    "s_and_b32 s38, s38, 0x2000\n"
    "s_or_b32 s63, s63, s38\n"
    "s_lshl_b32 s90, s90, 10\n"
    "buffer_load_dwordx4 v[72:75], v0, s[16:19], s63 offen sc1\n"
    "s_ff1_i32_b64 s62, s[66:67]\n"
    "s_bitset0_b64 s[66:67], s62\n"
    "s_lshl_b32 s91, s62, 5\n"
    "s_add_u32 s91, s91, s55\n"
    "s_lshl_b32 s39, s91, 10\n"
    "s_andn2_b32 s63, s39, 0x1fff\n"
    "s_add_u32 s63, s63, s39\n"
    "s_add_u32 s63, s63, 241172480\n"
    "s_lshr_b32 s38, s63, 8\n"
    "s_xor_b32 s38, s38, s57\n"
    "s_and_b32 s38, s38, 0x2000\n"
    "s_or_b32 s63, s63, s38\n"
    "s_lshl_b32 s91, s91, 10\n"
    "buffer_load_dwordx4 v[76:79], v0, s[16:19], s63 offen sc1\n"
    "s_ff1_i32_b64 s62, s[66:67]\n"
    "s_bitset0_b64 s[66:67], s62\n"
    "s_lshl_b32 s92, s62, 5\n"
    "s_add_u32 s92, s92, s55\n"
    "s_lshl_b32 s39, s92, 10\n"
    "s_andn2_b32 s63, s39, 0x1fff\n"
    "s_add_u32 s63, s63, s39\n"
    "s_add_u32 s63, s63, 241172480\n"
    "s_lshr_b32 s38, s63, 8\n"
    "s_xor_b32 s38, s38, s57\n"
    "s_and_b32 s38, s38, 0x2000\n"
    "s_or_b32 s63, s63, s38\n"
    "s_lshl_b32 s92, s92, 10\n"
    "buffer_load_dwordx4 v[80:83], v0, s[16:19], s63 offen sc1\n"
    "s_ff1_i32_b64 s62, s[66:67]\n"
    "s_bitset0_b64 s[66:67], s62\n"
    "s_lshl_b32 s93, s62, 5\n"
    "s_add_u32 s93, s93, s55\n"
    "s_lshl_b32 s39, s93, 10\n"
    "s_andn2_b32 s63, s39, 0x1fff\n"
    "s_add_u32 s63, s63, s39\n"
    "s_add_u32 s63, s63, 241172480\n"
    "s_lshr_b32 s38, s63, 8\n"
    "s_xor_b32 s38, s38, s57\n"
    "s_and_b32 s38, s38, 0x2000\n"
    "s_or_b32 s63, s63, s38\n"
    "s_lshl_b32 s93, s93, 10\n"
    "buffer_load_dwordx4 v[84:87], v0, s[16:19], s63 offen sc1\n"
    "s_ff1_i32_b64 s62, s[66:67]\n"
    "s_bitset0_b64 s[66:67], s62\n"
    "s_lshl_b32 s94, s62, 5\n"
    "s_add_u32 s94, s94, s55\n"
    "s_lshl_b32 s39, s94, 10\n"
    "s_andn2_b32 s63, s39, 0x1fff\n"
    "s_add_u32 s63, s63, s39\n"
    "s_add_u32 s63, s63, 241172480\n"
    "s_lshr_b32 s38, s63, 8\n"
    "s_xor_b32 s38, s38, s57\n"
    "s_and_b32 s38, s38, 0x2000\n"
    "s_or_b32 s63, s63, s38\n"
    "s_lshl_b32 s94, s94, 10\n"
    "buffer_load_dwordx4 v[88:91], v0, s[16:19], s63 offen sc1\n"
    "s_ff1_i32_b64 s62, s[66:67]\n"
    "s_bitset0_b64 s[66:67], s62\n"
    "s_lshl_b32 s95, s62, 5\n"
    "s_add_u32 s95, s95, s55\n"
    "s_lshl_b32 s39, s95, 10\n"
    "s_andn2_b32 s63, s39, 0x1fff\n"
    "s_add_u32 s63, s63, s39\n"
    "s_add_u32 s63, s63, 241172480\n"
    "s_lshr_b32 s38, s63, 8\n"
    "s_xor_b32 s38, s38, s57\n"
    "s_and_b32 s38, s38, 0x2000\n"
    "s_or_b32 s63, s63, s38\n"
    "s_lshl_b32 s95, s95, 10\n"
    "buffer_load_dwordx4 v[92:95], v0, s[16:19], s63 offen sc1\n"
    "s_ff1_i32_b64 s62, s[66:67]\n"
    "s_bitset0_b64 s[66:67], s62\n"
    "s_lshl_b32 s96, s62, 5\n"
    "s_add_u32 s96, s96, s55\n"
    "s_lshl_b32 s39, s96, 10\n"
    "s_andn2_b32 s63, s39, 0x1fff\n"
    "s_add_u32 s63, s63, s39\n"
    "s_add_u32 s63, s63, 241172480\n"
    "s_lshr_b32 s38, s63, 8\n"
    "s_xor_b32 s38, s38, s57\n"
    "s_and_b32 s38, s38, 0x2000\n"
    "s_or_b32 s63, s63, s38\n"
    "s_lshl_b32 s96, s96, 10\n"
    "buffer_load_dwordx4 v[96:99], v0, s[16:19], s63 offen sc1\n"
    "s_ff1_i32_b64 s62, s[66:67]\n"
    "s_bitset0_b64 s[66:67], s62\n"
    "s_lshl_b32 s97, s62, 5\n"
    "s_add_u32 s97, s97, s55\n"
    "s_lshl_b32 s39, s97, 10\n"
    "s_andn2_b32 s63, s39, 0x1fff\n"
    "s_add_u32 s63, s63, s39\n"
    "s_add_u32 s63, s63, 241172480\n"
    "s_lshr_b32 s38, s63, 8\n"
    "s_xor_b32 s38, s38, s57\n"
    "s_and_b32 s38, s38, 0x2000\n"
    "s_or_b32 s63, s63, s38\n"
    "s_lshl_b32 s97, s97, 10\n"
    "buffer_load_dwordx4 v[100:103], v0, s[16:19], s63 offen sc1\n"
    "s_waitcnt vmcnt(7)\n"
    "buffer_store_dwordx4 v[72:75], v0, s[24:27], s90 offen offset:0 sc1\n"
    "s_waitcnt vmcnt(7)\n"
    "buffer_store_dwordx4 v[76:79], v0, s[24:27], s91 offen offset:0 sc1\n"
    "s_waitcnt vmcnt(7)\n"
    "buffer_store_dwordx4 v[80:83], v0, s[24:27], s92 offen offset:0 sc1\n"
    "s_waitcnt vmcnt(7)\n"
    "buffer_store_dwordx4 v[84:87], v0, s[24:27], s93 offen offset:0 sc1\n"
    "s_waitcnt vmcnt(7)\n"
    "buffer_store_dwordx4 v[88:91], v0, s[24:27], s94 offen offset:0 sc1\n"
    "s_waitcnt vmcnt(7)\n"
    "buffer_store_dwordx4 v[92:95], v0, s[24:27], s95 offen offset:0 sc1\n"
    "s_waitcnt vmcnt(7)\n"
    "buffer_store_dwordx4 v[96:99], v0, s[24:27], s96 offen offset:0 sc1\n"
    "s_waitcnt vmcnt(7)\n"
    "buffer_store_dwordx4 v[100:103], v0, s[24:27], s97 offen offset:0 sc1\n"
    "s_branch LFL%=\n"
    "LFAIL%=:\n"
    "buffer_store_dword v6, off, s[16:19], s59 sc1\n"
    "LEND%=:\n"
    "s_endpgm\n"
    :
    : "{v0}"(hv0), "{v1}"(hv1), "{v2}"(hv2), "{v3}"(hv3), "{v4}"(hv4), "{v5}"(hv5), "{v6}"(hv6), "{v7}"(hv7), "{v8}"(hv8), "{v9}"(hv9), "{v10}"(hv10), "{v11}"(hv11), "{v12}"(hv12), "{v13}"(hv13), "{v14}"(hv14), "{v15}"(hv15), "{v16}"(hv16), "{v17}"(hv17), "{v18}"(hv18), "{v19}"(hv19), "{v20}"(hv20), "{v21}"(hv21)
    : "v255", "a255", "s101", "vcc", "scc", "memory");
  }
  __builtin_unreachable();
}
#define NPROBE 5
#define PACE_ARG
#define PACE (Pace{4080,2835,300,2550,3060,2048,60})
#define BIG_LDS 163840
#define SLPB 600u
#define SLPA 600u
#define WARM 0
#define XB 5
#define KSIZE 82000
#define BOOT_BYTES 73728
#define BT_X 1024
#define BT_D 2048
#define BT_R 10240
__device__ __forceinline__ void gate(unsigned* bt) {
  gu32* const r = (gu32*)(bt + BT_R + blockIdx.x * 32);
  __hip_atomic_store(r, 0u, RLX_AGENT);
  vm0();
  unsigned last = 0u;
  for (unsigned it = 0; it < SPINCAP; ++it) {
    const unsigned x = __builtin_amdgcn_readfirstlane(__hip_atomic_load((gu32*)(bt + BT_X + (blockIdx.x & 7) * 32), RLX_AGENT));
    if (__builtin_amdgcn_readfirstlane(__hip_atomic_load(r, RLX_AGENT)) != 0u) break;
    if (it == 0 || x != last) __hip_atomic_store((gu32*)(bt + BT_D + blockIdx.x * 32), x, RLX_AGENT);
    last = x;
    __builtin_amdgcn_s_sleep(1);
  }
}
__device__ __forceinline__ void serve(unsigned* bt, char* lds) {
  const int j = threadIdx.x;
  gu32* const d = (gu32*)(bt + BT_D + j * 32);
  const unsigned d0 = __hip_atomic_load(d, RLX_AGENT);
  __builtin_amdgcn_raw_buffer_store_b128((v4u){0u, 0u, 0u, 0u}, __builtin_amdgcn_make_buffer_rsrc((void*)bt, 0, 4096, 0x00020000), j * 16, 0, AUX_SC1);
  unsigned* const seen = (unsigned*)lds;
  if (j < 16) seen[j] = 0u;
  __syncthreads();
  if (d0 - 1u < 512u) atomicOr(seen + ((d0 - 1u) >> 5), 1u << ((d0 - 1u) & 31));
  vm0();
  __syncthreads();
  unsigned n = 0u;
  for (int q = 15; q >= 0; --q) if (~seen[q]) n = q * 32 + __builtin_ctz(~seen[q]) + 1;
  if (j < 8) __hip_atomic_store((gu32*)(bt + BT_X + j * 32), n, RLX_AGENT);
  const bool late = (j & 7) == 0 || (j & 7) == 7 || ((j & 7) < 3 && !(j & 8));
  bool need = j != (int)blockIdx.x;
  for (unsigned it = 0; it < SPINCAP && __ballot(need) != 0ull; ++it) {
    const unsigned e = need ? __hip_atomic_load(d, RLX_AGENT) : 0u, v = late ? __hip_atomic_load((gu32*)(bt + 128), RLX_AGENT) : 1u;
    if (e == n && v != 0u) { __hip_atomic_store((gu32*)(bt + BT_R + j * 32), v, RLX_AGENT); need = false; }
    __builtin_amdgcn_s_sleep(1);
  }
  __syncthreads();
}
__device__ __forceinline__ v4u first_state(int f, const u16* mem0) {
  v4u m0 = (v4u){~0u, ~0u, ~0u, ~0u};
  if (f >= 0 && f < 512) {
    const int tk = (f >> 3) & 7, n7 = (f & 7) ^ (tk >= 4 ? 4 : 0);
    const u16* p = mem0 + tk * DM + 64 * (f >> 6) + 32 * (n7 >> 2) + (n7 & 3);
#pragma unroll
    for (int d = 0; d < 4; ++d) m0[d] = (unsigned)p[8 * d] | ((unsigned)p[8 * d + 4] << 16);
    m0[3] = min(m0[3], 0xfffffffeu);
  }
  return m0;
}
__device__ __forceinline__ unsigned choice(int w, unsigned* bt, char* lds) {
  if (w == 0) *(volatile unsigned*)(lds + 8192) = wait_word(bt + 640 + (blockIdx.x & 7) * 32);
  __syncthreads();
  return __builtin_amdgcn_readfirstlane(*(volatile unsigned*)(lds + 8192)) & 1u;
}
__device__ __forceinline__ void boot2(int n, int w, unsigned* bt, char* lds, char* ws, const u16* mem0, const u16* Wq, const u16* Wk, const u16* Wv) {
  const rsrc_t rs = __builtin_amdgcn_make_buffer_rsrc((void*)ws, 0, ZWS_BYTES, 0x00020000);
  const int lane = lane_id(), t = n * 256 + w * 64 + lane;
  const v4u EMPTY = (v4u){~0u, ~0u, ~0u, ~0u}, m0 = first_state(t, mem0);
#pragma unroll
  for (int m = 0; m < WARM; ++m) {
    const rsrc_t rw = __builtin_amdgcn_make_buffer_rsrc((void*)(m == 0 ? Wv : (m == 1 ? Wk : Wq)), 0, DM * HD * 2, 0x00020000);
#pragma unroll
    for (int k = 0; k < 16; ++k) dma16(rw, lds + w * 1024, t * 16, k * 262144);
  }
  if (w == 0) gate(bt);
  const unsigned cb = choice(w, bt, lds), c2 = (cb << 13) ^ 0x2000u;
#pragma unroll
  for (unsigned k = 0; k < 2; ++k) { const unsigned o = (k * 16384 + t) * 16; st4c(rs, o < (Z_SEND - Z_X2P) / 2 ? blk(Z_X2P, o, c2) : OOR, EMPTY); }
#pragma unroll
  for (int k = 0; k < 8; ++k) st4c(rs, blk(Z_CH + (1 + (k >> 2)) * MBY(2), ((k & 3) * 16384 + t) * 16, c2), (k & 3) == 2 ? m0 : EMPTY);
  vm0();
  __syncthreads();
  if (w == 0) {
    unsigned o = 0u;
    if (lane == 0) o = __hip_atomic_fetch_add((gu32*)(bt + 96), 1u, RLX_AGENT);
    if (__builtin_amdgcn_readfirstlane(o) == 63u) {
      const unsigned v = wait_word(bt + 128);
      if (lane < 4) __hip_atomic_store((gu32*)(bt + 256 + lane * 32), v, RLX_AGENT);
    }
  }
}
__device__ __forceinline__ void boot(int n, int w, unsigned* bt, char* lds, char* ws, const u16* x, const u16* mem0) {
  const rsrc_t rs = __builtin_amdgcn_make_buffer_rsrc((void*)ws, 0, ZWS_BYTES, 0x00020000), rx = __builtin_amdgcn_make_buffer_rsrc((void*)x, 0, S_TOK * DM * 2, 0x00020000);
  const int lane = lane_id(), t = n * 256 + w * 64 + lane;
  const int v = n < 32 ? (w ? n * 3 + w - 1 : 2048) : n * 4 + w - 32;
  v4u y[10];
#pragma unroll
  for (int k = 0; k < 10; ++k) y[k] = ld4(rx, (k * 224 + v) * 1024 + lane * 16);
  if (w == (n < 32)) gate(bt);
  if (n < 32) {
    unsigned tm[2] = {~0u, ~0u};
    if (w == 0) for (int q = 0; q < NPROBE; ++q) {
      const unsigned long long t0 = __builtin_amdgcn_s_memtime();
      asm volatile("s_waitcnt lgkmcnt(0)" ::: "memory");
      st1c(rs, lane ? OOR : Z_PART + (n & 3) * MBY(2) + ((q ^ n) & 1) * 8192 + n * 128, 0u);
      vm0();
      tm[q & 1] = min(tm[q & 1], (unsigned)(__builtin_amdgcn_s_memtime() - t0));
    }
    __syncthreads();
    if (w == 0) {
      const unsigned d = (unsigned)min(max((int)(tm[0] - tm[1]), -200), 200);
      unsigned long long o = 0ull;
      if (lane == 0) o = __hip_atomic_fetch_add((gu64*)bt, ((unsigned long long)d << 32) | 1ull, RLX_AGENT);
      if (__builtin_amdgcn_readfirstlane((unsigned)o) == 31u && lane < 8)
        __hip_atomic_store((gu32*)(bt + 640 + lane * 32), (int)(__builtin_amdgcn_readfirstlane((unsigned)(o >> 32)) + d) > 0 ? 3u : 2u, RLX_AGENT);
    }
  }
  const v4u EMPTY = (v4u){~0u, ~0u, ~0u, ~0u};
  const v4u m0 = first_state(t - 8192, mem0);
#pragma unroll
  for (unsigned k = 0; k < 3; ++k) { const unsigned o = (k * 16384 + t) * 16; st4c(rs, o < Z_ZEND - Z_FLG ? Z_FLG + o : OOR, (v4u){0u, 0u, 0u, 0u}); }
#pragma unroll
  for (int k = 0; k < 10; ++k) y[k] = ln_row(y[k]);
  const unsigned cb = choice(w, bt, lds), c1 = cb << 13;
#pragma unroll
  for (int k = 0; k < 10; ++k) st4c(rs, k * 224 + v < S_TOK ? blk(Z_XN1, (k * 224 + v) * 1024, c1) + lane * 16 : OOR, y[k]);
#pragma unroll
  for (int k = 0; k < 4; ++k) st4c(rs, blk(Z_CH, (k * 16384 + (t ^ 8192)) * 16, c1), k == 2 ? m0 : EMPTY);
  vm0();
  __syncthreads();
  if (w == 0) {
    unsigned o = 0u;
    if (lane == 0) o = __hip_atomic_fetch_add((gu32*)(bt + 32), 1u, RLX_AGENT);
    if (__builtin_amdgcn_readfirstlane(o) == 63u && lane < 4) __hip_atomic_store((gu32*)(bt + 128 + lane * 32), 2u | cb, RLX_AGENT);
  }
}

__global__ __launch_bounds__(256, 1) void main_kernel(unsigned* bt, char* ws, const u16* x, const u16* mem0, const u16* Wq, const u16* Wk, const u16* Wv, const u16* Wo, const u16* w_r,
                                                      const u16* wg, const u16* wu, const u16* wd, u16* out PACE_ARG) {
  extern __shared__ char lds[];
  const int b = blockIdx.x, xc = SW && (b & 3) == 3 ? (b & 7) ^ 4 : b & 7, i = b >> 3, w = __builtin_amdgcn_readfirstlane(threadIdx.x >> 6);
  unsigned* rdy = bt + 128 + xc * 32;
  {
    const unsigned o = min((unsigned)((i & 3) * 256 + threadIdx.x) * 128u, (unsigned)KSIZE);
    asm volatile("s_mov_b32 m0, 163584\n s_getpc_b64 s[20:21]\n global_load_lds_dword %0, s[20:21]" :: "v"(o) : "s20", "s21", "memory");
  }
  const unsigned off = w == 1 && (xc == 0 || xc == 4 || xc == 5) ? clock_offset() : 0u;
  if (xc == 4 || xc == 5) {
    const unsigned t0 = (unsigned)__builtin_amdgcn_s_memrealtime();
    boot2(i * 2 + xc - 4, w, bt, lds, ws, mem0, Wq, Wk, Wv);
    while ((unsigned)__builtin_amdgcn_s_memrealtime() - t0 < (xc == 4 ? SLPB : SLPA)) __builtin_amdgcn_s_sleep(2);
  }
  if (xc == 0 || xc == XB) chain8<false>(w, i, xc >> 2, off, bt, xc ? rdy : bt + BT_R + b * 32, ws, lds, Wq, Wk, Wv, Wo, PACE);
  if (xc == 9 - XB) chain8<true>(w, i, 2, off, bt, rdy, ws, lds, Wq, Wk, Wv, Wo, PACE);
  if ((b & 7) == 3 || ((b & 7) < 3 && (i & 1))) boot(xc < 3 ? 31 + i + xc - 1 : i, w, bt, lds, ws, x, mem0);
  else if (b == 254) serve(bt, lds);
  else if (xc > 3) {
    if (w == 0) gate(bt);
    __syncthreads();
  }
  if (xc <= 3) t_role8(w, xc - 1, i, (b & 7) < 3 && (i & 1), bt, ws, xc == 1 ? Wk : (xc == 2 ? Wv : Wq));
  if ((i >> 3) != 2) o_role8(w, i >= 16 ? 1 : (xc == 7 ? 0 : 2), i >= 16 ? (i & 7) + (xc & 1) * 8 : i, rdy, ws, Wo);
  if (xc == 7) l_role8<true>(w, i - 16, rdy, ws, x, w_r, wg, wu, wd, out);
  l_role8<false>(w, i - 16, rdy, ws, x, w_r, wg, wu, wd, out);
}
extern "C" void kernel_launch(void* const* d_in, const int* in_sizes, int n_in, void* d_out, int out_size,
                              void* d_ws, size_t ws_size, hipStream_t stream) {
  char* ws = (char*)(((size_t)d_ws + BOOT_BYTES + MBY(2) - 1) & ~(size_t)(MBY(2) - 1));
  (void)hipFuncSetAttribute((const void*)main_kernel, hipFuncAttributeMaxDynamicSharedMemorySize, BIG_LDS);
  main_kernel<<<256, 256, BIG_LDS, stream>>>((unsigned*)d_ws, ws, (const u16*)d_in[0], (const u16*)d_in[1], (const u16*)d_in[2], (const u16*)d_in[3], (const u16*)d_in[4], (const u16*)d_in[5],
                                             (const u16*)d_in[8], (const u16*)d_in[9], (const u16*)d_in[10], (const u16*)d_in[11], (u16*)d_out);
}
```
